```python
import torch

def _softmax(x, axis=-1):
    return torch.softmax(x, dim=axis)

def _gelu(x):
    return torch.nn.functional.gelu(x, approximate='tanh')

def _silu(x):
    return torch.nn.functional.silu(x)

def _sigmoid(x):
    return torch.sigmoid(x)

def _layernorm(x, axis=-1, eps=1e-05):
    m = x.mean(dim=axis, keepdim=True)
    v = x.var(dim=axis, keepdim=True, unbiased=False)
    return (x - m) / torch.sqrt(v + eps)

def _scatter_add(tgt, idx, src, axis=0):
    return tgt.clone().index_add_(axis, idx, src)

def _segment_sum(src, seg, n):
    out = torch.zeros((n,) + src.shape[1:], dtype=src.dtype)
    return out.index_add_(0, seg, src)

def _pad(x, pw):
    flat = tuple((v for pair in reversed(pw) for v in pair))
    return torch.nn.functional.pad(x, flat)
kernel_name = 'adaLN_cond__mha_block_cached__moe_arch_mod__prompt'

def setup_inputs():

    _w = lambda a: (lambda t: t.to(torch.bfloat16) if t.is_floating_point() else t)(torch.as_tensor(a))

    def _A_setup():
        B, Sq, D = (1, 2048, 1024)
        H, Sk, Dh = (32, 2048, 128)
        return {'x': _w((lambda *s: torch.randn(*s, dtype=torch.bfloat16) * 0.1)(B, Sq, D)), 'k_cache': _w((lambda *s: torch.randn(*s, dtype=torch.bfloat16) * 0.8)(B, H, Sk, Dh)), 'v_cache': _w((lambda *s: torch.randn(*s, dtype=torch.bfloat16) * 0.8)(B, H, Sk, Dh)), 'wq': _w((lambda *s: torch.randn(*s, dtype=torch.bfloat16) * 0.8)(D, H * Dh) * D ** (-0.5)), 'wo': _w((lambda *s: torch.randn(*s, dtype=torch.bfloat16) * 0.8)(H * Dh, D) * (H * Dh) ** (-0.5))}
    _A_in = _A_setup()

    def _B_setup():
        B, S, D, F, C = (1, 2048, 1024, 3584, 2)
        return {'x': _w((lambda *s: torch.randn(*s, dtype=torch.bfloat16) * 0.1)(B, S, D)), 'w_r': _w((lambda *s: torch.randn(*s, dtype=torch.bfloat16) * 3.2)(D)), 'w_gate': _w((lambda *s: torch.randn(*s, dtype=torch.bfloat16) * 0.2)(D, F)), 'w_up': _w((lambda *s: torch.randn(*s, dtype=torch.bfloat16) * 0.2)(D, F)), 'w_down': _w((lambda *s: torch.randn(*s, dtype=torch.bfloat16) * 0.2)(F, D))}
    _B_in = _B_setup()
    return {'A_x': _A_in['x'], 'A_k_cache': _A_in['k_cache'], 'A_v_cache': _A_in['v_cache'], 'A_wq': _A_in['wq'], 'A_wo': _A_in['wo'], 'B_x': _B_in['x'], 'B_w_r': _B_in['w_r'], 'B_w_gate': _B_in['w_gate'], 'B_w_up': _B_in['w_up'], 'B_w_down': _B_in['w_down'], 'c': _w((lambda *s: torch.randn(*s, dtype=torch.bfloat16) * 0.2)(1, 1024) * 8.0), 'w_mod': _w((lambda *s: torch.randn(*s, dtype=torch.bfloat16) * 0.2)(1024, 6 * 1024) * 1024 ** (-0.5) * 8.0)}

def reference(A_x, A_k_cache, A_v_cache, A_wq, A_wo, B_x, B_w_r, B_w_gate, B_w_up, B_w_down, c, w_mod):

    def _A_ref(x, k_cache, v_cache, wq, wo):
        B, Sq, D = x.shape
        H, Dh = (32, 128)
        q = (lambda x, *d: x.permute(*d))((x @ wq).reshape(B, Sq, H, Dh), 0, 2, 1, 3)
        scores = torch.einsum('bhqd,bhkd->bhqk', q, k_cache) * Dh ** (-0.5)
        p = _softmax(scores, axis=-1)
        o = torch.einsum('bhqk,bhkd->bhqd', p, v_cache)
        return (lambda x, *d: x.permute(*d))(o, 0, 2, 1, 3).reshape(B, Sq, H * Dh) @ wo

    def _B_ref(x, w_r, w_gate, w_up, w_down):
        B, S, D = x.shape
        C = 2
        r = x @ w_r
        idx = torch.argsort(-r, axis=-1)[:, :C]
        idx_e = (lambda x, n, axis: torch.repeat_interleave(x, n, dim=axis))(idx.reshape(B, C, 1), D, axis=2)
        x_sel = (lambda x, i, axis: torch.take_along_dim(x, i, dim=axis))(x, idx_e, axis=1)
        g = _silu(x_sel @ w_gate)
        u = x_sel @ w_up
        y = g * u @ w_down
        r_sel = _sigmoid((lambda x, i, axis: torch.take_along_dim(x, i, dim=axis))(r, idx, axis=1))
        y = y * r_sel.reshape(B, C, 1)
        b_off = (torch.arange(B) * S).reshape(B, 1)
        flat_idx = (idx + b_off).reshape(B * C)
        zero = (x * 0.0).reshape(B * S, D)
        upd = _scatter_add(zero, flat_idx, y.reshape(B * C, D), axis=0)
        return x + upd.reshape(B, S, D)
    x = A_x
    mod = (_silu(c) @ w_mod).reshape(c.shape[0], 6, 1, -1)
    sa, ca, ga, sb, cb, gb = (mod[:, 0].clone(), mod[:, 1].clone(), mod[:, 2].clone(), mod[:, 3].clone(), mod[:, 4].clone(), mod[:, 5].clone())
    h = _layernorm(x, axis=-1) * (1 + ca) + sa
    x = x + ga * _A_ref(h, A_k_cache, A_v_cache, A_wq, A_wo)
    h = _layernorm(x, axis=-1) * (1 + cb) + sb
    x = x + gb * _B_ref(h, B_w_r, B_w_gate, B_w_up, B_w_down)
    return x
```

```cpp
#include <hip/hip_runtime.h>
#include <hip/hip_bf16.h>
#include <utility>
#include <cstdint>

#pragma clang fp contract(off)
typedef float    f32x16 __attribute__((ext_vector_type(16)));
typedef float    f32x2  __attribute__((ext_vector_type(2)));
typedef float    f32x4  __attribute__((ext_vector_type(4)));
typedef __bf16   bf16x8 __attribute__((ext_vector_type(8)));
typedef __bf16   bf16x4 __attribute__((ext_vector_type(4)));
typedef __bf16   bf16x2 __attribute__((ext_vector_type(2)));
typedef unsigned u32x4  __attribute__((ext_vector_type(4)));
typedef unsigned u32x2  __attribute__((ext_vector_type(2)));
typedef short    s16x4  __attribute__((ext_vector_type(4)));
typedef unsigned short u16;
template <int... Is> using iseq = std::integer_sequence<int, Is...>;
template <int N> using make_iseq = std::make_integer_sequence<int, N>;
#define DI __device__ __forceinline__
#define TAILB
#define PACE2
#define SELB

constexpr int SEQ = 2048, DM = 1024, NHEAD = 32, DHEAD = 128, DQ = NHEAD * DHEAD, DFF = 3584, NMOD = 6 * DM;
constexpr size_t WS_EP    = 0;
constexpr size_t WS_RST   = 32768;
constexpr size_t WS_CNT   = 65536;
constexpr size_t WS_MOD   = 131072;
constexpr size_t WS_Z     = 262144;
constexpr size_t WS_MODT  = WS_Z;
constexpr size_t WS_XFL   = WS_Z + 16384;
#define KEY_REP 8u
constexpr size_t WS_KEYS  = WS_Z + 32768;
constexpr unsigned Z_SHARE = 2560;
static_assert(KEY_REP <= 8 && Z_SHARE * 256 == 655360, "zeroed area");
constexpr size_t WS_MD    = WS_Z + 49152;
constexpr size_t WS_PFL   = WS_Z + 65536;
constexpr size_t WS_OFL   = WS_Z + 196608;
constexpr size_t WS_PK    = WS_Z + 327680;
constexpr size_t WS_U     = WS_Z + 344064;
constexpr size_t WS_PX    = WS_Z + 393216;
constexpr size_t WS_H     = 1048576;
constexpr size_t WS_PART  = WS_H  + (size_t)SEQ * DM * 2;
constexpr size_t WS_XIN   = WS_PART + (size_t)256 * 2 * DM * 4;
#define BOXV 38016
constexpr unsigned BOX    = BOXV;
constexpr size_t WS_END   = WS_XIN + (size_t)BOX * (SEQ / 2);

DI float bf2f(u16 b) { return __uint_as_float((unsigned)b << 16); }
DI float rbf(float x) { return (float)(__bf16)x; }
DI u16   f2bf(float x) { return __builtin_bit_cast(u16, (__bf16)x); }
DI float lo16(unsigned w) { return __uint_as_float(w << 16); }
DI float hi16(unsigned w) { return __uint_as_float(w & 0xffff0000u); }
DI unsigned cvtpk(float lo, float hi) {
  f32x2 v = {lo, hi}; bf16x2 b = __builtin_convertvector(v, bf16x2); return __builtin_bit_cast(unsigned, b);
}
template <int CTRL> DI float dpp_add(float v) { return v + __uint_as_float(__builtin_amdgcn_update_dpp(0u, __float_as_uint(v), CTRL, 0xf, 0xf, true)); }
DI float swap32_add(float a, float b) {
  auto r = __builtin_amdgcn_permlane32_swap(__float_as_uint(a), __float_as_uint(b), false, false); return __uint_as_float(r[0]) + __uint_as_float(r[1]);
}
DI float swap16_add(float a, float b) {
  auto r = __builtin_amdgcn_permlane16_swap(__float_as_uint(a), __float_as_uint(b), false, false); return __uint_as_float(r[0]) + __uint_as_float(r[1]);
}
constexpr int DPP_XOR1 = 0xB1, DPP_XOR2 = 0x4E, DPP_ROR4 = 0x124, DPP_ROR8 = 0x128;
DI float wave_sum(float v) {
  v = dpp_add<DPP_XOR1>(v); v = dpp_add<DPP_XOR2>(v); v = dpp_add<DPP_ROR4>(v); v = dpp_add<DPP_ROR8>(v);
  v = swap16_add(v, v); return swap32_add(v, v);
}

DI void st16_wt(void* p, u32x4 v) { asm volatile("global_store_dwordx4 %0, %1, off sc1\n\ts_nop 1" :: "v"(p), "v"(v) : "memory"); }

DI f32x2 unpk(unsigned w) { return f32x2{lo16(w), hi16(w)}; }
DI unsigned cvtpk2(f32x2 v) { return __builtin_bit_cast(unsigned, __builtin_convertvector(v, bf16x2)); }
DI f32x2 rb2(f32x2 v) { return f32x2{__uint_as_float(cvtpk(0.f, v[0])), __uint_as_float(cvtpk(0.f, v[1]))}; }
DI unsigned ln_pair(float x0, float x1, float m, float rden, float c0, float c1, float s0, float s1) {
  const unsigned d = cvtpk2(f32x2{x0, x1} - m), l = cvtpk2(unpk(d) * rden), t = cvtpk2(unpk(l) * f32x2{c0, c1});
  return cvtpk2(unpk(t) + f32x2{s0, s1});
}
#define GAS __attribute__((address_space(1)))
#define LAS __attribute__((address_space(3)))
typedef GAS unsigned long long gu64;
typedef short v4i16_t __attribute__((ext_vector_type(4)));
DI bf16x4 lds_tr(unsigned a) { return __builtin_bit_cast(bf16x4, __builtin_amdgcn_ds_read_tr16_b64_v4i16((LAS v4i16_t*)a)); }
#define RLX_AGENT __ATOMIC_RELAXED, __HIP_MEMORY_SCOPE_AGENT
constexpr unsigned SPIN_CAP = 1u << 16;

static_assert(BOX == 38016, "box size");
#define NP_FILL 8
#define GOT_WAIT "8"
#define UNITP 1
#define INS 0
#define XP2 0
#define FA_CLOBBERS "v4", "v5", "v6", "v7", "v12", "v13", "v14", "v15", "v20", "v21", "v22", "v23", "v28", "v29", "v30", "v31", "v36", "v37", "v38", "v39", "v44", "v45", "v46", "v47", "v52", "v53", "v54", "v55", "v60", "v61", "v62", "v63", "v64", "v65", "v66", "v67", "v68", "v69", "v70", "v71", "v72", "v73", "v74", "v75", "v76", "v77", "v78", "v79", "v80", "v81", "v82", "v83", "v84", "v85", "v86", "v87", "v88", "v89", "v90", "v91", "v92", "v93", "v94", "v95", "v112", "v113", "v114", "v115", "v116", "v117", "v118", "v119", "v120", "v121", "v122", "v123", "v124", "v125", "v126", "v127", "v128", "v129", "v130", "v131", "v132", "v133", "v134", "v135", "v136", "v137", "v138", "v139", "v140", "v141", "v142", "v143", "v144", "v145", "v146", "v147", "v148", "v149", "v150", "v151", "v152", "v153", "v154", "v155", "v156", "v157", "v158", "v159", "v160", "v161", "v162", "v163", "v164", "v165", "v166", "v167", "v168", "v169", "v170", "v171", "v172", "v173", "v174", "v175", "v176", "v177", "v178", "v179", "v180", "v181", "v182", "v183", "v184", "v185", "v186", "v187", "v188", "v189", "v190", "v191", "v192", "v193", "v194", "v195", "v196", "v197", "v198", "v199", "v200", "v201", "v202", "v203", "v204", "v205", "v206", "v207", "v208", "v209", "v210", "v211", "v212", "v213", "v214", "v215", "v216", "v217", "v219", "v221", "v222", "v223", "v224", "v230", "v231", "v232", "v233", "v234", "v235", "v237", "a32", "a33", "a34", "a35", "a36", "a37", "a38", "a39", "a40", "a41", "a42", "a43", "a44", "a45", "a46", "a47", "a48", "a49", "a50", "a51", "a52", "a53", "a54", "a55", "a56", "a57", "a58", "a59", "a60", "a61", "a62", "a63", "a128", "a129", "a130", "a131", "a132", "a133", "a134", "a135", "a136", "a137", "a138", "a139", "a140", "a141", "a142", "a143", "a144", "a145", "a146", "a147", "a148", "a149", "a150", "a151", "a152", "a153", "a154", "a155", "a156", "a157", "a158", "a159", "a160", "a161", "a162", "a163", "a164", "a165", "a166", "a167", "a168", "a169", "a170", "a171", "a172", "a173", "a174", "a175", "a176", "a177", "a178", "a179", "a180", "a181", "a182", "a183", "a184", "a185", "a186", "a187", "a188", "a189", "a190", "a191", "a192", "a193", "a194", "a195", "a196", "a197", "a198", "a199", "a200", "a201", "a202", "a203", "a204", "a205", "a206", "a207", "a208", "a209", "a210", "a211", "a212", "a213", "a214", "a215", "a216", "a217", "a218", "a219", "a220", "a221", "a222", "a223", "a224", "a225", "a226", "a227", "a228", "a229", "a230", "a231", "a232", "a233", "a234", "a235", "a236", "a237", "a238", "a239", "a240", "a241", "a242", "a243", "a244", "a245", "a246", "a247", "a248", "a249", "a250", "a251", "a252", "a253", "a254", "a255", "s40", "s41", "s42", "s43", "s44", "s45", "s46", "s47", "s48", "s49", "s50", "s51", "s52", "s53", "s54", "s55", "s56", "s57", "s58", "s59", "s60", "s61", "s62", "s63", "s64", "s65", "s66", "s67", "s68", "s69", "vcc", "m0", "scc", "memory"
#define EARLY2 0
#define SHW 1
#define QROT 0
#define SKV 0
#define KVPAR 0
#define QT2 0
#define QSTAG 0
#define ATAIL 1
static_assert(WS_MODT == 262144 && WS_XFL == 278528 && WS_XIN == 7340032, "workspace layout");
#define FA_OUTS [got] "=&s"(got), [k1] "=&s"(k1), [k2] "=&s"(k2), [lat] "=&s"(lat), "=&{v[0:3]}"(x1q[0]), "=&{v[8:11]}"(x1q[1]), "=&{v[16:19]}"(x1q[2]), "=&{v[24:27]}"(x1q[3]), "=&{v[32:35]}"(x1q[4]), "=&{v[40:43]}"(x1q[5]), "=&{v[48:51]}"(x1q[6]), "=&{v[56:59]}"(x1q[7]), "=&{v[96:99]}"(yo[0]), "=&{v[100:103]}"(yo[1]), "=&{v[104:107]}"(yo[2]), "=&{v[108:111]}"(yo[3]), "=&{v218}"(mrow), "=&{v220}"(rdrow), "=&{a[64:67]}"(wf[0]), "=&{a[68:71]}"(wf[1]), "=&{a[72:75]}"(wf[2]), "=&{a[76:79]}"(wf[3]), "=&{a[80:83]}"(wf[4]), "=&{a[84:87]}"(wf[5]), "=&{a[88:91]}"(wf[6]), "=&{a[92:95]}"(wf[7]), "=&{a[96:99]}"(wf[8]), "=&{a[100:103]}"(wf[9]), "=&{a[104:107]}"(wf[10]), "=&{a[108:111]}"(wf[11]), "=&{a[112:115]}"(wf[12]), "=&{a[116:119]}"(wf[13]), "=&{a[120:123]}"(wf[14]), "=&{a[124:127]}"(wf[15]), "=&{a[0:3]}"(wf[16]), "=&{a[4:7]}"(wf[17]), "=&{a[8:11]}"(wf[18]), "=&{a[12:15]}"(wf[19]), "=&{a[16:19]}"(wf[20]), "=&{a[20:23]}"(wf[21]), "=&{a[24:27]}"(wf[22]), "=&{a[28:31]}"(wf[23]), "+{v229}"(fl), "+{v225}"(vwb), "+{v226}"(wd0), "+{v227}"(hq0), "+{v228}"(sd0), "+{v236}"(tq0), [kb0] "+v"(kb0), [vvb] "+v"(vvb), [kd0] "+v"(kd0), [vd0] "+v"(vd0), [oq0] "+v"(oq0), [rd0] "+v"(rd0), [wo0] "+v"(wo0), [vg0] "+v"(vg0), [vx0] "+v"(vx0), [vdn] "+v"(vdn), [ta0] "+v"(ta0), [vxs] "+v"(vxs), [vct] "+v"(vct)
#define FA_BODY \
"s_branch adaln_body\n" \
".p2align 12\n" \
"adaln_pg7:\n" \
"adaln_body:\n" \
"s_cbranch_execz adaln_fr\n" \
"s_nop 0\n" \
"s_mov_b32 s55, 0x80000\n" \
"v_add_u32_e32 v219, 0x8000, v227\n" \
"v_add_u32_e32 v220, 0x10000, v227\n" \
"v_add_u32_e32 v221, 0x18000, v227\n" \
"s_mov_b32 s50, 0\n" \
"s_movk_i32 s53, 0x1000\n" \
"s_mov_b32 s52, 0\n" \
"s_nop 0\n" \
"s_mov_b32 s49, 1572864\n" \
"v_add_u32_e32 v216, 0xfffffc80, v226\n" \
"v_add_u32_e32 v217, 0xf800, v226\n" \
"v_add_u32_e32 v218, 0xf480, v226\n" \
"s_add_u32 s40, %[m0b], 0\n" \
"s_nop 0\n" \
"s_add_u32 s44, %[m0b], 65536\n" \
"s_add_u32 s41, %[m0b], 16384\n" \
"s_add_u32 s45, %[m0b], 81920\n" \
"s_add_u32 s42, %[m0b], 32768\n" \
"s_add_u32 s46, %[m0b], 98304\n" \
"s_add_u32 s43, %[m0b], 49152\n" \
"s_add_u32 s47, %[m0b], 114688\n" \
"v_xor_b32_e64 v208, 64, %[kb0]\n" \
"v_xor_b32_e32 v209, 0x80, %[kb0]\n" \
"v_xor_b32_e32 v210, 0xc0, %[kb0]\n" \
"v_xor_b32_e64 v212, 64, %[kd0]\n" \
"v_xor_b32_e32 v213, 0x80, %[kd0]\n" \
"v_xor_b32_e32 v214, 0xc0, %[kd0]\n" \
"v_xor_b32_e64 v211, 32, %[vvb]\n" \
"v_add_u32_e32 v215, 0xfffffc80, %[vd0]\n" \
"v_readlane_b32 s64, %[tlo], 17\n" \
"v_readlane_b32 s65, %[thi], 17\n" \
"s_mov_b32 s66, 12582912\n" \
"s_mov_b32 s67, 0x20000\n" \
"v_mov_b64_e32 v[0:1], 0\n" \
"v_mov_b64_e32 v[2:3], 0\n" \
"v_mov_b64_e32 v[4:5], 0\n" \
"v_mov_b64_e32 v[6:7], 0\n" \
"v_mov_b64_e32 v[8:9], 0\n" \
"v_mov_b64_e32 v[10:11], 0\n" \
"v_mov_b64_e32 v[12:13], 0\n" \
"v_mov_b64_e32 v[14:15], 0\n" \
"v_mov_b64_e32 v[16:17], 0\n" \
"v_mov_b64_e32 v[18:19], 0\n" \
"v_mov_b64_e32 v[20:21], 0\n" \
"v_mov_b64_e32 v[22:23], 0\n" \
"v_mov_b64_e32 v[24:25], 0\n" \
"v_mov_b64_e32 v[26:27], 0\n" \
"v_mov_b64_e32 v[28:29], 0\n" \
"v_mov_b64_e32 v[30:31], 0\n" \
"v_mov_b64_e32 v[32:33], 0\n" \
"v_mov_b64_e32 v[34:35], 0\n" \
"v_mov_b64_e32 v[36:37], 0\n" \
"v_mov_b64_e32 v[38:39], 0\n" \
"v_mov_b64_e32 v[40:41], 0\n" \
"v_mov_b64_e32 v[42:43], 0\n" \
"v_mov_b64_e32 v[44:45], 0\n" \
"v_mov_b64_e32 v[46:47], 0\n" \
"v_mov_b64_e32 v[48:49], 0\n" \
"v_mov_b64_e32 v[50:51], 0\n" \
"v_mov_b64_e32 v[52:53], 0\n" \
"v_mov_b64_e32 v[54:55], 0\n" \
"v_mov_b64_e32 v[56:57], 0\n" \
"v_mov_b64_e32 v[58:59], 0\n" \
"v_mov_b64_e32 v[60:61], 0\n" \
"v_mov_b64_e32 v[62:63], 0\n" \
"v_mov_b64_e32 v[64:65], 0\n" \
"v_mov_b64_e32 v[66:67], 0\n" \
"v_mov_b64_e32 v[68:69], 0\n" \
"v_mov_b64_e32 v[70:71], 0\n" \
"v_mov_b64_e32 v[72:73], 0\n" \
"v_mov_b64_e32 v[74:75], 0\n" \
"v_mov_b64_e32 v[76:77], 0\n" \
"v_mov_b64_e32 v[78:79], 0\n" \
"v_mov_b64_e32 v[80:81], 0\n" \
"v_mov_b64_e32 v[82:83], 0\n" \
"v_mov_b64_e32 v[84:85], 0\n" \
"v_mov_b64_e32 v[86:87], 0\n" \
"v_mov_b64_e32 v[88:89], 0\n" \
"v_mov_b64_e32 v[90:91], 0\n" \
"v_mov_b64_e32 v[92:93], 0\n" \
"v_mov_b64_e32 v[94:95], 0\n" \
"v_mov_b64_e32 v[96:97], 0\n" \
"v_mov_b64_e32 v[98:99], 0\n" \
"v_mov_b64_e32 v[100:101], 0\n" \
"v_mov_b64_e32 v[102:103], 0\n" \
"v_mov_b64_e32 v[104:105], 0\n" \
"v_mov_b64_e32 v[106:107], 0\n" \
"v_mov_b64_e32 v[108:109], 0\n" \
"v_mov_b64_e32 v[110:111], 0\n" \
"v_mov_b64_e32 v[112:113], 0\n" \
"v_mov_b64_e32 v[114:115], 0\n" \
"v_mov_b64_e32 v[116:117], 0\n" \
"v_mov_b64_e32 v[118:119], 0\n" \
"v_mov_b64_e32 v[120:121], 0\n" \
"v_mov_b64_e32 v[122:123], 0\n" \
"v_mov_b64_e32 v[124:125], 0\n" \
"v_mov_b64_e32 v[126:127], 0\n" \
"s_mov_b32 s51, 0x10000\n" \
"v_mbcnt_lo_u32_b32 v232, -1, 0\n" \
"v_mov_b64_e32 v[234:235], %[tag]\n" \
"v_lshlrev_b32_e32 v233, 3, v232\n" \
"v_lshlrev_b32_e32 v232, 8, v232\n" \
"s_waitcnt vmcnt(0)\n" \
"s_barrier\n" \
"s_cmp_lg_u32 %[wid], 0\n" \
"s_cbranch_scc1 Lwalk_%=\n" \
"s_mov_b32 exec_hi, 0\n" \
"global_store_dwordx2 v232, v[234:235], %[psig] sc1\n" \
"s_mov_b32 exec_hi, -1\n" \
"Lgo_%=:\n" \
"s_nop 0\n" \
"ds_read_b64_tr_b16 v[128:129], v225 offset:0\n" \
"ds_read_b64_tr_b16 v[130:131], v225 offset:256\n" \
"ds_read_b64_tr_b16 v[132:133], v225 offset:8192\n" \
"ds_read_b64_tr_b16 v[134:135], v225 offset:8448\n" \
"ds_read_b64_tr_b16 v[136:137], v225 offset:8\n" \
"ds_read_b64_tr_b16 v[138:139], v225 offset:264\n" \
"ds_read_b64_tr_b16 v[140:141], v225 offset:8200\n" \
"ds_read_b64_tr_b16 v[142:143], v225 offset:8456\n" \
"ds_read_b64_tr_b16 v[144:145], v225 offset:512\n" \
"ds_read_b64_tr_b16 v[146:147], v225 offset:768\n" \
"ds_read_b64_tr_b16 v[148:149], v225 offset:8704\n" \
"ds_read_b64_tr_b16 v[150:151], v225 offset:8960\n" \
"ds_read_b64_tr_b16 v[152:153], v225 offset:520\n" \
"ds_read_b64_tr_b16 v[154:155], v225 offset:776\n" \
"ds_read_b64_tr_b16 v[156:157], v225 offset:8712\n" \
"ds_read_b64_tr_b16 v[158:159], v225 offset:8968\n" \
"s_mov_b32 s60, 4194304\n" \
"v_and_b32_e32 v233, 56, v233\n" \
"s_lshl_b32 s61, %[wid], 6\n" \
"v_add_u32_e64 v233, s61, v233\n" \
"Lpoll_%=:\n" \
"global_load_dwordx2 v[230:231], v233, %[pcnt] sc1\n" \
"s_waitcnt vmcnt(0)\n" \
"v_cmp_ne_u64_e32 vcc, v[230:231], v[234:235]\n" \
"s_sub_u32 s51, s51, 1\n" \
"s_cbranch_scc1 Lpd_%=\n" \
"s_nop 3\n" \
"s_cbranch_vccnz Lpoll_%=\n" \
"Lpd_%=:\n" \
"buffer_load_dwordx4 a[128:131], v227, %[srdz], s50 offen offset:0 sc1\n" \
"buffer_load_dwordx4 a[132:135], v219, %[srdz], s50 offen offset:0 sc1\n" \
"buffer_load_dwordx4 a[136:139], v220, %[srdz], s50 offen offset:0 sc1\n" \
"buffer_load_dwordx4 a[140:143], v221, %[srdz], s50 offen offset:0 sc1\n" \
"buffer_load_dwordx4 a[144:147], v227, %[srdz], s50 offen offset:1024 sc1\n" \
"buffer_load_dwordx4 a[148:151], v219, %[srdz], s50 offen offset:1024 sc1\n" \
"buffer_load_dwordx4 a[152:155], v220, %[srdz], s50 offen offset:1024 sc1\n" \
"buffer_load_dwordx4 a[156:159], v221, %[srdz], s50 offen offset:1024 sc1\n" \
"buffer_load_dwordx4 a[160:163], v227, %[srdz], s50 offen offset:2048 sc1\n" \
"buffer_load_dwordx4 a[164:167], v219, %[srdz], s50 offen offset:2048 sc1\n" \
"buffer_load_dwordx4 a[168:171], v220, %[srdz], s50 offen offset:2048 sc1\n" \
"buffer_load_dwordx4 a[172:175], v221, %[srdz], s50 offen offset:2048 sc1\n" \
"buffer_load_dwordx4 a[176:179], v227, %[srdz], s50 offen offset:3072 sc1\n" \
"buffer_load_dwordx4 a[180:183], v219, %[srdz], s50 offen offset:3072 sc1\n" \
"buffer_load_dwordx4 a[184:187], v220, %[srdz], s50 offen offset:3072 sc1\n" \
"buffer_load_dwordx4 a[188:191], v221, %[srdz], s50 offen offset:3072 sc1\n" \
"s_add_u32 s50, s50, s53\n" \
"s_nop 0\n" \
"buffer_load_dwordx4 a[192:195], v227, %[srdz], s50 offen offset:0 sc1\n" \
"buffer_load_dwordx4 a[196:199], v219, %[srdz], s50 offen offset:0 sc1\n" \
"buffer_load_dwordx4 a[200:203], v220, %[srdz], s50 offen offset:0 sc1\n" \
"buffer_load_dwordx4 a[204:207], v221, %[srdz], s50 offen offset:0 sc1\n" \
"buffer_load_dwordx4 a[208:211], v227, %[srdz], s50 offen offset:1024 sc1\n" \
"buffer_load_dwordx4 a[212:215], v219, %[srdz], s50 offen offset:1024 sc1\n" \
"buffer_load_dwordx4 a[216:219], v220, %[srdz], s50 offen offset:1024 sc1\n" \
"buffer_load_dwordx4 a[220:223], v221, %[srdz], s50 offen offset:1024 sc1\n" \
"s_mov_b32 s51, 3\n" \
"s_waitcnt vmcnt(16) lgkmcnt(0)\n" \
"Lq_%=:\n" \
"v_mfma_f32_16x16x32_bf16 v[0:3], v[128:131], a[128:131], v[0:3]\n" \
"ds_read_b64_tr_b16 v[160:161], v225 offset:1024\n" \
"v_mfma_f32_16x16x32_bf16 v[0:3], v[132:135], a[144:147], v[0:3]\n" \
"ds_read_b64_tr_b16 v[162:163], v225 offset:1280\n" \
"v_mfma_f32_16x16x32_bf16 v[32:35], v[128:131], a[132:135], v[32:35]\n" \
"s_mov_b32 m0, s47\n" \
"s_nop 0\n" \
"buffer_load_dwordx4 v217, %[srdw], s49 offen offset:2048 lds\n" \
"v_mfma_f32_16x16x32_bf16 v[32:35], v[132:135], a[148:151], v[32:35]\n" \
"ds_read_b64_tr_b16 v[164:165], v225 offset:9216\n" \
"v_mfma_f32_16x16x32_bf16 v[64:67], v[128:131], a[136:139], v[64:67]\n" \
"ds_read_b64_tr_b16 v[166:167], v225 offset:9472\n" \
"v_mfma_f32_16x16x32_bf16 v[64:67], v[132:135], a[152:155], v[64:67]\n" \
"v_mfma_f32_16x16x32_bf16 v[96:99], v[128:131], a[140:143], v[96:99]\n" \
"ds_read_b64_tr_b16 v[168:169], v225 offset:1032\n" \
"v_mfma_f32_16x16x32_bf16 v[96:99], v[132:135], a[156:159], v[96:99]\n" \
"ds_read_b64_tr_b16 v[170:171], v225 offset:1288\n" \
"v_mfma_f32_16x16x32_bf16 v[4:7], v[136:139], a[128:131], v[4:7]\n" \
"buffer_load_dwordx4 v218, %[srdw], s49 offen offset:3072 lds\n" \
"v_mfma_f32_16x16x32_bf16 v[4:7], v[140:143], a[144:147], v[4:7]\n" \
"ds_read_b64_tr_b16 v[172:173], v225 offset:9224\n" \
"v_mfma_f32_16x16x32_bf16 v[36:39], v[136:139], a[132:135], v[36:39]\n" \
"ds_read_b64_tr_b16 v[174:175], v225 offset:9480\n" \
"v_mfma_f32_16x16x32_bf16 v[36:39], v[140:143], a[148:151], v[36:39]\n" \
"v_mfma_f32_16x16x32_bf16 v[68:71], v[136:139], a[136:139], v[68:71]\n" \
"ds_read_b64_tr_b16 v[176:177], v225 offset:1536\n" \
"v_mfma_f32_16x16x32_bf16 v[68:71], v[140:143], a[152:155], v[68:71]\n" \
"ds_read_b64_tr_b16 v[178:179], v225 offset:1792\n" \
"v_mfma_f32_16x16x32_bf16 v[100:103], v[136:139], a[140:143], v[100:103]\n" \
"buffer_load_dwordx4 a[224:227], v227, %[srdz], s50 offen offset:2048 sc1\n" \
"v_mfma_f32_16x16x32_bf16 v[100:103], v[140:143], a[156:159], v[100:103]\n" \
"ds_read_b64_tr_b16 v[180:181], v225 offset:9728\n" \
"v_mfma_f32_16x16x32_bf16 v[8:11], v[144:147], a[128:131], v[8:11]\n" \
"ds_read_b64_tr_b16 v[182:183], v225 offset:9984\n" \
"v_mfma_f32_16x16x32_bf16 v[8:11], v[148:151], a[144:147], v[8:11]\n" \
"v_mfma_f32_16x16x32_bf16 v[40:43], v[144:147], a[132:135], v[40:43]\n" \
"ds_read_b64_tr_b16 v[184:185], v225 offset:1544\n" \
"v_mfma_f32_16x16x32_bf16 v[40:43], v[148:151], a[148:151], v[40:43]\n" \
"ds_read_b64_tr_b16 v[186:187], v225 offset:1800\n" \
"v_mfma_f32_16x16x32_bf16 v[72:75], v[144:147], a[136:139], v[72:75]\n" \
"buffer_load_dwordx4 a[228:231], v219, %[srdz], s50 offen offset:2048 sc1\n" \
"v_mfma_f32_16x16x32_bf16 v[72:75], v[148:151], a[152:155], v[72:75]\n" \
"ds_read_b64_tr_b16 v[188:189], v225 offset:9736\n" \
"v_mfma_f32_16x16x32_bf16 v[104:107], v[144:147], a[140:143], v[104:107]\n" \
"ds_read_b64_tr_b16 v[190:191], v225 offset:9992\n" \
"v_mfma_f32_16x16x32_bf16 v[104:107], v[148:151], a[156:159], v[104:107]\n" \
"v_mfma_f32_16x16x32_bf16 v[12:15], v[152:155], a[128:131], v[12:15]\n" \
"buffer_load_dwordx4 a[232:235], v220, %[srdz], s50 offen offset:2048 sc1\n" \
"v_mfma_f32_16x16x32_bf16 v[12:15], v[156:159], a[144:147], v[12:15]\n" \
"v_mfma_f32_16x16x32_bf16 v[44:47], v[152:155], a[132:135], v[44:47]\n" \
"v_mfma_f32_16x16x32_bf16 v[44:47], v[156:159], a[148:151], v[44:47]\n" \
"v_mfma_f32_16x16x32_bf16 v[76:79], v[152:155], a[136:139], v[76:79]\n" \
"buffer_load_dwordx4 a[236:239], v221, %[srdz], s50 offen offset:2048 sc1\n" \
"v_mfma_f32_16x16x32_bf16 v[76:79], v[156:159], a[152:155], v[76:79]\n" \
"v_mfma_f32_16x16x32_bf16 v[108:111], v[152:155], a[140:143], v[108:111]\n" \
"v_mfma_f32_16x16x32_bf16 v[108:111], v[156:159], a[156:159], v[108:111]\n" \
"s_waitcnt vmcnt(29) lgkmcnt(0)\n" \
"s_barrier\n" \
"v_mfma_f32_16x16x32_bf16 v[16:19], v[160:163], a[128:131], v[16:19]\n" \
"ds_read_b64_tr_b16 v[128:129], v225 offset:16384\n" \
"v_mfma_f32_16x16x32_bf16 v[16:19], v[164:167], a[144:147], v[16:19]\n" \
"ds_read_b64_tr_b16 v[130:131], v225 offset:16640\n" \
"v_mfma_f32_16x16x32_bf16 v[48:51], v[160:163], a[132:135], v[48:51]\n" \
"s_add_u32 s49, s49, s55\n" \
"s_mov_b32 m0, s44\n" \
"s_nop 0\n" \
"buffer_load_dwordx4 v226, %[srdw], s49 offen offset:0 lds\n" \
"v_mfma_f32_16x16x32_bf16 v[48:51], v[164:167], a[148:151], v[48:51]\n" \
"ds_read_b64_tr_b16 v[132:133], v225 offset:24576\n" \
"v_mfma_f32_16x16x32_bf16 v[80:83], v[160:163], a[136:139], v[80:83]\n" \
"ds_read_b64_tr_b16 v[134:135], v225 offset:24832\n" \
"v_mfma_f32_16x16x32_bf16 v[80:83], v[164:167], a[152:155], v[80:83]\n" \
"v_mfma_f32_16x16x32_bf16 v[112:115], v[160:163], a[140:143], v[112:115]\n" \
"ds_read_b64_tr_b16 v[136:137], v225 offset:16392\n" \
"v_mfma_f32_16x16x32_bf16 v[112:115], v[164:167], a[156:159], v[112:115]\n" \
"ds_read_b64_tr_b16 v[138:139], v225 offset:16648\n" \
"v_mfma_f32_16x16x32_bf16 v[20:23], v[168:171], a[128:131], v[20:23]\n" \
"buffer_load_dwordx4 v216, %[srdw], s49 offen offset:1024 lds\n" \
"v_mfma_f32_16x16x32_bf16 v[20:23], v[172:175], a[144:147], v[20:23]\n" \
"ds_read_b64_tr_b16 v[140:141], v225 offset:24584\n" \
"v_mfma_f32_16x16x32_bf16 v[52:55], v[168:171], a[132:135], v[52:55]\n" \
"ds_read_b64_tr_b16 v[142:143], v225 offset:24840\n" \
"v_mfma_f32_16x16x32_bf16 v[52:55], v[172:175], a[148:151], v[52:55]\n" \
"v_mfma_f32_16x16x32_bf16 v[84:87], v[168:171], a[136:139], v[84:87]\n" \
"ds_read_b64_tr_b16 v[144:145], v225 offset:16896\n" \
"v_mfma_f32_16x16x32_bf16 v[84:87], v[172:175], a[152:155], v[84:87]\n" \
"ds_read_b64_tr_b16 v[146:147], v225 offset:17152\n" \
"v_mfma_f32_16x16x32_bf16 v[116:119], v[168:171], a[140:143], v[116:119]\n" \
"buffer_load_dwordx4 a[240:243], v227, %[srdz], s50 offen offset:3072 sc1\n" \
"v_mfma_f32_16x16x32_bf16 v[116:119], v[172:175], a[156:159], v[116:119]\n" \
"ds_read_b64_tr_b16 v[148:149], v225 offset:25088\n" \
"v_mfma_f32_16x16x32_bf16 v[24:27], v[176:179], a[128:131], v[24:27]\n" \
"ds_read_b64_tr_b16 v[150:151], v225 offset:25344\n" \
"v_mfma_f32_16x16x32_bf16 v[24:27], v[180:183], a[144:147], v[24:27]\n" \
"v_mfma_f32_16x16x32_bf16 v[56:59], v[176:179], a[132:135], v[56:59]\n" \
"ds_read_b64_tr_b16 v[152:153], v225 offset:16904\n" \
"v_mfma_f32_16x16x32_bf16 v[56:59], v[180:183], a[148:151], v[56:59]\n" \
"ds_read_b64_tr_b16 v[154:155], v225 offset:17160\n" \
"v_mfma_f32_16x16x32_bf16 v[88:91], v[176:179], a[136:139], v[88:91]\n" \
"buffer_load_dwordx4 a[244:247], v219, %[srdz], s50 offen offset:3072 sc1\n" \
"v_mfma_f32_16x16x32_bf16 v[88:91], v[180:183], a[152:155], v[88:91]\n" \
"ds_read_b64_tr_b16 v[156:157], v225 offset:25096\n" \
"v_mfma_f32_16x16x32_bf16 v[120:123], v[176:179], a[140:143], v[120:123]\n" \
"ds_read_b64_tr_b16 v[158:159], v225 offset:25352\n" \
"v_mfma_f32_16x16x32_bf16 v[120:123], v[180:183], a[156:159], v[120:123]\n" \
"v_mfma_f32_16x16x32_bf16 v[28:31], v[184:187], a[128:131], v[28:31]\n" \
"buffer_load_dwordx4 a[248:251], v220, %[srdz], s50 offen offset:3072 sc1\n" \
"v_mfma_f32_16x16x32_bf16 v[28:31], v[188:191], a[144:147], v[28:31]\n" \
"v_mfma_f32_16x16x32_bf16 v[60:63], v[184:187], a[132:135], v[60:63]\n" \
"v_mfma_f32_16x16x32_bf16 v[60:63], v[188:191], a[148:151], v[60:63]\n" \
"v_mfma_f32_16x16x32_bf16 v[92:95], v[184:187], a[136:139], v[92:95]\n" \
"buffer_load_dwordx4 a[252:255], v221, %[srdz], s50 offen offset:3072 sc1\n" \
"v_mfma_f32_16x16x32_bf16 v[92:95], v[188:191], a[152:155], v[92:95]\n" \
"v_mfma_f32_16x16x32_bf16 v[124:127], v[184:187], a[140:143], v[124:127]\n" \
"v_mfma_f32_16x16x32_bf16 v[124:127], v[188:191], a[156:159], v[124:127]\n" \
"s_waitcnt vmcnt(20) lgkmcnt(0)\n" \
"v_mfma_f32_16x16x32_bf16 v[0:3], v[128:131], a[160:163], v[0:3]\n" \
"ds_read_b64_tr_b16 v[160:161], v225 offset:17408\n" \
"v_mfma_f32_16x16x32_bf16 v[0:3], v[132:135], a[176:179], v[0:3]\n" \
"ds_read_b64_tr_b16 v[162:163], v225 offset:17664\n" \
"v_mfma_f32_16x16x32_bf16 v[32:35], v[128:131], a[164:167], v[32:35]\n" \
"s_mov_b32 m0, s44\n" \
"s_nop 0\n" \
"buffer_load_dwordx4 v217, %[srdw], s49 offen offset:2048 lds\n" \
"v_mfma_f32_16x16x32_bf16 v[32:35], v[132:135], a[180:183], v[32:35]\n" \
"ds_read_b64_tr_b16 v[164:165], v225 offset:25600\n" \
"v_mfma_f32_16x16x32_bf16 v[64:67], v[128:131], a[168:171], v[64:67]\n" \
"ds_read_b64_tr_b16 v[166:167], v225 offset:25856\n" \
"v_mfma_f32_16x16x32_bf16 v[64:67], v[132:135], a[184:187], v[64:67]\n" \
"v_mfma_f32_16x16x32_bf16 v[96:99], v[128:131], a[172:175], v[96:99]\n" \
"ds_read_b64_tr_b16 v[168:169], v225 offset:17416\n" \
"v_mfma_f32_16x16x32_bf16 v[96:99], v[132:135], a[188:191], v[96:99]\n" \
"ds_read_b64_tr_b16 v[170:171], v225 offset:17672\n" \
"v_mfma_f32_16x16x32_bf16 v[4:7], v[136:139], a[160:163], v[4:7]\n" \
"buffer_load_dwordx4 v218, %[srdw], s49 offen offset:3072 lds\n" \
"v_mfma_f32_16x16x32_bf16 v[4:7], v[140:143], a[176:179], v[4:7]\n" \
"ds_read_b64_tr_b16 v[172:173], v225 offset:25608\n" \
"v_mfma_f32_16x16x32_bf16 v[36:39], v[136:139], a[164:167], v[36:39]\n" \
"ds_read_b64_tr_b16 v[174:175], v225 offset:25864\n" \
"v_mfma_f32_16x16x32_bf16 v[36:39], v[140:143], a[180:183], v[36:39]\n" \
"v_mfma_f32_16x16x32_bf16 v[68:71], v[136:139], a[168:171], v[68:71]\n" \
"ds_read_b64_tr_b16 v[176:177], v225 offset:17920\n" \
"v_mfma_f32_16x16x32_bf16 v[68:71], v[140:143], a[184:187], v[68:71]\n" \
"ds_read_b64_tr_b16 v[178:179], v225 offset:18176\n" \
"v_mfma_f32_16x16x32_bf16 v[100:103], v[136:139], a[172:175], v[100:103]\n" \
"s_add_u32 s50, s50, s53\n" \
"buffer_load_dwordx4 a[128:131], v227, %[srdz], s50 offen offset:0 sc1\n" \
"v_mfma_f32_16x16x32_bf16 v[100:103], v[140:143], a[188:191], v[100:103]\n" \
"ds_read_b64_tr_b16 v[180:181], v225 offset:26112\n" \
"v_mfma_f32_16x16x32_bf16 v[8:11], v[144:147], a[160:163], v[8:11]\n" \
"ds_read_b64_tr_b16 v[182:183], v225 offset:26368\n" \
"v_mfma_f32_16x16x32_bf16 v[8:11], v[148:151], a[176:179], v[8:11]\n" \
"v_mfma_f32_16x16x32_bf16 v[40:43], v[144:147], a[164:167], v[40:43]\n" \
"ds_read_b64_tr_b16 v[184:185], v225 offset:17928\n" \
"v_mfma_f32_16x16x32_bf16 v[40:43], v[148:151], a[180:183], v[40:43]\n" \
"ds_read_b64_tr_b16 v[186:187], v225 offset:18184\n" \
"v_mfma_f32_16x16x32_bf16 v[72:75], v[144:147], a[168:171], v[72:75]\n" \
"buffer_load_dwordx4 a[132:135], v219, %[srdz], s50 offen offset:0 sc1\n" \
"v_mfma_f32_16x16x32_bf16 v[72:75], v[148:151], a[184:187], v[72:75]\n" \
"ds_read_b64_tr_b16 v[188:189], v225 offset:26120\n" \
"v_mfma_f32_16x16x32_bf16 v[104:107], v[144:147], a[172:175], v[104:107]\n" \
"ds_read_b64_tr_b16 v[190:191], v225 offset:26376\n" \
"v_mfma_f32_16x16x32_bf16 v[104:107], v[148:151], a[188:191], v[104:107]\n" \
"v_mfma_f32_16x16x32_bf16 v[12:15], v[152:155], a[160:163], v[12:15]\n" \
"buffer_load_dwordx4 a[136:139], v220, %[srdz], s50 offen offset:0 sc1\n" \
"v_mfma_f32_16x16x32_bf16 v[12:15], v[156:159], a[176:179], v[12:15]\n" \
"v_mfma_f32_16x16x32_bf16 v[44:47], v[152:155], a[164:167], v[44:47]\n" \
"v_mfma_f32_16x16x32_bf16 v[44:47], v[156:159], a[180:183], v[44:47]\n" \
"v_mfma_f32_16x16x32_bf16 v[76:79], v[152:155], a[168:171], v[76:79]\n" \
"buffer_load_dwordx4 a[140:143], v221, %[srdz], s50 offen offset:0 sc1\n" \
"v_mfma_f32_16x16x32_bf16 v[76:79], v[156:159], a[184:187], v[76:79]\n" \
"v_mfma_f32_16x16x32_bf16 v[108:111], v[152:155], a[172:175], v[108:111]\n" \
"v_mfma_f32_16x16x32_bf16 v[108:111], v[156:159], a[188:191], v[108:111]\n" \
"s_waitcnt vmcnt(29) lgkmcnt(0)\n" \
"s_barrier\n" \
"v_mfma_f32_16x16x32_bf16 v[16:19], v[160:163], a[160:163], v[16:19]\n" \
"ds_read_b64_tr_b16 v[128:129], v225 offset:32768\n" \
"v_mfma_f32_16x16x32_bf16 v[16:19], v[164:167], a[176:179], v[16:19]\n" \
"ds_read_b64_tr_b16 v[130:131], v225 offset:33024\n" \
"v_mfma_f32_16x16x32_bf16 v[48:51], v[160:163], a[164:167], v[48:51]\n" \
"s_add_u32 s49, s49, s55\n" \
"s_mov_b32 m0, s45\n" \
"s_nop 0\n" \
"buffer_load_dwordx4 v226, %[srdw], s49 offen offset:0 lds\n" \
"v_mfma_f32_16x16x32_bf16 v[48:51], v[164:167], a[180:183], v[48:51]\n" \
"ds_read_b64_tr_b16 v[132:133], v225 offset:40960\n" \
"v_mfma_f32_16x16x32_bf16 v[80:83], v[160:163], a[168:171], v[80:83]\n" \
"ds_read_b64_tr_b16 v[134:135], v225 offset:41216\n" \
"v_mfma_f32_16x16x32_bf16 v[80:83], v[164:167], a[184:187], v[80:83]\n" \
"v_mfma_f32_16x16x32_bf16 v[112:115], v[160:163], a[172:175], v[112:115]\n" \
"ds_read_b64_tr_b16 v[136:137], v225 offset:32776\n" \
"v_mfma_f32_16x16x32_bf16 v[112:115], v[164:167], a[188:191], v[112:115]\n" \
"ds_read_b64_tr_b16 v[138:139], v225 offset:33032\n" \
"v_mfma_f32_16x16x32_bf16 v[20:23], v[168:171], a[160:163], v[20:23]\n" \
"buffer_load_dwordx4 v216, %[srdw], s49 offen offset:1024 lds\n" \
"v_mfma_f32_16x16x32_bf16 v[20:23], v[172:175], a[176:179], v[20:23]\n" \
"ds_read_b64_tr_b16 v[140:141], v225 offset:40968\n" \
"v_mfma_f32_16x16x32_bf16 v[52:55], v[168:171], a[164:167], v[52:55]\n" \
"ds_read_b64_tr_b16 v[142:143], v225 offset:41224\n" \
"v_mfma_f32_16x16x32_bf16 v[52:55], v[172:175], a[180:183], v[52:55]\n" \
"v_mfma_f32_16x16x32_bf16 v[84:87], v[168:171], a[168:171], v[84:87]\n" \
"ds_read_b64_tr_b16 v[144:145], v225 offset:33280\n" \
"v_mfma_f32_16x16x32_bf16 v[84:87], v[172:175], a[184:187], v[84:87]\n" \
"ds_read_b64_tr_b16 v[146:147], v225 offset:33536\n" \
"v_mfma_f32_16x16x32_bf16 v[116:119], v[168:171], a[172:175], v[116:119]\n" \
"buffer_load_dwordx4 a[144:147], v227, %[srdz], s50 offen offset:1024 sc1\n" \
"v_mfma_f32_16x16x32_bf16 v[116:119], v[172:175], a[188:191], v[116:119]\n" \
"ds_read_b64_tr_b16 v[148:149], v225 offset:41472\n" \
"v_mfma_f32_16x16x32_bf16 v[24:27], v[176:179], a[160:163], v[24:27]\n" \
"ds_read_b64_tr_b16 v[150:151], v225 offset:41728\n" \
"v_mfma_f32_16x16x32_bf16 v[24:27], v[180:183], a[176:179], v[24:27]\n" \
"v_mfma_f32_16x16x32_bf16 v[56:59], v[176:179], a[164:167], v[56:59]\n" \
"ds_read_b64_tr_b16 v[152:153], v225 offset:33288\n" \
"v_mfma_f32_16x16x32_bf16 v[56:59], v[180:183], a[180:183], v[56:59]\n" \
"ds_read_b64_tr_b16 v[154:155], v225 offset:33544\n" \
"v_mfma_f32_16x16x32_bf16 v[88:91], v[176:179], a[168:171], v[88:91]\n" \
"buffer_load_dwordx4 a[148:151], v219, %[srdz], s50 offen offset:1024 sc1\n" \
"v_mfma_f32_16x16x32_bf16 v[88:91], v[180:183], a[184:187], v[88:91]\n" \
"ds_read_b64_tr_b16 v[156:157], v225 offset:41480\n" \
"v_mfma_f32_16x16x32_bf16 v[120:123], v[176:179], a[172:175], v[120:123]\n" \
"ds_read_b64_tr_b16 v[158:159], v225 offset:41736\n" \
"v_mfma_f32_16x16x32_bf16 v[120:123], v[180:183], a[188:191], v[120:123]\n" \
"v_mfma_f32_16x16x32_bf16 v[28:31], v[184:187], a[160:163], v[28:31]\n" \
"buffer_load_dwordx4 a[152:155], v220, %[srdz], s50 offen offset:1024 sc1\n" \
"v_mfma_f32_16x16x32_bf16 v[28:31], v[188:191], a[176:179], v[28:31]\n" \
"v_mfma_f32_16x16x32_bf16 v[60:63], v[184:187], a[164:167], v[60:63]\n" \
"v_mfma_f32_16x16x32_bf16 v[60:63], v[188:191], a[180:183], v[60:63]\n" \
"v_mfma_f32_16x16x32_bf16 v[92:95], v[184:187], a[168:171], v[92:95]\n" \
"buffer_load_dwordx4 a[156:159], v221, %[srdz], s50 offen offset:1024 sc1\n" \
"v_mfma_f32_16x16x32_bf16 v[92:95], v[188:191], a[184:187], v[92:95]\n" \
"v_mfma_f32_16x16x32_bf16 v[124:127], v[184:187], a[172:175], v[124:127]\n" \
"v_mfma_f32_16x16x32_bf16 v[124:127], v[188:191], a[188:191], v[124:127]\n" \
"s_waitcnt vmcnt(24) lgkmcnt(0)\n" \
"v_mfma_f32_16x16x32_bf16 v[0:3], v[128:131], a[192:195], v[0:3]\n" \
"ds_read_b64_tr_b16 v[160:161], v225 offset:33792\n" \
"v_mfma_f32_16x16x32_bf16 v[0:3], v[132:135], a[208:211], v[0:3]\n" \
"ds_read_b64_tr_b16 v[162:163], v225 offset:34048\n" \
"v_mfma_f32_16x16x32_bf16 v[32:35], v[128:131], a[196:199], v[32:35]\n" \
"s_mov_b32 m0, s45\n" \
"s_nop 0\n" \
"buffer_load_dwordx4 v217, %[srdw], s49 offen offset:2048 lds\n" \
"v_mfma_f32_16x16x32_bf16 v[32:35], v[132:135], a[212:215], v[32:35]\n" \
"ds_read_b64_tr_b16 v[164:165], v225 offset:41984\n" \
"v_mfma_f32_16x16x32_bf16 v[64:67], v[128:131], a[200:203], v[64:67]\n" \
"ds_read_b64_tr_b16 v[166:167], v225 offset:42240\n" \
"v_mfma_f32_16x16x32_bf16 v[64:67], v[132:135], a[216:219], v[64:67]\n" \
"v_mfma_f32_16x16x32_bf16 v[96:99], v[128:131], a[204:207], v[96:99]\n" \
"ds_read_b64_tr_b16 v[168:169], v225 offset:33800\n" \
"v_mfma_f32_16x16x32_bf16 v[96:99], v[132:135], a[220:223], v[96:99]\n" \
"ds_read_b64_tr_b16 v[170:171], v225 offset:34056\n" \
"v_mfma_f32_16x16x32_bf16 v[4:7], v[136:139], a[192:195], v[4:7]\n" \
"buffer_load_dwordx4 v218, %[srdw], s49 offen offset:3072 lds\n" \
"v_mfma_f32_16x16x32_bf16 v[4:7], v[140:143], a[208:211], v[4:7]\n" \
"ds_read_b64_tr_b16 v[172:173], v225 offset:41992\n" \
"v_mfma_f32_16x16x32_bf16 v[36:39], v[136:139], a[196:199], v[36:39]\n" \
"ds_read_b64_tr_b16 v[174:175], v225 offset:42248\n" \
"v_mfma_f32_16x16x32_bf16 v[36:39], v[140:143], a[212:215], v[36:39]\n" \
"v_mfma_f32_16x16x32_bf16 v[68:71], v[136:139], a[200:203], v[68:71]\n" \
"ds_read_b64_tr_b16 v[176:177], v225 offset:34304\n" \
"v_mfma_f32_16x16x32_bf16 v[68:71], v[140:143], a[216:219], v[68:71]\n" \
"ds_read_b64_tr_b16 v[178:179], v225 offset:34560\n" \
"v_mfma_f32_16x16x32_bf16 v[100:103], v[136:139], a[204:207], v[100:103]\n" \
"buffer_load_dwordx4 a[160:163], v227, %[srdz], s50 offen offset:2048 sc1\n" \
"v_mfma_f32_16x16x32_bf16 v[100:103], v[140:143], a[220:223], v[100:103]\n" \
"ds_read_b64_tr_b16 v[180:181], v225 offset:42496\n" \
"v_mfma_f32_16x16x32_bf16 v[8:11], v[144:147], a[192:195], v[8:11]\n" \
"ds_read_b64_tr_b16 v[182:183], v225 offset:42752\n" \
"v_mfma_f32_16x16x32_bf16 v[8:11], v[148:151], a[208:211], v[8:11]\n" \
"v_mfma_f32_16x16x32_bf16 v[40:43], v[144:147], a[196:199], v[40:43]\n" \
"ds_read_b64_tr_b16 v[184:185], v225 offset:34312\n" \
"v_mfma_f32_16x16x32_bf16 v[40:43], v[148:151], a[212:215], v[40:43]\n" \
"ds_read_b64_tr_b16 v[186:187], v225 offset:34568\n" \
"v_mfma_f32_16x16x32_bf16 v[72:75], v[144:147], a[200:203], v[72:75]\n" \
"buffer_load_dwordx4 a[164:167], v219, %[srdz], s50 offen offset:2048 sc1\n" \
"v_mfma_f32_16x16x32_bf16 v[72:75], v[148:151], a[216:219], v[72:75]\n" \
"ds_read_b64_tr_b16 v[188:189], v225 offset:42504\n" \
"v_mfma_f32_16x16x32_bf16 v[104:107], v[144:147], a[204:207], v[104:107]\n" \
"ds_read_b64_tr_b16 v[190:191], v225 offset:42760\n" \
"v_mfma_f32_16x16x32_bf16 v[104:107], v[148:151], a[220:223], v[104:107]\n" \
"v_mfma_f32_16x16x32_bf16 v[12:15], v[152:155], a[192:195], v[12:15]\n" \
"buffer_load_dwordx4 a[168:171], v220, %[srdz], s50 offen offset:2048 sc1\n" \
"v_mfma_f32_16x16x32_bf16 v[12:15], v[156:159], a[208:211], v[12:15]\n" \
"v_mfma_f32_16x16x32_bf16 v[44:47], v[152:155], a[196:199], v[44:47]\n" \
"v_mfma_f32_16x16x32_bf16 v[44:47], v[156:159], a[212:215], v[44:47]\n" \
"v_mfma_f32_16x16x32_bf16 v[76:79], v[152:155], a[200:203], v[76:79]\n" \
"buffer_load_dwordx4 a[172:175], v221, %[srdz], s50 offen offset:2048 sc1\n" \
"v_mfma_f32_16x16x32_bf16 v[76:79], v[156:159], a[216:219], v[76:79]\n" \
"v_mfma_f32_16x16x32_bf16 v[108:111], v[152:155], a[204:207], v[108:111]\n" \
"v_mfma_f32_16x16x32_bf16 v[108:111], v[156:159], a[220:223], v[108:111]\n" \
"s_waitcnt vmcnt(28) lgkmcnt(0)\n" \
"s_barrier\n" \
"v_mfma_f32_16x16x32_bf16 v[16:19], v[160:163], a[192:195], v[16:19]\n" \
"ds_read_b64_tr_b16 v[128:129], v225 offset:49152\n" \
"v_mfma_f32_16x16x32_bf16 v[16:19], v[164:167], a[208:211], v[16:19]\n" \
"ds_read_b64_tr_b16 v[130:131], v225 offset:49408\n" \
"v_mfma_f32_16x16x32_bf16 v[48:51], v[160:163], a[196:199], v[48:51]\n" \
"s_add_u32 s49, s49, s55\n" \
"s_mov_b32 m0, s46\n" \
"s_nop 0\n" \
"buffer_load_dwordx4 v226, %[srdw], s49 offen offset:0 lds\n" \
"v_mfma_f32_16x16x32_bf16 v[48:51], v[164:167], a[212:215], v[48:51]\n" \
"ds_read_b64_tr_b16 v[132:133], v225 offset:57344\n" \
"v_mfma_f32_16x16x32_bf16 v[80:83], v[160:163], a[200:203], v[80:83]\n" \
"ds_read_b64_tr_b16 v[134:135], v225 offset:57600\n" \
"v_mfma_f32_16x16x32_bf16 v[80:83], v[164:167], a[216:219], v[80:83]\n" \
"v_mfma_f32_16x16x32_bf16 v[112:115], v[160:163], a[204:207], v[112:115]\n" \
"ds_read_b64_tr_b16 v[136:137], v225 offset:49160\n" \
"v_mfma_f32_16x16x32_bf16 v[112:115], v[164:167], a[220:223], v[112:115]\n" \
"ds_read_b64_tr_b16 v[138:139], v225 offset:49416\n" \
"v_mfma_f32_16x16x32_bf16 v[20:23], v[168:171], a[192:195], v[20:23]\n" \
"buffer_load_dwordx4 v216, %[srdw], s49 offen offset:1024 lds\n" \
"v_mfma_f32_16x16x32_bf16 v[20:23], v[172:175], a[208:211], v[20:23]\n" \
"ds_read_b64_tr_b16 v[140:141], v225 offset:57352\n" \
"v_mfma_f32_16x16x32_bf16 v[52:55], v[168:171], a[196:199], v[52:55]\n" \
"ds_read_b64_tr_b16 v[142:143], v225 offset:57608\n" \
"v_mfma_f32_16x16x32_bf16 v[52:55], v[172:175], a[212:215], v[52:55]\n" \
"v_mfma_f32_16x16x32_bf16 v[84:87], v[168:171], a[200:203], v[84:87]\n" \
"ds_read_b64_tr_b16 v[144:145], v225 offset:49664\n" \
"v_mfma_f32_16x16x32_bf16 v[84:87], v[172:175], a[216:219], v[84:87]\n" \
"ds_read_b64_tr_b16 v[146:147], v225 offset:49920\n" \
"v_mfma_f32_16x16x32_bf16 v[116:119], v[168:171], a[204:207], v[116:119]\n" \
"buffer_load_dwordx4 a[176:179], v227, %[srdz], s50 offen offset:3072 sc1\n" \
"v_mfma_f32_16x16x32_bf16 v[116:119], v[172:175], a[220:223], v[116:119]\n" \
"ds_read_b64_tr_b16 v[148:149], v225 offset:57856\n" \
"v_mfma_f32_16x16x32_bf16 v[24:27], v[176:179], a[192:195], v[24:27]\n" \
"ds_read_b64_tr_b16 v[150:151], v225 offset:58112\n" \
"v_mfma_f32_16x16x32_bf16 v[24:27], v[180:183], a[208:211], v[24:27]\n" \
"v_mfma_f32_16x16x32_bf16 v[56:59], v[176:179], a[196:199], v[56:59]\n" \
"ds_read_b64_tr_b16 v[152:153], v225 offset:49672\n" \
"v_mfma_f32_16x16x32_bf16 v[56:59], v[180:183], a[212:215], v[56:59]\n" \
"ds_read_b64_tr_b16 v[154:155], v225 offset:49928\n" \
"v_mfma_f32_16x16x32_bf16 v[88:91], v[176:179], a[200:203], v[88:91]\n" \
"buffer_load_dwordx4 a[180:183], v219, %[srdz], s50 offen offset:3072 sc1\n" \
"v_mfma_f32_16x16x32_bf16 v[88:91], v[180:183], a[216:219], v[88:91]\n" \
"ds_read_b64_tr_b16 v[156:157], v225 offset:57864\n" \
"v_mfma_f32_16x16x32_bf16 v[120:123], v[176:179], a[204:207], v[120:123]\n" \
"ds_read_b64_tr_b16 v[158:159], v225 offset:58120\n" \
"v_mfma_f32_16x16x32_bf16 v[120:123], v[180:183], a[220:223], v[120:123]\n" \
"v_mfma_f32_16x16x32_bf16 v[28:31], v[184:187], a[192:195], v[28:31]\n" \
"buffer_load_dwordx4 a[184:187], v220, %[srdz], s50 offen offset:3072 sc1\n" \
"v_mfma_f32_16x16x32_bf16 v[28:31], v[188:191], a[208:211], v[28:31]\n" \
"v_mfma_f32_16x16x32_bf16 v[60:63], v[184:187], a[196:199], v[60:63]\n" \
"v_mfma_f32_16x16x32_bf16 v[60:63], v[188:191], a[212:215], v[60:63]\n" \
"v_mfma_f32_16x16x32_bf16 v[92:95], v[184:187], a[200:203], v[92:95]\n" \
"buffer_load_dwordx4 a[188:191], v221, %[srdz], s50 offen offset:3072 sc1\n" \
"v_mfma_f32_16x16x32_bf16 v[92:95], v[188:191], a[216:219], v[92:95]\n" \
"v_mfma_f32_16x16x32_bf16 v[124:127], v[184:187], a[204:207], v[124:127]\n" \
"v_mfma_f32_16x16x32_bf16 v[124:127], v[188:191], a[220:223], v[124:127]\n" \
"s_waitcnt vmcnt(24) lgkmcnt(0)\n" \
"v_mfma_f32_16x16x32_bf16 v[0:3], v[128:131], a[224:227], v[0:3]\n" \
"ds_read_b64_tr_b16 v[160:161], v225 offset:50176\n" \
"v_mfma_f32_16x16x32_bf16 v[0:3], v[132:135], a[240:243], v[0:3]\n" \
"ds_read_b64_tr_b16 v[162:163], v225 offset:50432\n" \
"v_mfma_f32_16x16x32_bf16 v[32:35], v[128:131], a[228:231], v[32:35]\n" \
"s_mov_b32 m0, s46\n" \
"s_nop 0\n" \
"buffer_load_dwordx4 v217, %[srdw], s49 offen offset:2048 lds\n" \
"v_mfma_f32_16x16x32_bf16 v[32:35], v[132:135], a[244:247], v[32:35]\n" \
"ds_read_b64_tr_b16 v[164:165], v225 offset:58368\n" \
"v_mfma_f32_16x16x32_bf16 v[64:67], v[128:131], a[232:235], v[64:67]\n" \
"ds_read_b64_tr_b16 v[166:167], v225 offset:58624\n" \
"v_mfma_f32_16x16x32_bf16 v[64:67], v[132:135], a[248:251], v[64:67]\n" \
"v_mfma_f32_16x16x32_bf16 v[96:99], v[128:131], a[236:239], v[96:99]\n" \
"ds_read_b64_tr_b16 v[168:169], v225 offset:50184\n" \
"v_mfma_f32_16x16x32_bf16 v[96:99], v[132:135], a[252:255], v[96:99]\n" \
"ds_read_b64_tr_b16 v[170:171], v225 offset:50440\n" \
"v_mfma_f32_16x16x32_bf16 v[4:7], v[136:139], a[224:227], v[4:7]\n" \
"buffer_load_dwordx4 v218, %[srdw], s49 offen offset:3072 lds\n" \
"v_mfma_f32_16x16x32_bf16 v[4:7], v[140:143], a[240:243], v[4:7]\n" \
"ds_read_b64_tr_b16 v[172:173], v225 offset:58376\n" \
"v_mfma_f32_16x16x32_bf16 v[36:39], v[136:139], a[228:231], v[36:39]\n" \
"ds_read_b64_tr_b16 v[174:175], v225 offset:58632\n" \
"v_mfma_f32_16x16x32_bf16 v[36:39], v[140:143], a[244:247], v[36:39]\n" \
"v_mfma_f32_16x16x32_bf16 v[68:71], v[136:139], a[232:235], v[68:71]\n" \
"ds_read_b64_tr_b16 v[176:177], v225 offset:50688\n" \
"v_mfma_f32_16x16x32_bf16 v[68:71], v[140:143], a[248:251], v[68:71]\n" \
"ds_read_b64_tr_b16 v[178:179], v225 offset:50944\n" \
"v_mfma_f32_16x16x32_bf16 v[100:103], v[136:139], a[236:239], v[100:103]\n" \
"s_add_u32 s50, s50, s53\n" \
"buffer_load_dwordx4 a[192:195], v227, %[srdz], s50 offen offset:0 sc1\n" \
"v_mfma_f32_16x16x32_bf16 v[100:103], v[140:143], a[252:255], v[100:103]\n" \
"ds_read_b64_tr_b16 v[180:181], v225 offset:58880\n" \
"v_mfma_f32_16x16x32_bf16 v[8:11], v[144:147], a[224:227], v[8:11]\n" \
"ds_read_b64_tr_b16 v[182:183], v225 offset:59136\n" \
"v_mfma_f32_16x16x32_bf16 v[8:11], v[148:151], a[240:243], v[8:11]\n" \
"v_mfma_f32_16x16x32_bf16 v[40:43], v[144:147], a[228:231], v[40:43]\n" \
"ds_read_b64_tr_b16 v[184:185], v225 offset:50696\n" \
"v_mfma_f32_16x16x32_bf16 v[40:43], v[148:151], a[244:247], v[40:43]\n" \
"ds_read_b64_tr_b16 v[186:187], v225 offset:50952\n" \
"v_mfma_f32_16x16x32_bf16 v[72:75], v[144:147], a[232:235], v[72:75]\n" \
"buffer_load_dwordx4 a[196:199], v219, %[srdz], s50 offen offset:0 sc1\n" \
"v_mfma_f32_16x16x32_bf16 v[72:75], v[148:151], a[248:251], v[72:75]\n" \
"ds_read_b64_tr_b16 v[188:189], v225 offset:58888\n" \
"v_mfma_f32_16x16x32_bf16 v[104:107], v[144:147], a[236:239], v[104:107]\n" \
"ds_read_b64_tr_b16 v[190:191], v225 offset:59144\n" \
"v_mfma_f32_16x16x32_bf16 v[104:107], v[148:151], a[252:255], v[104:107]\n" \
"v_mfma_f32_16x16x32_bf16 v[12:15], v[152:155], a[224:227], v[12:15]\n" \
"buffer_load_dwordx4 a[200:203], v220, %[srdz], s50 offen offset:0 sc1\n" \
"v_mfma_f32_16x16x32_bf16 v[12:15], v[156:159], a[240:243], v[12:15]\n" \
"v_mfma_f32_16x16x32_bf16 v[44:47], v[152:155], a[228:231], v[44:47]\n" \
"buffer_load_dword v237, v236, %[srdw], s60 offen\n" \
"s_add_u32 s60, s60, 0x200000\n" \
"v_mfma_f32_16x16x32_bf16 v[44:47], v[156:159], a[244:247], v[44:47]\n" \
"v_mfma_f32_16x16x32_bf16 v[76:79], v[152:155], a[232:235], v[76:79]\n" \
"buffer_load_dwordx4 a[204:207], v221, %[srdz], s50 offen offset:0 sc1\n" \
"v_mfma_f32_16x16x32_bf16 v[76:79], v[156:159], a[248:251], v[76:79]\n" \
".if . - adaln_body != 4096\n" \
".error \"size model\"\n" \
".endif\n" \
"Lk0_%=:\n" \
"s_cbranch_execz Lk1_%=\n" \
"s_nop 0\n" \
"v_mfma_f32_16x16x32_bf16 v[108:111], v[152:155], a[236:239], v[108:111]\n" \
"v_mfma_f32_16x16x32_bf16 v[108:111], v[156:159], a[252:255], v[108:111]\n" \
"s_waitcnt vmcnt(29) lgkmcnt(0)\n" \
"s_barrier\n" \
"v_mfma_f32_16x16x32_bf16 v[16:19], v[160:163], a[224:227], v[16:19]\n" \
"ds_read_b64_tr_b16 v[128:129], v225 offset:0\n" \
"v_mfma_f32_16x16x32_bf16 v[16:19], v[164:167], a[240:243], v[16:19]\n" \
"ds_read_b64_tr_b16 v[130:131], v225 offset:256\n" \
"v_mfma_f32_16x16x32_bf16 v[48:51], v[160:163], a[228:231], v[48:51]\n" \
"s_add_u32 s49, s49, s55\n" \
"s_mov_b32 m0, s47\n" \
"s_nop 0\n" \
"buffer_load_dwordx4 v226, %[srdw], s49 offen offset:0 lds\n" \
"v_mfma_f32_16x16x32_bf16 v[48:51], v[164:167], a[244:247], v[48:51]\n" \
"ds_read_b64_tr_b16 v[132:133], v225 offset:8192\n" \
"v_mfma_f32_16x16x32_bf16 v[80:83], v[160:163], a[232:235], v[80:83]\n" \
"ds_read_b64_tr_b16 v[134:135], v225 offset:8448\n" \
"v_mfma_f32_16x16x32_bf16 v[80:83], v[164:167], a[248:251], v[80:83]\n" \
"v_mfma_f32_16x16x32_bf16 v[112:115], v[160:163], a[236:239], v[112:115]\n" \
"ds_read_b64_tr_b16 v[136:137], v225 offset:8\n" \
"v_mfma_f32_16x16x32_bf16 v[112:115], v[164:167], a[252:255], v[112:115]\n" \
"ds_read_b64_tr_b16 v[138:139], v225 offset:264\n" \
"v_mfma_f32_16x16x32_bf16 v[20:23], v[168:171], a[224:227], v[20:23]\n" \
"buffer_load_dwordx4 v216, %[srdw], s49 offen offset:1024 lds\n" \
"v_mfma_f32_16x16x32_bf16 v[20:23], v[172:175], a[240:243], v[20:23]\n" \
"ds_read_b64_tr_b16 v[140:141], v225 offset:8200\n" \
"v_mfma_f32_16x16x32_bf16 v[52:55], v[168:171], a[228:231], v[52:55]\n" \
"ds_read_b64_tr_b16 v[142:143], v225 offset:8456\n" \
"v_mfma_f32_16x16x32_bf16 v[52:55], v[172:175], a[244:247], v[52:55]\n" \
"v_mfma_f32_16x16x32_bf16 v[84:87], v[168:171], a[232:235], v[84:87]\n" \
"ds_read_b64_tr_b16 v[144:145], v225 offset:512\n" \
"v_mfma_f32_16x16x32_bf16 v[84:87], v[172:175], a[248:251], v[84:87]\n" \
"ds_read_b64_tr_b16 v[146:147], v225 offset:768\n" \
"v_mfma_f32_16x16x32_bf16 v[116:119], v[168:171], a[236:239], v[116:119]\n" \
"buffer_load_dwordx4 a[208:211], v227, %[srdz], s50 offen offset:1024 sc1\n" \
"v_mfma_f32_16x16x32_bf16 v[116:119], v[172:175], a[252:255], v[116:119]\n" \
"ds_read_b64_tr_b16 v[148:149], v225 offset:8704\n" \
"v_mfma_f32_16x16x32_bf16 v[24:27], v[176:179], a[224:227], v[24:27]\n" \
"ds_read_b64_tr_b16 v[150:151], v225 offset:8960\n" \
"v_mfma_f32_16x16x32_bf16 v[24:27], v[180:183], a[240:243], v[24:27]\n" \
"v_mfma_f32_16x16x32_bf16 v[56:59], v[176:179], a[228:231], v[56:59]\n" \
"ds_read_b64_tr_b16 v[152:153], v225 offset:520\n" \
"v_mfma_f32_16x16x32_bf16 v[56:59], v[180:183], a[244:247], v[56:59]\n" \
"ds_read_b64_tr_b16 v[154:155], v225 offset:776\n" \
"v_mfma_f32_16x16x32_bf16 v[88:91], v[176:179], a[232:235], v[88:91]\n" \
"buffer_load_dwordx4 a[212:215], v219, %[srdz], s50 offen offset:1024 sc1\n" \
"v_mfma_f32_16x16x32_bf16 v[88:91], v[180:183], a[248:251], v[88:91]\n" \
"ds_read_b64_tr_b16 v[156:157], v225 offset:8712\n" \
"v_mfma_f32_16x16x32_bf16 v[120:123], v[176:179], a[236:239], v[120:123]\n" \
"ds_read_b64_tr_b16 v[158:159], v225 offset:8968\n" \
"v_mfma_f32_16x16x32_bf16 v[120:123], v[180:183], a[252:255], v[120:123]\n" \
"v_mfma_f32_16x16x32_bf16 v[28:31], v[184:187], a[224:227], v[28:31]\n" \
"buffer_load_dwordx4 a[216:219], v220, %[srdz], s50 offen offset:1024 sc1\n" \
"v_mfma_f32_16x16x32_bf16 v[28:31], v[188:191], a[240:243], v[28:31]\n" \
"v_mfma_f32_16x16x32_bf16 v[60:63], v[184:187], a[228:231], v[60:63]\n" \
"v_mfma_f32_16x16x32_bf16 v[60:63], v[188:191], a[244:247], v[60:63]\n" \
"v_mfma_f32_16x16x32_bf16 v[92:95], v[184:187], a[232:235], v[92:95]\n" \
"buffer_load_dwordx4 a[220:223], v221, %[srdz], s50 offen offset:1024 sc1\n" \
"v_mfma_f32_16x16x32_bf16 v[92:95], v[188:191], a[248:251], v[92:95]\n" \
"v_mfma_f32_16x16x32_bf16 v[124:127], v[184:187], a[236:239], v[124:127]\n" \
"v_mfma_f32_16x16x32_bf16 v[124:127], v[188:191], a[252:255], v[124:127]\n" \
"s_waitcnt vmcnt(25) lgkmcnt(0)\n" \
"s_sub_u32 s51, s51, 1\n" \
"s_cmp_lg_u32 s51, 0\n" \
"s_cbranch_scc1 Lq_%=\n" \
"s_nop 0\n" \
"v_mfma_f32_16x16x32_bf16 v[0:3], v[128:131], a[128:131], v[0:3]\n" \
"ds_read_b64_tr_b16 v[160:161], v225 offset:1024\n" \
"v_mfma_f32_16x16x32_bf16 v[0:3], v[132:135], a[144:147], v[0:3]\n" \
"ds_read_b64_tr_b16 v[162:163], v225 offset:1280\n" \
"v_mfma_f32_16x16x32_bf16 v[32:35], v[128:131], a[132:135], v[32:35]\n" \
"s_mov_b32 m0, s47\n" \
"s_nop 0\n" \
"buffer_load_dwordx4 v217, %[srdw], s49 offen offset:2048 lds\n" \
"v_mfma_f32_16x16x32_bf16 v[32:35], v[132:135], a[148:151], v[32:35]\n" \
"ds_read_b64_tr_b16 v[164:165], v225 offset:9216\n" \
"v_mfma_f32_16x16x32_bf16 v[64:67], v[128:131], a[136:139], v[64:67]\n" \
"ds_read_b64_tr_b16 v[166:167], v225 offset:9472\n" \
"v_mfma_f32_16x16x32_bf16 v[64:67], v[132:135], a[152:155], v[64:67]\n" \
"v_mfma_f32_16x16x32_bf16 v[96:99], v[128:131], a[140:143], v[96:99]\n" \
"ds_read_b64_tr_b16 v[168:169], v225 offset:1032\n" \
"v_mfma_f32_16x16x32_bf16 v[96:99], v[132:135], a[156:159], v[96:99]\n" \
"ds_read_b64_tr_b16 v[170:171], v225 offset:1288\n" \
"v_mfma_f32_16x16x32_bf16 v[4:7], v[136:139], a[128:131], v[4:7]\n" \
"buffer_load_dwordx4 v218, %[srdw], s49 offen offset:3072 lds\n" \
"v_mfma_f32_16x16x32_bf16 v[4:7], v[140:143], a[144:147], v[4:7]\n" \
"ds_read_b64_tr_b16 v[172:173], v225 offset:9224\n" \
"v_mfma_f32_16x16x32_bf16 v[36:39], v[136:139], a[132:135], v[36:39]\n" \
"ds_read_b64_tr_b16 v[174:175], v225 offset:9480\n" \
"v_mfma_f32_16x16x32_bf16 v[36:39], v[140:143], a[148:151], v[36:39]\n" \
"v_mfma_f32_16x16x32_bf16 v[68:71], v[136:139], a[136:139], v[68:71]\n" \
"ds_read_b64_tr_b16 v[176:177], v225 offset:1536\n" \
"v_mfma_f32_16x16x32_bf16 v[68:71], v[140:143], a[152:155], v[68:71]\n" \
"ds_read_b64_tr_b16 v[178:179], v225 offset:1792\n" \
"v_mfma_f32_16x16x32_bf16 v[100:103], v[136:139], a[140:143], v[100:103]\n" \
"buffer_load_dwordx4 a[224:227], v227, %[srdz], s50 offen offset:2048 sc1\n" \
"v_mfma_f32_16x16x32_bf16 v[100:103], v[140:143], a[156:159], v[100:103]\n" \
"ds_read_b64_tr_b16 v[180:181], v225 offset:9728\n" \
"v_mfma_f32_16x16x32_bf16 v[8:11], v[144:147], a[128:131], v[8:11]\n" \
"ds_read_b64_tr_b16 v[182:183], v225 offset:9984\n" \
"v_mfma_f32_16x16x32_bf16 v[8:11], v[148:151], a[144:147], v[8:11]\n" \
"v_mfma_f32_16x16x32_bf16 v[40:43], v[144:147], a[132:135], v[40:43]\n" \
"ds_read_b64_tr_b16 v[184:185], v225 offset:1544\n" \
"v_mfma_f32_16x16x32_bf16 v[40:43], v[148:151], a[148:151], v[40:43]\n" \
"ds_read_b64_tr_b16 v[186:187], v225 offset:1800\n" \
"v_mfma_f32_16x16x32_bf16 v[72:75], v[144:147], a[136:139], v[72:75]\n" \
"buffer_load_dwordx4 a[228:231], v219, %[srdz], s50 offen offset:2048 sc1\n" \
"v_mfma_f32_16x16x32_bf16 v[72:75], v[148:151], a[152:155], v[72:75]\n" \
"ds_read_b64_tr_b16 v[188:189], v225 offset:9736\n" \
"v_mfma_f32_16x16x32_bf16 v[104:107], v[144:147], a[140:143], v[104:107]\n" \
"ds_read_b64_tr_b16 v[190:191], v225 offset:9992\n" \
"v_mfma_f32_16x16x32_bf16 v[104:107], v[148:151], a[156:159], v[104:107]\n" \
"v_mfma_f32_16x16x32_bf16 v[12:15], v[152:155], a[128:131], v[12:15]\n" \
"buffer_load_dwordx4 a[232:235], v220, %[srdz], s50 offen offset:2048 sc1\n" \
"v_mfma_f32_16x16x32_bf16 v[12:15], v[156:159], a[144:147], v[12:15]\n" \
"v_mfma_f32_16x16x32_bf16 v[44:47], v[152:155], a[132:135], v[44:47]\n" \
"v_mfma_f32_16x16x32_bf16 v[44:47], v[156:159], a[148:151], v[44:47]\n" \
"v_mfma_f32_16x16x32_bf16 v[76:79], v[152:155], a[136:139], v[76:79]\n" \
"buffer_load_dwordx4 a[236:239], v221, %[srdz], s50 offen offset:2048 sc1\n" \
"v_mfma_f32_16x16x32_bf16 v[76:79], v[156:159], a[152:155], v[76:79]\n" \
"v_mfma_f32_16x16x32_bf16 v[108:111], v[152:155], a[140:143], v[108:111]\n" \
"v_mfma_f32_16x16x32_bf16 v[108:111], v[156:159], a[156:159], v[108:111]\n" \
"s_waitcnt vmcnt(29) lgkmcnt(0)\n" \
"s_barrier\n" \
"v_mfma_f32_16x16x32_bf16 v[16:19], v[160:163], a[128:131], v[16:19]\n" \
"ds_read_b64_tr_b16 v[128:129], v225 offset:16384\n" \
"v_mfma_f32_16x16x32_bf16 v[16:19], v[164:167], a[144:147], v[16:19]\n" \
"ds_read_b64_tr_b16 v[130:131], v225 offset:16640\n" \
"v_mfma_f32_16x16x32_bf16 v[48:51], v[160:163], a[132:135], v[48:51]\n" \
"s_mov_b32 s48, 16384\n" \
"s_mov_b32 m0, s44\n" \
"s_nop 0\n" \
"buffer_load_dwordx4 %[vd0], %[srdv], s48 offen offset:0 lds\n" \
"v_mfma_f32_16x16x32_bf16 v[48:51], v[164:167], a[148:151], v[48:51]\n" \
"ds_read_b64_tr_b16 v[132:133], v225 offset:24576\n" \
"v_mfma_f32_16x16x32_bf16 v[80:83], v[160:163], a[136:139], v[80:83]\n" \
"ds_read_b64_tr_b16 v[134:135], v225 offset:24832\n" \
"v_mfma_f32_16x16x32_bf16 v[80:83], v[164:167], a[152:155], v[80:83]\n" \
"v_mfma_f32_16x16x32_bf16 v[112:115], v[160:163], a[140:143], v[112:115]\n" \
"ds_read_b64_tr_b16 v[136:137], v225 offset:16392\n" \
"v_mfma_f32_16x16x32_bf16 v[112:115], v[164:167], a[156:159], v[112:115]\n" \
"ds_read_b64_tr_b16 v[138:139], v225 offset:16648\n" \
"v_mfma_f32_16x16x32_bf16 v[20:23], v[168:171], a[128:131], v[20:23]\n" \
"buffer_load_dwordx4 v215, %[srdv], s48 offen offset:1024 lds\n" \
"v_mfma_f32_16x16x32_bf16 v[20:23], v[172:175], a[144:147], v[20:23]\n" \
"ds_read_b64_tr_b16 v[140:141], v225 offset:24584\n" \
"v_mfma_f32_16x16x32_bf16 v[52:55], v[168:171], a[132:135], v[52:55]\n" \
"ds_read_b64_tr_b16 v[142:143], v225 offset:24840\n" \
"v_mfma_f32_16x16x32_bf16 v[52:55], v[172:175], a[148:151], v[52:55]\n" \
"v_mfma_f32_16x16x32_bf16 v[84:87], v[168:171], a[136:139], v[84:87]\n" \
"ds_read_b64_tr_b16 v[144:145], v225 offset:16896\n" \
"v_mfma_f32_16x16x32_bf16 v[84:87], v[172:175], a[152:155], v[84:87]\n" \
"ds_read_b64_tr_b16 v[146:147], v225 offset:17152\n" \
"v_mfma_f32_16x16x32_bf16 v[116:119], v[168:171], a[140:143], v[116:119]\n" \
"buffer_load_dwordx4 a[240:243], v227, %[srdz], s50 offen offset:3072 sc1\n" \
"v_mfma_f32_16x16x32_bf16 v[116:119], v[172:175], a[156:159], v[116:119]\n" \
"ds_read_b64_tr_b16 v[148:149], v225 offset:25088\n" \
"v_mfma_f32_16x16x32_bf16 v[24:27], v[176:179], a[128:131], v[24:27]\n" \
"ds_read_b64_tr_b16 v[150:151], v225 offset:25344\n" \
"v_mfma_f32_16x16x32_bf16 v[24:27], v[180:183], a[144:147], v[24:27]\n" \
"v_mfma_f32_16x16x32_bf16 v[56:59], v[176:179], a[132:135], v[56:59]\n" \
"ds_read_b64_tr_b16 v[152:153], v225 offset:16904\n" \
"v_mfma_f32_16x16x32_bf16 v[56:59], v[180:183], a[148:151], v[56:59]\n" \
"ds_read_b64_tr_b16 v[154:155], v225 offset:17160\n" \
"v_mfma_f32_16x16x32_bf16 v[88:91], v[176:179], a[136:139], v[88:91]\n" \
"buffer_load_dwordx4 a[244:247], v219, %[srdz], s50 offen offset:3072 sc1\n" \
"v_mfma_f32_16x16x32_bf16 v[88:91], v[180:183], a[152:155], v[88:91]\n" \
"ds_read_b64_tr_b16 v[156:157], v225 offset:25096\n" \
"v_mfma_f32_16x16x32_bf16 v[120:123], v[176:179], a[140:143], v[120:123]\n" \
"ds_read_b64_tr_b16 v[158:159], v225 offset:25352\n" \
"v_mfma_f32_16x16x32_bf16 v[120:123], v[180:183], a[156:159], v[120:123]\n" \
"v_mfma_f32_16x16x32_bf16 v[28:31], v[184:187], a[128:131], v[28:31]\n" \
"buffer_load_dwordx4 a[248:251], v220, %[srdz], s50 offen offset:3072 sc1\n" \
"v_mfma_f32_16x16x32_bf16 v[28:31], v[188:191], a[144:147], v[28:31]\n" \
"v_mfma_f32_16x16x32_bf16 v[60:63], v[184:187], a[132:135], v[60:63]\n" \
"v_mfma_f32_16x16x32_bf16 v[60:63], v[188:191], a[148:151], v[60:63]\n" \
"v_mfma_f32_16x16x32_bf16 v[92:95], v[184:187], a[136:139], v[92:95]\n" \
"buffer_load_dwordx4 a[252:255], v221, %[srdz], s50 offen offset:3072 sc1\n" \
"v_mfma_f32_16x16x32_bf16 v[92:95], v[188:191], a[152:155], v[92:95]\n" \
"v_mfma_f32_16x16x32_bf16 v[124:127], v[184:187], a[140:143], v[124:127]\n" \
"v_mfma_f32_16x16x32_bf16 v[124:127], v[188:191], a[156:159], v[124:127]\n" \
"s_waitcnt vmcnt(25) lgkmcnt(0)\n" \
"v_mfma_f32_16x16x32_bf16 v[0:3], v[128:131], a[160:163], v[0:3]\n" \
"ds_read_b64_tr_b16 v[160:161], v225 offset:17408\n" \
"v_mfma_f32_16x16x32_bf16 v[0:3], v[132:135], a[176:179], v[0:3]\n" \
"ds_read_b64_tr_b16 v[162:163], v225 offset:17664\n" \
"v_mfma_f32_16x16x32_bf16 v[32:35], v[128:131], a[164:167], v[32:35]\n" \
"s_mov_b32 s48, 16384\n" \
"s_mov_b32 m0, s44\n" \
"s_nop 0\n" \
"buffer_load_dwordx4 %[vd0], %[srdv], s48 offen offset:2048 lds\n" \
"v_mfma_f32_16x16x32_bf16 v[32:35], v[132:135], a[180:183], v[32:35]\n" \
"ds_read_b64_tr_b16 v[164:165], v225 offset:25600\n" \
"v_mfma_f32_16x16x32_bf16 v[64:67], v[128:131], a[168:171], v[64:67]\n" \
"ds_read_b64_tr_b16 v[166:167], v225 offset:25856\n" \
"v_mfma_f32_16x16x32_bf16 v[64:67], v[132:135], a[184:187], v[64:67]\n" \
"v_mfma_f32_16x16x32_bf16 v[96:99], v[128:131], a[172:175], v[96:99]\n" \
"ds_read_b64_tr_b16 v[168:169], v225 offset:17416\n" \
"v_mfma_f32_16x16x32_bf16 v[96:99], v[132:135], a[188:191], v[96:99]\n" \
"ds_read_b64_tr_b16 v[170:171], v225 offset:17672\n" \
"v_mfma_f32_16x16x32_bf16 v[4:7], v[136:139], a[160:163], v[4:7]\n" \
"buffer_load_dwordx4 v215, %[srdv], s48 offen offset:3072 lds\n" \
"v_mfma_f32_16x16x32_bf16 v[4:7], v[140:143], a[176:179], v[4:7]\n" \
"ds_read_b64_tr_b16 v[172:173], v225 offset:25608\n" \
"v_mfma_f32_16x16x32_bf16 v[36:39], v[136:139], a[164:167], v[36:39]\n" \
"ds_read_b64_tr_b16 v[174:175], v225 offset:25864\n" \
"v_mfma_f32_16x16x32_bf16 v[36:39], v[140:143], a[180:183], v[36:39]\n" \
"v_mfma_f32_16x16x32_bf16 v[68:71], v[136:139], a[168:171], v[68:71]\n" \
"ds_read_b64_tr_b16 v[176:177], v225 offset:17920\n" \
"v_mfma_f32_16x16x32_bf16 v[68:71], v[140:143], a[184:187], v[68:71]\n" \
"ds_read_b64_tr_b16 v[178:179], v225 offset:18176\n" \
"v_mfma_f32_16x16x32_bf16 v[100:103], v[136:139], a[172:175], v[100:103]\n" \
"s_mov_b32 s48, 0\n" \
"s_mov_b32 m0, s40\n" \
"s_nop 0\n" \
"buffer_load_dwordx4 %[kd0], %[srdk], s48 offen offset:0 lds\n" \
"v_mfma_f32_16x16x32_bf16 v[100:103], v[140:143], a[188:191], v[100:103]\n" \
"ds_read_b64_tr_b16 v[180:181], v225 offset:26112\n" \
"v_mfma_f32_16x16x32_bf16 v[8:11], v[144:147], a[160:163], v[8:11]\n" \
"ds_read_b64_tr_b16 v[182:183], v225 offset:26368\n" \
"v_mfma_f32_16x16x32_bf16 v[8:11], v[148:151], a[176:179], v[8:11]\n" \
"v_mfma_f32_16x16x32_bf16 v[40:43], v[144:147], a[164:167], v[40:43]\n" \
"ds_read_b64_tr_b16 v[184:185], v225 offset:17928\n" \
"v_mfma_f32_16x16x32_bf16 v[40:43], v[148:151], a[180:183], v[40:43]\n" \
"ds_read_b64_tr_b16 v[186:187], v225 offset:18184\n" \
"v_mfma_f32_16x16x32_bf16 v[72:75], v[144:147], a[168:171], v[72:75]\n" \
"buffer_load_dwordx4 v212, %[srdk], s48 offen offset:1024 lds\n" \
"v_mfma_f32_16x16x32_bf16 v[72:75], v[148:151], a[184:187], v[72:75]\n" \
"ds_read_b64_tr_b16 v[188:189], v225 offset:26120\n" \
"v_mfma_f32_16x16x32_bf16 v[104:107], v[144:147], a[172:175], v[104:107]\n" \
"ds_read_b64_tr_b16 v[190:191], v225 offset:26376\n" \
"v_mfma_f32_16x16x32_bf16 v[104:107], v[148:151], a[188:191], v[104:107]\n" \
"v_mfma_f32_16x16x32_bf16 v[12:15], v[152:155], a[160:163], v[12:15]\n" \
"buffer_load_dwordx4 v213, %[srdk], s48 offen offset:2048 lds\n" \
"v_mfma_f32_16x16x32_bf16 v[12:15], v[156:159], a[176:179], v[12:15]\n" \
"v_mfma_f32_16x16x32_bf16 v[44:47], v[152:155], a[164:167], v[44:47]\n" \
"v_mfma_f32_16x16x32_bf16 v[44:47], v[156:159], a[180:183], v[44:47]\n" \
"v_mfma_f32_16x16x32_bf16 v[76:79], v[152:155], a[168:171], v[76:79]\n" \
"buffer_load_dwordx4 v214, %[srdk], s48 offen offset:3072 lds\n" \
"v_mfma_f32_16x16x32_bf16 v[76:79], v[156:159], a[184:187], v[76:79]\n" \
"v_mfma_f32_16x16x32_bf16 v[108:111], v[152:155], a[172:175], v[108:111]\n" \
"v_mfma_f32_16x16x32_bf16 v[108:111], v[156:159], a[188:191], v[108:111]\n" \
"s_waitcnt vmcnt(29) lgkmcnt(0)\n" \
"s_barrier\n" \
"v_mfma_f32_16x16x32_bf16 v[16:19], v[160:163], a[160:163], v[16:19]\n" \
"ds_read_b64_tr_b16 v[128:129], v225 offset:32768\n" \
"v_mfma_f32_16x16x32_bf16 v[16:19], v[164:167], a[176:179], v[16:19]\n" \
"ds_read_b64_tr_b16 v[130:131], v225 offset:33024\n" \
"v_mfma_f32_16x16x32_bf16 v[48:51], v[160:163], a[164:167], v[48:51]\n" \
"s_mov_b32 s48, 32768\n" \
"s_mov_b32 m0, s45\n" \
"s_nop 0\n" \
"buffer_load_dwordx4 %[vd0], %[srdv], s48 offen offset:0 lds\n" \
"v_mfma_f32_16x16x32_bf16 v[48:51], v[164:167], a[180:183], v[48:51]\n" \
"ds_read_b64_tr_b16 v[132:133], v225 offset:40960\n" \
"v_mfma_f32_16x16x32_bf16 v[80:83], v[160:163], a[168:171], v[80:83]\n" \
"ds_read_b64_tr_b16 v[134:135], v225 offset:41216\n" \
"v_mfma_f32_16x16x32_bf16 v[80:83], v[164:167], a[184:187], v[80:83]\n" \
"v_mfma_f32_16x16x32_bf16 v[112:115], v[160:163], a[172:175], v[112:115]\n" \
"ds_read_b64_tr_b16 v[136:137], v225 offset:32776\n" \
"v_mfma_f32_16x16x32_bf16 v[112:115], v[164:167], a[188:191], v[112:115]\n" \
"ds_read_b64_tr_b16 v[138:139], v225 offset:33032\n" \
"v_mfma_f32_16x16x32_bf16 v[20:23], v[168:171], a[160:163], v[20:23]\n" \
"buffer_load_dwordx4 v215, %[srdv], s48 offen offset:1024 lds\n" \
"v_mfma_f32_16x16x32_bf16 v[20:23], v[172:175], a[176:179], v[20:23]\n" \
"ds_read_b64_tr_b16 v[140:141], v225 offset:40968\n" \
"v_mfma_f32_16x16x32_bf16 v[52:55], v[168:171], a[164:167], v[52:55]\n" \
"ds_read_b64_tr_b16 v[142:143], v225 offset:41224\n" \
"v_mfma_f32_16x16x32_bf16 v[52:55], v[172:175], a[180:183], v[52:55]\n" \
"v_mfma_f32_16x16x32_bf16 v[84:87], v[168:171], a[168:171], v[84:87]\n" \
"ds_read_b64_tr_b16 v[144:145], v225 offset:33280\n" \
"v_mfma_f32_16x16x32_bf16 v[84:87], v[172:175], a[184:187], v[84:87]\n" \
"ds_read_b64_tr_b16 v[146:147], v225 offset:33536\n" \
"v_mfma_f32_16x16x32_bf16 v[116:119], v[168:171], a[172:175], v[116:119]\n" \
"s_mov_b32 s48, 16384\n" \
"s_mov_b32 m0, s41\n" \
"s_nop 0\n" \
"buffer_load_dwordx4 %[kd0], %[srdk], s48 offen offset:0 lds\n" \
"v_mfma_f32_16x16x32_bf16 v[116:119], v[172:175], a[188:191], v[116:119]\n" \
"ds_read_b64_tr_b16 v[148:149], v225 offset:41472\n" \
"v_mfma_f32_16x16x32_bf16 v[24:27], v[176:179], a[160:163], v[24:27]\n" \
"ds_read_b64_tr_b16 v[150:151], v225 offset:41728\n" \
"v_mfma_f32_16x16x32_bf16 v[24:27], v[180:183], a[176:179], v[24:27]\n" \
"v_mfma_f32_16x16x32_bf16 v[56:59], v[176:179], a[164:167], v[56:59]\n" \
"ds_read_b64_tr_b16 v[152:153], v225 offset:33288\n" \
"v_mfma_f32_16x16x32_bf16 v[56:59], v[180:183], a[180:183], v[56:59]\n" \
"ds_read_b64_tr_b16 v[154:155], v225 offset:33544\n" \
"v_mfma_f32_16x16x32_bf16 v[88:91], v[176:179], a[168:171], v[88:91]\n" \
"buffer_load_dwordx4 v212, %[srdk], s48 offen offset:1024 lds\n" \
"v_mfma_f32_16x16x32_bf16 v[88:91], v[180:183], a[184:187], v[88:91]\n" \
"ds_read_b64_tr_b16 v[156:157], v225 offset:41480\n" \
"v_mfma_f32_16x16x32_bf16 v[120:123], v[176:179], a[172:175], v[120:123]\n" \
"ds_read_b64_tr_b16 v[158:159], v225 offset:41736\n" \
"v_mfma_f32_16x16x32_bf16 v[120:123], v[180:183], a[188:191], v[120:123]\n" \
"v_mfma_f32_16x16x32_bf16 v[28:31], v[184:187], a[160:163], v[28:31]\n" \
"buffer_load_dwordx4 v213, %[srdk], s48 offen offset:2048 lds\n" \
"v_mfma_f32_16x16x32_bf16 v[28:31], v[188:191], a[176:179], v[28:31]\n" \
"v_mfma_f32_16x16x32_bf16 v[60:63], v[184:187], a[164:167], v[60:63]\n" \
"v_mfma_f32_16x16x32_bf16 v[60:63], v[188:191], a[180:183], v[60:63]\n" \
"v_mfma_f32_16x16x32_bf16 v[92:95], v[184:187], a[168:171], v[92:95]\n" \
"buffer_load_dwordx4 v214, %[srdk], s48 offen offset:3072 lds\n" \
"v_mfma_f32_16x16x32_bf16 v[92:95], v[188:191], a[184:187], v[92:95]\n" \
"v_mfma_f32_16x16x32_bf16 v[124:127], v[184:187], a[172:175], v[124:127]\n" \
"v_mfma_f32_16x16x32_bf16 v[124:127], v[188:191], a[188:191], v[124:127]\n" \
"s_waitcnt vmcnt(24) lgkmcnt(0)\n" \
"v_mfma_f32_16x16x32_bf16 v[0:3], v[128:131], a[192:195], v[0:3]\n" \
"ds_read_b64_tr_b16 v[160:161], v225 offset:33792\n" \
"v_mfma_f32_16x16x32_bf16 v[0:3], v[132:135], a[208:211], v[0:3]\n" \
"ds_read_b64_tr_b16 v[162:163], v225 offset:34048\n" \
"v_mfma_f32_16x16x32_bf16 v[32:35], v[128:131], a[196:199], v[32:35]\n" \
"s_mov_b32 s48, 32768\n" \
"s_mov_b32 m0, s45\n" \
"s_nop 0\n" \
"buffer_load_dwordx4 %[vd0], %[srdv], s48 offen offset:2048 lds\n" \
"v_mfma_f32_16x16x32_bf16 v[32:35], v[132:135], a[212:215], v[32:35]\n" \
"ds_read_b64_tr_b16 v[164:165], v225 offset:41984\n" \
"v_mfma_f32_16x16x32_bf16 v[64:67], v[128:131], a[200:203], v[64:67]\n" \
"ds_read_b64_tr_b16 v[166:167], v225 offset:42240\n" \
"v_mfma_f32_16x16x32_bf16 v[64:67], v[132:135], a[216:219], v[64:67]\n" \
"v_mfma_f32_16x16x32_bf16 v[96:99], v[128:131], a[204:207], v[96:99]\n" \
"ds_read_b64_tr_b16 v[168:169], v225 offset:33800\n" \
"v_mfma_f32_16x16x32_bf16 v[96:99], v[132:135], a[220:223], v[96:99]\n" \
"ds_read_b64_tr_b16 v[170:171], v225 offset:34056\n" \
"v_mfma_f32_16x16x32_bf16 v[4:7], v[136:139], a[192:195], v[4:7]\n" \
"buffer_load_dwordx4 v215, %[srdv], s48 offen offset:3072 lds\n" \
"v_mfma_f32_16x16x32_bf16 v[4:7], v[140:143], a[208:211], v[4:7]\n" \
"ds_read_b64_tr_b16 v[172:173], v225 offset:41992\n" \
"v_mfma_f32_16x16x32_bf16 v[36:39], v[136:139], a[196:199], v[36:39]\n" \
"ds_read_b64_tr_b16 v[174:175], v225 offset:42248\n" \
"v_mfma_f32_16x16x32_bf16 v[36:39], v[140:143], a[212:215], v[36:39]\n" \
"v_mfma_f32_16x16x32_bf16 v[68:71], v[136:139], a[200:203], v[68:71]\n" \
"ds_read_b64_tr_b16 v[176:177], v225 offset:34304\n" \
"v_mfma_f32_16x16x32_bf16 v[68:71], v[140:143], a[216:219], v[68:71]\n" \
"ds_read_b64_tr_b16 v[178:179], v225 offset:34560\n" \
"v_mfma_f32_16x16x32_bf16 v[100:103], v[136:139], a[204:207], v[100:103]\n" \
"s_mov_b32 s48, 32768\n" \
"s_mov_b32 m0, s42\n" \
"s_nop 0\n" \
"buffer_load_dwordx4 %[kd0], %[srdk], s48 offen offset:0 lds\n" \
"v_mfma_f32_16x16x32_bf16 v[100:103], v[140:143], a[220:223], v[100:103]\n" \
"ds_read_b64_tr_b16 v[180:181], v225 offset:42496\n" \
"v_mfma_f32_16x16x32_bf16 v[8:11], v[144:147], a[192:195], v[8:11]\n" \
"ds_read_b64_tr_b16 v[182:183], v225 offset:42752\n" \
"v_mfma_f32_16x16x32_bf16 v[8:11], v[148:151], a[208:211], v[8:11]\n" \
"v_mfma_f32_16x16x32_bf16 v[40:43], v[144:147], a[196:199], v[40:43]\n" \
"ds_read_b64_tr_b16 v[184:185], v225 offset:34312\n" \
"v_mfma_f32_16x16x32_bf16 v[40:43], v[148:151], a[212:215], v[40:43]\n" \
"ds_read_b64_tr_b16 v[186:187], v225 offset:34568\n" \
"v_mfma_f32_16x16x32_bf16 v[72:75], v[144:147], a[200:203], v[72:75]\n" \
"buffer_load_dwordx4 v212, %[srdk], s48 offen offset:1024 lds\n" \
"v_mfma_f32_16x16x32_bf16 v[72:75], v[148:151], a[216:219], v[72:75]\n" \
"ds_read_b64_tr_b16 v[188:189], v225 offset:42504\n" \
"v_mfma_f32_16x16x32_bf16 v[104:107], v[144:147], a[204:207], v[104:107]\n" \
"ds_read_b64_tr_b16 v[190:191], v225 offset:42760\n" \
"v_mfma_f32_16x16x32_bf16 v[104:107], v[148:151], a[220:223], v[104:107]\n" \
"v_mfma_f32_16x16x32_bf16 v[12:15], v[152:155], a[192:195], v[12:15]\n" \
"buffer_load_dwordx4 v213, %[srdk], s48 offen offset:2048 lds\n" \
"v_mfma_f32_16x16x32_bf16 v[12:15], v[156:159], a[208:211], v[12:15]\n" \
"v_mfma_f32_16x16x32_bf16 v[44:47], v[152:155], a[196:199], v[44:47]\n" \
"v_mfma_f32_16x16x32_bf16 v[44:47], v[156:159], a[212:215], v[44:47]\n" \
"v_mfma_f32_16x16x32_bf16 v[76:79], v[152:155], a[200:203], v[76:79]\n" \
"buffer_load_dwordx4 v214, %[srdk], s48 offen offset:3072 lds\n" \
"v_mfma_f32_16x16x32_bf16 v[76:79], v[156:159], a[216:219], v[76:79]\n" \
"v_mfma_f32_16x16x32_bf16 v[108:111], v[152:155], a[204:207], v[108:111]\n" \
"v_mfma_f32_16x16x32_bf16 v[108:111], v[156:159], a[220:223], v[108:111]\n" \
"s_waitcnt vmcnt(28) lgkmcnt(0)\n" \
"s_barrier\n" \
"v_mfma_f32_16x16x32_bf16 v[16:19], v[160:163], a[192:195], v[16:19]\n" \
"ds_read_b64_tr_b16 v[128:129], v225 offset:49152\n" \
"s_mov_b32 s54, 0x4000\n" \
"v_mfma_f32_16x16x32_bf16 v[16:19], v[164:167], a[208:211], v[16:19]\n" \
"ds_read_b64_tr_b16 v[130:131], v225 offset:49408\n" \
"v_mfma_f32_16x16x32_bf16 v[48:51], v[160:163], a[196:199], v[48:51]\n" \
"s_mov_b32 s48, 49152\n" \
"s_mov_b32 m0, s46\n" \
"s_nop 0\n" \
"buffer_load_dwordx4 %[vd0], %[srdv], s48 offen offset:0 lds\n" \
"v_mbcnt_lo_u32_b32 v231, -1, 0\n" \
"v_mfma_f32_16x16x32_bf16 v[48:51], v[164:167], a[212:215], v[48:51]\n" \
"ds_read_b64_tr_b16 v[132:133], v225 offset:57344\n" \
"v_mfma_f32_16x16x32_bf16 v[80:83], v[160:163], a[200:203], v[80:83]\n" \
"ds_read_b64_tr_b16 v[134:135], v225 offset:57600\n" \
"v_mfma_f32_16x16x32_bf16 v[80:83], v[164:167], a[216:219], v[80:83]\n" \
"v_mbcnt_hi_u32_b32 v231, -1, v231\n" \
"v_mfma_f32_16x16x32_bf16 v[112:115], v[160:163], a[204:207], v[112:115]\n" \
"ds_read_b64_tr_b16 v[136:137], v225 offset:49160\n" \
"v_mfma_f32_16x16x32_bf16 v[112:115], v[164:167], a[220:223], v[112:115]\n" \
"ds_read_b64_tr_b16 v[138:139], v225 offset:49416\n" \
"v_mfma_f32_16x16x32_bf16 v[20:23], v[168:171], a[192:195], v[20:23]\n" \
"buffer_load_dwordx4 v215, %[srdv], s48 offen offset:1024 lds\n" \
"v_lshrrev_b32_e32 v232, 4, v231\n" \
"v_mfma_f32_16x16x32_bf16 v[20:23], v[172:175], a[208:211], v[20:23]\n" \
"ds_read_b64_tr_b16 v[140:141], v225 offset:57352\n" \
"v_mfma_f32_16x16x32_bf16 v[52:55], v[168:171], a[196:199], v[52:55]\n" \
"ds_read_b64_tr_b16 v[142:143], v225 offset:57608\n" \
"v_xor_b32_e64 v232, v232, v231\n" \
"v_mfma_f32_16x16x32_bf16 v[52:55], v[172:175], a[212:215], v[52:55]\n" \
"v_mfma_f32_16x16x32_bf16 v[84:87], v[168:171], a[200:203], v[84:87]\n" \
"ds_read_b64_tr_b16 v[144:145], v225 offset:49664\n" \
"v_mfma_f32_16x16x32_bf16 v[84:87], v[172:175], a[216:219], v[84:87]\n" \
"ds_read_b64_tr_b16 v[146:147], v225 offset:49920\n" \
"v_bfe_i32 v232, v232, 1, 1\n" \
"v_mfma_f32_16x16x32_bf16 v[116:119], v[168:171], a[204:207], v[116:119]\n" \
"s_mov_b32 s48, 49152\n" \
"s_mov_b32 m0, s43\n" \
"s_nop 0\n" \
"buffer_load_dwordx4 %[kd0], %[srdk], s48 offen offset:0 lds\n" \
"v_mfma_f32_16x16x32_bf16 v[116:119], v[172:175], a[220:223], v[116:119]\n" \
"ds_read_b64_tr_b16 v[148:149], v225 offset:57856\n" \
"v_mfma_f32_16x16x32_bf16 v[24:27], v[176:179], a[192:195], v[24:27]\n" \
"ds_read_b64_tr_b16 v[150:151], v225 offset:58112\n" \
"v_not_b32_e64 v232, v232\n" \
"v_mfma_f32_16x16x32_bf16 v[24:27], v[180:183], a[208:211], v[24:27]\n" \
"v_mfma_f32_16x16x32_bf16 v[56:59], v[176:179], a[196:199], v[56:59]\n" \
"ds_read_b64_tr_b16 v[152:153], v225 offset:49672\n" \
"v_mfma_f32_16x16x32_bf16 v[56:59], v[180:183], a[212:215], v[56:59]\n" \
"ds_read_b64_tr_b16 v[154:155], v225 offset:49928\n" \
"v_and_b32_e32 v230, 0x3f803f80, v232\n" \
"v_mfma_f32_16x16x32_bf16 v[88:91], v[176:179], a[200:203], v[88:91]\n" \
"buffer_load_dwordx4 v212, %[srdk], s48 offen offset:1024 lds\n" \
"v_mfma_f32_16x16x32_bf16 v[88:91], v[180:183], a[216:219], v[88:91]\n" \
"ds_read_b64_tr_b16 v[156:157], v225 offset:57864\n" \
"v_readlane_b32 s62, %[tlo], 18\n" \
"v_mfma_f32_16x16x32_bf16 v[120:123], v[176:179], a[204:207], v[120:123]\n" \
"ds_read_b64_tr_b16 v[158:159], v225 offset:58120\n" \
"v_mfma_f32_16x16x32_bf16 v[120:123], v[180:183], a[220:223], v[120:123]\n" \
"v_mfma_f32_16x16x32_bf16 v[28:31], v[184:187], a[192:195], v[28:31]\n" \
"buffer_load_dwordx4 v213, %[srdk], s48 offen offset:2048 lds\n" \
"v_readlane_b32 s61, %[tlo], 21\n" \
"v_mfma_f32_16x16x32_bf16 v[28:31], v[188:191], a[208:211], v[28:31]\n" \
"v_mfma_f32_16x16x32_bf16 v[60:63], v[184:187], a[196:199], v[60:63]\n" \
"v_mfma_f32_16x16x32_bf16 v[60:63], v[188:191], a[212:215], v[60:63]\n" \
"v_lshlrev_b32_e64 v224, 7, v231\n" \
"v_mfma_f32_16x16x32_bf16 v[92:95], v[184:187], a[200:203], v[92:95]\n" \
"buffer_load_dwordx4 v214, %[srdk], s48 offen offset:3072 lds\n" \
"v_mfma_f32_16x16x32_bf16 v[92:95], v[188:191], a[216:219], v[92:95]\n" \
"v_mfma_f32_16x16x32_bf16 v[124:127], v[184:187], a[204:207], v[124:127]\n" \
"v_mfma_f32_16x16x32_bf16 v[124:127], v[188:191], a[220:223], v[124:127]\n" \
"s_waitcnt vmcnt(24) lgkmcnt(0)\n" \
"v_mfma_f32_16x16x32_bf16 v[0:3], v[128:131], a[224:227], v[0:3]\n" \
"ds_read_b64_tr_b16 v[160:161], v225 offset:50176\n" \
"v_accvgpr_write_b32 a192, v230\n" \
"v_mfma_f32_16x16x32_bf16 v[0:3], v[132:135], a[240:243], v[0:3]\n" \
"ds_read_b64_tr_b16 v[162:163], v225 offset:50432\n" \
"v_mfma_f32_16x16x32_bf16 v[32:35], v[128:131], a[228:231], v[32:35]\n" \
"s_mov_b32 s48, 49152\n" \
"s_mov_b32 m0, s46\n" \
"s_nop 0\n" \
"buffer_load_dwordx4 %[vd0], %[srdv], s48 offen offset:2048 lds\n" \
"v_accvgpr_write_b32 a193, v230\n" \
"v_mfma_f32_16x16x32_bf16 v[32:35], v[132:135], a[244:247], v[32:35]\n" \
"ds_read_b64_tr_b16 v[164:165], v225 offset:58368\n" \
"v_mfma_f32_16x16x32_bf16 v[64:67], v[128:131], a[232:235], v[64:67]\n" \
"ds_read_b64_tr_b16 v[166:167], v225 offset:58624\n" \
"v_accvgpr_write_b32 a194, v230\n" \
"v_mfma_f32_16x16x32_bf16 v[64:67], v[132:135], a[248:251], v[64:67]\n" \
"v_mfma_f32_16x16x32_bf16 v[96:99], v[128:131], a[236:239], v[96:99]\n" \
"ds_read_b64_tr_b16 v[168:169], v225 offset:50184\n" \
"v_accvgpr_write_b32 a195, v230\n" \
"v_mfma_f32_16x16x32_bf16 v[96:99], v[132:135], a[252:255], v[96:99]\n" \
"ds_read_b64_tr_b16 v[170:171], v225 offset:50440\n" \
"v_mfma_f32_16x16x32_bf16 v[4:7], v[136:139], a[224:227], v[4:7]\n" \
"buffer_load_dwordx4 v215, %[srdv], s48 offen offset:3072 lds\n" \
"v_accvgpr_write_b32 a196, -1\n" \
"v_mfma_f32_16x16x32_bf16 v[4:7], v[140:143], a[240:243], v[4:7]\n" \
"ds_read_b64_tr_b16 v[172:173], v225 offset:58376\n" \
"v_mfma_f32_16x16x32_bf16 v[36:39], v[136:139], a[228:231], v[36:39]\n" \
"ds_read_b64_tr_b16 v[174:175], v225 offset:58632\n" \
"v_accvgpr_write_b32 a197, -1\n" \
"v_mfma_f32_16x16x32_bf16 v[36:39], v[140:143], a[244:247], v[36:39]\n" \
"v_mfma_f32_16x16x32_bf16 v[68:71], v[136:139], a[232:235], v[68:71]\n" \
"ds_read_b64_tr_b16 v[176:177], v225 offset:50688\n" \
"v_accvgpr_write_b32 a198, -1\n" \
"v_mfma_f32_16x16x32_bf16 v[68:71], v[140:143], a[248:251], v[68:71]\n" \
"ds_read_b64_tr_b16 v[178:179], v225 offset:50944\n" \
"v_mfma_f32_16x16x32_bf16 v[100:103], v[136:139], a[236:239], v[100:103]\n" \
"v_accvgpr_write_b32 a199, -1\n" \
"v_mfma_f32_16x16x32_bf16 v[100:103], v[140:143], a[252:255], v[100:103]\n" \
"ds_read_b64_tr_b16 v[180:181], v225 offset:58880\n" \
"v_mfma_f32_16x16x32_bf16 v[8:11], v[144:147], a[224:227], v[8:11]\n" \
"ds_read_b64_tr_b16 v[182:183], v225 offset:59136\n" \
"v_lshrrev_b32_e32 v231, 3, v224\n" \
"v_mfma_f32_16x16x32_bf16 v[8:11], v[148:151], a[240:243], v[8:11]\n" \
"v_mfma_f32_16x16x32_bf16 v[40:43], v[144:147], a[228:231], v[40:43]\n" \
"ds_read_b64_tr_b16 v[184:185], v225 offset:50696\n" \
"v_mov_b32_e64 v232, 0\n" \
"v_mfma_f32_16x16x32_bf16 v[40:43], v[148:151], a[244:247], v[40:43]\n" \
"ds_read_b64_tr_b16 v[186:187], v225 offset:50952\n" \
"v_mfma_f32_16x16x32_bf16 v[72:75], v[144:147], a[232:235], v[72:75]\n" \
"v_mov_b32_e64 v233, 0\n" \
"v_mfma_f32_16x16x32_bf16 v[72:75], v[148:151], a[248:251], v[72:75]\n" \
"ds_read_b64_tr_b16 v[188:189], v225 offset:58888\n" \
"v_mfma_f32_16x16x32_bf16 v[104:107], v[144:147], a[236:239], v[104:107]\n" \
"ds_read_b64_tr_b16 v[190:191], v225 offset:59144\n" \
"v_mov_b32_e64 v234, 0\n" \
"v_mfma_f32_16x16x32_bf16 v[104:107], v[148:151], a[252:255], v[104:107]\n" \
"v_mfma_f32_16x16x32_bf16 v[12:15], v[152:155], a[224:227], v[12:15]\n" \
"v_mov_b32_e64 v235, 0\n" \
"v_mfma_f32_16x16x32_bf16 v[12:15], v[156:159], a[240:243], v[12:15]\n" \
"v_mfma_f32_16x16x32_bf16 v[44:47], v[152:155], a[228:231], v[44:47]\n" \
"buffer_store_dwordx4 v[232:235], v231, %[srdz], s61 offen offset:0 sc1\n" \
"v_mfma_f32_16x16x32_bf16 v[44:47], v[156:159], a[244:247], v[44:47]\n" \
"v_mfma_f32_16x16x32_bf16 v[76:79], v[152:155], a[232:235], v[76:79]\n" \
"buffer_store_dwordx4 v[232:235], v231, %[srdz], s61 offen offset:1024 sc1\n" \
"v_mfma_f32_16x16x32_bf16 v[76:79], v[156:159], a[248:251], v[76:79]\n" \
"v_mfma_f32_16x16x32_bf16 v[108:111], v[152:155], a[236:239], v[108:111]\n" \
"v_mfma_f32_16x16x32_bf16 v[108:111], v[156:159], a[252:255], v[108:111]\n" \
"s_waitcnt vmcnt(22) lgkmcnt(0)\n" \
"s_barrier\n" \
"v_mfma_f32_16x16x32_bf16 v[16:19], v[160:163], a[224:227], v[16:19]\n" \
"ds_read_b128 a[64:67], %[kb0] offset:0\n" \
"v_mfma_f32_16x16x32_bf16 v[16:19], v[164:167], a[240:243], v[16:19]\n" \
"v_mfma_f32_16x16x32_bf16 v[20:23], v[168:171], a[224:227], v[20:23]\n" \
"v_cvt_pk_bf16_f32 v230, v0, v1\n" \
"v_cvt_pk_bf16_f32 v231, v2, v3\n" \
"v_cvt_pk_bf16_f32 v232, v4, v5\n" \
"ds_read_b128 a[68:71], v208 offset:0\n" \
"v_mfma_f32_16x16x32_bf16 v[20:23], v[172:175], a[240:243], v[20:23]\n" \
"v_cvt_pk_bf16_f32 v233, v6, v7\n" \
"v_accvgpr_write_b32 a0, v230\n" \
"v_accvgpr_write_b32 a1, v231\n" \
"v_mfma_f32_16x16x32_bf16 v[24:27], v[176:179], a[224:227], v[24:27]\n" \
"v_accvgpr_write_b32 a2, v232\n" \
"v_accvgpr_write_b32 a3, v233\n" \
"v_cvt_pk_bf16_f32 v234, v8, v9\n" \
"ds_read_b128 a[72:75], v209 offset:0\n" \
"v_mfma_f32_16x16x32_bf16 v[24:27], v[180:183], a[240:243], v[24:27]\n" \
"v_cvt_pk_bf16_f32 v235, v10, v11\n" \
"v_cvt_pk_bf16_f32 v236, v12, v13\n" \
"v_cvt_pk_bf16_f32 v237, v14, v15\n" \
"v_mfma_f32_16x16x32_bf16 v[28:31], v[184:187], a[224:227], v[28:31]\n" \
"v_accvgpr_write_b32 a4, v234\n" \
"v_accvgpr_write_b32 a5, v235\n" \
"v_accvgpr_write_b32 a6, v236\n" \
".if . - adaln_body != 8192\n" \
".error \"size model\"\n" \
".endif\n" \
"Lk1_%=:\n" \
"s_cbranch_execz Lk2_%=\n" \
"s_nop 0\n" \
"ds_read_b128 a[76:79], v210 offset:0\n" \
"v_mfma_f32_16x16x32_bf16 v[28:31], v[188:191], a[240:243], v[28:31]\n" \
"v_accvgpr_write_b32 a7, v237\n" \
"v_cvt_pk_bf16_f32 v230, v32, v33\n" \
"v_cvt_pk_bf16_f32 v231, v34, v35\n" \
"v_mfma_f32_16x16x32_bf16 v[48:51], v[160:163], a[228:231], v[48:51]\n" \
"v_cvt_pk_bf16_f32 v232, v36, v37\n" \
"v_cvt_pk_bf16_f32 v233, v38, v39\n" \
"v_accvgpr_write_b32 a16, v230\n" \
"ds_read_b128 a[80:83], %[kb0] offset:4096\n" \
"v_mfma_f32_16x16x32_bf16 v[48:51], v[164:167], a[244:247], v[48:51]\n" \
"v_accvgpr_write_b32 a17, v231\n" \
"v_accvgpr_write_b32 a18, v232\n" \
"v_accvgpr_write_b32 a19, v233\n" \
"v_mfma_f32_16x16x32_bf16 v[52:55], v[168:171], a[228:231], v[52:55]\n" \
"v_cvt_pk_bf16_f32 v234, v40, v41\n" \
"v_cvt_pk_bf16_f32 v235, v42, v43\n" \
"v_cvt_pk_bf16_f32 v236, v44, v45\n" \
"ds_read_b128 a[84:87], v208 offset:4096\n" \
"v_mfma_f32_16x16x32_bf16 v[52:55], v[172:175], a[244:247], v[52:55]\n" \
"v_cvt_pk_bf16_f32 v237, v46, v47\n" \
"v_accvgpr_write_b32 a20, v234\n" \
"v_accvgpr_write_b32 a21, v235\n" \
"v_mfma_f32_16x16x32_bf16 v[56:59], v[176:179], a[228:231], v[56:59]\n" \
"v_accvgpr_write_b32 a22, v236\n" \
"v_accvgpr_write_b32 a23, v237\n" \
"v_cvt_pk_bf16_f32 v230, v16, v17\n" \
"ds_read_b128 a[88:91], v209 offset:4096\n" \
"v_mfma_f32_16x16x32_bf16 v[56:59], v[180:183], a[244:247], v[56:59]\n" \
"v_cvt_pk_bf16_f32 v231, v18, v19\n" \
"v_cvt_pk_bf16_f32 v232, v20, v21\n" \
"v_cvt_pk_bf16_f32 v233, v22, v23\n" \
"v_mfma_f32_16x16x32_bf16 v[60:63], v[184:187], a[228:231], v[60:63]\n" \
"v_accvgpr_write_b32 a8, v230\n" \
"v_accvgpr_write_b32 a9, v231\n" \
"v_accvgpr_write_b32 a10, v232\n" \
"ds_read_b128 a[92:95], v210 offset:4096\n" \
"v_mfma_f32_16x16x32_bf16 v[60:63], v[188:191], a[244:247], v[60:63]\n" \
"v_accvgpr_write_b32 a11, v233\n" \
"v_cvt_pk_bf16_f32 v234, v24, v25\n" \
"v_cvt_pk_bf16_f32 v235, v26, v27\n" \
"v_mfma_f32_16x16x32_bf16 v[80:83], v[160:163], a[232:235], v[80:83]\n" \
"v_cvt_pk_bf16_f32 v236, v28, v29\n" \
"v_cvt_pk_bf16_f32 v237, v30, v31\n" \
"v_accvgpr_write_b32 a12, v234\n" \
"v_mfma_f32_16x16x32_bf16 v[80:83], v[164:167], a[248:251], v[80:83]\n" \
"v_accvgpr_write_b32 a13, v235\n" \
"v_accvgpr_write_b32 a14, v236\n" \
"v_accvgpr_write_b32 a15, v237\n" \
"v_mfma_f32_16x16x32_bf16 v[84:87], v[168:171], a[232:235], v[84:87]\n" \
"v_cvt_pk_bf16_f32 v230, v64, v65\n" \
"v_cvt_pk_bf16_f32 v231, v66, v67\n" \
"v_cvt_pk_bf16_f32 v232, v68, v69\n" \
"v_mfma_f32_16x16x32_bf16 v[84:87], v[172:175], a[248:251], v[84:87]\n" \
"v_cvt_pk_bf16_f32 v233, v70, v71\n" \
"v_accvgpr_write_b32 a32, v230\n" \
"v_accvgpr_write_b32 a33, v231\n" \
"v_mfma_f32_16x16x32_bf16 v[88:91], v[176:179], a[232:235], v[88:91]\n" \
"v_accvgpr_write_b32 a34, v232\n" \
"v_accvgpr_write_b32 a35, v233\n" \
"v_cvt_pk_bf16_f32 v234, v72, v73\n" \
"v_mfma_f32_16x16x32_bf16 v[88:91], v[180:183], a[248:251], v[88:91]\n" \
"v_cvt_pk_bf16_f32 v235, v74, v75\n" \
"v_cvt_pk_bf16_f32 v236, v76, v77\n" \
"v_cvt_pk_bf16_f32 v237, v78, v79\n" \
"v_mfma_f32_16x16x32_bf16 v[92:95], v[184:187], a[232:235], v[92:95]\n" \
"v_accvgpr_write_b32 a36, v234\n" \
"v_accvgpr_write_b32 a37, v235\n" \
"v_accvgpr_write_b32 a38, v236\n" \
"v_mfma_f32_16x16x32_bf16 v[92:95], v[188:191], a[248:251], v[92:95]\n" \
"v_accvgpr_write_b32 a39, v237\n" \
"v_cvt_pk_bf16_f32 v230, v48, v49\n" \
"v_cvt_pk_bf16_f32 v231, v50, v51\n" \
"v_mfma_f32_16x16x32_bf16 v[112:115], v[160:163], a[236:239], v[112:115]\n" \
"v_cvt_pk_bf16_f32 v232, v52, v53\n" \
"v_cvt_pk_bf16_f32 v233, v54, v55\n" \
"v_accvgpr_write_b32 a24, v230\n" \
"v_mfma_f32_16x16x32_bf16 v[112:115], v[164:167], a[252:255], v[112:115]\n" \
"v_accvgpr_write_b32 a25, v231\n" \
"v_accvgpr_write_b32 a26, v232\n" \
"v_accvgpr_write_b32 a27, v233\n" \
"v_mfma_f32_16x16x32_bf16 v[116:119], v[168:171], a[236:239], v[116:119]\n" \
"v_cvt_pk_bf16_f32 v234, v56, v57\n" \
"v_cvt_pk_bf16_f32 v235, v58, v59\n" \
"v_cvt_pk_bf16_f32 v236, v60, v61\n" \
"v_mfma_f32_16x16x32_bf16 v[116:119], v[172:175], a[252:255], v[116:119]\n" \
"v_cvt_pk_bf16_f32 v237, v62, v63\n" \
"v_accvgpr_write_b32 a28, v234\n" \
"v_accvgpr_write_b32 a29, v235\n" \
"v_mfma_f32_16x16x32_bf16 v[120:123], v[176:179], a[236:239], v[120:123]\n" \
"v_accvgpr_write_b32 a30, v236\n" \
"v_accvgpr_write_b32 a31, v237\n" \
"v_cvt_pk_bf16_f32 v230, v96, v97\n" \
"v_mfma_f32_16x16x32_bf16 v[120:123], v[180:183], a[252:255], v[120:123]\n" \
"v_cvt_pk_bf16_f32 v231, v98, v99\n" \
"v_cvt_pk_bf16_f32 v232, v100, v101\n" \
"v_cvt_pk_bf16_f32 v233, v102, v103\n" \
"v_mfma_f32_16x16x32_bf16 v[124:127], v[184:187], a[236:239], v[124:127]\n" \
"v_accvgpr_write_b32 a48, v230\n" \
"v_accvgpr_write_b32 a49, v231\n" \
"v_accvgpr_write_b32 a50, v232\n" \
"v_mfma_f32_16x16x32_bf16 v[124:127], v[188:191], a[252:255], v[124:127]\n" \
"v_accvgpr_write_b32 a51, v233\n" \
"s_waitcnt vmcnt(63) lgkmcnt(0)\n" \
"s_nop 0\n" \
"s_mov_b32 s48, 65536\n" \
"s_mov_b32 s60, 0\n" \
"s_waitcnt lgkmcnt(0)\n" \
"v_mfma_f32_16x16x32_bf16 v[128:131], a[64:67], a[0:3], 0\n" \
"ds_read_b128 a[96:99], %[kb0] offset:8192\n" \
"v_cvt_pk_bf16_f32 v234, v80, v81\n" \
"v_cvt_pk_bf16_f32 v235, v82, v83\n" \
"v_mfma_f32_16x16x32_bf16 v[128:131], a[68:71], a[4:7], v[128:131]\n" \
"ds_read_b128 a[100:103], v208 offset:8192\n" \
"v_cvt_pk_bf16_f32 v236, v84, v85\n" \
"v_cvt_pk_bf16_f32 v237, v86, v87\n" \
"v_mfma_f32_16x16x32_bf16 v[128:131], a[72:75], a[8:11], v[128:131]\n" \
"ds_read_b128 a[104:107], v209 offset:8192\n" \
"v_accvgpr_write_b32 a40, v234\n" \
"v_accvgpr_write_b32 a41, v235\n" \
"v_mfma_f32_16x16x32_bf16 v[128:131], a[76:79], a[12:15], v[128:131]\n" \
"ds_read_b128 a[108:111], v210 offset:8192\n" \
"v_accvgpr_write_b32 a42, v236\n" \
"v_accvgpr_write_b32 a43, v237\n" \
"v_mfma_f32_16x16x32_bf16 v[132:135], a[80:83], a[0:3], 0\n" \
"ds_read_b128 a[112:115], %[kb0] offset:12288\n" \
"v_cvt_pk_bf16_f32 v230, v88, v89\n" \
"v_cvt_pk_bf16_f32 v231, v90, v91\n" \
"v_mfma_f32_16x16x32_bf16 v[132:135], a[84:87], a[4:7], v[132:135]\n" \
"ds_read_b128 a[116:119], v208 offset:12288\n" \
"v_cvt_pk_bf16_f32 v232, v92, v93\n" \
"v_cvt_pk_bf16_f32 v233, v94, v95\n" \
"v_mfma_f32_16x16x32_bf16 v[132:135], a[88:91], a[8:11], v[132:135]\n" \
"ds_read_b128 a[120:123], v209 offset:12288\n" \
"v_accvgpr_write_b32 a44, v230\n" \
"v_accvgpr_write_b32 a45, v231\n" \
"v_mfma_f32_16x16x32_bf16 v[132:135], a[92:95], a[12:15], v[132:135]\n" \
"ds_read_b128 a[124:127], v210 offset:12288\n" \
"v_accvgpr_write_b32 a46, v232\n" \
"v_accvgpr_write_b32 a47, v233\n" \
"v_mfma_f32_16x16x32_bf16 v[136:139], a[64:67], a[16:19], 0\n" \
"ds_read_b64_tr_b16 a[128:129], %[vvb] offset:0\n" \
"v_cvt_pk_bf16_f32 v234, v104, v105\n" \
"v_cvt_pk_bf16_f32 v235, v106, v107\n" \
"v_mfma_f32_16x16x32_bf16 v[136:139], a[68:71], a[20:23], v[136:139]\n" \
"ds_read_b64_tr_b16 a[130:131], %[vvb] offset:4096\n" \
"v_cvt_pk_bf16_f32 v236, v108, v109\n" \
"v_cvt_pk_bf16_f32 v237, v110, v111\n" \
"v_mfma_f32_16x16x32_bf16 v[136:139], a[72:75], a[24:27], v[136:139]\n" \
"ds_read_b64_tr_b16 a[132:133], v211 offset:0\n" \
"v_accvgpr_write_b32 a52, v234\n" \
"v_accvgpr_write_b32 a53, v235\n" \
"v_mfma_f32_16x16x32_bf16 v[136:139], a[76:79], a[28:31], v[136:139]\n" \
"ds_read_b64_tr_b16 a[134:135], v211 offset:4096\n" \
"v_accvgpr_write_b32 a54, v236\n" \
"v_accvgpr_write_b32 a55, v237\n" \
"v_mfma_f32_16x16x32_bf16 v[140:143], a[80:83], a[16:19], 0\n" \
"ds_read_b64_tr_b16 a[136:137], %[vvb] offset:512\n" \
"v_cvt_pk_bf16_f32 v230, v112, v113\n" \
"v_cvt_pk_bf16_f32 v231, v114, v115\n" \
"v_mfma_f32_16x16x32_bf16 v[140:143], a[84:87], a[20:23], v[140:143]\n" \
"ds_read_b64_tr_b16 a[138:139], %[vvb] offset:4608\n" \
"v_cvt_pk_bf16_f32 v232, v116, v117\n" \
"v_cvt_pk_bf16_f32 v233, v118, v119\n" \
"v_mfma_f32_16x16x32_bf16 v[140:143], a[88:91], a[24:27], v[140:143]\n" \
"ds_read_b64_tr_b16 a[140:141], v211 offset:512\n" \
"v_accvgpr_write_b32 a56, v230\n" \
"v_accvgpr_write_b32 a57, v231\n" \
"v_mfma_f32_16x16x32_bf16 v[140:143], a[92:95], a[28:31], v[140:143]\n" \
"ds_read_b64_tr_b16 a[142:143], v211 offset:4608\n" \
"v_accvgpr_write_b32 a58, v232\n" \
"v_accvgpr_write_b32 a59, v233\n" \
"v_mfma_f32_16x16x32_bf16 v[144:147], a[64:67], a[32:35], 0\n" \
"ds_read_b64_tr_b16 a[144:145], %[vvb] offset:1024\n" \
"v_cvt_pk_bf16_f32 v234, v120, v121\n" \
"v_cvt_pk_bf16_f32 v235, v122, v123\n" \
"v_mfma_f32_16x16x32_bf16 v[144:147], a[68:71], a[36:39], v[144:147]\n" \
"ds_read_b64_tr_b16 a[146:147], %[vvb] offset:5120\n" \
"v_cvt_pk_bf16_f32 v236, v124, v125\n" \
"v_cvt_pk_bf16_f32 v237, v126, v127\n" \
"v_mfma_f32_16x16x32_bf16 v[144:147], a[72:75], a[40:43], v[144:147]\n" \
"ds_read_b64_tr_b16 a[148:149], v211 offset:1024\n" \
"v_accvgpr_write_b32 a60, v234\n" \
"v_accvgpr_write_b32 a61, v235\n" \
"v_mfma_f32_16x16x32_bf16 v[144:147], a[76:79], a[44:47], v[144:147]\n" \
"ds_read_b64_tr_b16 a[150:151], v211 offset:5120\n" \
"v_accvgpr_write_b32 a62, v236\n" \
"v_accvgpr_write_b32 a63, v237\n" \
"v_mfma_f32_16x16x32_bf16 v[148:151], a[80:83], a[32:35], 0\n" \
"ds_read_b64_tr_b16 a[152:153], %[vvb] offset:1536\n" \
"v_max3_f32 v192, v128, v129, v130\n" \
"v_max3_f32 v193, v131, v132, v133\n" \
"v_mfma_f32_16x16x32_bf16 v[148:151], a[84:87], a[36:39], v[148:151]\n" \
"ds_read_b64_tr_b16 a[154:155], %[vvb] offset:5632\n" \
"v_max3_f32 v192, v192, v134, v135\n" \
"v_max_f32_e64 v225, v192, v193\n" \
"v_mfma_f32_16x16x32_bf16 v[148:151], a[88:91], a[40:43], v[148:151]\n" \
"ds_read_b64_tr_b16 a[156:157], v211 offset:1536\n" \
"v_max3_f32 v192, v136, v137, v138\n" \
"v_max3_f32 v193, v139, v140, v141\n" \
"v_mfma_f32_16x16x32_bf16 v[148:151], a[92:95], a[44:47], v[148:151]\n" \
"ds_read_b64_tr_b16 a[158:159], v211 offset:5632\n" \
"v_max3_f32 v192, v192, v142, v143\n" \
"v_max_f32_e64 v226, v192, v193\n" \
"v_mfma_f32_16x16x32_bf16 v[152:155], a[64:67], a[48:51], 0\n" \
"ds_read_b128 a[200:203], v228 offset:0\n" \
"s_mov_b32 s44, %[m0t]\n" \
"s_mov_b32 s45, 0\n" \
"v_mfma_f32_16x16x32_bf16 v[152:155], a[68:71], a[52:55], v[152:155]\n" \
"ds_read_b128 a[204:207], v228 offset:64\n" \
"v_mfma_f32_16x16x32_bf16 v[152:155], a[72:75], a[56:59], v[152:155]\n" \
"ds_read_b128 a[208:211], v228 offset:128\n" \
"v_mfma_f32_16x16x32_bf16 v[152:155], a[76:79], a[60:63], v[152:155]\n" \
"ds_read_b128 a[212:215], v228 offset:192\n" \
"v_mfma_f32_16x16x32_bf16 v[156:159], a[80:83], a[48:51], 0\n" \
"ds_read_b128 a[216:219], v228 offset:256\n" \
"v_mfma_f32_16x16x32_bf16 v[156:159], a[84:87], a[52:55], v[156:159]\n" \
"ds_read_b128 a[220:223], v228 offset:320\n" \
"v_mfma_f32_16x16x32_bf16 v[156:159], a[88:91], a[56:59], v[156:159]\n" \
"ds_read_b128 a[224:227], v228 offset:384\n" \
"v_max3_f32 v192, v144, v145, v146\n" \
"v_max3_f32 v193, v147, v148, v149\n" \
"v_mfma_f32_16x16x32_bf16 v[156:159], a[92:95], a[60:63], v[156:159]\n" \
"ds_read_b128 a[228:231], v228 offset:448\n" \
"v_max3_f32 v192, v192, v150, v151\n" \
"v_max_f32_e64 v227, v192, v193\n" \
"s_nop 3\n" \
"s_nop 0\n" \
"v_max3_f32 v230, v152, v153, v154\n" \
"v_max3_f32 v231, v155, v156, v157\n" \
"v_max3_f32 v230, v230, v158, v159\n" \
"v_max_f32_e32 v228, v230, v231\n" \
"v_mov_b32_e32 v232, v225\n" \
"v_mov_b32_e32 v233, v226\n" \
"v_mov_b32_e32 v234, v227\n" \
"v_mov_b32_e32 v235, v228\n" \
"s_nop 0\n" \
"v_permlane16_swap_b32_e32 v225, v232\n" \
"v_permlane16_swap_b32_e32 v226, v233\n" \
"v_permlane16_swap_b32_e32 v227, v234\n" \
"v_permlane16_swap_b32_e32 v228, v235\n" \
"v_max_f32_e32 v225, v225, v232\n" \
"v_max_f32_e32 v226, v226, v233\n" \
"v_max_f32_e32 v227, v227, v234\n" \
"v_max_f32_e32 v228, v228, v235\n" \
"v_mov_b32_e32 v232, v225\n" \
"v_mov_b32_e32 v233, v226\n" \
"v_mov_b32_e32 v234, v227\n" \
"v_mov_b32_e32 v235, v228\n" \
"s_nop 0\n" \
"v_permlane32_swap_b32_e32 v225, v232\n" \
"v_permlane32_swap_b32_e32 v226, v233\n" \
"v_permlane32_swap_b32_e32 v227, v234\n" \
"v_permlane32_swap_b32_e32 v228, v235\n" \
"v_max_f32_e32 v225, v225, v232\n" \
"v_max_f32_e32 v226, v226, v233\n" \
"v_max_f32_e32 v227, v227, v234\n" \
"v_max_f32_e64 v228, v228, v235\n" \
"Lhavem_%=:\n" \
"s_nop 3\n" \
"v_mov_b64_e32 v[176:177], 0\n" \
"v_mov_b64_e32 v[178:179], 0\n" \
"v_mov_b64_e32 v[180:181], 0\n" \
"v_mov_b64_e32 v[182:183], 0\n" \
"v_sub_f32_e32 v160, 0, v225\n" \
"v_sub_f32_e32 v161, 0, v225\n" \
"v_sub_f32_e32 v162, 0, v225\n" \
"v_sub_f32_e32 v163, 0, v225\n" \
"v_sub_f32_e32 v164, 0, v226\n" \
"v_sub_f32_e32 v165, 0, v226\n" \
"v_sub_f32_e32 v166, 0, v226\n" \
"v_sub_f32_e32 v167, 0, v226\n" \
"v_sub_f32_e32 v168, 0, v227\n" \
"v_sub_f32_e32 v169, 0, v227\n" \
"v_sub_f32_e32 v170, 0, v227\n" \
"v_sub_f32_e32 v171, 0, v227\n" \
"v_sub_f32_e32 v172, 0, v228\n" \
"v_sub_f32_e32 v173, 0, v228\n" \
"v_sub_f32_e32 v174, 0, v228\n" \
"v_sub_f32_e32 v175, 0, v228\n" \
"v_sub_f32_e32 v128, v128, v225\n" \
"v_sub_f32_e32 v129, v129, v225\n" \
"v_sub_f32_e32 v130, v130, v225\n" \
"v_sub_f32_e32 v131, v131, v225\n" \
"v_sub_f32_e32 v132, v132, v225\n" \
"v_sub_f32_e32 v133, v133, v225\n" \
"v_sub_f32_e32 v134, v134, v225\n" \
"v_sub_f32_e32 v135, v135, v225\n" \
"v_sub_f32_e32 v136, v136, v226\n" \
"v_sub_f32_e32 v137, v137, v226\n" \
"v_sub_f32_e32 v138, v138, v226\n" \
"v_sub_f32_e32 v139, v139, v226\n" \
"v_sub_f32_e32 v140, v140, v226\n" \
"v_sub_f32_e32 v141, v141, v226\n" \
"v_sub_f32_e32 v142, v142, v226\n" \
"v_sub_f32_e32 v143, v143, v226\n" \
"v_sub_f32_e32 v144, v144, v227\n" \
"v_sub_f32_e32 v145, v145, v227\n" \
"v_sub_f32_e32 v146, v146, v227\n" \
"v_sub_f32_e32 v147, v147, v227\n" \
"v_sub_f32_e32 v148, v148, v227\n" \
"v_sub_f32_e32 v149, v149, v227\n" \
"v_sub_f32_e32 v150, v150, v227\n" \
"v_sub_f32_e32 v151, v151, v227\n" \
"v_sub_f32_e32 v152, v152, v228\n" \
"v_sub_f32_e32 v153, v153, v228\n" \
"v_sub_f32_e32 v154, v154, v228\n" \
"v_sub_f32_e32 v155, v155, v228\n" \
"v_sub_f32_e32 v156, v156, v228\n" \
"v_sub_f32_e32 v157, v157, v228\n" \
"v_sub_f32_e32 v158, v158, v228\n" \
"v_sub_f32_e32 v159, v159, v228\n" \
"v_exp_f32_e32 v128, v128\n" \
"v_exp_f32_e32 v129, v129\n" \
"v_exp_f32_e32 v130, v130\n" \
"v_exp_f32_e32 v131, v131\n" \
"v_exp_f32_e32 v132, v132\n" \
"v_exp_f32_e32 v133, v133\n" \
"v_exp_f32_e32 v134, v134\n" \
"v_exp_f32_e32 v135, v135\n" \
"v_exp_f32_e32 v136, v136\n" \
"v_exp_f32_e32 v137, v137\n" \
"v_exp_f32_e32 v138, v138\n" \
"v_exp_f32_e32 v139, v139\n" \
"v_exp_f32_e32 v140, v140\n" \
"v_exp_f32_e32 v141, v141\n" \
"v_exp_f32_e32 v142, v142\n" \
"v_exp_f32_e32 v143, v143\n" \
"v_exp_f32_e32 v144, v144\n" \
"v_exp_f32_e32 v145, v145\n" \
"v_exp_f32_e32 v146, v146\n" \
"v_exp_f32_e32 v147, v147\n" \
"v_exp_f32_e32 v148, v148\n" \
"v_exp_f32_e32 v149, v149\n" \
"v_exp_f32_e32 v150, v150\n" \
"v_exp_f32_e32 v151, v151\n" \
"v_exp_f32_e32 v152, v152\n" \
"v_exp_f32_e32 v153, v153\n" \
"v_exp_f32_e32 v154, v154\n" \
"v_exp_f32_e64 v155, v155\n" \
"v_bfe_i32 v228, v224, 7, 1\n" \
"v_and_b32_e32 v228, 0xaaaa, v228\n" \
"v_or_b32_e32 v228, 0x4444, v228\n" \
"v_cvt_pk_bf16_f32 v184, v128, v129\n" \
"v_cvt_pk_bf16_f32 v185, v130, v131\n" \
"v_cvt_pk_bf16_f32 v186, v132, v133\n" \
"v_cvt_pk_bf16_f32 v187, v134, v135\n" \
"v_cvt_pk_bf16_f32 v188, v136, v137\n" \
"v_cvt_pk_bf16_f32 v189, v138, v139\n" \
"v_cvt_pk_bf16_f32 v190, v140, v141\n" \
"v_cvt_pk_bf16_f32 v191, v142, v143\n" \
"v_cvt_pk_bf16_f32 v216, v144, v145\n" \
"v_cvt_pk_bf16_f32 v217, v146, v147\n" \
"v_cvt_pk_bf16_f32 v218, v148, v149\n" \
"v_cvt_pk_bf16_f32 v219, v150, v151\n" \
"v_lshrrev_b32_e64 v235, 3, v224\n" \
"s_waitcnt vmcnt(2) lgkmcnt(0)\n" \
"s_barrier\n" \
"v_mfma_f32_16x16x32_bf16 v[128:131], a[96:99], a[0:3], v[160:163]\n" \
"v_exp_f32_e64 v156, v156\n" \
"v_mfma_f32_16x16x32_bf16 v[128:131], a[100:103], a[4:7], v[128:131]\n" \
"s_add_u32 m0, %[m0b], 114688\n" \
"v_mfma_f32_16x16x32_bf16 v[128:131], a[104:107], a[8:11], v[128:131]\n" \
"v_exp_f32_e64 v157, v157\n" \
"v_mfma_f32_16x16x32_bf16 v[128:131], a[108:111], a[12:15], v[128:131]\n" \
"v_cvt_pk_bf16_f32 v220, v152, v153\n" \
"v_cvt_pk_bf16_f32 v221, v154, v155\n" \
"v_mfma_f32_16x16x32_bf16 v[132:135], a[112:115], a[0:3], v[160:163]\n" \
"v_exp_f32_e64 v158, v158\n" \
"v_mfma_f32_16x16x32_bf16 v[132:135], a[116:119], a[4:7], v[132:135]\n" \
"v_mfma_f32_16x16x32_bf16 v[132:135], a[120:123], a[8:11], v[132:135]\n" \
"buffer_load_dwordx4 %[vd0], %[srdv], s48 offen offset:0 lds\n" \
"v_mfma_f32_16x16x32_bf16 v[132:135], a[124:127], a[12:15], v[132:135]\n" \
"v_exp_f32_e64 v159, v159\n" \
"v_mfma_f32_16x16x32_bf16 v[0:3], a[128:131], v[184:187], 0\n" \
"ds_read_b128 a[64:67], %[kb0] offset:16384\n" \
"v_mfma_f32_16x16x32_bf16 v[32:35], a[128:131], v[188:191], 0\n" \
"v_exp_f32_e64 v128, v128\n" \
"v_mfma_f32_16x16x32_bf16 v[4:7], a[132:135], v[184:187], 0\n" \
"v_cvt_pk_bf16_f32 v222, v156, v157\n" \
"v_cvt_pk_bf16_f32 v223, v158, v159\n" \
"v_mfma_f32_16x16x32_bf16 v[36:39], a[132:135], v[188:191], 0\n" \
"v_exp_f32_e64 v129, v129\n" \
"v_mfma_f32_16x16x32_bf16 v[8:11], a[136:139], v[184:187], 0\n" \
"ds_read_b128 a[68:71], v208 offset:16384\n" \
"v_mfma_f32_16x16x32_bf16 v[40:43], a[136:139], v[188:191], 0\n" \
"v_exp_f32_e64 v130, v130\n" \
"v_mfma_f32_16x16x32_bf16 v[12:15], a[140:143], v[184:187], 0\n" \
"ds_read_b128 a[72:75], v209 offset:16384\n" \
"v_mfma_f32_16x16x32_bf16 v[44:47], a[140:143], v[188:191], 0\n" \
"v_exp_f32_e64 v131, v131\n" \
"v_smfmac_f32_16x16x64_bf16 v[176:179], a[192:195], v[184:191], v228\n" \
"v_mfma_f32_16x16x32_bf16 v[136:139], a[96:99], a[16:19], v[164:167]\n" \
"v_exp_f32_e64 v132, v132\n" \
"v_mfma_f32_16x16x32_bf16 v[136:139], a[100:103], a[20:23], v[136:139]\n" \
"ds_read_b128 a[76:79], v210 offset:16384\n" \
"v_mfma_f32_16x16x32_bf16 v[136:139], a[104:107], a[24:27], v[136:139]\n" \
"v_exp_f32_e64 v133, v133\n" \
"v_mfma_f32_16x16x32_bf16 v[136:139], a[108:111], a[28:31], v[136:139]\n" \
"v_cvt_pk_bf16_f32 v192, v128, v129\n" \
"v_cvt_pk_bf16_f32 v193, v130, v131\n" \
"v_mfma_f32_16x16x32_bf16 v[140:143], a[112:115], a[16:19], v[164:167]\n" \
"v_exp_f32_e64 v134, v134\n" \
"v_mfma_f32_16x16x32_bf16 v[140:143], a[116:119], a[20:23], v[140:143]\n" \
"ds_read_b128 a[80:83], %[kb0] offset:20480\n" \
"v_mfma_f32_16x16x32_bf16 v[140:143], a[120:123], a[24:27], v[140:143]\n" \
"buffer_load_dwordx4 v215, %[srdv], s48 offen offset:1024 lds\n" \
"v_mfma_f32_16x16x32_bf16 v[140:143], a[124:127], a[28:31], v[140:143]\n" \
"v_exp_f32_e64 v135, v135\n" \
"v_mfma_f32_16x16x32_bf16 v[16:19], a[144:147], v[184:187], 0\n" \
"ds_read_b128 a[84:87], v208 offset:20480\n" \
"v_mfma_f32_16x16x32_bf16 v[48:51], a[144:147], v[188:191], 0\n" \
"v_exp_f32_e64 v136, v136\n" \
"v_mfma_f32_16x16x32_bf16 v[20:23], a[148:151], v[184:187], 0\n" \
"v_cvt_pk_bf16_f32 v194, v132, v133\n" \
"v_cvt_pk_bf16_f32 v195, v134, v135\n" \
"v_mfma_f32_16x16x32_bf16 v[52:55], a[148:151], v[188:191], 0\n" \
"v_exp_f32_e64 v137, v137\n" \
"v_mfma_f32_16x16x32_bf16 v[24:27], a[152:155], v[184:187], 0\n" \
"v_exp_f32_e64 v138, v138\n" \
"v_mfma_f32_16x16x32_bf16 v[56:59], a[152:155], v[188:191], 0\n" \
"ds_read_b128 a[88:91], v209 offset:20480\n" \
"v_mfma_f32_16x16x32_bf16 v[28:31], a[156:159], v[184:187], 0\n" \
"v_exp_f32_e64 v139, v139\n" \
"v_mfma_f32_16x16x32_bf16 v[60:63], a[156:159], v[188:191], 0\n" \
"v_mfma_f32_16x16x32_bf16 v[144:147], a[96:99], a[32:35], v[168:171]\n" \
"v_exp_f32_e64 v140, v140\n" \
"v_mfma_f32_16x16x32_bf16 v[144:147], a[100:103], a[36:39], v[144:147]\n" \
"ds_read_b128 a[92:95], v210 offset:20480\n" \
"v_mfma_f32_16x16x32_bf16 v[144:147], a[104:107], a[40:43], v[144:147]\n" \
"v_exp_f32_e64 v141, v141\n" \
"v_mfma_f32_16x16x32_bf16 v[144:147], a[108:111], a[44:47], v[144:147]\n" \
"v_cvt_pk_bf16_f32 v196, v136, v137\n" \
"v_cvt_pk_bf16_f32 v197, v138, v139\n" \
"v_mfma_f32_16x16x32_bf16 v[148:151], a[112:115], a[32:35], v[168:171]\n" \
"v_exp_f32_e64 v142, v142\n" \
"v_mfma_f32_16x16x32_bf16 v[148:151], a[116:119], a[36:39], v[148:151]\n" \
"v_mfma_f32_16x16x32_bf16 v[148:151], a[120:123], a[40:43], v[148:151]\n" \
"buffer_load_dwordx4 %[vd0], %[srdv], s48 offen offset:2048 lds\n" \
"v_mfma_f32_16x16x32_bf16 v[148:151], a[124:127], a[44:47], v[148:151]\n" \
"v_exp_f32_e64 v143, v143\n" \
"v_mfma_f32_16x16x32_bf16 v[64:67], a[128:131], v[216:219], 0\n" \
"ds_read_b64_tr_b16 a[160:161], %[vvb] offset:8192\n" \
"v_mfma_f32_16x16x32_bf16 v[96:99], a[128:131], v[220:223], 0\n" \
"v_exp_f32_e64 v144, v144\n" \
"v_mfma_f32_16x16x32_bf16 v[68:71], a[132:135], v[216:219], 0\n" \
"v_cvt_pk_bf16_f32 v198, v140, v141\n" \
"v_cvt_pk_bf16_f32 v199, v142, v143\n" \
"v_mfma_f32_16x16x32_bf16 v[100:103], a[132:135], v[220:223], 0\n" \
"v_exp_f32_e64 v145, v145\n" \
"v_mfma_f32_16x16x32_bf16 v[72:75], a[136:139], v[216:219], 0\n" \
"ds_read_b64_tr_b16 a[162:163], %[vvb] offset:12288\n" \
"ds_read_b64_tr_b16 a[164:165], v211 offset:8192\n" \
"v_mfma_f32_16x16x32_bf16 v[104:107], a[136:139], v[220:223], 0\n" \
"v_exp_f32_e64 v146, v146\n" \
"v_mfma_f32_16x16x32_bf16 v[76:79], a[140:143], v[216:219], 0\n" \
"ds_read_b64_tr_b16 a[166:167], v211 offset:12288\n" \
"v_mfma_f32_16x16x32_bf16 v[108:111], a[140:143], v[220:223], 0\n" \
"v_exp_f32_e64 v147, v147\n" \
"v_smfmac_f32_16x16x64_bf16 v[180:183], a[192:195], v[216:223], v228\n" \
"ds_read_b64_tr_b16 a[168:169], %[vvb] offset:8704\n" \
"v_mfma_f32_16x16x32_bf16 v[152:155], a[96:99], a[48:51], v[172:175]\n" \
"v_exp_f32_e64 v148, v148\n" \
"v_mfma_f32_16x16x32_bf16 v[152:155], a[100:103], a[52:55], v[152:155]\n" \
"ds_read_b64_tr_b16 a[170:171], %[vvb] offset:12800\n" \
"v_mfma_f32_16x16x32_bf16 v[152:155], a[104:107], a[56:59], v[152:155]\n" \
"v_exp_f32_e64 v149, v149\n" \
"v_mfma_f32_16x16x32_bf16 v[152:155], a[108:111], a[60:63], v[152:155]\n" \
"v_cvt_pk_bf16_f32 v200, v144, v145\n" \
"v_cvt_pk_bf16_f32 v201, v146, v147\n" \
"v_mfma_f32_16x16x32_bf16 v[156:159], a[112:115], a[48:51], v[172:175]\n" \
"v_exp_f32_e64 v150, v150\n" \
"v_mfma_f32_16x16x32_bf16 v[156:159], a[116:119], a[52:55], v[156:159]\n" \
"ds_read_b64_tr_b16 a[172:173], v211 offset:8704\n" \
"v_mfma_f32_16x16x32_bf16 v[156:159], a[120:123], a[56:59], v[156:159]\n" \
"buffer_load_dwordx4 v215, %[srdv], s48 offen offset:3072 lds\n" \
"v_mfma_f32_16x16x32_bf16 v[156:159], a[124:127], a[60:63], v[156:159]\n" \
"v_exp_f32_e64 v151, v151\n" \
"v_mfma_f32_16x16x32_bf16 v[80:83], a[144:147], v[216:219], 0\n" \
"ds_read_b64_tr_b16 a[174:175], v211 offset:12800\n" \
"ds_read_b64_tr_b16 a[176:177], %[vvb] offset:9216\n" \
"v_mfma_f32_16x16x32_bf16 v[112:115], a[144:147], v[220:223], 0\n" \
"v_exp_f32_e64 v152, v152\n" \
"v_mfma_f32_16x16x32_bf16 v[84:87], a[148:151], v[216:219], 0\n" \
"v_cvt_pk_bf16_f32 v202, v148, v149\n" \
"v_cvt_pk_bf16_f32 v203, v150, v151\n" \
"v_mfma_f32_16x16x32_bf16 v[116:119], a[148:151], v[220:223], 0\n" \
"v_exp_f32_e64 v153, v153\n" \
"v_mfma_f32_16x16x32_bf16 v[88:91], a[152:155], v[216:219], 0\n" \
"v_exp_f32_e64 v154, v154\n" \
"v_mfma_f32_16x16x32_bf16 v[120:123], a[152:155], v[220:223], 0\n" \
"ds_read_b64_tr_b16 a[178:179], %[vvb] offset:13312\n" \
"v_mfma_f32_16x16x32_bf16 v[92:95], a[156:159], v[216:219], 0\n" \
"v_exp_f32_e64 v155, v155\n" \
"v_mov_b64_e32 v[216:217], 0\n" \
"v_mov_b64_e32 v[218:219], 0\n" \
"v_mfma_f32_16x16x32_bf16 v[124:127], a[156:159], v[220:223], 0\n" \
"ds_read_b64_tr_b16 a[180:181], v211 offset:9216\n" \
"s_waitcnt lgkmcnt(13)\n" \
"s_nop 0\n" \
"LmA_%=:\n" \
"v_mfma_f32_16x16x32_bf16 v[128:131], a[64:67], a[0:3], v[160:163]\n" \
"v_exp_f32_e64 v156, v156\n" \
"v_mfma_f32_16x16x32_bf16 v[128:131], a[68:71], a[4:7], v[128:131]\n" \
"ds_read_b64_tr_b16 a[182:183], v211 offset:13312\n" \
"s_add_u32 m0, %[m0b], 0\n" \
"v_mfma_f32_16x16x32_bf16 v[128:131], a[72:75], a[8:11], v[128:131]\n" \
"v_exp_f32_e32 v157, v157\n" \
"v_mfma_f32_16x16x32_bf16 v[128:131], a[76:79], a[12:15], v[128:131]\n" \
"v_cvt_pk_bf16_f32 v204, v152, v153\n" \
"v_cvt_pk_bf16_f32 v205, v154, v155\n" \
"v_mfma_f32_16x16x32_bf16 v[132:135], a[80:83], a[0:3], v[160:163]\n" \
"v_exp_f32_e64 v158, v158\n" \
"v_mfma_f32_16x16x32_bf16 v[132:135], a[84:87], a[4:7], v[132:135]\n" \
"ds_read_b64_tr_b16 a[184:185], %[vvb] offset:9728\n" \
"s_waitcnt lgkmcnt(5)\n" \
"v_mfma_f32_16x16x32_bf16 v[132:135], a[88:91], a[8:11], v[132:135]\n" \
"buffer_load_dwordx4 %[kd0], %[srdk], s48 offen offset:0 lds\n" \
"v_mfma_f32_16x16x32_bf16 v[132:135], a[92:95], a[12:15], v[132:135]\n" \
"v_exp_f32_e32 v159, v159\n" \
"v_mfma_f32_16x16x32_bf16 v[64:67], a[128:131], v[216:219], v[64:67]\n" \
"ds_read_b64_tr_b16 a[186:187], %[vvb] offset:13824\n" \
"ds_read_b64_tr_b16 a[188:189], v211 offset:9728\n" \
"v_mfma_f32_16x16x32_bf16 v[64:67], a[160:163], v[200:203], v[64:67]\n" \
"v_exp_f32_e64 v128, v128\n" \
"v_mfma_f32_16x16x32_bf16 v[68:71], a[132:135], v[216:219], v[68:71]\n" \
"v_cvt_pk_bf16_f32 v206, v156, v157\n" \
"v_cvt_pk_bf16_f32 v207, v158, v159\n" \
"v_mfma_f32_16x16x32_bf16 v[68:71], a[164:167], v[200:203], v[68:71]\n" \
"v_exp_f32_e64 v129, v129\n" \
"v_mfma_f32_16x16x32_bf16 v[72:75], a[136:139], v[216:219], v[72:75]\n" \
"ds_read_b64_tr_b16 a[190:191], v211 offset:13824\n" \
"ds_read_b64_tr_b16 a[128:129], %[vvb] offset:16384\n" \
"s_add_u32 s60, s60, 1\n" \
"s_nop 0\n" \
"v_readlane_b32 s58, %[tlo], s60\n" \
"v_mfma_f32_16x16x32_bf16 v[72:75], a[168:171], v[200:203], v[72:75]\n" \
"v_exp_f32_e64 v130, v130\n" \
"v_mfma_f32_16x16x32_bf16 v[76:79], a[140:143], v[216:219], v[76:79]\n" \
"ds_read_b64_tr_b16 a[130:131], %[vvb] offset:20480\n" \
"ds_read_b64_tr_b16 a[132:133], v211 offset:16384\n" \
"v_readlane_b32 s59, %[thi], s60\n" \
"v_mfma_f32_16x16x32_bf16 v[76:79], a[172:175], v[200:203], v[76:79]\n" \
"v_exp_f32_e64 v131, v131\n" \
"v_smfmac_f32_16x16x64_bf16 v[176:179], a[192:195], v[192:199], v228\n" \
"ds_read_b64_tr_b16 a[134:135], v211 offset:20480\n" \
"ds_read_b64_tr_b16 a[136:137], %[vvb] offset:16896\n" \
"v_mfma_f32_16x16x32_bf16 v[136:139], a[64:67], a[16:19], v[164:167]\n" \
"v_exp_f32_e64 v132, v132\n" \
"v_mfma_f32_16x16x32_bf16 v[136:139], a[68:71], a[20:23], v[136:139]\n" \
"ds_read_b64_tr_b16 a[138:139], %[vvb] offset:20992\n" \
"ds_read_b64_tr_b16 a[140:141], v211 offset:16896\n" \
"v_mfma_f32_16x16x32_bf16 v[136:139], a[72:75], a[24:27], v[136:139]\n" \
"v_exp_f32_e64 v133, v133\n" \
"v_mfma_f32_16x16x32_bf16 v[136:139], a[76:79], a[28:31], v[136:139]\n" \
"v_cvt_pk_bf16_f32 v184, v128, v129\n" \
"v_cvt_pk_bf16_f32 v185, v130, v131\n" \
"v_mfma_f32_16x16x32_bf16 v[140:143], a[80:83], a[16:19], v[164:167]\n" \
"v_exp_f32_e64 v134, v134\n" \
"v_mfma_f32_16x16x32_bf16 v[140:143], a[84:87], a[20:23], v[140:143]\n" \
"ds_read_b64_tr_b16 a[142:143], v211 offset:20992\n" \
"s_waitcnt lgkmcnt(8)\n" \
"v_mfma_f32_16x16x32_bf16 v[140:143], a[88:91], a[24:27], v[140:143]\n" \
"buffer_load_dwordx4 v212, %[srdk], s48 offen offset:1024 lds\n" \
"v_mfma_f32_16x16x32_bf16 v[140:143], a[92:95], a[28:31], v[140:143]\n" \
"v_exp_f32_e32 v135, v135\n" \
"v_mfma_f32_16x16x32_bf16 v[80:83], a[144:147], v[216:219], v[80:83]\n" \
"ds_read_b64_tr_b16 a[144:145], %[vvb] offset:17408\n" \
"ds_read_b64_tr_b16 a[146:147], %[vvb] offset:21504\n" \
"v_mfma_f32_16x16x32_bf16 v[80:83], a[176:179], v[200:203], v[80:83]\n" \
"v_exp_f32_e64 v136, v136\n" \
"v_mfma_f32_16x16x32_bf16 v[84:87], a[148:151], v[216:219], v[84:87]\n" \
"v_cvt_pk_bf16_f32 v186, v132, v133\n" \
"v_cvt_pk_bf16_f32 v187, v134, v135\n" \
"v_mfma_f32_16x16x32_bf16 v[84:87], a[180:183], v[200:203], v[84:87]\n" \
"v_exp_f32_e64 v137, v137\n" \
"v_mfma_f32_16x16x32_bf16 v[88:91], a[152:155], v[216:219], v[88:91]\n" \
".if . - adaln_body != 12288\n" \
".error \"size model\"\n" \
".endif\n" \
"Lk2_%=:\n" \
"s_cbranch_execz Lk3_%=\n" \
"s_nop 0\n" \
"v_exp_f32_e64 v138, v138\n" \
"v_mfma_f32_16x16x32_bf16 v[88:91], a[184:187], v[200:203], v[88:91]\n" \
"ds_read_b64_tr_b16 a[148:149], v211 offset:17408\n" \
"ds_read_b64_tr_b16 a[150:151], v211 offset:21504\n" \
"v_mfma_f32_16x16x32_bf16 v[92:95], a[156:159], v[216:219], v[92:95]\n" \
"v_exp_f32_e64 v139, v139\n" \
"v_mfma_f32_16x16x32_bf16 v[92:95], a[188:191], v[200:203], v[92:95]\n" \
"ds_read_b64_tr_b16 a[152:153], %[vvb] offset:17920\n" \
"v_mfma_f32_16x16x32_bf16 v[144:147], a[64:67], a[32:35], v[168:171]\n" \
"v_exp_f32_e64 v140, v140\n" \
"v_mfma_f32_16x16x32_bf16 v[144:147], a[68:71], a[36:39], v[144:147]\n" \
"ds_read_b64_tr_b16 a[154:155], %[vvb] offset:22016\n" \
"ds_read_b64_tr_b16 a[156:157], v211 offset:17920\n" \
"v_mfma_f32_16x16x32_bf16 v[144:147], a[72:75], a[40:43], v[144:147]\n" \
"v_exp_f32_e64 v141, v141\n" \
"v_mfma_f32_16x16x32_bf16 v[144:147], a[76:79], a[44:47], v[144:147]\n" \
"v_cvt_pk_bf16_f32 v188, v136, v137\n" \
"v_cvt_pk_bf16_f32 v189, v138, v139\n" \
"v_mfma_f32_16x16x32_bf16 v[148:151], a[80:83], a[32:35], v[168:171]\n" \
"v_exp_f32_e64 v142, v142\n" \
"v_mfma_f32_16x16x32_bf16 v[148:151], a[84:87], a[36:39], v[148:151]\n" \
"ds_read_b64_tr_b16 a[158:159], v211 offset:22016\n" \
"s_waitcnt lgkmcnt(8)\n" \
"v_mfma_f32_16x16x32_bf16 v[148:151], a[88:91], a[40:43], v[148:151]\n" \
"buffer_load_dwordx4 v213, %[srdk], s48 offen offset:2048 lds\n" \
"v_mfma_f32_16x16x32_bf16 v[148:151], a[92:95], a[44:47], v[148:151]\n" \
"v_exp_f32_e32 v143, v143\n" \
"v_mfma_f32_16x16x32_bf16 v[0:3], a[160:163], v[192:195], v[0:3]\n" \
"ds_read_b128 a[96:99], %[kb0] offset:24576\n" \
"v_mfma_f32_16x16x32_bf16 v[0:3], a[128:131], v[184:187], v[0:3]\n" \
"v_exp_f32_e64 v144, v144\n" \
"v_mfma_f32_16x16x32_bf16 v[4:7], a[164:167], v[192:195], v[4:7]\n" \
"v_cvt_pk_bf16_f32 v190, v140, v141\n" \
"v_cvt_pk_bf16_f32 v191, v142, v143\n" \
"v_mfma_f32_16x16x32_bf16 v[4:7], a[132:135], v[184:187], v[4:7]\n" \
"v_exp_f32_e64 v145, v145\n" \
"v_mfma_f32_16x16x32_bf16 v[8:11], a[168:171], v[192:195], v[8:11]\n" \
"ds_read_b128 a[100:103], v208 offset:24576\n" \
"v_mfma_f32_16x16x32_bf16 v[8:11], a[136:139], v[184:187], v[8:11]\n" \
"v_exp_f32_e64 v146, v146\n" \
"v_mfma_f32_16x16x32_bf16 v[12:15], a[172:175], v[192:195], v[12:15]\n" \
"ds_read_b128 a[104:107], v209 offset:24576\n" \
"v_mfma_f32_16x16x32_bf16 v[12:15], a[140:143], v[184:187], v[12:15]\n" \
"v_exp_f32_e64 v147, v147\n" \
"v_smfmac_f32_16x16x64_bf16 v[180:183], a[192:195], v[200:207], v228\n" \
"ds_read_b128 a[108:111], v210 offset:24576\n" \
"v_mfma_f32_16x16x32_bf16 v[152:155], a[64:67], a[48:51], v[172:175]\n" \
"v_exp_f32_e64 v148, v148\n" \
"v_mfma_f32_16x16x32_bf16 v[152:155], a[68:71], a[52:55], v[152:155]\n" \
"ds_read_b128 a[112:115], %[kb0] offset:28672\n" \
"v_mfma_f32_16x16x32_bf16 v[152:155], a[72:75], a[56:59], v[152:155]\n" \
"v_exp_f32_e64 v149, v149\n" \
"v_mfma_f32_16x16x32_bf16 v[152:155], a[76:79], a[60:63], v[152:155]\n" \
"v_cvt_pk_bf16_f32 v216, v144, v145\n" \
"v_cvt_pk_bf16_f32 v217, v146, v147\n" \
"v_mfma_f32_16x16x32_bf16 v[156:159], a[80:83], a[48:51], v[172:175]\n" \
"v_exp_f32_e64 v150, v150\n" \
"v_mfma_f32_16x16x32_bf16 v[156:159], a[84:87], a[52:55], v[156:159]\n" \
"ds_read_b128 a[116:119], v208 offset:28672\n" \
"s_waitcnt lgkmcnt(6)\n" \
"v_mfma_f32_16x16x32_bf16 v[156:159], a[88:91], a[56:59], v[156:159]\n" \
"buffer_load_dwordx4 v214, %[srdk], s48 offen offset:3072 lds\n" \
"v_mfma_f32_16x16x32_bf16 v[156:159], a[92:95], a[60:63], v[156:159]\n" \
"v_exp_f32_e32 v151, v151\n" \
"v_mfma_f32_16x16x32_bf16 v[16:19], a[176:179], v[192:195], v[16:19]\n" \
"ds_read_b128 a[120:123], v209 offset:28672\n" \
"v_mfma_f32_16x16x32_bf16 v[16:19], a[144:147], v[184:187], v[16:19]\n" \
"v_exp_f32_e64 v152, v152\n" \
"v_mfma_f32_16x16x32_bf16 v[20:23], a[180:183], v[192:195], v[20:23]\n" \
"v_cvt_pk_bf16_f32 v218, v148, v149\n" \
"v_cvt_pk_bf16_f32 v219, v150, v151\n" \
"v_mfma_f32_16x16x32_bf16 v[20:23], a[148:151], v[184:187], v[20:23]\n" \
"v_exp_f32_e64 v153, v153\n" \
"v_mfma_f32_16x16x32_bf16 v[24:27], a[184:187], v[192:195], v[24:27]\n" \
"v_exp_f32_e64 v154, v154\n" \
"v_mfma_f32_16x16x32_bf16 v[24:27], a[152:155], v[184:187], v[24:27]\n" \
"ds_read_b128 a[124:127], v210 offset:28672\n" \
"s_add_u32 s48, s48, 0x4000\n" \
"s_mov_b32 m0, s44\n" \
"s_addk_i32 s44, 0x400\n" \
"buffer_load_dwordx4 v229, s[64:67], s45 offen offset:0 nt lds\n" \
"s_add_u32 s45, s45, 393216\n" \
"v_mfma_f32_16x16x32_bf16 v[28:31], a[188:191], v[192:195], v[28:31]\n" \
"v_exp_f32_e64 v155, v155\n" \
"v_mfma_f32_16x16x32_bf16 v[28:31], a[156:159], v[184:187], v[28:31]\n" \
"s_waitcnt vmcnt(14) lgkmcnt(2)\n" \
"s_barrier\n" \
"v_mfma_f32_16x16x32_bf16 v[128:131], a[96:99], a[0:3], v[160:163]\n" \
"v_exp_f32_e64 v156, v156\n" \
"v_mfma_f32_16x16x32_bf16 v[128:131], a[100:103], a[4:7], v[128:131]\n" \
"s_add_u32 m0, %[m0b], 65536\n" \
"v_mfma_f32_16x16x32_bf16 v[128:131], a[104:107], a[8:11], v[128:131]\n" \
"v_exp_f32_e64 v157, v157\n" \
"v_mfma_f32_16x16x32_bf16 v[128:131], a[108:111], a[12:15], v[128:131]\n" \
"v_cvt_pk_bf16_f32 v220, v152, v153\n" \
"v_cvt_pk_bf16_f32 v221, v154, v155\n" \
"v_mfma_f32_16x16x32_bf16 v[132:135], a[112:115], a[0:3], v[160:163]\n" \
"v_exp_f32_e64 v158, v158\n" \
"v_mfma_f32_16x16x32_bf16 v[132:135], a[116:119], a[4:7], v[132:135]\n" \
"s_waitcnt lgkmcnt(0)\n" \
"v_mfma_f32_16x16x32_bf16 v[132:135], a[120:123], a[8:11], v[132:135]\n" \
"buffer_load_dwordx4 %[vd0], %[srdv], s48 offen offset:0 lds\n" \
"v_mfma_f32_16x16x32_bf16 v[132:135], a[124:127], a[12:15], v[132:135]\n" \
"v_exp_f32_e32 v159, v159\n" \
"v_mfma_f32_16x16x32_bf16 v[32:35], a[160:163], v[196:199], v[32:35]\n" \
"ds_read_b128 a[64:67], %[kb0] offset:32768\n" \
"v_mfma_f32_16x16x32_bf16 v[32:35], a[128:131], v[188:191], v[32:35]\n" \
"v_exp_f32_e64 v128, v128\n" \
"v_mfma_f32_16x16x32_bf16 v[36:39], a[164:167], v[196:199], v[36:39]\n" \
"v_cvt_pk_bf16_f32 v222, v156, v157\n" \
"v_cvt_pk_bf16_f32 v223, v158, v159\n" \
"v_mfma_f32_16x16x32_bf16 v[36:39], a[132:135], v[188:191], v[36:39]\n" \
"v_exp_f32_e64 v129, v129\n" \
"v_mfma_f32_16x16x32_bf16 v[40:43], a[168:171], v[196:199], v[40:43]\n" \
"ds_read_b128 a[68:71], v208 offset:32768\n" \
"v_mfma_f32_16x16x32_bf16 v[40:43], a[136:139], v[188:191], v[40:43]\n" \
"v_exp_f32_e64 v130, v130\n" \
"v_mfma_f32_16x16x32_bf16 v[44:47], a[172:175], v[196:199], v[44:47]\n" \
"ds_read_b128 a[72:75], v209 offset:32768\n" \
"v_mfma_f32_16x16x32_bf16 v[44:47], a[140:143], v[188:191], v[44:47]\n" \
"v_exp_f32_e64 v131, v131\n" \
"v_smfmac_f32_16x16x64_bf16 v[176:179], a[192:195], v[184:191], v228\n" \
"v_mfma_f32_16x16x32_bf16 v[136:139], a[96:99], a[16:19], v[164:167]\n" \
"v_exp_f32_e64 v132, v132\n" \
"v_mfma_f32_16x16x32_bf16 v[136:139], a[100:103], a[20:23], v[136:139]\n" \
"ds_read_b128 a[76:79], v210 offset:32768\n" \
"v_mfma_f32_16x16x32_bf16 v[136:139], a[104:107], a[24:27], v[136:139]\n" \
"v_exp_f32_e64 v133, v133\n" \
"v_mfma_f32_16x16x32_bf16 v[136:139], a[108:111], a[28:31], v[136:139]\n" \
"v_cvt_pk_bf16_f32 v192, v128, v129\n" \
"v_cvt_pk_bf16_f32 v193, v130, v131\n" \
"v_mfma_f32_16x16x32_bf16 v[140:143], a[112:115], a[16:19], v[164:167]\n" \
"v_exp_f32_e64 v134, v134\n" \
"v_mfma_f32_16x16x32_bf16 v[140:143], a[116:119], a[20:23], v[140:143]\n" \
"ds_read_b128 a[80:83], %[kb0] offset:36864\n" \
"v_mfma_f32_16x16x32_bf16 v[140:143], a[120:123], a[24:27], v[140:143]\n" \
"buffer_load_dwordx4 v215, %[srdv], s48 offen offset:1024 lds\n" \
"v_mfma_f32_16x16x32_bf16 v[140:143], a[124:127], a[28:31], v[140:143]\n" \
"v_exp_f32_e64 v135, v135\n" \
"v_mfma_f32_16x16x32_bf16 v[48:51], a[176:179], v[196:199], v[48:51]\n" \
"ds_read_b128 a[84:87], v208 offset:36864\n" \
"v_mfma_f32_16x16x32_bf16 v[48:51], a[144:147], v[188:191], v[48:51]\n" \
"v_exp_f32_e64 v136, v136\n" \
"v_mfma_f32_16x16x32_bf16 v[52:55], a[180:183], v[196:199], v[52:55]\n" \
"v_cvt_pk_bf16_f32 v194, v132, v133\n" \
"v_cvt_pk_bf16_f32 v195, v134, v135\n" \
"v_mfma_f32_16x16x32_bf16 v[52:55], a[148:151], v[188:191], v[52:55]\n" \
"v_exp_f32_e64 v137, v137\n" \
"v_mfma_f32_16x16x32_bf16 v[56:59], a[184:187], v[196:199], v[56:59]\n" \
"v_exp_f32_e64 v138, v138\n" \
"v_mfma_f32_16x16x32_bf16 v[56:59], a[152:155], v[188:191], v[56:59]\n" \
"ds_read_b128 a[88:91], v209 offset:36864\n" \
"v_mfma_f32_16x16x32_bf16 v[60:63], a[188:191], v[196:199], v[60:63]\n" \
"v_exp_f32_e64 v139, v139\n" \
"v_mfma_f32_16x16x32_bf16 v[60:63], a[156:159], v[188:191], v[60:63]\n" \
"s_cmp_lg_u32 %[wid], 0\n" \
"s_cbranch_scc1 LtA3_%=\n" \
"global_load_dword v237, v224, s[58:59]\n" \
"s_add_u32 s61, s48, 49152\n" \
"s_mov_b32 exec_hi, 0\n" \
"s_nop 0\n" \
"buffer_load_dword v237, v224, %[srdp], s61 offen\n" \
"s_mov_b32 exec_hi, -1\n" \
"s_nop 0\n" \
"LtA3_%=:\n" \
"v_mfma_f32_16x16x32_bf16 v[144:147], a[96:99], a[32:35], v[168:171]\n" \
"v_exp_f32_e64 v140, v140\n" \
"v_mfma_f32_16x16x32_bf16 v[144:147], a[100:103], a[36:39], v[144:147]\n" \
"ds_read_b128 a[92:95], v210 offset:36864\n" \
"v_mfma_f32_16x16x32_bf16 v[144:147], a[104:107], a[40:43], v[144:147]\n" \
"v_exp_f32_e64 v141, v141\n" \
"v_mfma_f32_16x16x32_bf16 v[144:147], a[108:111], a[44:47], v[144:147]\n" \
"v_cvt_pk_bf16_f32 v196, v136, v137\n" \
"v_cvt_pk_bf16_f32 v197, v138, v139\n" \
"v_mfma_f32_16x16x32_bf16 v[148:151], a[112:115], a[32:35], v[168:171]\n" \
"v_exp_f32_e64 v142, v142\n" \
"v_mfma_f32_16x16x32_bf16 v[148:151], a[116:119], a[36:39], v[148:151]\n" \
"v_mfma_f32_16x16x32_bf16 v[148:151], a[120:123], a[40:43], v[148:151]\n" \
"buffer_load_dwordx4 %[vd0], %[srdv], s48 offen offset:2048 lds\n" \
"v_mfma_f32_16x16x32_bf16 v[148:151], a[124:127], a[44:47], v[148:151]\n" \
"v_exp_f32_e64 v143, v143\n" \
"v_mfma_f32_16x16x32_bf16 v[96:99], a[160:163], v[204:207], v[96:99]\n" \
"ds_read_b64_tr_b16 a[160:161], %[vvb] offset:24576\n" \
"v_mfma_f32_16x16x32_bf16 v[96:99], a[128:131], v[220:223], v[96:99]\n" \
"v_exp_f32_e64 v144, v144\n" \
"v_mfma_f32_16x16x32_bf16 v[100:103], a[164:167], v[204:207], v[100:103]\n" \
"v_cvt_pk_bf16_f32 v198, v140, v141\n" \
"v_cvt_pk_bf16_f32 v199, v142, v143\n" \
"v_mfma_f32_16x16x32_bf16 v[100:103], a[132:135], v[220:223], v[100:103]\n" \
"v_exp_f32_e64 v145, v145\n" \
"v_mfma_f32_16x16x32_bf16 v[104:107], a[168:171], v[204:207], v[104:107]\n" \
"ds_read_b64_tr_b16 a[162:163], %[vvb] offset:28672\n" \
"ds_read_b64_tr_b16 a[164:165], v211 offset:24576\n" \
"v_mfma_f32_16x16x32_bf16 v[104:107], a[136:139], v[220:223], v[104:107]\n" \
"v_exp_f32_e64 v146, v146\n" \
"v_mfma_f32_16x16x32_bf16 v[108:111], a[172:175], v[204:207], v[108:111]\n" \
"ds_read_b64_tr_b16 a[166:167], v211 offset:28672\n" \
"v_mfma_f32_16x16x32_bf16 v[108:111], a[140:143], v[220:223], v[108:111]\n" \
"v_exp_f32_e64 v147, v147\n" \
"v_smfmac_f32_16x16x64_bf16 v[180:183], a[192:195], v[216:223], v228\n" \
"ds_read_b64_tr_b16 a[168:169], %[vvb] offset:25088\n" \
"v_mfma_f32_16x16x32_bf16 v[152:155], a[96:99], a[48:51], v[172:175]\n" \
"v_exp_f32_e64 v148, v148\n" \
"v_mfma_f32_16x16x32_bf16 v[152:155], a[100:103], a[52:55], v[152:155]\n" \
"ds_read_b64_tr_b16 a[170:171], %[vvb] offset:29184\n" \
"v_mfma_f32_16x16x32_bf16 v[152:155], a[104:107], a[56:59], v[152:155]\n" \
"v_exp_f32_e64 v149, v149\n" \
"v_mfma_f32_16x16x32_bf16 v[152:155], a[108:111], a[60:63], v[152:155]\n" \
"v_cvt_pk_bf16_f32 v200, v144, v145\n" \
"v_cvt_pk_bf16_f32 v201, v146, v147\n" \
"v_mfma_f32_16x16x32_bf16 v[156:159], a[112:115], a[48:51], v[172:175]\n" \
"v_exp_f32_e64 v150, v150\n" \
"v_mfma_f32_16x16x32_bf16 v[156:159], a[116:119], a[52:55], v[156:159]\n" \
"ds_read_b64_tr_b16 a[172:173], v211 offset:25088\n" \
"v_mfma_f32_16x16x32_bf16 v[156:159], a[120:123], a[56:59], v[156:159]\n" \
"buffer_load_dwordx4 v215, %[srdv], s48 offen offset:3072 lds\n" \
"v_mfma_f32_16x16x32_bf16 v[156:159], a[124:127], a[60:63], v[156:159]\n" \
"v_exp_f32_e64 v151, v151\n" \
"v_mfma_f32_16x16x32_bf16 v[112:115], a[176:179], v[204:207], v[112:115]\n" \
"ds_read_b64_tr_b16 a[174:175], v211 offset:29184\n" \
"ds_read_b64_tr_b16 a[176:177], %[vvb] offset:25600\n" \
"v_mfma_f32_16x16x32_bf16 v[112:115], a[144:147], v[220:223], v[112:115]\n" \
"v_exp_f32_e64 v152, v152\n" \
"v_mfma_f32_16x16x32_bf16 v[116:119], a[180:183], v[204:207], v[116:119]\n" \
"v_cvt_pk_bf16_f32 v202, v148, v149\n" \
"v_cvt_pk_bf16_f32 v203, v150, v151\n" \
"v_mfma_f32_16x16x32_bf16 v[116:119], a[148:151], v[220:223], v[116:119]\n" \
"v_exp_f32_e64 v153, v153\n" \
"v_mfma_f32_16x16x32_bf16 v[120:123], a[184:187], v[204:207], v[120:123]\n" \
"v_exp_f32_e64 v154, v154\n" \
"v_mfma_f32_16x16x32_bf16 v[120:123], a[152:155], v[220:223], v[120:123]\n" \
"ds_read_b64_tr_b16 a[178:179], %[vvb] offset:29696\n" \
"v_mfma_f32_16x16x32_bf16 v[124:127], a[188:191], v[204:207], v[124:127]\n" \
"v_exp_f32_e64 v155, v155\n" \
"v_mfma_f32_16x16x32_bf16 v[124:127], a[156:159], v[220:223], v[124:127]\n" \
"ds_read_b64_tr_b16 a[180:181], v211 offset:25600\n" \
"s_waitcnt lgkmcnt(13)\n" \
"v_mfma_f32_16x16x32_bf16 v[128:131], a[64:67], a[0:3], v[160:163]\n" \
"v_exp_f32_e32 v156, v156\n" \
"v_mfma_f32_16x16x32_bf16 v[128:131], a[68:71], a[4:7], v[128:131]\n" \
"ds_read_b64_tr_b16 a[182:183], v211 offset:29696\n" \
"s_add_u32 m0, %[m0b], 16384\n" \
"v_mfma_f32_16x16x32_bf16 v[128:131], a[72:75], a[8:11], v[128:131]\n" \
"v_exp_f32_e64 v157, v157\n" \
"v_mfma_f32_16x16x32_bf16 v[128:131], a[76:79], a[12:15], v[128:131]\n" \
"v_cvt_pk_bf16_f32 v204, v152, v153\n" \
"v_cvt_pk_bf16_f32 v205, v154, v155\n" \
"v_mfma_f32_16x16x32_bf16 v[132:135], a[80:83], a[0:3], v[160:163]\n" \
"v_exp_f32_e64 v158, v158\n" \
"v_mfma_f32_16x16x32_bf16 v[132:135], a[84:87], a[4:7], v[132:135]\n" \
"ds_read_b64_tr_b16 a[184:185], %[vvb] offset:26112\n" \
"s_waitcnt lgkmcnt(5)\n" \
"v_mfma_f32_16x16x32_bf16 v[132:135], a[88:91], a[8:11], v[132:135]\n" \
"buffer_load_dwordx4 %[kd0], %[srdk], s48 offen offset:0 lds\n" \
"v_mfma_f32_16x16x32_bf16 v[132:135], a[92:95], a[12:15], v[132:135]\n" \
"v_exp_f32_e32 v159, v159\n" \
"v_mfma_f32_16x16x32_bf16 v[64:67], a[128:131], v[216:219], v[64:67]\n" \
"ds_read_b64_tr_b16 a[186:187], %[vvb] offset:30208\n" \
"ds_read_b64_tr_b16 a[188:189], v211 offset:26112\n" \
"v_mfma_f32_16x16x32_bf16 v[64:67], a[160:163], v[200:203], v[64:67]\n" \
"v_exp_f32_e64 v128, v128\n" \
"v_mfma_f32_16x16x32_bf16 v[68:71], a[132:135], v[216:219], v[68:71]\n" \
"v_cvt_pk_bf16_f32 v206, v156, v157\n" \
"v_cvt_pk_bf16_f32 v207, v158, v159\n" \
"v_mfma_f32_16x16x32_bf16 v[68:71], a[164:167], v[200:203], v[68:71]\n" \
"v_exp_f32_e64 v129, v129\n" \
"v_mfma_f32_16x16x32_bf16 v[72:75], a[136:139], v[216:219], v[72:75]\n" \
"ds_read_b64_tr_b16 a[190:191], v211 offset:30208\n" \
"ds_read_b64_tr_b16 a[128:129], %[vvb] offset:32768\n" \
"v_mfma_f32_16x16x32_bf16 v[72:75], a[168:171], v[200:203], v[72:75]\n" \
"v_exp_f32_e64 v130, v130\n" \
"v_mfma_f32_16x16x32_bf16 v[76:79], a[140:143], v[216:219], v[76:79]\n" \
"ds_read_b64_tr_b16 a[130:131], %[vvb] offset:36864\n" \
"ds_read_b64_tr_b16 a[132:133], v211 offset:32768\n" \
"v_mfma_f32_16x16x32_bf16 v[76:79], a[172:175], v[200:203], v[76:79]\n" \
"v_exp_f32_e64 v131, v131\n" \
"v_smfmac_f32_16x16x64_bf16 v[176:179], a[192:195], v[192:199], v228\n" \
"ds_read_b64_tr_b16 a[134:135], v211 offset:36864\n" \
"ds_read_b64_tr_b16 a[136:137], %[vvb] offset:33280\n" \
"v_mfma_f32_16x16x32_bf16 v[136:139], a[64:67], a[16:19], v[164:167]\n" \
"v_exp_f32_e64 v132, v132\n" \
"v_mfma_f32_16x16x32_bf16 v[136:139], a[68:71], a[20:23], v[136:139]\n" \
"ds_read_b64_tr_b16 a[138:139], %[vvb] offset:37376\n" \
"ds_read_b64_tr_b16 a[140:141], v211 offset:33280\n" \
"v_mfma_f32_16x16x32_bf16 v[136:139], a[72:75], a[24:27], v[136:139]\n" \
"v_exp_f32_e64 v133, v133\n" \
"v_mfma_f32_16x16x32_bf16 v[136:139], a[76:79], a[28:31], v[136:139]\n" \
"v_cvt_pk_bf16_f32 v184, v128, v129\n" \
"v_cvt_pk_bf16_f32 v185, v130, v131\n" \
"v_mfma_f32_16x16x32_bf16 v[140:143], a[80:83], a[16:19], v[164:167]\n" \
"v_exp_f32_e64 v134, v134\n" \
"v_mfma_f32_16x16x32_bf16 v[140:143], a[84:87], a[20:23], v[140:143]\n" \
"ds_read_b64_tr_b16 a[142:143], v211 offset:37376\n" \
"s_waitcnt lgkmcnt(8)\n" \
"v_mfma_f32_16x16x32_bf16 v[140:143], a[88:91], a[24:27], v[140:143]\n" \
"buffer_load_dwordx4 v212, %[srdk], s48 offen offset:1024 lds\n" \
"v_mfma_f32_16x16x32_bf16 v[140:143], a[92:95], a[28:31], v[140:143]\n" \
"v_exp_f32_e32 v135, v135\n" \
"v_mfma_f32_16x16x32_bf16 v[80:83], a[144:147], v[216:219], v[80:83]\n" \
"ds_read_b64_tr_b16 a[144:145], %[vvb] offset:33792\n" \
"ds_read_b64_tr_b16 a[146:147], %[vvb] offset:37888\n" \
"v_mfma_f32_16x16x32_bf16 v[80:83], a[176:179], v[200:203], v[80:83]\n" \
"v_exp_f32_e64 v136, v136\n" \
"v_mfma_f32_16x16x32_bf16 v[84:87], a[148:151], v[216:219], v[84:87]\n" \
"v_cvt_pk_bf16_f32 v186, v132, v133\n" \
"v_cvt_pk_bf16_f32 v187, v134, v135\n" \
"v_mfma_f32_16x16x32_bf16 v[84:87], a[180:183], v[200:203], v[84:87]\n" \
"v_exp_f32_e64 v137, v137\n" \
"v_mfma_f32_16x16x32_bf16 v[88:91], a[152:155], v[216:219], v[88:91]\n" \
"v_exp_f32_e64 v138, v138\n" \
"v_mfma_f32_16x16x32_bf16 v[88:91], a[184:187], v[200:203], v[88:91]\n" \
"ds_read_b64_tr_b16 a[148:149], v211 offset:33792\n" \
"ds_read_b64_tr_b16 a[150:151], v211 offset:37888\n" \
"v_mfma_f32_16x16x32_bf16 v[92:95], a[156:159], v[216:219], v[92:95]\n" \
"v_exp_f32_e64 v139, v139\n" \
"v_mfma_f32_16x16x32_bf16 v[92:95], a[188:191], v[200:203], v[92:95]\n" \
"ds_read_b64_tr_b16 a[152:153], %[vvb] offset:34304\n" \
"buffer_store_dwordx4 a[196:199], v235, %[srdz], s62 offen offset:0 sc1\n" \
"v_mfma_f32_16x16x32_bf16 v[144:147], a[64:67], a[32:35], v[168:171]\n" \
"v_exp_f32_e64 v140, v140\n" \
"v_mfma_f32_16x16x32_bf16 v[144:147], a[68:71], a[36:39], v[144:147]\n" \
"ds_read_b64_tr_b16 a[154:155], %[vvb] offset:38400\n" \
"ds_read_b64_tr_b16 a[156:157], v211 offset:34304\n" \
"v_mfma_f32_16x16x32_bf16 v[144:147], a[72:75], a[40:43], v[144:147]\n" \
"v_exp_f32_e64 v141, v141\n" \
"v_mfma_f32_16x16x32_bf16 v[144:147], a[76:79], a[44:47], v[144:147]\n" \
"v_cvt_pk_bf16_f32 v188, v136, v137\n" \
"v_cvt_pk_bf16_f32 v189, v138, v139\n" \
"v_mfma_f32_16x16x32_bf16 v[148:151], a[80:83], a[32:35], v[168:171]\n" \
"v_exp_f32_e64 v142, v142\n" \
"v_mfma_f32_16x16x32_bf16 v[148:151], a[84:87], a[36:39], v[148:151]\n" \
"ds_read_b64_tr_b16 a[158:159], v211 offset:38400\n" \
"s_waitcnt lgkmcnt(8)\n" \
"v_mfma_f32_16x16x32_bf16 v[148:151], a[88:91], a[40:43], v[148:151]\n" \
"buffer_load_dwordx4 v213, %[srdk], s48 offen offset:2048 lds\n" \
"v_mfma_f32_16x16x32_bf16 v[148:151], a[92:95], a[44:47], v[148:151]\n" \
"v_exp_f32_e32 v143, v143\n" \
"v_mfma_f32_16x16x32_bf16 v[0:3], a[160:163], v[192:195], v[0:3]\n" \
"ds_read_b128 a[96:99], %[kb0] offset:40960\n" \
"v_mfma_f32_16x16x32_bf16 v[0:3], a[128:131], v[184:187], v[0:3]\n" \
"v_exp_f32_e64 v144, v144\n" \
"v_mfma_f32_16x16x32_bf16 v[4:7], a[164:167], v[192:195], v[4:7]\n" \
"v_cvt_pk_bf16_f32 v190, v140, v141\n" \
"v_cvt_pk_bf16_f32 v191, v142, v143\n" \
"v_mfma_f32_16x16x32_bf16 v[4:7], a[132:135], v[184:187], v[4:7]\n" \
"v_exp_f32_e64 v145, v145\n" \
"v_mfma_f32_16x16x32_bf16 v[8:11], a[168:171], v[192:195], v[8:11]\n" \
"ds_read_b128 a[100:103], v208 offset:40960\n" \
"v_mfma_f32_16x16x32_bf16 v[8:11], a[136:139], v[184:187], v[8:11]\n" \
"v_exp_f32_e64 v146, v146\n" \
"v_mfma_f32_16x16x32_bf16 v[12:15], a[172:175], v[192:195], v[12:15]\n" \
"ds_read_b128 a[104:107], v209 offset:40960\n" \
"v_mfma_f32_16x16x32_bf16 v[12:15], a[140:143], v[184:187], v[12:15]\n" \
"v_exp_f32_e64 v147, v147\n" \
"v_smfmac_f32_16x16x64_bf16 v[180:183], a[192:195], v[200:207], v228\n" \
"ds_read_b128 a[108:111], v210 offset:40960\n" \
"v_mfma_f32_16x16x32_bf16 v[152:155], a[64:67], a[48:51], v[172:175]\n" \
"v_exp_f32_e64 v148, v148\n" \
"v_mfma_f32_16x16x32_bf16 v[152:155], a[68:71], a[52:55], v[152:155]\n" \
"ds_read_b128 a[112:115], %[kb0] offset:45056\n" \
"v_mfma_f32_16x16x32_bf16 v[152:155], a[72:75], a[56:59], v[152:155]\n" \
"v_exp_f32_e64 v149, v149\n" \
"v_mfma_f32_16x16x32_bf16 v[152:155], a[76:79], a[60:63], v[152:155]\n" \
"v_cvt_pk_bf16_f32 v216, v144, v145\n" \
"v_cvt_pk_bf16_f32 v217, v146, v147\n" \
"v_mfma_f32_16x16x32_bf16 v[156:159], a[80:83], a[48:51], v[172:175]\n" \
"v_exp_f32_e64 v150, v150\n" \
"v_mfma_f32_16x16x32_bf16 v[156:159], a[84:87], a[52:55], v[156:159]\n" \
"ds_read_b128 a[116:119], v208 offset:45056\n" \
"s_waitcnt lgkmcnt(6)\n" \
"v_mfma_f32_16x16x32_bf16 v[156:159], a[88:91], a[56:59], v[156:159]\n" \
"buffer_load_dwordx4 v214, %[srdk], s48 offen offset:3072 lds\n" \
"v_mfma_f32_16x16x32_bf16 v[156:159], a[92:95], a[60:63], v[156:159]\n" \
"v_exp_f32_e32 v151, v151\n" \
"v_mfma_f32_16x16x32_bf16 v[16:19], a[176:179], v[192:195], v[16:19]\n" \
"ds_read_b128 a[120:123], v209 offset:45056\n" \
"v_mfma_f32_16x16x32_bf16 v[16:19], a[144:147], v[184:187], v[16:19]\n" \
"v_exp_f32_e64 v152, v152\n" \
"v_mfma_f32_16x16x32_bf16 v[20:23], a[180:183], v[192:195], v[20:23]\n" \
"v_cvt_pk_bf16_f32 v218, v148, v149\n" \
"v_cvt_pk_bf16_f32 v219, v150, v151\n" \
"v_mfma_f32_16x16x32_bf16 v[20:23], a[148:151], v[184:187], v[20:23]\n" \
"v_exp_f32_e64 v153, v153\n" \
"v_mfma_f32_16x16x32_bf16 v[24:27], a[184:187], v[192:195], v[24:27]\n" \
"v_exp_f32_e64 v154, v154\n" \
"v_mfma_f32_16x16x32_bf16 v[24:27], a[152:155], v[184:187], v[24:27]\n" \
"ds_read_b128 a[124:127], v210 offset:45056\n" \
"s_add_u32 s48, s48, 0x4000\n" \
"v_mfma_f32_16x16x32_bf16 v[28:31], a[188:191], v[192:195], v[28:31]\n" \
"v_exp_f32_e64 v155, v155\n" \
"v_mfma_f32_16x16x32_bf16 v[28:31], a[156:159], v[184:187], v[28:31]\n" \
"s_waitcnt vmcnt(14) lgkmcnt(2)\n" \
"s_barrier\n" \
"v_mfma_f32_16x16x32_bf16 v[128:131], a[96:99], a[0:3], v[160:163]\n" \
"v_exp_f32_e64 v156, v156\n" \
"v_mfma_f32_16x16x32_bf16 v[128:131], a[100:103], a[4:7], v[128:131]\n" \
"s_add_u32 m0, %[m0b], 81920\n" \
"v_mfma_f32_16x16x32_bf16 v[128:131], a[104:107], a[8:11], v[128:131]\n" \
"v_exp_f32_e64 v157, v157\n" \
"v_mfma_f32_16x16x32_bf16 v[128:131], a[108:111], a[12:15], v[128:131]\n" \
"v_cvt_pk_bf16_f32 v220, v152, v153\n" \
"v_cvt_pk_bf16_f32 v221, v154, v155\n" \
"v_mfma_f32_16x16x32_bf16 v[132:135], a[112:115], a[0:3], v[160:163]\n" \
"v_exp_f32_e64 v158, v158\n" \
"v_mfma_f32_16x16x32_bf16 v[132:135], a[116:119], a[4:7], v[132:135]\n" \
"s_waitcnt lgkmcnt(0)\n" \
"v_mfma_f32_16x16x32_bf16 v[132:135], a[120:123], a[8:11], v[132:135]\n" \
"buffer_load_dwordx4 %[vd0], %[srdv], s48 offen offset:0 lds\n" \
"v_mfma_f32_16x16x32_bf16 v[132:135], a[124:127], a[12:15], v[132:135]\n" \
"v_exp_f32_e32 v159, v159\n" \
"v_mfma_f32_16x16x32_bf16 v[32:35], a[160:163], v[196:199], v[32:35]\n" \
"ds_read_b128 a[64:67], %[kb0] offset:49152\n" \
"v_mfma_f32_16x16x32_bf16 v[32:35], a[128:131], v[188:191], v[32:35]\n" \
"v_exp_f32_e64 v128, v128\n" \
"v_mfma_f32_16x16x32_bf16 v[36:39], a[164:167], v[196:199], v[36:39]\n" \
"v_cvt_pk_bf16_f32 v222, v156, v157\n" \
"v_cvt_pk_bf16_f32 v223, v158, v159\n" \
"v_mfma_f32_16x16x32_bf16 v[36:39], a[132:135], v[188:191], v[36:39]\n" \
"v_exp_f32_e64 v129, v129\n" \
"v_mfma_f32_16x16x32_bf16 v[40:43], a[168:171], v[196:199], v[40:43]\n" \
"ds_read_b128 a[68:71], v208 offset:49152\n" \
"v_mfma_f32_16x16x32_bf16 v[40:43], a[136:139], v[188:191], v[40:43]\n" \
"v_exp_f32_e64 v130, v130\n" \
"v_mfma_f32_16x16x32_bf16 v[44:47], a[172:175], v[196:199], v[44:47]\n" \
"ds_read_b128 a[72:75], v209 offset:49152\n" \
"v_mfma_f32_16x16x32_bf16 v[44:47], a[140:143], v[188:191], v[44:47]\n" \
"v_exp_f32_e64 v131, v131\n" \
"v_smfmac_f32_16x16x64_bf16 v[176:179], a[192:195], v[184:191], v228\n" \
"v_mfma_f32_16x16x32_bf16 v[136:139], a[96:99], a[16:19], v[164:167]\n" \
"v_exp_f32_e64 v132, v132\n" \
"v_mfma_f32_16x16x32_bf16 v[136:139], a[100:103], a[20:23], v[136:139]\n" \
"ds_read_b128 a[76:79], v210 offset:49152\n" \
"v_mfma_f32_16x16x32_bf16 v[136:139], a[104:107], a[24:27], v[136:139]\n" \
"v_exp_f32_e64 v133, v133\n" \
"v_mfma_f32_16x16x32_bf16 v[136:139], a[108:111], a[28:31], v[136:139]\n" \
"v_cvt_pk_bf16_f32 v192, v128, v129\n" \
"v_cvt_pk_bf16_f32 v193, v130, v131\n" \
"v_mfma_f32_16x16x32_bf16 v[140:143], a[112:115], a[16:19], v[164:167]\n" \
"v_exp_f32_e64 v134, v134\n" \
"v_mfma_f32_16x16x32_bf16 v[140:143], a[116:119], a[20:23], v[140:143]\n" \
"ds_read_b128 a[80:83], %[kb0] offset:53248\n" \
"v_mfma_f32_16x16x32_bf16 v[140:143], a[120:123], a[24:27], v[140:143]\n" \
"buffer_load_dwordx4 v215, %[srdv], s48 offen offset:1024 lds\n" \
"v_mfma_f32_16x16x32_bf16 v[140:143], a[124:127], a[28:31], v[140:143]\n" \
"v_exp_f32_e64 v135, v135\n" \
"v_mfma_f32_16x16x32_bf16 v[48:51], a[176:179], v[196:199], v[48:51]\n" \
"ds_read_b128 a[84:87], v208 offset:53248\n" \
"v_mfma_f32_16x16x32_bf16 v[48:51], a[144:147], v[188:191], v[48:51]\n" \
"v_exp_f32_e64 v136, v136\n" \
"v_mfma_f32_16x16x32_bf16 v[52:55], a[180:183], v[196:199], v[52:55]\n" \
"v_cvt_pk_bf16_f32 v194, v132, v133\n" \
"v_cvt_pk_bf16_f32 v195, v134, v135\n" \
"v_mfma_f32_16x16x32_bf16 v[52:55], a[148:151], v[188:191], v[52:55]\n" \
"v_exp_f32_e64 v137, v137\n" \
"v_mfma_f32_16x16x32_bf16 v[56:59], a[184:187], v[196:199], v[56:59]\n" \
"v_exp_f32_e64 v138, v138\n" \
"v_mfma_f32_16x16x32_bf16 v[56:59], a[152:155], v[188:191], v[56:59]\n" \
"ds_read_b128 a[88:91], v209 offset:53248\n" \
"v_mfma_f32_16x16x32_bf16 v[60:63], a[188:191], v[196:199], v[60:63]\n" \
"v_exp_f32_e64 v139, v139\n" \
"v_mfma_f32_16x16x32_bf16 v[60:63], a[156:159], v[188:191], v[60:63]\n" \
"s_cmp_lg_u32 %[wid], 1\n" \
"s_cbranch_scc1 LtA5_%=\n" \
"global_load_dword v237, v224, s[58:59]\n" \
"s_add_u32 s61, s48, 49152\n" \
"s_mov_b32 exec_hi, 0\n" \
"s_nop 0\n" \
"buffer_load_dword v237, v224, %[srdp], s61 offen\n" \
"s_mov_b32 exec_hi, -1\n" \
"s_nop 0\n" \
"LtA5_%=:\n" \
"v_mfma_f32_16x16x32_bf16 v[144:147], a[96:99], a[32:35], v[168:171]\n" \
"v_exp_f32_e64 v140, v140\n" \
"v_mfma_f32_16x16x32_bf16 v[144:147], a[100:103], a[36:39], v[144:147]\n" \
"ds_read_b128 a[92:95], v210 offset:53248\n" \
"v_mfma_f32_16x16x32_bf16 v[144:147], a[104:107], a[40:43], v[144:147]\n" \
"v_exp_f32_e64 v141, v141\n" \
"v_mfma_f32_16x16x32_bf16 v[144:147], a[108:111], a[44:47], v[144:147]\n" \
"v_cvt_pk_bf16_f32 v196, v136, v137\n" \
"v_cvt_pk_bf16_f32 v197, v138, v139\n" \
"v_mfma_f32_16x16x32_bf16 v[148:151], a[112:115], a[32:35], v[168:171]\n" \
"v_exp_f32_e64 v142, v142\n" \
"v_mfma_f32_16x16x32_bf16 v[148:151], a[116:119], a[36:39], v[148:151]\n" \
"v_mfma_f32_16x16x32_bf16 v[148:151], a[120:123], a[40:43], v[148:151]\n" \
"buffer_load_dwordx4 %[vd0], %[srdv], s48 offen offset:2048 lds\n" \
"v_mfma_f32_16x16x32_bf16 v[148:151], a[124:127], a[44:47], v[148:151]\n" \
"v_exp_f32_e64 v143, v143\n" \
"v_mfma_f32_16x16x32_bf16 v[96:99], a[160:163], v[204:207], v[96:99]\n" \
"ds_read_b64_tr_b16 a[160:161], %[vvb] offset:40960\n" \
"v_mfma_f32_16x16x32_bf16 v[96:99], a[128:131], v[220:223], v[96:99]\n" \
"v_exp_f32_e64 v144, v144\n" \
"v_mfma_f32_16x16x32_bf16 v[100:103], a[164:167], v[204:207], v[100:103]\n" \
"v_cvt_pk_bf16_f32 v198, v140, v141\n" \
"v_cvt_pk_bf16_f32 v199, v142, v143\n" \
"v_mfma_f32_16x16x32_bf16 v[100:103], a[132:135], v[220:223], v[100:103]\n" \
"v_exp_f32_e64 v145, v145\n" \
"v_mfma_f32_16x16x32_bf16 v[104:107], a[168:171], v[204:207], v[104:107]\n" \
"ds_read_b64_tr_b16 a[162:163], %[vvb] offset:45056\n" \
"ds_read_b64_tr_b16 a[164:165], v211 offset:40960\n" \
"v_mfma_f32_16x16x32_bf16 v[104:107], a[136:139], v[220:223], v[104:107]\n" \
"v_exp_f32_e64 v146, v146\n" \
"v_mfma_f32_16x16x32_bf16 v[108:111], a[172:175], v[204:207], v[108:111]\n" \
"ds_read_b64_tr_b16 a[166:167], v211 offset:45056\n" \
"v_mfma_f32_16x16x32_bf16 v[108:111], a[140:143], v[220:223], v[108:111]\n" \
"v_exp_f32_e64 v147, v147\n" \
"v_smfmac_f32_16x16x64_bf16 v[180:183], a[192:195], v[216:223], v228\n" \
"ds_read_b64_tr_b16 a[168:169], %[vvb] offset:41472\n" \
"v_mfma_f32_16x16x32_bf16 v[152:155], a[96:99], a[48:51], v[172:175]\n" \
"v_exp_f32_e64 v148, v148\n" \
"v_mfma_f32_16x16x32_bf16 v[152:155], a[100:103], a[52:55], v[152:155]\n" \
"ds_read_b64_tr_b16 a[170:171], %[vvb] offset:45568\n" \
"v_mfma_f32_16x16x32_bf16 v[152:155], a[104:107], a[56:59], v[152:155]\n" \
"v_exp_f32_e64 v149, v149\n" \
"v_mfma_f32_16x16x32_bf16 v[152:155], a[108:111], a[60:63], v[152:155]\n" \
"v_cvt_pk_bf16_f32 v200, v144, v145\n" \
"v_cvt_pk_bf16_f32 v201, v146, v147\n" \
"v_mfma_f32_16x16x32_bf16 v[156:159], a[112:115], a[48:51], v[172:175]\n" \
"v_exp_f32_e64 v150, v150\n" \
"v_mfma_f32_16x16x32_bf16 v[156:159], a[116:119], a[52:55], v[156:159]\n" \
"ds_read_b64_tr_b16 a[172:173], v211 offset:41472\n" \
"v_mfma_f32_16x16x32_bf16 v[156:159], a[120:123], a[56:59], v[156:159]\n" \
"buffer_load_dwordx4 v215, %[srdv], s48 offen offset:3072 lds\n" \
"v_mfma_f32_16x16x32_bf16 v[156:159], a[124:127], a[60:63], v[156:159]\n" \
"v_exp_f32_e64 v151, v151\n" \
"v_mfma_f32_16x16x32_bf16 v[112:115], a[176:179], v[204:207], v[112:115]\n" \
"ds_read_b64_tr_b16 a[174:175], v211 offset:45568\n" \
"ds_read_b64_tr_b16 a[176:177], %[vvb] offset:41984\n" \
"v_mfma_f32_16x16x32_bf16 v[112:115], a[144:147], v[220:223], v[112:115]\n" \
"v_exp_f32_e64 v152, v152\n" \
"v_mfma_f32_16x16x32_bf16 v[116:119], a[180:183], v[204:207], v[116:119]\n" \
"v_cvt_pk_bf16_f32 v202, v148, v149\n" \
".if . - adaln_body != 16384\n" \
".error \"size model\"\n" \
".endif\n" \
"Lk3_%=:\n" \
"s_cbranch_execz Lk4_%=\n" \
"s_nop 0\n" \
"v_cvt_pk_bf16_f32 v203, v150, v151\n" \
"v_mfma_f32_16x16x32_bf16 v[116:119], a[148:151], v[220:223], v[116:119]\n" \
"v_exp_f32_e64 v153, v153\n" \
"v_mfma_f32_16x16x32_bf16 v[120:123], a[184:187], v[204:207], v[120:123]\n" \
"v_exp_f32_e64 v154, v154\n" \
"v_mfma_f32_16x16x32_bf16 v[120:123], a[152:155], v[220:223], v[120:123]\n" \
"ds_read_b64_tr_b16 a[178:179], %[vvb] offset:46080\n" \
"v_mfma_f32_16x16x32_bf16 v[124:127], a[188:191], v[204:207], v[124:127]\n" \
"v_exp_f32_e64 v155, v155\n" \
"v_mfma_f32_16x16x32_bf16 v[124:127], a[156:159], v[220:223], v[124:127]\n" \
"ds_read_b64_tr_b16 a[180:181], v211 offset:41984\n" \
"s_waitcnt lgkmcnt(13)\n" \
"v_mfma_f32_16x16x32_bf16 v[128:131], a[64:67], a[0:3], v[160:163]\n" \
"v_exp_f32_e32 v156, v156\n" \
"v_mfma_f32_16x16x32_bf16 v[128:131], a[68:71], a[4:7], v[128:131]\n" \
"ds_read_b64_tr_b16 a[182:183], v211 offset:46080\n" \
"s_add_u32 m0, %[m0b], 32768\n" \
"v_mfma_f32_16x16x32_bf16 v[128:131], a[72:75], a[8:11], v[128:131]\n" \
"v_exp_f32_e64 v157, v157\n" \
"v_mfma_f32_16x16x32_bf16 v[128:131], a[76:79], a[12:15], v[128:131]\n" \
"v_cvt_pk_bf16_f32 v204, v152, v153\n" \
"v_cvt_pk_bf16_f32 v205, v154, v155\n" \
"v_mfma_f32_16x16x32_bf16 v[132:135], a[80:83], a[0:3], v[160:163]\n" \
"v_exp_f32_e64 v158, v158\n" \
"v_mfma_f32_16x16x32_bf16 v[132:135], a[84:87], a[4:7], v[132:135]\n" \
"ds_read_b64_tr_b16 a[184:185], %[vvb] offset:42496\n" \
"s_waitcnt lgkmcnt(5)\n" \
"v_mfma_f32_16x16x32_bf16 v[132:135], a[88:91], a[8:11], v[132:135]\n" \
"buffer_load_dwordx4 %[kd0], %[srdk], s48 offen offset:0 lds\n" \
"v_mfma_f32_16x16x32_bf16 v[132:135], a[92:95], a[12:15], v[132:135]\n" \
"v_exp_f32_e32 v159, v159\n" \
"v_mfma_f32_16x16x32_bf16 v[64:67], a[128:131], v[216:219], v[64:67]\n" \
"ds_read_b64_tr_b16 a[186:187], %[vvb] offset:46592\n" \
"ds_read_b64_tr_b16 a[188:189], v211 offset:42496\n" \
"v_mfma_f32_16x16x32_bf16 v[64:67], a[160:163], v[200:203], v[64:67]\n" \
"v_exp_f32_e64 v128, v128\n" \
"v_mfma_f32_16x16x32_bf16 v[68:71], a[132:135], v[216:219], v[68:71]\n" \
"v_cvt_pk_bf16_f32 v206, v156, v157\n" \
"v_cvt_pk_bf16_f32 v207, v158, v159\n" \
"v_mfma_f32_16x16x32_bf16 v[68:71], a[164:167], v[200:203], v[68:71]\n" \
"v_exp_f32_e64 v129, v129\n" \
"v_mfma_f32_16x16x32_bf16 v[72:75], a[136:139], v[216:219], v[72:75]\n" \
"ds_read_b64_tr_b16 a[190:191], v211 offset:46592\n" \
"ds_read_b64_tr_b16 a[128:129], %[vvb] offset:49152\n" \
"v_mfma_f32_16x16x32_bf16 v[72:75], a[168:171], v[200:203], v[72:75]\n" \
"v_exp_f32_e64 v130, v130\n" \
"v_mfma_f32_16x16x32_bf16 v[76:79], a[140:143], v[216:219], v[76:79]\n" \
"ds_read_b64_tr_b16 a[130:131], %[vvb] offset:53248\n" \
"ds_read_b64_tr_b16 a[132:133], v211 offset:49152\n" \
"v_mfma_f32_16x16x32_bf16 v[76:79], a[172:175], v[200:203], v[76:79]\n" \
"v_exp_f32_e64 v131, v131\n" \
"v_smfmac_f32_16x16x64_bf16 v[176:179], a[192:195], v[192:199], v228\n" \
"ds_read_b64_tr_b16 a[134:135], v211 offset:53248\n" \
"ds_read_b64_tr_b16 a[136:137], %[vvb] offset:49664\n" \
"v_mfma_f32_16x16x32_bf16 v[136:139], a[64:67], a[16:19], v[164:167]\n" \
"v_exp_f32_e64 v132, v132\n" \
"v_mfma_f32_16x16x32_bf16 v[136:139], a[68:71], a[20:23], v[136:139]\n" \
"ds_read_b64_tr_b16 a[138:139], %[vvb] offset:53760\n" \
"ds_read_b64_tr_b16 a[140:141], v211 offset:49664\n" \
"v_mfma_f32_16x16x32_bf16 v[136:139], a[72:75], a[24:27], v[136:139]\n" \
"v_exp_f32_e64 v133, v133\n" \
"v_mfma_f32_16x16x32_bf16 v[136:139], a[76:79], a[28:31], v[136:139]\n" \
"v_cvt_pk_bf16_f32 v184, v128, v129\n" \
"v_cvt_pk_bf16_f32 v185, v130, v131\n" \
"v_mfma_f32_16x16x32_bf16 v[140:143], a[80:83], a[16:19], v[164:167]\n" \
"v_exp_f32_e64 v134, v134\n" \
"v_mfma_f32_16x16x32_bf16 v[140:143], a[84:87], a[20:23], v[140:143]\n" \
"ds_read_b64_tr_b16 a[142:143], v211 offset:53760\n" \
"s_waitcnt lgkmcnt(8)\n" \
"v_mfma_f32_16x16x32_bf16 v[140:143], a[88:91], a[24:27], v[140:143]\n" \
"buffer_load_dwordx4 v212, %[srdk], s48 offen offset:1024 lds\n" \
"v_mfma_f32_16x16x32_bf16 v[140:143], a[92:95], a[28:31], v[140:143]\n" \
"v_exp_f32_e32 v135, v135\n" \
"v_mfma_f32_16x16x32_bf16 v[80:83], a[144:147], v[216:219], v[80:83]\n" \
"ds_read_b64_tr_b16 a[144:145], %[vvb] offset:50176\n" \
"ds_read_b64_tr_b16 a[146:147], %[vvb] offset:54272\n" \
"v_mfma_f32_16x16x32_bf16 v[80:83], a[176:179], v[200:203], v[80:83]\n" \
"v_exp_f32_e64 v136, v136\n" \
"v_mfma_f32_16x16x32_bf16 v[84:87], a[148:151], v[216:219], v[84:87]\n" \
"v_cvt_pk_bf16_f32 v186, v132, v133\n" \
"v_cvt_pk_bf16_f32 v187, v134, v135\n" \
"v_mfma_f32_16x16x32_bf16 v[84:87], a[180:183], v[200:203], v[84:87]\n" \
"v_exp_f32_e64 v137, v137\n" \
"v_mfma_f32_16x16x32_bf16 v[88:91], a[152:155], v[216:219], v[88:91]\n" \
"v_exp_f32_e64 v138, v138\n" \
"v_mfma_f32_16x16x32_bf16 v[88:91], a[184:187], v[200:203], v[88:91]\n" \
"ds_read_b64_tr_b16 a[148:149], v211 offset:50176\n" \
"ds_read_b64_tr_b16 a[150:151], v211 offset:54272\n" \
"v_mfma_f32_16x16x32_bf16 v[92:95], a[156:159], v[216:219], v[92:95]\n" \
"v_exp_f32_e64 v139, v139\n" \
"v_mfma_f32_16x16x32_bf16 v[92:95], a[188:191], v[200:203], v[92:95]\n" \
"ds_read_b64_tr_b16 a[152:153], %[vvb] offset:50688\n" \
"global_load_dword v237, %[vct], %[pct]\n" \
"v_mfma_f32_16x16x32_bf16 v[144:147], a[64:67], a[32:35], v[168:171]\n" \
"v_exp_f32_e64 v140, v140\n" \
"v_mfma_f32_16x16x32_bf16 v[144:147], a[68:71], a[36:39], v[144:147]\n" \
"ds_read_b64_tr_b16 a[154:155], %[vvb] offset:54784\n" \
"ds_read_b64_tr_b16 a[156:157], v211 offset:50688\n" \
"v_mfma_f32_16x16x32_bf16 v[144:147], a[72:75], a[40:43], v[144:147]\n" \
"v_exp_f32_e64 v141, v141\n" \
"v_mfma_f32_16x16x32_bf16 v[144:147], a[76:79], a[44:47], v[144:147]\n" \
"v_cvt_pk_bf16_f32 v188, v136, v137\n" \
"v_cvt_pk_bf16_f32 v189, v138, v139\n" \
"v_mfma_f32_16x16x32_bf16 v[148:151], a[80:83], a[32:35], v[168:171]\n" \
"v_exp_f32_e64 v142, v142\n" \
"v_mfma_f32_16x16x32_bf16 v[148:151], a[84:87], a[36:39], v[148:151]\n" \
"ds_read_b64_tr_b16 a[158:159], v211 offset:54784\n" \
"s_waitcnt lgkmcnt(8)\n" \
"v_mfma_f32_16x16x32_bf16 v[148:151], a[88:91], a[40:43], v[148:151]\n" \
"buffer_load_dwordx4 v213, %[srdk], s48 offen offset:2048 lds\n" \
"v_mfma_f32_16x16x32_bf16 v[148:151], a[92:95], a[44:47], v[148:151]\n" \
"v_exp_f32_e32 v143, v143\n" \
"v_mfma_f32_16x16x32_bf16 v[0:3], a[160:163], v[192:195], v[0:3]\n" \
"ds_read_b128 a[96:99], %[kb0] offset:57344\n" \
"v_mfma_f32_16x16x32_bf16 v[0:3], a[128:131], v[184:187], v[0:3]\n" \
"v_exp_f32_e64 v144, v144\n" \
"v_mfma_f32_16x16x32_bf16 v[4:7], a[164:167], v[192:195], v[4:7]\n" \
"v_cvt_pk_bf16_f32 v190, v140, v141\n" \
"v_cvt_pk_bf16_f32 v191, v142, v143\n" \
"v_mfma_f32_16x16x32_bf16 v[4:7], a[132:135], v[184:187], v[4:7]\n" \
"v_exp_f32_e64 v145, v145\n" \
"v_mfma_f32_16x16x32_bf16 v[8:11], a[168:171], v[192:195], v[8:11]\n" \
"ds_read_b128 a[100:103], v208 offset:57344\n" \
"v_mfma_f32_16x16x32_bf16 v[8:11], a[136:139], v[184:187], v[8:11]\n" \
"v_exp_f32_e64 v146, v146\n" \
"v_mfma_f32_16x16x32_bf16 v[12:15], a[172:175], v[192:195], v[12:15]\n" \
"ds_read_b128 a[104:107], v209 offset:57344\n" \
"v_mfma_f32_16x16x32_bf16 v[12:15], a[140:143], v[184:187], v[12:15]\n" \
"v_exp_f32_e64 v147, v147\n" \
"v_smfmac_f32_16x16x64_bf16 v[180:183], a[192:195], v[200:207], v228\n" \
"ds_read_b128 a[108:111], v210 offset:57344\n" \
"v_mfma_f32_16x16x32_bf16 v[152:155], a[64:67], a[48:51], v[172:175]\n" \
"v_exp_f32_e64 v148, v148\n" \
"v_mfma_f32_16x16x32_bf16 v[152:155], a[68:71], a[52:55], v[152:155]\n" \
"ds_read_b128 a[112:115], %[kb0] offset:61440\n" \
"v_mfma_f32_16x16x32_bf16 v[152:155], a[72:75], a[56:59], v[152:155]\n" \
"v_exp_f32_e64 v149, v149\n" \
"v_mfma_f32_16x16x32_bf16 v[152:155], a[76:79], a[60:63], v[152:155]\n" \
"v_cvt_pk_bf16_f32 v216, v144, v145\n" \
"v_cvt_pk_bf16_f32 v217, v146, v147\n" \
"v_mfma_f32_16x16x32_bf16 v[156:159], a[80:83], a[48:51], v[172:175]\n" \
"v_exp_f32_e64 v150, v150\n" \
"v_mfma_f32_16x16x32_bf16 v[156:159], a[84:87], a[52:55], v[156:159]\n" \
"ds_read_b128 a[116:119], v208 offset:61440\n" \
"s_waitcnt lgkmcnt(6)\n" \
"v_mfma_f32_16x16x32_bf16 v[156:159], a[88:91], a[56:59], v[156:159]\n" \
"buffer_load_dwordx4 v214, %[srdk], s48 offen offset:3072 lds\n" \
"v_mfma_f32_16x16x32_bf16 v[156:159], a[92:95], a[60:63], v[156:159]\n" \
"v_exp_f32_e32 v151, v151\n" \
"v_mfma_f32_16x16x32_bf16 v[16:19], a[176:179], v[192:195], v[16:19]\n" \
"ds_read_b128 a[120:123], v209 offset:61440\n" \
"v_mfma_f32_16x16x32_bf16 v[16:19], a[144:147], v[184:187], v[16:19]\n" \
"v_exp_f32_e64 v152, v152\n" \
"v_mfma_f32_16x16x32_bf16 v[20:23], a[180:183], v[192:195], v[20:23]\n" \
"v_cvt_pk_bf16_f32 v218, v148, v149\n" \
"v_cvt_pk_bf16_f32 v219, v150, v151\n" \
"v_mfma_f32_16x16x32_bf16 v[20:23], a[148:151], v[184:187], v[20:23]\n" \
"v_exp_f32_e64 v153, v153\n" \
"v_mfma_f32_16x16x32_bf16 v[24:27], a[184:187], v[192:195], v[24:27]\n" \
"v_exp_f32_e64 v154, v154\n" \
"v_mfma_f32_16x16x32_bf16 v[24:27], a[152:155], v[184:187], v[24:27]\n" \
"ds_read_b128 a[124:127], v210 offset:61440\n" \
"s_add_u32 s48, s48, 0x4000\n" \
"s_mov_b32 m0, s44\n" \
"s_addk_i32 s44, 0x400\n" \
"buffer_load_dwordx4 v229, s[64:67], s45 offen offset:0 nt lds\n" \
"s_add_u32 s45, s45, 393216\n" \
"v_mfma_f32_16x16x32_bf16 v[28:31], a[188:191], v[192:195], v[28:31]\n" \
"v_exp_f32_e64 v155, v155\n" \
"v_mfma_f32_16x16x32_bf16 v[28:31], a[156:159], v[184:187], v[28:31]\n" \
"s_waitcnt vmcnt(15) lgkmcnt(2)\n" \
"s_barrier\n" \
"v_mfma_f32_16x16x32_bf16 v[128:131], a[96:99], a[0:3], v[160:163]\n" \
"v_exp_f32_e64 v156, v156\n" \
"v_mfma_f32_16x16x32_bf16 v[128:131], a[100:103], a[4:7], v[128:131]\n" \
"s_add_u32 m0, %[m0b], 98304\n" \
"v_mfma_f32_16x16x32_bf16 v[128:131], a[104:107], a[8:11], v[128:131]\n" \
"v_exp_f32_e64 v157, v157\n" \
"v_mfma_f32_16x16x32_bf16 v[128:131], a[108:111], a[12:15], v[128:131]\n" \
"v_cvt_pk_bf16_f32 v220, v152, v153\n" \
"v_cvt_pk_bf16_f32 v221, v154, v155\n" \
"v_mfma_f32_16x16x32_bf16 v[132:135], a[112:115], a[0:3], v[160:163]\n" \
"v_exp_f32_e64 v158, v158\n" \
"v_mfma_f32_16x16x32_bf16 v[132:135], a[116:119], a[4:7], v[132:135]\n" \
"s_waitcnt lgkmcnt(0)\n" \
"v_mfma_f32_16x16x32_bf16 v[132:135], a[120:123], a[8:11], v[132:135]\n" \
"buffer_load_dwordx4 %[vd0], %[srdv], s48 offen offset:0 lds\n" \
"v_mfma_f32_16x16x32_bf16 v[132:135], a[124:127], a[12:15], v[132:135]\n" \
"v_exp_f32_e32 v159, v159\n" \
"v_mfma_f32_16x16x32_bf16 v[32:35], a[160:163], v[196:199], v[32:35]\n" \
"ds_read_b128 a[64:67], %[kb0] offset:0\n" \
"v_mfma_f32_16x16x32_bf16 v[32:35], a[128:131], v[188:191], v[32:35]\n" \
"v_exp_f32_e64 v128, v128\n" \
"v_mfma_f32_16x16x32_bf16 v[36:39], a[164:167], v[196:199], v[36:39]\n" \
"v_cvt_pk_bf16_f32 v222, v156, v157\n" \
"v_cvt_pk_bf16_f32 v223, v158, v159\n" \
"v_mfma_f32_16x16x32_bf16 v[36:39], a[132:135], v[188:191], v[36:39]\n" \
"v_exp_f32_e64 v129, v129\n" \
"v_mfma_f32_16x16x32_bf16 v[40:43], a[168:171], v[196:199], v[40:43]\n" \
"ds_read_b128 a[68:71], v208 offset:0\n" \
"v_mfma_f32_16x16x32_bf16 v[40:43], a[136:139], v[188:191], v[40:43]\n" \
"v_exp_f32_e64 v130, v130\n" \
"v_mfma_f32_16x16x32_bf16 v[44:47], a[172:175], v[196:199], v[44:47]\n" \
"ds_read_b128 a[72:75], v209 offset:0\n" \
"v_mfma_f32_16x16x32_bf16 v[44:47], a[140:143], v[188:191], v[44:47]\n" \
"v_exp_f32_e64 v131, v131\n" \
"v_smfmac_f32_16x16x64_bf16 v[176:179], a[192:195], v[184:191], v228\n" \
"v_mfma_f32_16x16x32_bf16 v[136:139], a[96:99], a[16:19], v[164:167]\n" \
"v_exp_f32_e64 v132, v132\n" \
"v_mfma_f32_16x16x32_bf16 v[136:139], a[100:103], a[20:23], v[136:139]\n" \
"ds_read_b128 a[76:79], v210 offset:0\n" \
"v_mfma_f32_16x16x32_bf16 v[136:139], a[104:107], a[24:27], v[136:139]\n" \
"v_exp_f32_e64 v133, v133\n" \
"v_mfma_f32_16x16x32_bf16 v[136:139], a[108:111], a[28:31], v[136:139]\n" \
"v_cvt_pk_bf16_f32 v192, v128, v129\n" \
"v_cvt_pk_bf16_f32 v193, v130, v131\n" \
"v_mfma_f32_16x16x32_bf16 v[140:143], a[112:115], a[16:19], v[164:167]\n" \
"v_exp_f32_e64 v134, v134\n" \
"v_mfma_f32_16x16x32_bf16 v[140:143], a[116:119], a[20:23], v[140:143]\n" \
"ds_read_b128 a[80:83], %[kb0] offset:4096\n" \
"v_mfma_f32_16x16x32_bf16 v[140:143], a[120:123], a[24:27], v[140:143]\n" \
"buffer_load_dwordx4 v215, %[srdv], s48 offen offset:1024 lds\n" \
"v_mfma_f32_16x16x32_bf16 v[140:143], a[124:127], a[28:31], v[140:143]\n" \
"v_exp_f32_e64 v135, v135\n" \
"v_mfma_f32_16x16x32_bf16 v[48:51], a[176:179], v[196:199], v[48:51]\n" \
"ds_read_b128 a[84:87], v208 offset:4096\n" \
"v_mfma_f32_16x16x32_bf16 v[48:51], a[144:147], v[188:191], v[48:51]\n" \
"v_exp_f32_e64 v136, v136\n" \
"v_mfma_f32_16x16x32_bf16 v[52:55], a[180:183], v[196:199], v[52:55]\n" \
"v_cvt_pk_bf16_f32 v194, v132, v133\n" \
"v_cvt_pk_bf16_f32 v195, v134, v135\n" \
"v_mfma_f32_16x16x32_bf16 v[52:55], a[148:151], v[188:191], v[52:55]\n" \
"v_exp_f32_e64 v137, v137\n" \
"v_mfma_f32_16x16x32_bf16 v[56:59], a[184:187], v[196:199], v[56:59]\n" \
"v_exp_f32_e64 v138, v138\n" \
"v_mfma_f32_16x16x32_bf16 v[56:59], a[152:155], v[188:191], v[56:59]\n" \
"ds_read_b128 a[88:91], v209 offset:4096\n" \
"v_mfma_f32_16x16x32_bf16 v[60:63], a[188:191], v[196:199], v[60:63]\n" \
"v_exp_f32_e64 v139, v139\n" \
"v_mfma_f32_16x16x32_bf16 v[60:63], a[156:159], v[188:191], v[60:63]\n" \
"s_cmp_lg_u32 %[wid], 2\n" \
"s_cbranch_scc1 LtA7_%=\n" \
"global_load_dword v237, v224, s[58:59]\n" \
"s_add_u32 s61, s48, 49152\n" \
"s_mov_b32 exec_hi, 0\n" \
"s_nop 0\n" \
"buffer_load_dword v237, v224, %[srdp], s61 offen\n" \
"s_mov_b32 exec_hi, -1\n" \
"s_nop 0\n" \
"LtA7_%=:\n" \
"v_mfma_f32_16x16x32_bf16 v[144:147], a[96:99], a[32:35], v[168:171]\n" \
"v_exp_f32_e64 v140, v140\n" \
"v_mfma_f32_16x16x32_bf16 v[144:147], a[100:103], a[36:39], v[144:147]\n" \
"ds_read_b128 a[92:95], v210 offset:4096\n" \
"v_mfma_f32_16x16x32_bf16 v[144:147], a[104:107], a[40:43], v[144:147]\n" \
"v_exp_f32_e64 v141, v141\n" \
"v_mfma_f32_16x16x32_bf16 v[144:147], a[108:111], a[44:47], v[144:147]\n" \
"v_cvt_pk_bf16_f32 v196, v136, v137\n" \
"v_cvt_pk_bf16_f32 v197, v138, v139\n" \
"v_mfma_f32_16x16x32_bf16 v[148:151], a[112:115], a[32:35], v[168:171]\n" \
"v_exp_f32_e64 v142, v142\n" \
"v_mfma_f32_16x16x32_bf16 v[148:151], a[116:119], a[36:39], v[148:151]\n" \
"v_mfma_f32_16x16x32_bf16 v[148:151], a[120:123], a[40:43], v[148:151]\n" \
"buffer_load_dwordx4 %[vd0], %[srdv], s48 offen offset:2048 lds\n" \
"v_mfma_f32_16x16x32_bf16 v[148:151], a[124:127], a[44:47], v[148:151]\n" \
"v_exp_f32_e64 v143, v143\n" \
"v_mfma_f32_16x16x32_bf16 v[96:99], a[160:163], v[204:207], v[96:99]\n" \
"ds_read_b64_tr_b16 a[160:161], %[vvb] offset:57344\n" \
"v_mfma_f32_16x16x32_bf16 v[96:99], a[128:131], v[220:223], v[96:99]\n" \
"v_exp_f32_e64 v144, v144\n" \
"v_mfma_f32_16x16x32_bf16 v[100:103], a[164:167], v[204:207], v[100:103]\n" \
"v_cvt_pk_bf16_f32 v198, v140, v141\n" \
"v_cvt_pk_bf16_f32 v199, v142, v143\n" \
"v_mfma_f32_16x16x32_bf16 v[100:103], a[132:135], v[220:223], v[100:103]\n" \
"v_exp_f32_e64 v145, v145\n" \
"v_mfma_f32_16x16x32_bf16 v[104:107], a[168:171], v[204:207], v[104:107]\n" \
"ds_read_b64_tr_b16 a[162:163], %[vvb] offset:61440\n" \
"ds_read_b64_tr_b16 a[164:165], v211 offset:57344\n" \
"v_mfma_f32_16x16x32_bf16 v[104:107], a[136:139], v[220:223], v[104:107]\n" \
"v_exp_f32_e64 v146, v146\n" \
"v_mfma_f32_16x16x32_bf16 v[108:111], a[172:175], v[204:207], v[108:111]\n" \
"ds_read_b64_tr_b16 a[166:167], v211 offset:61440\n" \
"v_mfma_f32_16x16x32_bf16 v[108:111], a[140:143], v[220:223], v[108:111]\n" \
"v_exp_f32_e64 v147, v147\n" \
"v_smfmac_f32_16x16x64_bf16 v[180:183], a[192:195], v[216:223], v228\n" \
"ds_read_b64_tr_b16 a[168:169], %[vvb] offset:57856\n" \
"v_mfma_f32_16x16x32_bf16 v[152:155], a[96:99], a[48:51], v[172:175]\n" \
"v_exp_f32_e64 v148, v148\n" \
"v_mfma_f32_16x16x32_bf16 v[152:155], a[100:103], a[52:55], v[152:155]\n" \
"ds_read_b64_tr_b16 a[170:171], %[vvb] offset:61952\n" \
"v_mfma_f32_16x16x32_bf16 v[152:155], a[104:107], a[56:59], v[152:155]\n" \
"v_exp_f32_e64 v149, v149\n" \
"v_mfma_f32_16x16x32_bf16 v[152:155], a[108:111], a[60:63], v[152:155]\n" \
"v_cvt_pk_bf16_f32 v200, v144, v145\n" \
"v_cvt_pk_bf16_f32 v201, v146, v147\n" \
"v_mfma_f32_16x16x32_bf16 v[156:159], a[112:115], a[48:51], v[172:175]\n" \
"v_exp_f32_e64 v150, v150\n" \
"v_mfma_f32_16x16x32_bf16 v[156:159], a[116:119], a[52:55], v[156:159]\n" \
"ds_read_b64_tr_b16 a[172:173], v211 offset:57856\n" \
"v_mfma_f32_16x16x32_bf16 v[156:159], a[120:123], a[56:59], v[156:159]\n" \
"buffer_load_dwordx4 v215, %[srdv], s48 offen offset:3072 lds\n" \
"v_mfma_f32_16x16x32_bf16 v[156:159], a[124:127], a[60:63], v[156:159]\n" \
"v_exp_f32_e64 v151, v151\n" \
"v_mfma_f32_16x16x32_bf16 v[112:115], a[176:179], v[204:207], v[112:115]\n" \
"ds_read_b64_tr_b16 a[174:175], v211 offset:61952\n" \
"ds_read_b64_tr_b16 a[176:177], %[vvb] offset:58368\n" \
"v_mfma_f32_16x16x32_bf16 v[112:115], a[144:147], v[220:223], v[112:115]\n" \
"v_exp_f32_e64 v152, v152\n" \
"v_mfma_f32_16x16x32_bf16 v[116:119], a[180:183], v[204:207], v[116:119]\n" \
"v_cvt_pk_bf16_f32 v202, v148, v149\n" \
"v_cvt_pk_bf16_f32 v203, v150, v151\n" \
"v_mfma_f32_16x16x32_bf16 v[116:119], a[148:151], v[220:223], v[116:119]\n" \
"v_exp_f32_e64 v153, v153\n" \
"v_mfma_f32_16x16x32_bf16 v[120:123], a[184:187], v[204:207], v[120:123]\n" \
"v_exp_f32_e64 v154, v154\n" \
"v_mfma_f32_16x16x32_bf16 v[120:123], a[152:155], v[220:223], v[120:123]\n" \
"ds_read_b64_tr_b16 a[178:179], %[vvb] offset:62464\n" \
"v_mfma_f32_16x16x32_bf16 v[124:127], a[188:191], v[204:207], v[124:127]\n" \
"v_exp_f32_e64 v155, v155\n" \
"v_mfma_f32_16x16x32_bf16 v[124:127], a[156:159], v[220:223], v[124:127]\n" \
"ds_read_b64_tr_b16 a[180:181], v211 offset:58368\n" \
"s_waitcnt lgkmcnt(13)\n" \
"v_mfma_f32_16x16x32_bf16 v[128:131], a[64:67], a[0:3], v[160:163]\n" \
"v_exp_f32_e32 v156, v156\n" \
"v_mfma_f32_16x16x32_bf16 v[128:131], a[68:71], a[4:7], v[128:131]\n" \
"ds_read_b64_tr_b16 a[182:183], v211 offset:62464\n" \
"s_add_u32 m0, %[m0b], 49152\n" \
"v_mfma_f32_16x16x32_bf16 v[128:131], a[72:75], a[8:11], v[128:131]\n" \
"v_exp_f32_e64 v157, v157\n" \
"v_mfma_f32_16x16x32_bf16 v[128:131], a[76:79], a[12:15], v[128:131]\n" \
"v_cvt_pk_bf16_f32 v204, v152, v153\n" \
"v_cvt_pk_bf16_f32 v205, v154, v155\n" \
"v_mfma_f32_16x16x32_bf16 v[132:135], a[80:83], a[0:3], v[160:163]\n" \
"v_exp_f32_e64 v158, v158\n" \
"v_mfma_f32_16x16x32_bf16 v[132:135], a[84:87], a[4:7], v[132:135]\n" \
"ds_read_b64_tr_b16 a[184:185], %[vvb] offset:58880\n" \
"s_waitcnt lgkmcnt(5)\n" \
"v_mfma_f32_16x16x32_bf16 v[132:135], a[88:91], a[8:11], v[132:135]\n" \
"buffer_load_dwordx4 %[kd0], %[srdk], s48 offen offset:0 lds\n" \
"v_mfma_f32_16x16x32_bf16 v[132:135], a[92:95], a[12:15], v[132:135]\n" \
"v_exp_f32_e32 v159, v159\n" \
"v_mfma_f32_16x16x32_bf16 v[64:67], a[128:131], v[216:219], v[64:67]\n" \
"ds_read_b64_tr_b16 a[186:187], %[vvb] offset:62976\n" \
"ds_read_b64_tr_b16 a[188:189], v211 offset:58880\n" \
"v_mfma_f32_16x16x32_bf16 v[64:67], a[160:163], v[200:203], v[64:67]\n" \
"v_exp_f32_e64 v128, v128\n" \
"v_mfma_f32_16x16x32_bf16 v[68:71], a[132:135], v[216:219], v[68:71]\n" \
"v_cvt_pk_bf16_f32 v206, v156, v157\n" \
"v_cvt_pk_bf16_f32 v207, v158, v159\n" \
"v_mfma_f32_16x16x32_bf16 v[68:71], a[164:167], v[200:203], v[68:71]\n" \
"v_exp_f32_e64 v129, v129\n" \
"v_mfma_f32_16x16x32_bf16 v[72:75], a[136:139], v[216:219], v[72:75]\n" \
"ds_read_b64_tr_b16 a[190:191], v211 offset:62976\n" \
"ds_read_b64_tr_b16 a[128:129], %[vvb] offset:0\n" \
"v_mfma_f32_16x16x32_bf16 v[72:75], a[168:171], v[200:203], v[72:75]\n" \
"v_exp_f32_e64 v130, v130\n" \
"v_mfma_f32_16x16x32_bf16 v[76:79], a[140:143], v[216:219], v[76:79]\n" \
"ds_read_b64_tr_b16 a[130:131], %[vvb] offset:4096\n" \
"ds_read_b64_tr_b16 a[132:133], v211 offset:0\n" \
"v_mfma_f32_16x16x32_bf16 v[76:79], a[172:175], v[200:203], v[76:79]\n" \
"v_exp_f32_e64 v131, v131\n" \
"v_smfmac_f32_16x16x64_bf16 v[176:179], a[192:195], v[192:199], v228\n" \
"ds_read_b64_tr_b16 a[134:135], v211 offset:4096\n" \
"ds_read_b64_tr_b16 a[136:137], %[vvb] offset:512\n" \
"v_mfma_f32_16x16x32_bf16 v[136:139], a[64:67], a[16:19], v[164:167]\n" \
"v_exp_f32_e64 v132, v132\n" \
"v_mfma_f32_16x16x32_bf16 v[136:139], a[68:71], a[20:23], v[136:139]\n" \
"ds_read_b64_tr_b16 a[138:139], %[vvb] offset:4608\n" \
"ds_read_b64_tr_b16 a[140:141], v211 offset:512\n" \
"v_mfma_f32_16x16x32_bf16 v[136:139], a[72:75], a[24:27], v[136:139]\n" \
"v_exp_f32_e64 v133, v133\n" \
"v_mfma_f32_16x16x32_bf16 v[136:139], a[76:79], a[28:31], v[136:139]\n" \
"v_cvt_pk_bf16_f32 v184, v128, v129\n" \
"v_cvt_pk_bf16_f32 v185, v130, v131\n" \
"v_mfma_f32_16x16x32_bf16 v[140:143], a[80:83], a[16:19], v[164:167]\n" \
"v_exp_f32_e64 v134, v134\n" \
"v_mfma_f32_16x16x32_bf16 v[140:143], a[84:87], a[20:23], v[140:143]\n" \
"ds_read_b64_tr_b16 a[142:143], v211 offset:4608\n" \
"s_waitcnt lgkmcnt(8)\n" \
"v_mfma_f32_16x16x32_bf16 v[140:143], a[88:91], a[24:27], v[140:143]\n" \
"buffer_load_dwordx4 v212, %[srdk], s48 offen offset:1024 lds\n" \
"v_mfma_f32_16x16x32_bf16 v[140:143], a[92:95], a[28:31], v[140:143]\n" \
"v_exp_f32_e32 v135, v135\n" \
"v_mfma_f32_16x16x32_bf16 v[80:83], a[144:147], v[216:219], v[80:83]\n" \
"ds_read_b64_tr_b16 a[144:145], %[vvb] offset:1024\n" \
"ds_read_b64_tr_b16 a[146:147], %[vvb] offset:5120\n" \
"v_mfma_f32_16x16x32_bf16 v[80:83], a[176:179], v[200:203], v[80:83]\n" \
"v_exp_f32_e64 v136, v136\n" \
"v_mfma_f32_16x16x32_bf16 v[84:87], a[148:151], v[216:219], v[84:87]\n" \
"v_cvt_pk_bf16_f32 v186, v132, v133\n" \
"v_cvt_pk_bf16_f32 v187, v134, v135\n" \
"v_mfma_f32_16x16x32_bf16 v[84:87], a[180:183], v[200:203], v[84:87]\n" \
"v_exp_f32_e64 v137, v137\n" \
"v_mfma_f32_16x16x32_bf16 v[88:91], a[152:155], v[216:219], v[88:91]\n" \
"v_exp_f32_e64 v138, v138\n" \
"v_mfma_f32_16x16x32_bf16 v[88:91], a[184:187], v[200:203], v[88:91]\n" \
"ds_read_b64_tr_b16 a[148:149], v211 offset:1024\n" \
"ds_read_b64_tr_b16 a[150:151], v211 offset:5120\n" \
"v_mfma_f32_16x16x32_bf16 v[92:95], a[156:159], v[216:219], v[92:95]\n" \
"v_exp_f32_e64 v139, v139\n" \
"v_mfma_f32_16x16x32_bf16 v[92:95], a[188:191], v[200:203], v[92:95]\n" \
"ds_read_b64_tr_b16 a[152:153], %[vvb] offset:1536\n" \
"buffer_store_dwordx4 a[196:199], v235, %[srdz], s62 offen offset:1024 sc1\n" \
"s_add_u32 s62, s62, 2048\n" \
"v_mfma_f32_16x16x32_bf16 v[144:147], a[64:67], a[32:35], v[168:171]\n" \
"v_exp_f32_e64 v140, v140\n" \
"v_mfma_f32_16x16x32_bf16 v[144:147], a[68:71], a[36:39], v[144:147]\n" \
"ds_read_b64_tr_b16 a[154:155], %[vvb] offset:5632\n" \
"ds_read_b64_tr_b16 a[156:157], v211 offset:1536\n" \
"v_mfma_f32_16x16x32_bf16 v[144:147], a[72:75], a[40:43], v[144:147]\n" \
"v_exp_f32_e64 v141, v141\n" \
"v_mfma_f32_16x16x32_bf16 v[144:147], a[76:79], a[44:47], v[144:147]\n" \
"v_cvt_pk_bf16_f32 v188, v136, v137\n" \
"v_cvt_pk_bf16_f32 v189, v138, v139\n" \
"v_mfma_f32_16x16x32_bf16 v[148:151], a[80:83], a[32:35], v[168:171]\n" \
"v_exp_f32_e64 v142, v142\n" \
"v_mfma_f32_16x16x32_bf16 v[148:151], a[84:87], a[36:39], v[148:151]\n" \
"ds_read_b64_tr_b16 a[158:159], v211 offset:5632\n" \
"s_waitcnt lgkmcnt(8)\n" \
"v_mfma_f32_16x16x32_bf16 v[148:151], a[88:91], a[40:43], v[148:151]\n" \
"buffer_load_dwordx4 v213, %[srdk], s48 offen offset:2048 lds\n" \
"v_mfma_f32_16x16x32_bf16 v[148:151], a[92:95], a[44:47], v[148:151]\n" \
"v_exp_f32_e32 v143, v143\n" \
"v_mfma_f32_16x16x32_bf16 v[0:3], a[160:163], v[192:195], v[0:3]\n" \
"ds_read_b128 a[96:99], %[kb0] offset:8192\n" \
"v_mfma_f32_16x16x32_bf16 v[0:3], a[128:131], v[184:187], v[0:3]\n" \
"v_exp_f32_e64 v144, v144\n" \
"v_mfma_f32_16x16x32_bf16 v[4:7], a[164:167], v[192:195], v[4:7]\n" \
"v_cvt_pk_bf16_f32 v190, v140, v141\n" \
"v_cvt_pk_bf16_f32 v191, v142, v143\n" \
"v_mfma_f32_16x16x32_bf16 v[4:7], a[132:135], v[184:187], v[4:7]\n" \
"v_exp_f32_e64 v145, v145\n" \
"v_mfma_f32_16x16x32_bf16 v[8:11], a[168:171], v[192:195], v[8:11]\n" \
"ds_read_b128 a[100:103], v208 offset:8192\n" \
"v_mfma_f32_16x16x32_bf16 v[8:11], a[136:139], v[184:187], v[8:11]\n" \
"v_exp_f32_e64 v146, v146\n" \
"v_mfma_f32_16x16x32_bf16 v[12:15], a[172:175], v[192:195], v[12:15]\n" \
"ds_read_b128 a[104:107], v209 offset:8192\n" \
"v_mfma_f32_16x16x32_bf16 v[12:15], a[140:143], v[184:187], v[12:15]\n" \
"v_exp_f32_e64 v147, v147\n" \
"v_smfmac_f32_16x16x64_bf16 v[180:183], a[192:195], v[200:207], v228\n" \
"ds_read_b128 a[108:111], v210 offset:8192\n" \
"v_mfma_f32_16x16x32_bf16 v[152:155], a[64:67], a[48:51], v[172:175]\n" \
"v_exp_f32_e64 v148, v148\n" \
"v_mfma_f32_16x16x32_bf16 v[152:155], a[68:71], a[52:55], v[152:155]\n" \
"ds_read_b128 a[112:115], %[kb0] offset:12288\n" \
"v_mfma_f32_16x16x32_bf16 v[152:155], a[72:75], a[56:59], v[152:155]\n" \
"v_exp_f32_e64 v149, v149\n" \
"v_mfma_f32_16x16x32_bf16 v[152:155], a[76:79], a[60:63], v[152:155]\n" \
"v_cvt_pk_bf16_f32 v216, v144, v145\n" \
"v_cvt_pk_bf16_f32 v217, v146, v147\n" \
"v_mfma_f32_16x16x32_bf16 v[156:159], a[80:83], a[48:51], v[172:175]\n" \
"v_exp_f32_e64 v150, v150\n" \
"v_mfma_f32_16x16x32_bf16 v[156:159], a[84:87], a[52:55], v[156:159]\n" \
"ds_read_b128 a[116:119], v208 offset:12288\n" \
"s_waitcnt lgkmcnt(6)\n" \
"v_mfma_f32_16x16x32_bf16 v[156:159], a[88:91], a[56:59], v[156:159]\n" \
"buffer_load_dwordx4 v214, %[srdk], s48 offen offset:3072 lds\n" \
"v_mfma_f32_16x16x32_bf16 v[156:159], a[92:95], a[60:63], v[156:159]\n" \
"v_exp_f32_e32 v151, v151\n" \
"v_mfma_f32_16x16x32_bf16 v[16:19], a[176:179], v[192:195], v[16:19]\n" \
"ds_read_b128 a[120:123], v209 offset:12288\n" \
"v_mfma_f32_16x16x32_bf16 v[16:19], a[144:147], v[184:187], v[16:19]\n" \
"v_exp_f32_e64 v152, v152\n" \
"v_mfma_f32_16x16x32_bf16 v[20:23], a[180:183], v[192:195], v[20:23]\n" \
"v_cvt_pk_bf16_f32 v218, v148, v149\n" \
"v_cvt_pk_bf16_f32 v219, v150, v151\n" \
"v_mfma_f32_16x16x32_bf16 v[20:23], a[148:151], v[184:187], v[20:23]\n" \
"v_exp_f32_e64 v153, v153\n" \
"v_mfma_f32_16x16x32_bf16 v[24:27], a[184:187], v[192:195], v[24:27]\n" \
"v_exp_f32_e64 v154, v154\n" \
"v_mfma_f32_16x16x32_bf16 v[24:27], a[152:155], v[184:187], v[24:27]\n" \
"ds_read_b128 a[124:127], v210 offset:12288\n" \
"s_add_u32 s48, s48, 0x4000\n" \
"v_mfma_f32_16x16x32_bf16 v[28:31], a[188:191], v[192:195], v[28:31]\n" \
"v_exp_f32_e64 v155, v155\n" \
"v_mfma_f32_16x16x32_bf16 v[28:31], a[156:159], v[184:187], v[28:31]\n" \
"s_waitcnt vmcnt(15) lgkmcnt(2)\n" \
"s_barrier\n" \
"v_mfma_f32_16x16x32_bf16 v[128:131], a[96:99], a[0:3], v[160:163]\n" \
"v_exp_f32_e64 v156, v156\n" \
"v_mfma_f32_16x16x32_bf16 v[128:131], a[100:103], a[4:7], v[128:131]\n" \
"s_add_u32 m0, %[m0b], 114688\n" \
"v_mfma_f32_16x16x32_bf16 v[128:131], a[104:107], a[8:11], v[128:131]\n" \
"v_exp_f32_e64 v157, v157\n" \
"v_mfma_f32_16x16x32_bf16 v[128:131], a[108:111], a[12:15], v[128:131]\n" \
"v_cvt_pk_bf16_f32 v220, v152, v153\n" \
"v_cvt_pk_bf16_f32 v221, v154, v155\n" \
"v_mfma_f32_16x16x32_bf16 v[132:135], a[112:115], a[0:3], v[160:163]\n" \
"v_exp_f32_e64 v158, v158\n" \
"v_mfma_f32_16x16x32_bf16 v[132:135], a[116:119], a[4:7], v[132:135]\n" \
"s_waitcnt lgkmcnt(0)\n" \
"v_mfma_f32_16x16x32_bf16 v[132:135], a[120:123], a[8:11], v[132:135]\n" \
"buffer_load_dwordx4 %[vd0], %[srdv], s48 offen offset:0 lds\n" \
"v_mfma_f32_16x16x32_bf16 v[132:135], a[124:127], a[12:15], v[132:135]\n" \
"v_exp_f32_e32 v159, v159\n" \
"v_mfma_f32_16x16x32_bf16 v[32:35], a[160:163], v[196:199], v[32:35]\n" \
"ds_read_b128 a[64:67], %[kb0] offset:16384\n" \
"v_mfma_f32_16x16x32_bf16 v[32:35], a[128:131], v[188:191], v[32:35]\n" \
"v_exp_f32_e64 v128, v128\n" \
"v_mfma_f32_16x16x32_bf16 v[36:39], a[164:167], v[196:199], v[36:39]\n" \
"v_cvt_pk_bf16_f32 v222, v156, v157\n" \
"v_cvt_pk_bf16_f32 v223, v158, v159\n" \
"v_mfma_f32_16x16x32_bf16 v[36:39], a[132:135], v[188:191], v[36:39]\n" \
"v_exp_f32_e64 v129, v129\n" \
"v_mfma_f32_16x16x32_bf16 v[40:43], a[168:171], v[196:199], v[40:43]\n" \
"ds_read_b128 a[68:71], v208 offset:16384\n" \
"v_mfma_f32_16x16x32_bf16 v[40:43], a[136:139], v[188:191], v[40:43]\n" \
"v_exp_f32_e64 v130, v130\n" \
"v_mfma_f32_16x16x32_bf16 v[44:47], a[172:175], v[196:199], v[44:47]\n" \
"ds_read_b128 a[72:75], v209 offset:16384\n" \
"v_mfma_f32_16x16x32_bf16 v[44:47], a[140:143], v[188:191], v[44:47]\n" \
"v_exp_f32_e64 v131, v131\n" \
"v_smfmac_f32_16x16x64_bf16 v[176:179], a[192:195], v[184:191], v228\n" \
"v_mfma_f32_16x16x32_bf16 v[136:139], a[96:99], a[16:19], v[164:167]\n" \
"v_exp_f32_e64 v132, v132\n" \
"v_mfma_f32_16x16x32_bf16 v[136:139], a[100:103], a[20:23], v[136:139]\n" \
"ds_read_b128 a[76:79], v210 offset:16384\n" \
"v_mfma_f32_16x16x32_bf16 v[136:139], a[104:107], a[24:27], v[136:139]\n" \
"v_exp_f32_e64 v133, v133\n" \
"v_mfma_f32_16x16x32_bf16 v[136:139], a[108:111], a[28:31], v[136:139]\n" \
"v_cvt_pk_bf16_f32 v192, v128, v129\n" \
"v_cvt_pk_bf16_f32 v193, v130, v131\n" \
"v_mfma_f32_16x16x32_bf16 v[140:143], a[112:115], a[16:19], v[164:167]\n" \
"v_exp_f32_e64 v134, v134\n" \
"v_mfma_f32_16x16x32_bf16 v[140:143], a[116:119], a[20:23], v[140:143]\n" \
"ds_read_b128 a[80:83], %[kb0] offset:20480\n" \
"v_mfma_f32_16x16x32_bf16 v[140:143], a[120:123], a[24:27], v[140:143]\n" \
"buffer_load_dwordx4 v215, %[srdv], s48 offen offset:1024 lds\n" \
"v_mfma_f32_16x16x32_bf16 v[140:143], a[124:127], a[28:31], v[140:143]\n" \
".if . - adaln_body != 20480\n" \
".error \"size model\"\n" \
".endif\n" \
"Lk4_%=:\n" \
"s_cbranch_execz Lk5_%=\n" \
"s_nop 0\n" \
"v_exp_f32_e64 v135, v135\n" \
"v_mfma_f32_16x16x32_bf16 v[48:51], a[176:179], v[196:199], v[48:51]\n" \
"ds_read_b128 a[84:87], v208 offset:20480\n" \
"v_mfma_f32_16x16x32_bf16 v[48:51], a[144:147], v[188:191], v[48:51]\n" \
"v_exp_f32_e64 v136, v136\n" \
"v_mfma_f32_16x16x32_bf16 v[52:55], a[180:183], v[196:199], v[52:55]\n" \
"v_cvt_pk_bf16_f32 v194, v132, v133\n" \
"v_cvt_pk_bf16_f32 v195, v134, v135\n" \
"v_mfma_f32_16x16x32_bf16 v[52:55], a[148:151], v[188:191], v[52:55]\n" \
"v_exp_f32_e64 v137, v137\n" \
"v_mfma_f32_16x16x32_bf16 v[56:59], a[184:187], v[196:199], v[56:59]\n" \
"v_exp_f32_e64 v138, v138\n" \
"v_mfma_f32_16x16x32_bf16 v[56:59], a[152:155], v[188:191], v[56:59]\n" \
"ds_read_b128 a[88:91], v209 offset:20480\n" \
"v_mfma_f32_16x16x32_bf16 v[60:63], a[188:191], v[196:199], v[60:63]\n" \
"v_exp_f32_e64 v139, v139\n" \
"v_mfma_f32_16x16x32_bf16 v[60:63], a[156:159], v[188:191], v[60:63]\n" \
"s_cmp_lg_u32 %[wid], 3\n" \
"s_cbranch_scc1 LtA1_%=\n" \
"global_load_dword v237, v224, s[58:59]\n" \
"s_add_u32 s61, s48, 49152\n" \
"s_mov_b32 exec_hi, 0\n" \
"s_nop 0\n" \
"buffer_load_dword v237, v224, %[srdp], s61 offen\n" \
"s_mov_b32 exec_hi, -1\n" \
"s_nop 0\n" \
"LtA1_%=:\n" \
"v_mfma_f32_16x16x32_bf16 v[144:147], a[96:99], a[32:35], v[168:171]\n" \
"v_exp_f32_e64 v140, v140\n" \
"v_mfma_f32_16x16x32_bf16 v[144:147], a[100:103], a[36:39], v[144:147]\n" \
"ds_read_b128 a[92:95], v210 offset:20480\n" \
"v_mfma_f32_16x16x32_bf16 v[144:147], a[104:107], a[40:43], v[144:147]\n" \
"v_exp_f32_e64 v141, v141\n" \
"v_mfma_f32_16x16x32_bf16 v[144:147], a[108:111], a[44:47], v[144:147]\n" \
"v_cvt_pk_bf16_f32 v196, v136, v137\n" \
"v_cvt_pk_bf16_f32 v197, v138, v139\n" \
"v_mfma_f32_16x16x32_bf16 v[148:151], a[112:115], a[32:35], v[168:171]\n" \
"v_exp_f32_e64 v142, v142\n" \
"v_mfma_f32_16x16x32_bf16 v[148:151], a[116:119], a[36:39], v[148:151]\n" \
"v_mfma_f32_16x16x32_bf16 v[148:151], a[120:123], a[40:43], v[148:151]\n" \
"buffer_load_dwordx4 %[vd0], %[srdv], s48 offen offset:2048 lds\n" \
"v_mfma_f32_16x16x32_bf16 v[148:151], a[124:127], a[44:47], v[148:151]\n" \
"v_exp_f32_e64 v143, v143\n" \
"v_mfma_f32_16x16x32_bf16 v[96:99], a[160:163], v[204:207], v[96:99]\n" \
"ds_read_b64_tr_b16 a[160:161], %[vvb] offset:8192\n" \
"v_mfma_f32_16x16x32_bf16 v[96:99], a[128:131], v[220:223], v[96:99]\n" \
"v_exp_f32_e64 v144, v144\n" \
"v_mfma_f32_16x16x32_bf16 v[100:103], a[164:167], v[204:207], v[100:103]\n" \
"v_cvt_pk_bf16_f32 v198, v140, v141\n" \
"v_cvt_pk_bf16_f32 v199, v142, v143\n" \
"v_mfma_f32_16x16x32_bf16 v[100:103], a[132:135], v[220:223], v[100:103]\n" \
"v_exp_f32_e64 v145, v145\n" \
"v_mfma_f32_16x16x32_bf16 v[104:107], a[168:171], v[204:207], v[104:107]\n" \
"ds_read_b64_tr_b16 a[162:163], %[vvb] offset:12288\n" \
"ds_read_b64_tr_b16 a[164:165], v211 offset:8192\n" \
"v_mfma_f32_16x16x32_bf16 v[104:107], a[136:139], v[220:223], v[104:107]\n" \
"v_exp_f32_e64 v146, v146\n" \
"v_mfma_f32_16x16x32_bf16 v[108:111], a[172:175], v[204:207], v[108:111]\n" \
"ds_read_b64_tr_b16 a[166:167], v211 offset:12288\n" \
"v_mfma_f32_16x16x32_bf16 v[108:111], a[140:143], v[220:223], v[108:111]\n" \
"v_exp_f32_e64 v147, v147\n" \
"v_smfmac_f32_16x16x64_bf16 v[180:183], a[192:195], v[216:223], v228\n" \
"ds_read_b64_tr_b16 a[168:169], %[vvb] offset:8704\n" \
"v_mfma_f32_16x16x32_bf16 v[152:155], a[96:99], a[48:51], v[172:175]\n" \
"v_exp_f32_e64 v148, v148\n" \
"v_mfma_f32_16x16x32_bf16 v[152:155], a[100:103], a[52:55], v[152:155]\n" \
"ds_read_b64_tr_b16 a[170:171], %[vvb] offset:12800\n" \
"v_mfma_f32_16x16x32_bf16 v[152:155], a[104:107], a[56:59], v[152:155]\n" \
"v_exp_f32_e64 v149, v149\n" \
"v_mfma_f32_16x16x32_bf16 v[152:155], a[108:111], a[60:63], v[152:155]\n" \
"v_cvt_pk_bf16_f32 v200, v144, v145\n" \
"v_cvt_pk_bf16_f32 v201, v146, v147\n" \
"v_mfma_f32_16x16x32_bf16 v[156:159], a[112:115], a[48:51], v[172:175]\n" \
"v_exp_f32_e64 v150, v150\n" \
"v_mfma_f32_16x16x32_bf16 v[156:159], a[116:119], a[52:55], v[156:159]\n" \
"ds_read_b64_tr_b16 a[172:173], v211 offset:8704\n" \
"v_mfma_f32_16x16x32_bf16 v[156:159], a[120:123], a[56:59], v[156:159]\n" \
"buffer_load_dwordx4 v215, %[srdv], s48 offen offset:3072 lds\n" \
"v_mfma_f32_16x16x32_bf16 v[156:159], a[124:127], a[60:63], v[156:159]\n" \
"v_exp_f32_e64 v151, v151\n" \
"v_mfma_f32_16x16x32_bf16 v[112:115], a[176:179], v[204:207], v[112:115]\n" \
"ds_read_b64_tr_b16 a[174:175], v211 offset:12800\n" \
"ds_read_b64_tr_b16 a[176:177], %[vvb] offset:9216\n" \
"v_mfma_f32_16x16x32_bf16 v[112:115], a[144:147], v[220:223], v[112:115]\n" \
"v_exp_f32_e64 v152, v152\n" \
"v_mfma_f32_16x16x32_bf16 v[116:119], a[180:183], v[204:207], v[116:119]\n" \
"v_cvt_pk_bf16_f32 v202, v148, v149\n" \
"v_cvt_pk_bf16_f32 v203, v150, v151\n" \
"v_mfma_f32_16x16x32_bf16 v[116:119], a[148:151], v[220:223], v[116:119]\n" \
"v_exp_f32_e64 v153, v153\n" \
"v_mfma_f32_16x16x32_bf16 v[120:123], a[184:187], v[204:207], v[120:123]\n" \
"v_exp_f32_e64 v154, v154\n" \
"v_mfma_f32_16x16x32_bf16 v[120:123], a[152:155], v[220:223], v[120:123]\n" \
"ds_read_b64_tr_b16 a[178:179], %[vvb] offset:13312\n" \
"v_mfma_f32_16x16x32_bf16 v[124:127], a[188:191], v[204:207], v[124:127]\n" \
"v_exp_f32_e64 v155, v155\n" \
"v_mfma_f32_16x16x32_bf16 v[124:127], a[156:159], v[220:223], v[124:127]\n" \
"ds_read_b64_tr_b16 a[180:181], v211 offset:9216\n" \
"s_waitcnt lgkmcnt(13)\n" \
"s_cmp_lg_u32 s60, 4\n" \
"s_cbranch_scc1 LmA_%=\n" \
"s_nop 0\n" \
"v_readlane_b32 s64, %[tlo], 9\n" \
"v_readlane_b32 s65, %[thi], 9\n" \
"s_mov_b32 s66, %[wbytes]\n" \
"v_mov_b32_e32 v229, %[vg0]\n" \
"s_mov_b32 s44, %[m0t]\n" \
"s_mov_b32 s45, 0\n" \
"s_cmp_lg_u32 s52, 0\n" \
"s_cbranch_scc1 LmB_%=\n" \
"s_add_u32 s62, %[sin], 8192\n" \
"s_add_u32 s63, %[sin], 28672\n" \
"s_waitcnt vmcnt(13)\n" \
"s_nop 0\n" \
"ds_read_b64_tr_b16 a[232:233], %[ta0] offset:0\n" \
"ds_read_b64_tr_b16 a[234:235], %[ta0] offset:128\n" \
"ds_read_b64_tr_b16 a[236:237], %[ta0] offset:1024\n" \
"ds_read_b64_tr_b16 a[238:239], %[ta0] offset:1152\n" \
"ds_read_b64_tr_b16 a[240:241], %[ta0] offset:2048\n" \
"ds_read_b64_tr_b16 a[242:243], %[ta0] offset:2176\n" \
"ds_read_b64_tr_b16 a[244:245], %[ta0] offset:3072\n" \
"ds_read_b64_tr_b16 a[246:247], %[ta0] offset:3200\n" \
"ds_read_b64_tr_b16 a[248:249], %[ta0] offset:4096\n" \
"ds_read_b64_tr_b16 a[250:251], %[ta0] offset:4224\n" \
"ds_read_b64_tr_b16 a[252:253], %[ta0] offset:5120\n" \
"ds_read_b64_tr_b16 a[254:255], %[ta0] offset:5248\n" \
"v_add_u32_e32 v234, %[m0t], v235\n" \
"s_waitcnt lgkmcnt(8)\n" \
"v_mfma_f32_16x16x32_bf16 v[230:233], a[232:235], a[200:203], 0\n" \
"v_mfma_f32_16x16x32_bf16 v[230:233], a[236:239], a[204:207], v[230:233]\n" \
"ds_read_b64_tr_b16 a[232:233], %[ta0] offset:6144\n" \
"ds_read_b64_tr_b16 a[234:235], %[ta0] offset:6272\n" \
"ds_read_b64_tr_b16 a[236:237], %[ta0] offset:7168\n" \
"ds_read_b64_tr_b16 a[238:239], %[ta0] offset:7296\n" \
"s_waitcnt lgkmcnt(4)\n" \
"s_nop 0\n" \
"v_mfma_f32_16x16x32_bf16 v[230:233], a[240:243], a[208:211], v[230:233]\n" \
"v_mfma_f32_16x16x32_bf16 v[230:233], a[244:247], a[212:215], v[230:233]\n" \
"v_mfma_f32_16x16x32_bf16 v[230:233], a[248:251], a[216:219], v[230:233]\n" \
"v_mfma_f32_16x16x32_bf16 v[230:233], a[252:255], a[220:223], v[230:233]\n" \
"s_waitcnt lgkmcnt(0)\n" \
"s_nop 0\n" \
"v_mfma_f32_16x16x32_bf16 v[230:233], a[232:235], a[224:227], v[230:233]\n" \
"v_mfma_f32_16x16x32_bf16 v[230:233], a[236:239], a[228:231], v[230:233]\n" \
"s_nop 7\n" \
"s_nop 0\n" \
"ds_write_b128 v234, v[230:233] offset:7168\n" \
"LmB_%=:\n" \
"v_mfma_f32_16x16x32_bf16 v[128:131], a[64:67], a[0:3], v[160:163]\n" \
"v_exp_f32_e64 v156, v156\n" \
"v_mfma_f32_16x16x32_bf16 v[128:131], a[68:71], a[4:7], v[128:131]\n" \
"ds_read_b64_tr_b16 a[182:183], v211 offset:13312\n" \
"s_add_u32 m0, %[m0b], 0\n" \
"v_mfma_f32_16x16x32_bf16 v[128:131], a[72:75], a[8:11], v[128:131]\n" \
"v_exp_f32_e32 v157, v157\n" \
"v_mfma_f32_16x16x32_bf16 v[128:131], a[76:79], a[12:15], v[128:131]\n" \
"v_cvt_pk_bf16_f32 v204, v152, v153\n" \
"v_cvt_pk_bf16_f32 v205, v154, v155\n" \
"v_mfma_f32_16x16x32_bf16 v[132:135], a[80:83], a[0:3], v[160:163]\n" \
"v_exp_f32_e64 v158, v158\n" \
"v_mfma_f32_16x16x32_bf16 v[132:135], a[84:87], a[4:7], v[132:135]\n" \
"ds_read_b64_tr_b16 a[184:185], %[vvb] offset:9728\n" \
"s_waitcnt lgkmcnt(5)\n" \
"v_mfma_f32_16x16x32_bf16 v[132:135], a[88:91], a[8:11], v[132:135]\n" \
"buffer_load_dwordx4 %[kd0], %[srdk], s48 offen offset:0 lds\n" \
"v_mfma_f32_16x16x32_bf16 v[132:135], a[92:95], a[12:15], v[132:135]\n" \
"v_exp_f32_e32 v159, v159\n" \
"v_mfma_f32_16x16x32_bf16 v[64:67], a[128:131], v[216:219], v[64:67]\n" \
"ds_read_b64_tr_b16 a[186:187], %[vvb] offset:13824\n" \
"ds_read_b64_tr_b16 a[188:189], v211 offset:9728\n" \
"v_mfma_f32_16x16x32_bf16 v[64:67], a[160:163], v[200:203], v[64:67]\n" \
"v_exp_f32_e64 v128, v128\n" \
"v_mfma_f32_16x16x32_bf16 v[68:71], a[132:135], v[216:219], v[68:71]\n" \
"v_cvt_pk_bf16_f32 v206, v156, v157\n" \
"v_cvt_pk_bf16_f32 v207, v158, v159\n" \
"v_mfma_f32_16x16x32_bf16 v[68:71], a[164:167], v[200:203], v[68:71]\n" \
"v_exp_f32_e64 v129, v129\n" \
"v_mfma_f32_16x16x32_bf16 v[72:75], a[136:139], v[216:219], v[72:75]\n" \
"ds_read_b64_tr_b16 a[190:191], v211 offset:13824\n" \
"ds_read_b64_tr_b16 a[128:129], %[vvb] offset:16384\n" \
"s_add_u32 s60, s60, 1\n" \
"s_nop 0\n" \
"v_readlane_b32 s58, %[tlo], s60\n" \
"v_mfma_f32_16x16x32_bf16 v[72:75], a[168:171], v[200:203], v[72:75]\n" \
"v_exp_f32_e64 v130, v130\n" \
"v_mfma_f32_16x16x32_bf16 v[76:79], a[140:143], v[216:219], v[76:79]\n" \
"ds_read_b64_tr_b16 a[130:131], %[vvb] offset:20480\n" \
"ds_read_b64_tr_b16 a[132:133], v211 offset:16384\n" \
"v_readlane_b32 s59, %[thi], s60\n" \
"v_mfma_f32_16x16x32_bf16 v[76:79], a[172:175], v[200:203], v[76:79]\n" \
"v_exp_f32_e64 v131, v131\n" \
"v_smfmac_f32_16x16x64_bf16 v[176:179], a[192:195], v[192:199], v228\n" \
"ds_read_b64_tr_b16 a[134:135], v211 offset:20480\n" \
"ds_read_b64_tr_b16 a[136:137], %[vvb] offset:16896\n" \
"s_cmp_eq_u32 s60, 7\n" \
"s_cselect_b32 s61, 56, 1\n" \
"v_mul_u32_u24_e64 v236, s61, v224\n" \
"v_mfma_f32_16x16x32_bf16 v[136:139], a[64:67], a[16:19], v[164:167]\n" \
"v_exp_f32_e64 v132, v132\n" \
"v_mfma_f32_16x16x32_bf16 v[136:139], a[68:71], a[20:23], v[136:139]\n" \
"ds_read_b64_tr_b16 a[138:139], %[vvb] offset:20992\n" \
"ds_read_b64_tr_b16 a[140:141], v211 offset:16896\n" \
"v_mfma_f32_16x16x32_bf16 v[136:139], a[72:75], a[24:27], v[136:139]\n" \
"v_exp_f32_e64 v133, v133\n" \
"v_mfma_f32_16x16x32_bf16 v[136:139], a[76:79], a[28:31], v[136:139]\n" \
"v_cvt_pk_bf16_f32 v184, v128, v129\n" \
"v_cvt_pk_bf16_f32 v185, v130, v131\n" \
"v_mfma_f32_16x16x32_bf16 v[140:143], a[80:83], a[16:19], v[164:167]\n" \
"v_exp_f32_e64 v134, v134\n" \
"v_mfma_f32_16x16x32_bf16 v[140:143], a[84:87], a[20:23], v[140:143]\n" \
"ds_read_b64_tr_b16 a[142:143], v211 offset:20992\n" \
"s_waitcnt lgkmcnt(8)\n" \
"v_mfma_f32_16x16x32_bf16 v[140:143], a[88:91], a[24:27], v[140:143]\n" \
"buffer_load_dwordx4 v212, %[srdk], s48 offen offset:1024 lds\n" \
"v_mfma_f32_16x16x32_bf16 v[140:143], a[92:95], a[28:31], v[140:143]\n" \
"v_exp_f32_e32 v135, v135\n" \
"v_mfma_f32_16x16x32_bf16 v[80:83], a[144:147], v[216:219], v[80:83]\n" \
"ds_read_b64_tr_b16 a[144:145], %[vvb] offset:17408\n" \
"ds_read_b64_tr_b16 a[146:147], %[vvb] offset:21504\n" \
"v_mfma_f32_16x16x32_bf16 v[80:83], a[176:179], v[200:203], v[80:83]\n" \
"v_exp_f32_e64 v136, v136\n" \
"v_mfma_f32_16x16x32_bf16 v[84:87], a[148:151], v[216:219], v[84:87]\n" \
"v_cvt_pk_bf16_f32 v186, v132, v133\n" \
"v_cvt_pk_bf16_f32 v187, v134, v135\n" \
"v_mfma_f32_16x16x32_bf16 v[84:87], a[180:183], v[200:203], v[84:87]\n" \
"v_exp_f32_e64 v137, v137\n" \
"v_mfma_f32_16x16x32_bf16 v[88:91], a[152:155], v[216:219], v[88:91]\n" \
"v_exp_f32_e64 v138, v138\n" \
"v_mfma_f32_16x16x32_bf16 v[88:91], a[184:187], v[200:203], v[88:91]\n" \
"ds_read_b64_tr_b16 a[148:149], v211 offset:17408\n" \
"ds_read_b64_tr_b16 a[150:151], v211 offset:21504\n" \
"v_mfma_f32_16x16x32_bf16 v[92:95], a[156:159], v[216:219], v[92:95]\n" \
"v_exp_f32_e64 v139, v139\n" \
"v_mfma_f32_16x16x32_bf16 v[92:95], a[188:191], v[200:203], v[92:95]\n" \
"ds_read_b64_tr_b16 a[152:153], %[vvb] offset:17920\n" \
"buffer_store_dwordx4 a[196:199], v235, %[srdz], s63 offen offset:0 sc1\n" \
"v_mfma_f32_16x16x32_bf16 v[144:147], a[64:67], a[32:35], v[168:171]\n" \
"v_exp_f32_e64 v140, v140\n" \
"v_mfma_f32_16x16x32_bf16 v[144:147], a[68:71], a[36:39], v[144:147]\n" \
"ds_read_b64_tr_b16 a[154:155], %[vvb] offset:22016\n" \
"ds_read_b64_tr_b16 a[156:157], v211 offset:17920\n" \
"v_mfma_f32_16x16x32_bf16 v[144:147], a[72:75], a[40:43], v[144:147]\n" \
"v_exp_f32_e64 v141, v141\n" \
"v_mfma_f32_16x16x32_bf16 v[144:147], a[76:79], a[44:47], v[144:147]\n" \
"v_cvt_pk_bf16_f32 v188, v136, v137\n" \
"v_cvt_pk_bf16_f32 v189, v138, v139\n" \
"v_mfma_f32_16x16x32_bf16 v[148:151], a[80:83], a[32:35], v[168:171]\n" \
"v_exp_f32_e64 v142, v142\n" \
"v_mfma_f32_16x16x32_bf16 v[148:151], a[84:87], a[36:39], v[148:151]\n" \
"ds_read_b64_tr_b16 a[158:159], v211 offset:22016\n" \
"s_waitcnt lgkmcnt(8)\n" \
"v_mfma_f32_16x16x32_bf16 v[148:151], a[88:91], a[40:43], v[148:151]\n" \
"buffer_load_dwordx4 v213, %[srdk], s48 offen offset:2048 lds\n" \
"v_mfma_f32_16x16x32_bf16 v[148:151], a[92:95], a[44:47], v[148:151]\n" \
"v_exp_f32_e32 v143, v143\n" \
"v_mfma_f32_16x16x32_bf16 v[0:3], a[160:163], v[192:195], v[0:3]\n" \
"ds_read_b128 a[96:99], %[kb0] offset:24576\n" \
"v_mfma_f32_16x16x32_bf16 v[0:3], a[128:131], v[184:187], v[0:3]\n" \
"v_exp_f32_e64 v144, v144\n" \
"v_mfma_f32_16x16x32_bf16 v[4:7], a[164:167], v[192:195], v[4:7]\n" \
"v_cvt_pk_bf16_f32 v190, v140, v141\n" \
"v_cvt_pk_bf16_f32 v191, v142, v143\n" \
"v_mfma_f32_16x16x32_bf16 v[4:7], a[132:135], v[184:187], v[4:7]\n" \
"v_exp_f32_e64 v145, v145\n" \
"v_mfma_f32_16x16x32_bf16 v[8:11], a[168:171], v[192:195], v[8:11]\n" \
"ds_read_b128 a[100:103], v208 offset:24576\n" \
"v_mfma_f32_16x16x32_bf16 v[8:11], a[136:139], v[184:187], v[8:11]\n" \
"v_exp_f32_e64 v146, v146\n" \
"v_mfma_f32_16x16x32_bf16 v[12:15], a[172:175], v[192:195], v[12:15]\n" \
"ds_read_b128 a[104:107], v209 offset:24576\n" \
"v_mfma_f32_16x16x32_bf16 v[12:15], a[140:143], v[184:187], v[12:15]\n" \
"v_exp_f32_e64 v147, v147\n" \
"v_smfmac_f32_16x16x64_bf16 v[180:183], a[192:195], v[200:207], v228\n" \
"ds_read_b128 a[108:111], v210 offset:24576\n" \
"v_mfma_f32_16x16x32_bf16 v[152:155], a[64:67], a[48:51], v[172:175]\n" \
"v_exp_f32_e64 v148, v148\n" \
"v_mfma_f32_16x16x32_bf16 v[152:155], a[68:71], a[52:55], v[152:155]\n" \
"ds_read_b128 a[112:115], %[kb0] offset:28672\n" \
"v_mfma_f32_16x16x32_bf16 v[152:155], a[72:75], a[56:59], v[152:155]\n" \
"v_exp_f32_e64 v149, v149\n" \
"v_mfma_f32_16x16x32_bf16 v[152:155], a[76:79], a[60:63], v[152:155]\n" \
"v_cvt_pk_bf16_f32 v216, v144, v145\n" \
"v_cvt_pk_bf16_f32 v217, v146, v147\n" \
"v_mfma_f32_16x16x32_bf16 v[156:159], a[80:83], a[48:51], v[172:175]\n" \
"v_exp_f32_e64 v150, v150\n" \
"v_mfma_f32_16x16x32_bf16 v[156:159], a[84:87], a[52:55], v[156:159]\n" \
"ds_read_b128 a[116:119], v208 offset:28672\n" \
"s_waitcnt lgkmcnt(6)\n" \
"v_mfma_f32_16x16x32_bf16 v[156:159], a[88:91], a[56:59], v[156:159]\n" \
"buffer_load_dwordx4 v214, %[srdk], s48 offen offset:3072 lds\n" \
"v_mfma_f32_16x16x32_bf16 v[156:159], a[92:95], a[60:63], v[156:159]\n" \
"v_exp_f32_e32 v151, v151\n" \
"v_mfma_f32_16x16x32_bf16 v[16:19], a[176:179], v[192:195], v[16:19]\n" \
"ds_read_b128 a[120:123], v209 offset:28672\n" \
"v_mfma_f32_16x16x32_bf16 v[16:19], a[144:147], v[184:187], v[16:19]\n" \
"v_exp_f32_e64 v152, v152\n" \
"v_mfma_f32_16x16x32_bf16 v[20:23], a[180:183], v[192:195], v[20:23]\n" \
"v_cvt_pk_bf16_f32 v218, v148, v149\n" \
"v_cvt_pk_bf16_f32 v219, v150, v151\n" \
"v_mfma_f32_16x16x32_bf16 v[20:23], a[148:151], v[184:187], v[20:23]\n" \
"v_exp_f32_e64 v153, v153\n" \
"v_mfma_f32_16x16x32_bf16 v[24:27], a[184:187], v[192:195], v[24:27]\n" \
"v_exp_f32_e64 v154, v154\n" \
"v_mfma_f32_16x16x32_bf16 v[24:27], a[152:155], v[184:187], v[24:27]\n" \
"ds_read_b128 a[124:127], v210 offset:28672\n" \
"s_add_u32 s48, s48, 0x4000\n" \
"s_mov_b32 m0, s44\n" \
"s_addk_i32 s44, 0x400\n" \
"buffer_load_dwordx4 v229, s[64:67], s45 offen offset:0 lds\n" \
"s_add_u32 s45, s45, 229376\n" \
"v_mfma_f32_16x16x32_bf16 v[28:31], a[188:191], v[192:195], v[28:31]\n" \
"v_exp_f32_e64 v155, v155\n" \
"v_mfma_f32_16x16x32_bf16 v[28:31], a[156:159], v[184:187], v[28:31]\n" \
"s_waitcnt vmcnt(15) lgkmcnt(2)\n" \
"s_barrier\n" \
"v_mfma_f32_16x16x32_bf16 v[128:131], a[96:99], a[0:3], v[160:163]\n" \
"v_exp_f32_e64 v156, v156\n" \
"v_mfma_f32_16x16x32_bf16 v[128:131], a[100:103], a[4:7], v[128:131]\n" \
"s_add_u32 m0, %[m0b], 65536\n" \
"v_mfma_f32_16x16x32_bf16 v[128:131], a[104:107], a[8:11], v[128:131]\n" \
"v_exp_f32_e64 v157, v157\n" \
"v_mfma_f32_16x16x32_bf16 v[128:131], a[108:111], a[12:15], v[128:131]\n" \
"v_cvt_pk_bf16_f32 v220, v152, v153\n" \
"v_cvt_pk_bf16_f32 v221, v154, v155\n" \
"v_mfma_f32_16x16x32_bf16 v[132:135], a[112:115], a[0:3], v[160:163]\n" \
"v_exp_f32_e64 v158, v158\n" \
"v_mfma_f32_16x16x32_bf16 v[132:135], a[116:119], a[4:7], v[132:135]\n" \
"s_cmp_lg_u32 s60, 5\n" \
"s_cbranch_scc1 Lsjb_%=\n" \
"ds_read_b32 v230, %[rd0]\n" \
"v_readlane_b32 s55, %[thi], 15\n" \
"v_lshrrev_b32_e32 v234, 5, v224\n" \
"s_waitcnt lgkmcnt(0)\n" \
"v_add_f32_dpp v230, v230, v230 row_ror:4 row_mask:0xf bank_mask:0xf\n" \
"s_nop 1\n" \
"v_add_f32_dpp v230, v230, v230 row_ror:8 row_mask:0xf bank_mask:0xf\n" \
"v_cvt_pk_bf16_f32 v230, v230, v230\n" \
"v_lshlrev_b32_e32 v230, 16, v230\n" \
"v_add_f32_e64 v230, s55, v230\n" \
"v_cvt_pk_bf16_f32 v230, v230, v230\n" \
"v_readlane_b32 s55, %[tlo], 15\n" \
"v_lshl_or_b32 v230, v230, 16, 1\n" \
"s_cmp_lg_u32 s52, 0\n" \
"s_cselect_b32 s55, 0x80000000, s55\n" \
"s_mov_b64 exec, 15\n" \
"buffer_store_dword v230, v234, %[srdz], s55 offen sc1\n" \
"s_mov_b64 exec, -1\n" \
"Lsjb_%=:\n" \
"v_mfma_f32_16x16x32_bf16 v[132:135], a[120:123], a[8:11], v[132:135]\n" \
"buffer_load_dwordx4 %[vd0], %[srdv], s48 offen offset:0 lds\n" \
"v_mfma_f32_16x16x32_bf16 v[132:135], a[124:127], a[12:15], v[132:135]\n" \
"v_exp_f32_e32 v159, v159\n" \
"v_mfma_f32_16x16x32_bf16 v[32:35], a[160:163], v[196:199], v[32:35]\n" \
"ds_read_b128 a[64:67], %[kb0] offset:32768\n" \
"v_mfma_f32_16x16x32_bf16 v[32:35], a[128:131], v[188:191], v[32:35]\n" \
"v_exp_f32_e64 v128, v128\n" \
"v_mfma_f32_16x16x32_bf16 v[36:39], a[164:167], v[196:199], v[36:39]\n" \
"v_cvt_pk_bf16_f32 v222, v156, v157\n" \
"v_cvt_pk_bf16_f32 v223, v158, v159\n" \
"v_mfma_f32_16x16x32_bf16 v[36:39], a[132:135], v[188:191], v[36:39]\n" \
"v_exp_f32_e64 v129, v129\n" \
"v_mfma_f32_16x16x32_bf16 v[40:43], a[168:171], v[196:199], v[40:43]\n" \
"ds_read_b128 a[68:71], v208 offset:32768\n" \
"v_mfma_f32_16x16x32_bf16 v[40:43], a[136:139], v[188:191], v[40:43]\n" \
"v_exp_f32_e64 v130, v130\n" \
"v_mfma_f32_16x16x32_bf16 v[44:47], a[172:175], v[196:199], v[44:47]\n" \
"ds_read_b128 a[72:75], v209 offset:32768\n" \
"v_mfma_f32_16x16x32_bf16 v[44:47], a[140:143], v[188:191], v[44:47]\n" \
"v_exp_f32_e64 v131, v131\n" \
"v_smfmac_f32_16x16x64_bf16 v[176:179], a[192:195], v[184:191], v228\n" \
"v_mfma_f32_16x16x32_bf16 v[136:139], a[96:99], a[16:19], v[164:167]\n" \
"v_exp_f32_e64 v132, v132\n" \
"v_mfma_f32_16x16x32_bf16 v[136:139], a[100:103], a[20:23], v[136:139]\n" \
"ds_read_b128 a[76:79], v210 offset:32768\n" \
"v_mfma_f32_16x16x32_bf16 v[136:139], a[104:107], a[24:27], v[136:139]\n" \
"v_exp_f32_e64 v133, v133\n" \
"v_mfma_f32_16x16x32_bf16 v[136:139], a[108:111], a[28:31], v[136:139]\n" \
"v_cvt_pk_bf16_f32 v192, v128, v129\n" \
"v_cvt_pk_bf16_f32 v193, v130, v131\n" \
"v_mfma_f32_16x16x32_bf16 v[140:143], a[112:115], a[16:19], v[164:167]\n" \
"v_exp_f32_e64 v134, v134\n" \
"v_mfma_f32_16x16x32_bf16 v[140:143], a[116:119], a[20:23], v[140:143]\n" \
"ds_read_b128 a[80:83], %[kb0] offset:36864\n" \
"v_mfma_f32_16x16x32_bf16 v[140:143], a[120:123], a[24:27], v[140:143]\n" \
"buffer_load_dwordx4 v215, %[srdv], s48 offen offset:1024 lds\n" \
"v_mfma_f32_16x16x32_bf16 v[140:143], a[124:127], a[28:31], v[140:143]\n" \
"v_exp_f32_e64 v135, v135\n" \
"v_mfma_f32_16x16x32_bf16 v[48:51], a[176:179], v[196:199], v[48:51]\n" \
"ds_read_b128 a[84:87], v208 offset:36864\n" \
"v_mfma_f32_16x16x32_bf16 v[48:51], a[144:147], v[188:191], v[48:51]\n" \
"v_exp_f32_e64 v136, v136\n" \
"v_mfma_f32_16x16x32_bf16 v[52:55], a[180:183], v[196:199], v[52:55]\n" \
"v_cvt_pk_bf16_f32 v194, v132, v133\n" \
"v_cvt_pk_bf16_f32 v195, v134, v135\n" \
"v_mfma_f32_16x16x32_bf16 v[52:55], a[148:151], v[188:191], v[52:55]\n" \
"v_exp_f32_e64 v137, v137\n" \
"v_mfma_f32_16x16x32_bf16 v[56:59], a[184:187], v[196:199], v[56:59]\n" \
"v_exp_f32_e64 v138, v138\n" \
"v_mfma_f32_16x16x32_bf16 v[56:59], a[152:155], v[188:191], v[56:59]\n" \
"ds_read_b128 a[88:91], v209 offset:36864\n" \
"v_mfma_f32_16x16x32_bf16 v[60:63], a[188:191], v[196:199], v[60:63]\n" \
"v_exp_f32_e64 v139, v139\n" \
"v_mfma_f32_16x16x32_bf16 v[60:63], a[156:159], v[188:191], v[60:63]\n" \
"s_cmp_lg_u32 %[wid], 0\n" \
"s_cbranch_scc1 LtB3_%=\n" \
"global_load_dword v237, v236, s[58:59]\n" \
"s_add_u32 s61, s48, 49152\n" \
"s_mov_b32 exec_hi, 0\n" \
"s_nop 0\n" \
"buffer_load_dword v237, v224, %[srdp], s61 offen\n" \
"s_mov_b32 exec_hi, -1\n" \
"s_nop 0\n" \
"LtB3_%=:\n" \
"v_mfma_f32_16x16x32_bf16 v[144:147], a[96:99], a[32:35], v[168:171]\n" \
"v_exp_f32_e64 v140, v140\n" \
"v_mfma_f32_16x16x32_bf16 v[144:147], a[100:103], a[36:39], v[144:147]\n" \
"ds_read_b128 a[92:95], v210 offset:36864\n" \
"v_mfma_f32_16x16x32_bf16 v[144:147], a[104:107], a[40:43], v[144:147]\n" \
"v_exp_f32_e64 v141, v141\n" \
"v_mfma_f32_16x16x32_bf16 v[144:147], a[108:111], a[44:47], v[144:147]\n" \
"v_cvt_pk_bf16_f32 v196, v136, v137\n" \
"v_cvt_pk_bf16_f32 v197, v138, v139\n" \
"v_mfma_f32_16x16x32_bf16 v[148:151], a[112:115], a[32:35], v[168:171]\n" \
"v_exp_f32_e64 v142, v142\n" \
"v_mfma_f32_16x16x32_bf16 v[148:151], a[116:119], a[36:39], v[148:151]\n" \
"v_mfma_f32_16x16x32_bf16 v[148:151], a[120:123], a[40:43], v[148:151]\n" \
"buffer_load_dwordx4 %[vd0], %[srdv], s48 offen offset:2048 lds\n" \
"v_mfma_f32_16x16x32_bf16 v[148:151], a[124:127], a[44:47], v[148:151]\n" \
"v_exp_f32_e64 v143, v143\n" \
"v_mfma_f32_16x16x32_bf16 v[96:99], a[160:163], v[204:207], v[96:99]\n" \
"ds_read_b64_tr_b16 a[160:161], %[vvb] offset:24576\n" \
"v_mfma_f32_16x16x32_bf16 v[96:99], a[128:131], v[220:223], v[96:99]\n" \
"v_exp_f32_e64 v144, v144\n" \
"v_mfma_f32_16x16x32_bf16 v[100:103], a[164:167], v[204:207], v[100:103]\n" \
"v_cvt_pk_bf16_f32 v198, v140, v141\n" \
"v_cvt_pk_bf16_f32 v199, v142, v143\n" \
"v_mfma_f32_16x16x32_bf16 v[100:103], a[132:135], v[220:223], v[100:103]\n" \
"v_exp_f32_e64 v145, v145\n" \
"v_mfma_f32_16x16x32_bf16 v[104:107], a[168:171], v[204:207], v[104:107]\n" \
"ds_read_b64_tr_b16 a[162:163], %[vvb] offset:28672\n" \
"ds_read_b64_tr_b16 a[164:165], v211 offset:24576\n" \
"v_mfma_f32_16x16x32_bf16 v[104:107], a[136:139], v[220:223], v[104:107]\n" \
"v_exp_f32_e64 v146, v146\n" \
"v_mfma_f32_16x16x32_bf16 v[108:111], a[172:175], v[204:207], v[108:111]\n" \
"ds_read_b64_tr_b16 a[166:167], v211 offset:28672\n" \
"v_mfma_f32_16x16x32_bf16 v[108:111], a[140:143], v[220:223], v[108:111]\n" \
"v_exp_f32_e64 v147, v147\n" \
"v_smfmac_f32_16x16x64_bf16 v[180:183], a[192:195], v[216:223], v228\n" \
"ds_read_b64_tr_b16 a[168:169], %[vvb] offset:25088\n" \
"buffer_store_dwordx4 a[196:199], v235, %[srdz], s62 offen offset:0 sc1\n" \
"v_mfma_f32_16x16x32_bf16 v[152:155], a[96:99], a[48:51], v[172:175]\n" \
"v_exp_f32_e64 v148, v148\n" \
"v_mfma_f32_16x16x32_bf16 v[152:155], a[100:103], a[52:55], v[152:155]\n" \
"ds_read_b64_tr_b16 a[170:171], %[vvb] offset:29184\n" \
"v_mfma_f32_16x16x32_bf16 v[152:155], a[104:107], a[56:59], v[152:155]\n" \
"v_exp_f32_e64 v149, v149\n" \
"v_mfma_f32_16x16x32_bf16 v[152:155], a[108:111], a[60:63], v[152:155]\n" \
"v_cvt_pk_bf16_f32 v200, v144, v145\n" \
"v_cvt_pk_bf16_f32 v201, v146, v147\n" \
"v_mfma_f32_16x16x32_bf16 v[156:159], a[112:115], a[48:51], v[172:175]\n" \
"v_exp_f32_e64 v150, v150\n" \
"v_mfma_f32_16x16x32_bf16 v[156:159], a[116:119], a[52:55], v[156:159]\n" \
"ds_read_b64_tr_b16 a[172:173], v211 offset:25088\n" \
"v_mfma_f32_16x16x32_bf16 v[156:159], a[120:123], a[56:59], v[156:159]\n" \
"buffer_load_dwordx4 v215, %[srdv], s48 offen offset:3072 lds\n" \
"v_mfma_f32_16x16x32_bf16 v[156:159], a[124:127], a[60:63], v[156:159]\n" \
"v_exp_f32_e64 v151, v151\n" \
"v_mfma_f32_16x16x32_bf16 v[112:115], a[176:179], v[204:207], v[112:115]\n" \
"ds_read_b64_tr_b16 a[174:175], v211 offset:29184\n" \
"ds_read_b64_tr_b16 a[176:177], %[vvb] offset:25600\n" \
"v_mfma_f32_16x16x32_bf16 v[112:115], a[144:147], v[220:223], v[112:115]\n" \
"v_exp_f32_e64 v152, v152\n" \
"v_mfma_f32_16x16x32_bf16 v[116:119], a[180:183], v[204:207], v[116:119]\n" \
"v_cvt_pk_bf16_f32 v202, v148, v149\n" \
"v_cvt_pk_bf16_f32 v203, v150, v151\n" \
"v_mfma_f32_16x16x32_bf16 v[116:119], a[148:151], v[220:223], v[116:119]\n" \
"v_exp_f32_e64 v153, v153\n" \
"v_mfma_f32_16x16x32_bf16 v[120:123], a[184:187], v[204:207], v[120:123]\n" \
"v_exp_f32_e64 v154, v154\n" \
"v_mfma_f32_16x16x32_bf16 v[120:123], a[152:155], v[220:223], v[120:123]\n" \
"ds_read_b64_tr_b16 a[178:179], %[vvb] offset:29696\n" \
"v_mfma_f32_16x16x32_bf16 v[124:127], a[188:191], v[204:207], v[124:127]\n" \
"v_exp_f32_e64 v155, v155\n" \
"v_mfma_f32_16x16x32_bf16 v[124:127], a[156:159], v[220:223], v[124:127]\n" \
"ds_read_b64_tr_b16 a[180:181], v211 offset:25600\n" \
"s_waitcnt lgkmcnt(13)\n" \
"v_mfma_f32_16x16x32_bf16 v[128:131], a[64:67], a[0:3], v[160:163]\n" \
"v_exp_f32_e32 v156, v156\n" \
"v_mfma_f32_16x16x32_bf16 v[128:131], a[68:71], a[4:7], v[128:131]\n" \
"ds_read_b64_tr_b16 a[182:183], v211 offset:29696\n" \
"s_add_u32 m0, %[m0b], 16384\n" \
"v_mfma_f32_16x16x32_bf16 v[128:131], a[72:75], a[8:11], v[128:131]\n" \
"v_exp_f32_e64 v157, v157\n" \
"v_mfma_f32_16x16x32_bf16 v[128:131], a[76:79], a[12:15], v[128:131]\n" \
"v_cvt_pk_bf16_f32 v204, v152, v153\n" \
"v_cvt_pk_bf16_f32 v205, v154, v155\n" \
"v_mfma_f32_16x16x32_bf16 v[132:135], a[80:83], a[0:3], v[160:163]\n" \
"v_exp_f32_e64 v158, v158\n" \
"v_mfma_f32_16x16x32_bf16 v[132:135], a[84:87], a[4:7], v[132:135]\n" \
"ds_read_b64_tr_b16 a[184:185], %[vvb] offset:26112\n" \
"s_waitcnt lgkmcnt(5)\n" \
"v_mfma_f32_16x16x32_bf16 v[132:135], a[88:91], a[8:11], v[132:135]\n" \
"buffer_load_dwordx4 %[kd0], %[srdk], s48 offen offset:0 lds\n" \
"v_mfma_f32_16x16x32_bf16 v[132:135], a[92:95], a[12:15], v[132:135]\n" \
"v_exp_f32_e32 v159, v159\n" \
"v_mfma_f32_16x16x32_bf16 v[64:67], a[128:131], v[216:219], v[64:67]\n" \
"ds_read_b64_tr_b16 a[186:187], %[vvb] offset:30208\n" \
"ds_read_b64_tr_b16 a[188:189], v211 offset:26112\n" \
"v_mfma_f32_16x16x32_bf16 v[64:67], a[160:163], v[200:203], v[64:67]\n" \
"v_exp_f32_e64 v128, v128\n" \
"v_mfma_f32_16x16x32_bf16 v[68:71], a[132:135], v[216:219], v[68:71]\n" \
"v_cvt_pk_bf16_f32 v206, v156, v157\n" \
"v_cvt_pk_bf16_f32 v207, v158, v159\n" \
"v_mfma_f32_16x16x32_bf16 v[68:71], a[164:167], v[200:203], v[68:71]\n" \
"v_exp_f32_e64 v129, v129\n" \
"v_mfma_f32_16x16x32_bf16 v[72:75], a[136:139], v[216:219], v[72:75]\n" \
"ds_read_b64_tr_b16 a[190:191], v211 offset:30208\n" \
"ds_read_b64_tr_b16 a[128:129], %[vvb] offset:32768\n" \
"v_mfma_f32_16x16x32_bf16 v[72:75], a[168:171], v[200:203], v[72:75]\n" \
"v_exp_f32_e64 v130, v130\n" \
"v_mfma_f32_16x16x32_bf16 v[76:79], a[140:143], v[216:219], v[76:79]\n" \
"ds_read_b64_tr_b16 a[130:131], %[vvb] offset:36864\n" \
"ds_read_b64_tr_b16 a[132:133], v211 offset:32768\n" \
"v_mfma_f32_16x16x32_bf16 v[76:79], a[172:175], v[200:203], v[76:79]\n" \
"v_exp_f32_e64 v131, v131\n" \
"v_smfmac_f32_16x16x64_bf16 v[176:179], a[192:195], v[192:199], v228\n" \
"ds_read_b64_tr_b16 a[134:135], v211 offset:36864\n" \
"ds_read_b64_tr_b16 a[136:137], %[vvb] offset:33280\n" \
"v_mfma_f32_16x16x32_bf16 v[136:139], a[64:67], a[16:19], v[164:167]\n" \
"v_exp_f32_e64 v132, v132\n" \
"v_mfma_f32_16x16x32_bf16 v[136:139], a[68:71], a[20:23], v[136:139]\n" \
"ds_read_b64_tr_b16 a[138:139], %[vvb] offset:37376\n" \
"ds_read_b64_tr_b16 a[140:141], v211 offset:33280\n" \
"v_mfma_f32_16x16x32_bf16 v[136:139], a[72:75], a[24:27], v[136:139]\n" \
"v_exp_f32_e64 v133, v133\n" \
"v_mfma_f32_16x16x32_bf16 v[136:139], a[76:79], a[28:31], v[136:139]\n" \
"v_cvt_pk_bf16_f32 v184, v128, v129\n" \
"v_cvt_pk_bf16_f32 v185, v130, v131\n" \
"v_mfma_f32_16x16x32_bf16 v[140:143], a[80:83], a[16:19], v[164:167]\n" \
"v_exp_f32_e64 v134, v134\n" \
"v_mfma_f32_16x16x32_bf16 v[140:143], a[84:87], a[20:23], v[140:143]\n" \
"ds_read_b64_tr_b16 a[142:143], v211 offset:37376\n" \
"s_waitcnt lgkmcnt(8)\n" \
"v_mfma_f32_16x16x32_bf16 v[140:143], a[88:91], a[24:27], v[140:143]\n" \
"buffer_load_dwordx4 v212, %[srdk], s48 offen offset:1024 lds\n" \
"v_mfma_f32_16x16x32_bf16 v[140:143], a[92:95], a[28:31], v[140:143]\n" \
"v_exp_f32_e32 v135, v135\n" \
"v_mfma_f32_16x16x32_bf16 v[80:83], a[144:147], v[216:219], v[80:83]\n" \
".if . - adaln_body != 24576\n" \
".error \"size model\"\n" \
".endif\n" \
"Lk5_%=:\n" \
"s_cbranch_execz adaln_kr\n" \
"s_nop 0\n" \
"ds_read_b64_tr_b16 a[144:145], %[vvb] offset:33792\n" \
"ds_read_b64_tr_b16 a[146:147], %[vvb] offset:37888\n" \
"v_mfma_f32_16x16x32_bf16 v[80:83], a[176:179], v[200:203], v[80:83]\n" \
"v_exp_f32_e64 v136, v136\n" \
"v_mfma_f32_16x16x32_bf16 v[84:87], a[148:151], v[216:219], v[84:87]\n" \
"v_cvt_pk_bf16_f32 v186, v132, v133\n" \
"v_cvt_pk_bf16_f32 v187, v134, v135\n" \
"v_mfma_f32_16x16x32_bf16 v[84:87], a[180:183], v[200:203], v[84:87]\n" \
"v_exp_f32_e64 v137, v137\n" \
"v_mfma_f32_16x16x32_bf16 v[88:91], a[152:155], v[216:219], v[88:91]\n" \
"v_exp_f32_e64 v138, v138\n" \
"v_mfma_f32_16x16x32_bf16 v[88:91], a[184:187], v[200:203], v[88:91]\n" \
"ds_read_b64_tr_b16 a[148:149], v211 offset:33792\n" \
"ds_read_b64_tr_b16 a[150:151], v211 offset:37888\n" \
"v_mfma_f32_16x16x32_bf16 v[92:95], a[156:159], v[216:219], v[92:95]\n" \
"v_exp_f32_e64 v139, v139\n" \
"v_mfma_f32_16x16x32_bf16 v[92:95], a[188:191], v[200:203], v[92:95]\n" \
"ds_read_b64_tr_b16 a[152:153], %[vvb] offset:34304\n" \
"buffer_store_dwordx4 a[196:199], v235, %[srdz], s63 offen offset:1024 sc1\n" \
"v_mfma_f32_16x16x32_bf16 v[144:147], a[64:67], a[32:35], v[168:171]\n" \
"v_exp_f32_e64 v140, v140\n" \
"v_mfma_f32_16x16x32_bf16 v[144:147], a[68:71], a[36:39], v[144:147]\n" \
"ds_read_b64_tr_b16 a[154:155], %[vvb] offset:38400\n" \
"ds_read_b64_tr_b16 a[156:157], v211 offset:34304\n" \
"v_mfma_f32_16x16x32_bf16 v[144:147], a[72:75], a[40:43], v[144:147]\n" \
"v_exp_f32_e64 v141, v141\n" \
"v_mfma_f32_16x16x32_bf16 v[144:147], a[76:79], a[44:47], v[144:147]\n" \
"v_cvt_pk_bf16_f32 v188, v136, v137\n" \
"v_cvt_pk_bf16_f32 v189, v138, v139\n" \
"v_mfma_f32_16x16x32_bf16 v[148:151], a[80:83], a[32:35], v[168:171]\n" \
"v_exp_f32_e64 v142, v142\n" \
"v_mfma_f32_16x16x32_bf16 v[148:151], a[84:87], a[36:39], v[148:151]\n" \
"ds_read_b64_tr_b16 a[158:159], v211 offset:38400\n" \
"s_waitcnt lgkmcnt(8)\n" \
"v_mfma_f32_16x16x32_bf16 v[148:151], a[88:91], a[40:43], v[148:151]\n" \
"buffer_load_dwordx4 v213, %[srdk], s48 offen offset:2048 lds\n" \
"v_mfma_f32_16x16x32_bf16 v[148:151], a[92:95], a[44:47], v[148:151]\n" \
"v_exp_f32_e32 v143, v143\n" \
"v_mfma_f32_16x16x32_bf16 v[0:3], a[160:163], v[192:195], v[0:3]\n" \
"ds_read_b128 a[96:99], %[kb0] offset:40960\n" \
"v_mfma_f32_16x16x32_bf16 v[0:3], a[128:131], v[184:187], v[0:3]\n" \
"v_exp_f32_e64 v144, v144\n" \
"v_mfma_f32_16x16x32_bf16 v[4:7], a[164:167], v[192:195], v[4:7]\n" \
"v_cvt_pk_bf16_f32 v190, v140, v141\n" \
"v_cvt_pk_bf16_f32 v191, v142, v143\n" \
"v_mfma_f32_16x16x32_bf16 v[4:7], a[132:135], v[184:187], v[4:7]\n" \
"v_exp_f32_e64 v145, v145\n" \
"v_mfma_f32_16x16x32_bf16 v[8:11], a[168:171], v[192:195], v[8:11]\n" \
"ds_read_b128 a[100:103], v208 offset:40960\n" \
"v_mfma_f32_16x16x32_bf16 v[8:11], a[136:139], v[184:187], v[8:11]\n" \
"v_exp_f32_e64 v146, v146\n" \
"v_mfma_f32_16x16x32_bf16 v[12:15], a[172:175], v[192:195], v[12:15]\n" \
"ds_read_b128 a[104:107], v209 offset:40960\n" \
"v_mfma_f32_16x16x32_bf16 v[12:15], a[140:143], v[184:187], v[12:15]\n" \
"v_exp_f32_e64 v147, v147\n" \
"v_smfmac_f32_16x16x64_bf16 v[180:183], a[192:195], v[200:207], v228\n" \
"ds_read_b128 a[108:111], v210 offset:40960\n" \
"v_mfma_f32_16x16x32_bf16 v[152:155], a[64:67], a[48:51], v[172:175]\n" \
"v_exp_f32_e64 v148, v148\n" \
"v_mfma_f32_16x16x32_bf16 v[152:155], a[68:71], a[52:55], v[152:155]\n" \
"ds_read_b128 a[112:115], %[kb0] offset:45056\n" \
"v_mfma_f32_16x16x32_bf16 v[152:155], a[72:75], a[56:59], v[152:155]\n" \
"v_exp_f32_e64 v149, v149\n" \
"v_mfma_f32_16x16x32_bf16 v[152:155], a[76:79], a[60:63], v[152:155]\n" \
"v_cvt_pk_bf16_f32 v216, v144, v145\n" \
"v_cvt_pk_bf16_f32 v217, v146, v147\n" \
"v_mfma_f32_16x16x32_bf16 v[156:159], a[80:83], a[48:51], v[172:175]\n" \
"v_exp_f32_e64 v150, v150\n" \
"v_mfma_f32_16x16x32_bf16 v[156:159], a[84:87], a[52:55], v[156:159]\n" \
"ds_read_b128 a[116:119], v208 offset:45056\n" \
"s_waitcnt lgkmcnt(6)\n" \
"v_mfma_f32_16x16x32_bf16 v[156:159], a[88:91], a[56:59], v[156:159]\n" \
"buffer_load_dwordx4 v214, %[srdk], s48 offen offset:3072 lds\n" \
"v_mfma_f32_16x16x32_bf16 v[156:159], a[92:95], a[60:63], v[156:159]\n" \
"v_exp_f32_e32 v151, v151\n" \
"v_mfma_f32_16x16x32_bf16 v[16:19], a[176:179], v[192:195], v[16:19]\n" \
"ds_read_b128 a[120:123], v209 offset:45056\n" \
"v_mfma_f32_16x16x32_bf16 v[16:19], a[144:147], v[184:187], v[16:19]\n" \
"v_exp_f32_e64 v152, v152\n" \
"v_mfma_f32_16x16x32_bf16 v[20:23], a[180:183], v[192:195], v[20:23]\n" \
"v_cvt_pk_bf16_f32 v218, v148, v149\n" \
"v_cvt_pk_bf16_f32 v219, v150, v151\n" \
"v_mfma_f32_16x16x32_bf16 v[20:23], a[148:151], v[184:187], v[20:23]\n" \
"v_exp_f32_e64 v153, v153\n" \
"v_mfma_f32_16x16x32_bf16 v[24:27], a[184:187], v[192:195], v[24:27]\n" \
"v_exp_f32_e64 v154, v154\n" \
"v_mfma_f32_16x16x32_bf16 v[24:27], a[152:155], v[184:187], v[24:27]\n" \
"ds_read_b128 a[124:127], v210 offset:45056\n" \
"s_add_u32 s48, s48, 0x4000\n" \
"v_mfma_f32_16x16x32_bf16 v[28:31], a[188:191], v[192:195], v[28:31]\n" \
"v_exp_f32_e64 v155, v155\n" \
"v_mfma_f32_16x16x32_bf16 v[28:31], a[156:159], v[184:187], v[28:31]\n" \
"s_waitcnt vmcnt(16) lgkmcnt(2)\n" \
"s_barrier\n" \
"v_mfma_f32_16x16x32_bf16 v[128:131], a[96:99], a[0:3], v[160:163]\n" \
"v_exp_f32_e64 v156, v156\n" \
"v_mfma_f32_16x16x32_bf16 v[128:131], a[100:103], a[4:7], v[128:131]\n" \
"s_add_u32 m0, %[m0b], 81920\n" \
"v_mfma_f32_16x16x32_bf16 v[128:131], a[104:107], a[8:11], v[128:131]\n" \
"v_exp_f32_e64 v157, v157\n" \
"v_mfma_f32_16x16x32_bf16 v[128:131], a[108:111], a[12:15], v[128:131]\n" \
"v_cvt_pk_bf16_f32 v220, v152, v153\n" \
"v_cvt_pk_bf16_f32 v221, v154, v155\n" \
"v_mfma_f32_16x16x32_bf16 v[132:135], a[112:115], a[0:3], v[160:163]\n" \
"v_exp_f32_e64 v158, v158\n" \
"v_mfma_f32_16x16x32_bf16 v[132:135], a[116:119], a[4:7], v[132:135]\n" \
"s_waitcnt lgkmcnt(0)\n" \
"v_mfma_f32_16x16x32_bf16 v[132:135], a[120:123], a[8:11], v[132:135]\n" \
"buffer_load_dwordx4 %[vd0], %[srdv], s48 offen offset:0 lds\n" \
"v_mfma_f32_16x16x32_bf16 v[132:135], a[124:127], a[12:15], v[132:135]\n" \
"v_exp_f32_e32 v159, v159\n" \
"v_mfma_f32_16x16x32_bf16 v[32:35], a[160:163], v[196:199], v[32:35]\n" \
"ds_read_b128 a[64:67], %[kb0] offset:49152\n" \
"v_mfma_f32_16x16x32_bf16 v[32:35], a[128:131], v[188:191], v[32:35]\n" \
"v_exp_f32_e64 v128, v128\n" \
"v_mfma_f32_16x16x32_bf16 v[36:39], a[164:167], v[196:199], v[36:39]\n" \
"v_cvt_pk_bf16_f32 v222, v156, v157\n" \
"v_cvt_pk_bf16_f32 v223, v158, v159\n" \
"v_mfma_f32_16x16x32_bf16 v[36:39], a[132:135], v[188:191], v[36:39]\n" \
"v_exp_f32_e64 v129, v129\n" \
"v_mfma_f32_16x16x32_bf16 v[40:43], a[168:171], v[196:199], v[40:43]\n" \
"ds_read_b128 a[68:71], v208 offset:49152\n" \
"v_mfma_f32_16x16x32_bf16 v[40:43], a[136:139], v[188:191], v[40:43]\n" \
"v_exp_f32_e64 v130, v130\n" \
"v_mfma_f32_16x16x32_bf16 v[44:47], a[172:175], v[196:199], v[44:47]\n" \
"ds_read_b128 a[72:75], v209 offset:49152\n" \
"v_mfma_f32_16x16x32_bf16 v[44:47], a[140:143], v[188:191], v[44:47]\n" \
"v_exp_f32_e64 v131, v131\n" \
"v_smfmac_f32_16x16x64_bf16 v[176:179], a[192:195], v[184:191], v228\n" \
"v_mfma_f32_16x16x32_bf16 v[136:139], a[96:99], a[16:19], v[164:167]\n" \
"v_exp_f32_e64 v132, v132\n" \
"v_mfma_f32_16x16x32_bf16 v[136:139], a[100:103], a[20:23], v[136:139]\n" \
"ds_read_b128 a[76:79], v210 offset:49152\n" \
"v_mfma_f32_16x16x32_bf16 v[136:139], a[104:107], a[24:27], v[136:139]\n" \
"v_exp_f32_e64 v133, v133\n" \
"v_mfma_f32_16x16x32_bf16 v[136:139], a[108:111], a[28:31], v[136:139]\n" \
"v_cvt_pk_bf16_f32 v192, v128, v129\n" \
"v_cvt_pk_bf16_f32 v193, v130, v131\n" \
"v_mfma_f32_16x16x32_bf16 v[140:143], a[112:115], a[16:19], v[164:167]\n" \
"v_exp_f32_e64 v134, v134\n" \
"v_mfma_f32_16x16x32_bf16 v[140:143], a[116:119], a[20:23], v[140:143]\n" \
"ds_read_b128 a[80:83], %[kb0] offset:53248\n" \
"v_mfma_f32_16x16x32_bf16 v[140:143], a[120:123], a[24:27], v[140:143]\n" \
"buffer_load_dwordx4 v215, %[srdv], s48 offen offset:1024 lds\n" \
"v_mfma_f32_16x16x32_bf16 v[140:143], a[124:127], a[28:31], v[140:143]\n" \
"v_exp_f32_e64 v135, v135\n" \
"v_mfma_f32_16x16x32_bf16 v[48:51], a[176:179], v[196:199], v[48:51]\n" \
"ds_read_b128 a[84:87], v208 offset:53248\n" \
"v_mfma_f32_16x16x32_bf16 v[48:51], a[144:147], v[188:191], v[48:51]\n" \
"v_exp_f32_e64 v136, v136\n" \
"v_mfma_f32_16x16x32_bf16 v[52:55], a[180:183], v[196:199], v[52:55]\n" \
"v_cvt_pk_bf16_f32 v194, v132, v133\n" \
"v_cvt_pk_bf16_f32 v195, v134, v135\n" \
"v_mfma_f32_16x16x32_bf16 v[52:55], a[148:151], v[188:191], v[52:55]\n" \
"v_exp_f32_e64 v137, v137\n" \
"v_mfma_f32_16x16x32_bf16 v[56:59], a[184:187], v[196:199], v[56:59]\n" \
"v_exp_f32_e64 v138, v138\n" \
"v_mfma_f32_16x16x32_bf16 v[56:59], a[152:155], v[188:191], v[56:59]\n" \
"ds_read_b128 a[88:91], v209 offset:53248\n" \
"v_mfma_f32_16x16x32_bf16 v[60:63], a[188:191], v[196:199], v[60:63]\n" \
"v_exp_f32_e64 v139, v139\n" \
"v_mfma_f32_16x16x32_bf16 v[60:63], a[156:159], v[188:191], v[60:63]\n" \
"s_cmp_lg_u32 %[wid], 1\n" \
"s_cbranch_scc1 LtB5_%=\n" \
"global_load_dword v237, v236, s[58:59]\n" \
"s_add_u32 s61, s48, 49152\n" \
"s_mov_b32 exec_hi, 0\n" \
"s_nop 0\n" \
"buffer_load_dword v237, v224, %[srdp], s61 offen\n" \
"s_mov_b32 exec_hi, -1\n" \
"s_nop 0\n" \
"LtB5_%=:\n" \
"v_mfma_f32_16x16x32_bf16 v[144:147], a[96:99], a[32:35], v[168:171]\n" \
"v_exp_f32_e64 v140, v140\n" \
"v_mfma_f32_16x16x32_bf16 v[144:147], a[100:103], a[36:39], v[144:147]\n" \
"ds_read_b128 a[92:95], v210 offset:53248\n" \
"v_mfma_f32_16x16x32_bf16 v[144:147], a[104:107], a[40:43], v[144:147]\n" \
"v_exp_f32_e64 v141, v141\n" \
"v_mfma_f32_16x16x32_bf16 v[144:147], a[108:111], a[44:47], v[144:147]\n" \
"v_cvt_pk_bf16_f32 v196, v136, v137\n" \
"v_cvt_pk_bf16_f32 v197, v138, v139\n" \
"v_mfma_f32_16x16x32_bf16 v[148:151], a[112:115], a[32:35], v[168:171]\n" \
"v_exp_f32_e64 v142, v142\n" \
"v_mfma_f32_16x16x32_bf16 v[148:151], a[116:119], a[36:39], v[148:151]\n" \
"v_mfma_f32_16x16x32_bf16 v[148:151], a[120:123], a[40:43], v[148:151]\n" \
"buffer_load_dwordx4 %[vd0], %[srdv], s48 offen offset:2048 lds\n" \
"v_mfma_f32_16x16x32_bf16 v[148:151], a[124:127], a[44:47], v[148:151]\n" \
"v_exp_f32_e64 v143, v143\n" \
"v_mfma_f32_16x16x32_bf16 v[96:99], a[160:163], v[204:207], v[96:99]\n" \
"ds_read_b64_tr_b16 a[160:161], %[vvb] offset:40960\n" \
"v_mfma_f32_16x16x32_bf16 v[96:99], a[128:131], v[220:223], v[96:99]\n" \
"v_exp_f32_e64 v144, v144\n" \
"v_mfma_f32_16x16x32_bf16 v[100:103], a[164:167], v[204:207], v[100:103]\n" \
"v_cvt_pk_bf16_f32 v198, v140, v141\n" \
"v_cvt_pk_bf16_f32 v199, v142, v143\n" \
"v_mfma_f32_16x16x32_bf16 v[100:103], a[132:135], v[220:223], v[100:103]\n" \
"v_exp_f32_e64 v145, v145\n" \
"v_mfma_f32_16x16x32_bf16 v[104:107], a[168:171], v[204:207], v[104:107]\n" \
"ds_read_b64_tr_b16 a[162:163], %[vvb] offset:45056\n" \
"ds_read_b64_tr_b16 a[164:165], v211 offset:40960\n" \
"v_mfma_f32_16x16x32_bf16 v[104:107], a[136:139], v[220:223], v[104:107]\n" \
"v_exp_f32_e64 v146, v146\n" \
"v_mfma_f32_16x16x32_bf16 v[108:111], a[172:175], v[204:207], v[108:111]\n" \
"ds_read_b64_tr_b16 a[166:167], v211 offset:45056\n" \
"v_mfma_f32_16x16x32_bf16 v[108:111], a[140:143], v[220:223], v[108:111]\n" \
"v_exp_f32_e64 v147, v147\n" \
"v_smfmac_f32_16x16x64_bf16 v[180:183], a[192:195], v[216:223], v228\n" \
"ds_read_b64_tr_b16 a[168:169], %[vvb] offset:41472\n" \
"v_mfma_f32_16x16x32_bf16 v[152:155], a[96:99], a[48:51], v[172:175]\n" \
"v_exp_f32_e64 v148, v148\n" \
"v_mfma_f32_16x16x32_bf16 v[152:155], a[100:103], a[52:55], v[152:155]\n" \
"ds_read_b64_tr_b16 a[170:171], %[vvb] offset:45568\n" \
"v_mfma_f32_16x16x32_bf16 v[152:155], a[104:107], a[56:59], v[152:155]\n" \
"v_exp_f32_e64 v149, v149\n" \
"v_mfma_f32_16x16x32_bf16 v[152:155], a[108:111], a[60:63], v[152:155]\n" \
"v_cvt_pk_bf16_f32 v200, v144, v145\n" \
"v_cvt_pk_bf16_f32 v201, v146, v147\n" \
"v_mfma_f32_16x16x32_bf16 v[156:159], a[112:115], a[48:51], v[172:175]\n" \
"v_exp_f32_e64 v150, v150\n" \
"v_mfma_f32_16x16x32_bf16 v[156:159], a[116:119], a[52:55], v[156:159]\n" \
"ds_read_b64_tr_b16 a[172:173], v211 offset:41472\n" \
"v_mfma_f32_16x16x32_bf16 v[156:159], a[120:123], a[56:59], v[156:159]\n" \
"buffer_load_dwordx4 v215, %[srdv], s48 offen offset:3072 lds\n" \
"v_mfma_f32_16x16x32_bf16 v[156:159], a[124:127], a[60:63], v[156:159]\n" \
"v_exp_f32_e64 v151, v151\n" \
"v_mfma_f32_16x16x32_bf16 v[112:115], a[176:179], v[204:207], v[112:115]\n" \
"ds_read_b64_tr_b16 a[174:175], v211 offset:45568\n" \
"ds_read_b64_tr_b16 a[176:177], %[vvb] offset:41984\n" \
"v_mfma_f32_16x16x32_bf16 v[112:115], a[144:147], v[220:223], v[112:115]\n" \
"v_exp_f32_e64 v152, v152\n" \
"v_mfma_f32_16x16x32_bf16 v[116:119], a[180:183], v[204:207], v[116:119]\n" \
"v_cvt_pk_bf16_f32 v202, v148, v149\n" \
"v_cvt_pk_bf16_f32 v203, v150, v151\n" \
"v_mfma_f32_16x16x32_bf16 v[116:119], a[148:151], v[220:223], v[116:119]\n" \
"v_exp_f32_e64 v153, v153\n" \
"v_mfma_f32_16x16x32_bf16 v[120:123], a[184:187], v[204:207], v[120:123]\n" \
"v_exp_f32_e64 v154, v154\n" \
"v_mfma_f32_16x16x32_bf16 v[120:123], a[152:155], v[220:223], v[120:123]\n" \
"ds_read_b64_tr_b16 a[178:179], %[vvb] offset:46080\n" \
"v_mfma_f32_16x16x32_bf16 v[124:127], a[188:191], v[204:207], v[124:127]\n" \
"v_exp_f32_e64 v155, v155\n" \
"v_mfma_f32_16x16x32_bf16 v[124:127], a[156:159], v[220:223], v[124:127]\n" \
"ds_read_b64_tr_b16 a[180:181], v211 offset:41984\n" \
"s_waitcnt lgkmcnt(13)\n" \
"v_mfma_f32_16x16x32_bf16 v[128:131], a[64:67], a[0:3], v[160:163]\n" \
"v_exp_f32_e32 v156, v156\n" \
"v_mfma_f32_16x16x32_bf16 v[128:131], a[68:71], a[4:7], v[128:131]\n" \
"ds_read_b64_tr_b16 a[182:183], v211 offset:46080\n" \
"s_add_u32 m0, %[m0b], 32768\n" \
"v_mfma_f32_16x16x32_bf16 v[128:131], a[72:75], a[8:11], v[128:131]\n" \
"v_exp_f32_e64 v157, v157\n" \
"v_mfma_f32_16x16x32_bf16 v[128:131], a[76:79], a[12:15], v[128:131]\n" \
"v_cvt_pk_bf16_f32 v204, v152, v153\n" \
"v_cvt_pk_bf16_f32 v205, v154, v155\n" \
"v_mfma_f32_16x16x32_bf16 v[132:135], a[80:83], a[0:3], v[160:163]\n" \
"v_exp_f32_e64 v158, v158\n" \
"v_mfma_f32_16x16x32_bf16 v[132:135], a[84:87], a[4:7], v[132:135]\n" \
"ds_read_b64_tr_b16 a[184:185], %[vvb] offset:42496\n" \
"s_waitcnt lgkmcnt(5)\n" \
"v_mfma_f32_16x16x32_bf16 v[132:135], a[88:91], a[8:11], v[132:135]\n" \
"buffer_load_dwordx4 %[kd0], %[srdk], s48 offen offset:0 lds\n" \
"v_mfma_f32_16x16x32_bf16 v[132:135], a[92:95], a[12:15], v[132:135]\n" \
"v_exp_f32_e32 v159, v159\n" \
"v_mfma_f32_16x16x32_bf16 v[64:67], a[128:131], v[216:219], v[64:67]\n" \
"ds_read_b64_tr_b16 a[186:187], %[vvb] offset:46592\n" \
"ds_read_b64_tr_b16 a[188:189], v211 offset:42496\n" \
"v_mfma_f32_16x16x32_bf16 v[64:67], a[160:163], v[200:203], v[64:67]\n" \
"v_exp_f32_e64 v128, v128\n" \
"v_mfma_f32_16x16x32_bf16 v[68:71], a[132:135], v[216:219], v[68:71]\n" \
"v_cvt_pk_bf16_f32 v206, v156, v157\n" \
"v_cvt_pk_bf16_f32 v207, v158, v159\n" \
"v_mfma_f32_16x16x32_bf16 v[68:71], a[164:167], v[200:203], v[68:71]\n" \
"v_exp_f32_e64 v129, v129\n" \
"v_mfma_f32_16x16x32_bf16 v[72:75], a[136:139], v[216:219], v[72:75]\n" \
"ds_read_b64_tr_b16 a[190:191], v211 offset:46592\n" \
"ds_read_b64_tr_b16 a[128:129], %[vvb] offset:49152\n" \
"v_mfma_f32_16x16x32_bf16 v[72:75], a[168:171], v[200:203], v[72:75]\n" \
"v_exp_f32_e64 v130, v130\n" \
"v_mfma_f32_16x16x32_bf16 v[76:79], a[140:143], v[216:219], v[76:79]\n" \
"ds_read_b64_tr_b16 a[130:131], %[vvb] offset:53248\n" \
"ds_read_b64_tr_b16 a[132:133], v211 offset:49152\n" \
"v_mfma_f32_16x16x32_bf16 v[76:79], a[172:175], v[200:203], v[76:79]\n" \
"v_exp_f32_e64 v131, v131\n" \
"v_smfmac_f32_16x16x64_bf16 v[176:179], a[192:195], v[192:199], v228\n" \
"ds_read_b64_tr_b16 a[134:135], v211 offset:53248\n" \
"ds_read_b64_tr_b16 a[136:137], %[vvb] offset:49664\n" \
"buffer_store_dwordx4 a[196:199], v235, %[srdz], s62 offen offset:1024 sc1\n" \
"v_mfma_f32_16x16x32_bf16 v[136:139], a[64:67], a[16:19], v[164:167]\n" \
"v_exp_f32_e64 v132, v132\n" \
"v_mfma_f32_16x16x32_bf16 v[136:139], a[68:71], a[20:23], v[136:139]\n" \
"ds_read_b64_tr_b16 a[138:139], %[vvb] offset:53760\n" \
"ds_read_b64_tr_b16 a[140:141], v211 offset:49664\n" \
"v_mfma_f32_16x16x32_bf16 v[136:139], a[72:75], a[24:27], v[136:139]\n" \
"v_exp_f32_e64 v133, v133\n" \
"v_mfma_f32_16x16x32_bf16 v[136:139], a[76:79], a[28:31], v[136:139]\n" \
"v_cvt_pk_bf16_f32 v184, v128, v129\n" \
"v_cvt_pk_bf16_f32 v185, v130, v131\n" \
"v_mfma_f32_16x16x32_bf16 v[140:143], a[80:83], a[16:19], v[164:167]\n" \
"v_exp_f32_e64 v134, v134\n" \
"v_mfma_f32_16x16x32_bf16 v[140:143], a[84:87], a[20:23], v[140:143]\n" \
"ds_read_b64_tr_b16 a[142:143], v211 offset:53760\n" \
"s_waitcnt lgkmcnt(8)\n" \
"v_mfma_f32_16x16x32_bf16 v[140:143], a[88:91], a[24:27], v[140:143]\n" \
"buffer_load_dwordx4 v212, %[srdk], s48 offen offset:1024 lds\n" \
"v_mfma_f32_16x16x32_bf16 v[140:143], a[92:95], a[28:31], v[140:143]\n" \
"v_exp_f32_e32 v135, v135\n" \
"v_mfma_f32_16x16x32_bf16 v[80:83], a[144:147], v[216:219], v[80:83]\n" \
"ds_read_b64_tr_b16 a[144:145], %[vvb] offset:50176\n" \
"ds_read_b64_tr_b16 a[146:147], %[vvb] offset:54272\n" \
"v_mfma_f32_16x16x32_bf16 v[80:83], a[176:179], v[200:203], v[80:83]\n" \
"v_exp_f32_e64 v136, v136\n" \
"v_mfma_f32_16x16x32_bf16 v[84:87], a[148:151], v[216:219], v[84:87]\n" \
"v_cvt_pk_bf16_f32 v186, v132, v133\n" \
"v_cvt_pk_bf16_f32 v187, v134, v135\n" \
"v_mfma_f32_16x16x32_bf16 v[84:87], a[180:183], v[200:203], v[84:87]\n" \
"v_exp_f32_e64 v137, v137\n" \
"v_mfma_f32_16x16x32_bf16 v[88:91], a[152:155], v[216:219], v[88:91]\n" \
"v_exp_f32_e64 v138, v138\n" \
"v_mfma_f32_16x16x32_bf16 v[88:91], a[184:187], v[200:203], v[88:91]\n" \
"ds_read_b64_tr_b16 a[148:149], v211 offset:50176\n" \
"ds_read_b64_tr_b16 a[150:151], v211 offset:54272\n" \
"v_mfma_f32_16x16x32_bf16 v[92:95], a[156:159], v[216:219], v[92:95]\n" \
"v_exp_f32_e64 v139, v139\n" \
"v_mfma_f32_16x16x32_bf16 v[92:95], a[188:191], v[200:203], v[92:95]\n" \
"ds_read_b64_tr_b16 a[152:153], %[vvb] offset:50688\n" \
"global_load_dword v237, %[vct], %[pct]\n" \
"v_mfma_f32_16x16x32_bf16 v[144:147], a[64:67], a[32:35], v[168:171]\n" \
"v_exp_f32_e64 v140, v140\n" \
"v_mfma_f32_16x16x32_bf16 v[144:147], a[68:71], a[36:39], v[144:147]\n" \
"ds_read_b64_tr_b16 a[154:155], %[vvb] offset:54784\n" \
"ds_read_b64_tr_b16 a[156:157], v211 offset:50688\n" \
"v_mfma_f32_16x16x32_bf16 v[144:147], a[72:75], a[40:43], v[144:147]\n" \
"v_exp_f32_e64 v141, v141\n" \
"v_mfma_f32_16x16x32_bf16 v[144:147], a[76:79], a[44:47], v[144:147]\n" \
"v_cvt_pk_bf16_f32 v188, v136, v137\n" \
"v_cvt_pk_bf16_f32 v189, v138, v139\n" \
"v_mfma_f32_16x16x32_bf16 v[148:151], a[80:83], a[32:35], v[168:171]\n" \
"v_exp_f32_e64 v142, v142\n" \
"v_mfma_f32_16x16x32_bf16 v[148:151], a[84:87], a[36:39], v[148:151]\n" \
"ds_read_b64_tr_b16 a[158:159], v211 offset:54784\n" \
"s_waitcnt lgkmcnt(8)\n" \
"v_mfma_f32_16x16x32_bf16 v[148:151], a[88:91], a[40:43], v[148:151]\n" \
"buffer_load_dwordx4 v213, %[srdk], s48 offen offset:2048 lds\n" \
"v_mfma_f32_16x16x32_bf16 v[148:151], a[92:95], a[44:47], v[148:151]\n" \
"v_exp_f32_e32 v143, v143\n" \
"v_mfma_f32_16x16x32_bf16 v[0:3], a[160:163], v[192:195], v[0:3]\n" \
"ds_read_b128 a[96:99], %[kb0] offset:57344\n" \
"v_mfma_f32_16x16x32_bf16 v[0:3], a[128:131], v[184:187], v[0:3]\n" \
"v_exp_f32_e64 v144, v144\n" \
"v_mfma_f32_16x16x32_bf16 v[4:7], a[164:167], v[192:195], v[4:7]\n" \
"v_cvt_pk_bf16_f32 v190, v140, v141\n" \
"v_cvt_pk_bf16_f32 v191, v142, v143\n" \
"v_mfma_f32_16x16x32_bf16 v[4:7], a[132:135], v[184:187], v[4:7]\n" \
"v_exp_f32_e64 v145, v145\n" \
"v_mfma_f32_16x16x32_bf16 v[8:11], a[168:171], v[192:195], v[8:11]\n" \
"ds_read_b128 a[100:103], v208 offset:57344\n" \
"v_mfma_f32_16x16x32_bf16 v[8:11], a[136:139], v[184:187], v[8:11]\n" \
"v_exp_f32_e64 v146, v146\n" \
"v_mfma_f32_16x16x32_bf16 v[12:15], a[172:175], v[192:195], v[12:15]\n" \
"ds_read_b128 a[104:107], v209 offset:57344\n" \
"v_mfma_f32_16x16x32_bf16 v[12:15], a[140:143], v[184:187], v[12:15]\n" \
"v_exp_f32_e64 v147, v147\n" \
"v_smfmac_f32_16x16x64_bf16 v[180:183], a[192:195], v[200:207], v228\n" \
"ds_read_b128 a[108:111], v210 offset:57344\n" \
"v_mfma_f32_16x16x32_bf16 v[152:155], a[64:67], a[48:51], v[172:175]\n" \
"v_exp_f32_e64 v148, v148\n" \
"v_mfma_f32_16x16x32_bf16 v[152:155], a[68:71], a[52:55], v[152:155]\n" \
"ds_read_b128 a[112:115], %[kb0] offset:61440\n" \
"v_mfma_f32_16x16x32_bf16 v[152:155], a[72:75], a[56:59], v[152:155]\n" \
"v_exp_f32_e64 v149, v149\n" \
"v_mfma_f32_16x16x32_bf16 v[152:155], a[76:79], a[60:63], v[152:155]\n" \
"v_cvt_pk_bf16_f32 v216, v144, v145\n" \
"v_cvt_pk_bf16_f32 v217, v146, v147\n" \
"v_mfma_f32_16x16x32_bf16 v[156:159], a[80:83], a[48:51], v[172:175]\n" \
"v_exp_f32_e64 v150, v150\n" \
"v_mfma_f32_16x16x32_bf16 v[156:159], a[84:87], a[52:55], v[156:159]\n" \
"ds_read_b128 a[116:119], v208 offset:61440\n" \
"s_waitcnt lgkmcnt(6)\n" \
"v_mfma_f32_16x16x32_bf16 v[156:159], a[88:91], a[56:59], v[156:159]\n" \
"buffer_load_dwordx4 v214, %[srdk], s48 offen offset:3072 lds\n" \
"v_mfma_f32_16x16x32_bf16 v[156:159], a[92:95], a[60:63], v[156:159]\n" \
"v_exp_f32_e32 v151, v151\n" \
"v_mfma_f32_16x16x32_bf16 v[16:19], a[176:179], v[192:195], v[16:19]\n" \
"ds_read_b128 a[120:123], v209 offset:61440\n" \
"v_mfma_f32_16x16x32_bf16 v[16:19], a[144:147], v[184:187], v[16:19]\n" \
"v_exp_f32_e64 v152, v152\n" \
"v_mfma_f32_16x16x32_bf16 v[20:23], a[180:183], v[192:195], v[20:23]\n" \
"v_cvt_pk_bf16_f32 v218, v148, v149\n" \
"v_cvt_pk_bf16_f32 v219, v150, v151\n" \
"v_mfma_f32_16x16x32_bf16 v[20:23], a[148:151], v[184:187], v[20:23]\n" \
"v_exp_f32_e64 v153, v153\n" \
"v_mfma_f32_16x16x32_bf16 v[24:27], a[184:187], v[192:195], v[24:27]\n" \
"v_exp_f32_e64 v154, v154\n" \
"v_mfma_f32_16x16x32_bf16 v[24:27], a[152:155], v[184:187], v[24:27]\n" \
"ds_read_b128 a[124:127], v210 offset:61440\n" \
"s_add_u32 s48, s48, 0x4000\n" \
"s_mov_b32 m0, s44\n" \
"s_addk_i32 s44, 0x400\n" \
"buffer_load_dwordx4 v229, s[64:67], s45 offen offset:0 lds\n" \
"s_add_u32 s45, s45, 229376\n" \
"v_mfma_f32_16x16x32_bf16 v[28:31], a[188:191], v[192:195], v[28:31]\n" \
"v_exp_f32_e64 v155, v155\n" \
"v_mfma_f32_16x16x32_bf16 v[28:31], a[156:159], v[184:187], v[28:31]\n" \
"s_waitcnt vmcnt(16) lgkmcnt(2)\n" \
"s_barrier\n" \
"v_mfma_f32_16x16x32_bf16 v[128:131], a[96:99], a[0:3], v[160:163]\n" \
"v_exp_f32_e64 v156, v156\n" \
"v_mfma_f32_16x16x32_bf16 v[128:131], a[100:103], a[4:7], v[128:131]\n" \
"s_add_u32 m0, %[m0b], 98304\n" \
"v_mfma_f32_16x16x32_bf16 v[128:131], a[104:107], a[8:11], v[128:131]\n" \
"v_exp_f32_e64 v157, v157\n" \
"v_mfma_f32_16x16x32_bf16 v[128:131], a[108:111], a[12:15], v[128:131]\n" \
"v_cvt_pk_bf16_f32 v220, v152, v153\n" \
"v_cvt_pk_bf16_f32 v221, v154, v155\n" \
"v_mfma_f32_16x16x32_bf16 v[132:135], a[112:115], a[0:3], v[160:163]\n" \
"v_exp_f32_e64 v158, v158\n" \
"v_mfma_f32_16x16x32_bf16 v[132:135], a[116:119], a[4:7], v[132:135]\n" \
"s_waitcnt lgkmcnt(0)\n" \
"v_mfma_f32_16x16x32_bf16 v[132:135], a[120:123], a[8:11], v[132:135]\n" \
"buffer_load_dwordx4 %[vd0], %[srdv], s48 offen offset:0 lds\n" \
"v_mfma_f32_16x16x32_bf16 v[132:135], a[124:127], a[12:15], v[132:135]\n" \
"v_exp_f32_e32 v159, v159\n" \
"v_mfma_f32_16x16x32_bf16 v[32:35], a[160:163], v[196:199], v[32:35]\n" \
"ds_read_b128 a[64:67], %[kb0] offset:0\n" \
"v_mfma_f32_16x16x32_bf16 v[32:35], a[128:131], v[188:191], v[32:35]\n" \
"v_exp_f32_e64 v128, v128\n" \
"v_mfma_f32_16x16x32_bf16 v[36:39], a[164:167], v[196:199], v[36:39]\n" \
"v_cvt_pk_bf16_f32 v222, v156, v157\n" \
"v_cvt_pk_bf16_f32 v223, v158, v159\n" \
"v_mfma_f32_16x16x32_bf16 v[36:39], a[132:135], v[188:191], v[36:39]\n" \
"v_exp_f32_e64 v129, v129\n" \
"v_mfma_f32_16x16x32_bf16 v[40:43], a[168:171], v[196:199], v[40:43]\n" \
"ds_read_b128 a[68:71], v208 offset:0\n" \
"v_mfma_f32_16x16x32_bf16 v[40:43], a[136:139], v[188:191], v[40:43]\n" \
"v_exp_f32_e64 v130, v130\n" \
"v_mfma_f32_16x16x32_bf16 v[44:47], a[172:175], v[196:199], v[44:47]\n" \
"ds_read_b128 a[72:75], v209 offset:0\n" \
"v_mfma_f32_16x16x32_bf16 v[44:47], a[140:143], v[188:191], v[44:47]\n" \
"v_exp_f32_e64 v131, v131\n" \
"v_smfmac_f32_16x16x64_bf16 v[176:179], a[192:195], v[184:191], v228\n" \
"buffer_store_dwordx4 a[196:199], v235, %[srdz], s63 offen offset:2048 sc1\n" \
"s_add_u32 s63, s63, 3072\n" \
"v_mfma_f32_16x16x32_bf16 v[136:139], a[96:99], a[16:19], v[164:167]\n" \
"v_exp_f32_e64 v132, v132\n" \
"v_mfma_f32_16x16x32_bf16 v[136:139], a[100:103], a[20:23], v[136:139]\n" \
"ds_read_b128 a[76:79], v210 offset:0\n" \
"v_mfma_f32_16x16x32_bf16 v[136:139], a[104:107], a[24:27], v[136:139]\n" \
"v_exp_f32_e64 v133, v133\n" \
"v_mfma_f32_16x16x32_bf16 v[136:139], a[108:111], a[28:31], v[136:139]\n" \
"v_cvt_pk_bf16_f32 v192, v128, v129\n" \
"v_cvt_pk_bf16_f32 v193, v130, v131\n" \
"v_mfma_f32_16x16x32_bf16 v[140:143], a[112:115], a[16:19], v[164:167]\n" \
"v_exp_f32_e64 v134, v134\n" \
"v_mfma_f32_16x16x32_bf16 v[140:143], a[116:119], a[20:23], v[140:143]\n" \
"ds_read_b128 a[80:83], %[kb0] offset:4096\n" \
"v_mfma_f32_16x16x32_bf16 v[140:143], a[120:123], a[24:27], v[140:143]\n" \
"buffer_load_dwordx4 v215, %[srdv], s48 offen offset:1024 lds\n" \
"v_mfma_f32_16x16x32_bf16 v[140:143], a[124:127], a[28:31], v[140:143]\n" \
"v_exp_f32_e64 v135, v135\n" \
"v_mfma_f32_16x16x32_bf16 v[48:51], a[176:179], v[196:199], v[48:51]\n" \
"ds_read_b128 a[84:87], v208 offset:4096\n" \
"v_mfma_f32_16x16x32_bf16 v[48:51], a[144:147], v[188:191], v[48:51]\n" \
"v_exp_f32_e64 v136, v136\n" \
"v_mfma_f32_16x16x32_bf16 v[52:55], a[180:183], v[196:199], v[52:55]\n" \
"v_cvt_pk_bf16_f32 v194, v132, v133\n" \
"v_cvt_pk_bf16_f32 v195, v134, v135\n" \
"v_mfma_f32_16x16x32_bf16 v[52:55], a[148:151], v[188:191], v[52:55]\n" \
"v_exp_f32_e64 v137, v137\n" \
"v_mfma_f32_16x16x32_bf16 v[56:59], a[184:187], v[196:199], v[56:59]\n" \
"v_exp_f32_e64 v138, v138\n" \
"v_mfma_f32_16x16x32_bf16 v[56:59], a[152:155], v[188:191], v[56:59]\n" \
"ds_read_b128 a[88:91], v209 offset:4096\n" \
"v_mfma_f32_16x16x32_bf16 v[60:63], a[188:191], v[196:199], v[60:63]\n" \
"v_exp_f32_e64 v139, v139\n" \
"v_mfma_f32_16x16x32_bf16 v[60:63], a[156:159], v[188:191], v[60:63]\n" \
"s_cmp_lg_u32 %[wid], 2\n" \
"s_cbranch_scc1 LtB7_%=\n" \
"global_load_dword v237, v236, s[58:59]\n" \
"s_add_u32 s61, s48, 49152\n" \
"s_mov_b32 exec_hi, 0\n" \
"s_nop 0\n" \
"buffer_load_dword v237, v224, %[srdp], s61 offen\n" \
"s_mov_b32 exec_hi, -1\n" \
"s_nop 0\n" \
"LtB7_%=:\n" \
"v_mfma_f32_16x16x32_bf16 v[144:147], a[96:99], a[32:35], v[168:171]\n" \
"v_exp_f32_e64 v140, v140\n" \
"v_mfma_f32_16x16x32_bf16 v[144:147], a[100:103], a[36:39], v[144:147]\n" \
"ds_read_b128 a[92:95], v210 offset:4096\n" \
"v_mfma_f32_16x16x32_bf16 v[144:147], a[104:107], a[40:43], v[144:147]\n" \
"v_exp_f32_e64 v141, v141\n" \
"v_mfma_f32_16x16x32_bf16 v[144:147], a[108:111], a[44:47], v[144:147]\n" \
"v_cvt_pk_bf16_f32 v196, v136, v137\n" \
"v_cvt_pk_bf16_f32 v197, v138, v139\n" \
"v_mfma_f32_16x16x32_bf16 v[148:151], a[112:115], a[32:35], v[168:171]\n" \
"v_exp_f32_e64 v142, v142\n" \
"v_mfma_f32_16x16x32_bf16 v[148:151], a[116:119], a[36:39], v[148:151]\n" \
"v_mfma_f32_16x16x32_bf16 v[148:151], a[120:123], a[40:43], v[148:151]\n" \
"buffer_load_dwordx4 %[vd0], %[srdv], s48 offen offset:2048 lds\n" \
"v_mfma_f32_16x16x32_bf16 v[148:151], a[124:127], a[44:47], v[148:151]\n" \
"v_exp_f32_e64 v143, v143\n" \
"v_mfma_f32_16x16x32_bf16 v[96:99], a[160:163], v[204:207], v[96:99]\n" \
"ds_read_b64_tr_b16 a[160:161], %[vvb] offset:57344\n" \
"v_mfma_f32_16x16x32_bf16 v[96:99], a[128:131], v[220:223], v[96:99]\n" \
"v_exp_f32_e64 v144, v144\n" \
"v_mfma_f32_16x16x32_bf16 v[100:103], a[164:167], v[204:207], v[100:103]\n" \
"v_cvt_pk_bf16_f32 v198, v140, v141\n" \
"v_cvt_pk_bf16_f32 v199, v142, v143\n" \
"v_mfma_f32_16x16x32_bf16 v[100:103], a[132:135], v[220:223], v[100:103]\n" \
"v_exp_f32_e64 v145, v145\n" \
"v_mfma_f32_16x16x32_bf16 v[104:107], a[168:171], v[204:207], v[104:107]\n" \
"ds_read_b64_tr_b16 a[162:163], %[vvb] offset:61440\n" \
"ds_read_b64_tr_b16 a[164:165], v211 offset:57344\n" \
"v_mfma_f32_16x16x32_bf16 v[104:107], a[136:139], v[220:223], v[104:107]\n" \
"v_exp_f32_e64 v146, v146\n" \
"v_mfma_f32_16x16x32_bf16 v[108:111], a[172:175], v[204:207], v[108:111]\n" \
"ds_read_b64_tr_b16 a[166:167], v211 offset:61440\n" \
"v_mfma_f32_16x16x32_bf16 v[108:111], a[140:143], v[220:223], v[108:111]\n" \
"v_exp_f32_e64 v147, v147\n" \
"v_smfmac_f32_16x16x64_bf16 v[180:183], a[192:195], v[216:223], v228\n" \
"ds_read_b64_tr_b16 a[168:169], %[vvb] offset:57856\n" \
"v_mfma_f32_16x16x32_bf16 v[152:155], a[96:99], a[48:51], v[172:175]\n" \
"v_exp_f32_e64 v148, v148\n" \
"v_mfma_f32_16x16x32_bf16 v[152:155], a[100:103], a[52:55], v[152:155]\n" \
"ds_read_b64_tr_b16 a[170:171], %[vvb] offset:61952\n" \
"v_mfma_f32_16x16x32_bf16 v[152:155], a[104:107], a[56:59], v[152:155]\n" \
"v_exp_f32_e64 v149, v149\n" \
"v_mfma_f32_16x16x32_bf16 v[152:155], a[108:111], a[60:63], v[152:155]\n" \
"v_cvt_pk_bf16_f32 v200, v144, v145\n" \
"v_cvt_pk_bf16_f32 v201, v146, v147\n" \
"v_mfma_f32_16x16x32_bf16 v[156:159], a[112:115], a[48:51], v[172:175]\n" \
".if . - adaln_body != 28672\n" \
".error \"size model\"\n" \
".endif\n" \
"Lk6_%=:\n" \
"s_cbranch_execz Lk7_%=\n" \
"s_nop 0\n" \
"v_exp_f32_e64 v150, v150\n" \
"v_mfma_f32_16x16x32_bf16 v[156:159], a[116:119], a[52:55], v[156:159]\n" \
"ds_read_b64_tr_b16 a[172:173], v211 offset:57856\n" \
"v_mfma_f32_16x16x32_bf16 v[156:159], a[120:123], a[56:59], v[156:159]\n" \
"buffer_load_dwordx4 v215, %[srdv], s48 offen offset:3072 lds\n" \
"v_mfma_f32_16x16x32_bf16 v[156:159], a[124:127], a[60:63], v[156:159]\n" \
"v_exp_f32_e64 v151, v151\n" \
"v_mfma_f32_16x16x32_bf16 v[112:115], a[176:179], v[204:207], v[112:115]\n" \
"ds_read_b64_tr_b16 a[174:175], v211 offset:61952\n" \
"ds_read_b64_tr_b16 a[176:177], %[vvb] offset:58368\n" \
"v_mfma_f32_16x16x32_bf16 v[112:115], a[144:147], v[220:223], v[112:115]\n" \
"v_exp_f32_e64 v152, v152\n" \
"v_mfma_f32_16x16x32_bf16 v[116:119], a[180:183], v[204:207], v[116:119]\n" \
"v_cvt_pk_bf16_f32 v202, v148, v149\n" \
"v_cvt_pk_bf16_f32 v203, v150, v151\n" \
"v_mfma_f32_16x16x32_bf16 v[116:119], a[148:151], v[220:223], v[116:119]\n" \
"v_exp_f32_e64 v153, v153\n" \
"v_mfma_f32_16x16x32_bf16 v[120:123], a[184:187], v[204:207], v[120:123]\n" \
"v_exp_f32_e64 v154, v154\n" \
"v_mfma_f32_16x16x32_bf16 v[120:123], a[152:155], v[220:223], v[120:123]\n" \
"ds_read_b64_tr_b16 a[178:179], %[vvb] offset:62464\n" \
"v_mfma_f32_16x16x32_bf16 v[124:127], a[188:191], v[204:207], v[124:127]\n" \
"v_exp_f32_e64 v155, v155\n" \
"v_mfma_f32_16x16x32_bf16 v[124:127], a[156:159], v[220:223], v[124:127]\n" \
"ds_read_b64_tr_b16 a[180:181], v211 offset:58368\n" \
"s_waitcnt lgkmcnt(13)\n" \
"v_mfma_f32_16x16x32_bf16 v[128:131], a[64:67], a[0:3], v[160:163]\n" \
"v_exp_f32_e32 v156, v156\n" \
"v_mfma_f32_16x16x32_bf16 v[128:131], a[68:71], a[4:7], v[128:131]\n" \
"ds_read_b64_tr_b16 a[182:183], v211 offset:62464\n" \
"s_add_u32 m0, %[m0b], 49152\n" \
"v_mfma_f32_16x16x32_bf16 v[128:131], a[72:75], a[8:11], v[128:131]\n" \
"v_exp_f32_e64 v157, v157\n" \
"v_mfma_f32_16x16x32_bf16 v[128:131], a[76:79], a[12:15], v[128:131]\n" \
"v_cvt_pk_bf16_f32 v204, v152, v153\n" \
"v_cvt_pk_bf16_f32 v205, v154, v155\n" \
"v_mfma_f32_16x16x32_bf16 v[132:135], a[80:83], a[0:3], v[160:163]\n" \
"v_exp_f32_e64 v158, v158\n" \
"v_mfma_f32_16x16x32_bf16 v[132:135], a[84:87], a[4:7], v[132:135]\n" \
"ds_read_b64_tr_b16 a[184:185], %[vvb] offset:58880\n" \
"s_waitcnt lgkmcnt(5)\n" \
"v_mfma_f32_16x16x32_bf16 v[132:135], a[88:91], a[8:11], v[132:135]\n" \
"buffer_load_dwordx4 %[kd0], %[srdk], s48 offen offset:0 lds\n" \
"v_mfma_f32_16x16x32_bf16 v[132:135], a[92:95], a[12:15], v[132:135]\n" \
"v_exp_f32_e32 v159, v159\n" \
"v_mfma_f32_16x16x32_bf16 v[64:67], a[128:131], v[216:219], v[64:67]\n" \
"ds_read_b64_tr_b16 a[186:187], %[vvb] offset:62976\n" \
"ds_read_b64_tr_b16 a[188:189], v211 offset:58880\n" \
"v_mfma_f32_16x16x32_bf16 v[64:67], a[160:163], v[200:203], v[64:67]\n" \
"v_exp_f32_e64 v128, v128\n" \
"v_mfma_f32_16x16x32_bf16 v[68:71], a[132:135], v[216:219], v[68:71]\n" \
"v_cvt_pk_bf16_f32 v206, v156, v157\n" \
"v_cvt_pk_bf16_f32 v207, v158, v159\n" \
"v_mfma_f32_16x16x32_bf16 v[68:71], a[164:167], v[200:203], v[68:71]\n" \
"v_exp_f32_e64 v129, v129\n" \
"v_mfma_f32_16x16x32_bf16 v[72:75], a[136:139], v[216:219], v[72:75]\n" \
"ds_read_b64_tr_b16 a[190:191], v211 offset:62976\n" \
"ds_read_b64_tr_b16 a[128:129], %[vvb] offset:0\n" \
"v_mfma_f32_16x16x32_bf16 v[72:75], a[168:171], v[200:203], v[72:75]\n" \
"v_exp_f32_e64 v130, v130\n" \
"v_mfma_f32_16x16x32_bf16 v[76:79], a[140:143], v[216:219], v[76:79]\n" \
"ds_read_b64_tr_b16 a[130:131], %[vvb] offset:4096\n" \
"ds_read_b64_tr_b16 a[132:133], v211 offset:0\n" \
"v_mfma_f32_16x16x32_bf16 v[76:79], a[172:175], v[200:203], v[76:79]\n" \
"v_exp_f32_e64 v131, v131\n" \
"v_smfmac_f32_16x16x64_bf16 v[176:179], a[192:195], v[192:199], v228\n" \
"ds_read_b64_tr_b16 a[134:135], v211 offset:4096\n" \
"ds_read_b64_tr_b16 a[136:137], %[vvb] offset:512\n" \
"v_mfma_f32_16x16x32_bf16 v[136:139], a[64:67], a[16:19], v[164:167]\n" \
"v_exp_f32_e64 v132, v132\n" \
"v_mfma_f32_16x16x32_bf16 v[136:139], a[68:71], a[20:23], v[136:139]\n" \
"ds_read_b64_tr_b16 a[138:139], %[vvb] offset:4608\n" \
"ds_read_b64_tr_b16 a[140:141], v211 offset:512\n" \
"v_mfma_f32_16x16x32_bf16 v[136:139], a[72:75], a[24:27], v[136:139]\n" \
"v_exp_f32_e64 v133, v133\n" \
"v_mfma_f32_16x16x32_bf16 v[136:139], a[76:79], a[28:31], v[136:139]\n" \
"v_cvt_pk_bf16_f32 v184, v128, v129\n" \
"v_cvt_pk_bf16_f32 v185, v130, v131\n" \
"v_mfma_f32_16x16x32_bf16 v[140:143], a[80:83], a[16:19], v[164:167]\n" \
"v_exp_f32_e64 v134, v134\n" \
"v_mfma_f32_16x16x32_bf16 v[140:143], a[84:87], a[20:23], v[140:143]\n" \
"ds_read_b64_tr_b16 a[142:143], v211 offset:4608\n" \
"s_waitcnt lgkmcnt(8)\n" \
"v_mfma_f32_16x16x32_bf16 v[140:143], a[88:91], a[24:27], v[140:143]\n" \
"buffer_load_dwordx4 v212, %[srdk], s48 offen offset:1024 lds\n" \
"v_mfma_f32_16x16x32_bf16 v[140:143], a[92:95], a[28:31], v[140:143]\n" \
"v_exp_f32_e32 v135, v135\n" \
"v_mfma_f32_16x16x32_bf16 v[80:83], a[144:147], v[216:219], v[80:83]\n" \
"ds_read_b64_tr_b16 a[144:145], %[vvb] offset:1024\n" \
"ds_read_b64_tr_b16 a[146:147], %[vvb] offset:5120\n" \
"v_mfma_f32_16x16x32_bf16 v[80:83], a[176:179], v[200:203], v[80:83]\n" \
"v_exp_f32_e64 v136, v136\n" \
"v_mfma_f32_16x16x32_bf16 v[84:87], a[148:151], v[216:219], v[84:87]\n" \
"v_cvt_pk_bf16_f32 v186, v132, v133\n" \
"v_cvt_pk_bf16_f32 v187, v134, v135\n" \
"v_mfma_f32_16x16x32_bf16 v[84:87], a[180:183], v[200:203], v[84:87]\n" \
"v_exp_f32_e64 v137, v137\n" \
"v_mfma_f32_16x16x32_bf16 v[88:91], a[152:155], v[216:219], v[88:91]\n" \
"v_exp_f32_e64 v138, v138\n" \
"v_mfma_f32_16x16x32_bf16 v[88:91], a[184:187], v[200:203], v[88:91]\n" \
"ds_read_b64_tr_b16 a[148:149], v211 offset:1024\n" \
"ds_read_b64_tr_b16 a[150:151], v211 offset:5120\n" \
"v_mfma_f32_16x16x32_bf16 v[92:95], a[156:159], v[216:219], v[92:95]\n" \
"v_exp_f32_e64 v139, v139\n" \
"v_mfma_f32_16x16x32_bf16 v[92:95], a[188:191], v[200:203], v[92:95]\n" \
"ds_read_b64_tr_b16 a[152:153], %[vvb] offset:1536\n" \
"buffer_store_dwordx4 a[196:199], v235, %[srdz], s62 offen offset:2048 sc1\n" \
"s_add_u32 s62, s62, 3072\n" \
"v_mfma_f32_16x16x32_bf16 v[144:147], a[64:67], a[32:35], v[168:171]\n" \
"v_exp_f32_e64 v140, v140\n" \
"v_mfma_f32_16x16x32_bf16 v[144:147], a[68:71], a[36:39], v[144:147]\n" \
"ds_read_b64_tr_b16 a[154:155], %[vvb] offset:5632\n" \
"ds_read_b64_tr_b16 a[156:157], v211 offset:1536\n" \
"v_mfma_f32_16x16x32_bf16 v[144:147], a[72:75], a[40:43], v[144:147]\n" \
"v_exp_f32_e64 v141, v141\n" \
"v_mfma_f32_16x16x32_bf16 v[144:147], a[76:79], a[44:47], v[144:147]\n" \
"v_cvt_pk_bf16_f32 v188, v136, v137\n" \
"v_cvt_pk_bf16_f32 v189, v138, v139\n" \
"v_mfma_f32_16x16x32_bf16 v[148:151], a[80:83], a[32:35], v[168:171]\n" \
"v_exp_f32_e64 v142, v142\n" \
"v_mfma_f32_16x16x32_bf16 v[148:151], a[84:87], a[36:39], v[148:151]\n" \
"ds_read_b64_tr_b16 a[158:159], v211 offset:5632\n" \
"s_waitcnt lgkmcnt(8)\n" \
"v_mfma_f32_16x16x32_bf16 v[148:151], a[88:91], a[40:43], v[148:151]\n" \
"buffer_load_dwordx4 v213, %[srdk], s48 offen offset:2048 lds\n" \
"v_mfma_f32_16x16x32_bf16 v[148:151], a[92:95], a[44:47], v[148:151]\n" \
"v_exp_f32_e32 v143, v143\n" \
"v_mfma_f32_16x16x32_bf16 v[0:3], a[160:163], v[192:195], v[0:3]\n" \
"ds_read_b128 a[96:99], %[kb0] offset:8192\n" \
"v_mfma_f32_16x16x32_bf16 v[0:3], a[128:131], v[184:187], v[0:3]\n" \
"v_exp_f32_e64 v144, v144\n" \
"v_mfma_f32_16x16x32_bf16 v[4:7], a[164:167], v[192:195], v[4:7]\n" \
"v_cvt_pk_bf16_f32 v190, v140, v141\n" \
"v_cvt_pk_bf16_f32 v191, v142, v143\n" \
"v_mfma_f32_16x16x32_bf16 v[4:7], a[132:135], v[184:187], v[4:7]\n" \
"v_exp_f32_e64 v145, v145\n" \
"v_mfma_f32_16x16x32_bf16 v[8:11], a[168:171], v[192:195], v[8:11]\n" \
"ds_read_b128 a[100:103], v208 offset:8192\n" \
"v_mfma_f32_16x16x32_bf16 v[8:11], a[136:139], v[184:187], v[8:11]\n" \
"v_exp_f32_e64 v146, v146\n" \
"v_mfma_f32_16x16x32_bf16 v[12:15], a[172:175], v[192:195], v[12:15]\n" \
"ds_read_b128 a[104:107], v209 offset:8192\n" \
"v_mfma_f32_16x16x32_bf16 v[12:15], a[140:143], v[184:187], v[12:15]\n" \
"v_exp_f32_e64 v147, v147\n" \
"v_smfmac_f32_16x16x64_bf16 v[180:183], a[192:195], v[200:207], v228\n" \
"ds_read_b128 a[108:111], v210 offset:8192\n" \
"v_mfma_f32_16x16x32_bf16 v[152:155], a[64:67], a[48:51], v[172:175]\n" \
"v_exp_f32_e64 v148, v148\n" \
"v_mfma_f32_16x16x32_bf16 v[152:155], a[68:71], a[52:55], v[152:155]\n" \
"ds_read_b128 a[112:115], %[kb0] offset:12288\n" \
"v_mfma_f32_16x16x32_bf16 v[152:155], a[72:75], a[56:59], v[152:155]\n" \
"v_exp_f32_e64 v149, v149\n" \
"v_mfma_f32_16x16x32_bf16 v[152:155], a[76:79], a[60:63], v[152:155]\n" \
"v_cvt_pk_bf16_f32 v216, v144, v145\n" \
"v_cvt_pk_bf16_f32 v217, v146, v147\n" \
"v_mfma_f32_16x16x32_bf16 v[156:159], a[80:83], a[48:51], v[172:175]\n" \
"v_exp_f32_e64 v150, v150\n" \
"v_mfma_f32_16x16x32_bf16 v[156:159], a[84:87], a[52:55], v[156:159]\n" \
"ds_read_b128 a[116:119], v208 offset:12288\n" \
"s_waitcnt lgkmcnt(6)\n" \
"v_mfma_f32_16x16x32_bf16 v[156:159], a[88:91], a[56:59], v[156:159]\n" \
"buffer_load_dwordx4 v214, %[srdk], s48 offen offset:3072 lds\n" \
"v_mfma_f32_16x16x32_bf16 v[156:159], a[92:95], a[60:63], v[156:159]\n" \
"v_exp_f32_e32 v151, v151\n" \
"v_mfma_f32_16x16x32_bf16 v[16:19], a[176:179], v[192:195], v[16:19]\n" \
"ds_read_b128 a[120:123], v209 offset:12288\n" \
"v_mfma_f32_16x16x32_bf16 v[16:19], a[144:147], v[184:187], v[16:19]\n" \
"v_exp_f32_e64 v152, v152\n" \
"v_mfma_f32_16x16x32_bf16 v[20:23], a[180:183], v[192:195], v[20:23]\n" \
"v_cvt_pk_bf16_f32 v218, v148, v149\n" \
"v_cvt_pk_bf16_f32 v219, v150, v151\n" \
"v_mfma_f32_16x16x32_bf16 v[20:23], a[148:151], v[184:187], v[20:23]\n" \
"v_exp_f32_e64 v153, v153\n" \
"v_mfma_f32_16x16x32_bf16 v[24:27], a[184:187], v[192:195], v[24:27]\n" \
"v_exp_f32_e64 v154, v154\n" \
"v_mfma_f32_16x16x32_bf16 v[24:27], a[152:155], v[184:187], v[24:27]\n" \
"ds_read_b128 a[124:127], v210 offset:12288\n" \
"s_add_u32 s48, s48, 0x4000\n" \
"v_mfma_f32_16x16x32_bf16 v[28:31], a[188:191], v[192:195], v[28:31]\n" \
"v_exp_f32_e64 v155, v155\n" \
"v_mfma_f32_16x16x32_bf16 v[28:31], a[156:159], v[184:187], v[28:31]\n" \
"s_waitcnt vmcnt(17) lgkmcnt(2)\n" \
"s_barrier\n" \
"v_mfma_f32_16x16x32_bf16 v[128:131], a[96:99], a[0:3], v[160:163]\n" \
"v_exp_f32_e64 v156, v156\n" \
"v_mfma_f32_16x16x32_bf16 v[128:131], a[100:103], a[4:7], v[128:131]\n" \
"s_add_u32 m0, %[m0b], 114688\n" \
"v_mfma_f32_16x16x32_bf16 v[128:131], a[104:107], a[8:11], v[128:131]\n" \
"v_exp_f32_e64 v157, v157\n" \
"v_mfma_f32_16x16x32_bf16 v[128:131], a[108:111], a[12:15], v[128:131]\n" \
"v_cvt_pk_bf16_f32 v220, v152, v153\n" \
"v_cvt_pk_bf16_f32 v221, v154, v155\n" \
"v_mfma_f32_16x16x32_bf16 v[132:135], a[112:115], a[0:3], v[160:163]\n" \
"v_exp_f32_e64 v158, v158\n" \
"v_mfma_f32_16x16x32_bf16 v[132:135], a[116:119], a[4:7], v[132:135]\n" \
"s_waitcnt lgkmcnt(0)\n" \
"v_mfma_f32_16x16x32_bf16 v[132:135], a[120:123], a[8:11], v[132:135]\n" \
"buffer_load_dwordx4 %[vd0], %[srdv], s48 offen offset:0 lds\n" \
"v_mfma_f32_16x16x32_bf16 v[132:135], a[124:127], a[12:15], v[132:135]\n" \
"v_exp_f32_e32 v159, v159\n" \
"v_mfma_f32_16x16x32_bf16 v[32:35], a[160:163], v[196:199], v[32:35]\n" \
"ds_read_b128 a[64:67], %[kb0] offset:16384\n" \
"v_mfma_f32_16x16x32_bf16 v[32:35], a[128:131], v[188:191], v[32:35]\n" \
"v_exp_f32_e64 v128, v128\n" \
"v_mfma_f32_16x16x32_bf16 v[36:39], a[164:167], v[196:199], v[36:39]\n" \
"v_cvt_pk_bf16_f32 v222, v156, v157\n" \
"v_cvt_pk_bf16_f32 v223, v158, v159\n" \
"v_mfma_f32_16x16x32_bf16 v[36:39], a[132:135], v[188:191], v[36:39]\n" \
"v_exp_f32_e64 v129, v129\n" \
"v_mfma_f32_16x16x32_bf16 v[40:43], a[168:171], v[196:199], v[40:43]\n" \
"ds_read_b128 a[68:71], v208 offset:16384\n" \
"v_mfma_f32_16x16x32_bf16 v[40:43], a[136:139], v[188:191], v[40:43]\n" \
"v_exp_f32_e64 v130, v130\n" \
"v_mfma_f32_16x16x32_bf16 v[44:47], a[172:175], v[196:199], v[44:47]\n" \
"ds_read_b128 a[72:75], v209 offset:16384\n" \
"v_mfma_f32_16x16x32_bf16 v[44:47], a[140:143], v[188:191], v[44:47]\n" \
"v_exp_f32_e64 v131, v131\n" \
"v_smfmac_f32_16x16x64_bf16 v[176:179], a[192:195], v[184:191], v228\n" \
"v_mfma_f32_16x16x32_bf16 v[136:139], a[96:99], a[16:19], v[164:167]\n" \
"v_exp_f32_e64 v132, v132\n" \
"v_mfma_f32_16x16x32_bf16 v[136:139], a[100:103], a[20:23], v[136:139]\n" \
"ds_read_b128 a[76:79], v210 offset:16384\n" \
"v_mfma_f32_16x16x32_bf16 v[136:139], a[104:107], a[24:27], v[136:139]\n" \
"v_exp_f32_e64 v133, v133\n" \
"v_mfma_f32_16x16x32_bf16 v[136:139], a[108:111], a[28:31], v[136:139]\n" \
"v_cvt_pk_bf16_f32 v192, v128, v129\n" \
"v_cvt_pk_bf16_f32 v193, v130, v131\n" \
"v_mfma_f32_16x16x32_bf16 v[140:143], a[112:115], a[16:19], v[164:167]\n" \
"v_exp_f32_e64 v134, v134\n" \
"v_mfma_f32_16x16x32_bf16 v[140:143], a[116:119], a[20:23], v[140:143]\n" \
"ds_read_b128 a[80:83], %[kb0] offset:20480\n" \
"v_mfma_f32_16x16x32_bf16 v[140:143], a[120:123], a[24:27], v[140:143]\n" \
"buffer_load_dwordx4 v215, %[srdv], s48 offen offset:1024 lds\n" \
"v_mfma_f32_16x16x32_bf16 v[140:143], a[124:127], a[28:31], v[140:143]\n" \
"v_exp_f32_e64 v135, v135\n" \
"v_mfma_f32_16x16x32_bf16 v[48:51], a[176:179], v[196:199], v[48:51]\n" \
"ds_read_b128 a[84:87], v208 offset:20480\n" \
"v_mfma_f32_16x16x32_bf16 v[48:51], a[144:147], v[188:191], v[48:51]\n" \
"v_exp_f32_e64 v136, v136\n" \
"v_mfma_f32_16x16x32_bf16 v[52:55], a[180:183], v[196:199], v[52:55]\n" \
"v_cvt_pk_bf16_f32 v194, v132, v133\n" \
"v_cvt_pk_bf16_f32 v195, v134, v135\n" \
"v_mfma_f32_16x16x32_bf16 v[52:55], a[148:151], v[188:191], v[52:55]\n" \
"v_exp_f32_e64 v137, v137\n" \
"v_mfma_f32_16x16x32_bf16 v[56:59], a[184:187], v[196:199], v[56:59]\n" \
"v_exp_f32_e64 v138, v138\n" \
"v_mfma_f32_16x16x32_bf16 v[56:59], a[152:155], v[188:191], v[56:59]\n" \
"ds_read_b128 a[88:91], v209 offset:20480\n" \
"v_mfma_f32_16x16x32_bf16 v[60:63], a[188:191], v[196:199], v[60:63]\n" \
"v_exp_f32_e64 v139, v139\n" \
"v_mfma_f32_16x16x32_bf16 v[60:63], a[156:159], v[188:191], v[60:63]\n" \
"s_cmp_lg_u32 %[wid], 3\n" \
"s_cbranch_scc1 LtB1_%=\n" \
"global_load_dword v237, v236, s[58:59]\n" \
"s_add_u32 s61, s48, 49152\n" \
"s_mov_b32 exec_hi, 0\n" \
"s_nop 0\n" \
"buffer_load_dword v237, v224, %[srdp], s61 offen\n" \
"s_mov_b32 exec_hi, -1\n" \
"s_nop 0\n" \
"LtB1_%=:\n" \
"v_mfma_f32_16x16x32_bf16 v[144:147], a[96:99], a[32:35], v[168:171]\n" \
"v_exp_f32_e64 v140, v140\n" \
"v_mfma_f32_16x16x32_bf16 v[144:147], a[100:103], a[36:39], v[144:147]\n" \
"ds_read_b128 a[92:95], v210 offset:20480\n" \
"v_mfma_f32_16x16x32_bf16 v[144:147], a[104:107], a[40:43], v[144:147]\n" \
"v_exp_f32_e64 v141, v141\n" \
"v_mfma_f32_16x16x32_bf16 v[144:147], a[108:111], a[44:47], v[144:147]\n" \
"v_cvt_pk_bf16_f32 v196, v136, v137\n" \
"v_cvt_pk_bf16_f32 v197, v138, v139\n" \
"v_mfma_f32_16x16x32_bf16 v[148:151], a[112:115], a[32:35], v[168:171]\n" \
"v_exp_f32_e64 v142, v142\n" \
"v_mfma_f32_16x16x32_bf16 v[148:151], a[116:119], a[36:39], v[148:151]\n" \
"v_mfma_f32_16x16x32_bf16 v[148:151], a[120:123], a[40:43], v[148:151]\n" \
"buffer_load_dwordx4 %[vd0], %[srdv], s48 offen offset:2048 lds\n" \
"v_mfma_f32_16x16x32_bf16 v[148:151], a[124:127], a[44:47], v[148:151]\n" \
"v_exp_f32_e64 v143, v143\n" \
"v_mfma_f32_16x16x32_bf16 v[96:99], a[160:163], v[204:207], v[96:99]\n" \
"ds_read_b64_tr_b16 a[160:161], %[vvb] offset:8192\n" \
"v_mfma_f32_16x16x32_bf16 v[96:99], a[128:131], v[220:223], v[96:99]\n" \
"v_exp_f32_e64 v144, v144\n" \
"v_mfma_f32_16x16x32_bf16 v[100:103], a[164:167], v[204:207], v[100:103]\n" \
"v_cvt_pk_bf16_f32 v198, v140, v141\n" \
"v_cvt_pk_bf16_f32 v199, v142, v143\n" \
"v_mfma_f32_16x16x32_bf16 v[100:103], a[132:135], v[220:223], v[100:103]\n" \
"v_exp_f32_e64 v145, v145\n" \
"v_mfma_f32_16x16x32_bf16 v[104:107], a[168:171], v[204:207], v[104:107]\n" \
"ds_read_b64_tr_b16 a[162:163], %[vvb] offset:12288\n" \
"ds_read_b64_tr_b16 a[164:165], v211 offset:8192\n" \
"v_mfma_f32_16x16x32_bf16 v[104:107], a[136:139], v[220:223], v[104:107]\n" \
"v_exp_f32_e64 v146, v146\n" \
"v_mfma_f32_16x16x32_bf16 v[108:111], a[172:175], v[204:207], v[108:111]\n" \
"ds_read_b64_tr_b16 a[166:167], v211 offset:12288\n" \
"v_mfma_f32_16x16x32_bf16 v[108:111], a[140:143], v[220:223], v[108:111]\n" \
"v_exp_f32_e64 v147, v147\n" \
"v_smfmac_f32_16x16x64_bf16 v[180:183], a[192:195], v[216:223], v228\n" \
"ds_read_b64_tr_b16 a[168:169], %[vvb] offset:8704\n" \
"v_mfma_f32_16x16x32_bf16 v[152:155], a[96:99], a[48:51], v[172:175]\n" \
"v_exp_f32_e64 v148, v148\n" \
"v_mfma_f32_16x16x32_bf16 v[152:155], a[100:103], a[52:55], v[152:155]\n" \
"ds_read_b64_tr_b16 a[170:171], %[vvb] offset:12800\n" \
"v_mfma_f32_16x16x32_bf16 v[152:155], a[104:107], a[56:59], v[152:155]\n" \
"v_exp_f32_e64 v149, v149\n" \
"v_mfma_f32_16x16x32_bf16 v[152:155], a[108:111], a[60:63], v[152:155]\n" \
"v_cvt_pk_bf16_f32 v200, v144, v145\n" \
"v_cvt_pk_bf16_f32 v201, v146, v147\n" \
"v_mfma_f32_16x16x32_bf16 v[156:159], a[112:115], a[48:51], v[172:175]\n" \
"v_exp_f32_e64 v150, v150\n" \
"v_mfma_f32_16x16x32_bf16 v[156:159], a[116:119], a[52:55], v[156:159]\n" \
"ds_read_b64_tr_b16 a[172:173], v211 offset:8704\n" \
"v_mfma_f32_16x16x32_bf16 v[156:159], a[120:123], a[56:59], v[156:159]\n" \
"buffer_load_dwordx4 v215, %[srdv], s48 offen offset:3072 lds\n" \
"v_mfma_f32_16x16x32_bf16 v[156:159], a[124:127], a[60:63], v[156:159]\n" \
"v_exp_f32_e64 v151, v151\n" \
"v_mfma_f32_16x16x32_bf16 v[112:115], a[176:179], v[204:207], v[112:115]\n" \
"ds_read_b64_tr_b16 a[174:175], v211 offset:12800\n" \
"ds_read_b64_tr_b16 a[176:177], %[vvb] offset:9216\n" \
"v_mfma_f32_16x16x32_bf16 v[112:115], a[144:147], v[220:223], v[112:115]\n" \
"v_exp_f32_e64 v152, v152\n" \
"v_mfma_f32_16x16x32_bf16 v[116:119], a[180:183], v[204:207], v[116:119]\n" \
"v_cvt_pk_bf16_f32 v202, v148, v149\n" \
"v_cvt_pk_bf16_f32 v203, v150, v151\n" \
"v_mfma_f32_16x16x32_bf16 v[116:119], a[148:151], v[220:223], v[116:119]\n" \
"v_exp_f32_e64 v153, v153\n" \
"v_mfma_f32_16x16x32_bf16 v[120:123], a[184:187], v[204:207], v[120:123]\n" \
"v_exp_f32_e64 v154, v154\n" \
"v_mfma_f32_16x16x32_bf16 v[120:123], a[152:155], v[220:223], v[120:123]\n" \
"ds_read_b64_tr_b16 a[178:179], %[vvb] offset:13312\n" \
"v_mfma_f32_16x16x32_bf16 v[124:127], a[188:191], v[204:207], v[124:127]\n" \
"v_exp_f32_e64 v155, v155\n" \
"v_mfma_f32_16x16x32_bf16 v[124:127], a[156:159], v[220:223], v[124:127]\n" \
"ds_read_b64_tr_b16 a[180:181], v211 offset:9216\n" \
"s_waitcnt lgkmcnt(13)\n" \
"s_cmp_lg_u32 s60, 7\n" \
"s_cbranch_scc1 LmB_%=\n" \
"s_nop 0\n" \
"v_readlane_b32 s48, %[tlo], 11\n" \
"v_readlane_b32 s58, %[tlo], 12\n" \
"v_lshlrev_b32_e64 v227, 2, v224\n" \
"v_and_b32_e32 v227, 0x1800, v227\n" \
"v_bfe_u32 v234, v224, 11, 1\n" \
"v_lshl_or_b32 v227, v234, 15, v227\n" \
"v_lshrrev_b32_e64 v234, 12, v224\n" \
"v_lshl_or_b32 v227, v234, 13, v227\n" \
"v_and_or_b32 v227, v235, 48, v227\n" \
"v_readlane_b32 s51, %[tlo], 31\n" \
"s_lshl_b32 s62, %[wid], 10\n" \
"s_add_u32 s62, s62, 262144\n" \
"s_add_u32 s55, s62, 8192\n" \
"v_readlane_b32 s68, %[tlo], 20\n" \
"v_readlane_b32 s69, %[thi], 20\n" \
"s_lshl_b32 s63, %[wid], 9\n" \
"v_lshrrev_b32_e64 v230, 1, v235\n" \
"buffer_load_dwordx4 a[200:203], v235, %[srdz], s62 offen sc1\n" \
"v_add_u32_e64 v230, s63, v230\n" \
"buffer_load_dwordx4 a[204:207], v235, %[srdz], s55 offen sc1\n" \
"s_nop 1\n" \
"global_load_dwordx2 a[208:209], v230, s[68:69]\n" \
"v_mfma_f32_16x16x32_bf16 v[128:131], a[64:67], a[0:3], v[160:163]\n" \
"v_exp_f32_e32 v156, v156\n" \
"v_mfma_f32_16x16x32_bf16 v[128:131], a[68:71], a[4:7], v[128:131]\n" \
"ds_read_b64_tr_b16 a[182:183], v211 offset:13312\n" \
"s_add_u32 m0, %[m0b], 0\n" \
"s_mov_b32 s49, s48\n" \
"v_mfma_f32_16x16x32_bf16 v[128:131], a[72:75], a[8:11], v[128:131]\n" \
"v_exp_f32_e64 v157, v157\n" \
"v_mfma_f32_16x16x32_bf16 v[128:131], a[76:79], a[12:15], v[128:131]\n" \
"v_cvt_pk_bf16_f32 v204, v152, v153\n" \
"v_cvt_pk_bf16_f32 v205, v154, v155\n" \
"v_mfma_f32_16x16x32_bf16 v[132:135], a[80:83], a[0:3], v[160:163]\n" \
"v_exp_f32_e64 v158, v158\n" \
"v_mfma_f32_16x16x32_bf16 v[132:135], a[84:87], a[4:7], v[132:135]\n" \
"ds_read_b64_tr_b16 a[184:185], %[vvb] offset:9728\n" \
"s_lshl_b32 s49, s49, 18\n" \
"s_or_b32 s49, s49, s58\n" \
"s_waitcnt lgkmcnt(5)\n" \
"v_mfma_f32_16x16x32_bf16 v[132:135], a[88:91], a[8:11], v[132:135]\n" \
"buffer_load_dwordx4 %[wo0], %[srdt], s49 offen offset:0 lds\n" \
"v_mfma_f32_16x16x32_bf16 v[132:135], a[92:95], a[12:15], v[132:135]\n" \
"v_exp_f32_e32 v159, v159\n" \
"v_mfma_f32_16x16x32_bf16 v[64:67], a[128:131], v[216:219], v[64:67]\n" \
"ds_read_b64_tr_b16 a[186:187], %[vvb] offset:13824\n" \
"ds_read_b64_tr_b16 a[188:189], v211 offset:9728\n" \
"v_readlane_b32 s60, %[thi], 11\n" \
"buffer_load_dwordx4 a[212:215], v227, %[srdt], s51 offen offset:0\n" \
"v_mfma_f32_16x16x32_bf16 v[64:67], a[160:163], v[200:203], v[64:67]\n" \
"v_exp_f32_e64 v128, v128\n" \
"v_mfma_f32_16x16x32_bf16 v[68:71], a[132:135], v[216:219], v[68:71]\n" \
"v_cvt_pk_bf16_f32 v206, v156, v157\n" \
"v_cvt_pk_bf16_f32 v207, v158, v159\n" \
"v_mfma_f32_16x16x32_bf16 v[68:71], a[164:167], v[200:203], v[68:71]\n" \
"v_exp_f32_e64 v129, v129\n" \
"v_mfma_f32_16x16x32_bf16 v[72:75], a[136:139], v[216:219], v[72:75]\n" \
"ds_read_b64_tr_b16 a[190:191], v211 offset:13824\n" \
"ds_read_b64_tr_b16 a[128:129], %[vvb] offset:16384\n" \
"v_readlane_b32 s59, %[thi], 12\n" \
"v_mfma_f32_16x16x32_bf16 v[72:75], a[168:171], v[200:203], v[72:75]\n" \
"v_exp_f32_e64 v130, v130\n" \
"v_mfma_f32_16x16x32_bf16 v[76:79], a[140:143], v[216:219], v[76:79]\n" \
"ds_read_b64_tr_b16 a[130:131], %[vvb] offset:20480\n" \
"ds_read_b64_tr_b16 a[132:133], v211 offset:16384\n" \
"v_readlane_b32 s53, %[tlo], 13\n" \
"buffer_load_dwordx4 a[216:219], v227, %[srdt], s51 offen offset:64\n" \
"v_mfma_f32_16x16x32_bf16 v[76:79], a[172:175], v[200:203], v[76:79]\n" \
"v_exp_f32_e64 v131, v131\n" \
"v_smfmac_f32_16x16x64_bf16 v[176:179], a[192:195], v[192:199], v228\n" \
"ds_read_b64_tr_b16 a[134:135], v211 offset:20480\n" \
"ds_read_b64_tr_b16 a[136:137], %[vvb] offset:16896\n" \
"v_readlane_b32 s50, %[thi], 13\n" \
"v_mfma_f32_16x16x32_bf16 v[136:139], a[64:67], a[16:19], v[164:167]\n" \
"v_exp_f32_e64 v132, v132\n" \
"v_mfma_f32_16x16x32_bf16 v[136:139], a[68:71], a[20:23], v[136:139]\n" \
"ds_read_b64_tr_b16 a[138:139], %[vvb] offset:20992\n" \
"ds_read_b64_tr_b16 a[140:141], v211 offset:16896\n" \
"v_readlane_b32 s46, %[tlo], 14\n" \
"v_add_u32_e32 v212, 0x3c00, %[wo0]\n" \
"v_mfma_f32_16x16x32_bf16 v[136:139], a[72:75], a[24:27], v[136:139]\n" \
"v_exp_f32_e64 v133, v133\n" \
"v_mfma_f32_16x16x32_bf16 v[136:139], a[76:79], a[28:31], v[136:139]\n" \
"v_cvt_pk_bf16_f32 v184, v128, v129\n" \
"v_cvt_pk_bf16_f32 v185, v130, v131\n" \
"v_mfma_f32_16x16x32_bf16 v[140:143], a[80:83], a[16:19], v[164:167]\n" \
"v_exp_f32_e64 v134, v134\n" \
"v_mfma_f32_16x16x32_bf16 v[140:143], a[84:87], a[20:23], v[140:143]\n" \
"ds_read_b64_tr_b16 a[142:143], v211 offset:20992\n" \
"v_readlane_b32 s47, %[thi], 14\n" \
"v_add_u32_e32 v213, 0x7800, %[wo0]\n" \
"buffer_load_dwordx4 a[220:223], v227, %[srdt], s51 offen offset:128\n" \
"s_waitcnt lgkmcnt(8)\n" \
"v_mfma_f32_16x16x32_bf16 v[140:143], a[88:91], a[24:27], v[140:143]\n" \
"buffer_load_dwordx4 v212, %[srdt], s49 offen offset:1024 lds\n" \
"v_mfma_f32_16x16x32_bf16 v[140:143], a[92:95], a[28:31], v[140:143]\n" \
"v_exp_f32_e32 v135, v135\n" \
"v_mfma_f32_16x16x32_bf16 v[80:83], a[144:147], v[216:219], v[80:83]\n" \
"ds_read_b64_tr_b16 a[144:145], %[vvb] offset:17408\n" \
"ds_read_b64_tr_b16 a[146:147], %[vvb] offset:21504\n" \
"v_mfma_f32_16x16x32_bf16 v[80:83], a[176:179], v[200:203], v[80:83]\n" \
"v_exp_f32_e64 v136, v136\n" \
"v_mfma_f32_16x16x32_bf16 v[84:87], a[148:151], v[216:219], v[84:87]\n" \
"v_cvt_pk_bf16_f32 v186, v132, v133\n" \
"v_cvt_pk_bf16_f32 v187, v134, v135\n" \
"v_mfma_f32_16x16x32_bf16 v[84:87], a[180:183], v[200:203], v[84:87]\n" \
"v_exp_f32_e64 v137, v137\n" \
"v_mfma_f32_16x16x32_bf16 v[88:91], a[152:155], v[216:219], v[88:91]\n" \
"v_exp_f32_e64 v138, v138\n" \
"v_mfma_f32_16x16x32_bf16 v[88:91], a[184:187], v[200:203], v[88:91]\n" \
"ds_read_b64_tr_b16 a[148:149], v211 offset:17408\n" \
"buffer_load_dwordx4 a[224:227], v227, %[srdt], s51 offen offset:192\n" \
"ds_read_b64_tr_b16 a[150:151], v211 offset:21504\n" \
"v_mfma_f32_16x16x32_bf16 v[92:95], a[156:159], v[216:219], v[92:95]\n" \
"v_exp_f32_e64 v139, v139\n" \
"v_mfma_f32_16x16x32_bf16 v[92:95], a[188:191], v[200:203], v[92:95]\n" \
"ds_read_b64_tr_b16 a[152:153], %[vvb] offset:17920\n" \
"v_mfma_f32_16x16x32_bf16 v[144:147], a[64:67], a[32:35], v[168:171]\n" \
"v_exp_f32_e64 v140, v140\n" \
"v_mfma_f32_16x16x32_bf16 v[144:147], a[68:71], a[36:39], v[144:147]\n" \
"ds_read_b64_tr_b16 a[154:155], %[vvb] offset:22016\n" \
"ds_read_b64_tr_b16 a[156:157], v211 offset:17920\n" \
"buffer_load_dwordx4 a[228:231], v227, %[srdt], s51 offen offset:256\n" \
"v_mfma_f32_16x16x32_bf16 v[144:147], a[72:75], a[40:43], v[144:147]\n" \
"v_exp_f32_e64 v141, v141\n" \
"v_mfma_f32_16x16x32_bf16 v[144:147], a[76:79], a[44:47], v[144:147]\n" \
"v_cvt_pk_bf16_f32 v188, v136, v137\n" \
"v_cvt_pk_bf16_f32 v189, v138, v139\n" \
"v_mfma_f32_16x16x32_bf16 v[148:151], a[80:83], a[32:35], v[168:171]\n" \
"v_exp_f32_e64 v142, v142\n" \
"v_mfma_f32_16x16x32_bf16 v[148:151], a[84:87], a[36:39], v[148:151]\n" \
"ds_read_b64_tr_b16 a[158:159], v211 offset:22016\n" \
"s_waitcnt lgkmcnt(8)\n" \
"v_mfma_f32_16x16x32_bf16 v[148:151], a[88:91], a[40:43], v[148:151]\n" \
"buffer_load_dwordx4 v213, %[srdt], s49 offen offset:2048 lds\n" \
"v_mfma_f32_16x16x32_bf16 v[148:151], a[92:95], a[44:47], v[148:151]\n" \
"v_exp_f32_e32 v143, v143\n" \
"v_mfma_f32_16x16x32_bf16 v[0:3], a[160:163], v[192:195], v[0:3]\n" \
"ds_read_b128 a[96:99], %[kb0] offset:24576\n" \
"buffer_load_dwordx4 a[232:235], v227, %[srdt], s51 offen offset:320\n" \
"s_waitcnt vmcnt(9)\n" \
"v_accvgpr_read_b32 v230, a208\n" \
"v_accvgpr_read_b32 v234, a204\n" \
"v_mfma_f32_16x16x32_bf16 v[0:3], a[128:131], v[184:187], v[0:3]\n" \
"v_exp_f32_e32 v144, v144\n" \
"v_accvgpr_read_b32 v225, a205\n" \
"v_accvgpr_read_b32 v226, a200\n" \
"v_mfma_f32_16x16x32_bf16 v[4:7], a[164:167], v[192:195], v[4:7]\n" \
"v_cvt_pk_bf16_f32 v190, v140, v141\n" \
"v_cvt_pk_bf16_f32 v191, v142, v143\n" \
"v_accvgpr_read_b32 v231, a201\n" \
"v_bitop3_b32 v229, v234, v225, -1 bitop3:0x80\n" \
"v_mfma_f32_16x16x32_bf16 v[4:7], a[132:135], v[184:187], v[4:7]\n" \
"v_exp_f32_e64 v145, v145\n" \
"v_bitop3_b32 v229, v226, v231, v229 bitop3:0x80\n" \
"v_lshlrev_b32_e64 v236, 16, v230\n" \
"v_mfma_f32_16x16x32_bf16 v[8:11], a[168:171], v[192:195], v[8:11]\n" \
"ds_read_b128 a[100:103], v208 offset:24576\n" \
"v_and_b32_e32 v230, 0xffff0000, v230\n" \
"v_mul_f32_e64 v234, v234, v236\n" \
"v_mfma_f32_16x16x32_bf16 v[8:11], a[136:139], v[184:187], v[8:11]\n" \
"v_exp_f32_e32 v146, v146\n" \
"v_mul_f32_e32 v225, v225, v230\n" \
"v_mul_f32_e64 v226, v226, v234\n" \
"v_mfma_f32_16x16x32_bf16 v[12:15], a[172:175], v[192:195], v[12:15]\n" \
"ds_read_b128 a[104:107], v209 offset:24576\n" \
"v_add_u32_e32 v214, 0xb400, %[wo0]\n" \
"buffer_load_dwordx4 a[236:239], v227, %[srdt], s51 offen offset:384\n" \
"v_mul_f32_e64 v231, v231, v225\n" \
"v_cvt_pk_bf16_f32 v236, v234, v225\n" \
"v_mfma_f32_16x16x32_bf16 v[12:15], a[140:143], v[184:187], v[12:15]\n" \
"v_exp_f32_e64 v147, v147\n" \
"v_cvt_pk_bf16_f32 v232, v226, v231\n" \
"v_smfmac_f32_16x16x64_bf16 v[180:183], a[192:195], v[200:207], v228\n" \
"ds_read_b128 a[108:111], v210 offset:24576\n" \
"v_accvgpr_read_b32 v230, a209\n" \
"v_accvgpr_read_b32 v234, a206\n" \
"v_mfma_f32_16x16x32_bf16 v[152:155], a[64:67], a[48:51], v[172:175]\n" \
"v_exp_f32_e64 v148, v148\n" \
"v_accvgpr_read_b32 v225, a207\n" \
"v_accvgpr_read_b32 v226, a202\n" \
"v_mfma_f32_16x16x32_bf16 v[152:155], a[68:71], a[52:55], v[152:155]\n" \
"ds_read_b128 a[112:115], %[kb0] offset:28672\n" \
"v_accvgpr_read_b32 v231, a203\n" \
"v_bitop3_b32 v229, v234, v225, v229 bitop3:0x80\n" \
"v_mfma_f32_16x16x32_bf16 v[152:155], a[72:75], a[56:59], v[152:155]\n" \
"v_exp_f32_e64 v149, v149\n" \
"v_bitop3_b32 v229, v226, v231, v229 bitop3:0x80\n" \
"v_lshlrev_b32_e64 v237, 16, v230\n" \
"v_mfma_f32_16x16x32_bf16 v[152:155], a[76:79], a[60:63], v[152:155]\n" \
"v_cvt_pk_bf16_f32 v216, v144, v145\n" \
"v_cvt_pk_bf16_f32 v217, v146, v147\n" \
"v_and_b32_e32 v230, 0xffff0000, v230\n" \
"v_mul_f32_e64 v234, v234, v237\n" \
"v_mfma_f32_16x16x32_bf16 v[156:159], a[80:83], a[48:51], v[172:175]\n" \
"v_exp_f32_e32 v150, v150\n" \
"v_mul_f32_e32 v225, v225, v230\n" \
"v_mul_f32_e64 v226, v226, v234\n" \
".if . - adaln_body != 32768\n" \
".error \"size model\"\n" \
".endif\n" \
"Lk7_%=:\n" \
"s_cbranch_execz Lk8_%=\n" \
"s_nop 0\n" \
"v_mfma_f32_16x16x32_bf16 v[156:159], a[84:87], a[52:55], v[156:159]\n" \
"ds_read_b128 a[116:119], v208 offset:28672\n" \
"buffer_load_dwordx4 a[196:199], v227, %[srdt], s51 offen offset:448\n" \
"v_mul_f32_e64 v231, v231, v225\n" \
"v_cvt_pk_bf16_f32 v237, v234, v225\n" \
"s_waitcnt lgkmcnt(6)\n" \
"v_mfma_f32_16x16x32_bf16 v[156:159], a[88:91], a[56:59], v[156:159]\n" \
"buffer_load_dwordx4 v214, %[srdt], s49 offen offset:3072 lds\n" \
"v_cvt_pk_bf16_f32 v233, v226, v231\n" \
"v_mfma_f32_16x16x32_bf16 v[156:159], a[92:95], a[60:63], v[156:159]\n" \
"v_exp_f32_e32 v151, v151\n" \
"v_lshrrev_b32_e64 v230, 1, v235\n" \
"s_add_u32 s62, %[m0t], 6144\n" \
"v_mfma_f32_16x16x32_bf16 v[16:19], a[176:179], v[192:195], v[16:19]\n" \
"ds_read_b128 a[120:123], v209 offset:28672\n" \
"v_add_u32_e32 v230, s62, v230\n" \
"v_and_b32_e32 v229, 1, v229\n" \
"v_mfma_f32_16x16x32_bf16 v[16:19], a[144:147], v[184:187], v[16:19]\n" \
"v_exp_f32_e64 v152, v152\n" \
"v_cmp_eq_u32_e64 s[68:69], 0, v229\n" \
"ds_write_b64 v230, v[232:233] offset:0\n" \
"v_mfma_f32_16x16x32_bf16 v[20:23], a[180:183], v[192:195], v[20:23]\n" \
"v_cvt_pk_bf16_f32 v218, v148, v149\n" \
"v_cvt_pk_bf16_f32 v219, v150, v151\n" \
"v_and_b32_e32 v230, 0x170, v235\n" \
"v_lshrrev_b32_e64 v234, 7, v224\n" \
"v_mfma_f32_16x16x32_bf16 v[20:23], a[148:151], v[184:187], v[20:23]\n" \
"v_exp_f32_e64 v153, v153\n" \
"v_and_or_b32 v230, v234, 8, v230\n" \
"v_lshrrev_b32_e64 v234, 12, v224\n" \
"v_mfma_f32_16x16x32_bf16 v[24:27], a[184:187], v[192:195], v[24:27]\n" \
"v_exp_f32_e64 v154, v154\n" \
"v_lshl_or_b32 v230, v234, 7, v230\n" \
"v_add_u32_e64 v230, s62, v230\n" \
"v_mfma_f32_16x16x32_bf16 v[24:27], a[152:155], v[184:187], v[24:27]\n" \
"ds_read_b128 a[124:127], v210 offset:28672\n" \
"ds_write_b64 v230, v[236:237] offset:512\n" \
"v_mfma_f32_16x16x32_bf16 v[28:31], a[188:191], v[192:195], v[28:31]\n" \
"v_exp_f32_e32 v155, v155\n" \
"s_cmp_lg_u64 s[68:69], 0\n" \
"s_cbranch_scc1 Lus_%=\n" \
"Lub_%=:\n" \
"v_lshrrev_b32_e32 v234, 1, v235\n" \
"v_mfma_f32_16x16x32_bf16 v[28:31], a[156:159], v[184:187], v[28:31]\n" \
"s_waitcnt vmcnt(15) lgkmcnt(4)\n" \
"s_barrier\n" \
"v_mfma_f32_16x16x32_bf16 v[128:131], a[96:99], a[0:3], v[160:163]\n" \
"v_exp_f32_e64 v156, v156\n" \
"v_readlane_b32 s64, %[tlo], 8\n" \
"v_readlane_b32 s65, %[thi], 8\n" \
"v_add_u32_e32 v234, s62, v234\n" \
"v_mov_b64_e32 v[232:233], 0\n" \
"v_mfma_f32_16x16x32_bf16 v[128:131], a[100:103], a[4:7], v[128:131]\n" \
"v_mbcnt_lo_u32_b32 v231, -1, 0\n" \
"ds_write_b64 v234, v[232:233] offset:1024\n" \
"s_mov_b32 s68, 0xf0000f\n" \
"v_mfma_f32_16x16x32_bf16 v[128:131], a[104:107], a[8:11], v[128:131]\n" \
"v_exp_f32_e64 v157, v157\n" \
"s_mov_b32 s69, 0xf0000f00\n" \
"v_mov_b32_e32 v234, 0x400\n" \
"v_mfma_f32_16x16x32_bf16 v[128:131], a[108:111], a[12:15], v[128:131]\n" \
"v_cvt_pk_bf16_f32 v220, v152, v153\n" \
"v_cvt_pk_bf16_f32 v221, v154, v155\n" \
"v_cndmask_b32_e64 v234, v234, 0, s[68:69]\n" \
"v_and_or_b32 v230, v235, 48, v234\n" \
"v_mfma_f32_16x16x32_bf16 v[132:135], a[112:115], a[0:3], v[160:163]\n" \
"v_exp_f32_e32 v158, v158\n" \
"v_add_u32_e32 v230, s62, v230\n" \
"v_mfma_f32_16x16x32_bf16 v[132:135], a[116:119], a[4:7], v[132:135]\n" \
"v_mbcnt_hi_u32_b32 v231, -1, v231\n" \
"s_waitcnt lgkmcnt(2)\n" \
"v_mfma_f32_16x16x32_bf16 v[132:135], a[120:123], a[8:11], v[132:135]\n" \
"s_add_u32 m0, %[m0b], 65536\n" \
"s_mov_b32 s61, 0\n" \
"buffer_load_dwordx4 %[vg0], s[64:67], s61 offen offset:0 lds\n" \
"v_mfma_f32_16x16x32_bf16 v[132:135], a[124:127], a[12:15], v[132:135]\n" \
"v_exp_f32_e64 v159, v159\n" \
"v_mfma_f32_16x16x32_bf16 v[32:35], a[160:163], v[196:199], v[32:35]\n" \
"ds_read_b128 a[64:67], %[kb0] offset:32768\n" \
"v_and_b32_e64 v226, 31, v231\n" \
"v_mfma_f32_16x16x32_bf16 v[32:35], a[128:131], v[188:191], v[32:35]\n" \
"v_exp_f32_e64 v128, v128\n" \
"v_mfma_f32_16x16x32_bf16 v[36:39], a[164:167], v[196:199], v[36:39]\n" \
"v_cvt_pk_bf16_f32 v222, v156, v157\n" \
"v_cvt_pk_bf16_f32 v223, v158, v159\n" \
"v_mfma_f32_16x16x32_bf16 v[36:39], a[132:135], v[188:191], v[36:39]\n" \
"v_exp_f32_e64 v129, v129\n" \
"v_mfma_f32_16x16x32_bf16 v[40:43], a[168:171], v[196:199], v[40:43]\n" \
"ds_read_b128 a[68:71], v208 offset:32768\n" \
"v_lshlrev_b32_e64 v226, 2, v226\n" \
"v_mfma_f32_16x16x32_bf16 v[40:43], a[136:139], v[188:191], v[40:43]\n" \
"v_exp_f32_e64 v130, v130\n" \
"v_mfma_f32_16x16x32_bf16 v[44:47], a[172:175], v[196:199], v[44:47]\n" \
"ds_read_b128 a[72:75], v209 offset:32768\n" \
"v_cmp_gt_u32_e32 vcc, 1, v231\n" \
"v_mfma_f32_16x16x32_bf16 v[44:47], a[140:143], v[188:191], v[44:47]\n" \
"v_exp_f32_e32 v131, v131\n" \
"ds_read_b128 a[200:203], v230 offset:0\n" \
"v_smfmac_f32_16x16x64_bf16 v[176:179], a[192:195], v[184:191], v228\n" \
"v_mov_b32_e32 v225, 0x80000000\n" \
"v_mfma_f32_16x16x32_bf16 v[136:139], a[96:99], a[16:19], v[164:167]\n" \
"v_exp_f32_e64 v132, v132\n" \
"v_mfma_f32_16x16x32_bf16 v[136:139], a[100:103], a[20:23], v[136:139]\n" \
"ds_read_b128 a[76:79], v210 offset:32768\n" \
"v_mfma_f32_16x16x32_bf16 v[136:139], a[104:107], a[24:27], v[136:139]\n" \
"v_exp_f32_e64 v133, v133\n" \
"v_mfma_f32_16x16x32_bf16 v[136:139], a[108:111], a[28:31], v[136:139]\n" \
"v_cvt_pk_bf16_f32 v192, v128, v129\n" \
"v_cvt_pk_bf16_f32 v193, v130, v131\n" \
"ds_read_b128 a[204:207], v230 offset:64\n" \
"v_mfma_f32_16x16x32_bf16 v[140:143], a[112:115], a[16:19], v[164:167]\n" \
"v_exp_f32_e64 v134, v134\n" \
"v_mfma_f32_16x16x32_bf16 v[140:143], a[116:119], a[20:23], v[140:143]\n" \
"ds_read_b128 a[80:83], %[kb0] offset:36864\n" \
"v_cndmask_b32_e32 v225, v225, v224, vcc\n" \
"v_mfma_f32_16x16x32_bf16 v[140:143], a[120:123], a[24:27], v[140:143]\n" \
"s_add_u32 m0, %[m0b], 66560\n" \
"s_mov_b32 s61, 229376\n" \
"buffer_load_dwordx4 %[vg0], s[64:67], s61 offen offset:0 lds\n" \
"v_mfma_f32_16x16x32_bf16 v[140:143], a[124:127], a[28:31], v[140:143]\n" \
"v_exp_f32_e32 v135, v135\n" \
"s_waitcnt vmcnt(12) lgkmcnt(3)\n" \
"v_mfma_f32_16x16x32_bf16 a[208:211], a[212:215], a[200:203], 0\n" \
"ds_read_b128 a[200:203], v230 offset:128\n" \
"v_mfma_f32_16x16x32_bf16 v[48:51], a[176:179], v[196:199], v[48:51]\n" \
"ds_read_b128 a[84:87], v208 offset:36864\n" \
"v_mfma_f32_16x16x32_bf16 v[48:51], a[144:147], v[188:191], v[48:51]\n" \
"v_exp_f32_e32 v136, v136\n" \
"v_mfma_f32_16x16x32_bf16 v[52:55], a[180:183], v[196:199], v[52:55]\n" \
"v_cvt_pk_bf16_f32 v194, v132, v133\n" \
"v_cvt_pk_bf16_f32 v195, v134, v135\n" \
"v_mfma_f32_16x16x32_bf16 v[52:55], a[148:151], v[188:191], v[52:55]\n" \
"v_exp_f32_e64 v137, v137\n" \
"v_mfma_f32_16x16x32_bf16 v[56:59], a[184:187], v[196:199], v[56:59]\n" \
"v_exp_f32_e64 v138, v138\n" \
"v_mfma_f32_16x16x32_bf16 v[56:59], a[152:155], v[188:191], v[56:59]\n" \
"ds_read_b128 a[88:91], v209 offset:36864\n" \
"s_waitcnt vmcnt(11) lgkmcnt(4)\n" \
"v_mfma_f32_16x16x32_bf16 a[208:211], a[216:219], a[204:207], a[208:211]\n" \
"ds_read_b128 a[204:207], v230 offset:192\n" \
"v_mfma_f32_16x16x32_bf16 v[60:63], a[188:191], v[196:199], v[60:63]\n" \
"v_exp_f32_e32 v139, v139\n" \
"v_mfma_f32_16x16x32_bf16 v[60:63], a[156:159], v[188:191], v[60:63]\n" \
"s_waitcnt vmcnt(10) lgkmcnt(3)\n" \
"v_mfma_f32_16x16x32_bf16 a[208:211], a[220:223], a[200:203], a[208:211]\n" \
"ds_read_b128 a[200:203], v230 offset:256\n" \
"v_mfma_f32_16x16x32_bf16 v[144:147], a[96:99], a[32:35], v[168:171]\n" \
"v_exp_f32_e32 v140, v140\n" \
"v_mfma_f32_16x16x32_bf16 v[144:147], a[100:103], a[36:39], v[144:147]\n" \
"ds_read_b128 a[92:95], v210 offset:36864\n" \
"s_sub_u32 s61, s48, 0\n" \
"s_cmp_lt_u32 s61, 4\n" \
"s_cselect_b64 vcc, -1, 0\n" \
"v_mfma_f32_16x16x32_bf16 v[144:147], a[104:107], a[40:43], v[144:147]\n" \
"v_exp_f32_e32 v141, v141\n" \
"v_mfma_f32_16x16x32_bf16 v[144:147], a[108:111], a[44:47], v[144:147]\n" \
"v_cvt_pk_bf16_f32 v196, v136, v137\n" \
"v_cvt_pk_bf16_f32 v197, v138, v139\n" \
"v_mfma_f32_16x16x32_bf16 v[148:151], a[112:115], a[32:35], v[168:171]\n" \
"v_exp_f32_e64 v142, v142\n" \
"v_mfma_f32_16x16x32_bf16 v[148:151], a[116:119], a[36:39], v[148:151]\n" \
"v_readlane_b32 s55, %[tlo], 16\n" \
"s_waitcnt vmcnt(8) lgkmcnt(2)\n" \
"v_mfma_f32_16x16x32_bf16 a[208:211], a[224:227], a[204:207], a[208:211]\n" \
"ds_read_b128 a[204:207], v230 offset:320\n" \
"v_mfma_f32_16x16x32_bf16 v[148:151], a[120:123], a[40:43], v[148:151]\n" \
"s_add_u32 m0, %[m0b], 67584\n" \
"s_mov_b32 s61, 458752\n" \
"buffer_load_dwordx4 %[vg0], s[64:67], s61 offen offset:0 lds\n" \
"v_mfma_f32_16x16x32_bf16 v[148:151], a[124:127], a[44:47], v[148:151]\n" \
"v_exp_f32_e32 v143, v143\n" \
"v_mfma_f32_16x16x32_bf16 v[96:99], a[160:163], v[204:207], v[96:99]\n" \
"ds_read_b64_tr_b16 a[160:161], %[vvb] offset:24576\n" \
"v_cmp_gt_u32_e64 s[56:57], 32, v231\n" \
"v_mfma_f32_16x16x32_bf16 v[96:99], a[128:131], v[220:223], v[96:99]\n" \
"v_exp_f32_e64 v144, v144\n" \
"v_mfma_f32_16x16x32_bf16 v[100:103], a[164:167], v[204:207], v[100:103]\n" \
"v_cvt_pk_bf16_f32 v198, v140, v141\n" \
"v_cvt_pk_bf16_f32 v199, v142, v143\n" \
"s_waitcnt vmcnt(8) lgkmcnt(3)\n" \
"v_mfma_f32_16x16x32_bf16 a[208:211], a[228:231], a[200:203], a[208:211]\n" \
"ds_read_b128 a[200:203], v230 offset:384\n" \
"v_mfma_f32_16x16x32_bf16 v[100:103], a[132:135], v[220:223], v[100:103]\n" \
"v_exp_f32_e32 v145, v145\n" \
"v_mfma_f32_16x16x32_bf16 v[104:107], a[168:171], v[204:207], v[104:107]\n" \
"ds_read_b64_tr_b16 a[162:163], %[vvb] offset:28672\n" \
"ds_read_b64_tr_b16 a[164:165], v211 offset:24576\n" \
"v_lshlrev_b32_e64 v232, 2, v224\n" \
"v_mfma_f32_16x16x32_bf16 v[104:107], a[136:139], v[220:223], v[104:107]\n" \
"v_exp_f32_e64 v146, v146\n" \
"v_mfma_f32_16x16x32_bf16 v[108:111], a[172:175], v[204:207], v[108:111]\n" \
"ds_read_b64_tr_b16 a[166:167], v211 offset:28672\n" \
"v_mov_b32_e32 v233, 0x80000000\n" \
"v_mfma_f32_16x16x32_bf16 v[108:111], a[140:143], v[220:223], v[108:111]\n" \
"v_exp_f32_e64 v147, v147\n" \
"v_smfmac_f32_16x16x64_bf16 v[180:183], a[192:195], v[216:223], v228\n" \
"ds_read_b64_tr_b16 a[168:169], %[vvb] offset:25088\n" \
"s_waitcnt vmcnt(6) lgkmcnt(6)\n" \
"v_mfma_f32_16x16x32_bf16 a[208:211], a[232:235], a[204:207], a[208:211]\n" \
"ds_read_b128 a[204:207], v230 offset:448\n" \
"v_mfma_f32_16x16x32_bf16 v[152:155], a[96:99], a[48:51], v[172:175]\n" \
"v_exp_f32_e32 v148, v148\n" \
"v_mfma_f32_16x16x32_bf16 v[152:155], a[100:103], a[52:55], v[152:155]\n" \
"ds_read_b64_tr_b16 a[170:171], %[vvb] offset:29184\n" \
"v_cndmask_b32_e64 v232, v233, v232, s[56:57]\n" \
"v_mfma_f32_16x16x32_bf16 v[152:155], a[104:107], a[56:59], v[152:155]\n" \
"v_exp_f32_e64 v149, v149\n" \
"v_mfma_f32_16x16x32_bf16 v[152:155], a[108:111], a[60:63], v[152:155]\n" \
"v_cvt_pk_bf16_f32 v200, v144, v145\n" \
"v_cvt_pk_bf16_f32 v201, v146, v147\n" \
"v_mfma_f32_16x16x32_bf16 v[156:159], a[112:115], a[48:51], v[172:175]\n" \
"v_exp_f32_e64 v150, v150\n" \
"v_mfma_f32_16x16x32_bf16 v[156:159], a[116:119], a[52:55], v[156:159]\n" \
"ds_read_b64_tr_b16 a[172:173], v211 offset:25088\n" \
"v_mov_b32_e32 v230, 1\n" \
"s_waitcnt vmcnt(5) lgkmcnt(7)\n" \
"v_mfma_f32_16x16x32_bf16 a[208:211], a[236:239], a[200:203], a[208:211]\n" \
"v_mfma_f32_16x16x32_bf16 v[156:159], a[120:123], a[56:59], v[156:159]\n" \
"s_add_u32 m0, %[m0b], 68608\n" \
"s_mov_b32 s61, 688128\n" \
"buffer_load_dwordx4 %[vg0], s[64:67], s61 offen offset:0 lds\n" \
"v_mfma_f32_16x16x32_bf16 v[156:159], a[124:127], a[60:63], v[156:159]\n" \
"v_exp_f32_e64 v151, v151\n" \
"v_mfma_f32_16x16x32_bf16 v[112:115], a[176:179], v[204:207], v[112:115]\n" \
"ds_read_b64_tr_b16 a[174:175], v211 offset:29184\n" \
"ds_read_b64_tr_b16 a[176:177], %[vvb] offset:25600\n" \
"v_mfma_f32_16x16x32_bf16 v[112:115], a[144:147], v[220:223], v[112:115]\n" \
"v_exp_f32_e64 v152, v152\n" \
"v_mfma_f32_16x16x32_bf16 v[116:119], a[180:183], v[204:207], v[116:119]\n" \
"v_cvt_pk_bf16_f32 v202, v148, v149\n" \
"v_cvt_pk_bf16_f32 v203, v150, v151\n" \
"s_waitcnt vmcnt(5) lgkmcnt(4)\n" \
"v_mfma_f32_16x16x32_bf16 a[208:211], a[196:199], a[204:207], a[208:211]\n" \
"v_mfma_f32_16x16x32_bf16 v[116:119], a[148:151], v[220:223], v[116:119]\n" \
"v_exp_f32_e32 v153, v153\n" \
"v_mfma_f32_16x16x32_bf16 v[120:123], a[184:187], v[204:207], v[120:123]\n" \
"v_exp_f32_e64 v154, v154\n" \
"v_mfma_f32_16x16x32_bf16 v[120:123], a[152:155], v[220:223], v[120:123]\n" \
"ds_read_b64_tr_b16 a[178:179], %[vvb] offset:29696\n" \
"buffer_store_dword v230, v232, %[srdz], s55 offen sc1\n" \
"v_mfma_f32_16x16x32_bf16 v[124:127], a[188:191], v[204:207], v[124:127]\n" \
"v_exp_f32_e64 v155, v155\n" \
"v_mfma_f32_16x16x32_bf16 v[124:127], a[156:159], v[220:223], v[124:127]\n" \
"ds_read_b64_tr_b16 a[180:181], v211 offset:25600\n" \
"s_waitcnt lgkmcnt(14)\n" \
"v_mfma_f32_16x16x32_bf16 v[128:131], a[64:67], a[0:3], v[160:163]\n" \
"v_exp_f32_e32 v156, v156\n" \
"v_mfma_f32_16x16x32_bf16 v[128:131], a[68:71], a[4:7], v[128:131]\n" \
"ds_read_b64_tr_b16 a[182:183], v211 offset:29696\n" \
"s_add_u32 m0, %[m0b], 16384\n" \
"s_mov_b32 s49, s48\n" \
"s_mov_b32 s68, 0xaaaaaaaa\n" \
"s_mov_b32 s69, 0xaaaaaaaa\n" \
"v_mfma_f32_16x16x32_bf16 v[128:131], a[72:75], a[8:11], v[128:131]\n" \
"v_exp_f32_e32 v157, v157\n" \
"v_mfma_f32_16x16x32_bf16 v[128:131], a[76:79], a[12:15], v[128:131]\n" \
"v_cvt_pk_bf16_f32 v204, v152, v153\n" \
"v_cvt_pk_bf16_f32 v205, v154, v155\n" \
"v_accvgpr_read_b32 v232, a208\n" \
"v_accvgpr_read_b32 v233, a209\n" \
"v_mfma_f32_16x16x32_bf16 v[132:135], a[80:83], a[0:3], v[160:163]\n" \
"v_exp_f32_e64 v158, v158\n" \
"v_mfma_f32_16x16x32_bf16 v[132:135], a[84:87], a[4:7], v[132:135]\n" \
"ds_read_b64_tr_b16 a[184:185], %[vvb] offset:26112\n" \
"s_lshl_b32 s49, s49, 18\n" \
"s_or_b32 s49, s49, s59\n" \
"v_accvgpr_read_b32 v234, a210\n" \
"v_accvgpr_read_b32 v236, a211\n" \
"s_waitcnt lgkmcnt(5)\n" \
"v_mfma_f32_16x16x32_bf16 v[132:135], a[88:91], a[8:11], v[132:135]\n" \
"buffer_load_dwordx4 %[wo0], %[srdt], s49 offen offset:0 lds\n" \
"v_mfma_f32_16x16x32_bf16 v[132:135], a[92:95], a[12:15], v[132:135]\n" \
"v_exp_f32_e32 v159, v159\n" \
"v_mfma_f32_16x16x32_bf16 v[64:67], a[128:131], v[216:219], v[64:67]\n" \
"ds_read_b64_tr_b16 a[186:187], %[vvb] offset:30208\n" \
"ds_read_b64_tr_b16 a[188:189], v211 offset:26112\n" \
"v_xor_b32_e32 v229, 0x80, %[vx0]\n" \
"v_cndmask_b32_e64 v232, v232, v233, s[68:69]\n" \
"v_cndmask_b32_e64 v234, v234, v236, s[68:69]\n" \
"v_mfma_f32_16x16x32_bf16 v[64:67], a[160:163], v[200:203], v[64:67]\n" \
"v_exp_f32_e64 v128, v128\n" \
"v_mfma_f32_16x16x32_bf16 v[68:71], a[132:135], v[216:219], v[68:71]\n" \
"v_cvt_pk_bf16_f32 v206, v156, v157\n" \
"v_cvt_pk_bf16_f32 v207, v158, v159\n" \
"s_mov_b32 s68, 0xcccccccc\n" \
"s_mov_b32 s69, 0xcccccccc\n" \
"v_mfma_f32_16x16x32_bf16 v[68:71], a[164:167], v[200:203], v[68:71]\n" \
"v_exp_f32_e64 v129, v129\n" \
"v_mfma_f32_16x16x32_bf16 v[72:75], a[136:139], v[216:219], v[72:75]\n" \
"ds_read_b64_tr_b16 a[190:191], v211 offset:30208\n" \
"ds_read_b64_tr_b16 a[128:129], %[vvb] offset:32768\n" \
"v_cndmask_b32_e64 v232, v232, v234, s[68:69]\n" \
"v_and_b32_e32 v230, 0xc0, v235\n" \
"v_mfma_f32_16x16x32_bf16 v[72:75], a[168:171], v[200:203], v[72:75]\n" \
"v_exp_f32_e64 v130, v130\n" \
"v_mfma_f32_16x16x32_bf16 v[76:79], a[140:143], v[216:219], v[76:79]\n" \
"ds_read_b64_tr_b16 a[130:131], %[vvb] offset:36864\n" \
"ds_read_b64_tr_b16 a[132:133], v211 offset:32768\n" \
"v_lshrrev_b32_e64 v233, 7, v224\n" \
"s_mul_i32 s62, %[wid], 8188\n" \
"v_mfma_f32_16x16x32_bf16 v[76:79], a[172:175], v[200:203], v[76:79]\n" \
"v_exp_f32_e64 v131, v131\n" \
"v_smfmac_f32_16x16x64_bf16 v[176:179], a[192:195], v[192:199], v228\n" \
"ds_read_b64_tr_b16 a[134:135], v211 offset:36864\n" \
"ds_read_b64_tr_b16 a[136:137], %[vvb] offset:33280\n" \
"global_load_dwordx2 a[240:241], %[vx0], %[px] offset:0\n" \
"v_mfma_f32_16x16x32_bf16 v[136:139], a[64:67], a[16:19], v[164:167]\n" \
"v_exp_f32_e64 v132, v132\n" \
"v_mfma_f32_16x16x32_bf16 v[136:139], a[68:71], a[20:23], v[136:139]\n" \
"ds_read_b64_tr_b16 a[138:139], %[vvb] offset:37376\n" \
"ds_read_b64_tr_b16 a[140:141], v211 offset:33280\n" \
"v_add_f32_dpp v232, v232, v232 quad_perm:[1,0,3,2] row_mask:0xf bank_mask:0xf\n" \
"v_and_or_b32 v230, v233, 48, v230\n" \
"v_mfma_f32_16x16x32_bf16 v[136:139], a[72:75], a[24:27], v[136:139]\n" \
"v_exp_f32_e64 v133, v133\n" \
"v_mfma_f32_16x16x32_bf16 v[136:139], a[76:79], a[28:31], v[136:139]\n" \
"v_cvt_pk_bf16_f32 v184, v128, v129\n" \
"v_cvt_pk_bf16_f32 v185, v130, v131\n" \
"s_sub_u32 s62, %[m0t], s62\n" \
"v_add_u32_e32 v230, s62, v230\n" \
"v_mfma_f32_16x16x32_bf16 v[140:143], a[80:83], a[16:19], v[164:167]\n" \
"v_exp_f32_e64 v134, v134\n" \
"v_mfma_f32_16x16x32_bf16 v[140:143], a[84:87], a[20:23], v[140:143]\n" \
"ds_read_b64_tr_b16 a[142:143], v211 offset:37376\n" \
"v_add_f32_dpp v232, v232, v232 quad_perm:[2,3,0,1] row_mask:0xf bank_mask:0xf\n" \
"s_waitcnt lgkmcnt(8)\n" \
"v_mfma_f32_16x16x32_bf16 v[140:143], a[88:91], a[24:27], v[140:143]\n" \
"buffer_load_dwordx4 v212, %[srdt], s49 offen offset:1024 lds\n" \
"v_mfma_f32_16x16x32_bf16 v[140:143], a[92:95], a[28:31], v[140:143]\n" \
"v_exp_f32_e32 v135, v135\n" \
"v_mfma_f32_16x16x32_bf16 v[80:83], a[144:147], v[216:219], v[80:83]\n" \
"ds_read_b64_tr_b16 a[144:145], %[vvb] offset:33792\n" \
"ds_read_b64_tr_b16 a[146:147], %[vvb] offset:37888\n" \
"ds_write_b32 v230, v232 offset:7680\n" \
"v_mfma_f32_16x16x32_bf16 v[80:83], a[176:179], v[200:203], v[80:83]\n" \
"v_exp_f32_e64 v136, v136\n" \
"v_mfma_f32_16x16x32_bf16 v[84:87], a[148:151], v[216:219], v[84:87]\n" \
"v_cvt_pk_bf16_f32 v186, v132, v133\n" \
"v_cvt_pk_bf16_f32 v187, v134, v135\n" \
"v_mfma_f32_16x16x32_bf16 v[84:87], a[180:183], v[200:203], v[84:87]\n" \
"v_exp_f32_e64 v137, v137\n" \
"v_mfma_f32_16x16x32_bf16 v[88:91], a[152:155], v[216:219], v[88:91]\n" \
"v_exp_f32_e64 v138, v138\n" \
"v_mfma_f32_16x16x32_bf16 v[88:91], a[184:187], v[200:203], v[88:91]\n" \
"ds_read_b64_tr_b16 a[148:149], v211 offset:33792\n" \
"ds_read_b64_tr_b16 a[150:151], v211 offset:37888\n" \
"v_mfma_f32_16x16x32_bf16 v[92:95], a[156:159], v[216:219], v[92:95]\n" \
"v_exp_f32_e64 v139, v139\n" \
"v_mfma_f32_16x16x32_bf16 v[92:95], a[188:191], v[200:203], v[92:95]\n" \
"ds_read_b64_tr_b16 a[152:153], %[vvb] offset:34304\n" \
"global_load_dwordx2 a[244:245], %[vx0], %[px] offset:1024\n" \
"v_mfma_f32_16x16x32_bf16 v[144:147], a[64:67], a[32:35], v[168:171]\n" \
"v_exp_f32_e64 v140, v140\n" \
"v_mfma_f32_16x16x32_bf16 v[144:147], a[68:71], a[36:39], v[144:147]\n" \
"ds_read_b64_tr_b16 a[154:155], %[vvb] offset:38400\n" \
"ds_read_b64_tr_b16 a[156:157], v211 offset:34304\n" \
"v_mfma_f32_16x16x32_bf16 v[144:147], a[72:75], a[40:43], v[144:147]\n" \
"v_exp_f32_e64 v141, v141\n" \
"v_mfma_f32_16x16x32_bf16 v[144:147], a[76:79], a[44:47], v[144:147]\n" \
"v_cvt_pk_bf16_f32 v188, v136, v137\n" \
"v_cvt_pk_bf16_f32 v189, v138, v139\n" \
"v_mfma_f32_16x16x32_bf16 v[148:151], a[80:83], a[32:35], v[168:171]\n" \
"v_exp_f32_e64 v142, v142\n" \
"v_mfma_f32_16x16x32_bf16 v[148:151], a[84:87], a[36:39], v[148:151]\n" \
"ds_read_b64_tr_b16 a[158:159], v211 offset:38400\n" \
"s_waitcnt lgkmcnt(9)\n" \
"v_mfma_f32_16x16x32_bf16 v[148:151], a[88:91], a[40:43], v[148:151]\n" \
"buffer_load_dwordx4 v213, %[srdt], s49 offen offset:2048 lds\n" \
"v_mfma_f32_16x16x32_bf16 v[148:151], a[92:95], a[44:47], v[148:151]\n" \
"v_exp_f32_e32 v143, v143\n" \
"v_mfma_f32_16x16x32_bf16 v[0:3], a[160:163], v[192:195], v[0:3]\n" \
"ds_read_b128 a[96:99], %[kb0] offset:40960\n" \
"v_mfma_f32_16x16x32_bf16 v[0:3], a[128:131], v[184:187], v[0:3]\n" \
"v_exp_f32_e64 v144, v144\n" \
"v_mfma_f32_16x16x32_bf16 v[4:7], a[164:167], v[192:195], v[4:7]\n" \
"v_cvt_pk_bf16_f32 v190, v140, v141\n" \
"v_cvt_pk_bf16_f32 v191, v142, v143\n" \
"v_mfma_f32_16x16x32_bf16 v[4:7], a[132:135], v[184:187], v[4:7]\n" \
"v_exp_f32_e64 v145, v145\n" \
"v_mfma_f32_16x16x32_bf16 v[8:11], a[168:171], v[192:195], v[8:11]\n" \
"ds_read_b128 a[100:103], v208 offset:40960\n" \
"v_mfma_f32_16x16x32_bf16 v[8:11], a[136:139], v[184:187], v[8:11]\n" \
"v_exp_f32_e64 v146, v146\n" \
"v_mfma_f32_16x16x32_bf16 v[12:15], a[172:175], v[192:195], v[12:15]\n" \
"ds_read_b128 a[104:107], v209 offset:40960\n" \
"v_mfma_f32_16x16x32_bf16 v[12:15], a[140:143], v[184:187], v[12:15]\n" \
"v_exp_f32_e64 v147, v147\n" \
"v_smfmac_f32_16x16x64_bf16 v[180:183], a[192:195], v[200:207], v228\n" \
"ds_read_b128 a[108:111], v210 offset:40960\n" \
"global_load_dwordx2 a[248:249], v229, %[px] offset:0\n" \
"v_mfma_f32_16x16x32_bf16 v[152:155], a[64:67], a[48:51], v[172:175]\n" \
"v_exp_f32_e64 v148, v148\n" \
"v_mfma_f32_16x16x32_bf16 v[152:155], a[68:71], a[52:55], v[152:155]\n" \
"ds_read_b128 a[112:115], %[kb0] offset:45056\n" \
"v_mfma_f32_16x16x32_bf16 v[152:155], a[72:75], a[56:59], v[152:155]\n" \
"v_exp_f32_e64 v149, v149\n" \
"v_mfma_f32_16x16x32_bf16 v[152:155], a[76:79], a[60:63], v[152:155]\n" \
"v_cvt_pk_bf16_f32 v216, v144, v145\n" \
"v_cvt_pk_bf16_f32 v217, v146, v147\n" \
"v_mfma_f32_16x16x32_bf16 v[156:159], a[80:83], a[48:51], v[172:175]\n" \
"v_exp_f32_e64 v150, v150\n" \
"v_mfma_f32_16x16x32_bf16 v[156:159], a[84:87], a[52:55], v[156:159]\n" \
"ds_read_b128 a[116:119], v208 offset:45056\n" \
"s_waitcnt lgkmcnt(6)\n" \
"v_mfma_f32_16x16x32_bf16 v[156:159], a[88:91], a[56:59], v[156:159]\n" \
"buffer_load_dwordx4 v214, %[srdt], s49 offen offset:3072 lds\n" \
"v_mfma_f32_16x16x32_bf16 v[156:159], a[92:95], a[60:63], v[156:159]\n" \
"v_exp_f32_e32 v151, v151\n" \
"v_mfma_f32_16x16x32_bf16 v[16:19], a[176:179], v[192:195], v[16:19]\n" \
"ds_read_b128 a[120:123], v209 offset:45056\n" \
"v_mfma_f32_16x16x32_bf16 v[16:19], a[144:147], v[184:187], v[16:19]\n" \
"v_exp_f32_e64 v152, v152\n" \
"v_mfma_f32_16x16x32_bf16 v[20:23], a[180:183], v[192:195], v[20:23]\n" \
"v_cvt_pk_bf16_f32 v218, v148, v149\n" \
"v_cvt_pk_bf16_f32 v219, v150, v151\n" \
"v_mfma_f32_16x16x32_bf16 v[20:23], a[148:151], v[184:187], v[20:23]\n" \
"v_exp_f32_e64 v153, v153\n" \
"v_mfma_f32_16x16x32_bf16 v[24:27], a[184:187], v[192:195], v[24:27]\n" \
"v_exp_f32_e64 v154, v154\n" \
"v_mfma_f32_16x16x32_bf16 v[24:27], a[152:155], v[184:187], v[24:27]\n" \
"ds_read_b128 a[124:127], v210 offset:45056\n" \
"global_load_dwordx2 a[252:253], v229, %[px] offset:1024\n" \
"v_mfma_f32_16x16x32_bf16 v[28:31], a[188:191], v[192:195], v[28:31]\n" \
"v_exp_f32_e64 v155, v155\n" \
"v_mfma_f32_16x16x32_bf16 v[28:31], a[156:159], v[184:187], v[28:31]\n" \
"s_waitcnt vmcnt(63) lgkmcnt(2)\n" \
"s_barrier\n" \
"v_mfma_f32_16x16x32_bf16 v[128:131], a[96:99], a[0:3], v[160:163]\n" \
"v_exp_f32_e64 v156, v156\n" \
"s_lshl_b32 s62, %[wid], 13\n" \
"s_sub_u32 s62, %[m0t], s62\n" \
"v_mfma_f32_16x16x32_bf16 v[128:131], a[100:103], a[4:7], v[128:131]\n" \
"v_lshrrev_b32_e64 v227, 5, v231\n" \
"v_and_b32_e32 v230, 0xf0, v235\n" \
"v_add_u32_e64 v230, s62, v230\n" \
"v_mfma_f32_16x16x32_bf16 v[128:131], a[104:107], a[8:11], v[128:131]\n" \
"v_exp_f32_e64 v157, v157\n" \
"v_mfma_f32_16x16x32_bf16 v[128:131], a[108:111], a[12:15], v[128:131]\n" \
"v_cvt_pk_bf16_f32 v220, v152, v153\n" \
"v_cvt_pk_bf16_f32 v221, v154, v155\n" \
"ds_read_b128 v[232:235], v230 offset:7680\n" \
"v_readlane_b32 s51, %[thi], 31\n" \
"v_mfma_f32_16x16x32_bf16 v[132:135], a[112:115], a[0:3], v[160:163]\n" \
"v_exp_f32_e64 v158, v158\n" \
"v_mfma_f32_16x16x32_bf16 v[132:135], a[116:119], a[4:7], v[132:135]\n" \
"v_lshlrev_b32_e32 v227, 10, v227\n" \
"s_waitcnt lgkmcnt(1)\n" \
"v_mfma_f32_16x16x32_bf16 v[132:135], a[120:123], a[8:11], v[132:135]\n" \
"s_add_u32 m0, %[m0b], 81920\n" \
"s_mov_b32 s61, 917504\n" \
"buffer_load_dwordx4 %[vg0], s[64:67], s61 offen offset:0 lds\n" \
"v_mfma_f32_16x16x32_bf16 v[132:135], a[124:127], a[12:15], v[132:135]\n" \
"v_exp_f32_e64 v159, v159\n" \
"v_mfma_f32_16x16x32_bf16 v[32:35], a[160:163], v[196:199], v[32:35]\n" \
"ds_read_b128 a[64:67], %[kb0] offset:49152\n" \
"v_add_u32_e32 v227, 0x10000, v227\n" \
"v_mfma_f32_16x16x32_bf16 v[32:35], a[128:131], v[188:191], v[32:35]\n" \
"v_exp_f32_e64 v128, v128\n" \
"v_mfma_f32_16x16x32_bf16 v[36:39], a[164:167], v[196:199], v[36:39]\n" \
"v_cvt_pk_bf16_f32 v222, v156, v157\n" \
"v_cvt_pk_bf16_f32 v223, v158, v159\n" \
"s_waitcnt lgkmcnt(1)\n" \
"v_add_f32_e32 v232, v232, v233\n" \
"v_add_f32_e64 v234, v234, v235\n" \
"v_mfma_f32_16x16x32_bf16 v[36:39], a[132:135], v[188:191], v[36:39]\n" \
"v_exp_f32_e64 v129, v129\n" \
"v_mfma_f32_16x16x32_bf16 v[40:43], a[168:171], v[196:199], v[40:43]\n" \
"ds_read_b128 a[68:71], v208 offset:49152\n" \
"v_sub_u32_e32 v215, %[vvb], v227\n" \
"v_add_f32_e32 v232, v232, v234\n" \
"v_lshrrev_b32_e64 v230, 6, v224\n" \
"v_mfma_f32_16x16x32_bf16 v[40:43], a[136:139], v[188:191], v[40:43]\n" \
"v_exp_f32_e64 v130, v130\n" \
"v_mfma_f32_16x16x32_bf16 v[44:47], a[172:175], v[196:199], v[44:47]\n" \
"ds_read_b128 a[72:75], v209 offset:49152\n" \
"v_xor_b32_e64 v227, 32, v215\n" \
"v_mov_b32_dpp v233, v232 quad_perm:[1,0,3,2] row_mask:0xf bank_mask:0xf\n" \
"v_mfma_f32_16x16x32_bf16 v[44:47], a[140:143], v[188:191], v[44:47]\n" \
"v_exp_f32_e64 v131, v131\n" \
"v_smfmac_f32_16x16x64_bf16 v[176:179], a[192:195], v[184:191], v228\n" \
"global_load_dwordx2 a[242:243], %[vx0], %[px] offset:64\n" \
"v_mfma_f32_16x16x32_bf16 v[136:139], a[96:99], a[16:19], v[164:167]\n" \
"v_exp_f32_e64 v132, v132\n" \
"v_mfma_f32_16x16x32_bf16 v[136:139], a[100:103], a[20:23], v[136:139]\n" \
"ds_read_b128 a[76:79], v210 offset:49152\n" \
"v_cvt_pk_bf16_f32 v232, v232, v233\n" \
"v_mfma_f32_16x16x32_bf16 v[136:139], a[104:107], a[24:27], v[136:139]\n" \
"v_exp_f32_e64 v133, v133\n" \
"v_mfma_f32_16x16x32_bf16 v[136:139], a[108:111], a[28:31], v[136:139]\n" \
"v_cvt_pk_bf16_f32 v192, v128, v129\n" \
"v_cvt_pk_bf16_f32 v193, v130, v131\n" \
"s_cmp_eq_u32 %[wid], 0\n" \
"s_cselect_b32 s51, s51, 0x80000000\n" \
"s_mov_b64 exec, 0x5555\n" \
"buffer_store_dword v232, v230, %[srdz], s51 offen sc1\n" \
"s_mov_b64 exec, -1\n" \
"v_mfma_f32_16x16x32_bf16 v[140:143], a[112:115], a[16:19], v[164:167]\n" \
"v_exp_f32_e64 v134, v134\n" \
"v_mfma_f32_16x16x32_bf16 v[140:143], a[116:119], a[20:23], v[140:143]\n" \
"ds_read_b128 a[80:83], %[kb0] offset:53248\n" \
"v_mfma_f32_16x16x32_bf16 v[140:143], a[120:123], a[24:27], v[140:143]\n" \
"s_add_u32 m0, %[m0b], 82944\n" \
"s_mov_b32 s61, 1146880\n" \
"buffer_load_dwordx4 %[vg0], s[64:67], s61 offen offset:0 lds\n" \
"v_mfma_f32_16x16x32_bf16 v[140:143], a[124:127], a[28:31], v[140:143]\n" \
"v_exp_f32_e64 v135, v135\n" \
"v_mfma_f32_16x16x32_bf16 v[48:51], a[176:179], v[196:199], v[48:51]\n" \
"ds_read_b128 a[84:87], v208 offset:53248\n" \
"v_mfma_f32_16x16x32_bf16 v[48:51], a[144:147], v[188:191], v[48:51]\n" \
"v_exp_f32_e64 v136, v136\n" \
"v_mfma_f32_16x16x32_bf16 v[52:55], a[180:183], v[196:199], v[52:55]\n" \
"v_cvt_pk_bf16_f32 v194, v132, v133\n" \
"v_cvt_pk_bf16_f32 v195, v134, v135\n" \
"v_mfma_f32_16x16x32_bf16 v[52:55], a[148:151], v[188:191], v[52:55]\n" \
"v_exp_f32_e64 v137, v137\n" \
"v_mfma_f32_16x16x32_bf16 v[56:59], a[184:187], v[196:199], v[56:59]\n" \
"v_exp_f32_e64 v138, v138\n" \
"v_mfma_f32_16x16x32_bf16 v[56:59], a[152:155], v[188:191], v[56:59]\n" \
"ds_read_b128 a[88:91], v209 offset:53248\n" \
"v_mfma_f32_16x16x32_bf16 v[60:63], a[188:191], v[196:199], v[60:63]\n" \
"v_exp_f32_e64 v139, v139\n" \
"v_mfma_f32_16x16x32_bf16 v[60:63], a[156:159], v[188:191], v[60:63]\n" \
"global_load_dwordx2 a[246:247], %[vx0], %[px] offset:1088\n" \
"v_mfma_f32_16x16x32_bf16 v[144:147], a[96:99], a[32:35], v[168:171]\n" \
"v_exp_f32_e64 v140, v140\n" \
"v_mfma_f32_16x16x32_bf16 v[144:147], a[100:103], a[36:39], v[144:147]\n" \
"ds_read_b128 a[92:95], v210 offset:53248\n" \
"v_lshrrev_b32_e64 v236, 3, v224\n" \
"v_and_b32_e32 v230, 0xb0, v236\n" \
"v_mfma_f32_16x16x32_bf16 v[144:147], a[104:107], a[40:43], v[144:147]\n" \
"v_exp_f32_e64 v141, v141\n" \
"v_mfma_f32_16x16x32_bf16 v[144:147], a[108:111], a[44:47], v[144:147]\n" \
"v_cvt_pk_bf16_f32 v196, v136, v137\n" \
"v_cvt_pk_bf16_f32 v197, v138, v139\n" \
"v_lshrrev_b32_e64 v236, 2, v236\n" \
"v_and_or_b32 v230, v236, 64, v230\n" \
"v_mfma_f32_16x16x32_bf16 v[148:151], a[112:115], a[32:35], v[168:171]\n" \
"v_exp_f32_e64 v142, v142\n" \
"v_mfma_f32_16x16x32_bf16 v[148:151], a[116:119], a[36:39], v[148:151]\n" \
"v_lshrrev_b32_e64 v236, 12, v224\n" \
"v_lshl_or_b32 v230, v236, 13, v230\n" \
"v_mfma_f32_16x16x32_bf16 v[148:151], a[120:123], a[40:43], v[148:151]\n" \
"s_add_u32 m0, %[m0b], 83968\n" \
"s_mov_b32 s61, 1376256\n" \
"buffer_load_dwordx4 %[vg0], s[64:67], s61 offen offset:0 lds\n" \
"v_mfma_f32_16x16x32_bf16 v[148:151], a[124:127], a[44:47], v[148:151]\n" \
".if . - adaln_body != 36864\n" \
".error \"size model\"\n" \
".endif\n" \
"Lk8_%=:\n" \
"s_cbranch_execz Lk9_%=\n" \
"s_nop 0\n" \
"v_exp_f32_e64 v143, v143\n" \
"v_readlane_b32 s51, %[thi], 26\n" \
"v_add_u32_e64 v230, s62, v230\n" \
"v_mfma_f32_16x16x32_bf16 v[96:99], a[160:163], v[204:207], v[96:99]\n" \
"ds_read_b64_tr_b16 a[160:161], %[vvb] offset:40960\n" \
"v_mfma_f32_16x16x32_bf16 v[96:99], a[128:131], v[220:223], v[96:99]\n" \
"v_exp_f32_e64 v144, v144\n" \
"v_mfma_f32_16x16x32_bf16 v[100:103], a[164:167], v[204:207], v[100:103]\n" \
"v_cvt_pk_bf16_f32 v198, v140, v141\n" \
"v_cvt_pk_bf16_f32 v199, v142, v143\n" \
"v_add_u32_e64 v230, s51, v230\n" \
"v_mfma_f32_16x16x32_bf16 v[100:103], a[132:135], v[220:223], v[100:103]\n" \
"v_exp_f32_e64 v145, v145\n" \
"v_mfma_f32_16x16x32_bf16 v[104:107], a[168:171], v[204:207], v[104:107]\n" \
"ds_read_b64_tr_b16 a[162:163], %[vvb] offset:45056\n" \
"ds_read_b64_tr_b16 a[164:165], v211 offset:40960\n" \
"v_xor_b32_e32 v236, 0x100, v230\n" \
"v_mfma_f32_16x16x32_bf16 v[104:107], a[136:139], v[220:223], v[104:107]\n" \
"v_exp_f32_e64 v146, v146\n" \
"v_mfma_f32_16x16x32_bf16 v[108:111], a[172:175], v[204:207], v[108:111]\n" \
"ds_read_b64_tr_b16 a[166:167], v211 offset:45056\n" \
"ds_read_b128 a[212:215], v230 offset:6656\n" \
"v_mfma_f32_16x16x32_bf16 v[108:111], a[140:143], v[220:223], v[108:111]\n" \
"v_exp_f32_e64 v147, v147\n" \
"v_smfmac_f32_16x16x64_bf16 v[180:183], a[192:195], v[216:223], v228\n" \
"ds_read_b64_tr_b16 a[168:169], %[vvb] offset:41472\n" \
"global_load_dwordx2 a[250:251], v229, %[px] offset:64\n" \
"ds_read_b128 a[216:219], v230 offset:23040\n" \
"v_mfma_f32_16x16x32_bf16 v[152:155], a[96:99], a[48:51], v[172:175]\n" \
"v_exp_f32_e64 v148, v148\n" \
"v_mfma_f32_16x16x32_bf16 v[152:155], a[100:103], a[52:55], v[152:155]\n" \
"ds_read_b64_tr_b16 a[170:171], %[vvb] offset:45568\n" \
"ds_read_b128 a[220:223], v236 offset:6656\n" \
"v_mfma_f32_16x16x32_bf16 v[152:155], a[104:107], a[56:59], v[152:155]\n" \
"v_exp_f32_e64 v149, v149\n" \
"v_mfma_f32_16x16x32_bf16 v[152:155], a[108:111], a[60:63], v[152:155]\n" \
"v_cvt_pk_bf16_f32 v200, v144, v145\n" \
"v_cvt_pk_bf16_f32 v201, v146, v147\n" \
"ds_read_b128 a[224:227], v236 offset:23040\n" \
"v_mfma_f32_16x16x32_bf16 v[156:159], a[112:115], a[48:51], v[172:175]\n" \
"v_exp_f32_e64 v150, v150\n" \
"v_mfma_f32_16x16x32_bf16 v[156:159], a[116:119], a[52:55], v[156:159]\n" \
"ds_read_b64_tr_b16 a[172:173], v211 offset:41472\n" \
"v_mfma_f32_16x16x32_bf16 v[156:159], a[120:123], a[56:59], v[156:159]\n" \
"s_add_u32 m0, %[m0b], 84992\n" \
"s_mov_b32 s61, 1605632\n" \
"buffer_load_dwordx4 %[vg0], s[64:67], s61 offen offset:0 lds\n" \
"v_mfma_f32_16x16x32_bf16 v[156:159], a[124:127], a[60:63], v[156:159]\n" \
"v_exp_f32_e64 v151, v151\n" \
"v_mfma_f32_16x16x32_bf16 v[112:115], a[176:179], v[204:207], v[112:115]\n" \
"ds_read_b64_tr_b16 a[174:175], v211 offset:45568\n" \
"ds_read_b64_tr_b16 a[176:177], %[vvb] offset:41984\n" \
"v_mfma_f32_16x16x32_bf16 v[112:115], a[144:147], v[220:223], v[112:115]\n" \
"v_exp_f32_e64 v152, v152\n" \
"v_mfma_f32_16x16x32_bf16 v[116:119], a[180:183], v[204:207], v[116:119]\n" \
"v_cvt_pk_bf16_f32 v202, v148, v149\n" \
"v_cvt_pk_bf16_f32 v203, v150, v151\n" \
"v_mfma_f32_16x16x32_bf16 v[116:119], a[148:151], v[220:223], v[116:119]\n" \
"v_exp_f32_e64 v153, v153\n" \
"v_mfma_f32_16x16x32_bf16 v[120:123], a[184:187], v[204:207], v[120:123]\n" \
"v_exp_f32_e64 v154, v154\n" \
"v_mfma_f32_16x16x32_bf16 v[120:123], a[152:155], v[220:223], v[120:123]\n" \
"ds_read_b64_tr_b16 a[178:179], %[vvb] offset:46080\n" \
"global_load_dwordx2 a[254:255], v229, %[px] offset:1088\n" \
"v_mfma_f32_16x16x32_bf16 v[124:127], a[188:191], v[204:207], v[124:127]\n" \
"v_exp_f32_e64 v155, v155\n" \
"v_mfma_f32_16x16x32_bf16 v[124:127], a[156:159], v[220:223], v[124:127]\n" \
"ds_read_b64_tr_b16 a[180:181], v211 offset:41984\n" \
"s_waitcnt lgkmcnt(14)\n" \
"v_mfma_f32_16x16x32_bf16 v[128:131], a[64:67], a[0:3], v[160:163]\n" \
"v_exp_f32_e32 v156, v156\n" \
"v_mfma_f32_16x16x32_bf16 v[128:131], a[68:71], a[4:7], v[128:131]\n" \
"ds_read_b64_tr_b16 a[182:183], v211 offset:46080\n" \
"s_add_u32 m0, %[m0b], 32768\n" \
"s_cmp_gt_u32 s48, 0\n" \
"v_mfma_f32_16x16x32_bf16 v[128:131], a[72:75], a[8:11], v[128:131]\n" \
"v_exp_f32_e32 v157, v157\n" \
"v_mfma_f32_16x16x32_bf16 v[128:131], a[76:79], a[12:15], v[128:131]\n" \
"v_cvt_pk_bf16_f32 v204, v152, v153\n" \
"v_cvt_pk_bf16_f32 v205, v154, v155\n" \
"v_mfma_f32_16x16x32_bf16 v[132:135], a[80:83], a[0:3], v[160:163]\n" \
"v_exp_f32_e64 v158, v158\n" \
"v_mfma_f32_16x16x32_bf16 v[132:135], a[84:87], a[4:7], v[132:135]\n" \
"ds_read_b64_tr_b16 a[184:185], %[vvb] offset:42496\n" \
"s_cselect_b32 s49, 0, 1\n" \
"s_lshl_b32 s49, s49, 18\n" \
"s_or_b32 s49, s49, s58\n" \
"s_waitcnt lgkmcnt(5)\n" \
"v_mfma_f32_16x16x32_bf16 v[132:135], a[88:91], a[8:11], v[132:135]\n" \
"buffer_load_dwordx4 %[wo0], %[srdt], s49 offen offset:0 lds\n" \
"v_mfma_f32_16x16x32_bf16 v[132:135], a[92:95], a[12:15], v[132:135]\n" \
"v_exp_f32_e64 v159, v159\n" \
"v_mfma_f32_16x16x32_bf16 v[64:67], a[128:131], v[216:219], v[64:67]\n" \
"ds_read_b64_tr_b16 a[186:187], %[vvb] offset:46592\n" \
"ds_read_b64_tr_b16 a[188:189], v211 offset:42496\n" \
"v_mfma_f32_16x16x32_bf16 v[64:67], a[160:163], v[200:203], v[64:67]\n" \
"v_exp_f32_e64 v128, v128\n" \
"v_mfma_f32_16x16x32_bf16 v[68:71], a[132:135], v[216:219], v[68:71]\n" \
"v_cvt_pk_bf16_f32 v206, v156, v157\n" \
"v_cvt_pk_bf16_f32 v207, v158, v159\n" \
"v_mfma_f32_16x16x32_bf16 v[68:71], a[164:167], v[200:203], v[68:71]\n" \
"v_exp_f32_e64 v129, v129\n" \
"v_mfma_f32_16x16x32_bf16 v[72:75], a[136:139], v[216:219], v[72:75]\n" \
"ds_read_b64_tr_b16 a[190:191], v211 offset:46592\n" \
"ds_read_b64_tr_b16 a[128:129], %[vvb] offset:49152\n" \
"v_mfma_f32_16x16x32_bf16 v[72:75], a[168:171], v[200:203], v[72:75]\n" \
"v_exp_f32_e64 v130, v130\n" \
"v_mfma_f32_16x16x32_bf16 v[76:79], a[140:143], v[216:219], v[76:79]\n" \
"ds_read_b64_tr_b16 a[130:131], %[vvb] offset:53248\n" \
"ds_read_b64_tr_b16 a[132:133], v211 offset:49152\n" \
"v_mfma_f32_16x16x32_bf16 v[76:79], a[172:175], v[200:203], v[76:79]\n" \
"v_exp_f32_e64 v131, v131\n" \
"v_smfmac_f32_16x16x64_bf16 v[176:179], a[192:195], v[192:199], v228\n" \
"ds_read_b64_tr_b16 a[134:135], v211 offset:53248\n" \
"ds_read_b64_tr_b16 a[136:137], %[vvb] offset:49664\n" \
"v_mfma_f32_16x16x32_bf16 v[136:139], a[64:67], a[16:19], v[164:167]\n" \
"v_exp_f32_e64 v132, v132\n" \
"v_mfma_f32_16x16x32_bf16 v[136:139], a[68:71], a[20:23], v[136:139]\n" \
"ds_read_b64_tr_b16 a[138:139], %[vvb] offset:53760\n" \
"ds_read_b64_tr_b16 a[140:141], v211 offset:49664\n" \
"v_mfma_f32_16x16x32_bf16 v[136:139], a[72:75], a[24:27], v[136:139]\n" \
"v_exp_f32_e64 v133, v133\n" \
"v_mfma_f32_16x16x32_bf16 v[136:139], a[76:79], a[28:31], v[136:139]\n" \
"v_cvt_pk_bf16_f32 v184, v128, v129\n" \
"v_cvt_pk_bf16_f32 v185, v130, v131\n" \
"v_mfma_f32_16x16x32_bf16 v[140:143], a[80:83], a[16:19], v[164:167]\n" \
"v_exp_f32_e64 v134, v134\n" \
"v_mfma_f32_16x16x32_bf16 v[140:143], a[84:87], a[20:23], v[140:143]\n" \
"ds_read_b64_tr_b16 a[142:143], v211 offset:53760\n" \
"s_waitcnt lgkmcnt(8)\n" \
"v_mfma_f32_16x16x32_bf16 v[140:143], a[88:91], a[24:27], v[140:143]\n" \
"buffer_load_dwordx4 v212, %[srdt], s49 offen offset:1024 lds\n" \
"v_mfma_f32_16x16x32_bf16 v[140:143], a[92:95], a[28:31], v[140:143]\n" \
"v_exp_f32_e32 v135, v135\n" \
"v_mfma_f32_16x16x32_bf16 v[80:83], a[144:147], v[216:219], v[80:83]\n" \
"ds_read_b64_tr_b16 a[144:145], %[vvb] offset:50176\n" \
"ds_read_b64_tr_b16 a[146:147], %[vvb] offset:54272\n" \
"v_mfma_f32_16x16x32_bf16 v[80:83], a[176:179], v[200:203], v[80:83]\n" \
"v_exp_f32_e64 v136, v136\n" \
"v_mfma_f32_16x16x32_bf16 v[84:87], a[148:151], v[216:219], v[84:87]\n" \
"v_cvt_pk_bf16_f32 v186, v132, v133\n" \
"v_cvt_pk_bf16_f32 v187, v134, v135\n" \
"v_mfma_f32_16x16x32_bf16 v[84:87], a[180:183], v[200:203], v[84:87]\n" \
"v_exp_f32_e64 v137, v137\n" \
"v_mfma_f32_16x16x32_bf16 v[88:91], a[152:155], v[216:219], v[88:91]\n" \
"v_exp_f32_e64 v138, v138\n" \
"v_mfma_f32_16x16x32_bf16 v[88:91], a[184:187], v[200:203], v[88:91]\n" \
"ds_read_b64_tr_b16 a[148:149], v211 offset:50176\n" \
"ds_read_b64_tr_b16 a[150:151], v211 offset:54272\n" \
"v_readlane_b32 s64, %[tlo], 9\n" \
"v_mfma_f32_16x16x32_bf16 v[92:95], a[156:159], v[216:219], v[92:95]\n" \
"v_exp_f32_e64 v139, v139\n" \
"v_mfma_f32_16x16x32_bf16 v[92:95], a[188:191], v[200:203], v[92:95]\n" \
"ds_read_b64_tr_b16 a[152:153], %[vvb] offset:50688\n" \
"v_readlane_b32 s65, %[thi], 9\n" \
"v_mfma_f32_16x16x32_bf16 v[144:147], a[64:67], a[32:35], v[168:171]\n" \
"v_exp_f32_e64 v140, v140\n" \
"v_mfma_f32_16x16x32_bf16 v[144:147], a[68:71], a[36:39], v[144:147]\n" \
"ds_read_b64_tr_b16 a[154:155], %[vvb] offset:54784\n" \
"ds_read_b64_tr_b16 a[156:157], v211 offset:50688\n" \
"v_mfma_f32_16x16x32_bf16 v[144:147], a[72:75], a[40:43], v[144:147]\n" \
"v_exp_f32_e64 v141, v141\n" \
"v_mfma_f32_16x16x32_bf16 v[144:147], a[76:79], a[44:47], v[144:147]\n" \
"v_cvt_pk_bf16_f32 v188, v136, v137\n" \
"v_cvt_pk_bf16_f32 v189, v138, v139\n" \
"v_mfma_f32_16x16x32_bf16 v[148:151], a[80:83], a[32:35], v[168:171]\n" \
"v_exp_f32_e64 v142, v142\n" \
"v_mfma_f32_16x16x32_bf16 v[148:151], a[84:87], a[36:39], v[148:151]\n" \
"ds_read_b64_tr_b16 a[158:159], v211 offset:54784\n" \
"s_waitcnt lgkmcnt(12)\n" \
"v_mfma_f32_16x16x32_bf16 v[148:151], a[88:91], a[40:43], v[148:151]\n" \
"buffer_load_dwordx4 v213, %[srdt], s49 offen offset:2048 lds\n" \
"v_mfma_f32_16x16x32_bf16 v[148:151], a[92:95], a[44:47], v[148:151]\n" \
"v_exp_f32_e32 v143, v143\n" \
"v_mfma_f32_16x16x32_bf16 v[0:3], a[160:163], v[192:195], v[0:3]\n" \
"ds_read_b128 a[96:99], %[kb0] offset:57344\n" \
"v_mfma_f32_16x16x32_bf16 v[0:3], a[128:131], v[184:187], v[0:3]\n" \
"v_exp_f32_e64 v144, v144\n" \
"v_mfma_f32_16x16x32_bf16 v[4:7], a[164:167], v[192:195], v[4:7]\n" \
"v_cvt_pk_bf16_f32 v190, v140, v141\n" \
"v_cvt_pk_bf16_f32 v191, v142, v143\n" \
"v_mfma_f32_16x16x32_bf16 v[4:7], a[132:135], v[184:187], v[4:7]\n" \
"v_exp_f32_e64 v145, v145\n" \
"v_mfma_f32_16x16x32_bf16 v[8:11], a[168:171], v[192:195], v[8:11]\n" \
"ds_read_b128 a[100:103], v208 offset:57344\n" \
"s_waitcnt vmcnt(3) lgkmcnt(10)\n" \
"v_mfma_f32_16x16x32_bf16 v[232:235], a[240:243], a[212:215], 0\n" \
"v_mfma_f32_16x16x32_bf16 v[8:11], a[136:139], v[184:187], v[8:11]\n" \
"v_exp_f32_e32 v146, v146\n" \
"v_mfma_f32_16x16x32_bf16 v[12:15], a[172:175], v[192:195], v[12:15]\n" \
"ds_read_b128 a[104:107], v209 offset:57344\n" \
"v_mfma_f32_16x16x32_bf16 v[232:235], a[244:247], a[216:219], v[232:235]\n" \
"v_mfma_f32_16x16x32_bf16 v[12:15], a[140:143], v[184:187], v[12:15]\n" \
"v_exp_f32_e64 v147, v147\n" \
"v_smfmac_f32_16x16x64_bf16 v[180:183], a[192:195], v[200:207], v228\n" \
"ds_read_b128 a[108:111], v210 offset:57344\n" \
"buffer_load_dword v229, v226, %[srdz], s50 offen sc1\n" \
"v_mfma_f32_16x16x32_bf16 v[232:235], a[248:251], a[220:223], v[232:235]\n" \
"v_mfma_f32_16x16x32_bf16 v[152:155], a[64:67], a[48:51], v[172:175]\n" \
"v_exp_f32_e64 v148, v148\n" \
"v_mfma_f32_16x16x32_bf16 v[152:155], a[68:71], a[52:55], v[152:155]\n" \
"ds_read_b128 a[112:115], %[kb0] offset:61440\n" \
"v_mfma_f32_16x16x32_bf16 v[232:235], a[252:255], a[224:227], v[232:235]\n" \
"v_mfma_f32_16x16x32_bf16 v[152:155], a[72:75], a[56:59], v[152:155]\n" \
"v_exp_f32_e64 v149, v149\n" \
"v_mfma_f32_16x16x32_bf16 v[152:155], a[76:79], a[60:63], v[152:155]\n" \
"v_cvt_pk_bf16_f32 v216, v144, v145\n" \
"v_cvt_pk_bf16_f32 v217, v146, v147\n" \
"v_mfma_f32_16x16x32_bf16 v[156:159], a[80:83], a[48:51], v[172:175]\n" \
"v_exp_f32_e64 v150, v150\n" \
"v_mfma_f32_16x16x32_bf16 v[156:159], a[84:87], a[52:55], v[156:159]\n" \
"ds_read_b128 a[116:119], v208 offset:61440\n" \
"s_mov_b32 s68, 0xaaaaaaaa\n" \
"s_mov_b32 s69, 0xaaaaaaaa\n" \
"s_waitcnt lgkmcnt(6)\n" \
"v_mfma_f32_16x16x32_bf16 v[156:159], a[88:91], a[56:59], v[156:159]\n" \
"buffer_load_dwordx4 v214, %[srdt], s49 offen offset:3072 lds\n" \
"v_mfma_f32_16x16x32_bf16 v[156:159], a[92:95], a[60:63], v[156:159]\n" \
"v_exp_f32_e32 v151, v151\n" \
"v_cndmask_b32_e64 v232, v232, v233, s[68:69]\n" \
"v_cndmask_b32_e64 v234, v234, v235, s[68:69]\n" \
"v_mfma_f32_16x16x32_bf16 v[16:19], a[176:179], v[192:195], v[16:19]\n" \
"ds_read_b128 a[120:123], v209 offset:61440\n" \
"v_mfma_f32_16x16x32_bf16 v[16:19], a[144:147], v[184:187], v[16:19]\n" \
"v_exp_f32_e64 v152, v152\n" \
"s_mov_b32 s68, 0xcccccccc\n" \
"s_mov_b32 s69, 0xcccccccc\n" \
"v_mfma_f32_16x16x32_bf16 v[20:23], a[180:183], v[192:195], v[20:23]\n" \
"v_cvt_pk_bf16_f32 v218, v148, v149\n" \
"v_cvt_pk_bf16_f32 v219, v150, v151\n" \
"v_mfma_f32_16x16x32_bf16 v[20:23], a[148:151], v[184:187], v[20:23]\n" \
"v_exp_f32_e64 v153, v153\n" \
"v_cndmask_b32_e64 v232, v232, v234, s[68:69]\n" \
"v_mfma_f32_16x16x32_bf16 v[24:27], a[184:187], v[192:195], v[24:27]\n" \
"v_exp_f32_e64 v154, v154\n" \
"s_mov_b32 s68, 0xf0000f\n" \
"s_mov_b32 s69, 0xf0000f00\n" \
"v_mfma_f32_16x16x32_bf16 v[24:27], a[152:155], v[184:187], v[24:27]\n" \
"ds_read_b128 a[124:127], v210 offset:61440\n" \
"v_mfma_f32_16x16x32_bf16 v[28:31], a[188:191], v[192:195], v[28:31]\n" \
"v_exp_f32_e64 v155, v155\n" \
"v_cndmask_b32_e64 %[rd0], 0, v232, s[68:69]\n" \
"v_mfma_f32_16x16x32_bf16 v[28:31], a[156:159], v[184:187], v[28:31]\n" \
"s_waitcnt vmcnt(27) lgkmcnt(2)\n" \
"s_barrier\n" \
"v_mfma_f32_16x16x32_bf16 v[128:131], a[96:99], a[0:3], v[160:163]\n" \
"v_exp_f32_e64 v156, v156\n" \
"s_mov_b32 m0, s44\n" \
"s_addk_i32 s44, 0x400\n" \
"buffer_load_dwordx4 %[vg0], s[64:67], s45 offen offset:0 lds\n" \
"s_add_u32 s45, s45, 229376\n" \
"v_mfma_f32_16x16x32_bf16 v[128:131], a[100:103], a[4:7], v[128:131]\n" \
"s_add_u32 m0, %[m0b], 98304\n" \
"s_cmp_gt_u32 s48, 0\n" \
"v_mfma_f32_16x16x32_bf16 v[128:131], a[104:107], a[8:11], v[128:131]\n" \
"v_exp_f32_e32 v157, v157\n" \
"v_mfma_f32_16x16x32_bf16 v[128:131], a[108:111], a[12:15], v[128:131]\n" \
"v_cvt_pk_bf16_f32 v220, v152, v153\n" \
"v_cvt_pk_bf16_f32 v221, v154, v155\n" \
"v_mfma_f32_16x16x32_bf16 v[132:135], a[112:115], a[0:3], v[160:163]\n" \
"v_exp_f32_e64 v158, v158\n" \
"v_mfma_f32_16x16x32_bf16 v[132:135], a[116:119], a[4:7], v[132:135]\n" \
"s_cselect_b32 s49, 0, 1\n" \
"s_lshl_b32 s49, s49, 18\n" \
"s_or_b32 s49, s49, s59\n" \
"s_waitcnt lgkmcnt(0)\n" \
"v_mfma_f32_16x16x32_bf16 v[132:135], a[120:123], a[8:11], v[132:135]\n" \
"buffer_load_dwordx4 %[wo0], %[srdt], s49 offen offset:0 lds\n" \
"v_mfma_f32_16x16x32_bf16 v[132:135], a[124:127], a[12:15], v[132:135]\n" \
"v_exp_f32_e64 v159, v159\n" \
"v_mfma_f32_16x16x32_bf16 v[32:35], a[160:163], v[196:199], v[32:35]\n" \
"ds_read_b64_tr_b16 a[196:197], v215 offset:0\n" \
"v_mfma_f32_16x16x32_bf16 v[32:35], a[128:131], v[188:191], v[32:35]\n" \
"v_exp_f32_e64 v128, v128\n" \
"v_mfma_f32_16x16x32_bf16 v[36:39], a[164:167], v[196:199], v[36:39]\n" \
"v_cvt_pk_bf16_f32 v222, v156, v157\n" \
"v_cvt_pk_bf16_f32 v223, v158, v159\n" \
"v_mfma_f32_16x16x32_bf16 v[36:39], a[132:135], v[188:191], v[36:39]\n" \
"v_exp_f32_e64 v129, v129\n" \
"v_mfma_f32_16x16x32_bf16 v[40:43], a[168:171], v[196:199], v[40:43]\n" \
"ds_read_b64_tr_b16 a[198:199], v215 offset:2048\n" \
"v_mfma_f32_16x16x32_bf16 v[40:43], a[136:139], v[188:191], v[40:43]\n" \
"v_exp_f32_e64 v130, v130\n" \
"v_mfma_f32_16x16x32_bf16 v[44:47], a[172:175], v[196:199], v[44:47]\n" \
"ds_read_b64_tr_b16 a[200:201], v215 offset:4096\n" \
"v_readlane_b32 s62, %[tlo], 32\n" \
"v_lshrrev_b32_e64 v237, 7, v224\n" \
"v_mfma_f32_16x16x32_bf16 v[44:47], a[140:143], v[188:191], v[44:47]\n" \
"v_exp_f32_e64 v131, v131\n" \
"v_smfmac_f32_16x16x64_bf16 v[176:179], a[192:195], v[184:191], v228\n" \
"v_and_b32_e64 v237, 48, v237\n" \
"v_mfma_f32_16x16x32_bf16 v[136:139], a[96:99], a[16:19], v[164:167]\n" \
"v_exp_f32_e64 v132, v132\n" \
"v_mfma_f32_16x16x32_bf16 v[136:139], a[100:103], a[20:23], v[136:139]\n" \
"ds_read_b64_tr_b16 a[202:203], v215 offset:6144\n" \
"v_mfma_f32_16x16x32_bf16 v[136:139], a[104:107], a[24:27], v[136:139]\n" \
"v_exp_f32_e64 v133, v133\n" \
"v_mfma_f32_16x16x32_bf16 v[136:139], a[108:111], a[28:31], v[136:139]\n" \
"v_cvt_pk_bf16_f32 v192, v128, v129\n" \
"v_cvt_pk_bf16_f32 v193, v130, v131\n" \
"v_mfma_f32_16x16x32_bf16 v[140:143], a[112:115], a[16:19], v[164:167]\n" \
"v_exp_f32_e64 v134, v134\n" \
"v_mfma_f32_16x16x32_bf16 v[140:143], a[116:119], a[20:23], v[140:143]\n" \
"ds_read_b64_tr_b16 a[204:205], v215 offset:8192\n" \
"v_mfma_f32_16x16x32_bf16 v[140:143], a[120:123], a[24:27], v[140:143]\n" \
"buffer_load_dwordx4 v212, %[srdt], s49 offen offset:1024 lds\n" \
"v_mfma_f32_16x16x32_bf16 v[140:143], a[124:127], a[28:31], v[140:143]\n" \
"v_exp_f32_e64 v135, v135\n" \
"v_mfma_f32_16x16x32_bf16 v[48:51], a[176:179], v[196:199], v[48:51]\n" \
"ds_read_b64_tr_b16 a[206:207], v215 offset:10240\n" \
"v_add_u32_e32 v208, 0x1000, %[oq0]\n" \
"v_mfma_f32_16x16x32_bf16 v[48:51], a[144:147], v[188:191], v[48:51]\n" \
"v_exp_f32_e64 v136, v136\n" \
"v_mfma_f32_16x16x32_bf16 v[52:55], a[180:183], v[196:199], v[52:55]\n" \
"v_cvt_pk_bf16_f32 v194, v132, v133\n" \
"v_cvt_pk_bf16_f32 v195, v134, v135\n" \
"v_mfma_f32_16x16x32_bf16 v[52:55], a[148:151], v[188:191], v[52:55]\n" \
"v_exp_f32_e64 v137, v137\n" \
"v_mfma_f32_16x16x32_bf16 v[56:59], a[184:187], v[196:199], v[56:59]\n" \
"v_exp_f32_e64 v138, v138\n" \
"v_mfma_f32_16x16x32_bf16 v[56:59], a[152:155], v[188:191], v[56:59]\n" \
"ds_read_b64_tr_b16 a[208:209], v215 offset:12288\n" \
"v_add_u32_e32 v209, 0x2000, %[oq0]\n" \
"v_mfma_f32_16x16x32_bf16 v[60:63], a[188:191], v[196:199], v[60:63]\n" \
"v_exp_f32_e64 v139, v139\n" \
"v_mfma_f32_16x16x32_bf16 v[60:63], a[156:159], v[188:191], v[60:63]\n" \
"v_add_u32_e32 v210, 0x3000, %[oq0]\n" \
"v_mfma_f32_16x16x32_bf16 v[144:147], a[96:99], a[32:35], v[168:171]\n" \
"v_exp_f32_e64 v140, v140\n" \
"v_mfma_f32_16x16x32_bf16 v[144:147], a[100:103], a[36:39], v[144:147]\n" \
"ds_read_b64_tr_b16 a[210:211], v215 offset:14336\n" \
"v_mfma_f32_16x16x32_bf16 v[144:147], a[104:107], a[40:43], v[144:147]\n" \
"v_exp_f32_e64 v141, v141\n" \
"v_mfma_f32_16x16x32_bf16 v[144:147], a[108:111], a[44:47], v[144:147]\n" \
"v_cvt_pk_bf16_f32 v196, v136, v137\n" \
"v_cvt_pk_bf16_f32 v197, v138, v139\n" \
"v_mfma_f32_16x16x32_bf16 v[148:151], a[112:115], a[32:35], v[168:171]\n" \
"v_exp_f32_e64 v142, v142\n" \
"v_mfma_f32_16x16x32_bf16 v[148:151], a[116:119], a[36:39], v[148:151]\n" \
"v_mfma_f32_16x16x32_bf16 v[148:151], a[120:123], a[40:43], v[148:151]\n" \
"buffer_load_dwordx4 v213, %[srdt], s49 offen offset:2048 lds\n" \
"v_mfma_f32_16x16x32_bf16 v[148:151], a[124:127], a[44:47], v[148:151]\n" \
"v_exp_f32_e64 v143, v143\n" \
"v_mfma_f32_16x16x32_bf16 v[96:99], a[160:163], v[204:207], v[96:99]\n" \
"ds_read_b64_tr_b16 a[160:161], %[vvb] offset:57344\n" \
"v_mfma_f32_16x16x32_bf16 v[96:99], a[128:131], v[220:223], v[96:99]\n" \
"v_exp_f32_e64 v144, v144\n" \
"v_mfma_f32_16x16x32_bf16 v[100:103], a[164:167], v[204:207], v[100:103]\n" \
"v_cvt_pk_bf16_f32 v198, v140, v141\n" \
"v_cvt_pk_bf16_f32 v199, v142, v143\n" \
"v_mfma_f32_16x16x32_bf16 v[100:103], a[132:135], v[220:223], v[100:103]\n" \
"v_exp_f32_e64 v145, v145\n" \
"v_mfma_f32_16x16x32_bf16 v[104:107], a[168:171], v[204:207], v[104:107]\n" \
"ds_read_b64_tr_b16 a[162:163], %[vvb] offset:61440\n" \
"ds_read_b64_tr_b16 a[164:165], v211 offset:57344\n" \
"v_mfma_f32_16x16x32_bf16 v[104:107], a[136:139], v[220:223], v[104:107]\n" \
"v_exp_f32_e64 v146, v146\n" \
"v_mfma_f32_16x16x32_bf16 v[108:111], a[172:175], v[204:207], v[108:111]\n" \
"ds_read_b64_tr_b16 a[166:167], v211 offset:61440\n" \
"v_mfma_f32_16x16x32_bf16 v[108:111], a[140:143], v[220:223], v[108:111]\n" \
"v_exp_f32_e64 v147, v147\n" \
"v_smfmac_f32_16x16x64_bf16 v[180:183], a[192:195], v[216:223], v228\n" \
"ds_read_b64_tr_b16 a[168:169], %[vvb] offset:57856\n" \
"v_mfma_f32_16x16x32_bf16 v[152:155], a[96:99], a[48:51], v[172:175]\n" \
"v_exp_f32_e64 v148, v148\n" \
"v_mfma_f32_16x16x32_bf16 v[152:155], a[100:103], a[52:55], v[152:155]\n" \
"ds_read_b64_tr_b16 a[170:171], %[vvb] offset:61952\n" \
"v_mfma_f32_16x16x32_bf16 v[152:155], a[104:107], a[56:59], v[152:155]\n" \
"v_exp_f32_e64 v149, v149\n" \
"v_mfma_f32_16x16x32_bf16 v[152:155], a[108:111], a[60:63], v[152:155]\n" \
"v_cvt_pk_bf16_f32 v200, v144, v145\n" \
"v_cvt_pk_bf16_f32 v201, v146, v147\n" \
"v_mfma_f32_16x16x32_bf16 v[156:159], a[112:115], a[48:51], v[172:175]\n" \
"v_exp_f32_e64 v150, v150\n" \
"v_mfma_f32_16x16x32_bf16 v[156:159], a[116:119], a[52:55], v[156:159]\n" \
"ds_read_b64_tr_b16 a[172:173], v211 offset:57856\n" \
"v_mfma_f32_16x16x32_bf16 v[156:159], a[120:123], a[56:59], v[156:159]\n" \
"buffer_load_dwordx4 v214, %[srdt], s49 offen offset:3072 lds\n" \
"v_mfma_f32_16x16x32_bf16 v[156:159], a[124:127], a[60:63], v[156:159]\n" \
"v_exp_f32_e64 v151, v151\n" \
"v_mfma_f32_16x16x32_bf16 v[112:115], a[176:179], v[204:207], v[112:115]\n" \
"ds_read_b64_tr_b16 a[174:175], v211 offset:61952\n" \
"ds_read_b64_tr_b16 a[176:177], %[vvb] offset:58368\n" \
"v_mfma_f32_16x16x32_bf16 v[112:115], a[144:147], v[220:223], v[112:115]\n" \
"v_exp_f32_e64 v152, v152\n" \
"s_mov_b32 m0, s44\n" \
"s_addk_i32 s44, 0x400\n" \
"buffer_load_dwordx4 %[vg0], s[64:67], s45 offen offset:0 lds\n" \
"s_add_u32 s45, s45, 229376\n" \
"v_mfma_f32_16x16x32_bf16 v[116:119], a[180:183], v[204:207], v[116:119]\n" \
"v_cvt_pk_bf16_f32 v202, v148, v149\n" \
"v_cvt_pk_bf16_f32 v203, v150, v151\n" \
"v_mfma_f32_16x16x32_bf16 v[116:119], a[148:151], v[220:223], v[116:119]\n" \
"v_exp_f32_e64 v153, v153\n" \
"v_mfma_f32_16x16x32_bf16 v[120:123], a[184:187], v[204:207], v[120:123]\n" \
"v_exp_f32_e64 v154, v154\n" \
"v_mfma_f32_16x16x32_bf16 v[120:123], a[152:155], v[220:223], v[120:123]\n" \
"ds_read_b64_tr_b16 a[178:179], %[vvb] offset:62464\n" \
"s_waitcnt vmcnt(7)\n" \
"v_cmp_eq_u32_e64 s[56:57], 0, v229\n" \
"s_cmp_lg_u64 s[56:57], 0\n" \
"s_cbranch_scc1 Lspk_%=\n" \
"Lbkk_%=:\n" \
"v_mfma_f32_16x16x32_bf16 v[124:127], a[188:191], v[204:207], v[124:127]\n" \
"v_exp_f32_e32 v155, v155\n" \
"v_mfma_f32_16x16x32_bf16 v[124:127], a[156:159], v[220:223], v[124:127]\n" \
"ds_read_b64_tr_b16 a[180:181], v211 offset:58368\n" \
"ds_read_b64_tr_b16 a[182:183], v211 offset:62464\n" \
"ds_read_b64_tr_b16 a[184:185], %[vvb] offset:58880\n" \
"ds_read_b64_tr_b16 a[186:187], %[vvb] offset:62976\n" \
"ds_read_b64_tr_b16 a[188:189], v211 offset:58880\n" \
"ds_read_b64_tr_b16 a[190:191], v211 offset:62976\n" \
"s_waitcnt lgkmcnt(8)\n" \
"s_nop 0\n" \
"v_smfmac_f32_16x16x64_bf16 v[176:179], a[192:195], v[192:199], v228\n" \
"v_exp_f32_e64 v156, v156\n" \
"ds_read_b64_tr_b16 a[212:213], v227 offset:0\n" \
"v_mfma_f32_16x16x32_bf16 v[0:3], a[160:163], v[192:195], v[0:3]\n" \
"v_exp_f32_e64 v157, v157\n" \
"ds_read_b64_tr_b16 a[214:215], v227 offset:2048\n" \
"v_mfma_f32_16x16x32_bf16 v[4:7], a[164:167], v[192:195], v[4:7]\n" \
"v_cvt_pk_bf16_f32 v204, v152, v153\n" \
"ds_read_b64_tr_b16 a[216:217], v227 offset:4096\n" \
"v_mfma_f32_16x16x32_bf16 v[8:11], a[168:171], v[192:195], v[8:11]\n" \
"v_cvt_pk_bf16_f32 v205, v154, v155\n" \
"ds_read_b64_tr_b16 a[218:219], v227 offset:6144\n" \
"v_mfma_f32_16x16x32_bf16 v[12:15], a[172:175], v[192:195], v[12:15]\n" \
"v_exp_f32_e64 v158, v158\n" \
"ds_read_b64_tr_b16 a[220:221], v227 offset:8192\n" \
"s_waitcnt lgkmcnt(5)\n" \
"s_nop 0\n" \
"v_mfma_f32_16x16x32_bf16 v[16:19], a[176:179], v[192:195], v[16:19]\n" \
"v_exp_f32_e64 v159, v159\n" \
"ds_read_b64_tr_b16 a[222:223], v227 offset:10240\n" \
"v_mfma_f32_16x16x32_bf16 v[20:23], a[180:183], v[192:195], v[20:23]\n" \
"v_cvt_pk_bf16_f32 v206, v156, v157\n" \
"ds_read_b64_tr_b16 a[224:225], v227 offset:12288\n" \
"v_mfma_f32_16x16x32_bf16 v[24:27], a[184:187], v[192:195], v[24:27]\n" \
"v_cvt_pk_bf16_f32 v207, v158, v159\n" \
"ds_read_b64_tr_b16 a[226:227], v227 offset:14336\n" \
"v_mfma_f32_16x16x32_bf16 v[28:31], a[188:191], v[192:195], v[28:31]\n" \
"v_add_f32_e64 v176, v176, v177\n" \
"v_smfmac_f32_16x16x64_bf16 v[180:183], a[192:195], v[200:207], v228\n" \
"v_add_f32_e32 v178, v178, v179\n" \
"v_add_f32_e32 v231, v176, v178\n" \
"v_rcp_f32_e32 v176, v176\n" \
"v_rcp_f32_e32 v178, v178\n" \
"v_mfma_f32_16x16x32_bf16 v[32:35], a[160:163], v[196:199], v[32:35]\n" \
"v_pk_mul_f32 v[0:1], v[0:1], v[176:177] op_sel_hi:[1,0]\n" \
"v_pk_mul_f32 v[2:3], v[2:3], v[176:177] op_sel_hi:[1,0]\n" \
"v_pk_mul_f32 v[4:5], v[4:5], v[176:177] op_sel_hi:[1,0]\n" \
"v_pk_mul_f32 v[6:7], v[6:7], v[176:177] op_sel_hi:[1,0]\n" \
"v_cvt_pk_bf16_f32 v0, v0, v1\n" \
"v_cvt_pk_bf16_f32 v1, v2, v3\n" \
"v_cvt_pk_bf16_f32 v2, v4, v5\n" \
"v_mfma_f32_16x16x32_bf16 v[36:39], a[164:167], v[196:199], v[36:39]\n" \
"v_cvt_pk_bf16_f32 v3, v6, v7\n" \
"s_mov_b32 s55, 0x4e800000\n" \
"v_mov_b32_e32 v236, %[flag]\n" \
"v_add_f32_e32 v180, v180, v181\n" \
"v_add_f32_e32 v182, v182, v183\n" \
"v_add_f32_e32 v231, v231, v180\n" \
"v_add_f32_e64 v231, v231, v182\n" \
"v_mfma_f32_16x16x32_bf16 v[40:43], a[168:171], v[196:199], v[40:43]\n" \
"v_rcp_f32_e64 v180, v180\n" \
"v_cmp_nle_f32_e64 s[56:57], v231, s55\n" \
"v_rcp_f32_e32 v182, v182\n" \
"s_nop 3\n" \
"s_cmp_lg_u64 s[56:57], 0\n" \
"s_cselect_b32 s55, 1, 0\n" \
"v_mov_b32_e64 v230, s55\n" \
"v_mfma_f32_16x16x32_bf16 v[44:47], a[172:175], v[196:199], v[44:47]\n" \
"s_mov_b32 s69, s55\n" \
"s_andn2_b32 s55, s55, s52\n" \
"s_cselect_b64 vcc, 0, vcc\n" \
"s_nop 0\n" \
"ds_write_b32 v236, v230\n" \
"v_and_b32_e32 v236, -16, v236\n" \
"s_cbranch_vccz Lg37_%=\n" \
"buffer_store_dwordx4 v[0:3], %[oq0], %[srdz], s60 offen offset:0 sc1\n" \
"Lg37_%=:\n" \
"v_pk_mul_f32 v[8:9], v[8:9], v[176:177] op_sel_hi:[1,0]\n" \
"v_mfma_f32_16x16x32_bf16 v[48:51], a[176:179], v[196:199], v[48:51]\n" \
"v_pk_mul_f32 v[10:11], v[10:11], v[176:177] op_sel_hi:[1,0]\n" \
"v_pk_mul_f32 v[12:13], v[12:13], v[176:177] op_sel_hi:[1,0]\n" \
"v_pk_mul_f32 v[14:15], v[14:15], v[176:177] op_sel_hi:[1,0]\n" \
"v_cvt_pk_bf16_f32 v8, v8, v9\n" \
"v_cvt_pk_bf16_f32 v9, v10, v11\n" \
"v_cvt_pk_bf16_f32 v10, v12, v13\n" \
"v_cvt_pk_bf16_f32 v11, v14, v15\n" \
"v_mfma_f32_16x16x32_bf16 v[52:55], a[180:183], v[196:199], v[52:55]\n" \
"s_cbranch_vccz Lg38_%=\n" \
"s_nop 0\n" \
"buffer_store_dwordx4 v[8:11], %[oq0], %[srdz], s60 offen offset:1024 sc1\n" \
"Lg38_%=:\n" \
"v_pk_mul_f32 v[16:17], v[16:17], v[176:177] op_sel_hi:[1,0]\n" \
"v_pk_mul_f32 v[18:19], v[18:19], v[176:177] op_sel_hi:[1,0]\n" \
"v_pk_mul_f32 v[20:21], v[20:21], v[176:177] op_sel_hi:[1,0]\n" \
"v_pk_mul_f32 v[22:23], v[22:23], v[176:177] op_sel_hi:[1,0]\n" \
"v_cvt_pk_bf16_f32 v16, v16, v17\n" \
"v_cvt_pk_bf16_f32 v17, v18, v19\n" \
"v_mfma_f32_16x16x32_bf16 v[56:59], a[184:187], v[196:199], v[56:59]\n" \
"v_cvt_pk_bf16_f32 v18, v20, v21\n" \
"v_cvt_pk_bf16_f32 v19, v22, v23\n" \
"s_cbranch_vccz Lg39_%=\n" \
"s_nop 0\n" \
"buffer_store_dwordx4 v[16:19], %[oq0], %[srdz], s60 offen offset:2048 sc1\n" \
"Lg39_%=:\n" \
"v_pk_mul_f32 v[24:25], v[24:25], v[176:177] op_sel_hi:[1,0]\n" \
"v_pk_mul_f32 v[26:27], v[26:27], v[176:177] op_sel_hi:[1,0]\n" \
"v_pk_mul_f32 v[28:29], v[28:29], v[176:177] op_sel_hi:[1,0]\n" \
"v_pk_mul_f32 v[30:31], v[30:31], v[176:177] op_sel_hi:[1,0]\n" \
"v_mfma_f32_16x16x32_bf16 v[60:63], a[188:191], v[196:199], v[60:63]\n" \
"v_cvt_pk_bf16_f32 v24, v24, v25\n" \
"v_cvt_pk_bf16_f32 v25, v26, v27\n" \
"v_cvt_pk_bf16_f32 v26, v28, v29\n" \
"v_cvt_pk_bf16_f32 v27, v30, v31\n" \
"s_cbranch_vccz Lg40_%=\n" \
"s_nop 0\n" \
"buffer_store_dwordx4 v[24:27], %[oq0], %[srdz], s60 offen offset:3072 sc1\n" \
"Lg40_%=:\n" \
"v_mfma_f32_16x16x32_bf16 v[64:67], a[128:131], v[216:219], v[64:67]\n" \
"v_pk_mul_f32 v[32:33], v[32:33], v[178:179] op_sel_hi:[1,0]\n" \
"v_pk_mul_f32 v[34:35], v[34:35], v[178:179] op_sel_hi:[1,0]\n" \
"v_pk_mul_f32 v[36:37], v[36:37], v[178:179] op_sel_hi:[1,0]\n" \
"v_mfma_f32_16x16x32_bf16 v[64:67], a[160:163], v[200:203], v[64:67]\n" \
"v_pk_mul_f32 v[38:39], v[38:39], v[178:179] op_sel_hi:[1,0]\n" \
"v_cvt_pk_bf16_f32 v32, v32, v33\n" \
"v_cvt_pk_bf16_f32 v33, v34, v35\n" \
"v_mfma_f32_16x16x32_bf16 v[68:71], a[132:135], v[216:219], v[68:71]\n" \
"v_cvt_pk_bf16_f32 v34, v36, v37\n" \
"v_cvt_pk_bf16_f32 v35, v38, v39\n" \
"s_cbranch_vccz Lg41_%=\n" \
"s_nop 0\n" \
"buffer_store_dwordx4 v[32:35], v208, %[srdz], s60 offen offset:0 sc1\n" \
"Lg41_%=:\n" \
"v_mfma_f32_16x16x32_bf16 v[68:71], a[164:167], v[200:203], v[68:71]\n" \
"v_pk_mul_f32 v[40:41], v[40:41], v[178:179] op_sel_hi:[1,0]\n" \
"v_pk_mul_f32 v[42:43], v[42:43], v[178:179] op_sel_hi:[1,0]\n" \
"v_pk_mul_f32 v[44:45], v[44:45], v[178:179] op_sel_hi:[1,0]\n" \
"v_mfma_f32_16x16x32_bf16 v[72:75], a[136:139], v[216:219], v[72:75]\n" \
"v_pk_mul_f32 v[46:47], v[46:47], v[178:179] op_sel_hi:[1,0]\n" \
"v_cvt_pk_bf16_f32 v40, v40, v41\n" \
"v_cvt_pk_bf16_f32 v41, v42, v43\n" \
"v_mfma_f32_16x16x32_bf16 v[72:75], a[168:171], v[200:203], v[72:75]\n" \
"v_cvt_pk_bf16_f32 v42, v44, v45\n" \
"v_cvt_pk_bf16_f32 v43, v46, v47\n" \
"s_cbranch_vccz Lg42_%=\n" \
"s_nop 0\n" \
".if . - adaln_body != 40960\n" \
".error \"size model\"\n" \
".endif\n" \
"Lk9_%=:\n" \
"s_cbranch_execz Lk10_%=\n" \
"s_nop 0\n" \
"buffer_store_dwordx4 v[40:43], v208, %[srdz], s60 offen offset:1024 sc1\n" \
"Lg42_%=:\n" \
"v_mfma_f32_16x16x32_bf16 v[76:79], a[140:143], v[216:219], v[76:79]\n" \
"v_pk_mul_f32 v[48:49], v[48:49], v[178:179] op_sel_hi:[1,0]\n" \
"v_pk_mul_f32 v[50:51], v[50:51], v[178:179] op_sel_hi:[1,0]\n" \
"v_pk_mul_f32 v[52:53], v[52:53], v[178:179] op_sel_hi:[1,0]\n" \
"v_mfma_f32_16x16x32_bf16 v[76:79], a[172:175], v[200:203], v[76:79]\n" \
"v_pk_mul_f32 v[54:55], v[54:55], v[178:179] op_sel_hi:[1,0]\n" \
"v_cvt_pk_bf16_f32 v48, v48, v49\n" \
"v_cvt_pk_bf16_f32 v49, v50, v51\n" \
"v_mfma_f32_16x16x32_bf16 v[80:83], a[144:147], v[216:219], v[80:83]\n" \
"v_cvt_pk_bf16_f32 v50, v52, v53\n" \
"v_cvt_pk_bf16_f32 v51, v54, v55\n" \
"s_cbranch_vccz Lg43_%=\n" \
"s_nop 0\n" \
"buffer_store_dwordx4 v[48:51], v208, %[srdz], s60 offen offset:2048 sc1\n" \
"Lg43_%=:\n" \
"v_mfma_f32_16x16x32_bf16 v[80:83], a[176:179], v[200:203], v[80:83]\n" \
"v_pk_mul_f32 v[56:57], v[56:57], v[178:179] op_sel_hi:[1,0]\n" \
"v_pk_mul_f32 v[58:59], v[58:59], v[178:179] op_sel_hi:[1,0]\n" \
"v_pk_mul_f32 v[60:61], v[60:61], v[178:179] op_sel_hi:[1,0]\n" \
"v_mfma_f32_16x16x32_bf16 v[84:87], a[148:151], v[216:219], v[84:87]\n" \
"v_pk_mul_f32 v[62:63], v[62:63], v[178:179] op_sel_hi:[1,0]\n" \
"v_cvt_pk_bf16_f32 v56, v56, v57\n" \
"v_cvt_pk_bf16_f32 v57, v58, v59\n" \
"v_mfma_f32_16x16x32_bf16 v[84:87], a[180:183], v[200:203], v[84:87]\n" \
"v_cvt_pk_bf16_f32 v58, v60, v61\n" \
"v_cvt_pk_bf16_f32 v59, v62, v63\n" \
"s_cbranch_vccz Lg44_%=\n" \
"s_nop 0\n" \
"buffer_store_dwordx4 v[56:59], v208, %[srdz], s60 offen offset:3072 sc1\n" \
"Lg44_%=:\n" \
"v_mfma_f32_16x16x32_bf16 v[88:91], a[152:155], v[216:219], v[88:91]\n" \
"v_mfma_f32_16x16x32_bf16 v[88:91], a[184:187], v[200:203], v[88:91]\n" \
"v_mfma_f32_16x16x32_bf16 v[92:95], a[156:159], v[216:219], v[92:95]\n" \
"v_mfma_f32_16x16x32_bf16 v[92:95], a[188:191], v[200:203], v[92:95]\n" \
"s_waitcnt vmcnt(14) lgkmcnt(0)\n" \
"s_bitcmp1_b32 0xf, s48\n" \
"s_cbranch_scc1 Lcw1_%=\n" \
"s_waitcnt vmcnt(6)\n" \
"Lcw1_%=:\n" \
"s_barrier\n" \
"s_nop 0\n" \
"ds_read_b128 v[232:235], v236\n" \
"v_mfma_f32_16x16x32_bf16 v[96:99], a[160:163], v[204:207], v[96:99]\n" \
"v_pk_mul_f32 v[64:65], v[64:65], v[180:181] op_sel_hi:[1,0]\n" \
"v_pk_mul_f32 v[66:67], v[66:67], v[180:181] op_sel_hi:[1,0]\n" \
"v_pk_mul_f32 v[68:69], v[68:69], v[180:181] op_sel_hi:[1,0]\n" \
"v_pk_mul_f32 v[70:71], v[70:71], v[180:181] op_sel_hi:[1,0]\n" \
"v_cvt_pk_bf16_f32 v64, v64, v65\n" \
"v_mfma_f32_16x16x32_bf16 v[100:103], a[164:167], v[204:207], v[100:103]\n" \
"v_cvt_pk_bf16_f32 v65, v66, v67\n" \
"v_cvt_pk_bf16_f32 v66, v68, v69\n" \
"v_cvt_pk_bf16_f32 v67, v70, v71\n" \
"s_cbranch_vccz Lg45_%=\n" \
"s_nop 0\n" \
"buffer_store_dwordx4 v[64:67], v209, %[srdz], s60 offen offset:0 sc1\n" \
"Lg45_%=:\n" \
"v_pk_mul_f32 v[72:73], v[72:73], v[180:181] op_sel_hi:[1,0]\n" \
"v_mfma_f32_16x16x32_bf16 v[104:107], a[168:171], v[204:207], v[104:107]\n" \
"v_pk_mul_f32 v[74:75], v[74:75], v[180:181] op_sel_hi:[1,0]\n" \
"v_pk_mul_f32 v[76:77], v[76:77], v[180:181] op_sel_hi:[1,0]\n" \
"v_pk_mul_f32 v[78:79], v[78:79], v[180:181] op_sel_hi:[1,0]\n" \
"v_cvt_pk_bf16_f32 v72, v72, v73\n" \
"v_cvt_pk_bf16_f32 v73, v74, v75\n" \
"v_mfma_f32_16x16x32_bf16 v[108:111], a[172:175], v[204:207], v[108:111]\n" \
"v_cvt_pk_bf16_f32 v74, v76, v77\n" \
"v_cvt_pk_bf16_f32 v75, v78, v79\n" \
"s_cbranch_vccz Lg46_%=\n" \
"s_nop 0\n" \
"buffer_store_dwordx4 v[72:75], v209, %[srdz], s60 offen offset:1024 sc1\n" \
"Lg46_%=:\n" \
"v_pk_mul_f32 v[80:81], v[80:81], v[180:181] op_sel_hi:[1,0]\n" \
"v_pk_mul_f32 v[82:83], v[82:83], v[180:181] op_sel_hi:[1,0]\n" \
"v_mfma_f32_16x16x32_bf16 v[112:115], a[176:179], v[204:207], v[112:115]\n" \
"v_pk_mul_f32 v[84:85], v[84:85], v[180:181] op_sel_hi:[1,0]\n" \
"v_pk_mul_f32 v[86:87], v[86:87], v[180:181] op_sel_hi:[1,0]\n" \
"v_cvt_pk_bf16_f32 v80, v80, v81\n" \
"v_cvt_pk_bf16_f32 v81, v82, v83\n" \
"v_cvt_pk_bf16_f32 v82, v84, v85\n" \
"v_mfma_f32_16x16x32_bf16 v[116:119], a[180:183], v[204:207], v[116:119]\n" \
"v_cvt_pk_bf16_f32 v83, v86, v87\n" \
"s_cbranch_vccz Lg47_%=\n" \
"s_nop 0\n" \
"buffer_store_dwordx4 v[80:83], v209, %[srdz], s60 offen offset:2048 sc1\n" \
"Lg47_%=:\n" \
"v_pk_mul_f32 v[88:89], v[88:89], v[180:181] op_sel_hi:[1,0]\n" \
"v_pk_mul_f32 v[90:91], v[90:91], v[180:181] op_sel_hi:[1,0]\n" \
"v_pk_mul_f32 v[92:93], v[92:93], v[180:181] op_sel_hi:[1,0]\n" \
"v_mfma_f32_16x16x32_bf16 v[120:123], a[184:187], v[204:207], v[120:123]\n" \
"v_pk_mul_f32 v[94:95], v[94:95], v[180:181] op_sel_hi:[1,0]\n" \
"v_cvt_pk_bf16_f32 v88, v88, v89\n" \
"v_cvt_pk_bf16_f32 v89, v90, v91\n" \
"v_cvt_pk_bf16_f32 v90, v92, v93\n" \
"v_cvt_pk_bf16_f32 v91, v94, v95\n" \
"v_mfma_f32_16x16x32_bf16 v[124:127], a[188:191], v[204:207], v[124:127]\n" \
"s_cbranch_vccz Lg48_%=\n" \
"s_nop 0\n" \
"buffer_store_dwordx4 v[88:91], v209, %[srdz], s60 offen offset:3072 sc1\n" \
"Lg48_%=:\n" \
"s_waitcnt lgkmcnt(0)\n" \
"s_nop 0\n" \
"v_or3_b32 v232, v232, v233, v234\n" \
"v_or_b32_e32 v232, v232, v235\n" \
"s_nop 0\n" \
"v_readfirstlane_b32 s55, v232\n" \
"s_nop 3\n" \
"s_andn2_b32 s55, s55, s52\n" \
"s_cbranch_scc1 Lslow_%=\n" \
"v_mfma_f32_16x16x32_bf16 v[192:195], a[196:199], v[0:3], 0\n" \
"v_mfma_f32_16x16x32_bf16 v[192:195], a[200:203], v[8:11], v[192:195]\n" \
"v_pk_mul_f32 v[96:97], v[96:97], v[182:183] op_sel_hi:[1,0]\n" \
"v_pk_mul_f32 v[98:99], v[98:99], v[182:183] op_sel_hi:[1,0]\n" \
"v_mfma_f32_16x16x32_bf16 v[192:195], a[204:207], v[16:19], v[192:195]\n" \
"v_pk_mul_f32 v[100:101], v[100:101], v[182:183] op_sel_hi:[1,0]\n" \
"v_pk_mul_f32 v[102:103], v[102:103], v[182:183] op_sel_hi:[1,0]\n" \
"s_add_u32 m0, %[m0b], 114688\n" \
"s_cmp_gt_u32 s48, 1\n" \
"s_cselect_b32 s49, 1, 2\n" \
"s_lshl_b32 s49, s49, 18\n" \
"s_or_b32 s49, s49, s58\n" \
"buffer_load_dwordx4 %[wo0], %[srdt], s49 offen offset:0 lds\n" \
"v_mfma_f32_16x16x32_bf16 v[192:195], a[208:211], v[24:27], v[192:195]\n" \
"v_cvt_pk_bf16_f32 v96, v96, v97\n" \
"v_cvt_pk_bf16_f32 v97, v98, v99\n" \
"v_mfma_f32_16x16x32_bf16 v[144:147], a[212:215], v[0:3], 0\n" \
"v_cvt_pk_bf16_f32 v98, v100, v101\n" \
"v_mfma_f32_16x16x32_bf16 v[144:147], a[216:219], v[8:11], v[144:147]\n" \
"v_cvt_pk_bf16_f32 v99, v102, v103\n" \
"s_cbranch_vccz Lg1_%=\n" \
"s_nop 0\n" \
"buffer_store_dwordx4 v[96:99], v210, %[srdz], s60 offen offset:0 sc1\n" \
"Lg1_%=:\n" \
"v_mfma_f32_16x16x32_bf16 v[144:147], a[220:223], v[16:19], v[144:147]\n" \
"ds_read_b64_tr_b16 a[228:229], v215 offset:512\n" \
"v_pk_mul_f32 v[104:105], v[104:105], v[182:183] op_sel_hi:[1,0]\n" \
"v_pk_mul_f32 v[106:107], v[106:107], v[182:183] op_sel_hi:[1,0]\n" \
"v_mfma_f32_16x16x32_bf16 v[144:147], a[224:227], v[24:27], v[144:147]\n" \
"ds_read_b64_tr_b16 a[230:231], v215 offset:2560\n" \
"v_pk_mul_f32 v[108:109], v[108:109], v[182:183] op_sel_hi:[1,0]\n" \
"v_mfma_f32_16x16x32_bf16 v[196:199], a[196:199], v[32:35], 0\n" \
"ds_read_b64_tr_b16 a[232:233], v215 offset:4608\n" \
"v_pk_mul_f32 v[110:111], v[110:111], v[182:183] op_sel_hi:[1,0]\n" \
"v_cvt_pk_bf16_f32 v104, v104, v105\n" \
"v_mfma_f32_16x16x32_bf16 v[196:199], a[200:203], v[40:43], v[196:199]\n" \
"ds_read_b64_tr_b16 a[234:235], v215 offset:6656\n" \
"v_cvt_pk_bf16_f32 v105, v106, v107\n" \
"v_cvt_pk_bf16_f32 v106, v108, v109\n" \
"v_mfma_f32_16x16x32_bf16 v[196:199], a[204:207], v[48:51], v[196:199]\n" \
"v_cvt_pk_bf16_f32 v107, v110, v111\n" \
"s_cbranch_vccz Lg2_%=\n" \
"s_nop 0\n" \
"buffer_store_dwordx4 v[104:107], v210, %[srdz], s60 offen offset:1024 sc1\n" \
"Lg2_%=:\n" \
"v_mfma_f32_16x16x32_bf16 v[196:199], a[208:211], v[56:59], v[196:199]\n" \
"v_pk_mul_f32 v[112:113], v[112:113], v[182:183] op_sel_hi:[1,0]\n" \
"v_mfma_f32_16x16x32_bf16 v[148:151], a[212:215], v[32:35], 0\n" \
"v_pk_mul_f32 v[114:115], v[114:115], v[182:183] op_sel_hi:[1,0]\n" \
"v_pk_mul_f32 v[116:117], v[116:117], v[182:183] op_sel_hi:[1,0]\n" \
"buffer_load_dwordx4 v212, %[srdt], s49 offen offset:1024 lds\n" \
"v_mfma_f32_16x16x32_bf16 v[148:151], a[216:219], v[40:43], v[148:151]\n" \
"v_pk_mul_f32 v[118:119], v[118:119], v[182:183] op_sel_hi:[1,0]\n" \
"v_cvt_pk_bf16_f32 v112, v112, v113\n" \
"v_mfma_f32_16x16x32_bf16 v[148:151], a[220:223], v[48:51], v[148:151]\n" \
"ds_read_b64_tr_b16 a[236:237], v215 offset:8704\n" \
"v_cvt_pk_bf16_f32 v113, v114, v115\n" \
"v_mfma_f32_16x16x32_bf16 v[148:151], a[224:227], v[56:59], v[148:151]\n" \
"v_cvt_pk_bf16_f32 v114, v116, v117\n" \
"v_cvt_pk_bf16_f32 v115, v118, v119\n" \
"v_mfma_f32_16x16x32_bf16 v[200:203], a[196:199], v[64:67], 0\n" \
"ds_read_b64_tr_b16 a[238:239], v215 offset:10752\n" \
"s_cbranch_vccz Lg3_%=\n" \
"s_nop 0\n" \
"buffer_store_dwordx4 v[112:115], v210, %[srdz], s60 offen offset:2048 sc1\n" \
"Lg3_%=:\n" \
"v_pk_mul_f32 v[120:121], v[120:121], v[182:183] op_sel_hi:[1,0]\n" \
"v_mfma_f32_16x16x32_bf16 v[200:203], a[200:203], v[72:75], v[200:203]\n" \
"v_pk_mul_f32 v[122:123], v[122:123], v[182:183] op_sel_hi:[1,0]\n" \
"v_pk_mul_f32 v[124:125], v[124:125], v[182:183] op_sel_hi:[1,0]\n" \
"v_mfma_f32_16x16x32_bf16 v[200:203], a[204:207], v[80:83], v[200:203]\n" \
"v_pk_mul_f32 v[126:127], v[126:127], v[182:183] op_sel_hi:[1,0]\n" \
"v_mfma_f32_16x16x32_bf16 v[200:203], a[208:211], v[88:91], v[200:203]\n" \
"v_cvt_pk_bf16_f32 v120, v120, v121\n" \
"v_cvt_pk_bf16_f32 v121, v122, v123\n" \
"v_mfma_f32_16x16x32_bf16 v[152:155], a[212:215], v[64:67], 0\n" \
"v_cvt_pk_bf16_f32 v122, v124, v125\n" \
"v_cvt_pk_bf16_f32 v123, v126, v127\n" \
"v_mfma_f32_16x16x32_bf16 v[152:155], a[216:219], v[72:75], v[152:155]\n" \
"s_cbranch_vccz Lg4_%=\n" \
"s_nop 0\n" \
"buffer_store_dwordx4 v[120:123], v210, %[srdz], s60 offen offset:3072 sc1\n" \
"Lg4_%=:\n" \
"v_mfma_f32_16x16x32_bf16 v[152:155], a[220:223], v[80:83], v[152:155]\n" \
"v_add_u32_e32 v220, 0x10000, v215\n" \
"v_mfma_f32_16x16x32_bf16 v[152:155], a[224:227], v[88:91], v[152:155]\n" \
"v_mov_b32_e64 v230, 1\n" \
"v_readlane_b32 s62, %[tlo], 19\n" \
"v_readlane_b32 s63, %[thi], 19\n" \
"v_and_b32_e32 v233, 0x380, v224\n" \
"v_lshlrev_b32_e64 v233, 2, v233\n" \
"buffer_load_dwordx4 v213, %[srdt], s49 offen offset:2048 lds\n" \
"v_mfma_f32_16x16x32_bf16 v[204:207], a[196:199], v[96:99], 0\n" \
"v_add_u32_e32 v221, 0x10000, v227\n" \
"v_readlane_b32 s40, %[tlo], 32\n" \
"v_mfma_f32_16x16x32_bf16 v[204:207], a[200:203], v[104:107], v[204:207]\n" \
"ds_read_b64_tr_b16 a[196:197], v215 offset:12800\n" \
"v_mfma_f32_16x16x32_bf16 v[204:207], a[204:207], v[112:115], v[204:207]\n" \
"ds_read_b64_tr_b16 a[198:199], v215 offset:14848\n" \
"v_mfma_f32_16x16x32_bf16 v[204:207], a[208:211], v[120:123], v[204:207]\n" \
"ds_read_b64_tr_b16 a[200:201], v227 offset:512\n" \
"v_mfma_f32_16x16x32_bf16 v[156:159], a[212:215], v[96:99], 0\n" \
"ds_read_b64_tr_b16 a[202:203], v227 offset:2560\n" \
"v_mfma_f32_16x16x32_bf16 v[156:159], a[216:219], v[104:107], v[156:159]\n" \
"ds_read_b64_tr_b16 a[204:205], v227 offset:4608\n" \
"v_mfma_f32_16x16x32_bf16 v[156:159], a[220:223], v[112:115], v[156:159]\n" \
"ds_read_b64_tr_b16 a[206:207], v227 offset:6656\n" \
"v_mfma_f32_16x16x32_bf16 v[156:159], a[224:227], v[120:123], v[156:159]\n" \
"s_waitcnt vmcnt(63) lgkmcnt(4)\n" \
"s_nop 0\n" \
"v_mfma_f32_16x16x32_bf16 v[160:163], a[228:231], v[0:3], 0\n" \
"ds_read_b64_tr_b16 a[208:209], v227 offset:8704\n" \
"v_mfma_f32_16x16x32_bf16 v[160:163], a[232:235], v[8:11], v[160:163]\n" \
"ds_read_b64_tr_b16 a[210:211], v227 offset:10752\n" \
"buffer_load_dwordx4 a[176:179], v237, %[srdz], s40 offen offset:0 sc1\n" \
"v_mfma_f32_16x16x32_bf16 v[164:167], a[228:231], v[32:35], 0\n" \
"ds_read_b64_tr_b16 a[212:213], v227 offset:12800\n" \
"v_mfma_f32_16x16x32_bf16 v[164:167], a[232:235], v[40:43], v[164:167]\n" \
"ds_read_b64_tr_b16 a[214:215], v227 offset:14848\n" \
"buffer_load_dwordx4 v214, %[srdt], s49 offen offset:3072 lds\n" \
"v_mfma_f32_16x16x32_bf16 v[168:171], a[228:231], v[64:67], 0\n" \
"v_mfma_f32_16x16x32_bf16 v[168:171], a[232:235], v[72:75], v[168:171]\n" \
"v_mfma_f32_16x16x32_bf16 v[172:175], a[228:231], v[96:99], 0\n" \
"ds_read_b64_tr_b16 a[216:217], v215 offset:16384\n" \
"v_mfma_f32_16x16x32_bf16 v[172:175], a[232:235], v[104:107], v[172:175]\n" \
"ds_read_b64_tr_b16 a[218:219], v215 offset:18432\n" \
"v_mfma_f32_16x16x32_bf16 v[160:163], a[236:239], v[16:19], v[160:163]\n" \
"ds_read_b64_tr_b16 a[220:221], v215 offset:20480\n" \
"v_mfma_f32_16x16x32_bf16 v[160:163], a[196:199], v[24:27], v[160:163]\n" \
"ds_read_b64_tr_b16 a[222:223], v215 offset:22528\n" \
"buffer_load_dwordx4 a[180:183], v237, %[srdz], s40 offen offset:64 sc1\n" \
"v_mfma_f32_16x16x32_bf16 v[164:167], a[236:239], v[48:51], v[164:167]\n" \
"v_mfma_f32_16x16x32_bf16 v[164:167], a[196:199], v[56:59], v[164:167]\n" \
"v_mfma_f32_16x16x32_bf16 v[168:171], a[236:239], v[80:83], v[168:171]\n" \
"s_waitcnt vmcnt(63) lgkmcnt(0)\n" \
"s_barrier\n" \
"v_mfma_f32_16x16x32_bf16 v[168:171], a[196:199], v[88:91], v[168:171]\n" \
"v_mfma_f32_16x16x32_bf16 v[172:175], a[236:239], v[112:115], v[172:175]\n" \
"ds_read_b64_tr_b16 a[224:225], v215 offset:24576\n" \
"v_mfma_f32_16x16x32_bf16 v[172:175], a[196:199], v[120:123], v[172:175]\n" \
"s_waitcnt vmcnt(63) lgkmcnt(5)\n" \
"s_nop 0\n" \
"v_mfma_f32_16x16x32_bf16 v[176:179], a[200:203], v[0:3], 0\n" \
"ds_read_b64_tr_b16 a[226:227], v215 offset:26624\n" \
"v_mfma_f32_16x16x32_bf16 v[176:179], a[204:207], v[8:11], v[176:179]\n" \
"ds_read_b64_tr_b16 a[228:229], v215 offset:28672\n" \
"buffer_load_dwordx4 a[184:187], v237, %[srdz], s40 offen offset:128 sc1\n" \
"v_mfma_f32_16x16x32_bf16 v[180:183], a[200:203], v[32:35], 0\n" \
"ds_read_b64_tr_b16 a[230:231], v215 offset:30720\n" \
"v_mfma_f32_16x16x32_bf16 v[180:183], a[204:207], v[40:43], v[180:183]\n" \
"s_add_u32 m0, %[m0b], 49152\n" \
"s_cmp_gt_u32 s48, 1\n" \
"s_cselect_b32 s49, 1, 2\n" \
"s_lshl_b32 s49, s49, 18\n" \
"s_or_b32 s49, s49, s59\n" \
"buffer_load_dwordx4 %[wo0], %[srdt], s49 offen offset:0 lds\n" \
"v_mfma_f32_16x16x32_bf16 v[184:187], a[200:203], v[64:67], 0\n" \
"v_mfma_f32_16x16x32_bf16 v[184:187], a[204:207], v[72:75], v[184:187]\n" \
"buffer_load_dwordx4 v212, %[srdt], s49 offen offset:1024 lds\n" \
"v_mfma_f32_16x16x32_bf16 v[188:191], a[200:203], v[96:99], 0\n" \
"ds_read_b64_tr_b16 a[232:233], v227 offset:16384\n" \
"v_mfma_f32_16x16x32_bf16 v[188:191], a[204:207], v[104:107], v[188:191]\n" \
"ds_read_b64_tr_b16 a[234:235], v227 offset:18432\n" \
"v_mfma_f32_16x16x32_bf16 v[176:179], a[208:211], v[16:19], v[176:179]\n" \
"ds_read_b64_tr_b16 a[236:237], v227 offset:20480\n" \
"v_mfma_f32_16x16x32_bf16 v[176:179], a[212:215], v[24:27], v[176:179]\n" \
"ds_read_b64_tr_b16 a[238:239], v227 offset:22528\n" \
"buffer_load_dwordx4 a[188:191], v237, %[srdz], s40 offen offset:192 sc1\n" \
"v_mfma_f32_16x16x32_bf16 v[180:183], a[208:211], v[48:51], v[180:183]\n" \
"v_mfma_f32_16x16x32_bf16 v[180:183], a[212:215], v[56:59], v[180:183]\n" \
"v_mfma_f32_16x16x32_bf16 v[184:187], a[208:211], v[80:83], v[184:187]\n" \
"v_mfma_f32_16x16x32_bf16 v[184:187], a[212:215], v[88:91], v[184:187]\n" \
"buffer_load_dwordx4 v213, %[srdt], s49 offen offset:2048 lds\n" \
"v_mfma_f32_16x16x32_bf16 v[188:191], a[208:211], v[112:115], v[188:191]\n" \
"ds_read_b64_tr_b16 a[196:197], v227 offset:24576\n" \
"v_mfma_f32_16x16x32_bf16 v[188:191], a[212:215], v[120:123], v[188:191]\n" \
"s_waitcnt vmcnt(63) lgkmcnt(5)\n" \
"s_nop 0\n" \
"v_mfma_f32_16x16x32_bf16 v[4:7], a[216:219], v[0:3], 0\n" \
"ds_read_b64_tr_b16 a[198:199], v227 offset:26624\n" \
"v_mfma_f32_16x16x32_bf16 v[4:7], a[220:223], v[8:11], v[4:7]\n" \
"ds_read_b64_tr_b16 a[200:201], v227 offset:28672\n" \
"v_mfma_f32_16x16x32_bf16 v[36:39], a[216:219], v[32:35], 0\n" \
"ds_read_b64_tr_b16 a[202:203], v227 offset:30720\n" \
"v_mfma_f32_16x16x32_bf16 v[36:39], a[220:223], v[40:43], v[36:39]\n" \
"v_mfma_f32_16x16x32_bf16 v[68:71], a[216:219], v[64:67], 0\n" \
"buffer_load_dwordx4 v214, %[srdt], s49 offen offset:3072 lds\n" \
"v_mfma_f32_16x16x32_bf16 v[68:71], a[220:223], v[72:75], v[68:71]\n" \
"v_mfma_f32_16x16x32_bf16 v[100:103], a[216:219], v[96:99], 0\n" \
"ds_read_b64_tr_b16 a[204:205], v215 offset:16896\n" \
"v_mfma_f32_16x16x32_bf16 v[100:103], a[220:223], v[104:107], v[100:103]\n" \
"ds_read_b64_tr_b16 a[206:207], v215 offset:18944\n" \
"v_mfma_f32_16x16x32_bf16 v[4:7], a[224:227], v[16:19], v[4:7]\n" \
"ds_read_b64_tr_b16 a[208:209], v215 offset:20992\n" \
"v_mfma_f32_16x16x32_bf16 v[4:7], a[228:231], v[24:27], v[4:7]\n" \
"ds_read_b64_tr_b16 a[210:211], v215 offset:23040\n" \
"v_mfma_f32_16x16x32_bf16 v[36:39], a[224:227], v[48:51], v[36:39]\n" \
"v_mfma_f32_16x16x32_bf16 v[36:39], a[228:231], v[56:59], v[36:39]\n" \
"v_mfma_f32_16x16x32_bf16 v[68:71], a[224:227], v[80:83], v[68:71]\n" \
"v_mfma_f32_16x16x32_bf16 v[68:71], a[228:231], v[88:91], v[68:71]\n" \
"v_mfma_f32_16x16x32_bf16 v[100:103], a[224:227], v[112:115], v[100:103]\n" \
"ds_read_b64_tr_b16 a[212:213], v215 offset:25088\n" \
"v_mfma_f32_16x16x32_bf16 v[100:103], a[228:231], v[120:123], v[100:103]\n" \
"s_waitcnt vmcnt(63) lgkmcnt(5)\n" \
"s_nop 0\n" \
"v_mfma_f32_16x16x32_bf16 v[12:15], a[232:235], v[0:3], 0\n" \
"ds_read_b64_tr_b16 a[214:215], v215 offset:27136\n" \
"s_cmp_gt_u32 s48, 0\n" \
"s_cselect_b32 s50, 0, 1\n" \
"s_mul_i32 s50, s50, 152064\n" \
"buffer_load_dwordx4 a[64:67], %[oq0], %[srdz], s50 offen offset:0 sc1\n" \
"v_mfma_f32_16x16x32_bf16 v[12:15], a[236:239], v[8:11], v[12:15]\n" \
"ds_read_b64_tr_b16 a[216:217], v215 offset:29184\n" \
"v_mfma_f32_16x16x32_bf16 v[44:47], a[232:235], v[32:35], 0\n" \
"ds_read_b64_tr_b16 a[218:219], v215 offset:31232\n" \
"buffer_load_dwordx4 a[68:71], %[oq0], %[srdz], s50 offen offset:1024 sc1\n" \
"v_mfma_f32_16x16x32_bf16 v[44:47], a[236:239], v[40:43], v[44:47]\n" \
"v_mfma_f32_16x16x32_bf16 v[76:79], a[232:235], v[64:67], 0\n" \
"buffer_load_dwordx4 a[80:83], v208, %[srdz], s50 offen offset:0 sc1\n" \
"v_mfma_f32_16x16x32_bf16 v[76:79], a[236:239], v[72:75], v[76:79]\n" \
"v_mfma_f32_16x16x32_bf16 v[108:111], a[232:235], v[96:99], 0\n" \
"ds_read_b64_tr_b16 a[220:221], v227 offset:16896\n" \
"buffer_load_dwordx4 a[84:87], v208, %[srdz], s50 offen offset:1024 sc1\n" \
"v_mfma_f32_16x16x32_bf16 v[108:111], a[236:239], v[104:107], v[108:111]\n" \
"ds_read_b64_tr_b16 a[222:223], v227 offset:18944\n" \
"v_mfma_f32_16x16x32_bf16 v[12:15], a[196:199], v[16:19], v[12:15]\n" \
"ds_read_b64_tr_b16 a[224:225], v227 offset:25088\n" \
"buffer_load_dwordx4 a[96:99], v209, %[srdz], s50 offen offset:0 sc1\n" \
"v_mfma_f32_16x16x32_bf16 v[12:15], a[200:203], v[24:27], v[12:15]\n" \
"ds_read_b64_tr_b16 a[226:227], v227 offset:27136\n" \
"v_mfma_f32_16x16x32_bf16 v[44:47], a[196:199], v[48:51], v[44:47]\n" \
"ds_read_b64_tr_b16 a[228:229], v227 offset:20992\n" \
"buffer_load_dwordx4 a[100:103], v209, %[srdz], s50 offen offset:1024 sc1\n" \
"v_mfma_f32_16x16x32_bf16 v[44:47], a[200:203], v[56:59], v[44:47]\n" \
"ds_read_b64_tr_b16 a[230:231], v227 offset:23040\n" \
"v_mfma_f32_16x16x32_bf16 v[76:79], a[196:199], v[80:83], v[76:79]\n" \
"ds_read_b64_tr_b16 a[232:233], v227 offset:29184\n" \
"buffer_load_dwordx4 a[112:115], v210, %[srdz], s50 offen offset:0 sc1\n" \
"v_mfma_f32_16x16x32_bf16 v[76:79], a[200:203], v[88:91], v[76:79]\n" \
"ds_read_b64_tr_b16 a[234:235], v227 offset:31232\n" \
"v_mfma_f32_16x16x32_bf16 v[108:111], a[196:199], v[112:115], v[108:111]\n" \
"ds_read_b64_tr_b16 a[236:237], v215 offset:32768\n" \
"buffer_load_dwordx4 a[116:119], v210, %[srdz], s50 offen offset:1024 sc1\n" \
"v_mfma_f32_16x16x32_bf16 v[108:111], a[200:203], v[120:123], v[108:111]\n" \
"s_waitcnt vmcnt(63) lgkmcnt(13)\n" \
"s_nop 0\n" \
"v_mfma_f32_16x16x32_bf16 v[20:23], a[204:207], v[0:3], 0\n" \
"ds_read_b64_tr_b16 a[238:239], v215 offset:34816\n" \
"buffer_load_dwordx4 a[72:75], %[oq0], %[srdz], s50 offen offset:2048 sc1\n" \
"v_mfma_f32_16x16x32_bf16 v[20:23], a[208:211], v[8:11], v[20:23]\n" \
"ds_read_b64_tr_b16 a[196:197], v215 offset:36864\n" \
"v_mfma_f32_16x16x32_bf16 v[52:55], a[204:207], v[32:35], 0\n" \
"ds_read_b64_tr_b16 a[198:199], v215 offset:38912\n" \
"buffer_load_dwordx4 a[76:79], %[oq0], %[srdz], s50 offen offset:3072 sc1\n" \
"v_mfma_f32_16x16x32_bf16 v[52:55], a[208:211], v[40:43], v[52:55]\n" \
"v_mfma_f32_16x16x32_bf16 v[84:87], a[204:207], v[64:67], 0\n" \
"s_waitcnt vmcnt(63) lgkmcnt(4)\n" \
"s_barrier\n" \
"v_mfma_f32_16x16x32_bf16 v[84:87], a[208:211], v[72:75], v[84:87]\n" \
"buffer_load_dwordx4 a[88:91], v208, %[srdz], s50 offen offset:2048 sc1\n" \
"v_mfma_f32_16x16x32_bf16 v[116:119], a[204:207], v[96:99], 0\n" \
"ds_read_b64_tr_b16 a[200:201], v215 offset:40960\n" \
"v_mfma_f32_16x16x32_bf16 v[116:119], a[208:211], v[104:107], v[116:119]\n" \
"ds_read_b64_tr_b16 a[202:203], v215 offset:43008\n" \
"buffer_load_dwordx4 a[92:95], v208, %[srdz], s50 offen offset:3072 sc1\n" \
"v_mfma_f32_16x16x32_bf16 v[20:23], a[212:215], v[16:19], v[20:23]\n" \
"ds_read_b64_tr_b16 a[204:205], v215 offset:45056\n" \
"v_mfma_f32_16x16x32_bf16 v[20:23], a[216:219], v[24:27], v[20:23]\n" \
"ds_read_b64_tr_b16 a[206:207], v215 offset:47104\n" \
"buffer_load_dwordx4 a[104:107], v209, %[srdz], s50 offen offset:2048 sc1\n" \
"s_waitcnt vmcnt(15)\n" \
"s_nop 0\n" \
"v_mfma_f32_16x16x32_bf16 v[52:55], a[212:215], v[48:51], v[52:55]\n" \
"v_mfma_f32_16x16x32_bf16 v[216:219], a[176:179], v[0:3], 0\n" \
"v_mfma_f32_16x16x32_bf16 v[216:219], a[180:183], v[8:11], v[216:219]\n" \
"v_mfma_f32_16x16x32_bf16 v[216:219], a[184:187], v[16:19], v[216:219]\n" \
"v_mfma_f32_16x16x32_bf16 v[216:219], a[188:191], v[24:27], v[216:219]\n" \
"v_mfma_f32_16x16x32_bf16 v[52:55], a[216:219], v[56:59], v[52:55]\n" \
"buffer_load_dwordx4 a[108:111], v209, %[srdz], s50 offen offset:3072 sc1\n" \
"v_mfma_f32_16x16x32_bf16 v[84:87], a[212:215], v[80:83], v[84:87]\n" \
"v_mfma_f32_16x16x32_bf16 v[84:87], a[216:219], v[88:91], v[84:87]\n" \
"buffer_load_dwordx4 a[120:123], v210, %[srdz], s50 offen offset:2048 sc1\n" \
"v_mfma_f32_16x16x32_bf16 v[116:119], a[212:215], v[112:115], v[116:119]\n" \
"v_mfma_f32_16x16x32_bf16 v[232:235], a[176:179], v[32:35], 0\n" \
"v_mfma_f32_16x16x32_bf16 v[232:235], a[180:183], v[40:43], v[232:235]\n" \
"v_mfma_f32_16x16x32_bf16 v[232:235], a[184:187], v[48:51], v[232:235]\n" \
"v_mfma_f32_16x16x32_bf16 v[232:235], a[188:191], v[56:59], v[232:235]\n" \
"v_mfma_f32_16x16x32_bf16 v[116:119], a[216:219], v[120:123], v[116:119]\n" \
"s_waitcnt vmcnt(63) lgkmcnt(10)\n" \
"s_nop 0\n" \
"v_mfma_f32_16x16x32_bf16 v[28:31], a[220:223], v[0:3], 0\n" \
"buffer_load_dwordx4 a[124:127], v210, %[srdz], s50 offen offset:3072 sc1\n" \
"v_mfma_f32_16x16x32_bf16 v[28:31], a[228:231], v[8:11], v[28:31]\n" \
"s_sub_u32 s61, s48, 4\n" \
"s_cmp_lt_u32 s61, 2\n" \
"s_cselect_b64 vcc, -1, 0\n" \
"s_nop 0\n" \
"v_mfma_f32_16x16x32_bf16 v[60:63], a[220:223], v[32:35], 0\n" \
"s_cbranch_vccz Lg5_%=\n" \
"s_nop 0\n" \
"buffer_store_dwordx4 v[0:3], %[oq0], %[srdz], s60 offen offset:0 sc1\n" \
"Lg5_%=:\n" \
"v_mov_b32_dpp v231, v216 quad_perm:[0,1,2,3] row_mask:0x1 bank_mask:0xf\n" \
"v_mfma_f32_16x16x32_bf16 v[216:219], a[176:179], v[64:67], 0\n" \
"v_mfma_f32_16x16x32_bf16 v[216:219], a[180:183], v[72:75], v[216:219]\n" \
"v_mfma_f32_16x16x32_bf16 v[216:219], a[184:187], v[80:83], v[216:219]\n" \
"v_mfma_f32_16x16x32_bf16 v[216:219], a[188:191], v[88:91], v[216:219]\n" \
"v_mfma_f32_16x16x32_bf16 v[60:63], a[228:231], v[40:43], v[60:63]\n" \
"v_mfma_f32_16x16x32_bf16 v[92:95], a[220:223], v[64:67], 0\n" \
"s_cmp_gt_u32 s48, 1\n" \
"s_cselect_b32 s50, 1, 2\n" \
"s_mul_i32 s50, s50, 152064\n" \
"buffer_load_dwordx4 a[0:3], %[oq0], %[srdz], s50 offen offset:0 sc1\n" \
"v_mfma_f32_16x16x32_bf16 v[92:95], a[228:231], v[72:75], v[92:95]\n" \
"v_mfma_f32_16x16x32_bf16 v[124:127], a[220:223], v[96:99], 0\n" \
"ds_read_b64_tr_b16 a[208:209], v227 offset:32768\n" \
"v_mov_b32_dpp v231, v232 quad_perm:[0,1,2,3] row_mask:0x2 bank_mask:0xf\n" \
"v_mfma_f32_16x16x32_bf16 v[232:235], a[176:179], v[96:99], 0\n" \
"v_mfma_f32_16x16x32_bf16 v[232:235], a[180:183], v[104:107], v[232:235]\n" \
"v_mfma_f32_16x16x32_bf16 v[232:235], a[184:187], v[112:115], v[232:235]\n" \
"v_mfma_f32_16x16x32_bf16 v[232:235], a[188:191], v[120:123], v[232:235]\n" \
"v_mfma_f32_16x16x32_bf16 v[124:127], a[228:231], v[104:107], v[124:127]\n" \
"ds_read_b64_tr_b16 a[210:211], v227 offset:34816\n" \
"s_waitcnt vmcnt(15) lgkmcnt(6)\n" \
"s_nop 0\n" \
"v_mfma_f32_16x16x32_bf16 v[128:131], a[236:239], a[64:67], v[192:195]\n" \
"ds_read_b64_tr_b16 a[212:213], v227 offset:36864\n" \
"v_mfma_f32_16x16x32_bf16 v[128:131], a[196:199], a[68:71], v[128:131]\n" \
"ds_read_b64_tr_b16 a[214:215], v227 offset:38912\n" \
"s_waitcnt vmcnt(13) lgkmcnt(15)\n" \
"s_nop 0\n" \
"v_mfma_f32_16x16x32_bf16 v[132:135], a[236:239], a[80:83], v[196:199]\n" \
"s_cbranch_vccz Lg6_%=\n" \
"s_nop 0\n" \
"buffer_store_dwordx4 v[8:11], %[oq0], %[srdz], s60 offen offset:1024 sc1\n" \
"Lg6_%=:\n" \
"v_mov_b32_dpp v231, v216 quad_perm:[0,1,2,3] row_mask:0x4 bank_mask:0xf\n" \
"v_readlane_b32 s41, %[tlo], 33\n" \
"v_mfma_f32_16x16x32_bf16 v[132:135], a[196:199], a[84:87], v[132:135]\n" \
"s_waitcnt vmcnt(13) lgkmcnt(15)\n" \
"s_bitcmp1_b32 0x30, s48\n" \
"s_cbranch_scc1 Lcw2_%=\n" \
"s_waitcnt vmcnt(11)\n" \
"Lcw2_%=:\n" \
"v_mfma_f32_16x16x32_bf16 v[136:139], a[236:239], a[96:99], v[200:203]\n" \
"buffer_load_dwordx4 a[4:7], %[oq0], %[srdz], s50 offen offset:1024 sc1\n" \
"v_mfma_f32_16x16x32_bf16 v[136:139], a[196:199], a[100:103], v[136:139]\n" \
"v_mov_b32_dpp v231, v232 quad_perm:[0,1,2,3] row_mask:0x8 bank_mask:0xf\n" \
"v_lshrrev_b32_e64 v222, 5, v224\n" \
"s_waitcnt vmcnt(12) lgkmcnt(15)\n" \
"s_bitcmp1_b32 0x30, s48\n" \
"s_cbranch_scc1 Lcw3_%=\n" \
"s_waitcnt vmcnt(10)\n" \
"Lcw3_%=:\n" \
"v_mfma_f32_16x16x32_bf16 v[140:143], a[236:239], a[112:115], v[204:207]\n" \
"ds_read_b64_tr_b16 a[216:217], v227 offset:40960\n" \
"v_cmp_u_f32_e64 s[42:43], v231, v231\n" \
"v_mfma_f32_16x16x32_bf16 v[140:143], a[196:199], a[116:119], v[140:143]\n" \
"s_waitcnt vmcnt(10) lgkmcnt(5)\n" \
"s_bitcmp1_b32 0x30, s48\n" \
"s_cbranch_scc1 Lcw4_%=\n" \
"s_waitcnt vmcnt(8)\n" \
"Lcw4_%=:\n" \
"v_mfma_f32_16x16x32_bf16 v[128:131], a[200:203], a[72:75], v[128:131]\n" \
"ds_read_b64_tr_b16 a[218:219], v227 offset:43008\n" \
"s_cmp_lg_u64 s[42:43], 0\n" \
"s_cbranch_scc1 Ls0_%=\n" \
"Ls0b_%=:\n" \
"v_or_b32_e64 v231, 1, v231\n" \
"buffer_load_dwordx4 a[8:11], %[oq0], %[srdz], s50 offen offset:2048 sc1\n" \
"v_mfma_f32_16x16x32_bf16 v[128:131], a[204:207], a[76:79], v[128:131]\n" \
"ds_read_b64_tr_b16 a[220:221], v227 offset:45056\n" \
"s_waitcnt vmcnt(9) lgkmcnt(15)\n" \
"s_bitcmp1_b32 0x30, s48\n" \
"s_cbranch_scc1 Lcw5_%=\n" \
"s_waitcnt vmcnt(7)\n" \
"Lcw5_%=:\n" \
"v_mfma_f32_16x16x32_bf16 v[132:135], a[200:203], a[88:91], v[132:135]\n" \
"ds_read_b64_tr_b16 a[222:223], v227 offset:47104\n" \
"v_mfma_f32_16x16x32_bf16 v[132:135], a[204:207], a[92:95], v[132:135]\n" \
"s_cbranch_vccz Lg7_%=\n" \
"s_nop 0\n" \
"buffer_store_dwordx4 v[16:19], %[oq0], %[srdz], s60 offen offset:2048 sc1\n" \
"Lg7_%=:\n" \
"s_waitcnt vmcnt(6) lgkmcnt(14)\n" \
"s_bitcmp1_b32 0x30, s48\n" \
"s_cbranch_scc1 Lcw6_%=\n" \
"s_waitcnt vmcnt(3)\n" \
"Lcw6_%=:\n" \
"v_mfma_f32_16x16x32_bf16 v[136:139], a[200:203], a[104:107], v[136:139]\n" \
"v_mfma_f32_16x16x32_bf16 v[136:139], a[204:207], a[108:111], v[136:139]\n" \
"buffer_store_dword v231, v222, %[srdz], s41 offen sc1\n" \
"v_mfma_f32_16x16x32_bf16 v[140:143], a[200:203], a[120:123], v[140:143]\n" \
"ds_read_b64_tr_b16 a[228:229], v215 offset:33280\n" \
"v_mfma_f32_16x16x32_bf16 v[140:143], a[204:207], a[124:127], v[140:143]\n" \
"ds_read_b64_tr_b16 a[230:231], v215 offset:35328\n" \
"buffer_load_dwordx4 a[12:15], %[oq0], %[srdz], s50 offen offset:3072 sc1\n" \
"v_mfma_f32_16x16x32_bf16 v[28:31], a[224:227], v[16:19], v[28:31]\n" \
"v_mfma_f32_16x16x32_bf16 v[28:31], a[232:235], v[24:27], v[28:31]\n" \
"buffer_load_dwordx4 a[16:19], v208, %[srdz], s50 offen offset:0 sc1\n" \
"v_mfma_f32_16x16x32_bf16 v[60:63], a[224:227], v[48:51], v[60:63]\n" \
"v_mfma_f32_16x16x32_bf16 v[60:63], a[232:235], v[56:59], v[60:63]\n" \
"s_cbranch_vccz Lg8_%=\n" \
"s_nop 0\n" \
"buffer_store_dwordx4 v[24:27], %[oq0], %[srdz], s60 offen offset:3072 sc1\n" \
"Lg8_%=:\n" \
"v_mfma_f32_16x16x32_bf16 v[92:95], a[224:227], v[80:83], v[92:95]\n" \
"v_mfma_f32_16x16x32_bf16 v[92:95], a[232:235], v[88:91], v[92:95]\n" \
"v_cmp_u_f32_e64 s[44:45], v128, v132\n" \
"buffer_load_dwordx4 a[20:23], v208, %[srdz], s50 offen offset:1024 sc1\n" \
"v_mfma_f32_16x16x32_bf16 v[124:127], a[224:227], v[112:115], v[124:127]\n" \
"v_mfma_f32_16x16x32_bf16 v[124:127], a[232:235], v[120:123], v[124:127]\n" \
"v_cmp_u_f32_e64 s[56:57], v136, v140\n" \
"s_or_b64 s[56:57], s[56:57], s[44:45]\n" \
"s_cbranch_scc1 Lsp1_%=\n" \
"Lbk1_%=:\n" \
"s_waitcnt vmcnt(63) lgkmcnt(2)\n" \
"s_nop 0\n" \
"v_mfma_f32_16x16x32_bf16 v[144:147], a[208:211], a[64:67], v[144:147]\n" \
"ds_read_b64_tr_b16 a[224:225], v215 offset:37376\n" \
"v_mfma_f32_16x16x32_bf16 v[144:147], a[212:215], a[68:71], v[144:147]\n" \
"ds_read_b64_tr_b16 a[226:227], v215 offset:39424\n" \
"buffer_load_dwordx4 a[24:27], v208, %[srdz], s50 offen offset:2048 sc1\n" \
"v_mfma_f32_16x16x32_bf16 v[148:151], a[208:211], a[80:83], v[148:151]\n" \
"ds_read_b64_tr_b16 a[196:197], v215 offset:41472\n" \
"v_mfma_f32_16x16x32_bf16 v[148:151], a[212:215], a[84:87], v[148:151]\n" \
"ds_read_b64_tr_b16 a[198:199], v215 offset:43520\n" \
"v_mfma_f32_16x16x32_bf16 v[152:155], a[208:211], a[96:99], v[152:155]\n" \
"ds_read_b64_tr_b16 a[200:201], v215 offset:45568\n" \
"s_cbranch_vccz Lg9_%=\n" \
"s_nop 0\n" \
"buffer_store_dwordx4 v[32:35], v208, %[srdz], s60 offen offset:0 sc1\n" \
"Lg9_%=:\n" \
"v_mfma_f32_16x16x32_bf16 v[152:155], a[212:215], a[100:103], v[152:155]\n" \
"ds_read_b64_tr_b16 a[202:203], v215 offset:47616\n" \
"v_mfma_f32_16x16x32_bf16 v[156:159], a[208:211], a[112:115], v[156:159]\n" \
"ds_read_b64_tr_b16 a[204:205], v227 offset:33280\n" \
"buffer_load_dwordx4 a[28:31], v208, %[srdz], s50 offen offset:3072 sc1\n" \
"v_mfma_f32_16x16x32_bf16 v[156:159], a[212:215], a[116:119], v[156:159]\n" \
"ds_read_b64_tr_b16 a[206:207], v227 offset:35328\n" \
"v_mfma_f32_16x16x32_bf16 v[144:147], a[216:219], a[72:75], v[144:147]\n" \
"ds_read_b64_tr_b16 a[232:233], v227 offset:37376\n" \
"v_mfma_f32_16x16x32_bf16 v[144:147], a[220:223], a[76:79], v[144:147]\n" \
"ds_read_b64_tr_b16 a[234:235], v227 offset:39424\n" \
"v_mfma_f32_16x16x32_bf16 v[148:151], a[216:219], a[88:91], v[148:151]\n" \
"buffer_load_dwordx4 a[32:35], v209, %[srdz], s50 offen offset:0 sc1\n" \
"v_mfma_f32_16x16x32_bf16 v[148:151], a[220:223], a[92:95], v[148:151]\n" \
"v_mfma_f32_16x16x32_bf16 v[152:155], a[216:219], a[104:107], v[152:155]\n" \
"s_cbranch_vccz Lg10_%=\n" \
"s_nop 0\n" \
"buffer_store_dwordx4 v[40:43], v208, %[srdz], s60 offen offset:1024 sc1\n" \
"Lg10_%=:\n" \
"v_mfma_f32_16x16x32_bf16 v[152:155], a[220:223], a[108:111], v[152:155]\n" \
"v_mfma_f32_16x16x32_bf16 v[156:159], a[216:219], a[120:123], v[156:159]\n" \
"ds_read_b64_tr_b16 a[236:237], v227 offset:41472\n" \
"buffer_load_dwordx4 a[36:39], v209, %[srdz], s50 offen offset:1024 sc1\n" \
"v_mfma_f32_16x16x32_bf16 v[156:159], a[220:223], a[124:127], v[156:159]\n" \
"s_waitcnt vmcnt(63) lgkmcnt(5)\n" \
"s_nop 0\n" \
"v_mfma_f32_16x16x32_bf16 v[160:163], a[228:231], a[64:67], v[160:163]\n" \
"ds_read_b64_tr_b16 a[238:239], v227 offset:43520\n" \
"v_mfma_f32_16x16x32_bf16 v[160:163], a[224:227], a[68:71], v[160:163]\n" \
"ds_read_b64_tr_b16 a[208:209], v227 offset:45568\n" \
"v_mfma_f32_16x16x32_bf16 v[164:167], a[228:231], a[80:83], v[164:167]\n" \
"ds_read_b64_tr_b16 a[210:211], v227 offset:47616\n" \
"buffer_load_dwordx4 a[40:43], v209, %[srdz], s50 offen offset:2048 sc1\n" \
"v_mfma_f32_16x16x32_bf16 v[164:167], a[224:227], a[84:87], v[164:167]\n" \
"v_mfma_f32_16x16x32_bf16 v[168:171], a[228:231], a[96:99], v[168:171]\n" \
"v_mfma_f32_16x16x32_bf16 v[168:171], a[224:227], a[100:103], v[168:171]\n" \
"s_cbranch_vccz Lg11_%=\n" \
"s_nop 0\n" \
"buffer_store_dwordx4 v[48:51], v208, %[srdz], s60 offen offset:2048 sc1\n" \
"Lg11_%=:\n" \
"v_mfma_f32_16x16x32_bf16 v[172:175], a[228:231], a[112:115], v[172:175]\n" \
"v_mfma_f32_16x16x32_bf16 v[172:175], a[224:227], a[116:119], v[172:175]\n" \
"buffer_load_dwordx4 a[44:47], v209, %[srdz], s50 offen offset:3072 sc1\n" \
"v_mfma_f32_16x16x32_bf16 v[160:163], a[196:199], a[72:75], v[160:163]\n" \
"v_mfma_f32_16x16x32_bf16 v[160:163], a[200:203], a[76:79], v[160:163]\n" \
"buffer_load_dwordx4 a[48:51], v210, %[srdz], s50 offen offset:0 sc1\n" \
"v_mfma_f32_16x16x32_bf16 v[164:167], a[196:199], a[88:91], v[164:167]\n" \
"v_mfma_f32_16x16x32_bf16 v[164:167], a[200:203], a[92:95], v[164:167]\n" \
"buffer_load_dwordx4 a[52:55], v210, %[srdz], s50 offen offset:1024 sc1\n" \
"v_mfma_f32_16x16x32_bf16 v[168:171], a[196:199], a[104:107], v[168:171]\n" \
"s_waitcnt vmcnt(60) lgkmcnt(0)\n" \
"s_bitcmp1_b32 0xf, s48\n" \
"s_cbranch_scc1 Lcw7_%=\n" \
"s_waitcnt vmcnt(51)\n" \
".if . - adaln_body != 45056\n" \
".error \"size model\"\n" \
".endif\n" \
"Lk10_%=:\n" \
"s_cbranch_execz Lk11_%=\n" \
"s_nop 0\n" \
"s_bitcmp1_b32 0x30, s48\n" \
"s_cbranch_scc1 Lcw7_%=\n" \
"s_waitcnt vmcnt(44)\n" \
"Lcw7_%=:\n" \
"s_barrier\n" \
"v_mfma_f32_16x16x32_bf16 v[168:171], a[200:203], a[108:111], v[168:171]\n" \
"ds_read_b64_tr_b16 a[212:213], v220 offset:32768\n" \
"s_cbranch_vccz Lg12_%=\n" \
"s_nop 0\n" \
"buffer_store_dwordx4 v[56:59], v208, %[srdz], s60 offen offset:3072 sc1\n" \
"Lg12_%=:\n" \
"v_mfma_f32_16x16x32_bf16 v[172:175], a[196:199], a[120:123], v[172:175]\n" \
"ds_read_b64_tr_b16 a[214:215], v220 offset:34816\n" \
"v_mfma_f32_16x16x32_bf16 v[172:175], a[200:203], a[124:127], v[172:175]\n" \
"s_waitcnt vmcnt(63) lgkmcnt(2)\n" \
"s_nop 0\n" \
"v_mfma_f32_16x16x32_bf16 v[176:179], a[204:207], a[64:67], v[176:179]\n" \
"ds_read_b64_tr_b16 a[216:217], v220 offset:36864\n" \
"buffer_load_dwordx4 a[56:59], v210, %[srdz], s50 offen offset:2048 sc1\n" \
"v_mfma_f32_16x16x32_bf16 v[176:179], a[232:235], a[68:71], v[176:179]\n" \
"ds_read_b64_tr_b16 a[218:219], v220 offset:38912\n" \
"v_mfma_f32_16x16x32_bf16 v[180:183], a[204:207], a[80:83], v[180:183]\n" \
"ds_read_b64_tr_b16 a[220:221], v220 offset:40960\n" \
"v_mfma_f32_16x16x32_bf16 v[180:183], a[232:235], a[84:87], v[180:183]\n" \
"ds_read_b64_tr_b16 a[222:223], v220 offset:43008\n" \
"v_mfma_f32_16x16x32_bf16 v[184:187], a[204:207], a[96:99], v[184:187]\n" \
"ds_read_b64_tr_b16 a[228:229], v220 offset:45056\n" \
"buffer_load_dwordx4 a[60:63], v210, %[srdz], s50 offen offset:3072 sc1\n" \
"v_mfma_f32_16x16x32_bf16 v[184:187], a[232:235], a[100:103], v[184:187]\n" \
"ds_read_b64_tr_b16 a[230:231], v220 offset:47104\n" \
"v_mfma_f32_16x16x32_bf16 v[188:191], a[204:207], a[112:115], v[188:191]\n" \
"ds_read_b64_tr_b16 a[224:225], v221 offset:32768\n" \
"s_cbranch_vccz Lg13_%=\n" \
"s_nop 0\n" \
"buffer_store_dwordx4 v[64:67], v209, %[srdz], s60 offen offset:0 sc1\n" \
"Lg13_%=:\n" \
"v_mfma_f32_16x16x32_bf16 v[188:191], a[232:235], a[116:119], v[188:191]\n" \
"ds_read_b64_tr_b16 a[226:227], v221 offset:34816\n" \
"v_mfma_f32_16x16x32_bf16 v[176:179], a[236:239], a[72:75], v[176:179]\n" \
"ds_read_b64_tr_b16 a[196:197], v221 offset:36864\n" \
"v_mfma_f32_16x16x32_bf16 v[176:179], a[208:211], a[76:79], v[176:179]\n" \
"ds_read_b64_tr_b16 a[198:199], v221 offset:38912\n" \
"v_mfma_f32_16x16x32_bf16 v[180:183], a[236:239], a[88:91], v[180:183]\n" \
"s_cmp_gt_u32 s48, 2\n" \
"s_cselect_b32 s50, 2, 3\n" \
"s_mul_i32 s50, s50, 152064\n" \
"buffer_load_dwordx4 a[128:131], %[oq0], %[srdz], s50 offen offset:0 sc1\n" \
"v_mfma_f32_16x16x32_bf16 v[180:183], a[208:211], a[92:95], v[180:183]\n" \
"v_mfma_f32_16x16x32_bf16 v[184:187], a[236:239], a[104:107], v[184:187]\n" \
"v_mfma_f32_16x16x32_bf16 v[184:187], a[208:211], a[108:111], v[184:187]\n" \
"v_mfma_f32_16x16x32_bf16 v[188:191], a[236:239], a[120:123], v[188:191]\n" \
"ds_read_b64_tr_b16 a[200:201], v221 offset:40960\n" \
"s_cbranch_vccz Lg14_%=\n" \
"s_nop 0\n" \
"buffer_store_dwordx4 v[72:75], v209, %[srdz], s60 offen offset:1024 sc1\n" \
"Lg14_%=:\n" \
"v_mfma_f32_16x16x32_bf16 v[188:191], a[208:211], a[124:127], v[188:191]\n" \
"s_waitcnt vmcnt(63) lgkmcnt(5)\n" \
"s_nop 0\n" \
"v_mfma_f32_16x16x32_bf16 v[4:7], a[212:215], a[64:67], v[4:7]\n" \
"ds_read_b64_tr_b16 a[202:203], v221 offset:43008\n" \
"v_mfma_f32_16x16x32_bf16 v[4:7], a[216:219], a[68:71], v[4:7]\n" \
"ds_read_b64_tr_b16 a[204:205], v221 offset:45056\n" \
"s_add_u32 m0, %[m0b], 0\n" \
"s_cmp_gt_u32 s48, 2\n" \
"s_cselect_b32 s49, 2, 3\n" \
"s_lshl_b32 s49, s49, 18\n" \
"s_or_b32 s49, s49, s58\n" \
"s_nop 0\n" \
"buffer_load_dwordx4 %[wo0], %[srdt], s49 offen offset:0 lds\n" \
"v_mfma_f32_16x16x32_bf16 v[36:39], a[212:215], a[80:83], v[36:39]\n" \
"ds_read_b64_tr_b16 a[206:207], v221 offset:47104\n" \
"v_mfma_f32_16x16x32_bf16 v[36:39], a[216:219], a[84:87], v[36:39]\n" \
"buffer_load_dwordx4 a[132:135], %[oq0], %[srdz], s50 offen offset:1024 sc1\n" \
"v_mfma_f32_16x16x32_bf16 v[68:71], a[212:215], a[96:99], v[68:71]\n" \
"v_mfma_f32_16x16x32_bf16 v[68:71], a[216:219], a[100:103], v[68:71]\n" \
"v_mfma_f32_16x16x32_bf16 v[100:103], a[212:215], a[112:115], v[100:103]\n" \
"ds_read_b64_tr_b16 a[232:233], v220 offset:33280\n" \
"v_mfma_f32_16x16x32_bf16 v[100:103], a[216:219], a[116:119], v[100:103]\n" \
"ds_read_b64_tr_b16 a[234:235], v220 offset:35328\n" \
"s_cbranch_vccz Lg15_%=\n" \
"s_nop 0\n" \
"buffer_store_dwordx4 v[80:83], v209, %[srdz], s60 offen offset:2048 sc1\n" \
"Lg15_%=:\n" \
"v_mfma_f32_16x16x32_bf16 v[4:7], a[220:223], a[72:75], v[4:7]\n" \
"ds_read_b64_tr_b16 a[236:237], v220 offset:37376\n" \
"s_lshl_b32 s40, %[wid], 12\n" \
"s_nop 0\n" \
"s_mov_b32 s43, 0xffff0000\n" \
"v_mfma_f32_16x16x32_bf16 v[4:7], a[228:231], a[76:79], v[4:7]\n" \
"ds_read_b64_tr_b16 a[238:239], v220 offset:39424\n" \
"buffer_load_dwordx4 a[136:139], %[oq0], %[srdz], s50 offen offset:2048 sc1\n" \
"v_mfma_f32_16x16x32_bf16 v[36:39], a[220:223], a[88:91], v[36:39]\n" \
"v_lshrrev_b32_e32 v222, 7, v224\n" \
"s_sub_u32 s40, %[m0b], s40\n" \
"v_mfma_f32_16x16x32_bf16 v[36:39], a[228:231], a[92:95], v[36:39]\n" \
"v_readlane_b32 s41, %[tlo], 26\n" \
"v_mfma_f32_16x16x32_bf16 v[68:71], a[220:223], a[104:107], v[68:71]\n" \
"v_readlane_b32 s42, %[thi], 26\n" \
"v_mfma_f32_16x16x32_bf16 v[68:71], a[228:231], a[108:111], v[68:71]\n" \
"buffer_load_dwordx4 a[140:143], %[oq0], %[srdz], s50 offen offset:3072 sc1\n" \
"v_mfma_f32_16x16x32_bf16 v[100:103], a[220:223], a[120:123], v[100:103]\n" \
"ds_read_b64_tr_b16 a[208:209], v220 offset:41472\n" \
"v_lshrrev_b32_e64 v217, 3, v224\n" \
"v_mfma_f32_16x16x32_bf16 v[100:103], a[228:231], a[124:127], v[100:103]\n" \
"s_waitcnt vmcnt(63) lgkmcnt(5)\n" \
"s_nop 0\n" \
"v_mfma_f32_16x16x32_bf16 v[12:15], a[224:227], a[64:67], v[12:15]\n" \
"ds_read_b64_tr_b16 a[210:211], v220 offset:43520\n" \
"s_cbranch_vccz Lg16_%=\n" \
"s_nop 0\n" \
"buffer_store_dwordx4 v[88:91], v209, %[srdz], s60 offen offset:3072 sc1\n" \
"Lg16_%=:\n" \
"v_mfma_f32_16x16x32_bf16 v[12:15], a[196:199], a[68:71], v[12:15]\n" \
"ds_read_b64_tr_b16 a[212:213], v220 offset:45568\n" \
"v_lshrrev_b32_e64 v219, 5, v222\n" \
"v_mfma_f32_16x16x32_bf16 v[44:47], a[224:227], a[80:83], v[44:47]\n" \
"ds_read_b64_tr_b16 a[214:215], v220 offset:47616\n" \
"buffer_load_dwordx4 v212, %[srdt], s49 offen offset:1024 lds\n" \
"v_mfma_f32_16x16x32_bf16 v[44:47], a[196:199], a[84:87], v[44:47]\n" \
"v_bfe_u32 v235, v222, 2, 1\n" \
"v_mfma_f32_16x16x32_bf16 v[76:79], a[224:227], a[96:99], v[76:79]\n" \
"v_lshl_or_b32 v219, v219, 1, v235\n" \
"v_mfma_f32_16x16x32_bf16 v[76:79], a[196:199], a[100:103], v[76:79]\n" \
"buffer_load_dwordx4 a[144:147], v208, %[srdz], s50 offen offset:0 sc1\n" \
"v_mfma_f32_16x16x32_bf16 v[108:111], a[224:227], a[112:115], v[108:111]\n" \
"ds_read_b64_tr_b16 a[216:217], v221 offset:33280\n" \
"v_add_u32_e64 v235, s41, v235\n" \
"v_mfma_f32_16x16x32_bf16 v[108:111], a[196:199], a[116:119], v[108:111]\n" \
"ds_read_b64_tr_b16 a[218:219], v221 offset:35328\n" \
"v_sub_u32_e32 %[ta0], 0x7ff, v235\n" \
"v_mfma_f32_16x16x32_bf16 v[12:15], a[200:203], a[72:75], v[12:15]\n" \
"ds_read_b64_tr_b16 a[220:221], v221 offset:41472\n" \
"s_cbranch_vccz Lg17_%=\n" \
"s_nop 0\n" \
"buffer_store_dwordx4 v[96:99], v210, %[srdz], s60 offen offset:0 sc1\n" \
"Lg17_%=:\n" \
"v_mfma_f32_16x16x32_bf16 v[12:15], a[204:207], a[76:79], v[12:15]\n" \
"ds_read_b64_tr_b16 a[222:223], v221 offset:43520\n" \
"v_and_b32_e64 v234, 16, v222\n" \
"v_mfma_f32_16x16x32_bf16 v[44:47], a[200:203], a[88:91], v[44:47]\n" \
"ds_read_b64_tr_b16 a[228:229], v221 offset:37376\n" \
"v_and_b32_e64 v233, 1, v222\n" \
"v_mfma_f32_16x16x32_bf16 v[44:47], a[204:207], a[92:95], v[44:47]\n" \
"ds_read_b64_tr_b16 a[230:231], v221 offset:39424\n" \
"v_cmp_eq_u32_e64 s[68:69], 1, v233\n" \
"v_mfma_f32_16x16x32_bf16 v[76:79], a[200:203], a[104:107], v[76:79]\n" \
"ds_read_b64_tr_b16 a[224:225], v221 offset:45568\n" \
"s_lshl_b32 s52, %[wid], 2\n" \
"s_nop 0\n" \
"v_mfma_f32_16x16x32_bf16 v[76:79], a[204:207], a[108:111], v[76:79]\n" \
"ds_read_b64_tr_b16 a[226:227], v221 offset:47616\n" \
"v_cndmask_b32_e64 v234, v234, 32, s[68:69]\n" \
"v_mfma_f32_16x16x32_bf16 v[108:111], a[200:203], a[120:123], v[108:111]\n" \
"buffer_load_dwordx4 a[148:151], v208, %[srdz], s50 offen offset:1024 sc1\n" \
"v_mfma_f32_16x16x32_bf16 v[108:111], a[204:207], a[124:127], v[108:111]\n" \
"s_waitcnt vmcnt(63) lgkmcnt(12)\n" \
"s_nop 0\n" \
"v_mfma_f32_16x16x32_bf16 v[20:23], a[232:235], a[64:67], v[20:23]\n" \
"s_cbranch_vccz Lg18_%=\n" \
"s_nop 0\n" \
"buffer_store_dwordx4 v[104:107], v210, %[srdz], s60 offen offset:1024 sc1\n" \
"Lg18_%=:\n" \
"v_mfma_f32_16x16x32_bf16 v[20:23], a[236:239], a[68:71], v[20:23]\n" \
"s_add_u32 s52, s52, s40\n" \
"s_nop 0\n" \
"v_mfma_f32_16x16x32_bf16 v[52:55], a[232:235], a[80:83], v[52:55]\n" \
"buffer_load_dwordx4 v213, %[srdt], s49 offen offset:2048 lds\n" \
"v_mfma_f32_16x16x32_bf16 v[52:55], a[236:239], a[84:87], v[52:55]\n" \
"v_mad_u32_u24 v228, v219, 48, v234\n" \
"s_add_u32 s63, s52, 29184\n" \
"v_mfma_f32_16x16x32_bf16 v[84:87], a[232:235], a[96:99], v[84:87]\n" \
"s_waitcnt vmcnt(63) lgkmcnt(0)\n" \
"s_bitcmp1_b32 0x30, s48\n" \
"s_cbranch_scc1 Lcw8_%=\n" \
"s_waitcnt vmcnt(49)\n" \
"Lcw8_%=:\n" \
"s_barrier\n" \
"s_nop 0\n" \
"v_mfma_f32_16x16x32_bf16 v[84:87], a[236:239], a[100:103], v[84:87]\n" \
"ds_read_b64_tr_b16 a[196:197], v220 offset:49152\n" \
"v_add_u32_e64 v228, s63, v228\n" \
"v_mfma_f32_16x16x32_bf16 v[116:119], a[232:235], a[112:115], v[116:119]\n" \
"ds_read_b64_tr_b16 a[198:199], v220 offset:51200\n" \
"buffer_load_dwordx4 a[152:155], v208, %[srdz], s50 offen offset:2048 sc1\n" \
"v_mfma_f32_16x16x32_bf16 v[116:119], a[236:239], a[116:119], v[116:119]\n" \
"ds_read_b64_tr_b16 a[200:201], v220 offset:53248\n" \
"v_and_b32_e64 v216, 15, v222\n" \
"v_mfma_f32_16x16x32_bf16 v[20:23], a[208:211], a[72:75], v[20:23]\n" \
"ds_read_b64_tr_b16 a[202:203], v220 offset:55296\n" \
"v_lshrrev_b32_e64 v218, 4, v222\n" \
"v_mfma_f32_16x16x32_bf16 v[20:23], a[212:215], a[76:79], v[20:23]\n" \
"ds_read_b64_tr_b16 a[204:205], v220 offset:57344\n" \
"s_cbranch_vccz Lg19_%=\n" \
"s_nop 0\n" \
"buffer_store_dwordx4 v[112:115], v210, %[srdz], s60 offen offset:2048 sc1\n" \
"Lg19_%=:\n" \
"v_mfma_f32_16x16x32_bf16 v[52:55], a[208:211], a[88:91], v[52:55]\n" \
"ds_read_b64_tr_b16 a[206:207], v220 offset:59392\n" \
"v_lshrrev_b32_e64 v230, 3, v216\n" \
"v_mfma_f32_16x16x32_bf16 v[52:55], a[212:215], a[92:95], v[52:55]\n" \
"ds_read_b64_tr_b16 a[232:233], v220 offset:61440\n" \
"v_lshl_or_b32 v230, v230, 1, v233\n" \
"v_mfma_f32_16x16x32_bf16 v[84:87], a[208:211], a[104:107], v[84:87]\n" \
"ds_read_b64_tr_b16 a[234:235], v220 offset:63488\n" \
"s_add_u32 s63, s40, 29184\n" \
"v_mfma_f32_16x16x32_bf16 v[84:87], a[212:215], a[108:111], v[84:87]\n" \
"buffer_load_dwordx4 a[156:159], v208, %[srdz], s50 offen offset:3072 sc1\n" \
"v_mfma_f32_16x16x32_bf16 v[116:119], a[208:211], a[120:123], v[116:119]\n" \
"v_mad_u32_u24 %[vg0], v230, 48, s63\n" \
"s_add_u32 s63, s52, 25600\n" \
"v_mfma_f32_16x16x32_bf16 v[116:119], a[212:215], a[124:127], v[116:119]\n" \
"s_waitcnt vmcnt(21) lgkmcnt(4)\n" \
"s_bitcmp1_b32 0x30, s48\n" \
"s_cbranch_scc1 Lcw9_%=\n" \
"s_waitcnt vmcnt(13)\n" \
"Lcw9_%=:\n" \
"v_mfma_f32_16x16x32_bf16 v[28:31], a[216:219], a[64:67], v[28:31]\n" \
"v_lshlrev_b32_e64 v235, 6, v218\n" \
"v_mfma_f32_16x16x32_bf16 v[28:31], a[228:231], a[68:71], v[28:31]\n" \
"s_cbranch_vccz Lg20_%=\n" \
"s_nop 0\n" \
"buffer_store_dwordx4 v[120:123], v210, %[srdz], s60 offen offset:3072 sc1\n" \
"Lg20_%=:\n" \
"v_mfma_f32_16x16x32_bf16 v[60:63], a[216:219], a[80:83], v[60:63]\n" \
"v_add_u32_e32 v234, s63, v235\n" \
"s_movk_i32 s52, 272\n" \
"v_mfma_f32_16x16x32_bf16 v[60:63], a[228:231], a[84:87], v[60:63]\n" \
"buffer_load_dwordx4 v214, %[srdt], s49 offen offset:3072 lds\n" \
"v_mfma_f32_16x16x32_bf16 v[92:95], a[216:219], a[96:99], v[92:95]\n" \
"v_mad_u32_u24 %[kd0], v216, s52, v234\n" \
"v_mfma_f32_16x16x32_bf16 v[92:95], a[228:231], a[100:103], v[92:95]\n" \
"buffer_load_dwordx4 a[160:163], v209, %[srdz], s50 offen offset:0 sc1\n" \
"v_mfma_f32_16x16x32_bf16 v[124:127], a[216:219], a[112:115], v[124:127]\n" \
"ds_read_b64_tr_b16 a[236:237], v221 offset:49152\n" \
"v_bfe_u32 v234, v222, 1, 2\n" \
"v_mfma_f32_16x16x32_bf16 v[124:127], a[228:231], a[116:119], v[124:127]\n" \
"ds_read_b64_tr_b16 a[238:239], v221 offset:51200\n" \
"v_lshl_add_u32 %[vx0], v234, 4, v235\n" \
"v_mfma_f32_16x16x32_bf16 v[192:195], a[196:199], a[0:3], v[128:131]\n" \
"ds_read_b64_tr_b16 a[208:209], v221 offset:53248\n" \
"s_add_u32 s63, s40, 25600\n" \
"v_mfma_f32_16x16x32_bf16 v[192:195], a[200:203], a[4:7], v[192:195]\n" \
"ds_read_b64_tr_b16 a[210:211], v221 offset:55296\n" \
"v_add_u32_e64 %[vx0], s63, %[vx0]\n" \
"v_mfma_f32_16x16x32_bf16 v[196:199], a[196:199], a[16:19], v[132:135]\n" \
"buffer_load_dwordx4 a[164:167], v209, %[srdz], s50 offen offset:1024 sc1\n" \
"v_mfma_f32_16x16x32_bf16 v[196:199], a[200:203], a[20:23], v[196:199]\n" \
"s_movk_i32 s52, 2112\n" \
"s_nop 0\n" \
"v_mfma_f32_16x16x32_bf16 v[200:203], a[196:199], a[32:35], v[136:139]\n" \
"v_mul_u32_u24_e64 v211, s52, v216\n" \
"v_mfma_f32_16x16x32_bf16 v[200:203], a[200:203], a[36:39], v[200:203]\n" \
"v_add_u32_e32 v211, 16384, v211\n" \
"v_mfma_f32_16x16x32_bf16 v[204:207], a[196:199], a[48:51], v[140:143]\n" \
"ds_read_b64_tr_b16 a[212:213], v221 offset:57344\n" \
"s_sub_u32 s61, s48, 6\n" \
"s_cmp_lt_u32 s61, 2\n" \
"s_cselect_b64 vcc, -1, 0\n" \
"s_nop 0\n" \
"v_mov_b32_e32 v235, 2048\n" \
"v_mfma_f32_16x16x32_bf16 v[204:207], a[200:203], a[52:55], v[204:207]\n" \
"s_waitcnt vmcnt(22) lgkmcnt(5)\n" \
"s_bitcmp1_b32 0x30, s48\n" \
"s_cbranch_scc1 Lcw10_%=\n" \
"s_waitcnt vmcnt(14)\n" \
"Lcw10_%=:\n" \
"v_mfma_f32_16x16x32_bf16 v[192:195], a[204:207], a[8:11], v[192:195]\n" \
"ds_read_b64_tr_b16 a[214:215], v221 offset:59392\n" \
"s_cbranch_vccz Lg21_%=\n" \
"s_nop 0\n" \
"buffer_store_dwordx4 v[0:3], %[oq0], %[srdz], s60 offen offset:0 sc1\n" \
"Lg21_%=:\n" \
"v_mfma_f32_16x16x32_bf16 v[192:195], a[232:235], a[12:15], v[192:195]\n" \
"ds_read_b64_tr_b16 a[216:217], v221 offset:61440\n" \
"v_cmp_eq_u32_e64 s[68:69], 4, v216\n" \
"v_mfma_f32_16x16x32_bf16 v[196:199], a[204:207], a[24:27], v[196:199]\n" \
"ds_read_b64_tr_b16 a[218:219], v221 offset:63488\n" \
"s_lshl_b32 s52, %[wid], 9\n" \
"s_nop 0\n" \
"v_mfma_f32_16x16x32_bf16 v[196:199], a[232:235], a[28:31], v[196:199]\n" \
"buffer_load_dwordx4 a[168:171], v209, %[srdz], s50 offen offset:2048 sc1\n" \
"v_mfma_f32_16x16x32_bf16 v[200:203], a[204:207], a[40:43], v[200:203]\n" \
"v_cndmask_b32_e64 v235, 0, v235, s[68:69]\n" \
"v_mfma_f32_16x16x32_bf16 v[200:203], a[232:235], a[44:47], v[200:203]\n" \
"v_cmp_gt_u32_e64 s[68:69], 4, v216\n" \
"s_add_u32 s52, s52, s40\n" \
"s_nop 0\n" \
"v_mfma_f32_16x16x32_bf16 v[204:207], a[204:207], a[56:59], v[204:207]\n" \
"ds_read_b64_tr_b16 a[228:229], v220 offset:49664\n" \
"v_cndmask_b32_e64 v211, v235, v211, s[68:69]\n" \
"v_mfma_f32_16x16x32_bf16 v[204:207], a[232:235], a[60:63], v[204:207]\n" \
"ds_read_b64_tr_b16 a[230:231], v220 offset:51712\n" \
"v_lshlrev_b32_e64 v235, 4, v218\n" \
"v_mfma_f32_16x16x32_bf16 v[28:31], a[220:223], a[72:75], v[28:31]\n" \
"s_cbranch_vccz Lg22_%=\n" \
"s_nop 0\n" \
"buffer_store_dwordx4 v[8:11], %[oq0], %[srdz], s60 offen offset:1024 sc1\n" \
"Lg22_%=:\n" \
"v_mfma_f32_16x16x32_bf16 v[28:31], a[224:227], a[76:79], v[28:31]\n" \
"v_add3_u32 v211, v211, v235, s52\n" \
"v_mfma_f32_16x16x32_bf16 v[60:63], a[220:223], a[88:91], v[60:63]\n" \
"buffer_load_dwordx4 a[172:175], v209, %[srdz], s50 offen offset:3072 sc1\n" \
"v_mfma_f32_16x16x32_bf16 v[60:63], a[224:227], a[92:95], v[60:63]\n" \
"v_and_b32_e32 v216, 0x2b0, v217\n" \
"v_mfma_f32_16x16x32_bf16 v[92:95], a[220:223], a[104:107], v[92:95]\n" \
"v_lshrrev_b32_e64 v218, 2, v217\n" \
"v_mfma_f32_16x16x32_bf16 v[92:95], a[224:227], a[108:111], v[92:95]\n" \
"v_cmp_u_f32_e64 s[44:45], v192, v196\n" \
"v_and_or_b32 v216, v218, 64, v216\n" \
"s_add_u32 s52, s40, s42\n" \
"s_nop 0\n" \
"v_mfma_f32_16x16x32_bf16 v[124:127], a[220:223], a[120:123], v[124:127]\n" \
"s_add_u32 m0, %[m0b], 16384\n" \
"s_cmp_gt_u32 s48, 2\n" \
"s_cselect_b32 s49, 2, 3\n" \
"s_lshl_b32 s49, s49, 18\n" \
"s_or_b32 s49, s49, s59\n" \
"buffer_load_dwordx4 %[wo0], %[srdt], s49 offen offset:0 lds\n" \
"v_mfma_f32_16x16x32_bf16 v[124:127], a[224:227], a[124:127], v[124:127]\n" \
"v_cmp_u_f32_e64 s[56:57], v200, v204\n" \
"s_or_b64 s[56:57], s[56:57], s[44:45]\n" \
"s_cbranch_scc1 Lsp2_%=\n" \
"Lbk2_%=:\n" \
"s_waitcnt vmcnt(63) lgkmcnt(2)\n" \
"s_nop 0\n" \
"v_mfma_f32_16x16x32_bf16 v[144:147], a[236:239], a[0:3], v[144:147]\n" \
"ds_read_b64_tr_b16 a[220:221], v220 offset:53760\n" \
"v_add_u32_e64 %[kb0], s52, v216\n" \
"v_mfma_f32_16x16x32_bf16 v[144:147], a[208:211], a[4:7], v[144:147]\n" \
"ds_read_b64_tr_b16 a[222:223], v220 offset:55808\n" \
"s_cbranch_vccz Lg23_%=\n" \
"s_nop 0\n" \
"buffer_store_dwordx4 v[16:19], %[oq0], %[srdz], s60 offen offset:2048 sc1\n" \
"Lg23_%=:\n" \
"v_mfma_f32_16x16x32_bf16 v[148:151], a[236:239], a[16:19], v[148:151]\n" \
"ds_read_b64_tr_b16 a[196:197], v220 offset:57856\n" \
"v_xor_b32_e32 %[vvb], 0x100, %[kb0]\n" \
"v_mfma_f32_16x16x32_bf16 v[148:151], a[208:211], a[20:23], v[148:151]\n" \
"ds_read_b64_tr_b16 a[198:199], v220 offset:59904\n" \
"buffer_load_dwordx4 a[176:179], v210, %[srdz], s50 offen offset:0 sc1\n" \
"v_mfma_f32_16x16x32_bf16 v[152:155], a[236:239], a[32:35], v[152:155]\n" \
"ds_read_b64_tr_b16 a[200:201], v220 offset:61952\n" \
"v_and_b32_e32 v235, 0x200, v217\n" \
"v_mfma_f32_16x16x32_bf16 v[152:155], a[208:211], a[36:39], v[152:155]\n" \
"ds_read_b64_tr_b16 a[202:203], v220 offset:64000\n" \
"v_and_b32_e64 v234, 8, v222\n" \
"v_mfma_f32_16x16x32_bf16 v[156:159], a[236:239], a[48:51], v[156:159]\n" \
"ds_read_b64_tr_b16 a[204:205], v221 offset:49664\n" \
"v_lshl_or_b32 v235, v234, 5, v235\n" \
"v_mfma_f32_16x16x32_bf16 v[156:159], a[208:211], a[52:55], v[156:159]\n" \
"ds_read_b64_tr_b16 a[206:207], v221 offset:51712\n" \
"v_and_b32_e64 v234, 16, v222\n" \
"v_mfma_f32_16x16x32_bf16 v[144:147], a[212:215], a[8:11], v[144:147]\n" \
"ds_read_b64_tr_b16 a[224:225], v221 offset:53760\n" \
"v_lshl_or_b32 v235, v234, 1, v235\n" \
"v_mfma_f32_16x16x32_bf16 v[144:147], a[216:219], a[12:15], v[144:147]\n" \
"ds_read_b64_tr_b16 a[226:227], v221 offset:55808\n" \
"v_lshl_or_b32 v235, v233, 6, v235\n" \
"v_mfma_f32_16x16x32_bf16 v[148:151], a[212:215], a[24:27], v[148:151]\n" \
"s_cbranch_vccz Lg24_%=\n" \
"s_nop 0\n" \
"buffer_store_dwordx4 v[24:27], %[oq0], %[srdz], s60 offen offset:3072 sc1\n" \
"Lg24_%=:\n" \
"v_mfma_f32_16x16x32_bf16 v[148:151], a[216:219], a[28:31], v[148:151]\n" \
"v_and_b32_e64 v216, 2, v222\n" \
"v_mfma_f32_16x16x32_bf16 v[152:155], a[212:215], a[40:43], v[152:155]\n" \
"buffer_load_dwordx4 a[180:183], v210, %[srdz], s50 offen offset:1024 sc1\n" \
"v_mfma_f32_16x16x32_bf16 v[152:155], a[216:219], a[44:47], v[152:155]\n" \
"v_lshl_or_b32 v235, v216, 3, v235\n" \
"s_lshr_b32 s52, s42, 1\n" \
"s_nop 0\n" \
"v_mfma_f32_16x16x32_bf16 v[156:159], a[212:215], a[56:59], v[156:159]\n" \
"ds_read_b64_tr_b16 a[232:233], v221 offset:57856\n" \
"v_sub_u32_e32 v234, 0x7ff, %[ta0]\n" \
"v_mfma_f32_16x16x32_bf16 v[156:159], a[216:219], a[60:63], v[156:159]\n" \
"s_waitcnt vmcnt(63) lgkmcnt(5)\n" \
"s_nop 0\n" \
"v_mfma_f32_16x16x32_bf16 v[160:163], a[228:231], a[0:3], v[160:163]\n" \
"ds_read_b64_tr_b16 a[234:235], v221 offset:59904\n" \
"buffer_load_dwordx4 v212, %[srdt], s49 offen offset:1024 lds\n" \
"v_mfma_f32_16x16x32_bf16 v[160:163], a[220:223], a[4:7], v[160:163]\n" \
"ds_read_b64_tr_b16 a[236:237], v221 offset:61952\n" \
"v_lshlrev_b32_e64 v234, 11, v234\n" \
"v_mfma_f32_16x16x32_bf16 v[164:167], a[228:231], a[16:19], v[164:167]\n" \
"ds_read_b64_tr_b16 a[238:239], v221 offset:64000\n" \
"v_add3_u32 %[vd0], v234, v235, s52\n" \
"v_mfma_f32_16x16x32_bf16 v[164:167], a[220:223], a[20:23], v[164:167]\n" \
"s_cbranch_vccz Lg25_%=\n" \
"s_nop 0\n" \
"buffer_store_dwordx4 v[32:35], v208, %[srdz], s60 offen offset:0 sc1\n" \
"Lg25_%=:\n" \
"v_mfma_f32_16x16x32_bf16 v[168:171], a[228:231], a[32:35], v[168:171]\n" \
"v_mfma_f32_16x16x32_bf16 v[168:171], a[220:223], a[36:39], v[168:171]\n" \
"buffer_load_dwordx4 a[184:187], v210, %[srdz], s50 offen offset:2048 sc1\n" \
"v_mfma_f32_16x16x32_bf16 v[172:175], a[228:231], a[48:51], v[172:175]\n" \
"v_mfma_f32_16x16x32_bf16 v[172:175], a[220:223], a[52:55], v[172:175]\n" \
"buffer_load_dwordx4 a[188:191], v210, %[srdz], s50 offen offset:3072 sc1\n" \
"v_mfma_f32_16x16x32_bf16 v[160:163], a[196:199], a[8:11], v[160:163]\n" \
"v_mfma_f32_16x16x32_bf16 v[160:163], a[200:203], a[12:15], v[160:163]\n" \
"v_mfma_f32_16x16x32_bf16 v[164:167], a[196:199], a[24:27], v[164:167]\n" \
"v_mfma_f32_16x16x32_bf16 v[164:167], a[200:203], a[28:31], v[164:167]\n" \
"v_mfma_f32_16x16x32_bf16 v[168:171], a[196:199], a[40:43], v[168:171]\n" \
"s_waitcnt vmcnt(63) lgkmcnt(0)\n" \
"s_bitcmp1_b32 0x30, s48\n" \
"s_cbranch_scc1 Lcw11_%=\n" \
"s_waitcnt vmcnt(60)\n" \
"s_bitcmp1_b32 0xc0, s48\n" \
"s_cbranch_scc1 Lcw11_%=\n" \
"s_waitcnt vmcnt(55)\n" \
"Lcw11_%=:\n" \
"s_barrier\n" \
"s_nop 0\n" \
"v_mfma_f32_16x16x32_bf16 v[168:171], a[200:203], a[44:47], v[168:171]\n" \
"ds_read_b64_tr_b16 a[208:209], v215 offset:49152\n" \
"s_cbranch_vccz Lg26_%=\n" \
"s_nop 0\n" \
"buffer_store_dwordx4 v[40:43], v208, %[srdz], s60 offen offset:1024 sc1\n" \
"Lg26_%=:\n" \
"v_mfma_f32_16x16x32_bf16 v[172:175], a[196:199], a[56:59], v[172:175]\n" \
"ds_read_b64_tr_b16 a[210:211], v215 offset:51200\n" \
"v_mfma_f32_16x16x32_bf16 v[172:175], a[200:203], a[60:63], v[172:175]\n" \
"s_waitcnt vmcnt(63) lgkmcnt(2)\n" \
"s_nop 0\n" \
"v_mfma_f32_16x16x32_bf16 v[176:179], a[204:207], a[0:3], v[176:179]\n" \
"ds_read_b64_tr_b16 a[212:213], v215 offset:53248\n" \
"global_load_dword v237, %[vct], %[pct]\n" \
"v_mfma_f32_16x16x32_bf16 v[176:179], a[224:227], a[4:7], v[176:179]\n" \
"ds_read_b64_tr_b16 a[214:215], v215 offset:55296\n" \
"v_mfma_f32_16x16x32_bf16 v[180:183], a[204:207], a[16:19], v[180:183]\n" \
"ds_read_b64_tr_b16 a[216:217], v215 offset:57344\n" \
"buffer_load_dwordx4 v213, %[srdt], s49 offen offset:2048 lds\n" \
"v_mfma_f32_16x16x32_bf16 v[180:183], a[224:227], a[20:23], v[180:183]\n" \
"ds_read_b64_tr_b16 a[218:219], v215 offset:59392\n" \
"v_mfma_f32_16x16x32_bf16 v[184:187], a[204:207], a[32:35], v[184:187]\n" \
"ds_read_b64_tr_b16 a[228:229], v215 offset:61440\n" \
"v_mfma_f32_16x16x32_bf16 v[184:187], a[224:227], a[36:39], v[184:187]\n" \
"ds_read_b64_tr_b16 a[230:231], v215 offset:63488\n" \
"s_cbranch_vccz Lg27_%=\n" \
"s_nop 0\n" \
"buffer_store_dwordx4 v[48:51], v208, %[srdz], s60 offen offset:2048 sc1\n" \
"Lg27_%=:\n" \
"v_mfma_f32_16x16x32_bf16 v[188:191], a[204:207], a[48:51], v[188:191]\n" \
"ds_read_b64_tr_b16 a[220:221], v227 offset:49152\n" \
"v_mfma_f32_16x16x32_bf16 v[188:191], a[224:227], a[52:55], v[188:191]\n" \
"ds_read_b64_tr_b16 a[222:223], v227 offset:51200\n" \
"v_mfma_f32_16x16x32_bf16 v[176:179], a[232:235], a[8:11], v[176:179]\n" \
"ds_read_b64_tr_b16 a[196:197], v227 offset:53248\n" \
"v_mfma_f32_16x16x32_bf16 v[176:179], a[236:239], a[12:15], v[176:179]\n" \
"ds_read_b64_tr_b16 a[198:199], v227 offset:55296\n" \
"s_cmp_gt_u32 s48, 3\n" \
"s_cselect_b32 s50, 3, 4\n" \
"s_mul_i32 s50, s50, 152064\n" \
"buffer_load_dwordx4 a[64:67], %[oq0], %[srdz], s50 offen offset:0 sc1\n" \
"v_mfma_f32_16x16x32_bf16 v[180:183], a[232:235], a[24:27], v[180:183]\n" \
"v_mfma_f32_16x16x32_bf16 v[180:183], a[236:239], a[28:31], v[180:183]\n" \
"buffer_load_dwordx4 v214, %[srdt], s49 offen offset:3072 lds\n" \
"v_mfma_f32_16x16x32_bf16 v[184:187], a[232:235], a[40:43], v[184:187]\n" \
"v_mfma_f32_16x16x32_bf16 v[184:187], a[236:239], a[44:47], v[184:187]\n" \
"v_mfma_f32_16x16x32_bf16 v[188:191], a[232:235], a[56:59], v[188:191]\n" \
"ds_read_b64_tr_b16 a[200:201], v227 offset:57344\n" \
"s_cbranch_vccz Lg28_%=\n" \
"s_nop 0\n" \
"buffer_store_dwordx4 v[56:59], v208, %[srdz], s60 offen offset:3072 sc1\n" \
"Lg28_%=:\n" \
"v_mfma_f32_16x16x32_bf16 v[188:191], a[236:239], a[60:63], v[188:191]\n" \
"s_waitcnt vmcnt(63) lgkmcnt(5)\n" \
"s_nop 0\n" \
"v_mfma_f32_16x16x32_bf16 v[4:7], a[208:211], a[0:3], v[4:7]\n" \
"ds_read_b64_tr_b16 a[202:203], v227 offset:59392\n" \
"buffer_load_dwordx4 a[68:71], %[oq0], %[srdz], s50 offen offset:1024 sc1\n" \
"v_mfma_f32_16x16x32_bf16 v[4:7], a[212:215], a[4:7], v[4:7]\n" \
"ds_read_b64_tr_b16 a[204:205], v227 offset:61440\n" \
"v_mfma_f32_16x16x32_bf16 v[36:39], a[208:211], a[16:19], v[36:39]\n" \
"ds_read_b64_tr_b16 a[206:207], v227 offset:63488\n" \
"v_mfma_f32_16x16x32_bf16 v[36:39], a[212:215], a[20:23], v[36:39]\n" \
"v_mfma_f32_16x16x32_bf16 v[68:71], a[208:211], a[32:35], v[68:71]\n" \
"buffer_load_dwordx4 a[72:75], %[oq0], %[srdz], s50 offen offset:2048 sc1\n" \
"v_mfma_f32_16x16x32_bf16 v[68:71], a[212:215], a[36:39], v[68:71]\n" \
"v_mfma_f32_16x16x32_bf16 v[100:103], a[208:211], a[48:51], v[100:103]\n" \
"ds_read_b64_tr_b16 a[224:225], v215 offset:49664\n" \
"v_mfma_f32_16x16x32_bf16 v[100:103], a[212:215], a[52:55], v[100:103]\n" \
"ds_read_b64_tr_b16 a[226:227], v215 offset:51712\n" \
"s_cbranch_vccz Lg29_%=\n" \
"s_nop 0\n" \
"buffer_store_dwordx4 v[64:67], v209, %[srdz], s60 offen offset:0 sc1\n" \
"Lg29_%=:\n" \
"v_mfma_f32_16x16x32_bf16 v[4:7], a[216:219], a[8:11], v[4:7]\n" \
"ds_read_b64_tr_b16 a[232:233], v215 offset:53760\n" \
"v_mfma_f32_16x16x32_bf16 v[4:7], a[228:231], a[12:15], v[4:7]\n" \
"ds_read_b64_tr_b16 a[234:235], v215 offset:55808\n" \
"v_mfma_f32_16x16x32_bf16 v[36:39], a[216:219], a[24:27], v[36:39]\n" \
"v_mfma_f32_16x16x32_bf16 v[36:39], a[228:231], a[28:31], v[36:39]\n" \
"buffer_load_dwordx4 a[76:79], %[oq0], %[srdz], s50 offen offset:3072 sc1\n" \
"v_mfma_f32_16x16x32_bf16 v[68:71], a[216:219], a[40:43], v[68:71]\n" \
"v_mfma_f32_16x16x32_bf16 v[68:71], a[228:231], a[44:47], v[68:71]\n" \
"v_mfma_f32_16x16x32_bf16 v[100:103], a[216:219], a[56:59], v[100:103]\n" \
"ds_read_b64_tr_b16 a[236:237], v215 offset:57856\n" \
"v_mfma_f32_16x16x32_bf16 v[100:103], a[228:231], a[60:63], v[100:103]\n" \
"s_waitcnt vmcnt(63) lgkmcnt(5)\n" \
"s_nop 0\n" \
"v_mfma_f32_16x16x32_bf16 v[12:15], a[220:223], a[0:3], v[12:15]\n" \
"ds_read_b64_tr_b16 a[238:239], v215 offset:59904\n" \
"s_cbranch_vccz Lg30_%=\n" \
"s_nop 0\n" \
"buffer_store_dwordx4 v[72:75], v209, %[srdz], s60 offen offset:1024 sc1\n" \
"Lg30_%=:\n" \
"v_mfma_f32_16x16x32_bf16 v[12:15], a[196:199], a[4:7], v[12:15]\n" \
"ds_read_b64_tr_b16 a[208:209], v215 offset:61952\n" \
"v_mfma_f32_16x16x32_bf16 v[44:47], a[220:223], a[16:19], v[44:47]\n" \
"ds_read_b64_tr_b16 a[210:211], v215 offset:64000\n" \
"v_mfma_f32_16x16x32_bf16 v[44:47], a[196:199], a[20:23], v[44:47]\n" \
"buffer_load_dwordx4 a[80:83], v208, %[srdz], s50 offen offset:0 sc1\n" \
"v_mfma_f32_16x16x32_bf16 v[76:79], a[220:223], a[32:35], v[76:79]\n" \
"v_mfma_f32_16x16x32_bf16 v[76:79], a[196:199], a[36:39], v[76:79]\n" \
"s_add_u32 m0, %[m0b], 32768\n" \
"s_cmp_gt_u32 s48, 3\n" \
"s_cselect_b32 s49, 3, 4\n" \
"s_lshl_b32 s49, s49, 18\n" \
"s_or_b32 s49, s49, s58\n" \
"buffer_load_dwordx4 %[wo0], %[srdt], s49 offen offset:0 lds\n" \
"v_mfma_f32_16x16x32_bf16 v[108:111], a[220:223], a[48:51], v[108:111]\n" \
"ds_read_b64_tr_b16 a[212:213], v227 offset:49664\n" \
"v_mfma_f32_16x16x32_bf16 v[108:111], a[196:199], a[52:55], v[108:111]\n" \
"ds_read_b64_tr_b16 a[214:215], v227 offset:51712\n" \
"v_mfma_f32_16x16x32_bf16 v[12:15], a[200:203], a[8:11], v[12:15]\n" \
"ds_read_b64_tr_b16 a[216:217], v227 offset:57856\n" \
"v_mfma_f32_16x16x32_bf16 v[12:15], a[204:207], a[12:15], v[12:15]\n" \
"ds_read_b64_tr_b16 a[218:219], v227 offset:59904\n" \
"s_cbranch_vccz Lg31_%=\n" \
"s_nop 0\n" \
"buffer_store_dwordx4 v[80:83], v209, %[srdz], s60 offen offset:2048 sc1\n" \
"Lg31_%=:\n" \
"v_mfma_f32_16x16x32_bf16 v[44:47], a[200:203], a[24:27], v[44:47]\n" \
"ds_read_b64_tr_b16 a[228:229], v227 offset:53760\n" \
"v_mfma_f32_16x16x32_bf16 v[44:47], a[204:207], a[28:31], v[44:47]\n" \
"ds_read_b64_tr_b16 a[230:231], v227 offset:55808\n" \
"v_mfma_f32_16x16x32_bf16 v[76:79], a[200:203], a[40:43], v[76:79]\n" \
"ds_read_b64_tr_b16 a[220:221], v227 offset:61952\n" \
"buffer_load_dwordx4 a[84:87], v208, %[srdz], s50 offen offset:1024 sc1\n" \
"v_mfma_f32_16x16x32_bf16 v[76:79], a[204:207], a[44:47], v[76:79]\n" \
"ds_read_b64_tr_b16 a[222:223], v227 offset:64000\n" \
"v_mfma_f32_16x16x32_bf16 v[108:111], a[200:203], a[56:59], v[108:111]\n" \
"v_mfma_f32_16x16x32_bf16 v[108:111], a[204:207], a[60:63], v[108:111]\n" \
"s_waitcnt vmcnt(63) lgkmcnt(12)\n" \
"s_nop 0\n" \
"v_mfma_f32_16x16x32_bf16 v[20:23], a[224:227], a[0:3], v[20:23]\n" \
"v_mfma_f32_16x16x32_bf16 v[20:23], a[232:235], a[4:7], v[20:23]\n" \
"v_mfma_f32_16x16x32_bf16 v[52:55], a[224:227], a[16:19], v[52:55]\n" \
"s_cbranch_vccz Lg32_%=\n" \
"s_nop 0\n" \
"buffer_store_dwordx4 v[88:91], v209, %[srdz], s60 offen offset:3072 sc1\n" \
"Lg32_%=:\n" \
"v_mfma_f32_16x16x32_bf16 v[52:55], a[232:235], a[20:23], v[52:55]\n" \
"v_mfma_f32_16x16x32_bf16 v[84:87], a[224:227], a[32:35], v[84:87]\n" \
"s_waitcnt vmcnt(32) lgkmcnt(0)\n" \
"s_nop 0\n" \
"s_bitcmp1_b32 0xc0, s48\n" \
"s_cbranch_scc1 Lcw12_%=\n" \
"s_waitcnt vmcnt(20)\n" \
"Lcw12_%=:\n" \
"s_barrier\n" \
"s_nop 0\n" \
"v_mfma_f32_16x16x32_bf16 v[84:87], a[232:235], a[36:39], v[84:87]\n" \
"ds_read_b64_tr_b16 a[196:197], v215 offset:0\n" \
"buffer_load_dwordx4 a[88:91], v208, %[srdz], s50 offen offset:2048 sc1\n" \
"v_mfma_f32_16x16x32_bf16 v[116:119], a[224:227], a[48:51], v[116:119]\n" \
"ds_read_b64_tr_b16 a[198:199], v215 offset:2048\n" \
"v_mfma_f32_16x16x32_bf16 v[116:119], a[232:235], a[52:55], v[116:119]\n" \
"ds_read_b64_tr_b16 a[200:201], v215 offset:4096\n" \
"buffer_load_dwordx4 v212, %[srdt], s49 offen offset:1024 lds\n" \
"v_mfma_f32_16x16x32_bf16 v[20:23], a[236:239], a[8:11], v[20:23]\n" \
"ds_read_b64_tr_b16 a[202:203], v215 offset:6144\n" \
"v_mfma_f32_16x16x32_bf16 v[20:23], a[208:211], a[12:15], v[20:23]\n" \
"ds_read_b64_tr_b16 a[204:205], v215 offset:8192\n" \
"v_mfma_f32_16x16x32_bf16 v[52:55], a[236:239], a[24:27], v[52:55]\n" \
"ds_read_b64_tr_b16 a[206:207], v215 offset:10240\n" \
"v_mfma_f32_16x16x32_bf16 v[52:55], a[208:211], a[28:31], v[52:55]\n" \
"ds_read_b64_tr_b16 a[224:225], v215 offset:12288\n" \
"s_cbranch_vccz Lg33_%=\n" \
"s_nop 0\n" \
"buffer_store_dwordx4 v[96:99], v210, %[srdz], s60 offen offset:0 sc1\n" \
"Lg33_%=:\n" \
"v_mfma_f32_16x16x32_bf16 v[84:87], a[236:239], a[40:43], v[84:87]\n" \
"ds_read_b64_tr_b16 a[226:227], v215 offset:14336\n" \
"v_mfma_f32_16x16x32_bf16 v[84:87], a[208:211], a[44:47], v[84:87]\n" \
"buffer_load_dwordx4 a[92:95], v208, %[srdz], s50 offen offset:3072 sc1\n" \
"v_mfma_f32_16x16x32_bf16 v[116:119], a[236:239], a[56:59], v[116:119]\n" \
"v_mfma_f32_16x16x32_bf16 v[116:119], a[208:211], a[60:63], v[116:119]\n" \
"s_waitcnt vmcnt(25) lgkmcnt(4)\n" \
"s_nop 0\n" \
"s_bitcmp1_b32 0xc0, s48\n" \
"s_cbranch_scc1 Lcw13_%=\n" \
"s_waitcnt vmcnt(16)\n" \
"Lcw13_%=:\n" \
"v_mfma_f32_16x16x32_bf16 v[28:31], a[212:215], a[0:3], v[28:31]\n" \
"buffer_load_dwordx4 a[96:99], v209, %[srdz], s50 offen offset:0 sc1\n" \
"v_mfma_f32_16x16x32_bf16 v[28:31], a[228:231], a[4:7], v[28:31]\n" \
"v_mfma_f32_16x16x32_bf16 v[60:63], a[212:215], a[16:19], v[60:63]\n" \
"buffer_load_dwordx4 v213, %[srdt], s49 offen offset:2048 lds\n" \
"v_mfma_f32_16x16x32_bf16 v[60:63], a[228:231], a[20:23], v[60:63]\n" \
"v_mfma_f32_16x16x32_bf16 v[92:95], a[212:215], a[32:35], v[92:95]\n" \
"s_cbranch_vccz Lg34_%=\n" \
"s_nop 0\n" \
"buffer_store_dwordx4 v[104:107], v210, %[srdz], s60 offen offset:1024 sc1\n" \
"Lg34_%=:\n" \
"v_mfma_f32_16x16x32_bf16 v[92:95], a[228:231], a[36:39], v[92:95]\n" \
"v_mfma_f32_16x16x32_bf16 v[124:127], a[212:215], a[48:51], v[124:127]\n" \
"ds_read_b64_tr_b16 a[232:233], v227 offset:0\n" \
"v_mfma_f32_16x16x32_bf16 v[124:127], a[228:231], a[52:55], v[124:127]\n" \
"ds_read_b64_tr_b16 a[234:235], v227 offset:2048\n" \
"v_mfma_f32_16x16x32_bf16 v[128:131], a[196:199], a[128:131], v[192:195]\n" \
"ds_read_b64_tr_b16 a[236:237], v227 offset:4096\n" \
"v_mfma_f32_16x16x32_bf16 v[128:131], a[200:203], a[132:135], v[128:131]\n" \
"ds_read_b64_tr_b16 a[238:239], v227 offset:6144\n" \
".if . - adaln_body != 49152\n" \
".error \"size model\"\n" \
".endif\n" \
"Lk11_%=:\n" \
"s_cbranch_execz adaln_kr\n" \
"s_nop 0\n" \
"buffer_load_dwordx4 a[100:103], v209, %[srdz], s50 offen offset:1024 sc1\n" \
"v_mfma_f32_16x16x32_bf16 v[132:135], a[196:199], a[144:147], v[196:199]\n" \
"v_mfma_f32_16x16x32_bf16 v[132:135], a[200:203], a[148:151], v[132:135]\n" \
"v_mfma_f32_16x16x32_bf16 v[136:139], a[196:199], a[160:163], v[200:203]\n" \
"v_mfma_f32_16x16x32_bf16 v[136:139], a[200:203], a[164:167], v[136:139]\n" \
"s_cbranch_vccz Lg35_%=\n" \
"s_nop 0\n" \
"buffer_store_dwordx4 v[112:115], v210, %[srdz], s60 offen offset:2048 sc1\n" \
"Lg35_%=:\n" \
"v_mfma_f32_16x16x32_bf16 v[140:143], a[196:199], a[176:179], v[204:207]\n" \
"ds_read_b64_tr_b16 a[208:209], v227 offset:8192\n" \
"v_mfma_f32_16x16x32_bf16 v[140:143], a[200:203], a[180:183], v[140:143]\n" \
"s_waitcnt vmcnt(26) lgkmcnt(5)\n" \
"s_nop 0\n" \
"s_bitcmp1_b32 0xc0, s48\n" \
"s_cbranch_scc1 Lcw14_%=\n" \
"s_waitcnt vmcnt(16)\n" \
"Lcw14_%=:\n" \
"v_mfma_f32_16x16x32_bf16 v[128:131], a[204:207], a[136:139], v[128:131]\n" \
"ds_read_b64_tr_b16 a[210:211], v227 offset:10240\n" \
"buffer_load_dwordx4 a[104:107], v209, %[srdz], s50 offen offset:2048 sc1\n" \
"v_mfma_f32_16x16x32_bf16 v[128:131], a[224:227], a[140:143], v[128:131]\n" \
"ds_read_b64_tr_b16 a[212:213], v227 offset:12288\n" \
"v_mfma_f32_16x16x32_bf16 v[132:135], a[204:207], a[152:155], v[132:135]\n" \
"ds_read_b64_tr_b16 a[214:215], v227 offset:14336\n" \
"v_mfma_f32_16x16x32_bf16 v[132:135], a[224:227], a[156:159], v[132:135]\n" \
"buffer_load_dwordx4 v214, %[srdt], s49 offen offset:3072 lds\n" \
"v_mfma_f32_16x16x32_bf16 v[136:139], a[204:207], a[168:171], v[136:139]\n" \
"v_mfma_f32_16x16x32_bf16 v[136:139], a[224:227], a[172:175], v[136:139]\n" \
"v_mfma_f32_16x16x32_bf16 v[140:143], a[204:207], a[184:187], v[140:143]\n" \
"ds_read_b64_tr_b16 a[228:229], v215 offset:512\n" \
"s_cbranch_vccz Lg36_%=\n" \
"s_nop 0\n" \
"buffer_store_dwordx4 v[120:123], v210, %[srdz], s60 offen offset:3072 sc1\n" \
"Lg36_%=:\n" \
"v_mfma_f32_16x16x32_bf16 v[140:143], a[224:227], a[188:191], v[140:143]\n" \
"ds_read_b64_tr_b16 a[230:231], v215 offset:2560\n" \
"v_mfma_f32_16x16x32_bf16 v[28:31], a[216:219], a[8:11], v[28:31]\n" \
"buffer_load_dwordx4 a[108:111], v209, %[srdz], s50 offen offset:3072 sc1\n" \
"v_mfma_f32_16x16x32_bf16 v[28:31], a[220:223], a[12:15], v[28:31]\n" \
"v_mfma_f32_16x16x32_bf16 v[60:63], a[216:219], a[24:27], v[60:63]\n" \
"v_mfma_f32_16x16x32_bf16 v[60:63], a[220:223], a[28:31], v[60:63]\n" \
"v_mfma_f32_16x16x32_bf16 v[92:95], a[216:219], a[40:43], v[92:95]\n" \
"buffer_load_dwordx4 a[112:115], v210, %[srdz], s50 offen offset:0 sc1\n" \
"v_mfma_f32_16x16x32_bf16 v[92:95], a[220:223], a[44:47], v[92:95]\n" \
"v_cmp_u_f32_e64 s[44:45], v128, v132\n" \
"v_mfma_f32_16x16x32_bf16 v[124:127], a[216:219], a[56:59], v[124:127]\n" \
"v_mfma_f32_16x16x32_bf16 v[124:127], a[220:223], a[60:63], v[124:127]\n" \
"v_cmp_u_f32_e64 s[56:57], v136, v140\n" \
"s_or_b64 s[56:57], s[56:57], s[44:45]\n" \
"s_cbranch_scc1 Lsp3_%=\n" \
"Lbk3_%=:\n" \
"s_waitcnt vmcnt(63) lgkmcnt(2)\n" \
"s_nop 0\n" \
"v_mfma_f32_16x16x32_bf16 v[144:147], a[232:235], a[128:131], v[144:147]\n" \
"ds_read_b64_tr_b16 a[216:217], v215 offset:4608\n" \
"v_mfma_f32_16x16x32_bf16 v[144:147], a[236:239], a[132:135], v[144:147]\n" \
"ds_read_b64_tr_b16 a[218:219], v215 offset:6656\n" \
"v_mfma_f32_16x16x32_bf16 v[148:151], a[232:235], a[144:147], v[148:151]\n" \
"ds_read_b64_tr_b16 a[196:197], v215 offset:8704\n" \
"v_mfma_f32_16x16x32_bf16 v[148:151], a[236:239], a[148:151], v[148:151]\n" \
"ds_read_b64_tr_b16 a[198:199], v215 offset:10752\n" \
"v_mfma_f32_16x16x32_bf16 v[152:155], a[232:235], a[160:163], v[152:155]\n" \
"ds_read_b64_tr_b16 a[200:201], v215 offset:12800\n" \
"v_mfma_f32_16x16x32_bf16 v[152:155], a[236:239], a[164:167], v[152:155]\n" \
"ds_read_b64_tr_b16 a[202:203], v215 offset:14848\n" \
"v_mfma_f32_16x16x32_bf16 v[156:159], a[232:235], a[176:179], v[156:159]\n" \
"ds_read_b64_tr_b16 a[204:205], v227 offset:512\n" \
"buffer_load_dwordx4 a[116:119], v210, %[srdz], s50 offen offset:1024 sc1\n" \
"v_mfma_f32_16x16x32_bf16 v[156:159], a[236:239], a[180:183], v[156:159]\n" \
"ds_read_b64_tr_b16 a[206:207], v227 offset:2560\n" \
"v_mfma_f32_16x16x32_bf16 v[144:147], a[208:211], a[136:139], v[144:147]\n" \
"ds_read_b64_tr_b16 a[220:221], v227 offset:4608\n" \
"v_mfma_f32_16x16x32_bf16 v[144:147], a[212:215], a[140:143], v[144:147]\n" \
"ds_read_b64_tr_b16 a[222:223], v227 offset:6656\n" \
"v_mfma_f32_16x16x32_bf16 v[148:151], a[208:211], a[152:155], v[148:151]\n" \
"s_add_u32 m0, %[m0b], 98304\n" \
"s_cmp_gt_u32 s48, 4\n" \
"s_cselect_b32 s49, 4, 5\n" \
"s_lshl_b32 s49, s49, 18\n" \
"s_or_b32 s49, s49, s58\n" \
"buffer_load_dwordx4 %[wo0], %[srdt], s49 offen offset:0 lds\n" \
"v_mfma_f32_16x16x32_bf16 v[148:151], a[212:215], a[156:159], v[148:151]\n" \
"v_mfma_f32_16x16x32_bf16 v[152:155], a[208:211], a[168:171], v[152:155]\n" \
"buffer_load_dwordx4 a[120:123], v210, %[srdz], s50 offen offset:2048 sc1\n" \
"v_mfma_f32_16x16x32_bf16 v[152:155], a[212:215], a[172:175], v[152:155]\n" \
"v_mfma_f32_16x16x32_bf16 v[156:159], a[208:211], a[184:187], v[156:159]\n" \
"ds_read_b64_tr_b16 a[224:225], v227 offset:8704\n" \
"v_mfma_f32_16x16x32_bf16 v[156:159], a[212:215], a[188:191], v[156:159]\n" \
"s_waitcnt vmcnt(63) lgkmcnt(5)\n" \
"s_nop 0\n" \
"v_mfma_f32_16x16x32_bf16 v[160:163], a[228:231], a[128:131], v[160:163]\n" \
"ds_read_b64_tr_b16 a[226:227], v227 offset:10752\n" \
"v_mfma_f32_16x16x32_bf16 v[160:163], a[216:219], a[132:135], v[160:163]\n" \
"ds_read_b64_tr_b16 a[232:233], v227 offset:12800\n" \
"v_mfma_f32_16x16x32_bf16 v[164:167], a[228:231], a[144:147], v[164:167]\n" \
"ds_read_b64_tr_b16 a[234:235], v227 offset:14848\n" \
"v_mfma_f32_16x16x32_bf16 v[164:167], a[216:219], a[148:151], v[164:167]\n" \
"buffer_load_dwordx4 a[124:127], v210, %[srdz], s50 offen offset:3072 sc1\n" \
"v_mfma_f32_16x16x32_bf16 v[168:171], a[228:231], a[160:163], v[168:171]\n" \
"v_mfma_f32_16x16x32_bf16 v[168:171], a[216:219], a[164:167], v[168:171]\n" \
"v_mfma_f32_16x16x32_bf16 v[172:175], a[228:231], a[176:179], v[172:175]\n" \
"v_mfma_f32_16x16x32_bf16 v[172:175], a[216:219], a[180:183], v[172:175]\n" \
"v_mfma_f32_16x16x32_bf16 v[160:163], a[196:199], a[136:139], v[160:163]\n" \
"v_mfma_f32_16x16x32_bf16 v[160:163], a[200:203], a[140:143], v[160:163]\n" \
"buffer_load_dwordx4 v212, %[srdt], s49 offen offset:1024 lds\n" \
"v_mfma_f32_16x16x32_bf16 v[164:167], a[196:199], a[152:155], v[164:167]\n" \
"v_mfma_f32_16x16x32_bf16 v[164:167], a[200:203], a[156:159], v[164:167]\n" \
"v_mfma_f32_16x16x32_bf16 v[168:171], a[196:199], a[168:171], v[168:171]\n" \
"s_waitcnt vmcnt(30) lgkmcnt(0)\n" \
"s_nop 0\n" \
"s_bitcmp1_b32 0xc0, s48\n" \
"s_cbranch_scc1 Lcw15_%=\n" \
"s_waitcnt vmcnt(21)\n" \
"Lcw15_%=:\n" \
"s_barrier\n" \
"s_nop 0\n" \
"v_mfma_f32_16x16x32_bf16 v[168:171], a[200:203], a[172:175], v[168:171]\n" \
"ds_read_b64_tr_b16 a[236:237], v215 offset:16384\n" \
"v_mfma_f32_16x16x32_bf16 v[172:175], a[196:199], a[184:187], v[172:175]\n" \
"ds_read_b64_tr_b16 a[238:239], v215 offset:18432\n" \
"v_mfma_f32_16x16x32_bf16 v[172:175], a[200:203], a[188:191], v[172:175]\n" \
"s_waitcnt vmcnt(63) lgkmcnt(2)\n" \
"s_nop 0\n" \
"v_mfma_f32_16x16x32_bf16 v[176:179], a[204:207], a[128:131], v[176:179]\n" \
"ds_read_b64_tr_b16 a[208:209], v215 offset:20480\n" \
"s_cmp_gt_u32 s48, 4\n" \
"s_cselect_b32 s50, 4, 5\n" \
"s_mul_i32 s50, s50, 152064\n" \
"buffer_load_dwordx4 v[0:3], %[oq0], %[srdz], s50 offen offset:0 sc1\n" \
"v_mfma_f32_16x16x32_bf16 v[176:179], a[220:223], a[132:135], v[176:179]\n" \
"ds_read_b64_tr_b16 a[210:211], v215 offset:22528\n" \
"v_mfma_f32_16x16x32_bf16 v[180:183], a[204:207], a[144:147], v[180:183]\n" \
"ds_read_b64_tr_b16 a[212:213], v215 offset:24576\n" \
"v_mfma_f32_16x16x32_bf16 v[180:183], a[220:223], a[148:151], v[180:183]\n" \
"ds_read_b64_tr_b16 a[214:215], v215 offset:26624\n" \
"v_mfma_f32_16x16x32_bf16 v[184:187], a[204:207], a[160:163], v[184:187]\n" \
"ds_read_b64_tr_b16 a[228:229], v215 offset:28672\n" \
"buffer_load_dwordx4 v[8:11], %[oq0], %[srdz], s50 offen offset:1024 sc1\n" \
"v_mfma_f32_16x16x32_bf16 v[184:187], a[220:223], a[164:167], v[184:187]\n" \
"ds_read_b64_tr_b16 a[230:231], v215 offset:30720\n" \
"v_mfma_f32_16x16x32_bf16 v[188:191], a[204:207], a[176:179], v[188:191]\n" \
"ds_read_b64_tr_b16 a[216:217], v227 offset:16384\n" \
"v_mfma_f32_16x16x32_bf16 v[188:191], a[220:223], a[180:183], v[188:191]\n" \
"ds_read_b64_tr_b16 a[218:219], v227 offset:18432\n" \
"v_mfma_f32_16x16x32_bf16 v[176:179], a[224:227], a[136:139], v[176:179]\n" \
"ds_read_b64_tr_b16 a[196:197], v227 offset:20480\n" \
"buffer_load_dwordx4 v[16:19], %[oq0], %[srdz], s50 offen offset:2048 sc1\n" \
"v_mfma_f32_16x16x32_bf16 v[176:179], a[232:235], a[140:143], v[176:179]\n" \
"ds_read_b64_tr_b16 a[198:199], v227 offset:22528\n" \
"v_mfma_f32_16x16x32_bf16 v[180:183], a[224:227], a[152:155], v[180:183]\n" \
"buffer_load_dwordx4 v213, %[srdt], s49 offen offset:2048 lds\n" \
"v_mfma_f32_16x16x32_bf16 v[180:183], a[232:235], a[156:159], v[180:183]\n" \
"v_mfma_f32_16x16x32_bf16 v[184:187], a[224:227], a[168:171], v[184:187]\n" \
"buffer_load_dwordx4 v[24:27], %[oq0], %[srdz], s50 offen offset:3072 sc1\n" \
"v_mfma_f32_16x16x32_bf16 v[184:187], a[232:235], a[172:175], v[184:187]\n" \
"v_mfma_f32_16x16x32_bf16 v[188:191], a[224:227], a[184:187], v[188:191]\n" \
"ds_read_b64_tr_b16 a[200:201], v227 offset:24576\n" \
"v_mfma_f32_16x16x32_bf16 v[188:191], a[232:235], a[188:191], v[188:191]\n" \
"s_waitcnt vmcnt(63) lgkmcnt(5)\n" \
"s_nop 0\n" \
"v_mfma_f32_16x16x32_bf16 v[4:7], a[236:239], a[128:131], v[4:7]\n" \
"ds_read_b64_tr_b16 a[202:203], v227 offset:26624\n" \
"buffer_load_dwordx4 v[32:35], v208, %[srdz], s50 offen offset:0 sc1\n" \
"v_mfma_f32_16x16x32_bf16 v[4:7], a[208:211], a[132:135], v[4:7]\n" \
"ds_read_b64_tr_b16 a[204:205], v227 offset:28672\n" \
"v_mfma_f32_16x16x32_bf16 v[36:39], a[236:239], a[144:147], v[36:39]\n" \
"ds_read_b64_tr_b16 a[206:207], v227 offset:30720\n" \
"v_mfma_f32_16x16x32_bf16 v[36:39], a[208:211], a[148:151], v[36:39]\n" \
"buffer_load_dwordx4 v[40:43], v208, %[srdz], s50 offen offset:1024 sc1\n" \
"v_mfma_f32_16x16x32_bf16 v[68:71], a[236:239], a[160:163], v[68:71]\n" \
"v_mfma_f32_16x16x32_bf16 v[68:71], a[208:211], a[164:167], v[68:71]\n" \
"buffer_load_dwordx4 v[48:51], v208, %[srdz], s50 offen offset:2048 sc1\n" \
"v_mfma_f32_16x16x32_bf16 v[100:103], a[236:239], a[176:179], v[100:103]\n" \
"ds_read_b64_tr_b16 a[220:221], v215 offset:16896\n" \
"v_mfma_f32_16x16x32_bf16 v[100:103], a[208:211], a[180:183], v[100:103]\n" \
"ds_read_b64_tr_b16 a[222:223], v215 offset:18944\n" \
"v_mfma_f32_16x16x32_bf16 v[4:7], a[212:215], a[136:139], v[4:7]\n" \
"ds_read_b64_tr_b16 a[224:225], v215 offset:20992\n" \
"v_mfma_f32_16x16x32_bf16 v[4:7], a[228:231], a[140:143], v[4:7]\n" \
"ds_read_b64_tr_b16 a[226:227], v215 offset:23040\n" \
"v_mfma_f32_16x16x32_bf16 v[36:39], a[212:215], a[152:155], v[36:39]\n" \
"buffer_load_dwordx4 v214, %[srdt], s49 offen offset:3072 lds\n" \
"v_mfma_f32_16x16x32_bf16 v[36:39], a[228:231], a[156:159], v[36:39]\n" \
"v_mfma_f32_16x16x32_bf16 v[68:71], a[212:215], a[168:171], v[68:71]\n" \
"buffer_load_dwordx4 v[56:59], v208, %[srdz], s50 offen offset:3072 sc1\n" \
"v_mfma_f32_16x16x32_bf16 v[68:71], a[228:231], a[172:175], v[68:71]\n" \
"v_mfma_f32_16x16x32_bf16 v[100:103], a[212:215], a[184:187], v[100:103]\n" \
"ds_read_b64_tr_b16 a[232:233], v215 offset:25088\n" \
"v_mfma_f32_16x16x32_bf16 v[100:103], a[228:231], a[188:191], v[100:103]\n" \
"s_waitcnt vmcnt(63) lgkmcnt(5)\n" \
"s_nop 0\n" \
"v_mfma_f32_16x16x32_bf16 v[12:15], a[216:219], a[128:131], v[12:15]\n" \
"ds_read_b64_tr_b16 a[234:235], v215 offset:27136\n" \
"buffer_load_dwordx4 v[64:67], v209, %[srdz], s50 offen offset:0 sc1\n" \
"v_mfma_f32_16x16x32_bf16 v[12:15], a[196:199], a[132:135], v[12:15]\n" \
"ds_read_b64_tr_b16 a[236:237], v215 offset:29184\n" \
"v_mfma_f32_16x16x32_bf16 v[44:47], a[216:219], a[144:147], v[44:47]\n" \
"ds_read_b64_tr_b16 a[238:239], v215 offset:31232\n" \
"v_mfma_f32_16x16x32_bf16 v[44:47], a[196:199], a[148:151], v[44:47]\n" \
"buffer_load_dwordx4 v[72:75], v209, %[srdz], s50 offen offset:1024 sc1\n" \
"v_mfma_f32_16x16x32_bf16 v[76:79], a[216:219], a[160:163], v[76:79]\n" \
"v_mfma_f32_16x16x32_bf16 v[76:79], a[196:199], a[164:167], v[76:79]\n" \
"buffer_load_dwordx4 v[80:83], v209, %[srdz], s50 offen offset:2048 sc1\n" \
"v_mfma_f32_16x16x32_bf16 v[108:111], a[216:219], a[176:179], v[108:111]\n" \
"ds_read_b64_tr_b16 a[208:209], v227 offset:16896\n" \
"v_mfma_f32_16x16x32_bf16 v[108:111], a[196:199], a[180:183], v[108:111]\n" \
"ds_read_b64_tr_b16 a[210:211], v227 offset:18944\n" \
"v_mfma_f32_16x16x32_bf16 v[12:15], a[200:203], a[136:139], v[12:15]\n" \
"ds_read_b64_tr_b16 a[212:213], v227 offset:25088\n" \
"v_mfma_f32_16x16x32_bf16 v[12:15], a[204:207], a[140:143], v[12:15]\n" \
"ds_read_b64_tr_b16 a[214:215], v227 offset:27136\n" \
"v_mfma_f32_16x16x32_bf16 v[44:47], a[200:203], a[152:155], v[44:47]\n" \
"ds_read_b64_tr_b16 a[228:229], v227 offset:20992\n" \
"v_mfma_f32_16x16x32_bf16 v[44:47], a[204:207], a[156:159], v[44:47]\n" \
"ds_read_b64_tr_b16 a[230:231], v227 offset:23040\n" \
"buffer_load_dwordx4 v[88:91], v209, %[srdz], s50 offen offset:3072 sc1\n" \
"v_mfma_f32_16x16x32_bf16 v[76:79], a[200:203], a[168:171], v[76:79]\n" \
"ds_read_b64_tr_b16 a[216:217], v227 offset:29184\n" \
"v_mfma_f32_16x16x32_bf16 v[76:79], a[204:207], a[172:175], v[76:79]\n" \
"ds_read_b64_tr_b16 a[218:219], v227 offset:31232\n" \
"v_mfma_f32_16x16x32_bf16 v[108:111], a[200:203], a[184:187], v[108:111]\n" \
"buffer_load_dwordx4 v[96:99], v210, %[srdz], s50 offen offset:0 sc1\n" \
"v_mfma_f32_16x16x32_bf16 v[108:111], a[204:207], a[188:191], v[108:111]\n" \
"s_waitcnt vmcnt(63) lgkmcnt(12)\n" \
"s_nop 0\n" \
"v_mfma_f32_16x16x32_bf16 v[20:23], a[220:223], a[128:131], v[20:23]\n" \
"s_add_u32 m0, %[m0b], 114688\n" \
"s_cmp_gt_u32 s48, 5\n" \
"s_cselect_b32 s49, 5, 6\n" \
"s_lshl_b32 s49, s49, 18\n" \
"s_or_b32 s49, s49, s58\n" \
"buffer_load_dwordx4 %[wo0], %[srdt], s49 offen offset:0 lds\n" \
"v_mfma_f32_16x16x32_bf16 v[20:23], a[224:227], a[132:135], v[20:23]\n" \
"v_mfma_f32_16x16x32_bf16 v[52:55], a[220:223], a[144:147], v[52:55]\n" \
"buffer_load_dwordx4 v[104:107], v210, %[srdz], s50 offen offset:1024 sc1\n" \
"v_mfma_f32_16x16x32_bf16 v[52:55], a[224:227], a[148:151], v[52:55]\n" \
"v_mfma_f32_16x16x32_bf16 v[84:87], a[220:223], a[160:163], v[84:87]\n" \
"s_waitcnt vmcnt(24) lgkmcnt(0)\n" \
"s_barrier\n" \
"v_mfma_f32_16x16x32_bf16 v[84:87], a[224:227], a[164:167], v[84:87]\n" \
"ds_read_b64_tr_b16 a[196:197], v215 offset:32768\n" \
"v_mfma_f32_16x16x32_bf16 v[116:119], a[220:223], a[176:179], v[116:119]\n" \
"ds_read_b64_tr_b16 a[198:199], v215 offset:34816\n" \
"v_mfma_f32_16x16x32_bf16 v[116:119], a[224:227], a[180:183], v[116:119]\n" \
"ds_read_b64_tr_b16 a[200:201], v215 offset:36864\n" \
"buffer_load_dwordx4 v[112:115], v210, %[srdz], s50 offen offset:2048 sc1\n" \
"v_mfma_f32_16x16x32_bf16 v[20:23], a[232:235], a[136:139], v[20:23]\n" \
"ds_read_b64_tr_b16 a[202:203], v215 offset:38912\n" \
"v_mfma_f32_16x16x32_bf16 v[20:23], a[236:239], a[140:143], v[20:23]\n" \
"ds_read_b64_tr_b16 a[204:205], v215 offset:40960\n" \
"v_mfma_f32_16x16x32_bf16 v[52:55], a[232:235], a[152:155], v[52:55]\n" \
"ds_read_b64_tr_b16 a[206:207], v215 offset:43008\n" \
"v_mfma_f32_16x16x32_bf16 v[52:55], a[236:239], a[156:159], v[52:55]\n" \
"ds_read_b64_tr_b16 a[220:221], v215 offset:45056\n" \
"buffer_load_dwordx4 v[120:123], v210, %[srdz], s50 offen offset:3072 sc1\n" \
"v_mfma_f32_16x16x32_bf16 v[84:87], a[232:235], a[168:171], v[84:87]\n" \
"ds_read_b64_tr_b16 a[222:223], v215 offset:47104\n" \
"v_readlane_b32 s54, %[thi], 16\n" \
"v_mfma_f32_16x16x32_bf16 v[84:87], a[236:239], a[172:175], v[84:87]\n" \
"v_mfma_f32_16x16x32_bf16 v[116:119], a[232:235], a[184:187], v[116:119]\n" \
"v_mfma_f32_16x16x32_bf16 v[116:119], a[236:239], a[188:191], v[116:119]\n" \
"s_waitcnt vmcnt(23) lgkmcnt(4)\n" \
"s_nop 0\n" \
"v_mfma_f32_16x16x32_bf16 v[28:31], a[208:211], a[128:131], v[28:31]\n" \
"buffer_load_dwordx4 v212, %[srdt], s49 offen offset:1024 lds\n" \
"v_mfma_f32_16x16x32_bf16 v[28:31], a[228:231], a[132:135], v[28:31]\n" \
"v_mfma_f32_16x16x32_bf16 v[60:63], a[208:211], a[144:147], v[60:63]\n" \
"s_cmp_gt_u32 s48, 5\n" \
"s_cselect_b32 s50, 5, 6\n" \
"s_mul_i32 s50, s50, 152064\n" \
"buffer_load_dwordx4 a[0:3], %[oq0], %[srdz], s50 offen offset:0 sc1\n" \
"v_mfma_f32_16x16x32_bf16 v[60:63], a[228:231], a[148:151], v[60:63]\n" \
"v_mfma_f32_16x16x32_bf16 v[92:95], a[208:211], a[160:163], v[92:95]\n" \
"buffer_load_dword v232, v226, %[srdz], s54 offen sc1\n" \
"v_mfma_f32_16x16x32_bf16 v[92:95], a[228:231], a[164:167], v[92:95]\n" \
"v_mfma_f32_16x16x32_bf16 v[124:127], a[208:211], a[176:179], v[124:127]\n" \
"ds_read_b64_tr_b16 a[224:225], v227 offset:32768\n" \
"buffer_load_dwordx4 a[4:7], %[oq0], %[srdz], s50 offen offset:1024 sc1\n" \
"v_mfma_f32_16x16x32_bf16 v[124:127], a[228:231], a[180:183], v[124:127]\n" \
"ds_read_b64_tr_b16 a[226:227], v227 offset:34816\n" \
"v_mfma_f32_16x16x32_bf16 v[192:195], a[196:199], a[64:67], v[128:131]\n" \
"ds_read_b64_tr_b16 a[232:233], v227 offset:36864\n" \
"v_mfma_f32_16x16x32_bf16 v[192:195], a[200:203], a[68:71], v[192:195]\n" \
"ds_read_b64_tr_b16 a[234:235], v227 offset:38912\n" \
"v_mfma_f32_16x16x32_bf16 v[196:199], a[196:199], a[80:83], v[132:135]\n" \
"buffer_load_dwordx4 a[8:11], %[oq0], %[srdz], s50 offen offset:2048 sc1\n" \
"v_mfma_f32_16x16x32_bf16 v[196:199], a[200:203], a[84:87], v[196:199]\n" \
"v_mfma_f32_16x16x32_bf16 v[200:203], a[196:199], a[96:99], v[136:139]\n" \
"buffer_load_dwordx4 a[12:15], %[oq0], %[srdz], s50 offen offset:3072 sc1\n" \
"v_mfma_f32_16x16x32_bf16 v[200:203], a[200:203], a[100:103], v[200:203]\n" \
"v_mfma_f32_16x16x32_bf16 v[204:207], a[196:199], a[112:115], v[140:143]\n" \
"ds_read_b64_tr_b16 a[236:237], v227 offset:40960\n" \
"v_mfma_f32_16x16x32_bf16 v[204:207], a[200:203], a[116:119], v[204:207]\n" \
"s_waitcnt vmcnt(26) lgkmcnt(5)\n" \
"s_nop 0\n" \
"v_mfma_f32_16x16x32_bf16 v[192:195], a[204:207], a[72:75], v[192:195]\n" \
"ds_read_b64_tr_b16 a[238:239], v227 offset:43008\n" \
"v_mfma_f32_16x16x32_bf16 v[192:195], a[220:223], a[76:79], v[192:195]\n" \
"ds_read_b64_tr_b16 a[208:209], v227 offset:45056\n" \
"buffer_load_dwordx4 v213, %[srdt], s49 offen offset:2048 lds\n" \
"v_mfma_f32_16x16x32_bf16 v[196:199], a[204:207], a[88:91], v[196:199]\n" \
"ds_read_b64_tr_b16 a[210:211], v227 offset:47104\n" \
"v_mfma_f32_16x16x32_bf16 v[196:199], a[220:223], a[92:95], v[196:199]\n" \
"buffer_load_dwordx4 a[16:19], v208, %[srdz], s50 offen offset:0 sc1\n" \
"v_mfma_f32_16x16x32_bf16 v[200:203], a[204:207], a[104:107], v[200:203]\n" \
"v_mfma_f32_16x16x32_bf16 v[200:203], a[220:223], a[108:111], v[200:203]\n" \
"buffer_load_dwordx4 a[20:23], v208, %[srdz], s50 offen offset:1024 sc1\n" \
"v_mfma_f32_16x16x32_bf16 v[204:207], a[204:207], a[120:123], v[204:207]\n" \
"ds_read_b64_tr_b16 a[228:229], v215 offset:33280\n" \
"v_mfma_f32_16x16x32_bf16 v[204:207], a[220:223], a[124:127], v[204:207]\n" \
"ds_read_b64_tr_b16 a[230:231], v215 offset:35328\n" \
"v_mfma_f32_16x16x32_bf16 v[28:31], a[212:215], a[136:139], v[28:31]\n" \
"buffer_load_dwordx4 a[24:27], v208, %[srdz], s50 offen offset:2048 sc1\n" \
"v_mfma_f32_16x16x32_bf16 v[28:31], a[216:219], a[140:143], v[28:31]\n" \
"v_mfma_f32_16x16x32_bf16 v[60:63], a[212:215], a[152:155], v[60:63]\n" \
"buffer_load_dwordx4 a[28:31], v208, %[srdz], s50 offen offset:3072 sc1\n" \
"v_mfma_f32_16x16x32_bf16 v[60:63], a[216:219], a[156:159], v[60:63]\n" \
"v_mfma_f32_16x16x32_bf16 v[92:95], a[212:215], a[168:171], v[92:95]\n" \
"v_mfma_f32_16x16x32_bf16 v[92:95], a[216:219], a[172:175], v[92:95]\n" \
"v_cmp_u_f32_e64 s[44:45], v192, v196\n" \
"buffer_load_dwordx4 v214, %[srdt], s49 offen offset:3072 lds\n" \
"v_mfma_f32_16x16x32_bf16 v[124:127], a[212:215], a[184:187], v[124:127]\n" \
"v_mfma_f32_16x16x32_bf16 v[124:127], a[216:219], a[188:191], v[124:127]\n" \
"v_cmp_u_f32_e64 s[56:57], v200, v204\n" \
"s_or_b64 s[56:57], s[56:57], s[44:45]\n" \
"s_cbranch_scc1 Lsp4_%=\n" \
"Lbk4_%=:\n" \
"s_waitcnt vmcnt(63) lgkmcnt(2)\n" \
"s_nop 0\n" \
"v_mfma_f32_16x16x32_bf16 v[144:147], a[224:227], a[64:67], v[144:147]\n" \
"ds_read_b64_tr_b16 a[212:213], v215 offset:37376\n" \
"v_mfma_f32_16x16x32_bf16 v[144:147], a[232:235], a[68:71], v[144:147]\n" \
"ds_read_b64_tr_b16 a[214:215], v215 offset:39424\n" \
"buffer_load_dwordx4 a[32:35], v209, %[srdz], s50 offen offset:0 sc1\n" \
"v_mfma_f32_16x16x32_bf16 v[148:151], a[224:227], a[80:83], v[148:151]\n" \
"ds_read_b64_tr_b16 a[196:197], v215 offset:41472\n" \
"v_mfma_f32_16x16x32_bf16 v[148:151], a[232:235], a[84:87], v[148:151]\n" \
"ds_read_b64_tr_b16 a[198:199], v215 offset:43520\n" \
"v_mfma_f32_16x16x32_bf16 v[152:155], a[224:227], a[96:99], v[152:155]\n" \
"ds_read_b64_tr_b16 a[200:201], v215 offset:45568\n" \
"v_mfma_f32_16x16x32_bf16 v[152:155], a[232:235], a[100:103], v[152:155]\n" \
"ds_read_b64_tr_b16 a[202:203], v215 offset:47616\n" \
"buffer_load_dwordx4 a[36:39], v209, %[srdz], s50 offen offset:1024 sc1\n" \
"v_mfma_f32_16x16x32_bf16 v[156:159], a[224:227], a[112:115], v[156:159]\n" \
"ds_read_b64_tr_b16 a[204:205], v227 offset:33280\n" \
"v_mfma_f32_16x16x32_bf16 v[156:159], a[232:235], a[116:119], v[156:159]\n" \
"ds_read_b64_tr_b16 a[206:207], v227 offset:35328\n" \
"v_mfma_f32_16x16x32_bf16 v[144:147], a[236:239], a[72:75], v[144:147]\n" \
"ds_read_b64_tr_b16 a[216:217], v227 offset:41472\n" \
"v_mfma_f32_16x16x32_bf16 v[144:147], a[208:211], a[76:79], v[144:147]\n" \
"ds_read_b64_tr_b16 a[218:219], v227 offset:43520\n" \
"buffer_load_dwordx4 a[40:43], v209, %[srdz], s50 offen offset:2048 sc1\n" \
"v_mfma_f32_16x16x32_bf16 v[148:151], a[236:239], a[88:91], v[148:151]\n" \
"ds_read_b64_tr_b16 a[220:221], v227 offset:37376\n" \
"v_mfma_f32_16x16x32_bf16 v[148:151], a[208:211], a[92:95], v[148:151]\n" \
"ds_read_b64_tr_b16 a[222:223], v227 offset:39424\n" \
"v_mfma_f32_16x16x32_bf16 v[152:155], a[236:239], a[104:107], v[152:155]\n" \
"ds_read_b64_tr_b16 a[224:225], v227 offset:45568\n" \
"v_mfma_f32_16x16x32_bf16 v[152:155], a[208:211], a[108:111], v[152:155]\n" \
"ds_read_b64_tr_b16 a[226:227], v227 offset:47616\n" \
"buffer_load_dwordx4 a[44:47], v209, %[srdz], s50 offen offset:3072 sc1\n" \
"v_mfma_f32_16x16x32_bf16 v[156:159], a[236:239], a[120:123], v[156:159]\n" \
"v_mfma_f32_16x16x32_bf16 v[156:159], a[208:211], a[124:127], v[156:159]\n" \
"s_waitcnt vmcnt(63) lgkmcnt(12)\n" \
"s_nop 0\n" \
"v_mfma_f32_16x16x32_bf16 v[160:163], a[228:231], a[64:67], v[160:163]\n" \
"buffer_load_dwordx4 a[48:51], v210, %[srdz], s50 offen offset:0 sc1\n" \
"v_mfma_f32_16x16x32_bf16 v[160:163], a[212:215], a[68:71], v[160:163]\n" \
"v_mfma_f32_16x16x32_bf16 v[164:167], a[228:231], a[80:83], v[164:167]\n" \
"buffer_load_dwordx4 a[52:55], v210, %[srdz], s50 offen offset:1024 sc1\n" \
"v_mfma_f32_16x16x32_bf16 v[164:167], a[212:215], a[84:87], v[164:167]\n" \
"v_mfma_f32_16x16x32_bf16 v[168:171], a[228:231], a[96:99], v[168:171]\n" \
"s_waitcnt vmcnt(28) lgkmcnt(0)\n" \
"s_barrier\n" \
"v_mfma_f32_16x16x32_bf16 v[168:171], a[212:215], a[100:103], v[168:171]\n" \
"ds_read_b64_tr_b16 a[232:233], v220 offset:32768\n" \
"v_mfma_f32_16x16x32_bf16 v[172:175], a[228:231], a[112:115], v[172:175]\n" \
"ds_read_b64_tr_b16 a[234:235], v220 offset:34816\n" \
"v_mfma_f32_16x16x32_bf16 v[172:175], a[212:215], a[116:119], v[172:175]\n" \
"ds_read_b64_tr_b16 a[236:237], v220 offset:36864\n" \
"v_mfma_f32_16x16x32_bf16 v[160:163], a[196:199], a[72:75], v[160:163]\n" \
"ds_read_b64_tr_b16 a[238:239], v220 offset:38912\n" \
"v_mfma_f32_16x16x32_bf16 v[160:163], a[200:203], a[76:79], v[160:163]\n" \
"ds_read_b64_tr_b16 a[208:209], v220 offset:40960\n" \
"s_add_u32 m0, %[m0b], 49152\n" \
"s_cmp_gt_u32 s48, 6\n" \
"s_cselect_b32 s49, 6, 7\n" \
"s_lshl_b32 s49, s49, 18\n" \
"s_or_b32 s49, s49, s58\n" \
"buffer_load_dwordx4 %[wo0], %[srdt], s49 offen offset:0 lds\n" \
"v_mfma_f32_16x16x32_bf16 v[164:167], a[196:199], a[88:91], v[164:167]\n" \
"ds_read_b64_tr_b16 a[210:211], v220 offset:43008\n" \
"v_mfma_f32_16x16x32_bf16 v[164:167], a[200:203], a[92:95], v[164:167]\n" \
"ds_read_b64_tr_b16 a[228:229], v220 offset:45056\n" \
"buffer_load_dwordx4 a[56:59], v210, %[srdz], s50 offen offset:2048 sc1\n" \
"v_mfma_f32_16x16x32_bf16 v[168:171], a[196:199], a[104:107], v[168:171]\n" \
"ds_read_b64_tr_b16 a[230:231], v220 offset:47104\n" \
"v_mfma_f32_16x16x32_bf16 v[168:171], a[200:203], a[108:111], v[168:171]\n" \
"buffer_load_dwordx4 a[60:63], v210, %[srdz], s50 offen offset:3072 sc1\n" \
"v_mfma_f32_16x16x32_bf16 v[172:175], a[196:199], a[120:123], v[172:175]\n" \
"v_mfma_f32_16x16x32_bf16 v[172:175], a[200:203], a[124:127], v[172:175]\n" \
"s_waitcnt vmcnt(23) lgkmcnt(4)\n" \
"s_nop 0\n" \
"v_mfma_f32_16x16x32_bf16 v[176:179], a[204:207], a[64:67], v[176:179]\n" \
"v_mfma_f32_16x16x32_bf16 v[176:179], a[220:223], a[68:71], v[176:179]\n" \
"s_cmp_gt_u32 s48, 6\n" \
"s_cselect_b32 s50, 6, 7\n" \
"s_mul_i32 s50, s50, 152064\n" \
"buffer_load_dwordx4 a[128:131], %[oq0], %[srdz], s50 offen offset:0 sc1\n" \
"v_mfma_f32_16x16x32_bf16 v[180:183], a[204:207], a[80:83], v[180:183]\n" \
"v_mfma_f32_16x16x32_bf16 v[180:183], a[220:223], a[84:87], v[180:183]\n" \
"v_mfma_f32_16x16x32_bf16 v[184:187], a[204:207], a[96:99], v[184:187]\n" \
"v_mfma_f32_16x16x32_bf16 v[184:187], a[220:223], a[100:103], v[184:187]\n" \
"buffer_load_dwordx4 v212, %[srdt], s49 offen offset:1024 lds\n" \
"v_mfma_f32_16x16x32_bf16 v[188:191], a[204:207], a[112:115], v[188:191]\n" \
"ds_read_b64_tr_b16 a[212:213], v221 offset:32768\n" \
"v_mfma_f32_16x16x32_bf16 v[188:191], a[220:223], a[116:119], v[188:191]\n" \
"ds_read_b64_tr_b16 a[214:215], v221 offset:34816\n" \
"v_mfma_f32_16x16x32_bf16 v[128:131], a[232:235], v[0:3], v[192:195]\n" \
"ds_read_b64_tr_b16 a[196:197], v221 offset:36864\n" \
"buffer_load_dwordx4 a[132:135], %[oq0], %[srdz], s50 offen offset:1024 sc1\n" \
"v_mfma_f32_16x16x32_bf16 v[128:131], a[236:239], v[8:11], v[128:131]\n" \
"ds_read_b64_tr_b16 a[198:199], v221 offset:38912\n" \
"v_mfma_f32_16x16x32_bf16 v[132:135], a[232:235], v[32:35], v[196:199]\n" \
"buffer_load_dwordx4 a[136:139], %[oq0], %[srdz], s50 offen offset:2048 sc1\n" \
"v_mfma_f32_16x16x32_bf16 v[132:135], a[236:239], v[40:43], v[132:135]\n" \
"v_mfma_f32_16x16x32_bf16 v[136:139], a[232:235], v[64:67], v[200:203]\n" \
"v_mfma_f32_16x16x32_bf16 v[136:139], a[236:239], v[72:75], v[136:139]\n" \
"buffer_load_dwordx4 a[140:143], %[oq0], %[srdz], s50 offen offset:3072 sc1\n" \
"v_mfma_f32_16x16x32_bf16 v[140:143], a[232:235], v[96:99], v[204:207]\n" \
"ds_read_b64_tr_b16 a[200:201], v221 offset:40960\n" \
"v_mfma_f32_16x16x32_bf16 v[140:143], a[236:239], v[104:107], v[140:143]\n" \
"s_waitcnt vmcnt(26) lgkmcnt(5)\n" \
"s_nop 0\n" \
"v_mfma_f32_16x16x32_bf16 v[128:131], a[208:211], v[16:19], v[128:131]\n" \
"ds_read_b64_tr_b16 a[202:203], v221 offset:43008\n" \
"v_mfma_f32_16x16x32_bf16 v[128:131], a[228:231], v[24:27], v[128:131]\n" \
"ds_read_b64_tr_b16 a[204:205], v221 offset:45056\n" \
"v_mfma_f32_16x16x32_bf16 v[132:135], a[208:211], v[48:51], v[132:135]\n" \
"ds_read_b64_tr_b16 a[206:207], v221 offset:47104\n" \
"v_mfma_f32_16x16x32_bf16 v[132:135], a[228:231], v[56:59], v[132:135]\n" \
"buffer_load_dwordx4 a[144:147], v208, %[srdz], s50 offen offset:0 sc1\n" \
"v_mfma_f32_16x16x32_bf16 v[136:139], a[208:211], v[80:83], v[136:139]\n" \
"v_mfma_f32_16x16x32_bf16 v[136:139], a[228:231], v[88:91], v[136:139]\n" \
"buffer_load_dwordx4 v213, %[srdt], s49 offen offset:2048 lds\n" \
"v_mfma_f32_16x16x32_bf16 v[140:143], a[208:211], v[112:115], v[140:143]\n" \
"ds_read_b64_tr_b16 a[220:221], v220 offset:33280\n" \
"v_mfma_f32_16x16x32_bf16 v[140:143], a[228:231], v[120:123], v[140:143]\n" \
"ds_read_b64_tr_b16 a[222:223], v220 offset:35328\n" \
"v_mfma_f32_16x16x32_bf16 v[176:179], a[216:219], a[72:75], v[176:179]\n" \
"buffer_load_dwordx4 a[148:151], v208, %[srdz], s50 offen offset:1024 sc1\n" \
"v_mfma_f32_16x16x32_bf16 v[176:179], a[224:227], a[76:79], v[176:179]\n" \
"v_mfma_f32_16x16x32_bf16 v[180:183], a[216:219], a[88:91], v[180:183]\n" \
"buffer_load_dwordx4 a[152:155], v208, %[srdz], s50 offen offset:2048 sc1\n" \
"v_mfma_f32_16x16x32_bf16 v[180:183], a[224:227], a[92:95], v[180:183]\n" \
"v_mfma_f32_16x16x32_bf16 v[184:187], a[216:219], a[104:107], v[184:187]\n" \
"v_mfma_f32_16x16x32_bf16 v[184:187], a[224:227], a[108:111], v[184:187]\n" \
"v_cmp_u_f32_e64 s[44:45], v128, v132\n" \
"buffer_load_dwordx4 a[156:159], v208, %[srdz], s50 offen offset:3072 sc1\n" \
"v_mfma_f32_16x16x32_bf16 v[188:191], a[216:219], a[120:123], v[188:191]\n" \
"v_mfma_f32_16x16x32_bf16 v[188:191], a[224:227], a[124:127], v[188:191]\n" \
"v_cmp_u_f32_e64 s[56:57], v136, v140\n" \
"s_or_b64 s[56:57], s[56:57], s[44:45]\n" \
"s_cbranch_scc1 Lsp5_%=\n" \
"Lbk5_%=:\n" \
"s_waitcnt vmcnt(63) lgkmcnt(2)\n" \
"s_nop 0\n" \
"v_mfma_f32_16x16x32_bf16 v[144:147], a[212:215], v[0:3], v[144:147]\n" \
"ds_read_b64_tr_b16 a[216:217], v220 offset:37376\n" \
"v_mfma_f32_16x16x32_bf16 v[144:147], a[196:199], v[8:11], v[144:147]\n" \
"ds_read_b64_tr_b16 a[218:219], v220 offset:39424\n" \
"v_mfma_f32_16x16x32_bf16 v[148:151], a[212:215], v[32:35], v[148:151]\n" \
"ds_read_b64_tr_b16 a[208:209], v220 offset:41472\n" \
"v_mfma_f32_16x16x32_bf16 v[148:151], a[196:199], v[40:43], v[148:151]\n" \
"ds_read_b64_tr_b16 a[210:211], v220 offset:43520\n" \
"v_mfma_f32_16x16x32_bf16 v[152:155], a[212:215], v[64:67], v[152:155]\n" \
"ds_read_b64_tr_b16 a[224:225], v220 offset:45568\n" \
"buffer_load_dwordx4 a[160:163], v209, %[srdz], s50 offen offset:0 sc1\n" \
"v_mfma_f32_16x16x32_bf16 v[152:155], a[196:199], v[72:75], v[152:155]\n" \
"ds_read_b64_tr_b16 a[226:227], v220 offset:47616\n" \
"v_mfma_f32_16x16x32_bf16 v[156:159], a[212:215], v[96:99], v[156:159]\n" \
"ds_read_b64_tr_b16 a[228:229], v221 offset:33280\n" \
"buffer_load_dwordx4 v214, %[srdt], s49 offen offset:3072 lds\n" \
"v_mfma_f32_16x16x32_bf16 v[156:159], a[196:199], v[104:107], v[156:159]\n" \
"ds_read_b64_tr_b16 a[230:231], v221 offset:35328\n" \
"v_mfma_f32_16x16x32_bf16 v[144:147], a[200:203], v[16:19], v[144:147]\n" \
"ds_read_b64_tr_b16 a[232:233], v221 offset:41472\n" \
"v_readlane_b32 s64, %[tlo], 10\n" \
"v_readlane_b32 s65, %[thi], 10\n" \
"buffer_load_dwordx4 a[164:167], v209, %[srdz], s50 offen offset:1024 sc1\n" \
"v_mfma_f32_16x16x32_bf16 v[144:147], a[204:207], v[24:27], v[144:147]\n" \
"ds_read_b64_tr_b16 a[234:235], v221 offset:43520\n" \
"v_mfma_f32_16x16x32_bf16 v[148:151], a[200:203], v[48:51], v[148:151]\n" \
"ds_read_b64_tr_b16 a[236:237], v221 offset:37376\n" \
"v_mfma_f32_16x16x32_bf16 v[148:151], a[204:207], v[56:59], v[148:151]\n" \
"ds_read_b64_tr_b16 a[238:239], v221 offset:39424\n" \
"v_mfma_f32_16x16x32_bf16 v[152:155], a[200:203], v[80:83], v[152:155]\n" \
"ds_read_b64_tr_b16 a[212:213], v221 offset:45568\n" \
"buffer_load_dwordx4 a[168:171], v209, %[srdz], s50 offen offset:2048 sc1\n" \
"v_mfma_f32_16x16x32_bf16 v[152:155], a[204:207], v[88:91], v[152:155]\n" \
"ds_read_b64_tr_b16 a[214:215], v221 offset:47616\n" \
"v_mfma_f32_16x16x32_bf16 v[156:159], a[200:203], v[112:115], v[156:159]\n" \
"buffer_load_dwordx4 a[172:175], v209, %[srdz], s50 offen offset:3072 sc1\n" \
"v_mfma_f32_16x16x32_bf16 v[156:159], a[204:207], v[120:123], v[156:159]\n" \
"s_waitcnt vmcnt(63) lgkmcnt(12)\n" \
"s_nop 0\n" \
"v_mfma_f32_16x16x32_bf16 v[160:163], a[220:223], v[0:3], v[160:163]\n" \
"v_mfma_f32_16x16x32_bf16 v[160:163], a[216:219], v[8:11], v[160:163]\n" \
"buffer_load_dwordx4 a[176:179], v210, %[srdz], s50 offen offset:0 sc1\n" \
"v_mfma_f32_16x16x32_bf16 v[164:167], a[220:223], v[32:35], v[164:167]\n" \
"v_mfma_f32_16x16x32_bf16 v[164:167], a[216:219], v[40:43], v[164:167]\n" \
"v_mfma_f32_16x16x32_bf16 v[168:171], a[220:223], v[64:67], v[168:171]\n" \
"s_waitcnt vmcnt(25) lgkmcnt(0)\n" \
"s_barrier\n" \
"v_mfma_f32_16x16x32_bf16 v[168:171], a[216:219], v[72:75], v[168:171]\n" \
"ds_read_b64_tr_b16 a[196:197], v220 offset:49152\n" \
"v_mfma_f32_16x16x32_bf16 v[172:175], a[220:223], v[96:99], v[172:175]\n" \
"ds_read_b64_tr_b16 a[198:199], v220 offset:51200\n" \
"v_mfma_f32_16x16x32_bf16 v[172:175], a[216:219], v[104:107], v[172:175]\n" \
"ds_read_b64_tr_b16 a[200:201], v220 offset:53248\n" \
"v_mfma_f32_16x16x32_bf16 v[160:163], a[208:211], v[16:19], v[160:163]\n" \
"ds_read_b64_tr_b16 a[202:203], v220 offset:55296\n" \
"buffer_load_dwordx4 a[180:183], v210, %[srdz], s50 offen offset:1024 sc1\n" \
"v_mfma_f32_16x16x32_bf16 v[160:163], a[224:227], v[24:27], v[160:163]\n" \
"ds_read_b64_tr_b16 a[204:205], v220 offset:57344\n" \
"v_mfma_f32_16x16x32_bf16 v[164:167], a[208:211], v[48:51], v[164:167]\n" \
"ds_read_b64_tr_b16 a[206:207], v220 offset:59392\n" \
"v_mfma_f32_16x16x32_bf16 v[164:167], a[224:227], v[56:59], v[164:167]\n" \
"ds_read_b64_tr_b16 a[220:221], v220 offset:61440\n" \
"v_mfma_f32_16x16x32_bf16 v[168:171], a[208:211], v[80:83], v[168:171]\n" \
"ds_read_b64_tr_b16 a[222:223], v220 offset:63488\n" \
"v_readlane_b32 s63, %[thi], 33\n" \
"v_lshlrev_b32_e64 v234, 3, v224\n" \
"buffer_load_dwordx4 a[184:187], v210, %[srdz], s50 offen offset:2048 sc1\n" \
"v_mfma_f32_16x16x32_bf16 v[168:171], a[224:227], v[88:91], v[168:171]\n" \
"v_mfma_f32_16x16x32_bf16 v[172:175], a[208:211], v[112:115], v[172:175]\n" \
"v_and_b32_e32 v234, 0xc00, v234\n" \
"v_lshlrev_b32_e64 v235, 2, v224\n" \
"buffer_load_dwordx4 a[188:191], v210, %[srdz], s50 offen offset:3072 sc1\n" \
"v_mfma_f32_16x16x32_bf16 v[172:175], a[224:227], v[120:123], v[172:175]\n" \
"s_waitcnt vmcnt(63) lgkmcnt(4)\n" \
"s_nop 0\n" \
"v_mfma_f32_16x16x32_bf16 v[176:179], a[228:231], v[0:3], v[176:179]\n" \
"v_and_b32_e32 v235, 0x7000, v235\n" \
"v_bfe_u32 v230, v224, 9, 1\n" \
"v_mfma_f32_16x16x32_bf16 v[176:179], a[236:239], v[8:11], v[176:179]\n" \
"v_mfma_f32_16x16x32_bf16 v[180:183], a[228:231], v[32:35], v[180:183]\n" \
"v_lshl_or_b32 v235, v230, 2, v235\n" \
"v_readlane_b32 s52, %[thi], 35\n" \
"v_mfma_f32_16x16x32_bf16 v[180:183], a[236:239], v[40:43], v[180:183]\n" \
"v_or_b32_e64 v234, v234, v235\n" \
"v_mfma_f32_16x16x32_bf16 v[184:187], a[228:231], v[64:67], v[184:187]\n" \
"v_mfma_f32_16x16x32_bf16 v[184:187], a[236:239], v[72:75], v[184:187]\n" \
"v_mfma_f32_16x16x32_bf16 v[188:191], a[228:231], v[96:99], v[188:191]\n" \
".if . - adaln_body != 53248\n" \
".error \"size model\"\n" \
".endif\n" \
"Lk12_%=:\n" \
"s_cbranch_execz Lk13_%=\n" \
"s_nop 0\n" \
"ds_read_b64_tr_b16 a[216:217], v221 offset:49152\n" \
"s_waitcnt vmcnt(22)\n" \
"v_cmp_eq_u32_e32 vcc, 0, v232\n" \
"s_cbranch_vccnz Lspp_%=\n" \
"Lbkp_%=:\n" \
"s_nop 0\n" \
"v_mfma_f32_16x16x32_bf16 v[188:191], a[236:239], v[104:107], v[188:191]\n" \
"ds_read_b64_tr_b16 a[218:219], v221 offset:51200\n" \
"v_mfma_f32_16x16x32_bf16 v[192:195], a[196:199], a[0:3], v[128:131]\n" \
"ds_read_b64_tr_b16 a[208:209], v221 offset:53248\n" \
"v_mfma_f32_16x16x32_bf16 v[192:195], a[200:203], a[4:7], v[192:195]\n" \
"ds_read_b64_tr_b16 a[210:211], v221 offset:55296\n" \
"buffer_load_dword v235, v234, %[srdz], s63 offen sc1\n" \
"v_mfma_f32_16x16x32_bf16 v[196:199], a[196:199], a[16:19], v[132:135]\n" \
"v_mfma_f32_16x16x32_bf16 v[196:199], a[200:203], a[20:23], v[196:199]\n" \
"s_add_u32 m0, %[m0b], 0\n" \
"s_cmp_gt_u32 s48, 3\n" \
"s_cselect_b32 s49, 3, 4\n" \
"s_lshl_b32 s49, s49, 18\n" \
"s_or_b32 s49, s49, s59\n" \
"s_nop 0\n" \
"buffer_load_dwordx4 %[wo0], %[srdt], s49 offen offset:0 lds\n" \
"v_mfma_f32_16x16x32_bf16 v[200:203], a[196:199], a[32:35], v[136:139]\n" \
"v_mfma_f32_16x16x32_bf16 v[200:203], a[200:203], a[36:39], v[200:203]\n" \
"v_mfma_f32_16x16x32_bf16 v[204:207], a[196:199], a[48:51], v[140:143]\n" \
"ds_read_b64_tr_b16 a[224:225], v221 offset:57344\n" \
"v_mfma_f32_16x16x32_bf16 v[204:207], a[200:203], a[52:55], v[204:207]\n" \
"s_waitcnt vmcnt(21) lgkmcnt(5)\n" \
"s_nop 0\n" \
"v_mfma_f32_16x16x32_bf16 v[192:195], a[204:207], a[8:11], v[192:195]\n" \
"ds_read_b64_tr_b16 a[226:227], v221 offset:59392\n" \
"v_mfma_f32_16x16x32_bf16 v[192:195], a[220:223], a[12:15], v[192:195]\n" \
"ds_read_b64_tr_b16 a[228:229], v221 offset:61440\n" \
"v_mfma_f32_16x16x32_bf16 v[196:199], a[204:207], a[24:27], v[196:199]\n" \
"ds_read_b64_tr_b16 a[230:231], v221 offset:63488\n" \
"v_mfma_f32_16x16x32_bf16 v[196:199], a[220:223], a[28:31], v[196:199]\n" \
"buffer_load_dwordx4 v212, %[srdt], s49 offen offset:1024 lds\n" \
"v_mfma_f32_16x16x32_bf16 v[200:203], a[204:207], a[40:43], v[200:203]\n" \
"v_mfma_f32_16x16x32_bf16 v[200:203], a[220:223], a[44:47], v[200:203]\n" \
"v_mfma_f32_16x16x32_bf16 v[204:207], a[204:207], a[56:59], v[204:207]\n" \
"ds_read_b64_tr_b16 a[236:237], v220 offset:49664\n" \
"v_mfma_f32_16x16x32_bf16 v[204:207], a[220:223], a[60:63], v[204:207]\n" \
"ds_read_b64_tr_b16 a[238:239], v220 offset:51712\n" \
"v_mfma_f32_16x16x32_bf16 v[176:179], a[232:235], v[16:19], v[176:179]\n" \
"v_mfma_f32_16x16x32_bf16 v[176:179], a[212:215], v[24:27], v[176:179]\n" \
"v_mfma_f32_16x16x32_bf16 v[180:183], a[232:235], v[48:51], v[180:183]\n" \
"buffer_load_dwordx4 v213, %[srdt], s49 offen offset:2048 lds\n" \
"v_mfma_f32_16x16x32_bf16 v[180:183], a[212:215], v[56:59], v[180:183]\n" \
"v_mfma_f32_16x16x32_bf16 v[184:187], a[232:235], v[80:83], v[184:187]\n" \
"s_add_u32 m0, %[m0b], 98304\n" \
"s_mov_b32 s61, 0\n" \
"s_nop 0\n" \
"buffer_load_dwordx4 %[vdn], s[64:67], s61 offen offset:0 lds\n" \
"v_mfma_f32_16x16x32_bf16 v[184:187], a[212:215], v[88:91], v[184:187]\n" \
"v_cmp_u_f32_e64 s[44:45], v192, v196\n" \
"v_mfma_f32_16x16x32_bf16 v[188:191], a[232:235], v[112:115], v[188:191]\n" \
"v_mfma_f32_16x16x32_bf16 v[188:191], a[212:215], v[120:123], v[188:191]\n" \
"v_cmp_u_f32_e64 s[56:57], v200, v204\n" \
"s_or_b64 s[56:57], s[56:57], s[44:45]\n" \
"s_cbranch_scc1 Lsp6_%=\n" \
"Lbk6_%=:\n" \
"s_waitcnt vmcnt(63) lgkmcnt(2)\n" \
"s_nop 0\n" \
"v_mfma_f32_16x16x32_bf16 v[144:147], a[216:219], a[0:3], v[144:147]\n" \
"ds_read_b64_tr_b16 a[232:233], v220 offset:53760\n" \
"v_mfma_f32_16x16x32_bf16 v[144:147], a[208:211], a[4:7], v[144:147]\n" \
"ds_read_b64_tr_b16 a[234:235], v220 offset:55808\n" \
"v_mfma_f32_16x16x32_bf16 v[148:151], a[216:219], a[16:19], v[148:151]\n" \
"ds_read_b64_tr_b16 a[196:197], v220 offset:57856\n" \
"v_mfma_f32_16x16x32_bf16 v[148:151], a[208:211], a[20:23], v[148:151]\n" \
"ds_read_b64_tr_b16 a[198:199], v220 offset:59904\n" \
"v_mfma_f32_16x16x32_bf16 v[152:155], a[216:219], a[32:35], v[152:155]\n" \
"ds_read_b64_tr_b16 a[200:201], v220 offset:61952\n" \
"v_mfma_f32_16x16x32_bf16 v[152:155], a[208:211], a[36:39], v[152:155]\n" \
"ds_read_b64_tr_b16 a[202:203], v220 offset:64000\n" \
"v_mfma_f32_16x16x32_bf16 v[156:159], a[216:219], a[48:51], v[156:159]\n" \
"ds_read_b64_tr_b16 a[204:205], v221 offset:49664\n" \
"v_mfma_f32_16x16x32_bf16 v[156:159], a[208:211], a[52:55], v[156:159]\n" \
"ds_read_b64_tr_b16 a[206:207], v221 offset:51712\n" \
"s_add_u32 m0, %[m0b], 0\n" \
"s_nop 0\n" \
"buffer_load_dwordx4 v214, %[srdt], s49 offen offset:3072 lds\n" \
"v_mfma_f32_16x16x32_bf16 v[144:147], a[224:227], a[8:11], v[144:147]\n" \
"ds_read_b64_tr_b16 a[212:213], v221 offset:57856\n" \
"v_mfma_f32_16x16x32_bf16 v[144:147], a[228:231], a[12:15], v[144:147]\n" \
"ds_read_b64_tr_b16 a[214:215], v221 offset:59904\n" \
"v_mfma_f32_16x16x32_bf16 v[148:151], a[224:227], a[24:27], v[148:151]\n" \
"ds_read_b64_tr_b16 a[220:221], v221 offset:53760\n" \
"v_mfma_f32_16x16x32_bf16 v[148:151], a[228:231], a[28:31], v[148:151]\n" \
"ds_read_b64_tr_b16 a[222:223], v221 offset:55808\n" \
"v_mfma_f32_16x16x32_bf16 v[152:155], a[224:227], a[40:43], v[152:155]\n" \
"ds_read_b64_tr_b16 a[216:217], v221 offset:61952\n" \
"v_mfma_f32_16x16x32_bf16 v[152:155], a[228:231], a[44:47], v[152:155]\n" \
"ds_read_b64_tr_b16 a[218:219], v221 offset:64000\n" \
"v_mfma_f32_16x16x32_bf16 v[156:159], a[224:227], a[56:59], v[156:159]\n" \
"v_xor_b32_e64 v218, 64, %[vdn]\n" \
"s_add_u32 m0, %[m0b], 99328\n" \
"s_mov_b32 s61, 4096\n" \
"buffer_load_dwordx4 v218, s[64:67], s61 offen offset:0 lds\n" \
"v_mfma_f32_16x16x32_bf16 v[156:159], a[228:231], a[60:63], v[156:159]\n" \
"s_waitcnt vmcnt(63) lgkmcnt(12)\n" \
"s_nop 0\n" \
"v_mfma_f32_16x16x32_bf16 v[160:163], a[236:239], a[0:3], v[160:163]\n" \
"v_mfma_f32_16x16x32_bf16 v[160:163], a[232:235], a[4:7], v[160:163]\n" \
"v_mfma_f32_16x16x32_bf16 v[164:167], a[236:239], a[16:19], v[164:167]\n" \
"v_mfma_f32_16x16x32_bf16 v[164:167], a[232:235], a[20:23], v[164:167]\n" \
"s_add_u32 m0, %[m0b], 16384\n" \
"s_cmp_gt_u32 s48, 4\n" \
"s_cselect_b32 s49, 4, 5\n" \
"s_lshl_b32 s49, s49, 18\n" \
"s_or_b32 s49, s49, s59\n" \
"buffer_load_dwordx4 %[wo0], %[srdt], s49 offen offset:0 lds\n" \
"v_mfma_f32_16x16x32_bf16 v[168:171], a[236:239], a[32:35], v[168:171]\n" \
"s_waitcnt vmcnt(15) lgkmcnt(0)\n" \
"s_barrier\n" \
"v_mfma_f32_16x16x32_bf16 v[168:171], a[232:235], a[36:39], v[168:171]\n" \
"ds_read_b64_tr_b16 a[208:209], v215 offset:49152\n" \
"v_mfma_f32_16x16x32_bf16 v[172:175], a[236:239], a[48:51], v[172:175]\n" \
"ds_read_b64_tr_b16 a[210:211], v215 offset:51200\n" \
"v_mfma_f32_16x16x32_bf16 v[172:175], a[232:235], a[52:55], v[172:175]\n" \
"ds_read_b64_tr_b16 a[224:225], v215 offset:53248\n" \
"v_mfma_f32_16x16x32_bf16 v[160:163], a[196:199], a[8:11], v[160:163]\n" \
"ds_read_b64_tr_b16 a[226:227], v215 offset:55296\n" \
"v_mfma_f32_16x16x32_bf16 v[160:163], a[200:203], a[12:15], v[160:163]\n" \
"ds_read_b64_tr_b16 a[228:229], v215 offset:57344\n" \
"v_mfma_f32_16x16x32_bf16 v[164:167], a[196:199], a[24:27], v[164:167]\n" \
"ds_read_b64_tr_b16 a[230:231], v215 offset:59392\n" \
"v_mfma_f32_16x16x32_bf16 v[164:167], a[200:203], a[28:31], v[164:167]\n" \
"ds_read_b64_tr_b16 a[236:237], v215 offset:61440\n" \
"v_mfma_f32_16x16x32_bf16 v[168:171], a[196:199], a[40:43], v[168:171]\n" \
"ds_read_b64_tr_b16 a[238:239], v215 offset:63488\n" \
"v_mfma_f32_16x16x32_bf16 v[168:171], a[200:203], a[44:47], v[168:171]\n" \
"buffer_load_dwordx4 v212, %[srdt], s49 offen offset:1024 lds\n" \
"v_mfma_f32_16x16x32_bf16 v[172:175], a[196:199], a[56:59], v[172:175]\n" \
"v_mfma_f32_16x16x32_bf16 v[172:175], a[200:203], a[60:63], v[172:175]\n" \
"s_waitcnt vmcnt(63) lgkmcnt(4)\n" \
"s_nop 0\n" \
"v_mfma_f32_16x16x32_bf16 v[176:179], a[204:207], a[0:3], v[176:179]\n" \
"v_xor_b32_e32 v218, 0x80, %[vdn]\n" \
"s_add_u32 m0, %[m0b], 100352\n" \
"s_mov_b32 s61, 8192\n" \
"buffer_load_dwordx4 v218, s[64:67], s61 offen offset:0 lds\n" \
"v_mfma_f32_16x16x32_bf16 v[176:179], a[220:223], a[4:7], v[176:179]\n" \
"v_mfma_f32_16x16x32_bf16 v[180:183], a[204:207], a[16:19], v[180:183]\n" \
"v_mfma_f32_16x16x32_bf16 v[180:183], a[220:223], a[20:23], v[180:183]\n" \
"v_mfma_f32_16x16x32_bf16 v[184:187], a[204:207], a[32:35], v[184:187]\n" \
"v_mfma_f32_16x16x32_bf16 v[184:187], a[220:223], a[36:39], v[184:187]\n" \
"v_mfma_f32_16x16x32_bf16 v[188:191], a[204:207], a[48:51], v[188:191]\n" \
"ds_read_b64_tr_b16 a[232:233], v227 offset:49152\n" \
"v_mfma_f32_16x16x32_bf16 v[188:191], a[220:223], a[52:55], v[188:191]\n" \
"ds_read_b64_tr_b16 a[234:235], v227 offset:51200\n" \
"v_mfma_f32_16x16x32_bf16 v[128:131], a[208:211], a[128:131], v[192:195]\n" \
"ds_read_b64_tr_b16 a[196:197], v227 offset:53248\n" \
"v_mfma_f32_16x16x32_bf16 v[128:131], a[224:227], a[132:135], v[128:131]\n" \
"ds_read_b64_tr_b16 a[198:199], v227 offset:55296\n" \
"v_mfma_f32_16x16x32_bf16 v[132:135], a[208:211], a[144:147], v[196:199]\n" \
"s_add_u32 m0, %[m0b], 16384\n" \
"s_nop 0\n" \
"s_nop 0\n" \
"buffer_load_dwordx4 v213, %[srdt], s49 offen offset:2048 lds\n" \
"v_mfma_f32_16x16x32_bf16 v[132:135], a[224:227], a[148:151], v[132:135]\n" \
"v_mfma_f32_16x16x32_bf16 v[136:139], a[208:211], a[160:163], v[200:203]\n" \
"s_waitcnt vmcnt(10)\n" \
"v_and_b32_e32 v230, 1, v235\n" \
"v_mfma_f32_16x16x32_bf16 v[136:139], a[224:227], a[164:167], v[136:139]\n" \
"v_cmp_eq_u32_e64 s[68:69], 0, v230\n" \
"v_mfma_f32_16x16x32_bf16 v[140:143], a[208:211], a[176:179], v[204:207]\n" \
"ds_read_b64_tr_b16 a[200:201], v227 offset:57344\n" \
"s_cmp_lg_u64 s[68:69], 0\n" \
"s_cbranch_scc1 Ls1_%=\n" \
"Ls1b_%=:\n" \
"v_add_f32_e64 v235, %[rd0], v235\n" \
"v_mfma_f32_16x16x32_bf16 v[140:143], a[224:227], a[180:183], v[140:143]\n" \
"s_waitcnt vmcnt(11) lgkmcnt(5)\n" \
"s_nop 0\n" \
"v_mfma_f32_16x16x32_bf16 v[128:131], a[228:231], a[136:139], v[128:131]\n" \
"ds_read_b64_tr_b16 a[202:203], v227 offset:59392\n" \
"v_bfe_u32 v234, v224, 9, 1\n" \
"v_mfma_f32_16x16x32_bf16 v[128:131], a[236:239], a[140:143], v[128:131]\n" \
"ds_read_b64_tr_b16 a[204:205], v227 offset:61440\n" \
"v_add_f32_dpp v235, v235, v235 quad_perm:[1,0,3,2] row_mask:0xf bank_mask:0xf\n" \
"v_add_u32_e64 v230, s52, v234\n" \
"v_xor_b32_e32 v218, 0xc0, %[vdn]\n" \
"s_add_u32 m0, %[m0b], 101376\n" \
"s_mov_b32 s61, 12288\n" \
"buffer_load_dwordx4 v218, s[64:67], s61 offen offset:0 lds\n" \
"v_mfma_f32_16x16x32_bf16 v[132:135], a[228:231], a[152:155], v[132:135]\n" \
"ds_read_b64_tr_b16 a[206:207], v227 offset:63488\n" \
"s_mul_i32 s63, %[wid], 4088\n" \
"v_mfma_f32_16x16x32_bf16 v[132:135], a[236:239], a[156:159], v[132:135]\n" \
"v_add_f32_dpp v235, v235, v235 quad_perm:[2,3,0,1] row_mask:0xf bank_mask:0xf\n" \
"s_sub_u32 s63, %[m0b], s63\n" \
"s_nop 0\n" \
"s_add_u32 m0, %[m0b], 16384\n" \
"s_nop 0\n" \
"s_nop 0\n" \
"buffer_load_dwordx4 v214, %[srdt], s49 offen offset:3072 lds\n" \
"v_mfma_f32_16x16x32_bf16 v[136:139], a[228:231], a[168:171], v[136:139]\n" \
"v_lshl_add_u32 v234, v234, 2, s63\n" \
"v_mfma_f32_16x16x32_bf16 v[136:139], a[236:239], a[172:175], v[136:139]\n" \
"v_add_f32_dpp v235, v235, v235 row_ror:8 row_mask:0xf bank_mask:0xf\n" \
"v_sub_u32_e32 v230, 0x7ff, v230\n" \
"v_mfma_f32_16x16x32_bf16 v[140:143], a[228:231], a[184:187], v[140:143]\n" \
"ds_read_b64_tr_b16 a[220:221], v215 offset:49664\n" \
"v_mov_b32_e64 v216, v235\n" \
"v_mfma_f32_16x16x32_bf16 v[140:143], a[236:239], a[188:191], v[140:143]\n" \
"ds_read_b64_tr_b16 a[222:223], v215 offset:51712\n" \
"v_add_u32_e32 v234, 0xcc04, v234\n" \
"v_mfma_f32_16x16x32_bf16 v[176:179], a[212:215], a[8:11], v[176:179]\n" \
"v_permlane16_swap_b32_e32 v235, v216\n" \
"s_nop 0\n" \
"v_mfma_f32_16x16x32_bf16 v[176:179], a[216:219], a[12:15], v[176:179]\n" \
"v_mfma_f32_16x16x32_bf16 v[180:183], a[212:215], a[24:27], v[180:183]\n" \
"v_add_f32_e64 v235, v235, v216\n" \
"v_mfma_f32_16x16x32_bf16 v[180:183], a[216:219], a[28:31], v[180:183]\n" \
"v_mov_b32_e64 v216, v235\n" \
"v_mfma_f32_16x16x32_bf16 v[184:187], a[212:215], a[40:43], v[184:187]\n" \
"s_nop 0\n" \
"s_nop 0\n" \
"v_mfma_f32_16x16x32_bf16 v[184:187], a[216:219], a[44:47], v[184:187]\n" \
"v_cmp_u_f32_e64 s[44:45], v128, v132\n" \
"v_permlane32_swap_b32_e32 v235, v216\n" \
"s_nop 0\n" \
"v_mfma_f32_16x16x32_bf16 v[188:191], a[212:215], a[56:59], v[188:191]\n" \
"v_add_f32_e64 v235, v235, v216\n" \
"v_mfma_f32_16x16x32_bf16 v[188:191], a[216:219], a[60:63], v[188:191]\n" \
"v_cmp_u_f32_e64 s[56:57], v136, v140\n" \
"s_or_b64 s[56:57], s[56:57], s[44:45]\n" \
"s_cbranch_scc1 Lsp7_%=\n" \
"Lbk7_%=:\n" \
"s_waitcnt vmcnt(63) lgkmcnt(2)\n" \
"s_nop 0\n" \
"v_mfma_f32_16x16x32_bf16 v[144:147], a[232:235], a[128:131], v[144:147]\n" \
"ds_read_b64_tr_b16 a[212:213], v215 offset:53760\n" \
"v_ashrrev_i32_e64 v216, 31, v235\n" \
"v_mfma_f32_16x16x32_bf16 v[144:147], a[196:199], a[132:135], v[144:147]\n" \
"ds_read_b64_tr_b16 a[214:215], v215 offset:55808\n" \
"v_or_b32_e32 v216, 0x80000000, v216\n" \
"v_mfma_f32_16x16x32_bf16 v[148:151], a[232:235], a[144:147], v[148:151]\n" \
"ds_read_b64_tr_b16 a[208:209], v215 offset:57856\n" \
"v_xor_b32_e64 v235, v235, v216\n" \
"v_mfma_f32_16x16x32_bf16 v[148:151], a[196:199], a[148:151], v[148:151]\n" \
"ds_read_b64_tr_b16 a[210:211], v215 offset:59904\n" \
"s_lshl_b32 s61, %[wid], 10\n" \
"v_lshrrev_b32_e32 v231, 3, v224\n" \
"v_add_u32_e64 v231, s61, v231\n" \
"v_and_b32_e32 v235, 0xfffff800, v235\n" \
"s_add_u32 m0, %[m0b], 32768\n" \
"s_cmp_gt_u32 s48, 5\n" \
"s_cselect_b32 s49, 5, 6\n" \
"s_lshl_b32 s49, s49, 18\n" \
"s_or_b32 s49, s49, s59\n" \
"buffer_load_dwordx4 %[wo0], %[srdt], s49 offen offset:0 lds\n" \
"v_mfma_f32_16x16x32_bf16 v[152:155], a[232:235], a[160:163], v[152:155]\n" \
"ds_read_b64_tr_b16 a[216:217], v215 offset:61952\n" \
"v_readlane_b32 s44, %[tlo], 20\n" \
"v_readlane_b32 s45, %[thi], 20\n" \
"v_lshrrev_b32_e64 v236, 1, v231\n" \
"v_max_u32_e32 v235, 0x800, v235\n" \
"v_mfma_f32_16x16x32_bf16 v[152:155], a[196:199], a[164:167], v[152:155]\n" \
"ds_read_b64_tr_b16 a[218:219], v215 offset:64000\n" \
"v_or_b32_e64 v235, v235, v230\n" \
"v_mfma_f32_16x16x32_bf16 v[156:159], a[232:235], a[176:179], v[156:159]\n" \
"ds_read_b64_tr_b16 a[224:225], v227 offset:49664\n" \
"ds_write_b32 v234, v235 offset:65532\n" \
"v_mfma_f32_16x16x32_bf16 v[156:159], a[196:199], a[180:183], v[156:159]\n" \
"ds_read_b64_tr_b16 a[226:227], v227 offset:51712\n" \
"v_mfma_f32_16x16x32_bf16 v[144:147], a[200:203], a[136:139], v[144:147]\n" \
"ds_read_b64_tr_b16 a[228:229], v227 offset:53760\n" \
"v_mfma_f32_16x16x32_bf16 v[144:147], a[204:207], a[140:143], v[144:147]\n" \
"ds_read_b64_tr_b16 a[230:231], v227 offset:55808\n" \
"v_mfma_f32_16x16x32_bf16 v[148:151], a[200:203], a[152:155], v[148:151]\n" \
"s_mov_b32 s61, 262144\n" \
"buffer_load_dwordx4 v[192:195], v231, %[srdz], s61 offen offset:0 sc1\n" \
"v_mfma_f32_16x16x32_bf16 v[148:151], a[204:207], a[156:159], v[148:151]\n" \
"v_mfma_f32_16x16x32_bf16 v[152:155], a[200:203], a[168:171], v[152:155]\n" \
"buffer_load_dwordx4 v212, %[srdt], s49 offen offset:1024 lds\n" \
"v_mfma_f32_16x16x32_bf16 v[152:155], a[204:207], a[172:175], v[152:155]\n" \
"v_mfma_f32_16x16x32_bf16 v[156:159], a[200:203], a[184:187], v[156:159]\n" \
"ds_read_b64_tr_b16 a[236:237], v227 offset:57856\n" \
"v_mfma_f32_16x16x32_bf16 v[156:159], a[204:207], a[188:191], v[156:159]\n" \
"s_waitcnt vmcnt(63) lgkmcnt(6)\n" \
"s_nop 0\n" \
"v_mfma_f32_16x16x32_bf16 v[160:163], a[220:223], a[128:131], v[160:163]\n" \
"ds_read_b64_tr_b16 a[238:239], v227 offset:59904\n" \
"v_mfma_f32_16x16x32_bf16 v[160:163], a[212:215], a[132:135], v[160:163]\n" \
"ds_read_b64_tr_b16 a[232:233], v227 offset:61952\n" \
"v_mfma_f32_16x16x32_bf16 v[164:167], a[220:223], a[144:147], v[164:167]\n" \
"ds_read_b64_tr_b16 a[234:235], v227 offset:64000\n" \
"s_mov_b32 s61, 266240\n" \
"buffer_load_dwordx4 v[196:199], v231, %[srdz], s61 offen offset:0 sc1\n" \
"v_mfma_f32_16x16x32_bf16 v[164:167], a[212:215], a[148:151], v[164:167]\n" \
"v_mfma_f32_16x16x32_bf16 v[168:171], a[220:223], a[160:163], v[168:171]\n" \
"v_cvt_pk_bf16_f32 v128, v128, v129\n" \
"buffer_load_dwordx4 v213, %[srdt], s49 offen offset:2048 lds\n" \
"v_mfma_f32_16x16x32_bf16 v[168:171], a[212:215], a[164:167], v[168:171]\n" \
"v_cvt_pk_bf16_f32 v129, v130, v131\n" \
"v_mfma_f32_16x16x32_bf16 v[172:175], a[220:223], a[176:179], v[172:175]\n" \
"v_cvt_pk_bf16_f32 v130, v144, v145\n" \
"v_mfma_f32_16x16x32_bf16 v[172:175], a[212:215], a[180:183], v[172:175]\n" \
"v_cvt_pk_bf16_f32 v131, v146, v147\n" \
"v_mfma_f32_16x16x32_bf16 v[160:163], a[208:211], a[136:139], v[160:163]\n" \
"s_add_u32 s55, s46, 0\n" \
"s_nop 0\n" \
"buffer_store_dwordx4 v[128:131], %[vxs], %[srdz], s55 offen offset:0 sc1\n" \
"v_lshrrev_b32_e32 v230, 5, v224\n" \
"s_lshl_b32 s63, %[wid], 12\n" \
"v_mfma_f32_16x16x32_bf16 v[160:163], a[216:219], a[140:143], v[160:163]\n" \
"v_and_b32_e32 v230, 28, v230\n" \
"s_sub_u32 s63, %[m0b], s63\n" \
"v_mfma_f32_16x16x32_bf16 v[164:167], a[208:211], a[152:155], v[164:167]\n" \
"v_cvt_pk_bf16_f32 v132, v132, v133\n" \
"s_mov_b32 s61, 270336\n" \
"buffer_load_dwordx4 v[200:203], v231, %[srdz], s61 offen offset:0 sc1\n" \
"s_add_u32 s63, s63, 117760\n" \
"v_add_u32_e64 v230, s63, v230\n" \
"v_mfma_f32_16x16x32_bf16 v[164:167], a[216:219], a[156:159], v[164:167]\n" \
"v_cvt_pk_bf16_f32 v133, v134, v135\n" \
"v_mfma_f32_16x16x32_bf16 v[168:171], a[208:211], a[168:171], v[168:171]\n" \
"s_waitcnt vmcnt(14) lgkmcnt(0)\n" \
"s_barrier\n" \
"v_mfma_f32_16x16x32_bf16 v[168:171], a[216:219], a[172:175], v[168:171]\n" \
"ds_read_b64_tr_b16 a[196:197], v215 offset:0\n" \
"v_cvt_pk_bf16_f32 v134, v148, v149\n" \
"ds_read_b32 v235, v230 offset:0\n" \
"v_readlane_b32 s63, %[thi], 34\n" \
"v_mfma_f32_16x16x32_bf16 v[172:175], a[208:211], a[184:187], v[172:175]\n" \
"ds_read_b64_tr_b16 a[198:199], v215 offset:2048\n" \
"v_cvt_pk_bf16_f32 v135, v150, v151\n" \
"v_mfma_f32_16x16x32_bf16 v[172:175], a[216:219], a[188:191], v[172:175]\n" \
"s_waitcnt vmcnt(63) lgkmcnt(7)\n" \
"s_nop 0\n" \
"v_mfma_f32_16x16x32_bf16 v[176:179], a[224:227], a[128:131], v[176:179]\n" \
"ds_read_b64_tr_b16 a[200:201], v215 offset:4096\n" \
"v_cvt_pk_bf16_f32 v136, v136, v137\n" \
"v_mfma_f32_16x16x32_bf16 v[176:179], a[228:231], a[132:135], v[176:179]\n" \
"ds_read_b64_tr_b16 a[202:203], v215 offset:6144\n" \
"v_cvt_pk_bf16_f32 v137, v138, v139\n" \
"v_mfma_f32_16x16x32_bf16 v[180:183], a[224:227], a[144:147], v[180:183]\n" \
"ds_read_b64_tr_b16 a[204:205], v215 offset:8192\n" \
"s_add_u32 s55, s46, 304128\n" \
"buffer_store_dwordx4 v[132:135], %[vxs], %[srdz], s55 offen offset:0 sc1\n" \
"v_mfma_f32_16x16x32_bf16 v[180:183], a[228:231], a[148:151], v[180:183]\n" \
"ds_read_b64_tr_b16 a[206:207], v215 offset:10240\n" \
"v_cvt_pk_bf16_f32 v138, v152, v153\n" \
"v_mfma_f32_16x16x32_bf16 v[184:187], a[224:227], a[160:163], v[184:187]\n" \
"ds_read_b64_tr_b16 a[220:221], v215 offset:12288\n" \
"v_cvt_pk_bf16_f32 v139, v154, v155\n" \
"s_mov_b32 s61, 274432\n" \
"buffer_load_dwordx4 v[204:207], v231, %[srdz], s61 offen offset:0 sc1\n" \
"s_waitcnt lgkmcnt(6)\n" \
"s_nop 0\n" \
"v_min_u32_dpp v234, v235, v235 quad_perm:[1,0,3,2] row_mask:0xf bank_mask:0xf\n" \
"v_max_u32_dpp v235, v235, v235 quad_perm:[1,0,3,2] row_mask:0xf bank_mask:0xf\n" \
"v_mfma_f32_16x16x32_bf16 v[184:187], a[228:231], a[164:167], v[184:187]\n" \
"ds_read_b64_tr_b16 a[222:223], v215 offset:14336\n" \
"v_mfma_f32_16x16x32_bf16 v[188:191], a[224:227], a[176:179], v[188:191]\n" \
"ds_read_b64_tr_b16 a[212:213], v227 offset:0\n" \
"v_cvt_pk_bf16_f32 v140, v140, v141\n" \
"v_min_u32_dpp v216, v235, v235 quad_perm:[2,3,0,1] row_mask:0xf bank_mask:0xf\n" \
"v_max_u32_dpp v230, v234, v234 quad_perm:[2,3,0,1] row_mask:0xf bank_mask:0xf\n" \
"buffer_load_dwordx4 v214, %[srdt], s49 offen offset:3072 lds\n" \
"v_mfma_f32_16x16x32_bf16 v[188:191], a[228:231], a[180:183], v[188:191]\n" \
"ds_read_b64_tr_b16 a[214:215], v227 offset:2048\n" \
"v_cvt_pk_bf16_f32 v141, v142, v143\n" \
"v_max_u32_dpp v235, v235, v235 quad_perm:[2,3,0,1] row_mask:0xf bank_mask:0xf\n" \
"v_max_u32_e64 v234, v216, v230\n" \
"v_mfma_f32_16x16x32_bf16 v[176:179], a[236:239], a[136:139], v[176:179]\n" \
"ds_read_b64_tr_b16 a[208:209], v227 offset:4096\n" \
"s_add_u32 s55, s46, 608256\n" \
"buffer_store_dwordx4 v[136:139], %[vxs], %[srdz], s55 offen offset:0 sc1\n" \
"v_mfma_f32_16x16x32_bf16 v[176:179], a[232:235], a[140:143], v[176:179]\n" \
"ds_read_b64_tr_b16 a[210:211], v227 offset:6144\n" \
"v_cvt_pk_bf16_f32 v142, v156, v157\n" \
"v_min_u32_dpp v216, v235, v235 row_half_mirror row_mask:0xf bank_mask:0xf\n" \
"v_max_u32_dpp v230, v234, v234 row_half_mirror row_mask:0xf bank_mask:0xf\n" \
"v_mfma_f32_16x16x32_bf16 v[180:183], a[236:239], a[152:155], v[180:183]\n" \
"v_cvt_pk_bf16_f32 v143, v158, v159\n" \
"v_max_u32_dpp v235, v235, v235 row_half_mirror row_mask:0xf bank_mask:0xf\n" \
"v_max_u32_e64 v234, v216, v230\n" \
"v_mfma_f32_16x16x32_bf16 v[180:183], a[232:235], a[156:159], v[180:183]\n" \
"s_add_u32 s55, s46, 912384\n" \
"buffer_store_dwordx4 v[140:143], %[vxs], %[srdz], s55 offen offset:0 sc1\n" \
"v_lshlrev_b32_e64 v230, 4, v224\n" \
"v_mfma_f32_16x16x32_bf16 v[184:187], a[236:239], a[168:171], v[184:187]\n" \
"v_mfma_f32_16x16x32_bf16 v[184:187], a[232:235], a[172:175], v[184:187]\n" \
"global_load_dwordx2 v[232:233], v236, s[44:45]\n" \
"v_mfma_f32_16x16x32_bf16 v[188:191], a[236:239], a[184:187], v[188:191]\n" \
"ds_read_b64_tr_b16 a[216:217], v227 offset:8192\n" \
"v_mfma_f32_16x16x32_bf16 v[188:191], a[232:235], a[188:191], v[188:191]\n" \
"s_waitcnt vmcnt(63) lgkmcnt(5)\n" \
"s_nop 0\n" \
"v_mfma_f32_16x16x32_bf16 v[4:7], a[196:199], a[64:67], v[4:7]\n" \
"ds_read_b64_tr_b16 a[218:219], v227 offset:10240\n" \
"s_mov_b64 exec, 3\n" \
"s_nop 0\n" \
"buffer_atomic_or_x2 v[234:235], v230, %[srdz], s63 offen sc1\n" \
"s_mov_b64 exec, -1\n" \
"s_nop 0\n" \
"v_mfma_f32_16x16x32_bf16 v[4:7], a[200:203], a[68:71], v[4:7]\n" \
"ds_read_b64_tr_b16 a[224:225], v227 offset:12288\n" \
"v_mfma_f32_16x16x32_bf16 v[36:39], a[196:199], a[80:83], v[36:39]\n" \
"ds_read_b64_tr_b16 a[226:227], v227 offset:14336\n" \
"v_mfma_f32_16x16x32_bf16 v[36:39], a[200:203], a[84:87], v[36:39]\n" \
"v_mfma_f32_16x16x32_bf16 v[68:71], a[196:199], a[96:99], v[68:71]\n" \
"v_cvt_pk_bf16_f32 v160, v160, v161\n" \
"v_mfma_f32_16x16x32_bf16 v[68:71], a[200:203], a[100:103], v[68:71]\n" \
"v_cvt_pk_bf16_f32 v161, v162, v163\n" \
"v_mfma_f32_16x16x32_bf16 v[100:103], a[196:199], a[112:115], v[100:103]\n" \
"ds_read_b64_tr_b16 a[228:229], v215 offset:512\n" \
"v_cvt_pk_bf16_f32 v162, v176, v177\n" \
"v_mfma_f32_16x16x32_bf16 v[100:103], a[200:203], a[116:119], v[100:103]\n" \
"ds_read_b64_tr_b16 a[230:231], v215 offset:2560\n" \
"v_cvt_pk_bf16_f32 v163, v178, v179\n" \
"v_mfma_f32_16x16x32_bf16 v[4:7], a[204:207], a[72:75], v[4:7]\n" \
"ds_read_b64_tr_b16 a[236:237], v215 offset:4608\n" \
"v_mfma_f32_16x16x32_bf16 v[4:7], a[220:223], a[76:79], v[4:7]\n" \
"ds_read_b64_tr_b16 a[238:239], v215 offset:6656\n" \
"v_mfma_f32_16x16x32_bf16 v[36:39], a[204:207], a[88:91], v[36:39]\n" \
"s_add_u32 s55, s46, 4096\n" \
"buffer_store_dwordx4 v[160:163], %[vxs], %[srdz], s55 offen offset:0 sc1\n" \
"v_mfma_f32_16x16x32_bf16 v[36:39], a[220:223], a[92:95], v[36:39]\n" \
"v_cvt_pk_bf16_f32 v164, v164, v165\n" \
"v_cvt_pk_bf16_f32 v165, v166, v167\n" \
"v_mfma_f32_16x16x32_bf16 v[68:71], a[204:207], a[104:107], v[68:71]\n" \
"v_cvt_pk_bf16_f32 v166, v180, v181\n" \
"s_add_u32 m0, %[m0b], 49152\n" \
"s_cmp_gt_u32 s48, 6\n" \
"s_cselect_b32 s49, 6, 7\n" \
"s_lshl_b32 s49, s49, 18\n" \
"s_or_b32 s49, s49, s59\n" \
"buffer_load_dwordx4 %[wo0], %[srdt], s49 offen offset:0 lds\n" \
"v_mfma_f32_16x16x32_bf16 v[68:71], a[220:223], a[108:111], v[68:71]\n" \
"v_cvt_pk_bf16_f32 v167, v182, v183\n" \
"v_mfma_f32_16x16x32_bf16 v[100:103], a[204:207], a[120:123], v[100:103]\n" \
"ds_read_b64_tr_b16 a[232:233], v215 offset:8704\n" \
"v_mfma_f32_16x16x32_bf16 v[100:103], a[220:223], a[124:127], v[100:103]\n" \
"s_waitcnt vmcnt(63) lgkmcnt(5)\n" \
"s_nop 0\n" \
"v_mfma_f32_16x16x32_bf16 v[12:15], a[212:215], a[64:67], v[12:15]\n" \
"ds_read_b64_tr_b16 a[234:235], v215 offset:10752\n" \
"v_cvt_pk_bf16_f32 v168, v168, v169\n" \
"v_mfma_f32_16x16x32_bf16 v[12:15], a[208:211], a[68:71], v[12:15]\n" \
"ds_read_b64_tr_b16 a[196:197], v215 offset:12800\n" \
"s_add_u32 s55, s46, 308224\n" \
"buffer_store_dwordx4 v[164:167], %[vxs], %[srdz], s55 offen offset:0 sc1\n" \
"v_mfma_f32_16x16x32_bf16 v[44:47], a[212:215], a[80:83], v[44:47]\n" \
"ds_read_b64_tr_b16 a[198:199], v215 offset:14848\n" \
"v_cvt_pk_bf16_f32 v169, v170, v171\n" \
"v_mfma_f32_16x16x32_bf16 v[44:47], a[208:211], a[84:87], v[44:47]\n" \
"v_cvt_pk_bf16_f32 v170, v184, v185\n" \
"v_cvt_pk_bf16_f32 v171, v186, v187\n" \
"v_mfma_f32_16x16x32_bf16 v[76:79], a[212:215], a[96:99], v[76:79]\n" \
"s_add_u32 s55, s46, 612352\n" \
"buffer_store_dwordx4 v[168:171], %[vxs], %[srdz], s55 offen offset:0 sc1\n" \
"v_mfma_f32_16x16x32_bf16 v[76:79], a[208:211], a[100:103], v[76:79]\n" \
"v_mfma_f32_16x16x32_bf16 v[108:111], a[212:215], a[112:115], v[108:111]\n" \
"ds_read_b64_tr_b16 a[200:201], v227 offset:512\n" \
"v_cvt_pk_bf16_f32 v172, v172, v173\n" \
"buffer_load_dwordx4 v212, %[srdt], s49 offen offset:1024 lds\n" \
"v_mfma_f32_16x16x32_bf16 v[108:111], a[208:211], a[116:119], v[108:111]\n" \
"ds_read_b64_tr_b16 a[202:203], v227 offset:2560\n" \
"v_cvt_pk_bf16_f32 v173, v174, v175\n" \
"v_mfma_f32_16x16x32_bf16 v[12:15], a[216:219], a[72:75], v[12:15]\n" \
"ds_read_b64_tr_b16 a[204:205], v227 offset:4608\n" \
"v_cvt_pk_bf16_f32 v174, v188, v189\n" \
"v_mfma_f32_16x16x32_bf16 v[12:15], a[224:227], a[76:79], v[12:15]\n" \
"ds_read_b64_tr_b16 a[206:207], v227 offset:6656\n" \
"v_cvt_pk_bf16_f32 v175, v190, v191\n" \
"v_mfma_f32_16x16x32_bf16 v[44:47], a[216:219], a[88:91], v[44:47]\n" \
"s_add_u32 s55, s46, 916480\n" \
"buffer_store_dwordx4 v[172:175], %[vxs], %[srdz], s55 offen offset:0 sc1\n" \
"v_mfma_f32_16x16x32_bf16 v[44:47], a[224:227], a[92:95], v[44:47]\n" \
"v_mfma_f32_16x16x32_bf16 v[76:79], a[216:219], a[104:107], v[76:79]\n" \
"buffer_load_dwordx4 v213, %[srdt], s49 offen offset:2048 lds\n" \
"v_mfma_f32_16x16x32_bf16 v[76:79], a[224:227], a[108:111], v[76:79]\n" \
"v_mfma_f32_16x16x32_bf16 v[108:111], a[216:219], a[120:123], v[108:111]\n" \
"ds_read_b64_tr_b16 a[220:221], v227 offset:8704\n" \
"v_mfma_f32_16x16x32_bf16 v[108:111], a[224:227], a[124:127], v[108:111]\n" \
"s_waitcnt vmcnt(63) lgkmcnt(5)\n" \
"s_nop 0\n" \
"v_mfma_f32_16x16x32_bf16 v[20:23], a[228:231], a[64:67], v[20:23]\n" \
"ds_read_b64_tr_b16 a[222:223], v227 offset:10752\n" \
"v_mfma_f32_16x16x32_bf16 v[20:23], a[236:239], a[68:71], v[20:23]\n" \
"ds_read_b64_tr_b16 a[212:213], v227 offset:12800\n" \
"v_mfma_f32_16x16x32_bf16 v[52:55], a[228:231], a[80:83], v[52:55]\n" \
"ds_read_b64_tr_b16 a[214:215], v227 offset:14848\n" \
"v_mfma_f32_16x16x32_bf16 v[52:55], a[236:239], a[84:87], v[52:55]\n" \
"global_load_dword v237, %[vct], %[pct]\n" \
"v_mfma_f32_16x16x32_bf16 v[84:87], a[228:231], a[96:99], v[84:87]\n" \
"v_mfma_f32_16x16x32_bf16 v[84:87], a[236:239], a[100:103], v[84:87]\n" \
"v_xor_b32_e32 v218, 0x80, %[vdn]\n" \
"s_add_u32 m0, %[m0b], 114688\n" \
"s_mov_b32 s61, 16384\n" \
"buffer_load_dwordx4 v218, s[64:67], s61 offen offset:0 lds\n" \
"v_mfma_f32_16x16x32_bf16 v[116:119], a[228:231], a[112:115], v[116:119]\n" \
"v_mfma_f32_16x16x32_bf16 v[116:119], a[236:239], a[116:119], v[116:119]\n" \
"s_add_u32 m0, %[m0b], 49152\n" \
"s_nop 0\n" \
"s_nop 0\n" \
"buffer_load_dwordx4 v214, %[srdt], s49 offen offset:3072 lds\n" \
"v_mfma_f32_16x16x32_bf16 v[20:23], a[232:235], a[72:75], v[20:23]\n" \
"v_mfma_f32_16x16x32_bf16 v[20:23], a[196:199], a[76:79], v[20:23]\n" \
"v_mfma_f32_16x16x32_bf16 v[52:55], a[232:235], a[88:91], v[52:55]\n" \
"v_mfma_f32_16x16x32_bf16 v[52:55], a[196:199], a[92:95], v[52:55]\n" \
"v_mfma_f32_16x16x32_bf16 v[84:87], a[232:235], a[104:107], v[84:87]\n" \
"s_waitcnt vmcnt(24) lgkmcnt(0)\n" \
"s_barrier\n" \
"v_mfma_f32_16x16x32_bf16 v[84:87], a[196:199], a[108:111], v[84:87]\n" \
"ds_read_b64_tr_b16 a[208:209], v215 offset:16384\n" \
"v_mfma_f32_16x16x32_bf16 v[116:119], a[232:235], a[120:123], v[116:119]\n" \
"ds_read_b64_tr_b16 a[210:211], v215 offset:18432\n" \
"v_mfma_f32_16x16x32_bf16 v[116:119], a[196:199], a[124:127], v[116:119]\n" \
"s_waitcnt vmcnt(63) lgkmcnt(2)\n" \
"s_nop 0\n" \
"v_mfma_f32_16x16x32_bf16 v[28:31], a[200:203], a[64:67], v[28:31]\n" \
"ds_read_b64_tr_b16 a[216:217], v215 offset:20480\n" \
"v_mfma_f32_16x16x32_bf16 v[28:31], a[204:207], a[68:71], v[28:31]\n" \
"ds_read_b64_tr_b16 a[218:219], v215 offset:22528\n" \
"v_mfma_f32_16x16x32_bf16 v[60:63], a[200:203], a[80:83], v[60:63]\n" \
"ds_read_b64_tr_b16 a[224:225], v215 offset:24576\n" \
"v_mfma_f32_16x16x32_bf16 v[60:63], a[204:207], a[84:87], v[60:63]\n" \
"ds_read_b64_tr_b16 a[226:227], v215 offset:26624\n" \
"v_mfma_f32_16x16x32_bf16 v[92:95], a[200:203], a[96:99], v[92:95]\n" \
"ds_read_b64_tr_b16 a[228:229], v215 offset:28672\n" \
"v_xor_b32_e32 v218, 0xc0, %[vdn]\n" \
"s_add_u32 m0, %[m0b], 115712\n" \
"s_mov_b32 s61, 20480\n" \
"buffer_load_dwordx4 v218, s[64:67], s61 offen offset:0 lds\n" \
"v_mfma_f32_16x16x32_bf16 v[92:95], a[204:207], a[100:103], v[92:95]\n" \
"ds_read_b64_tr_b16 a[230:231], v215 offset:30720\n" \
"v_mfma_f32_16x16x32_bf16 v[124:127], a[200:203], a[112:115], v[124:127]\n" \
"ds_read_b64_tr_b16 a[236:237], v227 offset:16384\n" \
"v_mfma_f32_16x16x32_bf16 v[124:127], a[204:207], a[116:119], v[124:127]\n" \
"ds_read_b64_tr_b16 a[238:239], v227 offset:18432\n" \
"v_mfma_f32_16x16x32_bf16 v[28:31], a[220:223], a[72:75], v[28:31]\n" \
"ds_read_b64_tr_b16 a[232:233], v227 offset:20480\n" \
"v_mfma_f32_16x16x32_bf16 v[28:31], a[212:215], a[76:79], v[28:31]\n" \
"ds_read_b64_tr_b16 a[234:235], v227 offset:22528\n" \
"v_mfma_f32_16x16x32_bf16 v[60:63], a[220:223], a[88:91], v[60:63]\n" \
"v_mfma_f32_16x16x32_bf16 v[60:63], a[212:215], a[92:95], v[60:63]\n" \
"v_mfma_f32_16x16x32_bf16 v[92:95], a[220:223], a[104:107], v[92:95]\n" \
"v_mfma_f32_16x16x32_bf16 v[92:95], a[212:215], a[108:111], v[92:95]\n" \
"v_mfma_f32_16x16x32_bf16 v[124:127], a[220:223], a[120:123], v[124:127]\n" \
"ds_read_b64_tr_b16 a[196:197], v227 offset:24576\n" \
"v_mfma_f32_16x16x32_bf16 v[124:127], a[212:215], a[124:127], v[124:127]\n" \
"s_waitcnt vmcnt(63) lgkmcnt(5)\n" \
"s_nop 0\n" \
"v_mfma_f32_16x16x32_bf16 v[4:7], a[208:211], v[0:3], v[4:7]\n" \
"ds_read_b64_tr_b16 a[198:199], v227 offset:26624\n" \
"v_mfma_f32_16x16x32_bf16 v[4:7], a[216:219], v[8:11], v[4:7]\n" \
"ds_read_b64_tr_b16 a[200:201], v227 offset:28672\n" \
"v_mfma_f32_16x16x32_bf16 v[36:39], a[208:211], v[32:35], v[36:39]\n" \
"ds_read_b64_tr_b16 a[202:203], v227 offset:30720\n" \
"v_mfma_f32_16x16x32_bf16 v[36:39], a[216:219], v[40:43], v[36:39]\n" \
"s_waitcnt vmcnt(12)\n" \
"s_nop 0\n" \
"v_and_or_b32 v236, v192, v193, 0\n" \
"v_bitop3_b32 v236, v236, v194, v195 bitop3:0x80\n" \
"s_add_u32 m0, %[m0b], 116736\n" \
"s_mov_b32 s61, 24576\n" \
"buffer_load_dwordx4 %[vdn], s[64:67], s61 offen offset:0 lds\n" \
"v_mfma_f32_16x16x32_bf16 v[68:71], a[208:211], v[64:67], v[68:71]\n" \
".if . - adaln_body != 57344\n" \
".error \"size model\"\n" \
".endif\n" \
"Lk13_%=:\n" \
"s_cbranch_execz Lk14_%=\n" \
"s_nop 0\n" \
"v_bitop3_b32 v236, v236, v196, v197 bitop3:0x80\n" \
"v_mfma_f32_16x16x32_bf16 v[68:71], a[216:219], v[72:75], v[68:71]\n" \
"v_bitop3_b32 v236, v236, v198, v199 bitop3:0x80\n" \
"v_mfma_f32_16x16x32_bf16 v[100:103], a[208:211], v[96:99], v[100:103]\n" \
"v_bitop3_b32 v236, v236, v200, v201 bitop3:0x80\n" \
"v_mfma_f32_16x16x32_bf16 v[100:103], a[216:219], v[104:107], v[100:103]\n" \
"v_bitop3_b32 v236, v236, v202, v203 bitop3:0x80\n" \
"v_mfma_f32_16x16x32_bf16 v[4:7], a[224:227], v[16:19], v[4:7]\n" \
"v_bitop3_b32 v236, v236, v204, v205 bitop3:0x80\n" \
"v_mfma_f32_16x16x32_bf16 v[4:7], a[228:231], v[24:27], v[4:7]\n" \
"v_bitop3_b32 v236, v236, v206, v207 bitop3:0x80\n" \
"v_mfma_f32_16x16x32_bf16 v[36:39], a[224:227], v[48:51], v[36:39]\n" \
"s_mov_b32 s54, 0x07060302\n" \
"v_and_b32_e64 v236, 1, v236\n" \
"v_mfma_f32_16x16x32_bf16 v[36:39], a[228:231], v[56:59], v[36:39]\n" \
"v_and_b32_e32 v231, 0x170, v231\n" \
"v_mfma_f32_16x16x32_bf16 v[68:71], a[224:227], v[80:83], v[68:71]\n" \
"s_waitcnt vmcnt(16) lgkmcnt(15)\n" \
"s_barrier\n" \
"v_mfma_f32_16x16x32_bf16 v[68:71], a[228:231], v[88:91], v[68:71]\n" \
"ds_read_b64_tr_b16 a[204:205], v215 offset:32768\n" \
"v_cmp_eq_u32_e64 s[56:57], 0, v236\n" \
"v_mfma_f32_16x16x32_bf16 v[100:103], a[224:227], v[112:115], v[100:103]\n" \
"ds_read_b64_tr_b16 a[206:207], v215 offset:34816\n" \
"v_lshrrev_b32_e64 v236, 7, v224\n" \
"v_mfma_f32_16x16x32_bf16 v[100:103], a[228:231], v[120:123], v[100:103]\n" \
"s_waitcnt vmcnt(13) lgkmcnt(2)\n" \
"s_nop 0\n" \
"v_mfma_f32_16x16x32_bf16 v[12:15], a[236:239], v[0:3], v[12:15]\n" \
"ds_read_b64_tr_b16 a[220:221], v215 offset:36864\n" \
"v_and_or_b32 v231, v236, 8, v231\n" \
"v_mfma_f32_16x16x32_bf16 v[12:15], a[232:235], v[8:11], v[12:15]\n" \
"ds_read_b64_tr_b16 a[222:223], v215 offset:38912\n" \
"v_lshrrev_b32_e64 v236, 12, v224\n" \
"v_mfma_f32_16x16x32_bf16 v[44:47], a[236:239], v[32:35], v[44:47]\n" \
"ds_read_b64_tr_b16 a[212:213], v215 offset:40960\n" \
"v_lshl_or_b32 v231, v236, 7, v231\n" \
"v_mfma_f32_16x16x32_bf16 v[44:47], a[232:235], v[40:43], v[44:47]\n" \
"ds_read_b64_tr_b16 a[214:215], v215 offset:43008\n" \
"s_mul_i32 s61, %[wid], 3584\n" \
"s_sub_u32 s61, %[m0b], s61\n" \
"v_add_u32_e32 v231, s61, v231\n" \
"v_mfma_f32_16x16x32_bf16 v[76:79], a[236:239], v[64:67], v[76:79]\n" \
"ds_read_b64_tr_b16 a[208:209], v215 offset:45056\n" \
"s_cmp_lg_u64 s[56:57], 0\n" \
"s_cbranch_scc1 Lpars_%=\n" \
"Lparb_%=:\n" \
"v_mov_b32_e64 v234, 0\n" \
"v_mfma_f32_16x16x32_bf16 v[76:79], a[232:235], v[72:75], v[76:79]\n" \
"ds_read_b64_tr_b16 a[210:211], v215 offset:47104\n" \
"v_lshlrev_b32_e64 v184, 16, v232\n" \
"v_mfma_f32_16x16x32_bf16 v[108:111], a[236:239], v[96:99], v[108:111]\n" \
"ds_read_b64_tr_b16 a[216:217], v227 offset:32768\n" \
"v_and_b32_e32 v185, 0xffff0000, v232\n" \
"v_mfma_f32_16x16x32_bf16 v[108:111], a[232:235], v[104:107], v[108:111]\n" \
"ds_read_b64_tr_b16 a[218:219], v227 offset:34816\n" \
"v_lshlrev_b32_e32 v186, 16, v233\n" \
"v_xor_b32_e32 v218, 64, %[vdn]\n" \
"s_add_u32 m0, %[m0b], 117760\n" \
"s_mov_b32 s61, 28672\n" \
"buffer_load_dwordx4 v218, s[64:67], s61 offen offset:0 lds\n" \
"v_mfma_f32_16x16x32_bf16 v[12:15], a[196:199], v[16:19], v[12:15]\n" \
"ds_read_b64_tr_b16 a[224:225], v227 offset:36864\n" \
"v_and_b32_e32 v187, 0xffff0000, v233\n" \
"v_mfma_f32_16x16x32_bf16 v[12:15], a[200:203], v[24:27], v[12:15]\n" \
"ds_read_b64_tr_b16 a[226:227], v227 offset:38912\n" \
"v_mul_f32_e64 v188, v200, v184\n" \
"v_mfma_f32_16x16x32_bf16 v[44:47], a[196:199], v[48:51], v[44:47]\n" \
"v_mul_f32_e32 v189, v201, v185\n" \
"v_mul_f32_e32 v190, v202, v186\n" \
"v_mfma_f32_16x16x32_bf16 v[44:47], a[200:203], v[56:59], v[44:47]\n" \
"v_mul_f32_e32 v191, v203, v187\n" \
"v_mul_f32_e32 v235, v196, v184\n" \
"v_mfma_f32_16x16x32_bf16 v[76:79], a[196:199], v[80:83], v[76:79]\n" \
"v_fmac_f32_e32 v235, v197, v185\n" \
"s_nop 0\n" \
"v_mfma_f32_16x16x32_bf16 v[76:79], a[200:203], v[88:91], v[76:79]\n" \
"v_fmac_f32_e32 v235, v198, v186\n" \
"s_nop 0\n" \
"v_mfma_f32_16x16x32_bf16 v[108:111], a[196:199], v[112:115], v[108:111]\n" \
"ds_read_b64_tr_b16 a[228:229], v227 offset:40960\n" \
"v_fmac_f32_e32 v235, v199, v187\n" \
"s_nop 0\n" \
"v_mfma_f32_16x16x32_bf16 v[108:111], a[200:203], v[120:123], v[108:111]\n" \
"s_waitcnt vmcnt(63) lgkmcnt(5)\n" \
"s_nop 0\n" \
"v_mfma_f32_16x16x32_bf16 v[4:7], a[204:207], a[0:3], v[4:7]\n" \
"ds_read_b64_tr_b16 a[230:231], v227 offset:43008\n" \
"v_add_f32_e64 v184, v188, v189\n" \
"v_mfma_f32_16x16x32_bf16 v[4:7], a[220:223], a[4:7], v[4:7]\n" \
"ds_read_b64_tr_b16 a[236:237], v227 offset:45056\n" \
"v_add_f32_e64 v185, v190, v191\n" \
"v_mfma_f32_16x16x32_bf16 v[36:39], a[204:207], a[16:19], v[36:39]\n" \
"ds_read_b64_tr_b16 a[238:239], v227 offset:47104\n" \
"v_add_f32_e64 v184, v184, v185\n" \
"v_mfma_f32_16x16x32_bf16 v[36:39], a[220:223], a[20:23], v[36:39]\n" \
"s_waitcnt vmcnt(7)\n" \
"v_mov_b32_e32 v230, 1\n" \
"buffer_store_dword v230, v225, %[srdz], s62 offen offset:64 sc1\n" \
"v_mfma_f32_16x16x32_bf16 v[68:71], a[204:207], a[32:35], v[68:71]\n" \
"v_lshrrev_b32_e32 v236, 12, v224\n" \
"v_permlane32_swap_b32_e32 v184, v235\n" \
"v_mfma_f32_16x16x32_bf16 v[68:71], a[220:223], a[36:39], v[68:71]\n" \
"v_mov_b32_e64 v230, 1\n" \
"buffer_atomic_add v230, v225, %[srdz], s47 offen sc1\n" \
"v_mfma_f32_16x16x32_bf16 v[100:103], a[204:207], a[48:51], v[100:103]\n" \
"v_add_f32_e64 v184, v184, v235\n" \
"s_mul_i32 s61, %[wid], 4092\n" \
"v_mfma_f32_16x16x32_bf16 v[100:103], a[220:223], a[52:55], v[100:103]\n" \
"v_add_f32_dpp v184, v184, v184 quad_perm:[1,0,3,2] row_mask:0xf bank_mask:0xf\n" \
"s_sub_u32 s61, %[m0b], s61\n" \
"s_nop 0\n" \
"v_mfma_f32_16x16x32_bf16 v[4:7], a[212:215], a[8:11], v[4:7]\n" \
"v_add_f32_dpp v184, v184, v184 quad_perm:[2,3,0,1] row_mask:0xf bank_mask:0xf\n" \
"v_mfma_f32_16x16x32_bf16 v[4:7], a[208:211], a[12:15], v[4:7]\n" \
"v_lshl_add_u32 v236, v236, 4, s61\n" \
"v_mfma_f32_16x16x32_bf16 v[36:39], a[212:215], a[24:27], v[36:39]\n" \
"v_add_f32_dpp v184, v184, v184 row_ror:4 row_mask:0xf bank_mask:0xf\n" \
"v_mfma_f32_16x16x32_bf16 v[36:39], a[208:211], a[28:31], v[36:39]\n" \
"s_nop 0\n" \
"s_nop 0\n" \
"v_mfma_f32_16x16x32_bf16 v[68:71], a[212:215], a[40:43], v[68:71]\n" \
"s_waitcnt vmcnt(5) lgkmcnt(15)\n" \
"s_barrier\n" \
"v_mfma_f32_16x16x32_bf16 v[68:71], a[208:211], a[44:47], v[68:71]\n" \
"ds_read_b64_tr_b16 a[232:233], v215 offset:49152\n" \
"v_add_f32_dpp v184, v184, v184 row_ror:8 row_mask:0xf bank_mask:0xf\n" \
"v_mfma_f32_16x16x32_bf16 v[100:103], a[212:215], a[56:59], v[100:103]\n" \
"ds_read_b64_tr_b16 a[234:235], v215 offset:51200\n" \
"v_mov_b32_e64 v185, v184\n" \
"v_mfma_f32_16x16x32_bf16 v[100:103], a[208:211], a[60:63], v[100:103]\n" \
"s_waitcnt vmcnt(63) lgkmcnt(2)\n" \
"s_nop 0\n" \
"v_mfma_f32_16x16x32_bf16 v[12:15], a[216:219], a[0:3], v[12:15]\n" \
"ds_read_b64_tr_b16 a[196:197], v215 offset:53248\n" \
"s_nop 0\n" \
"s_nop 0\n" \
"v_mfma_f32_16x16x32_bf16 v[12:15], a[224:227], a[4:7], v[12:15]\n" \
"ds_read_b64_tr_b16 a[198:199], v215 offset:55296\n" \
"v_permlane16_swap_b32_e32 v184, v185\n" \
"s_nop 0\n" \
"v_mfma_f32_16x16x32_bf16 v[44:47], a[216:219], a[16:19], v[44:47]\n" \
"ds_read_b64_tr_b16 a[200:201], v215 offset:57344\n" \
"v_add_f32_e64 v184, v184, v185\n" \
"v_mfma_f32_16x16x32_bf16 v[44:47], a[224:227], a[20:23], v[44:47]\n" \
"ds_read_b64_tr_b16 a[202:203], v215 offset:59392\n" \
"v_bfe_u32 v186, v231, 3, 1\n" \
"v_mfma_f32_16x16x32_bf16 v[76:79], a[216:219], a[32:35], v[76:79]\n" \
"ds_read_b64_tr_b16 a[204:205], v215 offset:61440\n" \
"s_movk_i32 s61, 2040\n" \
"s_nop 0\n" \
"v_mfma_f32_16x16x32_bf16 v[76:79], a[224:227], a[36:39], v[76:79]\n" \
"ds_read_b64_tr_b16 a[206:207], v215 offset:63488\n" \
"v_mad_u32_u24 v186, v186, s61, v231\n" \
"v_mfma_f32_16x16x32_bf16 v[108:111], a[216:219], a[48:51], v[108:111]\n" \
"ds_read_b64_tr_b16 a[220:221], v227 offset:49152\n" \
"v_perm_b32 v196, v197, v196, s54\n" \
"v_mfma_f32_16x16x32_bf16 v[108:111], a[224:227], a[52:55], v[108:111]\n" \
"ds_read_b64_tr_b16 a[222:223], v227 offset:51200\n" \
"v_perm_b32 v197, v199, v198, s54\n" \
"v_mfma_f32_16x16x32_bf16 v[12:15], a[228:231], a[8:11], v[12:15]\n" \
"ds_read_b64_tr_b16 a[212:213], v227 offset:53248\n" \
"v_perm_b32 v200, v201, v200, s54\n" \
"v_mfma_f32_16x16x32_bf16 v[12:15], a[236:239], a[12:15], v[12:15]\n" \
"ds_read_b64_tr_b16 a[214:215], v227 offset:55296\n" \
"v_perm_b32 v201, v203, v202, s54\n" \
"v_mfma_f32_16x16x32_bf16 v[44:47], a[228:231], a[24:27], v[44:47]\n" \
"v_perm_b32 v204, v205, v204, s54\n" \
"v_perm_b32 v205, v207, v206, s54\n" \
"v_mfma_f32_16x16x32_bf16 v[44:47], a[236:239], a[28:31], v[44:47]\n" \
"ds_write_b32 v236, v184 offset:14368\n" \
"v_mfma_f32_16x16x32_bf16 v[76:79], a[228:231], a[40:43], v[76:79]\n" \
"ds_write_b32 v236, v234 offset:14336\n" \
"v_mfma_f32_16x16x32_bf16 v[76:79], a[236:239], a[44:47], v[76:79]\n" \
"ds_write_b128 v186, v[192:195] offset:6144\n" \
"v_mfma_f32_16x16x32_bf16 v[108:111], a[228:231], a[56:59], v[108:111]\n" \
"ds_read_b64_tr_b16 a[208:209], v227 offset:57344\n" \
"ds_write_b128 v186, v[188:191] offset:10240\n" \
"v_mfma_f32_16x16x32_bf16 v[108:111], a[236:239], a[60:63], v[108:111]\n" \
"s_waitcnt vmcnt(63) lgkmcnt(9)\n" \
"s_nop 0\n" \
"v_mfma_f32_16x16x32_bf16 v[4:7], a[232:235], a[128:131], v[4:7]\n" \
"ds_read_b64_tr_b16 a[210:211], v227 offset:59392\n" \
"ds_write_b64 v231, v[196:197] offset:0\n" \
"v_mfma_f32_16x16x32_bf16 v[4:7], a[196:199], a[132:135], v[4:7]\n" \
"ds_read_b64_tr_b16 a[216:217], v227 offset:61440\n" \
"ds_write_b64 v231, v[200:201] offset:2048\n" \
"v_mfma_f32_16x16x32_bf16 v[36:39], a[232:235], a[144:147], v[36:39]\n" \
"ds_read_b64_tr_b16 a[218:219], v227 offset:63488\n" \
"ds_write_b64 v231, v[204:205] offset:4096\n" \
"v_mfma_f32_16x16x32_bf16 v[36:39], a[196:199], a[148:151], v[36:39]\n" \
"v_mfma_f32_16x16x32_bf16 v[68:71], a[232:235], a[160:163], v[68:71]\n" \
"v_mfma_f32_16x16x32_bf16 v[68:71], a[196:199], a[164:167], v[68:71]\n" \
"v_mfma_f32_16x16x32_bf16 v[100:103], a[232:235], a[176:179], v[100:103]\n" \
"ds_read_b64_tr_b16 a[224:225], v215 offset:16896\n" \
"v_mfma_f32_16x16x32_bf16 v[100:103], a[196:199], a[180:183], v[100:103]\n" \
"ds_read_b64_tr_b16 a[226:227], v215 offset:18944\n" \
"v_mfma_f32_16x16x32_bf16 v[4:7], a[200:203], a[136:139], v[4:7]\n" \
"ds_read_b64_tr_b16 a[228:229], v215 offset:20992\n" \
"v_mfma_f32_16x16x32_bf16 v[4:7], a[204:207], a[140:143], v[4:7]\n" \
"ds_read_b64_tr_b16 a[230:231], v215 offset:23040\n" \
"v_mfma_f32_16x16x32_bf16 v[36:39], a[200:203], a[152:155], v[36:39]\n" \
"s_waitcnt vmcnt(63) lgkmcnt(4)\n" \
"s_barrier\n" \
"v_mfma_f32_16x16x32_bf16 v[36:39], a[204:207], a[156:159], v[36:39]\n" \
"v_mov_b32_e64 v192, s40\n" \
"v_mfma_f32_16x16x32_bf16 v[68:71], a[200:203], a[168:171], v[68:71]\n" \
"ds_read_b128 v[196:199], v192 offset:14368\n" \
"v_mfma_f32_16x16x32_bf16 v[68:71], a[204:207], a[172:175], v[68:71]\n" \
"ds_read_b128 v[128:131], %[kb0] offset:6144\n" \
"v_mfma_f32_16x16x32_bf16 v[100:103], a[200:203], a[184:187], v[100:103]\n" \
"ds_read_b64_tr_b16 a[236:237], v215 offset:25088\n" \
"ds_read_b128 v[132:135], %[kb0] offset:8192\n" \
"v_mfma_f32_16x16x32_bf16 v[100:103], a[204:207], a[188:191], v[100:103]\n" \
"s_waitcnt vmcnt(63) lgkmcnt(9)\n" \
"s_nop 0\n" \
"v_mfma_f32_16x16x32_bf16 v[12:15], a[220:223], a[128:131], v[12:15]\n" \
"ds_read_b64_tr_b16 a[238:239], v215 offset:27136\n" \
"ds_read_b128 v[136:139], %[kb0] offset:7168\n" \
"v_mfma_f32_16x16x32_bf16 v[12:15], a[212:215], a[132:135], v[12:15]\n" \
"ds_read_b64_tr_b16 a[232:233], v215 offset:29184\n" \
"ds_read_b128 v[140:143], %[kb0] offset:9216\n" \
"v_mfma_f32_16x16x32_bf16 v[44:47], a[220:223], a[144:147], v[44:47]\n" \
"ds_read_b64_tr_b16 a[234:235], v215 offset:31232\n" \
"ds_read_b128 v[144:147], %[vvb] offset:6144\n" \
"v_mfma_f32_16x16x32_bf16 v[44:47], a[212:215], a[148:151], v[44:47]\n" \
"ds_read_b128 v[148:151], %[vvb] offset:8192\n" \
"v_mfma_f32_16x16x32_bf16 v[76:79], a[220:223], a[160:163], v[76:79]\n" \
"ds_read_b128 v[152:155], %[vvb] offset:7168\n" \
"v_mfma_f32_16x16x32_bf16 v[76:79], a[212:215], a[164:167], v[76:79]\n" \
"ds_read_b128 v[156:159], %[vvb] offset:9216\n" \
"v_mfma_f32_16x16x32_bf16 v[108:111], a[220:223], a[176:179], v[108:111]\n" \
"ds_read_b64_tr_b16 a[196:197], v227 offset:16896\n" \
"ds_read_b128 v[160:163], %[kb0] offset:10240\n" \
"v_mfma_f32_16x16x32_bf16 v[108:111], a[212:215], a[180:183], v[108:111]\n" \
"ds_read_b64_tr_b16 a[198:199], v227 offset:18944\n" \
"ds_read_b128 v[164:167], %[kb0] offset:12288\n" \
"v_mfma_f32_16x16x32_bf16 v[12:15], a[208:211], a[136:139], v[12:15]\n" \
"ds_read_b64_tr_b16 a[200:201], v227 offset:20992\n" \
"ds_read_b128 v[168:171], %[kb0] offset:11264\n" \
"v_mfma_f32_16x16x32_bf16 v[12:15], a[216:219], a[140:143], v[12:15]\n" \
"ds_read_b64_tr_b16 a[202:203], v227 offset:23040\n" \
"ds_read_b128 v[172:175], %[kb0] offset:13312\n" \
"v_mfma_f32_16x16x32_bf16 v[44:47], a[208:211], a[152:155], v[44:47]\n" \
"ds_read_b128 v[176:179], %[vvb] offset:10240\n" \
"v_mfma_f32_16x16x32_bf16 v[44:47], a[216:219], a[156:159], v[44:47]\n" \
"ds_read_b128 v[180:183], %[vvb] offset:12288\n" \
"v_mfma_f32_16x16x32_bf16 v[76:79], a[208:211], a[168:171], v[76:79]\n" \
"ds_read_b128 v[184:187], %[vvb] offset:11264\n" \
"v_mfma_f32_16x16x32_bf16 v[76:79], a[216:219], a[172:175], v[76:79]\n" \
"ds_read_b128 v[188:191], %[vvb] offset:13312\n" \
"v_mfma_f32_16x16x32_bf16 v[108:111], a[208:211], a[184:187], v[108:111]\n" \
"ds_read_b64_tr_b16 a[204:205], v227 offset:25088\n" \
"s_waitcnt lgkmcnt(14)\n" \
"v_add_f32_e32 v223, v196, v197\n" \
"v_mfma_f32_16x16x32_bf16 v[108:111], a[216:219], a[188:191], v[108:111]\n" \
"s_waitcnt vmcnt(63) lgkmcnt(14)\n" \
"s_nop 0\n" \
"v_mfma_f32_16x16x32_bf16 v[20:23], a[224:227], v[0:3], v[20:23]\n" \
"ds_read_b64_tr_b16 a[206:207], v227 offset:27136\n" \
"v_add_f32_e64 v198, v198, v199\n" \
"v_mfma_f32_16x16x32_bf16 v[20:23], a[228:231], v[8:11], v[20:23]\n" \
"ds_read_b64_tr_b16 a[220:221], v227 offset:29184\n" \
"v_add_f32_e64 v223, v223, v198\n" \
"v_mfma_f32_16x16x32_bf16 v[52:55], a[224:227], v[32:35], v[52:55]\n" \
"ds_read_b64_tr_b16 a[222:223], v227 offset:31232\n" \
"ds_read_b128 v[192:195], %[kb0] offset:2048\n" \
"v_mfma_f32_16x16x32_bf16 v[52:55], a[228:231], v[40:43], v[52:55]\n" \
"ds_read_b128 v[196:199], %[kb0] offset:3072\n" \
"v_mfma_f32_16x16x32_bf16 v[84:87], a[224:227], v[64:67], v[84:87]\n" \
"v_cvt_pk_bf16_f32 v4, v4, v5\n" \
"ds_read_b128 v[200:203], %[vvb] offset:2048\n" \
"v_mfma_f32_16x16x32_bf16 v[84:87], a[228:231], v[72:75], v[84:87]\n" \
"v_cvt_pk_bf16_f32 v5, v6, v7\n" \
"ds_read_b128 v[204:207], %[vvb] offset:3072\n" \
"v_mfma_f32_16x16x32_bf16 v[116:119], a[224:227], v[96:99], v[116:119]\n" \
"ds_read_b64_tr_b16 a[212:213], v215 offset:33280\n" \
"v_cvt_pk_bf16_f32 v6, v12, v13\n" \
"v_mfma_f32_16x16x32_bf16 v[116:119], a[228:231], v[104:107], v[116:119]\n" \
"ds_read_b64_tr_b16 a[214:215], v215 offset:35328\n" \
"v_cvt_pk_bf16_f32 v7, v14, v15\n" \
"v_mfma_f32_16x16x32_bf16 v[20:23], a[236:239], v[16:19], v[20:23]\n" \
"ds_read_b64_tr_b16 a[208:209], v215 offset:37376\n" \
"v_mfma_f32_16x16x32_bf16 v[20:23], a[232:235], v[24:27], v[20:23]\n" \
"ds_read_b64_tr_b16 a[210:211], v215 offset:39424\n" \
"v_mfma_f32_16x16x32_bf16 v[52:55], a[236:239], v[48:51], v[52:55]\n" \
"s_add_u32 s55, s46, 8192\n" \
"buffer_store_dwordx4 v[4:7], %[vxs], %[srdz], s55 offen offset:0 sc1\n" \
"v_mfma_f32_16x16x32_bf16 v[52:55], a[232:235], v[56:59], v[52:55]\n" \
"v_cvt_pk_bf16_f32 v36, v36, v37\n" \
"v_cvt_pk_bf16_f32 v37, v38, v39\n" \
"v_mfma_f32_16x16x32_bf16 v[84:87], a[236:239], v[80:83], v[84:87]\n" \
"v_cvt_pk_bf16_f32 v38, v44, v45\n" \
"v_mfma_f32_16x16x32_bf16 v[84:87], a[232:235], v[88:91], v[84:87]\n" \
"v_cvt_pk_bf16_f32 v39, v46, v47\n" \
"v_mfma_f32_16x16x32_bf16 v[116:119], a[236:239], v[112:115], v[116:119]\n" \
"ds_read_b64_tr_b16 a[216:217], v215 offset:41472\n" \
"v_mfma_f32_16x16x32_bf16 v[116:119], a[232:235], v[120:123], v[116:119]\n" \
"s_waitcnt vmcnt(63) lgkmcnt(9)\n" \
"s_nop 0\n" \
"v_mfma_f32_16x16x32_bf16 v[28:31], a[196:199], v[0:3], v[28:31]\n" \
"ds_read_b64_tr_b16 a[218:219], v215 offset:43520\n" \
"v_cvt_pk_bf16_f32 v68, v68, v69\n" \
"v_mfma_f32_16x16x32_bf16 v[28:31], a[200:203], v[8:11], v[28:31]\n" \
"ds_read_b64_tr_b16 a[224:225], v215 offset:45568\n" \
"s_add_u32 s55, s46, 312320\n" \
"buffer_store_dwordx4 v[36:39], %[vxs], %[srdz], s55 offen offset:0 sc1\n" \
"v_mfma_f32_16x16x32_bf16 v[60:63], a[196:199], v[32:35], v[60:63]\n" \
"ds_read_b64_tr_b16 a[226:227], v215 offset:47616\n" \
"v_cvt_pk_bf16_f32 v69, v70, v71\n" \
"v_mfma_f32_16x16x32_bf16 v[60:63], a[200:203], v[40:43], v[60:63]\n" \
"v_cvt_pk_bf16_f32 v70, v76, v77\n" \
"v_cvt_pk_bf16_f32 v71, v78, v79\n" \
"v_mfma_f32_16x16x32_bf16 v[92:95], a[196:199], v[64:67], v[92:95]\n" \
"s_add_u32 s55, s46, 616448\n" \
"buffer_store_dwordx4 v[68:71], %[vxs], %[srdz], s55 offen offset:0 sc1\n" \
"v_mfma_f32_16x16x32_bf16 v[92:95], a[200:203], v[72:75], v[92:95]\n" \
"v_accvgpr_read_b32 v1, a240\n" \
"v_mfma_f32_16x16x32_bf16 v[124:127], a[196:199], v[96:99], v[124:127]\n" \
"ds_read_b64_tr_b16 a[228:229], v227 offset:33280\n" \
"v_cvt_pk_bf16_f32 v100, v100, v101\n" \
"v_mfma_f32_16x16x32_bf16 v[124:127], a[200:203], v[104:107], v[124:127]\n" \
"ds_read_b64_tr_b16 a[230:231], v227 offset:35328\n" \
"v_cvt_pk_bf16_f32 v101, v102, v103\n" \
"v_mfma_f32_16x16x32_bf16 v[28:31], a[204:207], v[16:19], v[28:31]\n" \
"ds_read_b64_tr_b16 a[236:237], v227 offset:37376\n" \
"v_cvt_pk_bf16_f32 v102, v108, v109\n" \
"v_readlane_b32 s68, %[thi], 18\n" \
"v_lshlrev_b32_e64 v226, 7, v226\n" \
"v_mfma_f32_16x16x32_bf16 v[28:31], a[220:223], v[24:27], v[28:31]\n" \
"ds_read_b64_tr_b16 a[238:239], v227 offset:39424\n" \
"v_cvt_pk_bf16_f32 v103, v110, v111\n" \
"v_mfma_f32_16x16x32_bf16 v[60:63], a[204:207], v[48:51], v[60:63]\n" \
"s_add_u32 s55, s46, 920576\n" \
"buffer_store_dwordx4 v[100:103], %[vxs], %[srdz], s55 offen offset:0 sc1\n" \
"v_mfma_f32_16x16x32_bf16 v[60:63], a[220:223], v[56:59], v[60:63]\n" \
"v_lshlrev_b32_e64 v0, 16, v1\n" \
"v_mfma_f32_16x16x32_bf16 v[92:95], a[204:207], v[80:83], v[92:95]\n" \
"v_mov_b32_e64 v230, 64\n" \
"buffer_atomic_add v230, v225, %[srdz], s47 offen sc1\n" \
"v_mfma_f32_16x16x32_bf16 v[92:95], a[220:223], v[88:91], v[92:95]\n" \
"v_and_b32_e64 v1, s43, v1\n" \
"v_mfma_f32_16x16x32_bf16 v[124:127], a[204:207], v[112:115], v[124:127]\n" \
"ds_read_b64_tr_b16 a[232:233], v227 offset:41472\n" \
"v_accvgpr_read_b32 v3, a241\n" \
"v_mfma_f32_16x16x32_bf16 v[124:127], a[220:223], v[120:123], v[124:127]\n" \
"s_waitcnt vmcnt(63) lgkmcnt(5)\n" \
"s_nop 0\n" \
"v_mfma_f32_16x16x32_bf16 v[20:23], a[212:215], a[0:3], v[20:23]\n" \
"ds_read_b64_tr_b16 a[234:235], v227 offset:43520\n" \
"v_lshlrev_b32_e64 v2, 16, v3\n" \
"v_mfma_f32_16x16x32_bf16 v[20:23], a[208:211], a[4:7], v[20:23]\n" \
"ds_read_b64_tr_b16 a[196:197], v227 offset:45568\n" \
"v_and_b32_e64 v3, s43, v3\n" \
"v_mfma_f32_16x16x32_bf16 v[52:55], a[212:215], a[16:19], v[52:55]\n" \
"ds_read_b64_tr_b16 a[198:199], v227 offset:47616\n" \
"v_accvgpr_read_b32 v9, a244\n" \
"v_mfma_f32_16x16x32_bf16 v[52:55], a[208:211], a[20:23], v[52:55]\n" \
"buffer_load_dword v216, v226, %[srdz], s68 offen offset:64 sc1\n" \
"v_mfma_f32_16x16x32_bf16 v[84:87], a[212:215], a[32:35], v[84:87]\n" \
"v_mov_b32_e32 v226, 0\n" \
"v_lshrrev_b32_e32 v217, 3, v224\n" \
"s_add_u32 s68, %[sin], 4096\n" \
"v_lshlrev_b32_e64 v8, 16, v9\n" \
"v_mfma_f32_16x16x32_bf16 v[84:87], a[208:211], a[36:39], v[84:87]\n" \
"v_and_b32_e64 v9, s43, v9\n" \
"v_mfma_f32_16x16x32_bf16 v[116:119], a[212:215], a[48:51], v[116:119]\n" \
"ds_read_b64_tr_b16 a[200:201], v215 offset:49664\n" \
"v_accvgpr_read_b32 v11, a245\n" \
"v_mfma_f32_16x16x32_bf16 v[116:119], a[208:211], a[52:55], v[116:119]\n" \
"ds_read_b64_tr_b16 a[202:203], v215 offset:51712\n" \
"v_lshlrev_b32_e64 v10, 16, v11\n" \
"v_mfma_f32_16x16x32_bf16 v[20:23], a[216:219], a[8:11], v[20:23]\n" \
"ds_read_b64_tr_b16 a[204:205], v215 offset:53760\n" \
"v_and_b32_e64 v11, s43, v11\n" \
"v_mfma_f32_16x16x32_bf16 v[20:23], a[224:227], a[12:15], v[20:23]\n" \
"ds_read_b64_tr_b16 a[206:207], v215 offset:55808\n" \
"v_accvgpr_read_b32 v33, a248\n" \
"v_mfma_f32_16x16x32_bf16 v[52:55], a[216:219], a[24:27], v[52:55]\n" \
"ds_read_b64_tr_b16 a[220:221], v215 offset:57856\n" \
"v_lshlrev_b32_e64 v32, 16, v33\n" \
"v_mfma_f32_16x16x32_bf16 v[52:55], a[224:227], a[28:31], v[52:55]\n" \
"ds_read_b64_tr_b16 a[222:223], v215 offset:59904\n" \
"v_and_b32_e64 v33, s43, v33\n" \
"v_mfma_f32_16x16x32_bf16 v[84:87], a[216:219], a[40:43], v[84:87]\n" \
"ds_read_b64_tr_b16 a[212:213], v215 offset:61952\n" \
"v_accvgpr_read_b32 v35, a249\n" \
"v_mfma_f32_16x16x32_bf16 v[84:87], a[224:227], a[44:47], v[84:87]\n" \
"ds_read_b64_tr_b16 a[214:215], v215 offset:64000\n" \
"v_lshlrev_b32_e64 v34, 16, v35\n" \
"v_mfma_f32_16x16x32_bf16 v[116:119], a[216:219], a[56:59], v[116:119]\n" \
"ds_read_b64_tr_b16 a[208:209], v227 offset:49664\n" \
"v_and_b32_e64 v35, s43, v35\n" \
"v_mfma_f32_16x16x32_bf16 v[116:119], a[224:227], a[60:63], v[116:119]\n" \
"s_waitcnt vmcnt(63) lgkmcnt(9)\n" \
"s_nop 0\n" \
"v_mfma_f32_16x16x32_bf16 v[28:31], a[228:231], a[0:3], v[28:31]\n" \
"ds_read_b64_tr_b16 a[210:211], v227 offset:51712\n" \
"v_accvgpr_read_b32 v41, a252\n" \
"v_mfma_f32_16x16x32_bf16 v[28:31], a[236:239], a[4:7], v[28:31]\n" \
"ds_read_b64_tr_b16 a[216:217], v227 offset:53760\n" \
"v_lshlrev_b32_e64 v40, 16, v41\n" \
"v_mfma_f32_16x16x32_bf16 v[60:63], a[228:231], a[16:19], v[60:63]\n" \
"ds_read_b64_tr_b16 a[218:219], v227 offset:55808\n" \
"v_and_b32_e64 v41, s43, v41\n" \
"v_mfma_f32_16x16x32_bf16 v[60:63], a[236:239], a[20:23], v[60:63]\n" \
"ds_read_b64_tr_b16 a[224:225], v227 offset:57856\n" \
"v_accvgpr_read_b32 v43, a253\n" \
"v_mfma_f32_16x16x32_bf16 v[92:95], a[228:231], a[32:35], v[92:95]\n" \
"ds_read_b64_tr_b16 a[226:227], v227 offset:59904\n" \
"v_lshlrev_b32_e64 v42, 16, v43\n" \
"v_mfma_f32_16x16x32_bf16 v[92:95], a[236:239], a[36:39], v[92:95]\n" \
"v_and_b32_e64 v43, s43, v43\n" \
"v_mfma_f32_16x16x32_bf16 v[124:127], a[228:231], a[48:51], v[124:127]\n" \
"v_accvgpr_read_b32 v17, a242\n" \
"v_mfma_f32_16x16x32_bf16 v[124:127], a[236:239], a[52:55], v[124:127]\n" \
"ds_read_b64_tr_b16 a[228:229], v227 offset:61952\n" \
"v_lshlrev_b32_e64 v16, 16, v17\n" \
"v_mfma_f32_16x16x32_bf16 v[28:31], a[232:235], a[8:11], v[28:31]\n" \
"ds_read_b64_tr_b16 a[230:231], v227 offset:64000\n" \
"v_and_b32_e64 v17, s43, v17\n" \
"v_mfma_f32_16x16x32_bf16 v[28:31], a[196:199], a[12:15], v[28:31]\n" \
"v_accvgpr_read_b32 v19, a243\n" \
"v_mfma_f32_16x16x32_bf16 v[60:63], a[232:235], a[24:27], v[60:63]\n" \
"v_lshlrev_b32_e64 v18, 16, v19\n" \
"v_mfma_f32_16x16x32_bf16 v[60:63], a[196:199], a[28:31], v[60:63]\n" \
"v_and_b32_e64 v19, s43, v19\n" \
"v_mfma_f32_16x16x32_bf16 v[92:95], a[232:235], a[40:43], v[92:95]\n" \
"v_accvgpr_read_b32 v25, a246\n" \
"v_mfma_f32_16x16x32_bf16 v[92:95], a[196:199], a[44:47], v[92:95]\n" \
"v_lshlrev_b32_e64 v24, 16, v25\n" \
"v_mfma_f32_16x16x32_bf16 v[124:127], a[232:235], a[56:59], v[124:127]\n" \
"v_and_b32_e64 v25, s43, v25\n" \
"v_mfma_f32_16x16x32_bf16 v[124:127], a[196:199], a[60:63], v[124:127]\n" \
"s_waitcnt vmcnt(63) lgkmcnt(0)\n" \
"s_nop 0\n" \
"v_mfma_f32_16x16x32_bf16 v[20:23], a[200:203], a[128:131], v[20:23]\n" \
"v_accvgpr_read_b32 v27, a247\n" \
"v_mfma_f32_16x16x32_bf16 v[20:23], a[204:207], a[132:135], v[20:23]\n" \
"v_lshlrev_b32_e64 v26, 16, v27\n" \
"v_mfma_f32_16x16x32_bf16 v[20:23], a[220:223], a[136:139], v[20:23]\n" \
"v_and_b32_e64 v27, s43, v27\n" \
"v_mfma_f32_16x16x32_bf16 v[20:23], a[212:215], a[140:143], v[20:23]\n" \
"v_accvgpr_read_b32 v49, a250\n" \
"v_mfma_f32_16x16x32_bf16 v[28:31], a[208:211], a[128:131], v[28:31]\n" \
"v_lshlrev_b32_e64 v48, 16, v49\n" \
"v_mfma_f32_16x16x32_bf16 v[28:31], a[216:219], a[132:135], v[28:31]\n" \
"v_and_b32_e64 v49, s43, v49\n" \
"v_mfma_f32_16x16x32_bf16 v[28:31], a[224:227], a[136:139], v[28:31]\n" \
"v_accvgpr_read_b32 v51, a251\n" \
"v_mfma_f32_16x16x32_bf16 v[28:31], a[228:231], a[140:143], v[28:31]\n" \
"v_lshlrev_b32_e64 v50, 16, v51\n" \
"v_mfma_f32_16x16x32_bf16 v[52:55], a[200:203], a[144:147], v[52:55]\n" \
"v_and_b32_e64 v51, s43, v51\n" \
"v_mfma_f32_16x16x32_bf16 v[52:55], a[204:207], a[148:151], v[52:55]\n" \
"v_accvgpr_read_b32 v57, a254\n" \
"v_mfma_f32_16x16x32_bf16 v[52:55], a[220:223], a[152:155], v[52:55]\n" \
"v_lshlrev_b32_e64 v56, 16, v57\n" \
"v_mfma_f32_16x16x32_bf16 v[52:55], a[212:215], a[156:159], v[52:55]\n" \
"v_cvt_pk_bf16_f32 v20, v20, v21\n" \
"v_cvt_pk_bf16_f32 v21, v22, v23\n" \
"v_and_b32_e64 v57, s43, v57\n" \
"v_mfma_f32_16x16x32_bf16 v[60:63], a[208:211], a[144:147], v[60:63]\n" \
"v_accvgpr_read_b32 v59, a255\n" \
"v_mfma_f32_16x16x32_bf16 v[60:63], a[216:219], a[148:151], v[60:63]\n" \
"v_cvt_pk_bf16_f32 v22, v28, v29\n" \
"v_cvt_pk_bf16_f32 v23, v30, v31\n" \
"v_lshlrev_b32_e64 v58, 16, v59\n" \
"v_mfma_f32_16x16x32_bf16 v[60:63], a[224:227], a[152:155], v[60:63]\n" \
"s_add_u32 s55, s46, 12288\n" \
"buffer_store_dwordx4 v[20:23], %[vxs], %[srdz], s55 offen offset:0 sc1\n" \
"v_mfma_f32_16x16x32_bf16 v[60:63], a[228:231], a[156:159], v[60:63]\n" \
"v_mfma_f32_16x16x32_bf16 v[84:87], a[200:203], a[160:163], v[84:87]\n" \
"v_and_b32_e64 v59, s43, v59\n" \
"v_mfma_f32_16x16x32_bf16 v[84:87], a[204:207], a[164:167], v[84:87]\n" \
"v_mfma_f32_16x16x32_bf16 v[84:87], a[220:223], a[168:171], v[84:87]\n" \
"v_mfma_f32_16x16x32_bf16 v[84:87], a[212:215], a[172:175], v[84:87]\n" \
"v_cvt_pk_bf16_f32 v52, v52, v53\n" \
"v_cvt_pk_bf16_f32 v53, v54, v55\n" \
"v_mfma_f32_16x16x32_bf16 v[92:95], a[208:211], a[160:163], v[92:95]\n" \
"v_mfma_f32_16x16x32_bf16 v[92:95], a[216:219], a[164:167], v[92:95]\n" \
"v_cvt_pk_bf16_f32 v54, v60, v61\n" \
"v_cvt_pk_bf16_f32 v55, v62, v63\n" \
"v_mfma_f32_16x16x32_bf16 v[92:95], a[224:227], a[168:171], v[92:95]\n" \
"s_add_u32 s55, s46, 316416\n" \
"buffer_store_dwordx4 v[52:55], %[vxs], %[srdz], s55 offen offset:0 sc1\n" \
"v_mfma_f32_16x16x32_bf16 v[92:95], a[228:231], a[172:175], v[92:95]\n" \
"v_mfma_f32_16x16x32_bf16 v[116:119], a[200:203], a[176:179], v[116:119]\n" \
"v_mfma_f32_16x16x32_bf16 v[116:119], a[204:207], a[180:183], v[116:119]\n" \
"v_mfma_f32_16x16x32_bf16 v[116:119], a[220:223], a[184:187], v[116:119]\n" \
"v_mfma_f32_16x16x32_bf16 v[116:119], a[212:215], a[188:191], v[116:119]\n" \
"v_cvt_pk_bf16_f32 v84, v84, v85\n" \
"v_cvt_pk_bf16_f32 v85, v86, v87\n" \
"v_mfma_f32_16x16x32_bf16 v[124:127], a[208:211], a[176:179], v[124:127]\n" \
"v_mfma_f32_16x16x32_bf16 v[124:127], a[216:219], a[180:183], v[124:127]\n" \
"v_cvt_pk_bf16_f32 v86, v92, v93\n" \
"v_cvt_pk_bf16_f32 v87, v94, v95\n" \
"v_mfma_f32_16x16x32_bf16 v[124:127], a[224:227], a[184:187], v[124:127]\n" \
"s_add_u32 s55, s46, 620544\n" \
"buffer_store_dwordx4 v[84:87], %[vxs], %[srdz], s55 offen offset:0 sc1\n" \
"v_mfma_f32_16x16x32_bf16 v[124:127], a[228:231], a[188:191], v[124:127]\n" \
"v_cvt_pk_bf16_f32 v116, v116, v117\n" \
"v_cvt_pk_bf16_f32 v117, v118, v119\n" \
"s_waitcnt lgkmcnt(0)\n" \
"s_nop 4\n" \
"v_cvt_pk_bf16_f32 v118, v124, v125\n" \
"v_cvt_pk_bf16_f32 v119, v126, v127\n" \
"s_add_u32 s55, s46, 924672\n" \
"buffer_store_dwordx4 v[116:119], %[vxs], %[srdz], s55 offen offset:0 sc1\n" \
"buffer_load_dword v229, v226, %[srdz], s53 offen sc1\n" \
"v_readlane_b32 s62, %[tlo], 35\n" \
"v_lshrrev_b32_e32 v72, 2, v224\n" \
"s_add_u32 s54, %[sin], 8192\n" \
"s_add_u32 s55, %[sin], 12288\n" \
"s_movk_i32 s56, 0x1000\n" \
"s_movk_i32 s57, 0x2000\n" \
"s_movk_i32 s58, 0x3000\n" \
"buffer_load_dwordx4 v[64:67], v72, %[srdz], s62 offen offset:0 sc1\n" \
"buffer_load_dwordx4 v[68:71], v72, %[srdz], s62 offen offset:16 sc1\n" \
"v_mov_b32_e32 v226, v211\n" \
"v_bfe_u32 v221, v222, 3, 1\n" \
"v_and_b32_e32 v208, 1, v222\n" \
"v_lshl_or_b32 v221, v221, 1, v208\n" \
"s_movk_i32 s63, 272\n" \
"v_mad_u32_u24 v221, v221, s63, %[vx0]\n" \
"v_readlane_b32 s64, %[tlo], 25\n" \
"v_readlane_b32 s65, %[thi], 25\n" \
"s_mov_b32 s66, 0x400000\n" \
"s_mov_b32 s67, 0x20000\n" \
"v_readlane_b32 s48, %[tlo], 27\n" \
"v_readlane_b32 s49, %[thi], 27\n" \
"v_readlane_b32 s69, %[tlo], 21\n" \
"v_accvgpr_write_b32 a128, -1\n" \
"v_lshrrev_b32_e32 v214, 4, v224\n" \
"s_add_u32 s62, %[m0b], 65536\n" \
"v_add_u32_e32 v215, %[m0t], v214\n" \
"v_add_u32_e32 v214, s62, v214\n" \
"ds_read_b64_tr_b16 a[64:65], v214 offset:0\n" \
"v_and_b32_e32 v4, 1, v222\n" \
"ds_read_b64_tr_b16 a[66:67], v214 offset:1024\n" \
"v_bfe_u32 v5, v222, 1, 3\n" \
"ds_read_b64_tr_b16 a[96:97], v215 offset:0\n" \
"v_lshlrev_b32_e32 v4, 4, v4\n" \
"ds_read_b64_tr_b16 a[98:99], v215 offset:1024\n" \
"v_mov_b32_e32 v6, 0x3f80\n" \
"ds_read_b64_tr_b16 a[68:69], v214 offset:512\n" \
"v_lshlrev_b32_e64 v5, v5, 1\n" \
"ds_read_b64_tr_b16 a[70:71], v214 offset:1536\n" \
"v_lshlrev_b32_e32 v4, v4, v6\n" \
"ds_read_b64_tr_b16 a[100:101], v215 offset:512\n" \
"v_bfe_i32 v230, v5, 0, 1\n" \
"ds_read_b64_tr_b16 a[102:103], v215 offset:1536\n" \
"v_bfe_i32 v231, v5, 1, 1\n" \
"ds_read_b64_tr_b16 a[72:73], v214 offset:16384\n" \
"v_bfe_i32 v232, v5, 2, 1\n" \
"ds_read_b64_tr_b16 a[74:75], v214 offset:17408\n" \
"v_bfe_i32 v233, v5, 3, 1\n" \
"ds_read_b64_tr_b16 a[104:105], v215 offset:4096\n" \
"v_bfe_i32 v234, v5, 4, 1\n" \
"ds_read_b64_tr_b16 a[106:107], v215 offset:5120\n" \
"v_bfe_i32 v235, v5, 5, 1\n" \
"ds_read_b64_tr_b16 a[76:77], v214 offset:16896\n" \
"v_bfe_i32 v236, v5, 6, 1\n" \
"ds_read_b64_tr_b16 a[78:79], v214 offset:17920\n" \
"v_bfe_i32 v237, v5, 7, 1\n" \
"ds_read_b64_tr_b16 a[108:109], v215 offset:4608\n" \
"v_and_b32_e32 v230, v4, v230\n" \
".if . - adaln_body != 61576\n" \
".error \"size model\"\n" \
".endif\n" \
"Lk14_%=:\n" \
"s_cbranch_execz Lk15_%=\n" \
"s_nop 0\n" \
"ds_read_b64_tr_b16 a[110:111], v215 offset:5632\n" \
"v_and_b32_e32 v231, v4, v231\n" \
"ds_read_b64_tr_b16 a[80:81], v214 offset:2048\n" \
"v_and_b32_e32 v232, v4, v232\n" \
"ds_read_b64_tr_b16 a[82:83], v214 offset:3072\n" \
"v_and_b32_e32 v233, v4, v233\n" \
"ds_read_b64_tr_b16 a[112:113], v215 offset:2048\n" \
"v_and_b32_e32 v234, v4, v234\n" \
"ds_read_b64_tr_b16 a[114:115], v215 offset:3072\n" \
"v_and_b32_e32 v235, v4, v235\n" \
"ds_read_b64_tr_b16 a[84:85], v214 offset:2560\n" \
"v_and_b32_e32 v236, v4, v236\n" \
"ds_read_b64_tr_b16 a[86:87], v214 offset:3584\n" \
"v_and_b32_e32 v237, v4, v237\n" \
"ds_read_b64_tr_b16 a[116:117], v215 offset:2560\n" \
"ds_read_b64_tr_b16 a[118:119], v215 offset:3584\n" \
"ds_read_b64_tr_b16 a[88:89], v214 offset:18432\n" \
"ds_read_b64_tr_b16 a[90:91], v214 offset:19456\n" \
"ds_read_b64_tr_b16 a[120:121], v215 offset:6144\n" \
"ds_read_b64_tr_b16 a[122:123], v215 offset:7168\n" \
"ds_read_b64_tr_b16 a[92:93], v214 offset:18944\n" \
"ds_read_b64_tr_b16 a[94:95], v214 offset:19968\n" \
"ds_read_b64_tr_b16 a[124:125], v215 offset:6656\n" \
"ds_read_b64_tr_b16 a[126:127], v215 offset:7680\n" \
"v_lshrrev_b32_e32 v208, 1, v217\n" \
"v_and_b32_e32 v210, 24, v208\n" \
"v_bfe_u32 v215, v222, 4, 1\n" \
"v_lshl_or_b32 v210, v215, 14, v210\n" \
"v_lshl_or_b32 v210, v215, 7, v210\n" \
"v_bfe_u32 v215, v222, 2, 2\n" \
"v_lshl_or_b32 v210, v215, 9, v210\n" \
"v_lshrrev_b32_e32 v211, 5, v222\n" \
"v_xor_b32_e32 v215, v215, v211\n" \
"v_lshl_or_b32 v210, v215, 5, v210\n" \
"s_add_u32 s62, %[m0b], 98304\n" \
"v_add_u32_e32 v210, s62, v210\n" \
"v_xor_b32_e32 v211, 64, v210\n" \
"v_xor_b32_e32 v213, 0x80, v210\n" \
"v_xor_b32_e32 v214, 0xc0, v210\n" \
"v_readlane_b32 s62, %[thi], 28\n" \
"v_and_b32_e32 v76, 7, v222\n" \
"v_lshrrev_b32_e32 v79, 1, v222\n" \
"v_and_b32_e32 v77, 1, v222\n" \
"v_lshlrev_b32_e32 v79, 13, v79\n" \
"v_lshl_or_b32 v79, v77, 4, v79\n" \
"v_add_u32_e32 v76, s62, v76\n" \
"v_accvgpr_write_b32 a34, v79\n" \
"v_lshrrev_b32_e32 v77, 8, v76\n" \
"v_lshlrev_b32_e32 v77, 7, v77\n" \
"v_bfe_u32 v79, v76, 6, 2\n" \
"v_lshl_or_b32 v77, v79, 5, v77\n" \
"v_bfe_u32 v79, v76, 1, 1\n" \
"v_lshl_or_b32 v77, v79, 4, v77\n" \
"v_bfe_u32 v79, v76, 2, 4\n" \
"v_or_b32_e32 v77, v79, v77\n" \
"v_lshrrev_b32_e32 v78, 8, v77\n" \
"v_lshlrev_b32_e32 v78, 9, v78\n" \
"v_accvgpr_write_b32 a32, v77\n" \
"v_bfe_u32 v79, v77, 6, 1\n" \
"v_lshl_or_b32 v78, v79, 8, v78\n" \
"v_bfe_u32 v79, v77, 7, 1\n" \
"v_lshl_or_b32 v78, v79, 7, v78\n" \
"v_bfe_u32 v79, v77, 4, 1\n" \
"v_lshl_or_b32 v78, v79, 6, v78\n" \
"v_bfe_u32 v79, v77, 2, 2\n" \
"v_lshl_or_b32 v78, v79, 4, v78\n" \
"v_bfe_u32 v79, v77, 5, 1\n" \
"v_lshl_or_b32 v78, v79, 3, v78\n" \
"v_bfe_u32 v79, v77, 0, 2\n" \
"v_lshl_or_b32 v78, v79, 1, v78\n" \
"v_accvgpr_write_b32 a33, v78\n" \
"s_mov_b32 s51, 0x10000\n" \
"s_mov_b32 s50, 0\n" \
"s_waitcnt vmcnt(7)\n" \
"v_cmp_eq_u32_e32 vcc, 0, v216\n" \
"s_mov_b32 %[got], 0\n" \
"s_cbranch_vccnz TLf1_%=\n" \
"s_mov_b32 %[got], 1\n" \
"buffer_load_dwordx4 a[196:199], v217, %[srdz], %[sin] offen offset:0 sc1\n" \
"buffer_load_dwordx4 a[200:203], v217, %[srdz], %[sin] offen offset:1024 sc1\n" \
"buffer_load_dwordx4 a[204:207], v217, %[srdz], %[sin] offen offset:2048 sc1\n" \
"buffer_load_dwordx4 a[208:211], v217, %[srdz], %[sin] offen offset:3072 sc1\n" \
"buffer_load_dwordx4 a[212:215], v217, %[srdz], s68 offen offset:0 sc1\n" \
"buffer_load_dwordx4 a[216:219], v217, %[srdz], s68 offen offset:1024 sc1\n" \
"buffer_load_dwordx4 a[220:223], v217, %[srdz], s68 offen offset:2048 sc1\n" \
"buffer_load_dwordx4 a[224:227], v217, %[srdz], s68 offen offset:3072 sc1\n" \
"TLf1_%=:\n" \
"ds_read_b64_tr_b16 a[0:1], v210 offset:0\n" \
"ds_read_b64_tr_b16 a[2:3], v213 offset:2048\n" \
"ds_read_b64_tr_b16 a[4:5], v211 offset:0\n" \
"ds_read_b64_tr_b16 a[6:7], v214 offset:2048\n" \
"ds_read_b64_tr_b16 a[8:9], v213 offset:0\n" \
"ds_read_b64_tr_b16 a[10:11], v210 offset:2048\n" \
"ds_read_b64_tr_b16 a[12:13], v214 offset:0\n" \
"ds_read_b64_tr_b16 a[14:15], v211 offset:2048\n" \
"ds_read_b64_tr_b16 a[16:17], v210 offset:256\n" \
"ds_read_b64_tr_b16 a[18:19], v213 offset:2304\n" \
"ds_read_b64_tr_b16 a[20:21], v211 offset:256\n" \
"ds_read_b64_tr_b16 a[22:23], v214 offset:2304\n" \
"ds_read_b64_tr_b16 a[24:25], v213 offset:256\n" \
"ds_read_b64_tr_b16 a[26:27], v210 offset:2304\n" \
"ds_read_b64_tr_b16 a[28:29], v214 offset:256\n" \
"ds_read_b64_tr_b16 a[30:31], v211 offset:2304\n" \
"s_waitcnt vmcnt(8)\n" \
"s_cmp_lg_u32 %[got], 0\n" \
"s_cbranch_scc1 TLrp4_%=\n" \
"s_waitcnt vmcnt(0)\n" \
"v_and_b32_e32 v208, 32, v229\n" \
"v_cmp_eq_u32_e32 vcc, 0, v208\n" \
"s_cbranch_vccz TLrl2_%=\n" \
"v_mov_b32_e32 v216, s40\n" \
"TLrq2_%=:\n" \
"buffer_load_dword v225, off, %[srdz], s53 offset:0 sc1\n" \
"s_mov_b32 s52, 10\n" \
"TLrs2_%=:\n" \
"ds_read_b32 v229, v216 offset:14336\n" \
"s_waitcnt lgkmcnt(0)\n" \
"v_and_b32_e32 v208, 32, v229\n" \
"v_cmp_eq_u32_e32 vcc, 0, v208\n" \
"s_cbranch_vccz TLrd2_%=\n" \
"s_sub_u32 s52, s52, 1\n" \
"s_cbranch_scc0 TLrs2_%=\n" \
"s_sub_u32 s51, s51, 1\n" \
"s_cselect_b32 s51, 0, s51\n" \
"s_waitcnt vmcnt(0)\n" \
"v_and_b32_e32 v208, 32, v225\n" \
"v_cmp_eq_u32_e32 vcc, 0, v208\n" \
"s_cbranch_scc1 TLrl2_%=\n" \
"s_cbranch_vccnz TLrq2_%=\n" \
"v_mov_b32_e32 v229, v225\n" \
"s_mov_b64 exec, 1\n" \
"ds_max_u32 v216, v225 offset:14336\n" \
"s_mov_b64 exec, -1\n" \
"TLrd2_%=:\n" \
"s_sleep 0\n" \
"TLrl2_%=:\n" \
"buffer_load_dwordx4 a[196:199], v217, %[srdz], %[sin] offen offset:0 sc1\n" \
"buffer_load_dwordx4 a[200:203], v217, %[srdz], %[sin] offen offset:1024 sc1\n" \
"buffer_load_dwordx4 a[204:207], v217, %[srdz], %[sin] offen offset:2048 sc1\n" \
"buffer_load_dwordx4 a[208:211], v217, %[srdz], %[sin] offen offset:3072 sc1\n" \
"buffer_load_dwordx4 a[212:215], v217, %[srdz], s68 offen offset:0 sc1\n" \
"buffer_load_dwordx4 a[216:219], v217, %[srdz], s68 offen offset:1024 sc1\n" \
"buffer_load_dwordx4 a[220:223], v217, %[srdz], s68 offen offset:2048 sc1\n" \
"buffer_load_dwordx4 a[224:227], v217, %[srdz], s68 offen offset:3072 sc1\n" \
"TLrp4_%=:\n" \
"v_and_b32_e32 v208, 0x800, v229\n" \
"v_cmp_eq_u32_e32 vcc, 0, v208\n" \
"s_cbranch_vccnz TLrn_%=\n" \
"s_mov_b32 s50, 1\n" \
"buffer_load_dwordx4 a[164:167], v217, %[srdz], s54 offen offset:0 sc1\n" \
"buffer_load_dwordx4 a[168:171], v217, %[srdz], s54 offen offset:1024 sc1\n" \
"buffer_load_dwordx4 a[172:175], v217, %[srdz], s54 offen offset:2048 sc1\n" \
"buffer_load_dwordx4 a[176:179], v217, %[srdz], s54 offen offset:3072 sc1\n" \
"buffer_load_dwordx4 a[180:183], v217, %[srdz], s55 offen offset:0 sc1\n" \
"buffer_load_dwordx4 a[184:187], v217, %[srdz], s55 offen offset:1024 sc1\n" \
"buffer_load_dwordx4 a[188:191], v217, %[srdz], s55 offen offset:2048 sc1\n" \
"buffer_load_dwordx4 a[192:195], v217, %[srdz], s55 offen offset:3072 sc1\n" \
"TLrn_%=:\n" \
"v_min3_u32 v208, v64, v65, v66\n" \
"v_min3_u32 v209, v67, v68, v69\n" \
"v_min3_u32 v208, v208, v70, v71\n" \
"v_min_u32_e32 v208, v208, v209\n" \
"v_cmp_eq_u32_e32 vcc, 0, v208\n" \
"s_cbranch_vccnz TLpk_%=\n" \
"TLpkb_%=:\n" \
"v_min_u32_e32 v208, v65, v67\n" \
"v_min_u32_e32 v209, v69, v71\n" \
"v_max_u32_e32 v65, v65, v67\n" \
"v_max_u32_e32 v69, v69, v71\n" \
"v_max3_u32 v64, v208, v64, v66\n" \
"v_max3_u32 v68, v209, v68, v70\n" \
"v_min_u32_e32 v208, v65, v69\n" \
"v_max_u32_e32 v65, v65, v69\n" \
"v_max3_u32 v64, v208, v64, v68\n" \
"v_mov_b64_e32 v[68:69], v[64:65]\n" \
"v_min_u32_dpp v208, v65, v65 quad_perm:[1,0,3,2] row_mask:0xf bank_mask:0xf\n" \
"v_max_u32_dpp v209, v64, v64 quad_perm:[1,0,3,2] row_mask:0xf bank_mask:0xf\n" \
"v_max_u32_dpp v65, v65, v65 quad_perm:[1,0,3,2] row_mask:0xf bank_mask:0xf\n" \
"v_max_u32_e32 v64, v208, v209\n" \
"s_nop 0\n" \
"v_min_u32_dpp v208, v65, v65 quad_perm:[2,3,0,1] row_mask:0xf bank_mask:0xf\n" \
"v_max_u32_dpp v209, v64, v64 quad_perm:[2,3,0,1] row_mask:0xf bank_mask:0xf\n" \
"v_max_u32_dpp v65, v65, v65 quad_perm:[2,3,0,1] row_mask:0xf bank_mask:0xf\n" \
"v_max_u32_e32 v64, v208, v209\n" \
"s_nop 0\n" \
"v_min_u32_dpp v208, v65, v65 row_half_mirror row_mask:0xf bank_mask:0xf\n" \
"v_max_u32_dpp v209, v64, v64 row_half_mirror row_mask:0xf bank_mask:0xf\n" \
"v_max_u32_dpp v65, v65, v65 row_half_mirror row_mask:0xf bank_mask:0xf\n" \
"v_max_u32_e32 v64, v208, v209\n" \
"s_nop 0\n" \
"v_min_u32_dpp v208, v65, v65 row_mirror row_mask:0xf bank_mask:0xf\n" \
"v_max_u32_dpp v209, v64, v64 row_mirror row_mask:0xf bank_mask:0xf\n" \
"v_max_u32_dpp v65, v65, v65 row_mirror row_mask:0xf bank_mask:0xf\n" \
"v_max_u32_e32 v64, v208, v209\n" \
"v_mov_b32_e32 v67, v65\n" \
"v_mov_b32_e32 v66, v64\n" \
"s_nop 0\n" \
"v_permlane16_swap_b32_e32 v65, v67\n" \
"v_permlane16_swap_b32_e32 v64, v66\n" \
"v_min_u32_e32 v208, v65, v67\n" \
"v_max_u32_e32 v65, v65, v67\n" \
"v_max3_u32 v64, v208, v64, v66\n" \
"v_mov_b32_e32 v67, v65\n" \
"v_mov_b32_e32 v66, v64\n" \
"s_nop 0\n" \
"v_permlane32_swap_b32_e32 v65, v67\n" \
"v_permlane32_swap_b32_e32 v64, v66\n" \
"v_min_u32_e32 v208, v65, v67\n" \
"v_max_u32_e32 v65, v65, v67\n" \
"v_max3_u32 v64, v208, v64, v66\n" \
"v_readfirstlane_b32 s44, v65\n" \
"v_readfirstlane_b32 s45, v64\n" \
"v_cmp_ge_u32_e32 vcc, v68, v64\n" \
"s_nop 1\n" \
"v_cndmask_b32_e64 v68, v68, 0, vcc\n" \
"v_cmp_ge_u32_e32 vcc, v69, v64\n" \
"s_nop 1\n" \
"v_cndmask_b32_e32 v69, v69, v68, vcc\n" \
"v_cndmask_b32_e64 v68, v68, 0, vcc\n" \
"s_nop 0\n" \
"v_min_u32_dpp v208, v69, v69 quad_perm:[1,0,3,2] row_mask:0xf bank_mask:0xf\n" \
"v_max_u32_dpp v209, v68, v68 quad_perm:[1,0,3,2] row_mask:0xf bank_mask:0xf\n" \
"v_max_u32_dpp v69, v69, v69 quad_perm:[1,0,3,2] row_mask:0xf bank_mask:0xf\n" \
"v_max_u32_e32 v68, v208, v209\n" \
"s_nop 0\n" \
"v_min_u32_dpp v208, v69, v69 quad_perm:[2,3,0,1] row_mask:0xf bank_mask:0xf\n" \
"v_max_u32_dpp v209, v68, v68 quad_perm:[2,3,0,1] row_mask:0xf bank_mask:0xf\n" \
"v_max_u32_dpp v69, v69, v69 quad_perm:[2,3,0,1] row_mask:0xf bank_mask:0xf\n" \
"v_max_u32_e32 v68, v208, v209\n" \
"s_nop 0\n" \
"v_min_u32_dpp v208, v69, v69 row_half_mirror row_mask:0xf bank_mask:0xf\n" \
"v_max_u32_dpp v209, v68, v68 row_half_mirror row_mask:0xf bank_mask:0xf\n" \
"v_max_u32_dpp v69, v69, v69 row_half_mirror row_mask:0xf bank_mask:0xf\n" \
"v_max_u32_e32 v68, v208, v209\n" \
"s_nop 0\n" \
"v_min_u32_dpp v208, v69, v69 row_mirror row_mask:0xf bank_mask:0xf\n" \
"v_max_u32_dpp v209, v68, v68 row_mirror row_mask:0xf bank_mask:0xf\n" \
"v_max_u32_dpp v69, v69, v69 row_mirror row_mask:0xf bank_mask:0xf\n" \
"v_max_u32_e32 v68, v208, v209\n" \
"v_mov_b32_e32 v67, v69\n" \
"v_mov_b32_e32 v66, v68\n" \
"s_nop 0\n" \
"v_permlane16_swap_b32_e32 v69, v67\n" \
"v_permlane16_swap_b32_e32 v68, v66\n" \
"v_min_u32_e32 v208, v69, v67\n" \
"v_max_u32_e32 v69, v69, v67\n" \
"v_max3_u32 v68, v208, v68, v66\n" \
"v_mov_b32_e32 v67, v69\n" \
"v_mov_b32_e32 v66, v68\n" \
"s_nop 0\n" \
"v_permlane32_swap_b32_e32 v69, v67\n" \
"v_permlane32_swap_b32_e32 v68, v66\n" \
"v_min_u32_e32 v208, v69, v67\n" \
"v_max_u32_e32 v69, v69, v67\n" \
"v_max3_u32 v68, v208, v68, v66\n" \
"v_readfirstlane_b32 s46, v69\n" \
"v_readfirstlane_b32 s47, v68\n" \
"s_andn2_b32 s44, 0x7ff, s44\n" \
"s_andn2_b32 s45, 0x7ff, s45\n" \
"s_andn2_b32 s46, 0x7ff, s46\n" \
"s_andn2_b32 s47, 0x7ff, s47\n" \
"v_and_b32_e32 v213, 4, v222\n" \
"v_mov_b32_e32 v208, s44\n" \
"v_cmp_ne_u32_e32 vcc, 0, v213\n" \
"v_mov_b32_e32 v214, s45\n" \
"v_mov_b32_e32 v209, s46\n" \
"v_mov_b32_e32 v215, s47\n" \
"v_cndmask_b32_e32 v208, v208, v214, vcc\n" \
"v_cndmask_b32_e32 v209, v209, v215, vcc\n" \
"v_cmp_lt_u32_e32 vcc, 31, v222\n" \
"v_and_b32_e32 v211, 0xf0, %[kb0]\n" \
"s_lshl_b32 s62, %[wid], 9\n" \
"v_cndmask_b32_e32 v210, v208, v209, vcc\n" \
"v_lshrrev_b32_e32 %[vdn], 6, v210\n" \
"v_lshlrev_b32_e32 %[vdn], 9, %[vdn]\n" \
"v_lshlrev_b32_e32 v213, 11, v210\n" \
"v_lshrrev_b32_e32 v214, 1, v210\n" \
"v_and_b32_e32 v214, 0x80, v214\n" \
"v_lshrrev_b32_e32 v215, 1, v211\n" \
"v_and_b32_e32 v215, 56, v215\n" \
"v_add3_u32 v213, v213, v214, v215\n" \
"v_and_b32_e32 v215, 0x80, v211\n" \
"v_lshlrev_b32_e32 v215, 1, v215\n" \
"v_add3_u32 v80, v213, v215, s62\n" \
"v_xor_b32_e32 v81, 0x80, v80\n" \
"s_mov_b32 s63, 278528\n" \
"buffer_load_dword %[vxs], %[vdn], %[srdz], s63 offen offset:0 sc1\n" \
"global_load_dwordx2 v[88:89], v80, %[px] offset:0\n" \
"global_load_dwordx2 v[90:91], v80, %[px] offset:64\n" \
"global_load_dwordx2 v[92:93], v81, %[px] offset:0\n" \
"global_load_dwordx2 v[94:95], v81, %[px] offset:64\n" \
"v_and_b32_e32 v213, 0x100, v210\n" \
"s_add_u32 s63, s62, s40\n" \
"v_add3_u32 v213, v213, v211, s63\n" \
"v_xor_b32_e32 v214, 0x100, v213\n" \
"ds_read_b128 v[104:107], v213 offset:2048\n" \
"ds_read_b128 v[108:111], v214 offset:2048\n" \
"ds_read_b128 v[64:67], v213 offset:6144\n" \
"ds_read_b128 v[68:71], v213 offset:8192\n" \
"ds_read_b128 v[72:75], v214 offset:6144\n" \
"ds_read_b128 v[76:79], v214 offset:8192\n" \
"ds_read_b128 v[112:115], v213 offset:10240\n" \
"ds_read_b128 v[116:119], v213 offset:12288\n" \
"ds_read_b128 v[120:123], v214 offset:10240\n" \
"ds_read_b128 v[124:127], v214 offset:12288\n" \
"s_movk_i32 s63, 2112\n" \
"v_mad_u32_u24 %[oq0], v219, s63, v213\n" \
"v_mad_u32_u24 %[wo0], v219, s63, v214\n" \
"s_lshl_b32 s62, %[wid], 10\n" \
"s_add_u32 s62, s62, 7340032\n" \
"v_and_b32_e32 v215, 0x3b0, v217\n" \
"v_add_u32_e32 v215, s62, v215\n" \
"s_mov_b32 s63, 38016\n" \
"v_lshrrev_b32_e32 %[vct], 1, v208\n" \
"v_and_b32_e32 v208, 1, v208\n" \
"v_mad_u32_u24 %[vct], %[vct], s63, v215\n" \
"v_lshl_add_u32 %[vct], v208, 6, %[vct]\n" \
"v_lshrrev_b32_e32 %[rd0], 1, v209\n" \
"v_and_b32_e32 v209, 1, v209\n" \
"v_mad_u32_u24 %[rd0], %[rd0], s63, v215\n" \
"v_lshl_add_u32 %[rd0], v209, 6, %[rd0]\n" \
"s_waitcnt vmcnt(13) lgkmcnt(0)\n" \
"s_cmp_lg_u32 s50, 0\n" \
"s_cbranch_scc1 TLrw_%=\n" \
"s_waitcnt vmcnt(5)\n" \
"TLrw_%=:\n" \
"v_mfma_f32_16x16x32_bf16 v[208:211], v[230:233], a[196:199], 0\n" \
"v_mfma_f32_16x16x32_bf16 v[212:215], v[230:233], a[204:207], 0\n" \
"v_mfma_f32_16x16x32_bf16 v[208:211], v[234:237], a[200:203], v[208:211]\n" \
"v_mfma_f32_16x16x32_bf16 v[212:215], v[234:237], a[208:211], v[212:215]\n" \
"v_lshlrev_b32_e32 v96, 16, v104\n" \
"v_and_b32_e32 v97, s43, v104\n" \
"v_lshlrev_b32_e32 v98, 16, v105\n" \
"v_and_b32_e32 v99, s43, v105\n" \
"v_lshlrev_b32_e32 v100, 16, v106\n" \
"v_and_b32_e32 v101, s43, v106\n" \
"v_lshlrev_b32_e32 v102, 16, v107\n" \
"v_and_b32_e32 v103, s43, v107\n" \
"v_pk_fma_f32 v[0:1], v[208:209], v[128:129], v[0:1]\n" \
"v_pk_fma_f32 v[2:3], v[210:211], v[130:131], v[2:3]\n" \
"v_pk_fma_f32 v[8:9], v[212:213], v[136:137], v[8:9]\n" \
"v_pk_fma_f32 v[10:11], v[214:215], v[138:139], v[10:11]\n" \
"v_pk_mul_f32 v[6:7], v[0:1], v[160:161]\n" \
"v_pk_mul_f32 v[12:13], v[0:1], v[0:1]\n" \
"v_pk_add_f32 v[4:5], v[0:1], v[2:3]\n" \
"v_pk_fma_f32 v[6:7], v[2:3], v[162:163], v[6:7]\n" \
"v_pk_fma_f32 v[12:13], v[2:3], v[2:3], v[12:13]\n" \
"v_pk_add_f32 v[4:5], v[4:5], v[8:9]\n" \
"v_pk_fma_f32 v[6:7], v[8:9], v[168:169], v[6:7]\n" \
"v_pk_fma_f32 v[12:13], v[8:9], v[8:9], v[12:13]\n" \
"v_pk_add_f32 v[4:5], v[4:5], v[10:11]\n" \
"v_pk_fma_f32 v[6:7], v[10:11], v[170:171], v[6:7]\n" \
"v_pk_fma_f32 v[12:13], v[10:11], v[10:11], v[12:13]\n" \
"v_mfma_f32_16x16x32_bf16 v[208:211], v[230:233], a[212:215], 0\n" \
"v_mfma_f32_16x16x32_bf16 v[212:215], v[230:233], a[220:223], 0\n" \
"v_mfma_f32_16x16x32_bf16 v[208:211], v[234:237], a[216:219], v[208:211]\n" \
"v_mfma_f32_16x16x32_bf16 v[212:215], v[234:237], a[224:227], v[212:215]\n" \
"v_lshlrev_b32_e32 v104, 16, v108\n" \
"v_and_b32_e32 v105, s43, v108\n" \
"v_lshlrev_b32_e32 v106, 16, v109\n" \
"v_and_b32_e32 v107, s43, v109\n" \
"v_lshlrev_b32_e32 v108, 16, v110\n" \
"v_and_b32_e32 v109, s43, v110\n" \
"v_lshlrev_b32_e32 v110, 16, v111\n" \
"v_and_b32_e32 v111, s43, v111\n" \
"v_pk_fma_f32 v[16:17], v[208:209], v[132:133], v[16:17]\n" \
"v_pk_fma_f32 v[18:19], v[210:211], v[134:135], v[18:19]\n" \
"v_pk_fma_f32 v[24:25], v[212:213], v[140:141], v[24:25]\n" \
"v_pk_fma_f32 v[26:27], v[214:215], v[142:143], v[26:27]\n" \
"v_pk_add_f32 v[4:5], v[4:5], v[16:17]\n" \
"v_pk_fma_f32 v[6:7], v[16:17], v[164:165], v[6:7]\n" \
"v_pk_fma_f32 v[12:13], v[16:17], v[16:17], v[12:13]\n" \
"v_pk_add_f32 v[4:5], v[4:5], v[18:19]\n" \
"v_pk_fma_f32 v[6:7], v[18:19], v[166:167], v[6:7]\n" \
"v_pk_fma_f32 v[12:13], v[18:19], v[18:19], v[12:13]\n" \
"v_pk_add_f32 v[4:5], v[4:5], v[24:25]\n" \
"v_pk_fma_f32 v[6:7], v[24:25], v[172:173], v[6:7]\n" \
"v_pk_fma_f32 v[12:13], v[24:25], v[24:25], v[12:13]\n" \
"v_pk_add_f32 v[4:5], v[4:5], v[26:27]\n" \
"v_pk_fma_f32 v[6:7], v[26:27], v[174:175], v[6:7]\n" \
"v_pk_fma_f32 v[12:13], v[26:27], v[26:27], v[12:13]\n" \
"s_cmp_lg_u32 s50, 0\n" \
"s_cbranch_scc1 TLre_%=\n" \
"v_and_b32_e32 v208, 0x800, v229\n" \
"v_cmp_eq_u32_e32 vcc, 0, v208\n" \
"s_cbranch_vccz TLrl4_%=\n" \
"v_mov_b32_e32 v216, s40\n" \
"TLrq4_%=:\n" \
"buffer_load_dword v225, off, %[srdz], s53 offset:0 sc1\n" \
"s_mov_b32 s52, 10\n" \
"TLrs4_%=:\n" \
"ds_read_b32 v229, v216 offset:14336\n" \
"s_waitcnt lgkmcnt(0)\n" \
"v_and_b32_e32 v208, 0x800, v229\n" \
"v_cmp_eq_u32_e32 vcc, 0, v208\n" \
"s_cbranch_vccz TLrd4_%=\n" \
"s_sub_u32 s52, s52, 1\n" \
"s_cbranch_scc0 TLrs4_%=\n" \
"s_sub_u32 s51, s51, 1\n" \
"s_cselect_b32 s51, 0, s51\n" \
"s_waitcnt vmcnt(0)\n" \
"v_and_b32_e32 v208, 0x800, v225\n" \
"v_cmp_eq_u32_e32 vcc, 0, v208\n" \
"s_cbranch_scc1 TLrl4_%=\n" \
"s_cbranch_vccnz TLrq4_%=\n" \
"v_mov_b32_e32 v229, v225\n" \
"s_mov_b64 exec, 1\n" \
"ds_max_u32 v216, v225 offset:14336\n" \
"s_mov_b64 exec, -1\n" \
"TLrd4_%=:\n" \
"s_sleep 4\n" \
"TLrl4_%=:\n" \
"s_mov_b32 s63, 278528\n" \
"s_nop 0\n" \
"buffer_load_dword %[vxs], %[vdn], %[srdz], s63 offen offset:0 sc1\n" \
"buffer_load_dwordx4 a[164:167], v217, %[srdz], s54 offen offset:0 sc1\n" \
"buffer_load_dwordx4 a[168:171], v217, %[srdz], s54 offen offset:1024 sc1\n" \
"buffer_load_dwordx4 a[172:175], v217, %[srdz], s54 offen offset:2048 sc1\n" \
"buffer_load_dwordx4 a[176:179], v217, %[srdz], s54 offen offset:3072 sc1\n" \
"buffer_load_dwordx4 a[180:183], v217, %[srdz], s55 offen offset:0 sc1\n" \
"buffer_load_dwordx4 a[184:187], v217, %[srdz], s55 offen offset:1024 sc1\n" \
"buffer_load_dwordx4 a[188:191], v217, %[srdz], s55 offen offset:2048 sc1\n" \
"buffer_load_dwordx4 a[192:195], v217, %[srdz], s55 offen offset:3072 sc1\n" \
"s_waitcnt vmcnt(4)\n" \
"s_branch TLrf_%=\n" \
"TLre_%=:\n" \
"s_waitcnt vmcnt(9)\n" \
"TLrf_%=:\n" \
"v_mfma_f32_16x16x32_bf16 v[208:211], v[230:233], a[164:167], 0\n" \
"v_mfma_f32_16x16x32_bf16 v[212:215], v[230:233], a[172:175], 0\n" \
"v_mfma_f32_16x16x32_bf16 v[208:211], v[234:237], a[168:171], v[208:211]\n" \
"v_mfma_f32_16x16x32_bf16 v[212:215], v[234:237], a[176:179], v[212:215]\n" \
"s_lshl_b32 s62, %[wid], 3\n" \
"v_and_b32_e32 v216, 4, v222\n" \
"s_add_u32 s62, s62, s40\n" \
"v_lshlrev_b32_e32 v229, 4, v224\n" \
"v_add_u32_e32 v216, s62, v216\n" \
"s_movk_i32 s52, 0x1000\n" \
"s_nop 0\n" \
"v_pk_fma_f32 v[32:33], v[208:209], v[144:145], v[32:33]\n" \
"v_pk_fma_f32 v[34:35], v[210:211], v[146:147], v[34:35]\n" \
"v_pk_fma_f32 v[40:41], v[212:213], v[152:153], v[40:41]\n" \
"v_pk_fma_f32 v[42:43], v[214:215], v[154:155], v[42:43]\n" \
"v_pk_mul_f32 v[36:37], v[32:33], v[176:177]\n" \
"v_pk_mul_f32 v[38:39], v[32:33], v[32:33]\n" \
"v_pk_add_f32 v[14:15], v[32:33], v[34:35]\n" \
"v_pk_fma_f32 v[36:37], v[34:35], v[178:179], v[36:37]\n" \
"v_pk_fma_f32 v[38:39], v[34:35], v[34:35], v[38:39]\n" \
"v_pk_add_f32 v[14:15], v[14:15], v[40:41]\n" \
"v_pk_fma_f32 v[36:37], v[40:41], v[184:185], v[36:37]\n" \
"v_pk_fma_f32 v[38:39], v[40:41], v[40:41], v[38:39]\n" \
"v_pk_add_f32 v[14:15], v[14:15], v[42:43]\n" \
"v_pk_fma_f32 v[36:37], v[42:43], v[186:187], v[36:37]\n" \
"v_pk_fma_f32 v[38:39], v[42:43], v[42:43], v[38:39]\n" \
"s_waitcnt vmcnt(0)\n" \
"v_and_b32_e32 v225, 7, v222\n" \
"v_lshl_add_u32 v225, v225, 2, s40\n" \
"v_and_b32_e32 v44, 0x820, %[vxs]\n" \
"v_cmp_ne_u32_e32 vcc, 0x820, v44\n" \
"s_mov_b32 s59, 0\n" \
"s_cbranch_vccnz TLcn_%=\n" \
"s_mov_b32 s59, 1\n" \
"buffer_load_dwordx4 a[128:131], %[vct], %[srdz], 0 offen offset:0 sc1\n" \
"buffer_load_dwordx4 a[132:135], %[rd0], %[srdz], 0 offen offset:0 sc1\n" \
"buffer_load_dwordx4 a[136:139], %[vct], %[srdz], s56 offen offset:0 sc1\n" \
"buffer_load_dwordx4 a[140:143], %[rd0], %[srdz], s56 offen offset:0 sc1\n" \
"buffer_load_dwordx4 a[144:147], %[vct], %[srdz], s57 offen offset:0 sc1\n" \
"buffer_load_dwordx4 a[148:151], %[rd0], %[srdz], s57 offen offset:0 sc1\n" \
"buffer_load_dwordx4 a[152:155], %[vct], %[srdz], s58 offen offset:0 sc1\n" \
"buffer_load_dwordx4 a[156:159], %[rd0], %[srdz], s58 offen offset:0 sc1\n" \
"TLcn_%=:\n" \
"v_mfma_f32_16x16x32_bf16 v[208:211], v[230:233], a[180:183], 0\n" \
"v_mfma_f32_16x16x32_bf16 v[212:215], v[230:233], a[188:191], 0\n" \
"v_mfma_f32_16x16x32_bf16 v[208:211], v[234:237], a[184:187], v[208:211]\n" \
"v_mfma_f32_16x16x32_bf16 v[212:215], v[234:237], a[192:195], v[212:215]\n" \
"v_lshlrev_b32_e32 v80, 16, v88\n" \
"v_and_b32_e32 v81, s43, v88\n" \
"v_lshlrev_b32_e32 v82, 16, v89\n" \
"v_and_b32_e32 v83, s43, v89\n" \
"v_lshlrev_b32_e32 v84, 16, v90\n" \
"v_and_b32_e32 v85, s43, v90\n" \
"v_lshlrev_b32_e32 v86, 16, v91\n" \
"v_pk_fma_f32 v[48:49], v[208:209], v[148:149], v[48:49]\n" \
"v_pk_fma_f32 v[50:51], v[210:211], v[150:151], v[50:51]\n" \
"v_pk_fma_f32 v[56:57], v[212:213], v[156:157], v[56:57]\n" \
"v_pk_fma_f32 v[58:59], v[214:215], v[158:159], v[58:59]\n" \
"v_pk_add_f32 v[14:15], v[14:15], v[48:49]\n" \
"v_pk_fma_f32 v[36:37], v[48:49], v[180:181], v[36:37]\n" \
"v_pk_fma_f32 v[38:39], v[48:49], v[48:49], v[38:39]\n" \
"v_pk_add_f32 v[14:15], v[14:15], v[50:51]\n" \
"v_pk_fma_f32 v[36:37], v[50:51], v[182:183], v[36:37]\n" \
"v_pk_fma_f32 v[38:39], v[50:51], v[50:51], v[38:39]\n" \
"v_pk_add_f32 v[14:15], v[14:15], v[56:57]\n" \
"v_pk_fma_f32 v[36:37], v[56:57], v[188:189], v[36:37]\n" \
"v_pk_fma_f32 v[38:39], v[56:57], v[56:57], v[38:39]\n" \
"v_pk_add_f32 v[14:15], v[14:15], v[58:59]\n" \
"v_pk_fma_f32 v[36:37], v[58:59], v[190:191], v[36:37]\n" \
"v_pk_fma_f32 v[38:39], v[58:59], v[58:59], v[38:39]\n" \
"v_pk_add_f32 v[4:5], v[4:5], v[14:15]\n" \
"v_pk_add_f32 v[6:7], v[6:7], v[36:37]\n" \
"v_pk_add_f32 v[12:13], v[12:13], v[38:39]\n" \
"v_add_f32_e32 v4, v4, v5\n" \
"v_add_f32_e32 v6, v6, v7\n" \
"v_add_f32_e32 v12, v12, v13\n" \
"v_and_b32_e32 v87, s43, v91\n" \
"v_mov_b32_e32 v208, v12\n" \
"v_permlane32_swap_b32_e32 v4, v6\n" \
"v_lshlrev_b32_e32 v88, 16, v92\n" \
"v_permlane32_swap_b32_e32 v12, v208\n" \
"v_add_f32_e32 v4, v4, v6\n" \
"v_add_f32_e32 v12, v12, v208\n" \
"v_and_b32_e32 v89, s43, v92\n" \
"v_lshlrev_b32_e32 v90, 16, v93\n" \
"v_permlane16_swap_b32_e32 v4, v12\n" \
"v_add_f32_e32 v4, v4, v12\n" \
"v_and_b32_e32 v91, s43, v93\n" \
"v_lshlrev_b32_e32 v92, 16, v94\n" \
"v_add_f32_dpp v4, v4, v4 quad_perm:[1,0,3,2] row_mask:0xf bank_mask:0xf\n" \
"v_and_b32_e32 v93, s43, v94\n" \
"v_lshlrev_b32_e32 v94, 16, v95\n" \
"v_add_f32_dpp v4, v4, v4 quad_perm:[2,3,0,1] row_mask:0xf bank_mask:0xf\n" \
"v_and_b32_e32 v95, s43, v95\n" \
"s_nop 0\n" \
"v_add_f32_dpp v4, v4, v4 row_ror:8 row_mask:0xf bank_mask:0xf\n" \
"v_mov_b32_e32 v12, v4\n" \
"s_nop 0\n" \
"s_nop 0\n" \
"v_permlane16_swap_b32_e32 v4, v12\n" \
"v_mov_b32_e32 v6, v4\n" \
"v_mul_f32_e32 v12, 0x3a800000, v12\n" \
"s_nop 0\n" \
"v_permlane32_swap_b32_e32 v4, v6\n" \
"v_mul_f32_e32 v218, 0x3a800000, v4\n" \
"v_mul_f32_e32 v208, v218, v218\n" \
"v_fma_f32 v6, -v218, v223, v6\n" \
"v_sub_f32_e32 v12, v12, v208\n" \
"v_cmp_nle_f32_e32 vcc, v208, v12\n" \
"v_add_f32_e32 v12, 0x3727c5ac, v12\n" \
"v_rsq_f32_e32 v220, v12\n" \
"s_mov_b64 exec, 17\n" \
"v_mul_f32_e32 v210, v6, v220\n" \
"v_ashrrev_i32_e32 v208, 31, v210\n" \
"v_or_b32_e32 v208, 0x80000000, v208\n" \
"v_xor_b32_e32 v210, v210, v208\n" \
"v_and_b32_e32 v210, 0xfffff800, v210\n" \
"v_max_u32_e32 v210, 0x800, v210\n" \
"v_or_b32_e32 v210, v210, %[ta0]\n" \
"ds_write_b32 v216, v210 offset:14400\n" \
"s_mov_b64 exec, -1\n" \
"s_cbranch_vccnz TLrare_%=\n" \
"TLrok_%=:\n" \
"s_movk_i32 s52, 0x1000\n" \
"s_waitcnt lgkmcnt(0)\n" \
"s_barrier\n" \
"ds_read_b32 v209, v225 offset:14400\n" \
"s_waitcnt lgkmcnt(0)\n" \
"v_min_u32_dpp v208, v209, v209 quad_perm:[1,0,3,2] row_mask:0xf bank_mask:0xf\n" \
"v_max_u32_dpp v209, v209, v209 quad_perm:[1,0,3,2] row_mask:0xf bank_mask:0xf\n" \
"s_nop 1\n" \
"v_min_u32_dpp v210, v209, v209 quad_perm:[2,3,0,1] row_mask:0xf bank_mask:0xf\n" \
"v_max_u32_dpp v211, v208, v208 quad_perm:[2,3,0,1] row_mask:0xf bank_mask:0xf\n" \
"v_max_u32_dpp v209, v209, v209 quad_perm:[2,3,0,1] row_mask:0xf bank_mask:0xf\n" \
"v_max_u32_e32 v208, v210, v211\n" \
"s_nop 0\n" \
"v_min_u32_dpp v210, v209, v209 row_half_mirror row_mask:0xf bank_mask:0xf\n" \
"v_max_u32_dpp v211, v208, v208 row_half_mirror row_mask:0xf bank_mask:0xf\n" \
"v_max_u32_dpp v209, v209, v209 row_half_mirror row_mask:0xf bank_mask:0xf\n" \
"v_max_u32_e32 v208, v210, v211\n" \
"s_mov_b64 exec, 3\n" \
"buffer_atomic_or_x2 v[208:209], v229, %[srdz], s48 offen sc1\n" \
"s_mov_b64 exec, -1\n" \
"s_waitcnt vmcnt(1)\n" \
"TLcs_%=:\n" \
"v_mfma_f32_16x16x32_bf16 v[128:131], v[230:233], a[128:131], 0\n" \
"v_mfma_f32_16x16x32_bf16 v[132:135], v[230:233], a[136:139], 0\n" \
"v_mfma_f32_16x16x32_bf16 v[128:131], v[234:237], a[132:135], v[128:131]\n" \
"v_mfma_f32_16x16x32_bf16 v[132:135], v[234:237], a[140:143], v[132:135]\n" \
"v_mfma_f32_16x16x32_bf16 v[136:139], v[230:233], a[144:147], 0\n" \
"v_mfma_f32_16x16x32_bf16 v[140:143], v[230:233], a[152:155], 0\n" \
"v_mfma_f32_16x16x32_bf16 v[136:139], v[234:237], a[148:151], v[136:139]\n" \
"v_mfma_f32_16x16x32_bf16 v[140:143], v[234:237], a[156:159], v[140:143]\n" \
"s_nop 2\n" \
"v_pk_fma_f32 v[144:145], v[128:129], v[64:65], v[80:81]\n" \
"v_pk_fma_f32 v[146:147], v[130:131], v[66:67], v[82:83]\n" \
"v_pk_fma_f32 v[148:149], v[132:133], v[68:69], v[84:85]\n" \
"v_pk_fma_f32 v[150:151], v[134:135], v[70:71], v[86:87]\n" \
"v_pk_mul_f32 v[160:161], v[144:145], v[96:97]\n" \
"v_pk_mul_f32 v[162:163], v[146:147], v[98:99]\n" \
"v_pk_mul_f32 v[164:165], v[148:149], v[100:101]\n" \
"v_pk_mul_f32 v[166:167], v[150:151], v[102:103]\n" \
"v_cvt_pk_bf16_f32 v176, v160, v161\n" \
"v_cvt_pk_bf16_f32 v177, v162, v163\n" \
"v_cvt_pk_bf16_f32 v178, v164, v165\n" \
"v_cvt_pk_bf16_f32 v179, v166, v167\n" \
"ds_write_b128 %[oq0], v[176:179] offset:16384\n" \
"v_pk_fma_f32 v[152:153], v[136:137], v[72:73], v[88:89]\n" \
"v_pk_fma_f32 v[154:155], v[138:139], v[74:75], v[90:91]\n" \
"v_pk_fma_f32 v[156:157], v[140:141], v[76:77], v[92:93]\n" \
"v_pk_fma_f32 v[158:159], v[142:143], v[78:79], v[94:95]\n" \
"v_pk_mul_f32 v[168:169], v[152:153], v[104:105]\n" \
"v_pk_mul_f32 v[170:171], v[154:155], v[106:107]\n" \
"v_pk_mul_f32 v[172:173], v[156:157], v[108:109]\n" \
"v_pk_mul_f32 v[174:175], v[158:159], v[110:111]\n" \
"v_cvt_pk_bf16_f32 v180, v168, v169\n" \
"v_cvt_pk_bf16_f32 v181, v170, v171\n" \
"v_cvt_pk_bf16_f32 v182, v172, v173\n" \
"v_cvt_pk_bf16_f32 v183, v174, v175\n" \
"ds_write_b128 %[wo0], v[180:183] offset:16384\n" \
"ds_read_b128 v[4:7], v226 offset:0\n" \
"ds_read_b128 v[12:15], v226 offset:64\n" \
"ds_read_b128 v[20:23], v226 offset:128\n" \
"ds_read_b128 v[28:31], v226 offset:192\n" \
"ds_read_b128 v[36:39], v226 offset:256\n" \
"ds_read_b128 v[44:47], v226 offset:320\n" \
"ds_read_b128 v[52:55], v226 offset:384\n" \
"ds_read_b128 v[60:63], v226 offset:448\n" \
"v_cmp_u_f32_e32 vcc, v128, v132\n" \
"v_cmp_u_f32_e64 s[62:63], v136, v140\n" \
"s_or_b64 vcc, vcc, s[62:63]\n" \
"s_cbranch_scc1 TLcx_%=\n" \
"TLco_%=:\n" \
"v_pk_add_f32 v[184:185], v[144:145], v[146:147]\n" \
"v_pk_mul_f32 v[186:187], v[144:145], v[144:145]\n" \
"v_pk_mul_f32 v[188:189], v[144:145], v[112:113]\n" \
"v_pk_fma_f32 v[186:187], v[146:147], v[146:147], v[186:187]\n" \
"v_pk_fma_f32 v[188:189], v[146:147], v[114:115], v[188:189]\n" \
"v_pk_add_f32 v[184:185], v[184:185], v[148:149]\n" \
"v_pk_fma_f32 v[186:187], v[148:149], v[148:149], v[186:187]\n" \
"v_pk_fma_f32 v[188:189], v[148:149], v[116:117], v[188:189]\n" \
"v_pk_add_f32 v[184:185], v[184:185], v[150:151]\n" \
"v_pk_fma_f32 v[186:187], v[150:151], v[150:151], v[186:187]\n" \
"v_pk_fma_f32 v[188:189], v[150:151], v[118:119], v[188:189]\n" \
"v_pk_add_f32 v[184:185], v[184:185], v[152:153]\n" \
"v_pk_fma_f32 v[186:187], v[152:153], v[152:153], v[186:187]\n" \
"v_pk_fma_f32 v[188:189], v[152:153], v[120:121], v[188:189]\n" \
"v_pk_add_f32 v[184:185], v[184:185], v[154:155]\n" \
"v_pk_fma_f32 v[186:187], v[154:155], v[154:155], v[186:187]\n" \
"v_pk_fma_f32 v[188:189], v[154:155], v[122:123], v[188:189]\n" \
"v_pk_add_f32 v[184:185], v[184:185], v[156:157]\n" \
"v_pk_fma_f32 v[186:187], v[156:157], v[156:157], v[186:187]\n" \
"v_pk_fma_f32 v[188:189], v[156:157], v[124:125], v[188:189]\n" \
"v_pk_add_f32 v[184:185], v[184:185], v[158:159]\n" \
"v_pk_fma_f32 v[186:187], v[158:159], v[158:159], v[186:187]\n" \
"v_pk_fma_f32 v[188:189], v[158:159], v[126:127], v[188:189]\n" \
"v_add_f32_e32 v184, v184, v185\n" \
".if . - adaln_body != 65536\n" \
".error \"size model\"\n" \
".endif\n" \
"Lk15_%=:\n" \
"s_cbranch_execz Lk16_%=\n" \
"s_nop 0\n" \
"v_add_f32_e32 v186, v186, v187\n" \
"v_add_f32_e32 v188, v188, v189\n" \
"s_waitcnt lgkmcnt(7)\n" \
"v_mfma_f32_16x16x32_bf16 v[208:211], a[64:67], v[4:7], 0\n" \
"v_mfma_f32_16x16x32_bf16 v[212:215], a[96:99], v[4:7], 0\n" \
"v_permlane16_swap_b32_e32 v184, v186\n" \
"v_mov_b32_e32 v189, v188\n" \
"s_waitcnt lgkmcnt(6)\n" \
"v_mfma_f32_16x16x32_bf16 v[208:211], a[68:71], v[12:15], v[208:211]\n" \
"v_mfma_f32_16x16x32_bf16 v[212:215], a[100:103], v[12:15], v[212:215]\n" \
"v_add_f32_e32 v184, v184, v186\n" \
"v_permlane16_swap_b32_e32 v188, v189\n" \
"s_waitcnt lgkmcnt(5)\n" \
"v_mfma_f32_16x16x32_bf16 v[208:211], a[72:75], v[20:23], v[208:211]\n" \
"v_mfma_f32_16x16x32_bf16 v[212:215], a[104:107], v[20:23], v[212:215]\n" \
"v_add_f32_e32 v188, v188, v189\n" \
"v_add_u32_e32 v190, 1632, %[kd0]\n" \
"s_waitcnt lgkmcnt(4)\n" \
"v_mfma_f32_16x16x32_bf16 v[208:211], a[76:79], v[28:31], v[208:211]\n" \
"v_mfma_f32_16x16x32_bf16 v[212:215], a[108:111], v[28:31], v[212:215]\n" \
"v_add_f32_dpp v184, v184, v184 quad_perm:[1,0,3,2] row_mask:0xf bank_mask:0xf\n" \
"v_add_f32_dpp v188, v188, v188 quad_perm:[1,0,3,2] row_mask:0xf bank_mask:0xf\n" \
"s_waitcnt lgkmcnt(3)\n" \
"v_mfma_f32_16x16x32_bf16 v[208:211], a[80:83], v[36:39], v[208:211]\n" \
"v_mfma_f32_16x16x32_bf16 v[212:215], a[112:115], v[36:39], v[212:215]\n" \
"v_add_f32_dpp v184, v184, v184 quad_perm:[2,3,0,1] row_mask:0xf bank_mask:0xf\n" \
"v_add_f32_dpp v188, v188, v188 quad_perm:[2,3,0,1] row_mask:0xf bank_mask:0xf\n" \
"s_waitcnt lgkmcnt(2)\n" \
"v_mfma_f32_16x16x32_bf16 v[208:211], a[84:87], v[44:47], v[208:211]\n" \
"v_mfma_f32_16x16x32_bf16 v[212:215], a[116:119], v[44:47], v[212:215]\n" \
"v_add_f32_dpp v184, v184, v184 row_ror:8 row_mask:0xf bank_mask:0xf\n" \
"v_add_f32_dpp v188, v188, v188 row_ror:8 row_mask:0xf bank_mask:0xf\n" \
"s_waitcnt lgkmcnt(1)\n" \
"v_mfma_f32_16x16x32_bf16 v[208:211], a[88:91], v[52:55], v[208:211]\n" \
"v_mfma_f32_16x16x32_bf16 v[212:215], a[120:123], v[52:55], v[212:215]\n" \
"s_mov_b32 vcc_lo, 0xaaaaaaaa\n" \
"s_mov_b32 vcc_hi, 0xaaaaaaaa\n" \
"v_cndmask_b32_e32 v184, v184, v188, vcc\n" \
"s_waitcnt lgkmcnt(0)\n" \
"v_mfma_f32_16x16x32_bf16 v[208:211], a[92:95], v[60:63], v[208:211]\n" \
"v_mfma_f32_16x16x32_bf16 v[212:215], a[124:127], v[60:63], v[212:215]\n" \
"s_mov_b32 exec_lo, 0x330033\n" \
"s_mov_b32 exec_hi, 0x330033\n" \
"ds_write_b32 v228, v184 offset:0\n" \
"s_mov_b32 exec_lo, 0x3f003f\n" \
"s_mov_b32 exec_hi, 0x3f003f\n" \
"s_nop 1\n" \
"ds_write2_b32 %[kd0], v208, v209 offset0:0 offset1:4\n" \
"ds_write2_b32 %[kd0], v210, v211 offset0:8 offset1:12\n" \
"ds_write2_b32 v190, v212, v213 offset0:0 offset1:4\n" \
"ds_write2_b32 v190, v214, v215 offset0:8 offset1:12\n" \
"s_mov_b64 exec, -1\n" \
"s_waitcnt lgkmcnt(0)\n" \
"s_barrier\n" \
"ds_read_b128 v[4:7], %[vg0] offset:0\n" \
"ds_read_b128 v[12:15], %[vg0] offset:16\n" \
"ds_read_b128 v[20:23], %[vg0] offset:32\n" \
"ds_read_b128 v[28:31], v221 offset:0\n" \
"ds_read_b128 v[36:39], %[vx0] offset:1088\n" \
"ds_read_b128 v[44:47], %[vx0] offset:1360\n" \
"ds_read_b128 v[52:55], v221 offset:1632\n" \
"ds_read_b128 v[60:63], %[vx0] offset:2720\n" \
"ds_read_b128 v[128:131], %[vx0] offset:2992\n" \
"v_bfe_u32 v208, v222, 3, 1\n" \
"v_and_b32_e32 v209, 1, v222\n" \
"v_lshl_or_b32 v208, v208, 1, v209\n" \
"s_waitcnt lgkmcnt(6)\n" \
"v_pk_add_f32 v[4:5], v[4:5], v[6:7]\n" \
"v_pk_add_f32 v[12:13], v[12:13], v[14:15]\n" \
"v_pk_add_f32 v[20:21], v[20:21], v[22:23]\n" \
"v_add_f32_e32 v4, v4, v5\n" \
"v_add_f32_e32 v12, v12, v13\n" \
"v_add_f32_e32 v20, v20, v21\n" \
"v_mul_f32_e32 v4, 0x3a800000, v4\n" \
"v_mul_f32_e32 v12, 0x3a800000, v12\n" \
"v_mul_f32_e32 v209, v4, v4\n" \
"v_fma_f32 v20, -v4, v223, v20\n" \
"v_sub_f32_e32 v12, v12, v209\n" \
"s_waitcnt lgkmcnt(3)\n" \
"v_cmp_nle_f32_e64 s[50:51], v209, v12\n" \
"v_add_f32_e32 v12, 0x3727c5ac, v12\n" \
"v_rsq_f32_e32 v132, v12\n" \
"v_pk_add_f32 v[28:29], v[28:29], v[30:31]\n" \
"v_pk_add_f32 v[36:37], v[36:37], v[38:39]\n" \
"v_pk_add_f32 v[44:45], v[44:45], v[46:47]\n" \
"v_add_f32_e32 v28, v28, v29\n" \
"v_add_f32_e32 v36, v36, v37\n" \
"v_add_f32_e32 v44, v44, v45\n" \
"v_mul_f32_e32 v20, v20, v132\n" \
"s_waitcnt lgkmcnt(0)\n" \
"v_pk_add_f32 v[52:53], v[52:53], v[54:55]\n" \
"v_ashrrev_i32_e32 v209, 31, v20\n" \
"v_pk_add_f32 v[60:61], v[60:61], v[62:63]\n" \
"v_or_b32_e32 v209, 0x80000000, v209\n" \
"v_pk_add_f32 v[128:129], v[128:129], v[130:131]\n" \
"v_xor_b32_e32 v210, v20, v209\n" \
"v_add_f32_e32 v52, v52, v53\n" \
"v_and_b32_e32 v210, 0xfffff800, v210\n" \
"v_add_f32_e32 v60, v60, v61\n" \
"v_max_u32_e32 v210, 0x800, v210\n" \
"v_add_f32_e32 v128, v128, v129\n" \
"v_or_b32_e32 v210, v210, v208\n" \
"v_fma_f32 v28, -v4, v36, v28\n" \
"v_fma_f32 v52, -v4, v60, v52\n" \
"v_fma_f32 v28, v28, v132, v44\n" \
"v_min_u32_dpp v211, v210, v210 quad_perm:[1,0,3,2] row_mask:0xf bank_mask:0xf\n" \
"v_fma_f32 v52, v52, v132, v128\n" \
"v_max_u32_dpp v210, v210, v210 quad_perm:[1,0,3,2] row_mask:0xf bank_mask:0xf\n" \
"v_cvt_pk_bf16_f32 v28, 0, v28\n" \
"v_cvt_pk_bf16_f32 v52, 0, v52\n" \
"v_min_u32_dpp v212, v210, v210 row_ror:8 row_mask:0xf bank_mask:0xf\n" \
"v_mul_f32_e32 v214, 0xbfb8aa3b, v28\n" \
"v_max_u32_dpp v213, v211, v211 row_ror:8 row_mask:0xf bank_mask:0xf\n" \
"v_exp_f32_e32 v214, v214\n" \
"v_max_u32_dpp v210, v210, v210 row_ror:8 row_mask:0xf bank_mask:0xf\n" \
"v_add_f32_e32 v214, 1.0, v214\n" \
"v_max_u32_e32 v211, v212, v213\n" \
"v_rcp_f32_e32 v214, v214\n" \
"s_nop 0\n" \
"v_mul_f32_e32 v28, v28, v214\n" \
"v_cvt_pk_bf16_f32 v28, 0, v28\n" \
"v_mul_f32_e32 v28, v28, v52\n" \
"v_cvt_pk_bf16_f32 v28, 0, v28\n" \
"v_cndmask_b32_e32 v133, v210, v211, vcc\n" \
"v_readfirstlane_b32 s60, v210\n" \
"v_readfirstlane_b32 s61, v211\n" \
"v_xor_b32_e32 v212, v133, v208\n" \
"v_and_b32_e32 v213, 1, v212\n" \
"v_and_b32_e32 v212, 2, v212\n" \
"v_cmp_ne_u32_e64 s[54:55], 0, v213\n" \
"v_cmp_ne_u32_e64 s[56:57], 0, v212\n" \
"s_mov_b32 s62, 0x07060302\n" \
"v_mov_b32_dpp v213, v28 quad_perm:[1,0,3,2] row_mask:0xf bank_mask:0xf\n" \
"v_cndmask_b32_e64 v28, v28, v213, s[54:55]\n" \
"s_nop 1\n" \
"v_mov_b32_dpp v214, v28 row_ror:8 row_mask:0xf bank_mask:0xf\n" \
"v_cndmask_b32_e64 v28, v28, v214, s[56:57]\n" \
"s_mov_b32 vcc_lo, 0xcccccccc\n" \
"s_mov_b32 vcc_hi, 0xcccccccc\n" \
"v_mov_b32_dpp v213, v28 quad_perm:[2,3,0,1] row_mask:0xf bank_mask:0xf\n" \
"s_mov_b32 s58, 0x30003\n" \
"s_mov_b32 s59, 0xc000c\n" \
"v_perm_b32 v214, v213, v28, s62\n" \
"v_perm_b32 v215, v28, v213, s62\n" \
"v_cndmask_b32_e32 v214, v214, v215, vcc\n" \
"s_mov_b32 vcc_lo, 0xf0f0f0f0\n" \
"s_mov_b32 vcc_hi, 0xf0f0f0f0\n" \
"v_mov_b32_dpp v213, v214 row_ror:4 row_mask:0xf bank_mask:0xf\n" \
"v_cndmask_b32_e32 v164, v214, v213, vcc\n" \
"v_cndmask_b32_e32 v165, v213, v214, vcc\n" \
"v_mov_b32_e32 v166, v164\n" \
"v_mov_b32_e32 v167, v165\n" \
"s_nop 0\n" \
"v_permlane32_swap_b32_e32 v164, v166\n" \
"v_permlane32_swap_b32_e32 v165, v167\n" \
"s_nop 0\n" \
"v_permlane16_swap_b32_e32 v164, v166\n" \
"v_permlane16_swap_b32_e32 v165, v167\n" \
"v_cndmask_b32_e64 v176, 0, v164, s[58:59]\n" \
"v_cndmask_b32_e64 v177, 0, v165, s[58:59]\n" \
"v_cndmask_b32_e64 v178, 0, v166, s[58:59]\n" \
"v_cndmask_b32_e64 v179, 0, v167, s[58:59]\n" \
"s_lshl_b64 s[58:59], s[58:59], 4\n" \
"v_cndmask_b32_e64 v180, 0, v164, s[58:59]\n" \
"v_cndmask_b32_e64 v181, 0, v165, s[58:59]\n" \
"v_cndmask_b32_e64 v182, 0, v166, s[58:59]\n" \
"v_cndmask_b32_e64 v183, 0, v167, s[58:59]\n" \
"s_lshl_b64 s[58:59], s[58:59], 4\n" \
"v_mfma_f32_16x16x32_bf16 v[168:171], v[176:179], a[0:3], 0\n" \
"v_mfma_f32_16x16x32_bf16 v[172:175], v[176:179], a[16:19], 0\n" \
"v_cndmask_b32_e64 v184, 0, v164, s[58:59]\n" \
"v_cndmask_b32_e64 v185, 0, v165, s[58:59]\n" \
"v_cndmask_b32_e64 v186, 0, v166, s[58:59]\n" \
"v_cndmask_b32_e64 v187, 0, v167, s[58:59]\n" \
"s_lshl_b64 s[58:59], s[58:59], 4\n" \
"v_mfma_f32_16x16x32_bf16 v[168:171], v[180:183], a[4:7], v[168:171]\n" \
"v_mfma_f32_16x16x32_bf16 v[172:175], v[180:183], a[20:23], v[172:175]\n" \
"v_cndmask_b32_e64 v188, 0, v164, s[58:59]\n" \
"v_cndmask_b32_e64 v189, 0, v165, s[58:59]\n" \
"v_cndmask_b32_e64 v190, 0, v166, s[58:59]\n" \
"v_cndmask_b32_e64 v191, 0, v167, s[58:59]\n" \
"v_mfma_f32_16x16x32_bf16 v[168:171], v[184:187], a[8:11], v[168:171]\n" \
"v_mfma_f32_16x16x32_bf16 v[172:175], v[184:187], a[24:27], v[172:175]\n" \
"v_mfma_f32_16x16x32_bf16 v[168:171], v[188:191], a[12:15], v[168:171]\n" \
"v_mfma_f32_16x16x32_bf16 v[172:175], v[188:191], a[28:31], v[172:175]\n" \
"ds_read_b128 v[64:67], %[kb0] offset:0\n" \
"ds_read_b128 v[68:71], %[kb0] offset:1024\n" \
"ds_read_b128 v[72:75], %[vvb] offset:0\n" \
"ds_read_b128 v[76:79], %[vvb] offset:1024\n" \
"ds_read_b128 v[80:83], %[kb0] offset:4096\n" \
"ds_read_b128 v[84:87], %[kb0] offset:5120\n" \
"ds_read_b128 v[88:91], %[vvb] offset:4096\n" \
"ds_read_b128 v[92:95], %[vvb] offset:5120\n" \
"v_and_or_b32 v168, v168, -4, 1\n" \
"v_and_or_b32 v169, v169, -4, 1\n" \
"v_and_or_b32 v170, v170, -4, 1\n" \
"v_and_or_b32 v171, v171, -4, 1\n" \
"buffer_store_dwordx4 v[168:171], v217, %[srdz], s69 offen offset:0 sc1\n" \
"v_and_or_b32 v172, v172, -4, 1\n" \
"v_and_or_b32 v173, v173, -4, 1\n" \
"v_and_or_b32 v174, v174, -4, 1\n" \
"v_and_or_b32 v175, v175, -4, 1\n" \
"buffer_store_dwordx4 v[172:175], v217, %[srdz], s69 offen offset:1024 sc1\n" \
"s_movk_i32 s62, -128\n" \
"s_sub_u32 s63, 65536, s40\n" \
"v_mul_i32_i24_e64 v208, v219, s62\n" \
"v_add_u32_e32 v208, s63, v208\n" \
"v_lshl_add_u32 v209, %[oq0], 1, v208\n" \
"v_lshl_add_u32 v210, %[wo0], 1, v208\n" \
"ds_write_b128 v209, v[144:147] offset:0\n" \
"ds_write_b128 v209, v[148:151] offset:16\n" \
"ds_write_b128 v210, v[152:155] offset:0\n" \
"ds_write_b128 v210, v[156:159] offset:16\n" \
"v_mov_b32_dpp v211, v4 quad_perm:[1,0,3,2] row_mask:0xf bank_mask:0xf\n" \
"v_mov_b32_dpp v213, v132 quad_perm:[1,0,3,2] row_mask:0xf bank_mask:0xf\n" \
"v_mov_b32_dpp v215, v20 quad_perm:[1,0,3,2] row_mask:0xf bank_mask:0xf\n" \
"v_cndmask_b32_e64 v4, v4, v211, s[54:55]\n" \
"v_cndmask_b32_e64 v132, v132, v213, s[54:55]\n" \
"v_cndmask_b32_e64 v20, v20, v215, s[54:55]\n" \
"v_mov_b32_dpp v212, v4 row_ror:8 row_mask:0xf bank_mask:0xf\n" \
"v_mov_b32_dpp v214, v132 row_ror:8 row_mask:0xf bank_mask:0xf\n" \
"v_mov_b32_dpp v208, v20 row_ror:8 row_mask:0xf bank_mask:0xf\n" \
"v_cndmask_b32_e64 v4, v4, v212, s[56:57]\n" \
"v_cndmask_b32_e64 v132, v132, v214, s[56:57]\n" \
"v_cndmask_b32_e64 v20, v20, v208, s[56:57]\n" \
"v_mov_b32_e32 v126, v132\n" \
"v_and_b32_e32 v125, 3, v133\n" \
"v_mul_f32_e64 v208, -v218, v220\n" \
"s_bitcmp1_b32 s60, 0\n" \
"s_cselect_b32 s56, s45, s44\n" \
"s_cselect_b32 s57, s47, s46\n" \
"s_bitcmp1_b32 s60, 1\n" \
"s_cselect_b32 s60, s57, s56\n" \
"s_bitcmp1_b32 s61, 0\n" \
"s_cselect_b32 s56, s45, s44\n" \
"s_cselect_b32 s57, s47, s46\n" \
"s_bitcmp1_b32 s61, 1\n" \
"s_cselect_b32 s61, s57, s56\n" \
"s_cmp_lg_u64 s[50:51], 0\n" \
"s_cselect_b32 s60, 0x1000, s60\n" \
"s_cselect_b32 s61, 0x1000, s61\n" \
"v_pk_fma_f32 v[128:129], v[0:1], v[220:221], v[208:209] op_sel_hi:[1,0,0]\n" \
"v_pk_fma_f32 v[130:131], v[2:3], v[220:221], v[208:209] op_sel_hi:[1,0,0]\n" \
"v_pk_fma_f32 v[132:133], v[8:9], v[220:221], v[208:209] op_sel_hi:[1,0,0]\n" \
"v_pk_fma_f32 v[134:135], v[10:11], v[220:221], v[208:209] op_sel_hi:[1,0,0]\n" \
"v_pk_fma_f32 v[136:137], v[16:17], v[220:221], v[208:209] op_sel_hi:[1,0,0]\n" \
"v_pk_fma_f32 v[138:139], v[18:19], v[220:221], v[208:209] op_sel_hi:[1,0,0]\n" \
"v_pk_fma_f32 v[140:141], v[24:25], v[220:221], v[208:209] op_sel_hi:[1,0,0]\n" \
"v_pk_fma_f32 v[142:143], v[26:27], v[220:221], v[208:209] op_sel_hi:[1,0,0]\n" \
"v_pk_fma_f32 v[144:145], v[32:33], v[220:221], v[208:209] op_sel_hi:[1,0,0]\n" \
"v_pk_fma_f32 v[146:147], v[34:35], v[220:221], v[208:209] op_sel_hi:[1,0,0]\n" \
"v_pk_fma_f32 v[148:149], v[40:41], v[220:221], v[208:209] op_sel_hi:[1,0,0]\n" \
"v_pk_fma_f32 v[150:151], v[42:43], v[220:221], v[208:209] op_sel_hi:[1,0,0]\n" \
"v_pk_fma_f32 v[152:153], v[48:49], v[220:221], v[208:209] op_sel_hi:[1,0,0]\n" \
"v_pk_fma_f32 v[154:155], v[50:51], v[220:221], v[208:209] op_sel_hi:[1,0,0]\n" \
"v_pk_fma_f32 v[156:157], v[56:57], v[220:221], v[208:209] op_sel_hi:[1,0,0]\n" \
"v_pk_fma_f32 v[158:159], v[58:59], v[220:221], v[208:209] op_sel_hi:[1,0,0]\n" \
"v_sub_u32_e32 v216, 0x7ff, %[ta0]\n" \
"v_cmp_eq_u32_e64 s[56:57], s60, v216\n" \
"v_cmp_eq_u32_e64 s[58:59], s61, v216\n" \
"s_cmp_eq_u32 %[wid], 3\n" \
"s_cselect_b32 s49, s49, 0x80000000\n" \
"v_lshrrev_b32_e32 v216, 2, v224\n" \
"s_mov_b32 s51, 0x10000\n" \
"s_waitcnt lgkmcnt(0)\n" \
"v_lshlrev_b32_e32 v160, 16, v192\n" \
"v_and_b32_e32 v161, s43, v192\n" \
"v_lshlrev_b32_e32 v176, 16, v64\n" \
"v_and_b32_e32 v177, s43, v64\n" \
"v_lshlrev_b32_e32 v162, 16, v193\n" \
"v_and_b32_e32 v163, s43, v193\n" \
"v_lshlrev_b32_e32 v178, 16, v65\n" \
"v_and_b32_e32 v179, s43, v65\n" \
"v_lshlrev_b32_e32 v164, 16, v196\n" \
"v_and_b32_e32 v165, s43, v196\n" \
"v_lshlrev_b32_e32 v180, 16, v68\n" \
"v_and_b32_e32 v181, s43, v68\n" \
"v_lshlrev_b32_e32 v166, 16, v197\n" \
"v_and_b32_e32 v167, s43, v197\n" \
"v_lshlrev_b32_e32 v182, 16, v69\n" \
"v_and_b32_e32 v183, s43, v69\n" \
"v_pk_fma_f32 v[128:129], v[128:129], v[160:161], v[176:177]\n" \
"v_pk_fma_f32 v[130:131], v[130:131], v[162:163], v[178:179]\n" \
"v_pk_fma_f32 v[132:133], v[132:133], v[164:165], v[180:181]\n" \
"v_pk_fma_f32 v[134:135], v[134:135], v[166:167], v[182:183]\n" \
"v_lshlrev_b32_e32 v184, 16, v80\n" \
"v_and_b32_e32 v185, s43, v80\n" \
"v_lshlrev_b32_e32 v186, 16, v81\n" \
"v_and_b32_e32 v187, s43, v81\n" \
"v_lshlrev_b32_e32 v188, 16, v84\n" \
"v_and_b32_e32 v189, s43, v84\n" \
"v_lshlrev_b32_e32 v190, 16, v85\n" \
"v_and_b32_e32 v191, s43, v85\n" \
"v_pk_fma_f32 v[128:129], v[128:129], v[184:185], v[0:1]\n" \
"v_pk_fma_f32 v[130:131], v[130:131], v[186:187], v[2:3]\n" \
"v_pk_fma_f32 v[132:133], v[132:133], v[188:189], v[8:9]\n" \
"v_pk_fma_f32 v[134:135], v[134:135], v[190:191], v[10:11]\n" \
"v_cvt_pk_bf16_f32 v128, v128, v129\n" \
"v_cvt_pk_bf16_f32 v130, v130, v131\n" \
"v_cvt_pk_bf16_f32 v132, v132, v133\n" \
"v_cvt_pk_bf16_f32 v134, v134, v135\n" \
"v_lshlrev_b32_e32 v160, 16, v194\n" \
"v_and_b32_e32 v161, s43, v194\n" \
"v_lshlrev_b32_e32 v176, 16, v66\n" \
"v_and_b32_e32 v177, s43, v66\n" \
"v_lshlrev_b32_e32 v162, 16, v195\n" \
"v_and_b32_e32 v163, s43, v195\n" \
"v_lshlrev_b32_e32 v178, 16, v67\n" \
"v_and_b32_e32 v179, s43, v67\n" \
"v_lshlrev_b32_e32 v164, 16, v198\n" \
"v_and_b32_e32 v165, s43, v198\n" \
"v_lshlrev_b32_e32 v180, 16, v70\n" \
"v_and_b32_e32 v181, s43, v70\n" \
"v_lshlrev_b32_e32 v166, 16, v199\n" \
"v_and_b32_e32 v167, s43, v199\n" \
"v_lshlrev_b32_e32 v182, 16, v71\n" \
"v_and_b32_e32 v183, s43, v71\n" \
"v_pk_fma_f32 v[136:137], v[136:137], v[160:161], v[176:177]\n" \
"v_pk_fma_f32 v[138:139], v[138:139], v[162:163], v[178:179]\n" \
"v_pk_fma_f32 v[140:141], v[140:141], v[164:165], v[180:181]\n" \
"v_pk_fma_f32 v[142:143], v[142:143], v[166:167], v[182:183]\n" \
"v_lshlrev_b32_e32 v184, 16, v82\n" \
"v_and_b32_e32 v185, s43, v82\n" \
"v_lshlrev_b32_e32 v186, 16, v83\n" \
"v_and_b32_e32 v187, s43, v83\n" \
"v_lshlrev_b32_e32 v188, 16, v86\n" \
"v_and_b32_e32 v189, s43, v86\n" \
"v_lshlrev_b32_e32 v190, 16, v87\n" \
"v_and_b32_e32 v191, s43, v87\n" \
"v_pk_fma_f32 v[136:137], v[136:137], v[184:185], v[16:17]\n" \
"v_pk_fma_f32 v[138:139], v[138:139], v[186:187], v[18:19]\n" \
"v_pk_fma_f32 v[140:141], v[140:141], v[188:189], v[24:25]\n" \
"v_pk_fma_f32 v[142:143], v[142:143], v[190:191], v[26:27]\n" \
"v_cvt_pk_bf16_f32 v136, v136, v137\n" \
"v_cvt_pk_bf16_f32 v138, v138, v139\n" \
"v_cvt_pk_bf16_f32 v140, v140, v141\n" \
"v_cvt_pk_bf16_f32 v142, v142, v143\n" \
"buffer_load_dwordx4 v[112:115], v216, %[srdz], s49 offen offset:0 sc1\n" \
"buffer_load_dwordx4 v[116:119], v216, %[srdz], s49 offen offset:16 sc1\n" \
"v_lshlrev_b32_e32 v160, 16, v200\n" \
"v_and_b32_e32 v161, s43, v200\n" \
"v_lshlrev_b32_e32 v176, 16, v72\n" \
"v_and_b32_e32 v177, s43, v72\n" \
"v_lshlrev_b32_e32 v162, 16, v201\n" \
"v_and_b32_e32 v163, s43, v201\n" \
"v_lshlrev_b32_e32 v178, 16, v73\n" \
"v_and_b32_e32 v179, s43, v73\n" \
"v_lshlrev_b32_e32 v164, 16, v204\n" \
"v_and_b32_e32 v165, s43, v204\n" \
"v_lshlrev_b32_e32 v180, 16, v76\n" \
"v_and_b32_e32 v181, s43, v76\n" \
"v_lshlrev_b32_e32 v166, 16, v205\n" \
"v_and_b32_e32 v167, s43, v205\n" \
"v_lshlrev_b32_e32 v182, 16, v77\n" \
"v_and_b32_e32 v183, s43, v77\n" \
"v_pk_fma_f32 v[144:145], v[144:145], v[160:161], v[176:177]\n" \
"v_pk_fma_f32 v[146:147], v[146:147], v[162:163], v[178:179]\n" \
"v_pk_fma_f32 v[148:149], v[148:149], v[164:165], v[180:181]\n" \
"v_pk_fma_f32 v[150:151], v[150:151], v[166:167], v[182:183]\n" \
"v_lshlrev_b32_e32 v184, 16, v88\n" \
"v_and_b32_e32 v185, s43, v88\n" \
"v_lshlrev_b32_e32 v186, 16, v89\n" \
"v_and_b32_e32 v187, s43, v89\n" \
"v_lshlrev_b32_e32 v188, 16, v92\n" \
"v_and_b32_e32 v189, s43, v92\n" \
"v_lshlrev_b32_e32 v190, 16, v93\n" \
"v_and_b32_e32 v191, s43, v93\n" \
"v_pk_fma_f32 v[144:145], v[144:145], v[184:185], v[32:33]\n" \
"v_pk_fma_f32 v[146:147], v[146:147], v[186:187], v[34:35]\n" \
"v_pk_fma_f32 v[148:149], v[148:149], v[188:189], v[40:41]\n" \
"v_pk_fma_f32 v[150:151], v[150:151], v[190:191], v[42:43]\n" \
"v_cvt_pk_bf16_f32 v144, v144, v145\n" \
"v_cvt_pk_bf16_f32 v146, v146, v147\n" \
"v_cvt_pk_bf16_f32 v148, v148, v149\n" \
"v_cvt_pk_bf16_f32 v150, v150, v151\n" \
"v_lshlrev_b32_e32 v160, 16, v202\n" \
"v_and_b32_e32 v161, s43, v202\n" \
"v_lshlrev_b32_e32 v176, 16, v74\n" \
"v_and_b32_e32 v177, s43, v74\n" \
"v_lshlrev_b32_e32 v162, 16, v203\n" \
"v_and_b32_e32 v163, s43, v203\n" \
"v_lshlrev_b32_e32 v178, 16, v75\n" \
"v_and_b32_e32 v179, s43, v75\n" \
"v_lshlrev_b32_e32 v164, 16, v206\n" \
"v_and_b32_e32 v165, s43, v206\n" \
"v_lshlrev_b32_e32 v180, 16, v78\n" \
"v_and_b32_e32 v181, s43, v78\n" \
"v_lshlrev_b32_e32 v166, 16, v207\n" \
"v_and_b32_e32 v167, s43, v207\n" \
"v_lshlrev_b32_e32 v182, 16, v79\n" \
"v_and_b32_e32 v183, s43, v79\n" \
"v_pk_fma_f32 v[152:153], v[152:153], v[160:161], v[176:177]\n" \
"v_pk_fma_f32 v[154:155], v[154:155], v[162:163], v[178:179]\n" \
"v_pk_fma_f32 v[156:157], v[156:157], v[164:165], v[180:181]\n" \
"v_pk_fma_f32 v[158:159], v[158:159], v[166:167], v[182:183]\n" \
"v_lshlrev_b32_e32 v184, 16, v90\n" \
"v_and_b32_e32 v185, s43, v90\n" \
"v_lshlrev_b32_e32 v186, 16, v91\n" \
"v_and_b32_e32 v187, s43, v91\n" \
"v_lshlrev_b32_e32 v188, 16, v94\n" \
"v_and_b32_e32 v189, s43, v94\n" \
"v_lshlrev_b32_e32 v190, 16, v95\n" \
"v_and_b32_e32 v191, s43, v95\n" \
"v_pk_fma_f32 v[152:153], v[152:153], v[184:185], v[48:49]\n" \
"v_pk_fma_f32 v[154:155], v[154:155], v[186:187], v[50:51]\n" \
"v_pk_fma_f32 v[156:157], v[156:157], v[188:189], v[56:57]\n" \
"v_pk_fma_f32 v[158:159], v[158:159], v[190:191], v[58:59]\n" \
"v_cvt_pk_bf16_f32 v152, v152, v153\n" \
"v_cvt_pk_bf16_f32 v154, v154, v155\n" \
"v_cvt_pk_bf16_f32 v156, v156, v157\n" \
"v_cvt_pk_bf16_f32 v158, v158, v159\n" \
"s_or_b64 %[lat], s[56:57], s[58:59]\n" \
"s_mov_b32 vcc_lo, 0x55555555\n" \
"s_mov_b32 vcc_hi, 0x55555555\n" \
"v_bfrev_b32_e32 v208, 1\n" \
"v_cndmask_b32_e64 v208, %[vd0], v208, %[lat]\n" \
"v_cndmask_b32_dpp v96, v136, v128, vcc quad_perm:[1,0,3,2] row_mask:0xf bank_mask:0xf\n" \
"v_cndmask_b32_dpp v97, v138, v130, vcc quad_perm:[1,0,3,2] row_mask:0xf bank_mask:0xf\n" \
"v_cndmask_b32_dpp v100, v140, v132, vcc quad_perm:[1,0,3,2] row_mask:0xf bank_mask:0xf\n" \
"v_cndmask_b32_dpp v101, v142, v134, vcc quad_perm:[1,0,3,2] row_mask:0xf bank_mask:0xf\n" \
"v_cndmask_b32_dpp v104, v152, v144, vcc quad_perm:[1,0,3,2] row_mask:0xf bank_mask:0xf\n" \
"v_cndmask_b32_dpp v105, v154, v146, vcc quad_perm:[1,0,3,2] row_mask:0xf bank_mask:0xf\n" \
"v_cndmask_b32_dpp v108, v156, v148, vcc quad_perm:[1,0,3,2] row_mask:0xf bank_mask:0xf\n" \
"v_cndmask_b32_dpp v109, v158, v150, vcc quad_perm:[1,0,3,2] row_mask:0xf bank_mask:0xf\n" \
"s_not_b64 vcc, vcc\n" \
"v_xor_b32_e32 v209, 0x80, v208\n" \
"v_cndmask_b32_dpp v98, v128, v136, vcc quad_perm:[1,0,3,2] row_mask:0xf bank_mask:0xf\n" \
"v_cndmask_b32_dpp v99, v130, v138, vcc quad_perm:[1,0,3,2] row_mask:0xf bank_mask:0xf\n" \
"v_cndmask_b32_dpp v102, v132, v140, vcc quad_perm:[1,0,3,2] row_mask:0xf bank_mask:0xf\n" \
"v_cndmask_b32_dpp v103, v134, v142, vcc quad_perm:[1,0,3,2] row_mask:0xf bank_mask:0xf\n" \
"v_cndmask_b32_dpp v106, v144, v152, vcc quad_perm:[1,0,3,2] row_mask:0xf bank_mask:0xf\n" \
"v_cndmask_b32_dpp v107, v146, v154, vcc quad_perm:[1,0,3,2] row_mask:0xf bank_mask:0xf\n" \
"v_cndmask_b32_dpp v110, v148, v156, vcc quad_perm:[1,0,3,2] row_mask:0xf bank_mask:0xf\n" \
"v_cndmask_b32_dpp v111, v150, v158, vcc quad_perm:[1,0,3,2] row_mask:0xf bank_mask:0xf\n" \
"buffer_store_dwordx4 v[96:99], v208, s[64:67], 0 offen offset:0 sc1\n" \
"buffer_store_dwordx4 v[100:103], v208, s[64:67], 0 offen offset:1024 sc1\n" \
"buffer_store_dwordx4 v[104:107], v209, s[64:67], 0 offen offset:0 sc1\n" \
"buffer_store_dwordx4 v[108:111], v209, s[64:67], 0 offen offset:1024 sc1\n" \
"v_readlane_b32 s63, %[tlo], 28\n" \
"v_accvgpr_read_b32 v124, a32\n" \
"v_accvgpr_read_b32 v214, a33\n" \
"v_accvgpr_read_b32 v73, a34\n" \
"s_add_u32 s62, s40, 65536\n" \
"v_lshl_add_u32 v125, v125, 11, v214\n" \
"v_mov_b32_e32 v72, s40\n" \
"v_lshl_add_u32 v125, v125, 1, s62\n" \
"v_add_u32_e32 v214, s40, v214\n" \
"v_and_b32_e32 v74, 1, v222\n" \
"s_add_u32 s58, s63, 0x100000\n" \
"s_cmp_eq_u32 %[wid], 3\n" \
"s_cselect_b32 s58, 0x80000000, s58\n" \
"s_cselect_b32 s59, 1, 0\n" \
"s_cbranch_scc0 TLkw_%=\n" \
"s_waitcnt vmcnt(4)\n" \
"TLk2_%=:\n" \
"v_min3_u32 v64, v112, v113, v114\n" \
"v_min3_u32 v65, v115, v116, v117\n" \
"v_min3_u32 v64, v64, v118, v119\n" \
"v_min_u32_e32 v64, v64, v65\n" \
"v_cmp_eq_u32_e32 vcc, 0, v64\n" \
"s_cbranch_vccnz TLk1_%=\n" \
"TLkw_%=:\n" \
"s_waitcnt lgkmcnt(0)\n" \
"s_barrier\n" \
"buffer_load_dwordx4 v[128:131], v73, %[srdz], s63 offen offset:0 sc1\n" \
"buffer_load_dwordx4 v[132:135], v73, %[srdz], s58 offen offset:0 sc1\n" \
"ds_read_b32 v120, v125 offset:0\n" \
"ds_read_u16 v121, v214 offset:2048\n" \
"ds_read_u16 v212, v214 offset:0\n" \
"ds_read_u16 v123, v214 offset:4096\n" \
"ds_read_b128 v[68:71], v72 offset:14384\n" \
"s_cmp_eq_u32 s59, 0\n" \
"s_cbranch_scc0 TLg1_%=\n" \
"s_waitcnt lgkmcnt(0)\n" \
"v_lshlrev_b32_e32 v121, 16, v121\n" \
"v_lshlrev_b32_e32 v212, 16, v212\n" \
"v_lshlrev_b32_e32 v123, 16, v123\n" \
"v_sub_f32_e32 v213, v120, v4\n" \
"v_mul_f32_e32 v213, v213, v126\n" \
"v_fma_f32 v127, v213, v121, v212\n" \
"v_add_f32_e32 v64, v68, v69\n" \
"v_add_f32_e32 v65, v70, v71\n" \
"v_add_f32_e32 v122, v64, v65\n" \
"v_add_f32_e32 v122, v122, v20\n" \
"v_cvt_pk_bf16_f32 v122, 0, v122\n" \
"v_mul_f32_e32 v122, 0xbfb8aa3b, v122\n" \
"v_exp_f32_e32 v122, v122\n" \
"s_nop 0\n" \
"v_add_f32_e32 v122, 1.0, v122\n" \
"v_rcp_f32_e32 v122, v122\n" \
"s_nop 0\n" \
"v_cvt_pk_bf16_f32 v122, 0, v122\n" \
"TLg2_%=:\n" \
"v_mov_b32_e32 v64, s59\n" \
"s_lshl_b32 s62, %[wid], 5\n" \
"v_lshrrev_b32_e32 v66, 2, v222\n" \
"s_add_u32 s62, s62, s40\n" \
"v_and_b32_e32 v66, 12, v66\n" \
"v_lshl_or_b32 v66, v74, 4, v66\n" \
"v_add_u32_e32 v66, s62, v66\n" \
"TLg3_%=:\n" \
"s_waitcnt vmcnt(0)\n" \
"v_bitop3_b32 v65, v132, v133, v134 bitop3:0x80\n" \
"v_bitop3_b32 v67, v128, v129, v130 bitop3:0x80\n" \
"v_bitop3_b32 v65, v65, v135, v64 bitop3:0xea\n" \
"v_bitop3_b32 v67, v67, v131, 1 bitop3:0x80\n" \
"v_pk_add_f32 v[128:129], v[128:129], v[132:133]\n" \
"v_and_b32_e32 v65, v65, v67\n" \
"v_pk_add_f32 v[130:131], v[130:131], v[134:135]\n" \
"v_cmp_eq_u32_e32 vcc, 0, v65\n" \
"s_nop 0\n" \
"v_permlane32_swap_b32_e32 v128, v130\n" \
"v_permlane32_swap_b32_e32 v129, v131\n" \
"v_add_f32_e32 v128, v128, v130\n" \
"v_add_f32_e32 v129, v129, v131\n" \
"s_cbranch_vccnz TLga_%=\n" \
"TLgb_%=:\n" \
"s_nop 0\n" \
"v_permlane16_swap_b32_e32 v128, v129\n" \
"v_add_f32_e32 v128, v128, v129\n" \
"s_nop 1\n" \
"v_add_f32_dpp v128, v128, v128 row_ror:8 row_mask:0xf bank_mask:0xf\n" \
"s_nop 1\n" \
"v_add_f32_dpp v128, v128, v128 row_ror:4 row_mask:0xf bank_mask:0xf\n" \
"s_nop 1\n" \
"v_add_f32_dpp v128, v128, v128 quad_perm:[2,3,0,1] row_mask:0xf bank_mask:0xf\n" \
"ds_write_b32 v66, v128 offset:29696\n" \
"v_and_b32_e32 v65, 7, v222\n" \
"v_mov_b32_e32 v74, s60\n" \
"v_lshl_add_u32 v65, v65, 2, s40\n" \
"v_mov_b32_e32 v75, s61\n" \
"s_mov_b32 vcc_lo, 0xaaaaaaaa\n" \
"s_min_u32 s56, s60, s61\n" \
"v_cndmask_b32_e32 v74, v74, v75, vcc\n" \
"s_max_u32 s57, s60, s61\n" \
"v_lshl_or_b32 v74, v74, 10, v124\n" \
"v_lshlrev_b32_e32 v74, 1, v74\n" \
"s_waitcnt lgkmcnt(0)\n" \
"s_barrier\n" \
"ds_read_b64 v[66:67], v72 offset:14432\n" \
"ds_read_b32 v68, v65 offset:29696\n" \
"ds_read_b32 v69, v65 offset:29728\n" \
"ds_read_b32 v70, v65 offset:29760\n" \
"ds_read_b32 v71, v65 offset:29792\n" \
"s_waitcnt lgkmcnt(4)\n" \
"v_readfirstlane_b32 %[k2], v66\n" \
"v_readfirstlane_b32 %[k1], v67\n" \
"s_waitcnt lgkmcnt(0)\n" \
"v_add_f32_e32 v68, v68, v69\n" \
"v_add_f32_e32 v70, v70, v71\n" \
"s_andn2_b32 %[k1], 0x7ff, %[k1]\n" \
"s_andn2_b32 %[k2], 0x7ff, %[k2]\n" \
"v_add_f32_e32 v68, v68, v70\n" \
"s_min_u32 s62, %[k1], %[k2]\n" \
"s_max_u32 s63, %[k1], %[k2]\n" \
"v_fma_f32 v68, v68, v122, v127\n" \
"s_cmp_lg_u64 s[62:63], s[56:57]\n" \
"s_cselect_b32 %[got], 1, 0\n" \
"v_fma_f32 v68, v68, v123, v120\n" \
"s_or_b32 s58, %[got], %[wid]\n" \
"s_cselect_b32 s58, 0x80000000, 0\n" \
"v_cvt_pk_bf16_f32 v68, v68, v68\n" \
"s_mov_b64 exec, 0xff\n" \
"buffer_store_short v68, v74, s[64:67], s58 offen offset:0 sc1\n" \
"s_mov_b64 exec, -1\n" \
"s_branch Lend_%=\n" \
"TLpk_%=:\n" \
"v_readlane_b32 s62, %[tlo], 35\n" \
"s_mov_b32 s51, 0x10000\n" \
"s_nop 4\n" \
"TLpk1_%=:\n" \
"buffer_load_dwordx4 v[64:67], v72, %[srdz], s62 offen offset:0 sc1\n" \
"buffer_load_dwordx4 v[68:71], v72, %[srdz], s62 offen offset:16 sc1\n" \
"s_waitcnt vmcnt(0)\n" \
"v_min3_u32 v208, v64, v65, v66\n" \
"v_min3_u32 v209, v67, v68, v69\n" \
"v_min3_u32 v208, v208, v70, v71\n" \
"v_min_u32_e32 v208, v208, v209\n" \
"v_cmp_eq_u32_e32 vcc, 0, v208\n" \
"s_sub_u32 s51, s51, 1\n" \
"s_cbranch_scc1 TLpkb_%=\n" \
"s_cbranch_vccz TLpkb_%=\n" \
"s_sleep 8\n" \
"s_branch TLpk1_%=\n" \
"TLrare_%=:\n" \
"v_cmp_u_f32_e32 vcc, v14, v15\n" \
"s_cbranch_vccz TLvar_%=\n" \
"s_sub_u32 s52, s52, 1\n" \
"s_cbranch_scc1 TLvar_%=\n" \
"buffer_load_dwordx4 a[164:167], v217, %[srdz], s54 offen offset:0 sc1\n" \
"buffer_load_dwordx4 a[168:171], v217, %[srdz], s54 offen offset:1024 sc1\n" \
"buffer_load_dwordx4 a[172:175], v217, %[srdz], s54 offen offset:2048 sc1\n" \
"buffer_load_dwordx4 a[176:179], v217, %[srdz], s54 offen offset:3072 sc1\n" \
"buffer_load_dwordx4 a[180:183], v217, %[srdz], s55 offen offset:0 sc1\n" \
"buffer_load_dwordx4 a[184:187], v217, %[srdz], s55 offen offset:1024 sc1\n" \
"buffer_load_dwordx4 a[188:191], v217, %[srdz], s55 offen offset:2048 sc1\n" \
"buffer_load_dwordx4 a[192:195], v217, %[srdz], s55 offen offset:3072 sc1\n" \
"v_accvgpr_read_b32 v33, a248\n" \
"v_lshlrev_b32_e32 v32, 16, v33\n" \
"v_and_b32_e32 v33, s43, v33\n" \
"v_accvgpr_read_b32 v35, a249\n" \
"v_lshlrev_b32_e32 v34, 16, v35\n" \
"v_and_b32_e32 v35, s43, v35\n" \
"v_accvgpr_read_b32 v41, a252\n" \
"v_lshlrev_b32_e32 v40, 16, v41\n" \
"v_and_b32_e32 v41, s43, v41\n" \
"v_accvgpr_read_b32 v43, a253\n" \
"v_lshlrev_b32_e32 v42, 16, v43\n" \
"v_and_b32_e32 v43, s43, v43\n" \
"v_accvgpr_read_b32 v49, a250\n" \
"v_lshlrev_b32_e32 v48, 16, v49\n" \
"v_and_b32_e32 v49, s43, v49\n" \
"v_accvgpr_read_b32 v51, a251\n" \
"v_lshlrev_b32_e32 v50, 16, v51\n" \
"v_and_b32_e32 v51, s43, v51\n" \
"v_accvgpr_read_b32 v57, a254\n" \
"v_lshlrev_b32_e32 v56, 16, v57\n" \
"v_and_b32_e32 v57, s43, v57\n" \
"v_accvgpr_read_b32 v59, a255\n" \
"v_lshlrev_b32_e32 v58, 16, v59\n" \
"v_and_b32_e32 v59, s43, v59\n" \
"v_pk_mul_f32 v[6:7], v[0:1], v[160:161]\n" \
"v_pk_mul_f32 v[12:13], v[0:1], v[0:1]\n" \
"v_pk_add_f32 v[4:5], v[0:1], v[2:3]\n" \
"v_pk_fma_f32 v[6:7], v[2:3], v[162:163], v[6:7]\n" \
"v_pk_fma_f32 v[12:13], v[2:3], v[2:3], v[12:13]\n" \
"v_pk_add_f32 v[4:5], v[4:5], v[8:9]\n" \
"v_pk_fma_f32 v[6:7], v[8:9], v[168:169], v[6:7]\n" \
"v_pk_fma_f32 v[12:13], v[8:9], v[8:9], v[12:13]\n" \
"v_pk_add_f32 v[4:5], v[4:5], v[10:11]\n" \
"v_pk_fma_f32 v[6:7], v[10:11], v[170:171], v[6:7]\n" \
"v_pk_fma_f32 v[12:13], v[10:11], v[10:11], v[12:13]\n" \
"v_pk_add_f32 v[4:5], v[4:5], v[16:17]\n" \
"v_pk_fma_f32 v[6:7], v[16:17], v[164:165], v[6:7]\n" \
"v_pk_fma_f32 v[12:13], v[16:17], v[16:17], v[12:13]\n" \
"v_pk_add_f32 v[4:5], v[4:5], v[18:19]\n" \
"v_pk_fma_f32 v[6:7], v[18:19], v[166:167], v[6:7]\n" \
"v_pk_fma_f32 v[12:13], v[18:19], v[18:19], v[12:13]\n" \
"v_pk_add_f32 v[4:5], v[4:5], v[24:25]\n" \
"v_pk_fma_f32 v[6:7], v[24:25], v[172:173], v[6:7]\n" \
"v_pk_fma_f32 v[12:13], v[24:25], v[24:25], v[12:13]\n" \
"v_pk_add_f32 v[4:5], v[4:5], v[26:27]\n" \
"v_pk_fma_f32 v[6:7], v[26:27], v[174:175], v[6:7]\n" \
"v_pk_fma_f32 v[12:13], v[26:27], v[26:27], v[12:13]\n" \
"s_waitcnt vmcnt(0)\n" \
"s_nop 1\n" \
"v_mfma_f32_16x16x32_bf16 v[208:211], v[230:233], a[164:167], 0\n" \
"v_mfma_f32_16x16x32_bf16 v[212:215], v[230:233], a[172:175], 0\n" \
"v_mfma_f32_16x16x32_bf16 v[208:211], v[234:237], a[168:171], v[208:211]\n" \
"v_mfma_f32_16x16x32_bf16 v[212:215], v[234:237], a[176:179], v[212:215]\n" \
"s_nop 6\n" \
"v_pk_fma_f32 v[32:33], v[208:209], v[144:145], v[32:33]\n" \
"v_pk_fma_f32 v[34:35], v[210:211], v[146:147], v[34:35]\n" \
"v_pk_fma_f32 v[40:41], v[212:213], v[152:153], v[40:41]\n" \
"v_pk_fma_f32 v[42:43], v[214:215], v[154:155], v[42:43]\n" \
"v_pk_mul_f32 v[36:37], v[32:33], v[176:177]\n" \
"v_pk_mul_f32 v[38:39], v[32:33], v[32:33]\n" \
"v_pk_add_f32 v[14:15], v[32:33], v[34:35]\n" \
"v_pk_fma_f32 v[36:37], v[34:35], v[178:179], v[36:37]\n" \
"v_pk_fma_f32 v[38:39], v[34:35], v[34:35], v[38:39]\n" \
"v_pk_add_f32 v[14:15], v[14:15], v[40:41]\n" \
"v_pk_fma_f32 v[36:37], v[40:41], v[184:185], v[36:37]\n" \
"v_pk_fma_f32 v[38:39], v[40:41], v[40:41], v[38:39]\n" \
"v_pk_add_f32 v[14:15], v[14:15], v[42:43]\n" \
".if . - adaln_body != 69632\n" \
".error \"size model\"\n" \
".endif\n" \
"Lk16_%=:\n" \
"s_cbranch_execz adaln_kr\n" \
"s_nop 0\n" \
"v_pk_fma_f32 v[36:37], v[42:43], v[186:187], v[36:37]\n" \
"v_pk_fma_f32 v[38:39], v[42:43], v[42:43], v[38:39]\n" \
"s_nop 7\n" \
"v_mfma_f32_16x16x32_bf16 v[208:211], v[230:233], a[180:183], 0\n" \
"v_mfma_f32_16x16x32_bf16 v[212:215], v[230:233], a[188:191], 0\n" \
"v_mfma_f32_16x16x32_bf16 v[208:211], v[234:237], a[184:187], v[208:211]\n" \
"v_mfma_f32_16x16x32_bf16 v[212:215], v[234:237], a[192:195], v[212:215]\n" \
"s_nop 6\n" \
"v_pk_fma_f32 v[48:49], v[208:209], v[148:149], v[48:49]\n" \
"v_pk_fma_f32 v[50:51], v[210:211], v[150:151], v[50:51]\n" \
"v_pk_fma_f32 v[56:57], v[212:213], v[156:157], v[56:57]\n" \
"v_pk_fma_f32 v[58:59], v[214:215], v[158:159], v[58:59]\n" \
"v_pk_add_f32 v[14:15], v[14:15], v[48:49]\n" \
"v_pk_fma_f32 v[36:37], v[48:49], v[180:181], v[36:37]\n" \
"v_pk_fma_f32 v[38:39], v[48:49], v[48:49], v[38:39]\n" \
"v_pk_add_f32 v[14:15], v[14:15], v[50:51]\n" \
"v_pk_fma_f32 v[36:37], v[50:51], v[182:183], v[36:37]\n" \
"v_pk_fma_f32 v[38:39], v[50:51], v[50:51], v[38:39]\n" \
"v_pk_add_f32 v[14:15], v[14:15], v[56:57]\n" \
"v_pk_fma_f32 v[36:37], v[56:57], v[188:189], v[36:37]\n" \
"v_pk_fma_f32 v[38:39], v[56:57], v[56:57], v[38:39]\n" \
"v_pk_add_f32 v[14:15], v[14:15], v[58:59]\n" \
"v_pk_fma_f32 v[36:37], v[58:59], v[190:191], v[36:37]\n" \
"v_pk_fma_f32 v[38:39], v[58:59], v[58:59], v[38:39]\n" \
"v_pk_add_f32 v[4:5], v[4:5], v[14:15]\n" \
"v_pk_add_f32 v[6:7], v[6:7], v[36:37]\n" \
"v_pk_add_f32 v[12:13], v[12:13], v[38:39]\n" \
"v_add_f32_e32 v4, v4, v5\n" \
"v_add_f32_e32 v6, v6, v7\n" \
"v_add_f32_e32 v12, v12, v13\n" \
"v_mov_b32_e32 v208, v12\n" \
"v_permlane32_swap_b32_e32 v4, v6\n" \
"s_nop 0\n" \
"v_permlane32_swap_b32_e32 v12, v208\n" \
"v_add_f32_e32 v4, v4, v6\n" \
"v_add_f32_e32 v12, v12, v208\n" \
"s_nop 0\n" \
"s_nop 0\n" \
"v_permlane16_swap_b32_e32 v4, v12\n" \
"v_add_f32_e32 v4, v4, v12\n" \
"s_nop 1\n" \
"v_add_f32_dpp v4, v4, v4 quad_perm:[1,0,3,2] row_mask:0xf bank_mask:0xf\n" \
"s_nop 1\n" \
"v_add_f32_dpp v4, v4, v4 quad_perm:[2,3,0,1] row_mask:0xf bank_mask:0xf\n" \
"s_nop 1\n" \
"v_add_f32_dpp v4, v4, v4 row_ror:8 row_mask:0xf bank_mask:0xf\n" \
"v_mov_b32_e32 v12, v4\n" \
"s_nop 0\n" \
"s_nop 0\n" \
"v_permlane16_swap_b32_e32 v4, v12\n" \
"v_mov_b32_e32 v6, v4\n" \
"v_mul_f32_e32 v12, 0x3a800000, v12\n" \
"s_nop 0\n" \
"v_permlane32_swap_b32_e32 v4, v6\n" \
"v_mul_f32_e32 v218, 0x3a800000, v4\n" \
"v_mul_f32_e32 v208, v218, v218\n" \
"v_fma_f32 v6, -v218, v223, v6\n" \
"v_sub_f32_e32 v12, v12, v208\n" \
"v_cmp_nle_f32_e32 vcc, v208, v12\n" \
"v_add_f32_e32 v12, 0x3727c5ac, v12\n" \
"v_rsq_f32_e32 v220, v12\n" \
"s_mov_b64 exec, 17\n" \
"v_mul_f32_e32 v210, v6, v220\n" \
"v_ashrrev_i32_e32 v208, 31, v210\n" \
"v_or_b32_e32 v208, 0x80000000, v208\n" \
"v_xor_b32_e32 v210, v210, v208\n" \
"v_and_b32_e32 v210, 0xfffff800, v210\n" \
"v_max_u32_e32 v210, 0x800, v210\n" \
"v_or_b32_e32 v210, v210, %[ta0]\n" \
"ds_write_b32 v216, v210 offset:14400\n" \
"s_mov_b64 exec, -1\n" \
"s_cbranch_vccnz TLrare_%=\n" \
"s_branch TLrok_%=\n" \
"TLvar_%=:\n" \
"v_sub_f32_e32 v210, 0, v218\n" \
"v_pk_add_f32 v[212:213], v[0:1], v[210:211] op_sel_hi:[1,0]\n" \
"v_pk_mul_f32 v[214:215], v[212:213], v[212:213]\n" \
"v_pk_add_f32 v[212:213], v[2:3], v[210:211] op_sel_hi:[1,0]\n" \
"v_pk_fma_f32 v[214:215], v[212:213], v[212:213], v[214:215]\n" \
"v_pk_add_f32 v[212:213], v[8:9], v[210:211] op_sel_hi:[1,0]\n" \
"v_pk_fma_f32 v[214:215], v[212:213], v[212:213], v[214:215]\n" \
"v_pk_add_f32 v[212:213], v[10:11], v[210:211] op_sel_hi:[1,0]\n" \
"v_pk_fma_f32 v[214:215], v[212:213], v[212:213], v[214:215]\n" \
"v_pk_add_f32 v[212:213], v[16:17], v[210:211] op_sel_hi:[1,0]\n" \
"v_pk_fma_f32 v[214:215], v[212:213], v[212:213], v[214:215]\n" \
"v_pk_add_f32 v[212:213], v[18:19], v[210:211] op_sel_hi:[1,0]\n" \
"v_pk_fma_f32 v[214:215], v[212:213], v[212:213], v[214:215]\n" \
"v_pk_add_f32 v[212:213], v[24:25], v[210:211] op_sel_hi:[1,0]\n" \
"v_pk_fma_f32 v[214:215], v[212:213], v[212:213], v[214:215]\n" \
"v_pk_add_f32 v[212:213], v[26:27], v[210:211] op_sel_hi:[1,0]\n" \
"v_pk_fma_f32 v[214:215], v[212:213], v[212:213], v[214:215]\n" \
"v_pk_add_f32 v[212:213], v[32:33], v[210:211] op_sel_hi:[1,0]\n" \
"v_pk_fma_f32 v[214:215], v[212:213], v[212:213], v[214:215]\n" \
"v_pk_add_f32 v[212:213], v[34:35], v[210:211] op_sel_hi:[1,0]\n" \
"v_pk_fma_f32 v[214:215], v[212:213], v[212:213], v[214:215]\n" \
"v_pk_add_f32 v[212:213], v[40:41], v[210:211] op_sel_hi:[1,0]\n" \
"v_pk_fma_f32 v[214:215], v[212:213], v[212:213], v[214:215]\n" \
"v_pk_add_f32 v[212:213], v[42:43], v[210:211] op_sel_hi:[1,0]\n" \
"v_pk_fma_f32 v[214:215], v[212:213], v[212:213], v[214:215]\n" \
"v_pk_add_f32 v[212:213], v[48:49], v[210:211] op_sel_hi:[1,0]\n" \
"v_pk_fma_f32 v[214:215], v[212:213], v[212:213], v[214:215]\n" \
"v_pk_add_f32 v[212:213], v[50:51], v[210:211] op_sel_hi:[1,0]\n" \
"v_pk_fma_f32 v[214:215], v[212:213], v[212:213], v[214:215]\n" \
"v_pk_add_f32 v[212:213], v[56:57], v[210:211] op_sel_hi:[1,0]\n" \
"v_pk_fma_f32 v[214:215], v[212:213], v[212:213], v[214:215]\n" \
"v_pk_add_f32 v[212:213], v[58:59], v[210:211] op_sel_hi:[1,0]\n" \
"v_pk_fma_f32 v[214:215], v[212:213], v[212:213], v[214:215]\n" \
"v_add_f32_e32 v12, v214, v215\n" \
"s_nop 1\n" \
"v_add_f32_dpp v12, v12, v12 quad_perm:[1,0,3,2] row_mask:0xf bank_mask:0xf\n" \
"s_nop 1\n" \
"v_add_f32_dpp v12, v12, v12 quad_perm:[2,3,0,1] row_mask:0xf bank_mask:0xf\n" \
"s_nop 1\n" \
"v_add_f32_dpp v12, v12, v12 row_ror:8 row_mask:0xf bank_mask:0xf\n" \
"v_mov_b32_e32 v210, v12\n" \
"s_nop 0\n" \
"s_nop 0\n" \
"v_permlane16_swap_b32_e32 v12, v210\n" \
"v_add_f32_e32 v12, v12, v210\n" \
"v_mov_b32_e32 v210, v12\n" \
"s_nop 0\n" \
"s_nop 0\n" \
"v_permlane32_swap_b32_e32 v12, v210\n" \
"v_add_f32_e32 v12, v12, v210\n" \
"v_mul_f32_e32 v12, 0x3a800000, v12\n" \
"v_add_f32_e32 v12, 0x3727c5ac, v12\n" \
"v_rsq_f32_e32 v220, v12\n" \
"s_mov_b64 exec, 17\n" \
"v_mul_f32_e32 v210, v6, v220\n" \
"v_ashrrev_i32_e32 v208, 31, v210\n" \
"v_or_b32_e32 v208, 0x80000000, v208\n" \
"v_xor_b32_e32 v210, v210, v208\n" \
"v_and_b32_e32 v210, 0xfffff800, v210\n" \
"v_max_u32_e32 v210, 0x800, v210\n" \
"v_or_b32_e32 v210, v210, %[ta0]\n" \
"ds_write_b32 v216, v210 offset:14400\n" \
"s_mov_b64 exec, -1\n" \
"s_branch TLrok_%=\n" \
"TLcw_%=:\n" \
"s_waitcnt lgkmcnt(0)\n" \
"s_mov_b32 s63, 278528\n" \
"s_mov_b32 s51, 0x10000\n" \
"TLcw1_%=:\n" \
"buffer_load_dword %[vxs], %[vdn], %[srdz], s63 offen offset:0 sc1\n" \
"s_waitcnt vmcnt(0)\n" \
"v_and_b32_e32 v44, 0x820, %[vxs]\n" \
"v_cmp_ne_u32_e32 vcc, 0x820, v44\n" \
"s_sub_u32 s51, s51, 1\n" \
"s_cbranch_scc1 TLcw2_%=\n" \
"s_nop 3\n" \
"s_cbranch_vccz TLcw2_%=\n" \
"s_sleep 8\n" \
"s_branch TLcw1_%=\n" \
"TLcw2_%=:\n" \
"s_sleep 4\n" \
"s_mov_b32 s59, 1\n" \
"buffer_load_dwordx4 a[128:131], %[vct], %[srdz], 0 offen offset:0 sc1\n" \
"buffer_load_dwordx4 a[132:135], %[rd0], %[srdz], 0 offen offset:0 sc1\n" \
"buffer_load_dwordx4 a[136:139], %[vct], %[srdz], s56 offen offset:0 sc1\n" \
"buffer_load_dwordx4 a[140:143], %[rd0], %[srdz], s56 offen offset:0 sc1\n" \
"buffer_load_dwordx4 a[144:147], %[vct], %[srdz], s57 offen offset:0 sc1\n" \
"buffer_load_dwordx4 a[148:151], %[rd0], %[srdz], s57 offen offset:0 sc1\n" \
"buffer_load_dwordx4 a[152:155], %[vct], %[srdz], s58 offen offset:0 sc1\n" \
"buffer_load_dwordx4 a[156:159], %[rd0], %[srdz], s58 offen offset:0 sc1\n" \
"s_waitcnt vmcnt(0)\n" \
"s_branch TLcs_%=\n" \
"TLcx_%=:\n" \
"s_cmp_eq_u32 s59, 0\n" \
"s_cbranch_scc1 TLcw_%=\n" \
"s_sub_u32 s52, s52, 1\n" \
"s_cbranch_scc1 TLco_%=\n" \
"buffer_load_dwordx4 a[128:131], %[vct], %[srdz], 0 offen offset:0 sc1\n" \
"buffer_load_dwordx4 a[132:135], %[rd0], %[srdz], 0 offen offset:0 sc1\n" \
"buffer_load_dwordx4 a[136:139], %[vct], %[srdz], s56 offen offset:0 sc1\n" \
"buffer_load_dwordx4 a[140:143], %[rd0], %[srdz], s56 offen offset:0 sc1\n" \
"buffer_load_dwordx4 a[144:147], %[vct], %[srdz], s57 offen offset:0 sc1\n" \
"buffer_load_dwordx4 a[148:151], %[rd0], %[srdz], s57 offen offset:0 sc1\n" \
"buffer_load_dwordx4 a[152:155], %[vct], %[srdz], s58 offen offset:0 sc1\n" \
"buffer_load_dwordx4 a[156:159], %[rd0], %[srdz], s58 offen offset:0 sc1\n" \
"s_waitcnt vmcnt(0) lgkmcnt(0)\n" \
"s_branch TLcs_%=\n" \
"TLk1_%=:\n" \
"s_sub_u32 s51, s51, 1\n" \
"s_cbranch_scc1 TLkw_%=\n" \
"s_sleep 3\n" \
"buffer_load_dwordx4 v[112:115], v216, %[srdz], s49 offen offset:0 sc1\n" \
"buffer_load_dwordx4 v[116:119], v216, %[srdz], s49 offen offset:16 sc1\n" \
"s_waitcnt vmcnt(0)\n" \
"s_branch TLk2_%=\n" \
"TLg1_%=:\n" \
"s_waitcnt lgkmcnt(0)\n" \
"v_min_u32_e32 v64, v113, v115\n" \
"v_min_u32_e32 v65, v117, v119\n" \
"v_max_u32_e32 v113, v113, v115\n" \
"v_max_u32_e32 v117, v117, v119\n" \
"v_max3_u32 v112, v64, v112, v114\n" \
"v_max3_u32 v116, v65, v116, v118\n" \
"v_min_u32_e32 v64, v113, v117\n" \
"v_max_u32_e32 v113, v113, v117\n" \
"v_max3_u32 v112, v64, v112, v116\n" \
"s_nop 0\n" \
"v_min_u32_dpp v64, v113, v113 quad_perm:[1,0,3,2] row_mask:0xf bank_mask:0xf\n" \
"v_max_u32_dpp v65, v112, v112 quad_perm:[1,0,3,2] row_mask:0xf bank_mask:0xf\n" \
"v_max_u32_dpp v113, v113, v113 quad_perm:[1,0,3,2] row_mask:0xf bank_mask:0xf\n" \
"v_max_u32_e32 v112, v64, v65\n" \
"s_nop 0\n" \
"v_min_u32_dpp v64, v113, v113 quad_perm:[2,3,0,1] row_mask:0xf bank_mask:0xf\n" \
"v_max_u32_dpp v65, v112, v112 quad_perm:[2,3,0,1] row_mask:0xf bank_mask:0xf\n" \
"v_max_u32_dpp v113, v113, v113 quad_perm:[2,3,0,1] row_mask:0xf bank_mask:0xf\n" \
"v_max_u32_e32 v112, v64, v65\n" \
"s_nop 0\n" \
"v_min_u32_dpp v64, v113, v113 row_half_mirror row_mask:0xf bank_mask:0xf\n" \
"v_max_u32_dpp v65, v112, v112 row_half_mirror row_mask:0xf bank_mask:0xf\n" \
"v_max_u32_dpp v113, v113, v113 row_half_mirror row_mask:0xf bank_mask:0xf\n" \
"v_max_u32_e32 v112, v64, v65\n" \
"s_nop 0\n" \
"v_min_u32_dpp v64, v113, v113 row_mirror row_mask:0xf bank_mask:0xf\n" \
"v_max_u32_dpp v65, v112, v112 row_mirror row_mask:0xf bank_mask:0xf\n" \
"v_max_u32_dpp v113, v113, v113 row_mirror row_mask:0xf bank_mask:0xf\n" \
"v_max_u32_e32 v112, v64, v65\n" \
"v_mov_b32_e32 v115, v113\n" \
"v_mov_b32_e32 v114, v112\n" \
"s_nop 0\n" \
"v_permlane16_swap_b32_e32 v113, v115\n" \
"v_permlane16_swap_b32_e32 v112, v114\n" \
"v_min_u32_e32 v64, v113, v115\n" \
"v_max_u32_e32 v113, v113, v115\n" \
"v_max3_u32 v112, v64, v112, v114\n" \
"v_mov_b32_e32 v115, v113\n" \
"v_mov_b32_e32 v114, v112\n" \
"s_nop 0\n" \
"v_permlane32_swap_b32_e32 v113, v115\n" \
"v_permlane32_swap_b32_e32 v112, v114\n" \
"v_min_u32_e32 v64, v113, v115\n" \
"v_max_u32_e32 v113, v113, v115\n" \
"v_max3_u32 v112, v64, v112, v114\n" \
"s_mov_b64 exec, 1\n" \
"ds_write_b64 v72, v[112:113] offset:14432\n" \
"s_mov_b64 exec, -1\n" \
"s_branch TLg2_%=\n" \
"TLga_%=:\n" \
"s_sub_u32 s51, s51, 1\n" \
"s_cbranch_scc1 TLgb_%=\n" \
"buffer_load_dwordx4 v[128:131], v73, %[srdz], s63 offen offset:0 sc1\n" \
"buffer_load_dwordx4 v[132:135], v73, %[srdz], s58 offen offset:0 sc1\n" \
"s_branch TLg3_%=\n" \
"Lsp1_%=:\n" \
"s_movk_i32 s51, 0x1000\n" \
"s_cmp_gt_u32 s48, 0\n" \
"s_cselect_b32 s61, 0, 1\n" \
"s_nop 0\n" \
"s_mul_i32 s61, s61, 152064\n" \
"Lsq1_%=:\n" \
"buffer_load_dwordx4 a[64:67], %[oq0], %[srdz], s61 offen offset:0 sc1\n" \
"buffer_load_dwordx4 a[68:71], %[oq0], %[srdz], s61 offen offset:1024 sc1\n" \
"buffer_load_dwordx4 a[72:75], %[oq0], %[srdz], s61 offen offset:2048 sc1\n" \
"buffer_load_dwordx4 a[76:79], %[oq0], %[srdz], s61 offen offset:3072 sc1\n" \
"buffer_load_dwordx4 a[80:83], v208, %[srdz], s61 offen offset:0 sc1\n" \
"buffer_load_dwordx4 a[84:87], v208, %[srdz], s61 offen offset:1024 sc1\n" \
"buffer_load_dwordx4 a[88:91], v208, %[srdz], s61 offen offset:2048 sc1\n" \
"buffer_load_dwordx4 a[92:95], v208, %[srdz], s61 offen offset:3072 sc1\n" \
"buffer_load_dwordx4 a[96:99], v209, %[srdz], s61 offen offset:0 sc1\n" \
"buffer_load_dwordx4 a[100:103], v209, %[srdz], s61 offen offset:1024 sc1\n" \
"buffer_load_dwordx4 a[104:107], v209, %[srdz], s61 offen offset:2048 sc1\n" \
"buffer_load_dwordx4 a[108:111], v209, %[srdz], s61 offen offset:3072 sc1\n" \
"buffer_load_dwordx4 a[112:115], v210, %[srdz], s61 offen offset:0 sc1\n" \
"buffer_load_dwordx4 a[116:119], v210, %[srdz], s61 offen offset:1024 sc1\n" \
"buffer_load_dwordx4 a[120:123], v210, %[srdz], s61 offen offset:2048 sc1\n" \
"buffer_load_dwordx4 a[124:127], v210, %[srdz], s61 offen offset:3072 sc1\n" \
"s_waitcnt vmcnt(0)\n" \
"s_nop 0\n" \
"v_mfma_f32_16x16x32_bf16 v[128:131], a[236:239], a[64:67], v[192:195]\n" \
"v_mfma_f32_16x16x32_bf16 v[128:131], a[196:199], a[68:71], v[128:131]\n" \
"v_mfma_f32_16x16x32_bf16 v[128:131], a[200:203], a[72:75], v[128:131]\n" \
"v_mfma_f32_16x16x32_bf16 v[128:131], a[204:207], a[76:79], v[128:131]\n" \
"v_mfma_f32_16x16x32_bf16 v[132:135], a[236:239], a[80:83], v[196:199]\n" \
"v_mfma_f32_16x16x32_bf16 v[132:135], a[196:199], a[84:87], v[132:135]\n" \
"v_mfma_f32_16x16x32_bf16 v[132:135], a[200:203], a[88:91], v[132:135]\n" \
"v_mfma_f32_16x16x32_bf16 v[132:135], a[204:207], a[92:95], v[132:135]\n" \
"v_mfma_f32_16x16x32_bf16 v[136:139], a[236:239], a[96:99], v[200:203]\n" \
"v_mfma_f32_16x16x32_bf16 v[136:139], a[196:199], a[100:103], v[136:139]\n" \
"v_mfma_f32_16x16x32_bf16 v[136:139], a[200:203], a[104:107], v[136:139]\n" \
"v_mfma_f32_16x16x32_bf16 v[136:139], a[204:207], a[108:111], v[136:139]\n" \
"v_mfma_f32_16x16x32_bf16 v[140:143], a[236:239], a[112:115], v[204:207]\n" \
"v_mfma_f32_16x16x32_bf16 v[140:143], a[196:199], a[116:119], v[140:143]\n" \
"v_mfma_f32_16x16x32_bf16 v[140:143], a[200:203], a[120:123], v[140:143]\n" \
"v_mfma_f32_16x16x32_bf16 v[140:143], a[204:207], a[124:127], v[140:143]\n" \
"s_nop 7\n" \
"s_nop 0\n" \
"v_cmp_u_f32_e64 s[44:45], v128, v132\n" \
"v_cmp_u_f32_e64 s[56:57], v136, v140\n" \
"s_or_b64 s[56:57], s[56:57], s[44:45]\n" \
"s_cbranch_scc0 Lbk1_%=\n" \
"s_sub_u32 s51, s51, 1\n" \
"s_cbranch_scc0 Lsq1_%=\n" \
"s_branch Lbk1_%=\n" \
"Lsp2_%=:\n" \
"s_movk_i32 s51, 0x1000\n" \
"s_cmp_gt_u32 s48, 1\n" \
"s_cselect_b32 s61, 1, 2\n" \
"s_mul_i32 s61, s61, 152064\n" \
"Lsq2_%=:\n" \
"buffer_load_dwordx4 a[0:3], %[oq0], %[srdz], s61 offen offset:0 sc1\n" \
"buffer_load_dwordx4 a[4:7], %[oq0], %[srdz], s61 offen offset:1024 sc1\n" \
"buffer_load_dwordx4 a[8:11], %[oq0], %[srdz], s61 offen offset:2048 sc1\n" \
"buffer_load_dwordx4 a[12:15], %[oq0], %[srdz], s61 offen offset:3072 sc1\n" \
"buffer_load_dwordx4 a[16:19], v208, %[srdz], s61 offen offset:0 sc1\n" \
"buffer_load_dwordx4 a[20:23], v208, %[srdz], s61 offen offset:1024 sc1\n" \
"buffer_load_dwordx4 a[24:27], v208, %[srdz], s61 offen offset:2048 sc1\n" \
"buffer_load_dwordx4 a[28:31], v208, %[srdz], s61 offen offset:3072 sc1\n" \
"buffer_load_dwordx4 a[32:35], v209, %[srdz], s61 offen offset:0 sc1\n" \
"buffer_load_dwordx4 a[36:39], v209, %[srdz], s61 offen offset:1024 sc1\n" \
"buffer_load_dwordx4 a[40:43], v209, %[srdz], s61 offen offset:2048 sc1\n" \
"buffer_load_dwordx4 a[44:47], v209, %[srdz], s61 offen offset:3072 sc1\n" \
"buffer_load_dwordx4 a[48:51], v210, %[srdz], s61 offen offset:0 sc1\n" \
"buffer_load_dwordx4 a[52:55], v210, %[srdz], s61 offen offset:1024 sc1\n" \
"buffer_load_dwordx4 a[56:59], v210, %[srdz], s61 offen offset:2048 sc1\n" \
"buffer_load_dwordx4 a[60:63], v210, %[srdz], s61 offen offset:3072 sc1\n" \
"s_waitcnt vmcnt(0)\n" \
"s_nop 0\n" \
"v_mfma_f32_16x16x32_bf16 v[192:195], a[196:199], a[0:3], v[128:131]\n" \
"v_mfma_f32_16x16x32_bf16 v[192:195], a[200:203], a[4:7], v[192:195]\n" \
"v_mfma_f32_16x16x32_bf16 v[192:195], a[204:207], a[8:11], v[192:195]\n" \
"v_mfma_f32_16x16x32_bf16 v[192:195], a[232:235], a[12:15], v[192:195]\n" \
"v_mfma_f32_16x16x32_bf16 v[196:199], a[196:199], a[16:19], v[132:135]\n" \
"v_mfma_f32_16x16x32_bf16 v[196:199], a[200:203], a[20:23], v[196:199]\n" \
"v_mfma_f32_16x16x32_bf16 v[196:199], a[204:207], a[24:27], v[196:199]\n" \
"v_mfma_f32_16x16x32_bf16 v[196:199], a[232:235], a[28:31], v[196:199]\n" \
"v_mfma_f32_16x16x32_bf16 v[200:203], a[196:199], a[32:35], v[136:139]\n" \
"v_mfma_f32_16x16x32_bf16 v[200:203], a[200:203], a[36:39], v[200:203]\n" \
"v_mfma_f32_16x16x32_bf16 v[200:203], a[204:207], a[40:43], v[200:203]\n" \
"v_mfma_f32_16x16x32_bf16 v[200:203], a[232:235], a[44:47], v[200:203]\n" \
"v_mfma_f32_16x16x32_bf16 v[204:207], a[196:199], a[48:51], v[140:143]\n" \
"v_mfma_f32_16x16x32_bf16 v[204:207], a[200:203], a[52:55], v[204:207]\n" \
"v_mfma_f32_16x16x32_bf16 v[204:207], a[204:207], a[56:59], v[204:207]\n" \
"v_mfma_f32_16x16x32_bf16 v[204:207], a[232:235], a[60:63], v[204:207]\n" \
"s_nop 7\n" \
"s_nop 0\n" \
"v_cmp_u_f32_e64 s[44:45], v192, v196\n" \
"v_cmp_u_f32_e64 s[56:57], v200, v204\n" \
"s_or_b64 s[56:57], s[56:57], s[44:45]\n" \
"s_cbranch_scc0 Lbk2_%=\n" \
"s_sub_u32 s51, s51, 1\n" \
"s_cbranch_scc0 Lsq2_%=\n" \
"s_branch Lbk2_%=\n" \
"Lsp3_%=:\n" \
"s_movk_i32 s51, 0x1000\n" \
"s_cmp_gt_u32 s48, 2\n" \
"s_cselect_b32 s61, 2, 3\n" \
"s_mul_i32 s61, s61, 152064\n" \
"Lsq3_%=:\n" \
"buffer_load_dwordx4 a[128:131], %[oq0], %[srdz], s61 offen offset:0 sc1\n" \
"buffer_load_dwordx4 a[132:135], %[oq0], %[srdz], s61 offen offset:1024 sc1\n" \
"buffer_load_dwordx4 a[136:139], %[oq0], %[srdz], s61 offen offset:2048 sc1\n" \
"buffer_load_dwordx4 a[140:143], %[oq0], %[srdz], s61 offen offset:3072 sc1\n" \
"buffer_load_dwordx4 a[144:147], v208, %[srdz], s61 offen offset:0 sc1\n" \
"buffer_load_dwordx4 a[148:151], v208, %[srdz], s61 offen offset:1024 sc1\n" \
"buffer_load_dwordx4 a[152:155], v208, %[srdz], s61 offen offset:2048 sc1\n" \
"buffer_load_dwordx4 a[156:159], v208, %[srdz], s61 offen offset:3072 sc1\n" \
"buffer_load_dwordx4 a[160:163], v209, %[srdz], s61 offen offset:0 sc1\n" \
"buffer_load_dwordx4 a[164:167], v209, %[srdz], s61 offen offset:1024 sc1\n" \
"buffer_load_dwordx4 a[168:171], v209, %[srdz], s61 offen offset:2048 sc1\n" \
"buffer_load_dwordx4 a[172:175], v209, %[srdz], s61 offen offset:3072 sc1\n" \
"buffer_load_dwordx4 a[176:179], v210, %[srdz], s61 offen offset:0 sc1\n" \
"buffer_load_dwordx4 a[180:183], v210, %[srdz], s61 offen offset:1024 sc1\n" \
"buffer_load_dwordx4 a[184:187], v210, %[srdz], s61 offen offset:2048 sc1\n" \
"buffer_load_dwordx4 a[188:191], v210, %[srdz], s61 offen offset:3072 sc1\n" \
"s_waitcnt vmcnt(0)\n" \
"s_nop 0\n" \
"v_mfma_f32_16x16x32_bf16 v[128:131], a[196:199], a[128:131], v[192:195]\n" \
"v_mfma_f32_16x16x32_bf16 v[128:131], a[200:203], a[132:135], v[128:131]\n" \
"v_mfma_f32_16x16x32_bf16 v[128:131], a[204:207], a[136:139], v[128:131]\n" \
"v_mfma_f32_16x16x32_bf16 v[128:131], a[224:227], a[140:143], v[128:131]\n" \
"v_mfma_f32_16x16x32_bf16 v[132:135], a[196:199], a[144:147], v[196:199]\n" \
"v_mfma_f32_16x16x32_bf16 v[132:135], a[200:203], a[148:151], v[132:135]\n" \
"v_mfma_f32_16x16x32_bf16 v[132:135], a[204:207], a[152:155], v[132:135]\n" \
"v_mfma_f32_16x16x32_bf16 v[132:135], a[224:227], a[156:159], v[132:135]\n" \
"v_mfma_f32_16x16x32_bf16 v[136:139], a[196:199], a[160:163], v[200:203]\n" \
"v_mfma_f32_16x16x32_bf16 v[136:139], a[200:203], a[164:167], v[136:139]\n" \
"v_mfma_f32_16x16x32_bf16 v[136:139], a[204:207], a[168:171], v[136:139]\n" \
"v_mfma_f32_16x16x32_bf16 v[136:139], a[224:227], a[172:175], v[136:139]\n" \
"v_mfma_f32_16x16x32_bf16 v[140:143], a[196:199], a[176:179], v[204:207]\n" \
"v_mfma_f32_16x16x32_bf16 v[140:143], a[200:203], a[180:183], v[140:143]\n" \
"v_mfma_f32_16x16x32_bf16 v[140:143], a[204:207], a[184:187], v[140:143]\n" \
"v_mfma_f32_16x16x32_bf16 v[140:143], a[224:227], a[188:191], v[140:143]\n" \
"s_nop 7\n" \
"s_nop 0\n" \
"v_cmp_u_f32_e64 s[44:45], v128, v132\n" \
"v_cmp_u_f32_e64 s[56:57], v136, v140\n" \
"s_or_b64 s[56:57], s[56:57], s[44:45]\n" \
"s_cbranch_scc0 Lbk3_%=\n" \
"s_sub_u32 s51, s51, 1\n" \
"s_cbranch_scc0 Lsq3_%=\n" \
"s_branch Lbk3_%=\n" \
"Lsp4_%=:\n" \
"s_movk_i32 s51, 0x1000\n" \
"s_cmp_gt_u32 s48, 3\n" \
"s_cselect_b32 s61, 3, 4\n" \
"s_mul_i32 s61, s61, 152064\n" \
"Lsq4_%=:\n" \
"buffer_load_dwordx4 a[64:67], %[oq0], %[srdz], s61 offen offset:0 sc1\n" \
"buffer_load_dwordx4 a[68:71], %[oq0], %[srdz], s61 offen offset:1024 sc1\n" \
"buffer_load_dwordx4 a[72:75], %[oq0], %[srdz], s61 offen offset:2048 sc1\n" \
"buffer_load_dwordx4 a[76:79], %[oq0], %[srdz], s61 offen offset:3072 sc1\n" \
"buffer_load_dwordx4 a[80:83], v208, %[srdz], s61 offen offset:0 sc1\n" \
"buffer_load_dwordx4 a[84:87], v208, %[srdz], s61 offen offset:1024 sc1\n" \
"buffer_load_dwordx4 a[88:91], v208, %[srdz], s61 offen offset:2048 sc1\n" \
"buffer_load_dwordx4 a[92:95], v208, %[srdz], s61 offen offset:3072 sc1\n" \
"buffer_load_dwordx4 a[96:99], v209, %[srdz], s61 offen offset:0 sc1\n" \
"buffer_load_dwordx4 a[100:103], v209, %[srdz], s61 offen offset:1024 sc1\n" \
"buffer_load_dwordx4 a[104:107], v209, %[srdz], s61 offen offset:2048 sc1\n" \
"buffer_load_dwordx4 a[108:111], v209, %[srdz], s61 offen offset:3072 sc1\n" \
"buffer_load_dwordx4 a[112:115], v210, %[srdz], s61 offen offset:0 sc1\n" \
"buffer_load_dwordx4 a[116:119], v210, %[srdz], s61 offen offset:1024 sc1\n" \
"buffer_load_dwordx4 a[120:123], v210, %[srdz], s61 offen offset:2048 sc1\n" \
"buffer_load_dwordx4 a[124:127], v210, %[srdz], s61 offen offset:3072 sc1\n" \
"s_waitcnt vmcnt(0)\n" \
"s_nop 0\n" \
"v_mfma_f32_16x16x32_bf16 v[192:195], a[196:199], a[64:67], v[128:131]\n" \
"v_mfma_f32_16x16x32_bf16 v[192:195], a[200:203], a[68:71], v[192:195]\n" \
"v_mfma_f32_16x16x32_bf16 v[192:195], a[204:207], a[72:75], v[192:195]\n" \
"v_mfma_f32_16x16x32_bf16 v[192:195], a[220:223], a[76:79], v[192:195]\n" \
"v_mfma_f32_16x16x32_bf16 v[196:199], a[196:199], a[80:83], v[132:135]\n" \
"v_mfma_f32_16x16x32_bf16 v[196:199], a[200:203], a[84:87], v[196:199]\n" \
"v_mfma_f32_16x16x32_bf16 v[196:199], a[204:207], a[88:91], v[196:199]\n" \
"v_mfma_f32_16x16x32_bf16 v[196:199], a[220:223], a[92:95], v[196:199]\n" \
"v_mfma_f32_16x16x32_bf16 v[200:203], a[196:199], a[96:99], v[136:139]\n" \
"v_mfma_f32_16x16x32_bf16 v[200:203], a[200:203], a[100:103], v[200:203]\n" \
"v_mfma_f32_16x16x32_bf16 v[200:203], a[204:207], a[104:107], v[200:203]\n" \
"v_mfma_f32_16x16x32_bf16 v[200:203], a[220:223], a[108:111], v[200:203]\n" \
"v_mfma_f32_16x16x32_bf16 v[204:207], a[196:199], a[112:115], v[140:143]\n" \
"v_mfma_f32_16x16x32_bf16 v[204:207], a[200:203], a[116:119], v[204:207]\n" \
"v_mfma_f32_16x16x32_bf16 v[204:207], a[204:207], a[120:123], v[204:207]\n" \
"v_mfma_f32_16x16x32_bf16 v[204:207], a[220:223], a[124:127], v[204:207]\n" \
"s_nop 7\n" \
"s_nop 0\n" \
"v_cmp_u_f32_e64 s[44:45], v192, v196\n" \
"v_cmp_u_f32_e64 s[56:57], v200, v204\n" \
"s_or_b64 s[56:57], s[56:57], s[44:45]\n" \
"s_cbranch_scc0 Lbk4_%=\n" \
"s_sub_u32 s51, s51, 1\n" \
"s_cbranch_scc0 Lsq4_%=\n" \
"s_branch Lbk4_%=\n" \
"Lsp5_%=:\n" \
"s_movk_i32 s51, 0x1000\n" \
"s_cmp_gt_u32 s48, 4\n" \
"s_cselect_b32 s61, 4, 5\n" \
"s_mul_i32 s61, s61, 152064\n" \
"Lsq5_%=:\n" \
"buffer_load_dwordx4 v[0:3], %[oq0], %[srdz], s61 offen offset:0 sc1\n" \
"buffer_load_dwordx4 v[8:11], %[oq0], %[srdz], s61 offen offset:1024 sc1\n" \
"buffer_load_dwordx4 v[16:19], %[oq0], %[srdz], s61 offen offset:2048 sc1\n" \
"buffer_load_dwordx4 v[24:27], %[oq0], %[srdz], s61 offen offset:3072 sc1\n" \
"buffer_load_dwordx4 v[32:35], v208, %[srdz], s61 offen offset:0 sc1\n" \
"buffer_load_dwordx4 v[40:43], v208, %[srdz], s61 offen offset:1024 sc1\n" \
"buffer_load_dwordx4 v[48:51], v208, %[srdz], s61 offen offset:2048 sc1\n" \
"buffer_load_dwordx4 v[56:59], v208, %[srdz], s61 offen offset:3072 sc1\n" \
"buffer_load_dwordx4 v[64:67], v209, %[srdz], s61 offen offset:0 sc1\n" \
"buffer_load_dwordx4 v[72:75], v209, %[srdz], s61 offen offset:1024 sc1\n" \
"buffer_load_dwordx4 v[80:83], v209, %[srdz], s61 offen offset:2048 sc1\n" \
"buffer_load_dwordx4 v[88:91], v209, %[srdz], s61 offen offset:3072 sc1\n" \
"buffer_load_dwordx4 v[96:99], v210, %[srdz], s61 offen offset:0 sc1\n" \
"buffer_load_dwordx4 v[104:107], v210, %[srdz], s61 offen offset:1024 sc1\n" \
"buffer_load_dwordx4 v[112:115], v210, %[srdz], s61 offen offset:2048 sc1\n" \
"buffer_load_dwordx4 v[120:123], v210, %[srdz], s61 offen offset:3072 sc1\n" \
"s_waitcnt vmcnt(0)\n" \
"s_nop 0\n" \
"v_mfma_f32_16x16x32_bf16 v[128:131], a[232:235], v[0:3], v[192:195]\n" \
"v_mfma_f32_16x16x32_bf16 v[128:131], a[236:239], v[8:11], v[128:131]\n" \
"v_mfma_f32_16x16x32_bf16 v[128:131], a[208:211], v[16:19], v[128:131]\n" \
"v_mfma_f32_16x16x32_bf16 v[128:131], a[228:231], v[24:27], v[128:131]\n" \
"v_mfma_f32_16x16x32_bf16 v[132:135], a[232:235], v[32:35], v[196:199]\n" \
"v_mfma_f32_16x16x32_bf16 v[132:135], a[236:239], v[40:43], v[132:135]\n" \
"v_mfma_f32_16x16x32_bf16 v[132:135], a[208:211], v[48:51], v[132:135]\n" \
"v_mfma_f32_16x16x32_bf16 v[132:135], a[228:231], v[56:59], v[132:135]\n" \
"v_mfma_f32_16x16x32_bf16 v[136:139], a[232:235], v[64:67], v[200:203]\n" \
"v_mfma_f32_16x16x32_bf16 v[136:139], a[236:239], v[72:75], v[136:139]\n" \
"v_mfma_f32_16x16x32_bf16 v[136:139], a[208:211], v[80:83], v[136:139]\n" \
"v_mfma_f32_16x16x32_bf16 v[136:139], a[228:231], v[88:91], v[136:139]\n" \
"v_mfma_f32_16x16x32_bf16 v[140:143], a[232:235], v[96:99], v[204:207]\n" \
"v_mfma_f32_16x16x32_bf16 v[140:143], a[236:239], v[104:107], v[140:143]\n" \
"v_mfma_f32_16x16x32_bf16 v[140:143], a[208:211], v[112:115], v[140:143]\n" \
"v_mfma_f32_16x16x32_bf16 v[140:143], a[228:231], v[120:123], v[140:143]\n" \
"s_nop 7\n" \
"s_nop 0\n" \
"v_cmp_u_f32_e64 s[44:45], v128, v132\n" \
"v_cmp_u_f32_e64 s[56:57], v136, v140\n" \
"s_or_b64 s[56:57], s[56:57], s[44:45]\n" \
"s_cbranch_scc0 Lbk5_%=\n" \
"s_sub_u32 s51, s51, 1\n" \
"s_cbranch_scc0 Lsq5_%=\n" \
"s_branch Lbk5_%=\n" \
"Lsp6_%=:\n" \
"s_movk_i32 s51, 0x1000\n" \
"s_cmp_gt_u32 s48, 5\n" \
"s_cselect_b32 s61, 5, 6\n" \
"s_mul_i32 s61, s61, 152064\n" \
"Lsq6_%=:\n" \
"buffer_load_dwordx4 a[0:3], %[oq0], %[srdz], s61 offen offset:0 sc1\n" \
"buffer_load_dwordx4 a[4:7], %[oq0], %[srdz], s61 offen offset:1024 sc1\n" \
"buffer_load_dwordx4 a[8:11], %[oq0], %[srdz], s61 offen offset:2048 sc1\n" \
"buffer_load_dwordx4 a[12:15], %[oq0], %[srdz], s61 offen offset:3072 sc1\n" \
"buffer_load_dwordx4 a[16:19], v208, %[srdz], s61 offen offset:0 sc1\n" \
"buffer_load_dwordx4 a[20:23], v208, %[srdz], s61 offen offset:1024 sc1\n" \
"buffer_load_dwordx4 a[24:27], v208, %[srdz], s61 offen offset:2048 sc1\n" \
"buffer_load_dwordx4 a[28:31], v208, %[srdz], s61 offen offset:3072 sc1\n" \
"buffer_load_dwordx4 a[32:35], v209, %[srdz], s61 offen offset:0 sc1\n" \
"buffer_load_dwordx4 a[36:39], v209, %[srdz], s61 offen offset:1024 sc1\n" \
"buffer_load_dwordx4 a[40:43], v209, %[srdz], s61 offen offset:2048 sc1\n" \
"buffer_load_dwordx4 a[44:47], v209, %[srdz], s61 offen offset:3072 sc1\n" \
"buffer_load_dwordx4 a[48:51], v210, %[srdz], s61 offen offset:0 sc1\n" \
"buffer_load_dwordx4 a[52:55], v210, %[srdz], s61 offen offset:1024 sc1\n" \
"buffer_load_dwordx4 a[56:59], v210, %[srdz], s61 offen offset:2048 sc1\n" \
"buffer_load_dwordx4 a[60:63], v210, %[srdz], s61 offen offset:3072 sc1\n" \
"s_waitcnt vmcnt(0)\n" \
"s_nop 0\n" \
"v_mfma_f32_16x16x32_bf16 v[192:195], a[196:199], a[0:3], v[128:131]\n" \
"v_mfma_f32_16x16x32_bf16 v[192:195], a[200:203], a[4:7], v[192:195]\n" \
"v_mfma_f32_16x16x32_bf16 v[192:195], a[204:207], a[8:11], v[192:195]\n" \
"v_mfma_f32_16x16x32_bf16 v[192:195], a[220:223], a[12:15], v[192:195]\n" \
"v_mfma_f32_16x16x32_bf16 v[196:199], a[196:199], a[16:19], v[132:135]\n" \
"v_mfma_f32_16x16x32_bf16 v[196:199], a[200:203], a[20:23], v[196:199]\n" \
"v_mfma_f32_16x16x32_bf16 v[196:199], a[204:207], a[24:27], v[196:199]\n" \
"v_mfma_f32_16x16x32_bf16 v[196:199], a[220:223], a[28:31], v[196:199]\n" \
"v_mfma_f32_16x16x32_bf16 v[200:203], a[196:199], a[32:35], v[136:139]\n" \
"v_mfma_f32_16x16x32_bf16 v[200:203], a[200:203], a[36:39], v[200:203]\n" \
"v_mfma_f32_16x16x32_bf16 v[200:203], a[204:207], a[40:43], v[200:203]\n" \
"v_mfma_f32_16x16x32_bf16 v[200:203], a[220:223], a[44:47], v[200:203]\n" \
"v_mfma_f32_16x16x32_bf16 v[204:207], a[196:199], a[48:51], v[140:143]\n" \
"v_mfma_f32_16x16x32_bf16 v[204:207], a[200:203], a[52:55], v[204:207]\n" \
"v_mfma_f32_16x16x32_bf16 v[204:207], a[204:207], a[56:59], v[204:207]\n" \
"v_mfma_f32_16x16x32_bf16 v[204:207], a[220:223], a[60:63], v[204:207]\n" \
"s_nop 7\n" \
"s_nop 0\n" \
"v_cmp_u_f32_e64 s[44:45], v192, v196\n" \
"v_cmp_u_f32_e64 s[56:57], v200, v204\n" \
"s_or_b64 s[56:57], s[56:57], s[44:45]\n" \
"s_cbranch_scc0 Lbk6_%=\n" \
"s_sub_u32 s51, s51, 1\n" \
"s_cbranch_scc0 Lsq6_%=\n" \
"s_branch Lbk6_%=\n" \
"Lsp7_%=:\n" \
"s_movk_i32 s51, 0x1000\n" \
"s_cmp_gt_u32 s48, 6\n" \
"s_cselect_b32 s61, 6, 7\n" \
"s_mul_i32 s61, s61, 152064\n" \
"Lsq7_%=:\n" \
"buffer_load_dwordx4 a[128:131], %[oq0], %[srdz], s61 offen offset:0 sc1\n" \
"buffer_load_dwordx4 a[132:135], %[oq0], %[srdz], s61 offen offset:1024 sc1\n" \
"buffer_load_dwordx4 a[136:139], %[oq0], %[srdz], s61 offen offset:2048 sc1\n" \
"buffer_load_dwordx4 a[140:143], %[oq0], %[srdz], s61 offen offset:3072 sc1\n" \
"buffer_load_dwordx4 a[144:147], v208, %[srdz], s61 offen offset:0 sc1\n" \
"buffer_load_dwordx4 a[148:151], v208, %[srdz], s61 offen offset:1024 sc1\n" \
"buffer_load_dwordx4 a[152:155], v208, %[srdz], s61 offen offset:2048 sc1\n" \
"buffer_load_dwordx4 a[156:159], v208, %[srdz], s61 offen offset:3072 sc1\n" \
"buffer_load_dwordx4 a[160:163], v209, %[srdz], s61 offen offset:0 sc1\n" \
"buffer_load_dwordx4 a[164:167], v209, %[srdz], s61 offen offset:1024 sc1\n" \
"buffer_load_dwordx4 a[168:171], v209, %[srdz], s61 offen offset:2048 sc1\n" \
"buffer_load_dwordx4 a[172:175], v209, %[srdz], s61 offen offset:3072 sc1\n" \
"buffer_load_dwordx4 a[176:179], v210, %[srdz], s61 offen offset:0 sc1\n" \
"buffer_load_dwordx4 a[180:183], v210, %[srdz], s61 offen offset:1024 sc1\n" \
"buffer_load_dwordx4 a[184:187], v210, %[srdz], s61 offen offset:2048 sc1\n" \
"buffer_load_dwordx4 a[188:191], v210, %[srdz], s61 offen offset:3072 sc1\n" \
"s_waitcnt vmcnt(0)\n" \
"s_nop 0\n" \
"v_mfma_f32_16x16x32_bf16 v[128:131], a[208:211], a[128:131], v[192:195]\n" \
"v_mfma_f32_16x16x32_bf16 v[128:131], a[224:227], a[132:135], v[128:131]\n" \
"v_mfma_f32_16x16x32_bf16 v[128:131], a[228:231], a[136:139], v[128:131]\n" \
"v_mfma_f32_16x16x32_bf16 v[128:131], a[236:239], a[140:143], v[128:131]\n" \
"v_mfma_f32_16x16x32_bf16 v[132:135], a[208:211], a[144:147], v[196:199]\n" \
"v_mfma_f32_16x16x32_bf16 v[132:135], a[224:227], a[148:151], v[132:135]\n" \
"v_mfma_f32_16x16x32_bf16 v[132:135], a[228:231], a[152:155], v[132:135]\n" \
"v_mfma_f32_16x16x32_bf16 v[132:135], a[236:239], a[156:159], v[132:135]\n" \
"v_mfma_f32_16x16x32_bf16 v[136:139], a[208:211], a[160:163], v[200:203]\n" \
"v_mfma_f32_16x16x32_bf16 v[136:139], a[224:227], a[164:167], v[136:139]\n" \
"v_mfma_f32_16x16x32_bf16 v[136:139], a[228:231], a[168:171], v[136:139]\n" \
"v_mfma_f32_16x16x32_bf16 v[136:139], a[236:239], a[172:175], v[136:139]\n" \
"v_mfma_f32_16x16x32_bf16 v[140:143], a[208:211], a[176:179], v[204:207]\n" \
"v_mfma_f32_16x16x32_bf16 v[140:143], a[224:227], a[180:183], v[140:143]\n" \
"v_mfma_f32_16x16x32_bf16 v[140:143], a[228:231], a[184:187], v[140:143]\n" \
"v_mfma_f32_16x16x32_bf16 v[140:143], a[236:239], a[188:191], v[140:143]\n" \
"s_nop 7\n" \
"s_nop 0\n" \
"v_cmp_u_f32_e64 s[44:45], v128, v132\n" \
"v_cmp_u_f32_e64 s[56:57], v136, v140\n" \
"s_or_b64 s[56:57], s[56:57], s[44:45]\n" \
"s_cbranch_scc0 Lbk7_%=\n" \
"s_sub_u32 s51, s51, 1\n" \
"s_cbranch_scc0 Lsq7_%=\n" \
"s_branch Lbk7_%=\n" \
"Lspk_%=:\n" \
"s_nop 0\n" \
"s_mov_b32 s51, 0x10000\n" \
"Lsqk_%=:\n" \
"buffer_load_dword v229, v226, %[srdz], s50 offen sc1\n" \
"s_waitcnt vmcnt(0)\n" \
"s_nop 0\n" \
"v_cmp_eq_u32_e64 s[56:57], 0, v229\n" \
"s_cmp_lg_u64 s[56:57], 0\n" \
"s_cbranch_scc0 Lbkk_%=\n" \
"s_sub_u32 s51, s51, 1\n" \
"s_cbranch_scc0 Lsqk_%=\n" \
"s_branch Lbkk_%=\n" \
"Lspp_%=:\n" \
"s_nop 0\n" \
"s_mov_b32 s51, 0x10000\n" \
"Lsqp_%=:\n" \
"buffer_load_dword v232, v226, %[srdz], s54 offen sc1\n" \
"s_waitcnt vmcnt(0)\n" \
"v_cmp_eq_u32_e32 vcc, 0, v232\n" \
"s_cbranch_vccz Lbkp_%=\n" \
"s_sub_u32 s51, s51, 1\n" \
"s_cbranch_scc0 Lsqp_%=\n" \
"s_branch Lbkp_%=\n" \
"Lpars_%=:\n" \
"s_mov_b32 s51, 0x10000\n" \
"s_lshl_b32 s61, %[wid], 10\n" \
"v_lshrrev_b32_e32 v235, 3, v224\n" \
"v_add_u32_e64 v235, s61, v235\n" \
"Lpar1_%=:\n" \
"s_mov_b32 s61, 262144\n" \
"s_nop 0\n" \
"s_nop 0\n" \
"buffer_load_dwordx4 v[192:195], v235, %[srdz], s61 offen offset:0 sc1\n" \
"s_mov_b32 s61, 266240\n" \
"s_nop 0\n" \
"s_nop 0\n" \
"buffer_load_dwordx4 v[196:199], v235, %[srdz], s61 offen offset:0 sc1\n" \
"s_mov_b32 s61, 270336\n" \
"s_nop 0\n" \
"s_nop 0\n" \
"buffer_load_dwordx4 v[200:203], v235, %[srdz], s61 offen offset:0 sc1\n" \
"s_mov_b32 s61, 274432\n" \
"s_nop 0\n" \
"s_nop 0\n" \
"buffer_load_dwordx4 v[204:207], v235, %[srdz], s61 offen offset:0 sc1\n" \
"s_waitcnt vmcnt(0)\n" \
"s_nop 0\n" \
"v_and_or_b32 v236, v192, v193, 0\n" \
"v_bitop3_b32 v236, v236, v194, v195 bitop3:0x80\n" \
"v_bitop3_b32 v236, v236, v196, v197 bitop3:0x80\n" \
"v_bitop3_b32 v236, v236, v198, v199 bitop3:0x80\n" \
"v_bitop3_b32 v236, v236, v200, v201 bitop3:0x80\n" \
"v_bitop3_b32 v236, v236, v202, v203 bitop3:0x80\n" \
"v_bitop3_b32 v236, v236, v204, v205 bitop3:0x80\n" \
"v_bitop3_b32 v236, v236, v206, v207 bitop3:0x80\n" \
"v_and_b32_e64 v236, 1, v236\n" \
"v_cmp_eq_u32_e64 s[56:57], 0, v236\n" \
"s_cmp_lg_u64 s[56:57], 0\n" \
"s_cbranch_scc0 Lparb_%=\n" \
"s_sub_u32 s51, s51, 1\n" \
"s_cbranch_scc0 Lpar1_%=\n" \
"s_branch Lparb_%=\n" \
"Ls0_%=:\n" \
"s_nop 0\n" \
"v_readlane_b32 s62, %[tlo], 32\n" \
"v_lshrrev_b32_e64 v237, 7, v224\n" \
"v_and_b32_e32 v237, 48, v237\n" \
"s_movk_i32 s51, 0x1000\n" \
"s_waitcnt vmcnt(0)\n" \
"Ls01_%=:\n" \
"s_nop 0\n" \
"buffer_load_dwordx4 a[176:179], v237, %[srdz], s62 offen offset:0 sc1\n" \
"buffer_load_dwordx4 a[180:183], v237, %[srdz], s62 offen offset:64 sc1\n" \
"buffer_load_dwordx4 a[184:187], v237, %[srdz], s62 offen offset:128 sc1\n" \
"buffer_load_dwordx4 a[188:191], v237, %[srdz], s62 offen offset:192 sc1\n" \
"s_waitcnt vmcnt(0)\n" \
"s_nop 0\n" \
"v_mfma_f32_16x16x32_bf16 v[216:219], a[176:179], v[0:3], 0\n" \
"v_mfma_f32_16x16x32_bf16 v[216:219], a[180:183], v[8:11], v[216:219]\n" \
"v_mfma_f32_16x16x32_bf16 v[216:219], a[184:187], v[16:19], v[216:219]\n" \
"v_mfma_f32_16x16x32_bf16 v[216:219], a[188:191], v[24:27], v[216:219]\n" \
"s_nop 7\n" \
"s_nop 0\n" \
"v_mov_b32_dpp v231, v216 quad_perm:[0,1,2,3] row_mask:0x1 bank_mask:0xf\n" \
"v_mfma_f32_16x16x32_bf16 v[232:235], a[176:179], v[32:35], 0\n" \
"v_mfma_f32_16x16x32_bf16 v[232:235], a[180:183], v[40:43], v[232:235]\n" \
"v_mfma_f32_16x16x32_bf16 v[232:235], a[184:187], v[48:51], v[232:235]\n" \
"v_mfma_f32_16x16x32_bf16 v[232:235], a[188:191], v[56:59], v[232:235]\n" \
"s_nop 7\n" \
"s_nop 0\n" \
"v_mov_b32_dpp v231, v232 quad_perm:[0,1,2,3] row_mask:0x2 bank_mask:0xf\n" \
"v_mfma_f32_16x16x32_bf16 v[216:219], a[176:179], v[64:67], 0\n" \
"v_mfma_f32_16x16x32_bf16 v[216:219], a[180:183], v[72:75], v[216:219]\n" \
"v_mfma_f32_16x16x32_bf16 v[216:219], a[184:187], v[80:83], v[216:219]\n" \
"v_mfma_f32_16x16x32_bf16 v[216:219], a[188:191], v[88:91], v[216:219]\n" \
"s_nop 7\n" \
"s_nop 0\n" \
"v_mov_b32_dpp v231, v216 quad_perm:[0,1,2,3] row_mask:0x4 bank_mask:0xf\n" \
"v_mfma_f32_16x16x32_bf16 v[232:235], a[176:179], v[96:99], 0\n" \
"v_mfma_f32_16x16x32_bf16 v[232:235], a[180:183], v[104:107], v[232:235]\n" \
"v_mfma_f32_16x16x32_bf16 v[232:235], a[184:187], v[112:115], v[232:235]\n" \
"v_mfma_f32_16x16x32_bf16 v[232:235], a[188:191], v[120:123], v[232:235]\n" \
"s_nop 7\n" \
"s_nop 0\n" \
"v_mov_b32_dpp v231, v232 quad_perm:[0,1,2,3] row_mask:0x8 bank_mask:0xf\n" \
"v_cmp_u_f32_e64 s[42:43], v231, v231\n" \
"s_cmp_lg_u64 s[42:43], 0\n" \
"s_cbranch_scc0 Ls02_%=\n" \
"s_sub_u32 s51, s51, 1\n" \
"s_cbranch_scc0 Ls01_%=\n" \
"Ls02_%=:\n" \
"v_readlane_b32 s62, %[tlo], 19\n" \
"s_branch Ls0b_%=\n" \
"Ls1_%=:\n" \
"s_nop 0\n" \
"s_mov_b32 s51, 0x10000\n" \
"Ls11_%=:\n" \
"buffer_load_dword v235, v234, %[srdz], s63 offen sc1\n" \
"s_waitcnt vmcnt(0)\n" \
"v_and_b32_e32 v230, 1, v235\n" \
"v_cmp_eq_u32_e64 s[68:69], 0, v230\n" \
"s_cmp_lg_u64 s[68:69], 0\n" \
"s_cbranch_scc0 Ls1b_%=\n" \
"s_sub_u32 s51, s51, 1\n" \
"s_cbranch_scc0 Ls11_%=\n" \
"s_branch Ls1b_%=\n" \
"Lus_%=:\n" \
"s_nop 0\n" \
"s_mov_b32 s51, 0x10000\n" \
"Lus1_%=:\n" \
"s_lshl_b32 s62, %[wid], 10\n" \
"s_nop 0\n" \
"s_add_u32 s62, s62, 262144\n" \
"s_add_u32 s55, s62, 8192\n" \
"v_readlane_b32 s68, %[tlo], 20\n" \
"v_readlane_b32 s69, %[thi], 20\n" \
"s_lshl_b32 s63, %[wid], 9\n" \
"v_lshrrev_b32_e32 v230, 1, v235\n" \
"buffer_load_dwordx4 a[200:203], v235, %[srdz], s62 offen sc1\n" \
"v_add_u32_e64 v230, s63, v230\n" \
"buffer_load_dwordx4 a[204:207], v235, %[srdz], s55 offen sc1\n" \
"s_nop 1\n" \
"s_nop 0\n" \
"global_load_dwordx2 a[208:209], v230, s[68:69]\n" \
"s_waitcnt vmcnt(0)\n" \
"s_nop 0\n" \
"v_accvgpr_read_b32 v230, a208\n" \
"v_accvgpr_read_b32 v234, a204\n" \
"v_accvgpr_read_b32 v225, a205\n" \
"v_accvgpr_read_b32 v226, a200\n" \
"v_accvgpr_read_b32 v231, a201\n" \
"v_bitop3_b32 v229, v234, v225, -1 bitop3:0x80\n" \
"v_bitop3_b32 v229, v226, v231, v229 bitop3:0x80\n" \
"v_lshlrev_b32_e64 v236, 16, v230\n" \
"v_and_b32_e32 v230, 0xffff0000, v230\n" \
"v_mul_f32_e32 v234, v234, v236\n" \
"v_mul_f32_e32 v225, v225, v230\n" \
"v_mul_f32_e32 v226, v226, v234\n" \
"v_mul_f32_e32 v231, v231, v225\n" \
"v_cvt_pk_bf16_f32 v236, v234, v225\n" \
"v_cvt_pk_bf16_f32 v232, v226, v231\n" \
"v_accvgpr_read_b32 v230, a209\n" \
"v_accvgpr_read_b32 v234, a206\n" \
"v_accvgpr_read_b32 v225, a207\n" \
"v_accvgpr_read_b32 v226, a202\n" \
"v_accvgpr_read_b32 v231, a203\n" \
"v_bitop3_b32 v229, v234, v225, v229 bitop3:0x80\n" \
"v_bitop3_b32 v229, v226, v231, v229 bitop3:0x80\n" \
"v_lshlrev_b32_e64 v237, 16, v230\n" \
"v_and_b32_e32 v230, 0xffff0000, v230\n" \
"v_mul_f32_e32 v234, v234, v237\n" \
"v_mul_f32_e32 v225, v225, v230\n" \
"v_mul_f32_e32 v226, v226, v234\n" \
"v_mul_f32_e32 v231, v231, v225\n" \
"v_cvt_pk_bf16_f32 v237, v234, v225\n" \
"v_cvt_pk_bf16_f32 v233, v226, v231\n" \
"v_lshrrev_b32_e64 v230, 1, v235\n" \
"s_add_u32 s62, %[m0t], 6144\n" \
"v_add_u32_e32 v230, s62, v230\n" \
"v_and_b32_e32 v229, 1, v229\n" \
"v_cmp_eq_u32_e64 s[68:69], 0, v229\n" \
"ds_write_b64 v230, v[232:233] offset:0\n" \
"v_and_b32_e32 v230, 0x170, v235\n" \
"v_lshrrev_b32_e64 v234, 7, v224\n" \
"v_and_or_b32 v230, v234, 8, v230\n" \
"v_lshrrev_b32_e64 v234, 12, v224\n" \
"v_lshl_or_b32 v230, v234, 7, v230\n" \
"v_add_u32_e64 v230, s62, v230\n" \
"ds_write_b64 v230, v[236:237] offset:512\n" \
"s_cmp_lg_u64 s[68:69], 0\n" \
"s_cbranch_scc0 Lub_%=\n" \
"s_sub_u32 s51, s51, 1\n" \
"s_cbranch_scc0 Lus1_%=\n" \
"s_branch Lub_%=\n" \
"s_nop 0\n" \
"Lwalk_%=:\n" \
"s_mov_b64 exec, 0\n" \
"s_cmp_eq_u32 %[wid], 1\n" \
"s_cbranch_scc1 Lk0_%=\n" \
"s_cmp_eq_u32 %[wid], 2\n" \
"s_cbranch_scc1 Lk6_%=\n" \
"s_branch Lk12_%=\n" \
"adaln_kr:\n" \
"s_mov_b64 exec, -1\n" \
"s_branch Lgo_%=\n" \
"Lslow_%=:\n" \
"s_mov_b32 s52, 1\n" \
"s_mov_b32 s48, 0\n" \
"s_mov_b32 s51, 32\n" \
"s_nop 0\n" \
"s_mov_b32 s62, 0x80000000\n" \
"s_mov_b32 s63, 0x80000000\n" \
"s_waitcnt vmcnt(0)\n" \
"s_nop 0\n" \
"v_readlane_b32 s64, %[tlo], 9\n" \
"v_readlane_b32 s65, %[thi], 9\n" \
"s_mov_b32 s66, %[wbytes]\n" \
"v_mov_b32_e32 v229, %[vg0]\n" \
"s_mov_b32 s44, %[m0t]\n" \
"s_mov_b32 s45, 0\n" \
"v_xor_b32_e64 v208, 64, %[kb0]\n" \
"v_xor_b32_e32 v209, 0x80, %[kb0]\n" \
"v_xor_b32_e32 v210, 0xc0, %[kb0]\n" \
"v_xor_b32_e64 v212, 64, %[kd0]\n" \
"v_xor_b32_e32 v213, 0x80, %[kd0]\n" \
"v_xor_b32_e32 v214, 0xc0, %[kd0]\n" \
"v_xor_b32_e64 v211, 32, %[vvb]\n" \
"v_add_u32_e32 v215, 0xfffffc80, %[vd0]\n" \
"s_mov_b32 s66, 0\n" \
"s_nop 0\n" \
"v_mov_b32_e32 v225, 0xff800000\n" \
"v_mov_b32_e32 v226, 0xff800000\n" \
"v_mov_b32_e32 v227, 0xff800000\n" \
"v_mov_b32_e32 v228, 0xff800000\n" \
"s_waitcnt vmcnt(0) lgkmcnt(0)\n" \
"s_barrier\n" \
"Lsl_%=:\n" \
"s_mov_b32 m0, s40\n" \
"s_nop 0\n" \
"buffer_load_dwordx4 %[kd0], %[srdk], s48 offen offset:0 lds\n" \
"buffer_load_dwordx4 v212, %[srdk], s48 offen offset:1024 lds\n" \
"buffer_load_dwordx4 v213, %[srdk], s48 offen offset:2048 lds\n" \
"buffer_load_dwordx4 v214, %[srdk], s48 offen offset:3072 lds\n" \
"s_waitcnt vmcnt(0)\n" \
"s_barrier\n" \
"ds_read_b128 a[64:67], %[kb0] offset:0\n" \
"ds_read_b128 a[68:71], v208 offset:0\n" \
"ds_read_b128 a[72:75], v209 offset:0\n" \
"ds_read_b128 a[76:79], v210 offset:0\n" \
"ds_read_b128 a[80:83], %[kb0] offset:4096\n" \
"ds_read_b128 a[84:87], v208 offset:4096\n" \
"ds_read_b128 a[88:91], v209 offset:4096\n" \
"ds_read_b128 a[92:95], v210 offset:4096\n" \
"ds_read_b128 a[96:99], %[kb0] offset:8192\n" \
"ds_read_b128 a[100:103], v208 offset:8192\n" \
"ds_read_b128 a[104:107], v209 offset:8192\n" \
"ds_read_b128 a[108:111], v210 offset:8192\n" \
"ds_read_b128 a[112:115], %[kb0] offset:12288\n" \
"ds_read_b128 a[116:119], v208 offset:12288\n" \
"ds_read_b128 a[120:123], v209 offset:12288\n" \
"ds_read_b128 a[124:127], v210 offset:12288\n" \
"s_waitcnt lgkmcnt(0)\n" \
"s_barrier\n" \
"v_mfma_f32_16x16x32_bf16 v[128:131], a[64:67], a[0:3], 0\n" \
"v_mfma_f32_16x16x32_bf16 v[128:131], a[68:71], a[4:7], v[128:131]\n" \
"v_mfma_f32_16x16x32_bf16 v[128:131], a[72:75], a[8:11], v[128:131]\n" \
"v_mfma_f32_16x16x32_bf16 v[128:131], a[76:79], a[12:15], v[128:131]\n" \
"v_mfma_f32_16x16x32_bf16 v[132:135], a[80:83], a[0:3], 0\n" \
"v_mfma_f32_16x16x32_bf16 v[132:135], a[84:87], a[4:7], v[132:135]\n" \
"v_mfma_f32_16x16x32_bf16 v[132:135], a[88:91], a[8:11], v[132:135]\n" \
"v_mfma_f32_16x16x32_bf16 v[132:135], a[92:95], a[12:15], v[132:135]\n" \
"v_mfma_f32_16x16x32_bf16 v[136:139], a[64:67], a[16:19], 0\n" \
"v_mfma_f32_16x16x32_bf16 v[136:139], a[68:71], a[20:23], v[136:139]\n" \
"v_mfma_f32_16x16x32_bf16 v[136:139], a[72:75], a[24:27], v[136:139]\n" \
"v_mfma_f32_16x16x32_bf16 v[136:139], a[76:79], a[28:31], v[136:139]\n" \
"v_mfma_f32_16x16x32_bf16 v[140:143], a[80:83], a[16:19], 0\n" \
"v_mfma_f32_16x16x32_bf16 v[140:143], a[84:87], a[20:23], v[140:143]\n" \
"v_mfma_f32_16x16x32_bf16 v[140:143], a[88:91], a[24:27], v[140:143]\n" \
"v_mfma_f32_16x16x32_bf16 v[140:143], a[92:95], a[28:31], v[140:143]\n" \
"v_mfma_f32_16x16x32_bf16 v[144:147], a[64:67], a[32:35], 0\n" \
"v_mfma_f32_16x16x32_bf16 v[144:147], a[68:71], a[36:39], v[144:147]\n" \
"v_mfma_f32_16x16x32_bf16 v[144:147], a[72:75], a[40:43], v[144:147]\n" \
"v_mfma_f32_16x16x32_bf16 v[144:147], a[76:79], a[44:47], v[144:147]\n" \
"v_mfma_f32_16x16x32_bf16 v[148:151], a[80:83], a[32:35], 0\n" \
"v_mfma_f32_16x16x32_bf16 v[148:151], a[84:87], a[36:39], v[148:151]\n" \
"v_mfma_f32_16x16x32_bf16 v[148:151], a[88:91], a[40:43], v[148:151]\n" \
"v_mfma_f32_16x16x32_bf16 v[148:151], a[92:95], a[44:47], v[148:151]\n" \
"v_mfma_f32_16x16x32_bf16 v[152:155], a[64:67], a[48:51], 0\n" \
"v_mfma_f32_16x16x32_bf16 v[152:155], a[68:71], a[52:55], v[152:155]\n" \
"v_mfma_f32_16x16x32_bf16 v[152:155], a[72:75], a[56:59], v[152:155]\n" \
"v_mfma_f32_16x16x32_bf16 v[152:155], a[76:79], a[60:63], v[152:155]\n" \
"v_mfma_f32_16x16x32_bf16 v[156:159], a[80:83], a[48:51], 0\n" \
"v_mfma_f32_16x16x32_bf16 v[156:159], a[84:87], a[52:55], v[156:159]\n" \
"v_mfma_f32_16x16x32_bf16 v[156:159], a[88:91], a[56:59], v[156:159]\n" \
"v_mfma_f32_16x16x32_bf16 v[156:159], a[92:95], a[60:63], v[156:159]\n" \
"s_nop 7\n" \
"s_nop 0\n" \
"v_max3_f32 v230, v128, v129, v130\n" \
"v_max3_f32 v231, v131, v132, v133\n" \
"v_max3_f32 v230, v230, v134, v135\n" \
"v_max3_f32 v225, v230, v231, v225\n" \
"v_max3_f32 v230, v136, v137, v138\n" \
"v_max3_f32 v231, v139, v140, v141\n" \
"v_max3_f32 v230, v230, v142, v143\n" \
"v_max3_f32 v226, v230, v231, v226\n" \
"v_max3_f32 v230, v144, v145, v146\n" \
"v_max3_f32 v231, v147, v148, v149\n" \
"v_max3_f32 v230, v230, v150, v151\n" \
"v_max3_f32 v227, v230, v231, v227\n" \
"v_max3_f32 v230, v152, v153, v154\n" \
"v_max3_f32 v231, v155, v156, v157\n" \
"v_max3_f32 v230, v230, v158, v159\n" \
"v_max3_f32 v228, v230, v231, v228\n" \
"s_nop 3\n" \
"s_nop 0\n" \
"v_mfma_f32_16x16x32_bf16 v[128:131], a[96:99], a[0:3], 0\n" \
"v_mfma_f32_16x16x32_bf16 v[128:131], a[100:103], a[4:7], v[128:131]\n" \
"v_mfma_f32_16x16x32_bf16 v[128:131], a[104:107], a[8:11], v[128:131]\n" \
"v_mfma_f32_16x16x32_bf16 v[128:131], a[108:111], a[12:15], v[128:131]\n" \
"v_mfma_f32_16x16x32_bf16 v[132:135], a[112:115], a[0:3], 0\n" \
"v_mfma_f32_16x16x32_bf16 v[132:135], a[116:119], a[4:7], v[132:135]\n" \
"v_mfma_f32_16x16x32_bf16 v[132:135], a[120:123], a[8:11], v[132:135]\n" \
"v_mfma_f32_16x16x32_bf16 v[132:135], a[124:127], a[12:15], v[132:135]\n" \
"v_mfma_f32_16x16x32_bf16 v[136:139], a[96:99], a[16:19], 0\n" \
"v_mfma_f32_16x16x32_bf16 v[136:139], a[100:103], a[20:23], v[136:139]\n" \
"v_mfma_f32_16x16x32_bf16 v[136:139], a[104:107], a[24:27], v[136:139]\n" \
"v_mfma_f32_16x16x32_bf16 v[136:139], a[108:111], a[28:31], v[136:139]\n" \
"v_mfma_f32_16x16x32_bf16 v[140:143], a[112:115], a[16:19], 0\n" \
"v_mfma_f32_16x16x32_bf16 v[140:143], a[116:119], a[20:23], v[140:143]\n" \
"v_mfma_f32_16x16x32_bf16 v[140:143], a[120:123], a[24:27], v[140:143]\n" \
"v_mfma_f32_16x16x32_bf16 v[140:143], a[124:127], a[28:31], v[140:143]\n" \
"v_mfma_f32_16x16x32_bf16 v[144:147], a[96:99], a[32:35], 0\n" \
"v_mfma_f32_16x16x32_bf16 v[144:147], a[100:103], a[36:39], v[144:147]\n" \
"v_mfma_f32_16x16x32_bf16 v[144:147], a[104:107], a[40:43], v[144:147]\n" \
"v_mfma_f32_16x16x32_bf16 v[144:147], a[108:111], a[44:47], v[144:147]\n" \
"v_mfma_f32_16x16x32_bf16 v[148:151], a[112:115], a[32:35], 0\n" \
"v_mfma_f32_16x16x32_bf16 v[148:151], a[116:119], a[36:39], v[148:151]\n" \
"v_mfma_f32_16x16x32_bf16 v[148:151], a[120:123], a[40:43], v[148:151]\n" \
"v_mfma_f32_16x16x32_bf16 v[148:151], a[124:127], a[44:47], v[148:151]\n" \
"v_mfma_f32_16x16x32_bf16 v[152:155], a[96:99], a[48:51], 0\n" \
"v_mfma_f32_16x16x32_bf16 v[152:155], a[100:103], a[52:55], v[152:155]\n" \
"v_mfma_f32_16x16x32_bf16 v[152:155], a[104:107], a[56:59], v[152:155]\n" \
"v_mfma_f32_16x16x32_bf16 v[152:155], a[108:111], a[60:63], v[152:155]\n" \
"v_mfma_f32_16x16x32_bf16 v[156:159], a[112:115], a[48:51], 0\n" \
"v_mfma_f32_16x16x32_bf16 v[156:159], a[116:119], a[52:55], v[156:159]\n" \
"v_mfma_f32_16x16x32_bf16 v[156:159], a[120:123], a[56:59], v[156:159]\n" \
"v_mfma_f32_16x16x32_bf16 v[156:159], a[124:127], a[60:63], v[156:159]\n" \
"s_nop 7\n" \
"s_nop 0\n" \
"v_max3_f32 v230, v128, v129, v130\n" \
"v_max3_f32 v231, v131, v132, v133\n" \
"v_max3_f32 v230, v230, v134, v135\n" \
"v_max3_f32 v225, v230, v231, v225\n" \
"v_max3_f32 v230, v136, v137, v138\n" \
"v_max3_f32 v231, v139, v140, v141\n" \
"v_max3_f32 v230, v230, v142, v143\n" \
"v_max3_f32 v226, v230, v231, v226\n" \
"v_max3_f32 v230, v144, v145, v146\n" \
"v_max3_f32 v231, v147, v148, v149\n" \
"v_max3_f32 v230, v230, v150, v151\n" \
"v_max3_f32 v227, v230, v231, v227\n" \
"v_max3_f32 v230, v152, v153, v154\n" \
"v_max3_f32 v231, v155, v156, v157\n" \
"v_max3_f32 v230, v230, v158, v159\n" \
"v_max3_f32 v228, v230, v231, v228\n" \
"s_nop 3\n" \
"s_add_u32 s48, s48, s54\n" \
"s_sub_u32 s51, s51, 1\n" \
"s_cmp_lg_u32 s51, 0\n" \
"s_cbranch_scc1 Lsl_%=\n" \
"s_nop 0\n" \
"v_mov_b32_e32 v232, v225\n" \
"v_mov_b32_e32 v233, v226\n" \
"v_mov_b32_e32 v234, v227\n" \
"v_mov_b32_e32 v235, v228\n" \
"s_nop 0\n" \
"v_permlane16_swap_b32_e32 v225, v232\n" \
"v_permlane16_swap_b32_e32 v226, v233\n" \
"v_permlane16_swap_b32_e32 v227, v234\n" \
"v_permlane16_swap_b32_e32 v228, v235\n" \
"v_max_f32_e32 v225, v225, v232\n" \
"v_max_f32_e32 v226, v226, v233\n" \
"v_max_f32_e32 v227, v227, v234\n" \
"v_max_f32_e32 v228, v228, v235\n" \
"v_mov_b32_e32 v232, v225\n" \
"v_mov_b32_e32 v233, v226\n" \
"v_mov_b32_e32 v234, v227\n" \
"v_mov_b32_e32 v235, v228\n" \
"s_nop 0\n" \
"v_permlane32_swap_b32_e32 v225, v232\n" \
"v_permlane32_swap_b32_e32 v226, v233\n" \
"v_permlane32_swap_b32_e32 v227, v234\n" \
"v_permlane32_swap_b32_e32 v228, v235\n" \
"v_max_f32_e32 v225, v225, v232\n" \
"v_max_f32_e32 v226, v226, v233\n" \
"v_max_f32_e32 v227, v227, v234\n" \
"v_max_f32_e32 v228, v228, v235\n" \
"s_cmp_lg_u32 s69, 0\n" \
"s_cbranch_scc1 Lnewm_%=\n" \
"v_sub_f32_e32 v225, 0, v160\n" \
"v_sub_f32_e32 v226, 0, v164\n" \
"v_sub_f32_e64 v227, 0, v168\n" \
"v_sub_f32_e32 v228, 0, v172\n" \
"Lnewm_%=:\n" \
"s_mov_b32 s48, 0\n" \
"s_mov_b32 m0, s40\n" \
"s_nop 0\n" \
"buffer_load_dwordx4 %[kd0], %[srdk], s48 offen offset:0 lds\n" \
"buffer_load_dwordx4 v212, %[srdk], s48 offen offset:1024 lds\n" \
"buffer_load_dwordx4 v213, %[srdk], s48 offen offset:2048 lds\n" \
"buffer_load_dwordx4 v214, %[srdk], s48 offen offset:3072 lds\n" \
"s_mov_b32 s48, 16384\n" \
"s_mov_b32 m0, s41\n" \
"s_nop 0\n" \
"buffer_load_dwordx4 %[kd0], %[srdk], s48 offen offset:0 lds\n" \
"buffer_load_dwordx4 v212, %[srdk], s48 offen offset:1024 lds\n" \
"buffer_load_dwordx4 v213, %[srdk], s48 offen offset:2048 lds\n" \
"buffer_load_dwordx4 v214, %[srdk], s48 offen offset:3072 lds\n" \
"s_add_u32 m0, %[m0b], 65536\n" \
"s_nop 0\n" \
"s_nop 0\n" \
"buffer_load_dwordx4 %[vd0], %[srdv], s48 offen offset:0 lds\n" \
"buffer_load_dwordx4 v215, %[srdv], s48 offen offset:1024 lds\n" \
"buffer_load_dwordx4 %[vd0], %[srdv], s48 offen offset:2048 lds\n" \
"buffer_load_dwordx4 v215, %[srdv], s48 offen offset:3072 lds\n" \
"s_mov_b32 s48, 32768\n" \
"s_mov_b32 m0, s42\n" \
"s_nop 0\n" \
"buffer_load_dwordx4 %[kd0], %[srdk], s48 offen offset:0 lds\n" \
"buffer_load_dwordx4 v212, %[srdk], s48 offen offset:1024 lds\n" \
"buffer_load_dwordx4 v213, %[srdk], s48 offen offset:2048 lds\n" \
"buffer_load_dwordx4 v214, %[srdk], s48 offen offset:3072 lds\n" \
"s_add_u32 m0, %[m0b], 81920\n" \
"s_nop 0\n" \
"s_nop 0\n" \
"buffer_load_dwordx4 %[vd0], %[srdv], s48 offen offset:0 lds\n" \
"buffer_load_dwordx4 v215, %[srdv], s48 offen offset:1024 lds\n" \
"buffer_load_dwordx4 %[vd0], %[srdv], s48 offen offset:2048 lds\n" \
"buffer_load_dwordx4 v215, %[srdv], s48 offen offset:3072 lds\n" \
"s_mov_b32 s48, 49152\n" \
"s_mov_b32 m0, s43\n" \
"s_nop 0\n" \
"buffer_load_dwordx4 %[kd0], %[srdk], s48 offen offset:0 lds\n" \
"buffer_load_dwordx4 v212, %[srdk], s48 offen offset:1024 lds\n" \
"buffer_load_dwordx4 v213, %[srdk], s48 offen offset:2048 lds\n" \
"buffer_load_dwordx4 v214, %[srdk], s48 offen offset:3072 lds\n" \
"s_add_u32 m0, %[m0b], 98304\n" \
"s_nop 0\n" \
"s_nop 0\n" \
"buffer_load_dwordx4 %[vd0], %[srdv], s48 offen offset:0 lds\n" \
"buffer_load_dwordx4 v215, %[srdv], s48 offen offset:1024 lds\n" \
"buffer_load_dwordx4 %[vd0], %[srdv], s48 offen offset:2048 lds\n" \
"buffer_load_dwordx4 v215, %[srdv], s48 offen offset:3072 lds\n" \
"s_waitcnt vmcnt(0)\n" \
"s_barrier\n" \
"ds_read_b128 a[64:67], %[kb0] offset:0\n" \
"ds_read_b128 a[68:71], v208 offset:0\n" \
"ds_read_b128 a[72:75], v209 offset:0\n" \
"ds_read_b128 a[76:79], v210 offset:0\n" \
"ds_read_b128 a[80:83], %[kb0] offset:4096\n" \
"ds_read_b128 a[84:87], v208 offset:4096\n" \
"ds_read_b128 a[88:91], v209 offset:4096\n" \
"ds_read_b128 a[92:95], v210 offset:4096\n" \
"s_mov_b32 s48, 65536\n" \
"s_mov_b32 s60, 0\n" \
"s_waitcnt lgkmcnt(0)\n" \
"v_mfma_f32_16x16x32_bf16 v[128:131], a[64:67], a[0:3], 0\n" \
"ds_read_b128 a[96:99], %[kb0] offset:8192\n" \
"v_mfma_f32_16x16x32_bf16 v[128:131], a[68:71], a[4:7], v[128:131]\n" \
"ds_read_b128 a[100:103], v208 offset:8192\n" \
"v_mfma_f32_16x16x32_bf16 v[128:131], a[72:75], a[8:11], v[128:131]\n" \
"ds_read_b128 a[104:107], v209 offset:8192\n" \
"v_mfma_f32_16x16x32_bf16 v[128:131], a[76:79], a[12:15], v[128:131]\n" \
"ds_read_b128 a[108:111], v210 offset:8192\n" \
"v_mfma_f32_16x16x32_bf16 v[132:135], a[80:83], a[0:3], 0\n" \
"ds_read_b128 a[112:115], %[kb0] offset:12288\n" \
"v_mfma_f32_16x16x32_bf16 v[132:135], a[84:87], a[4:7], v[132:135]\n" \
"ds_read_b128 a[116:119], v208 offset:12288\n" \
"v_mfma_f32_16x16x32_bf16 v[132:135], a[88:91], a[8:11], v[132:135]\n" \
"ds_read_b128 a[120:123], v209 offset:12288\n" \
"v_mfma_f32_16x16x32_bf16 v[132:135], a[92:95], a[12:15], v[132:135]\n" \
"ds_read_b128 a[124:127], v210 offset:12288\n" \
"v_mfma_f32_16x16x32_bf16 v[136:139], a[64:67], a[16:19], 0\n" \
"ds_read_b64_tr_b16 a[128:129], %[vvb] offset:0\n" \
"v_mfma_f32_16x16x32_bf16 v[136:139], a[68:71], a[20:23], v[136:139]\n" \
"ds_read_b64_tr_b16 a[130:131], %[vvb] offset:4096\n" \
"v_mfma_f32_16x16x32_bf16 v[136:139], a[72:75], a[24:27], v[136:139]\n" \
"ds_read_b64_tr_b16 a[132:133], v211 offset:0\n" \
"v_mfma_f32_16x16x32_bf16 v[136:139], a[76:79], a[28:31], v[136:139]\n" \
"ds_read_b64_tr_b16 a[134:135], v211 offset:4096\n" \
"v_mfma_f32_16x16x32_bf16 v[140:143], a[80:83], a[16:19], 0\n" \
"ds_read_b64_tr_b16 a[136:137], %[vvb] offset:512\n" \
"v_mfma_f32_16x16x32_bf16 v[140:143], a[84:87], a[20:23], v[140:143]\n" \
"ds_read_b64_tr_b16 a[138:139], %[vvb] offset:4608\n" \
"v_mfma_f32_16x16x32_bf16 v[140:143], a[88:91], a[24:27], v[140:143]\n" \
"ds_read_b64_tr_b16 a[140:141], v211 offset:512\n" \
"v_mfma_f32_16x16x32_bf16 v[140:143], a[92:95], a[28:31], v[140:143]\n" \
"ds_read_b64_tr_b16 a[142:143], v211 offset:4608\n" \
"v_mfma_f32_16x16x32_bf16 v[144:147], a[64:67], a[32:35], 0\n" \
"ds_read_b64_tr_b16 a[144:145], %[vvb] offset:1024\n" \
"v_mfma_f32_16x16x32_bf16 v[144:147], a[68:71], a[36:39], v[144:147]\n" \
"ds_read_b64_tr_b16 a[146:147], %[vvb] offset:5120\n" \
"v_mfma_f32_16x16x32_bf16 v[144:147], a[72:75], a[40:43], v[144:147]\n" \
"ds_read_b64_tr_b16 a[148:149], v211 offset:1024\n" \
"v_mfma_f32_16x16x32_bf16 v[144:147], a[76:79], a[44:47], v[144:147]\n" \
"ds_read_b64_tr_b16 a[150:151], v211 offset:5120\n" \
"v_mfma_f32_16x16x32_bf16 v[148:151], a[80:83], a[32:35], 0\n" \
"ds_read_b64_tr_b16 a[152:153], %[vvb] offset:1536\n" \
"v_mfma_f32_16x16x32_bf16 v[148:151], a[84:87], a[36:39], v[148:151]\n" \
"ds_read_b64_tr_b16 a[154:155], %[vvb] offset:5632\n" \
"v_mfma_f32_16x16x32_bf16 v[148:151], a[88:91], a[40:43], v[148:151]\n" \
"ds_read_b64_tr_b16 a[156:157], v211 offset:1536\n" \
"v_mfma_f32_16x16x32_bf16 v[148:151], a[92:95], a[44:47], v[148:151]\n" \
"ds_read_b64_tr_b16 a[158:159], v211 offset:5632\n" \
"v_mfma_f32_16x16x32_bf16 v[152:155], a[64:67], a[48:51], 0\n" \
"v_mfma_f32_16x16x32_bf16 v[152:155], a[68:71], a[52:55], v[152:155]\n" \
"v_mfma_f32_16x16x32_bf16 v[152:155], a[72:75], a[56:59], v[152:155]\n" \
"v_mfma_f32_16x16x32_bf16 v[152:155], a[76:79], a[60:63], v[152:155]\n" \
"v_mfma_f32_16x16x32_bf16 v[156:159], a[80:83], a[48:51], 0\n" \
"v_mfma_f32_16x16x32_bf16 v[156:159], a[84:87], a[52:55], v[156:159]\n" \
"v_mfma_f32_16x16x32_bf16 v[156:159], a[88:91], a[56:59], v[156:159]\n" \
"v_mfma_f32_16x16x32_bf16 v[156:159], a[92:95], a[60:63], v[156:159]\n" \
"s_branch Lhavem_%=\n" \
"s_nop 0\n" \
"Lend_%=:\n" \
"s_waitcnt lgkmcnt(0)\n" \
"s_nop 0\n" \
""

#define SRD_WORD3 0x00020000
DI u32x4 ld16_sc1(const void* p) { u32x4 v; asm volatile("global_load_dwordx4 %0, %1, off sc1" : "=v"(v) : "v"(p) : "memory"); return v; }
DI unsigned ord_key(float r, unsigned row) {
  const unsigned b = __float_as_uint(r), o = (b & 0x80000000u) ? ~b : (b | 0x80000000u);
  return max(o & 0xfffff800u, 0x800u) | (2047u - row);
}
DI void top2_merge(unsigned& a1, unsigned& a2, unsigned b1, unsigned b2) {
  const unsigned n1 = max(a1, b1), n2 = max(min(a1, b1), max(a2, b2)); a1 = n1; a2 = n2;
}
template <int CTRL> DI void top2_dpp(unsigned& k1, unsigned& k2) {
  const unsigned p1 = __builtin_amdgcn_update_dpp(0u, k1, CTRL, 0xf, 0xf, true), p2 = __builtin_amdgcn_update_dpp(0u, k2, CTRL, 0xf, 0xf, true); top2_merge(k1, k2, p1, p2);
}
DI float row_sum(float v) {
  v = dpp_add<DPP_XOR1>(v); v = dpp_add<DPP_XOR2>(v); v = dpp_add<DPP_ROR8>(v);
  v = swap16_add(v, v); return swap32_add(v, v);
}
DI unsigned h2_pair(float x0, float x1, float m, float rden, unsigned c1w, unsigned sbw) { return ln_pair(x0, x1, m, rden, lo16(c1w), hi16(c1w), lo16(sbw), hi16(sbw)); }
#define PAGE(n, nx) asm volatile("s_branch adaln_pe" #n "\n.p2align 12\nadaln_pg" #n ":\n\ts_cbranch_execz " nx "\nadaln_pe" #n ":")
constexpr int MOE_WGS = DFF / 16;
constexpr unsigned RETRY_CAP = 4096;
#define ASLEEP 2
#define KLOOK 4
#define YSPLIT 8
#define FSLEEP 14
#define KSLEEP 3
#define YEARLY 8
#define FREST 0
#define KL0 1u
#define KLS 2u
#define KST 2u
#define KCHK 8
#define KPAUSE 2
#define KEY_SEL b

#define WBYTES (moe ? DM * DFF * 2 : 0)
namespace fa {
constexpr unsigned LDS_BYTES = 163840;
constexpr float QSCALE = 0.12751743082f;
struct Args { const u16 *x, *wmod, *wq, *kc, *vc, *wo, *wg, *wu, *wd, *c, *wr; char* ws; u16* out;
};
__global__ __launch_bounds__(256, 1) void fattn_k(const Args p) {
  extern __shared__ __attribute__((aligned(16))) char lds[];
  const unsigned wid = __builtin_amdgcn_readfirstlane(threadIdx.x >> 6);
  const unsigned b = blockIdx.x, idx = b >> 3, kg = b & 3u, m = idx & 7u, hd = 8u * kg + m, y = (b & 4u) + (idx >> 3);
  const unsigned ldsb = (unsigned)(size_t)lds;
  const unsigned m0b = ldsb + 4096u * wid, m0t = ldsb + 131072u + 8192u * wid;
  const __amdgpu_buffer_rsrc_t srdz = __builtin_amdgcn_make_buffer_rsrc((void*)p.ws, (short)0, (int)WS_END, SRD_WORD3);
  const bool moe = b < MOE_WGS;
  const unsigned sl = 4u * ((b & 7u) * 7u + (idx >> 2)) + (idx & 3u), yp2 = (y & 1u) * 2u;
  const unsigned sin = (unsigned)WS_XIN + ((y * 32u + hd) * 4u + wid) * BOX;
  u32x2 xq[8]; unsigned fl, fl2, got, got2; unsigned long long tag;
  unsigned k1, k2; unsigned long long lat; f32x4 x1q[8]; u32x4 yo[4], wf[24]; float mrow, rdrow;
  const unsigned sxf = (unsigned)WS_XFL + (y * 4u + wid) * 512u, sxl = (unsigned)WS_XFL + (y * 4u + kg) * 512u;
#define FQ(i)
#define TQ(i)
  {
    const unsigned tid = threadIdx.x, lane = tid & 63u, l16 = lane & 15u, g = lane >> 4, hi = lane >> 5, r8 = (lane >> 2) & 7u;
    unsigned kb0 = ldsb + 256u * l16 + 16u * (g ^ l16);
    unsigned vvb = ldsb + 65536u + 2048u * hi + 288u * (g & 1u) + 64u * ((lane >> 2) & 3u) + 8u * (lane & 3u);
    unsigned vwb = ldsb + 65536u + 2048u * g + 64u * ((lane >> 2) & 3u) + 16u * (lane & 3u);
    unsigned kd0 = 4096u * wid + 256u * g + 16u * (l16 ^ g);
    unsigned vd0 = 4096u * wid + 256u * r8 + 64u * hi + 16u * ((lane & 3u) ^ (2u * (r8 >> 2))) + 1024u;
    unsigned wd0 = 8192u * (16u * wid + r8) + 64u * hi + 16u * (lane & 3u) + 4096u;
    unsigned hq0 = (unsigned)WS_H + (16u * y + 4u * wid) * 32768u + 16u * lane;
    unsigned oq0 = (unsigned)WS_XIN + ((y * 32u + kg * 8u) * 4u + wid) * BOX + 20480u + 16u * lane;
    const unsigned flag = ldsb + 49152u + 4u * wid;
    const __amdgpu_buffer_rsrc_t srdw = __builtin_amdgcn_make_buffer_rsrc((void*)((const char*)p.wq + 256u * hd - 4096), (short)0, (int)(DM * DQ * 2 - 256u * hd + 4096u), SRD_WORD3);
    const __amdgpu_buffer_rsrc_t srdk = __builtin_amdgcn_make_buffer_rsrc((void*)((const char*)p.kc + (size_t)hd * (SEQ * 256)), (short)0, SEQ * 256, SRD_WORD3);
    const __amdgpu_buffer_rsrc_t srdv = __builtin_amdgcn_make_buffer_rsrc((void*)((const char*)p.vc + (size_t)hd * (SEQ * 256) - 17408), (short)0, SEQ * 256 + 17408, SRD_WORD3);
    unsigned long long pct; unsigned clen;
    asm volatile("s_getpc_b64 %0\n\ts_mov_b32 %1, adaln_text_end@rel32@lo+4" : "=s"(pct), "=s"(clen));
    unsigned vct = min(((idx * 4u + wid) * 6u + min(lane, 5u)) * 128u, (clen - 4u) & ~3u);
#define XNT " nt"
#define WNT " nt"
      const unsigned gr = 128u * (b & 7u) + 4u * (tid >> 3), gc = 64u * (b >> 3) + 8u * (tid & 7u), gran0 = b;
      u32x4 w[4], xs[4]; u32x2 ep, cv;
#define XV(i) ({ u32x4 v_; asm volatile("v_accvgpr_read_b32 %0, a[%4]\n\tv_accvgpr_read_b32 %1, a[%4 + 1]\n\tv_accvgpr_read_b32 %2, a[%4 + 2]\n\tv_accvgpr_read_b32 %3, a[%4 + 3]" : "=v"(v_[0]), "=v"(v_[1]), "=v"(v_[2]), "=v"(v_[3]) : "n"(200 + 4 * (i))); v_; })
      #pragma unroll
      for (int j = 0; j < 4; ++j) asm volatile("global_load_dwordx4 %0, %1, %2" WNT : "=v"(w[j]) : "v"(((gr + j) * NMOD + gc) * 2u), "s"(p.wmod) : "memory");
      asm volatile("global_load_dwordx2 %0, %1, %2" : "=v"(cv) : "v"(gr * 2u), "s"(p.c) : "memory");
      #pragma unroll
      for (int q = 0; q < 4; ++q) asm volatile("global_load_dwordx4 %0, %1, %2 offset:%3" XNT : "=v"(xs[q]) : "v"((8u * (y * 32u + hd) + 2u * wid) * 2048u + 16u * lane), "s"(p.x), "n"(1024 * q) : "memory");
      asm volatile("global_load_dwordx2 %0, %1, %2 sc1" : "=v"(ep) : "v"(0u), "s"(p.ws + WS_EP) : "memory");
      const unsigned zv = b * Z_SHARE + tid * 16u - (unsigned)(WS_U - WS_Z) < 16384u ? ~0u : 0u;
      asm volatile("buffer_store_dwordx4 %0, %1, %2, 0 offen sc1\n\ts_nop 1" :: "v"(u32x4{zv, zv, zv, zv}), "v"(tid < Z_SHARE / 16u ? (unsigned)WS_Z + b * Z_SHARE + tid * 16u : 0x80000000u), "s"(srdz) : "memory");
      FQ(6);
      asm volatile("global_load_dword a255, %0, %1" :: "v"(vct), "s"(pct) : "memory");
#define PRET 0
      asm volatile("s_mov_b64 exec, 0\n\ts_cmp_eq_u32 %0, 1\n\ts_cbranch_scc1 adaln_pg5\n\ts_cmp_eq_u32 %0, 2\n\ts_cbranch_scc1 adaln_pg6\n\ts_cmp_eq_u32 %0, 3\n\ts_cbranch_scc1 adaln_pg7\nadaln_fr:\n\ts_mov_b64 exec, -1" :: "s"(wid) : "scc");
      FQ(7);
      float* red = (float*)(lds + 131072 + 4096);
      asm volatile("s_waitcnt vmcnt(%5)" : "+v"(cv), "+v"(w[0]), "+v"(w[1]), "+v"(w[2]), "+v"(w[3]) : "n"(PRET ? 8 : 7) : "memory");
      FQ(4);
      const unsigned rho = 0;
      unsigned tq0 = (idx * 8u + wid * 2u + ((lane >> 3) & 1u)) * 8192u + (b & 4u) * 256u + (lane & 7u) * 128u + 4096u - 256u * m;
#define WQT 0
#define KVT2 0
#define WQT2 4
#define TNT ""
      asm volatile("buffer_load_dword a255, %0, %1, 0 offen" TNT :: "v"(tq0), "s"(srdw) : "memory");
      auto silu = [](float v) { return rbf(v * __builtin_amdgcn_rcpf(1.f + __builtin_amdgcn_exp2f(v * -1.44269504089f))); };
      {
        float acc[8];
        #pragma unroll
        for (int e = 0; e < 8; ++e) acc[e] = 0.f;
        #pragma unroll
        for (int j = 0; j < 4; ++j) {
          const float sv = silu((j & 1) ? hi16(cv[j >> 1]) : lo16(cv[j >> 1]));
          #pragma unroll
          for (int e = 0; e < 4; ++e) { acc[2 * e] = fmaf(sv, lo16(w[j][e]), acc[2 * e]); acc[2 * e + 1] = fmaf(sv, hi16(w[j][e]), acc[2 * e + 1]); }
        }
        float t[4], u[2];
        #pragma unroll
        for (int e = 0; e < 4; ++e) t[e] = swap32_add(acc[e], acc[e + 4]);
        #pragma unroll
        for (int e = 0; e < 2; ++e) u[e] = dpp_add<DPP_ROR8>(swap16_add(t[e], t[e + 2]));
        if ((lane & 8u) == 0) *(f32x2*)&red[wid * 64u + 8u * (tid & 7u) + 2u * g] = f32x2{u[0], u[1]};
      }
      asm volatile("s_waitcnt vmcnt(%5) lgkmcnt(0)\n\ts_barrier" : "+v"(xs[0]), "+v"(xs[1]), "+v"(xs[2]), "+v"(xs[3]), "+v"(ep) : "n"(PRET ? 3 : 2) : "memory");

      tag = (((unsigned long long)__builtin_amdgcn_readfirstlane(ep[1]) << 32) | __builtin_amdgcn_readfirstlane(ep[0])) + 1ull;
      const unsigned tl = (unsigned)tag & 0xffu, th = (unsigned)(tag >> 8);
      auto gran = [&](float v) { return ((unsigned long long)th << 32) | (((__float_as_uint(v) + 0x80u) & 0xffffff00u) | tl); };
      if (wid == 0u) __hip_atomic_store((gu64*)(p.ws + WS_MOD) + gran0 * 64u + lane, gran((red[lane] + red[64u + lane]) + (red[128u + lane] + red[192u + lane])), RLX_AGENT);
      {
        float t4[4] = {0.f, 0.f, 0.f, 0.f};
        #pragma unroll
        for (int e = 0; e < 16; ++e) {
          const unsigned xw = xs[e >> 2][e & 3]; const bf16x2 v = __builtin_bit_cast(bf16x2, xw);
          t4[2 * (e >> 3)] = __builtin_amdgcn_fdot2_f32_bf16(v, __builtin_bit_cast(bf16x2, 0x3f803f80u), t4[2 * (e >> 3)], false);
          t4[2 * (e >> 3) + 1] = __builtin_amdgcn_fdot2_f32_bf16(v, v, t4[2 * (e >> 3) + 1], false);
        }
        float u = swap16_add(swap32_add(t4[0], t4[2]), swap32_add(t4[1], t4[3]));
        u = dpp_add<DPP_XOR1>(u); u = dpp_add<DPP_XOR2>(u); u = dpp_add<DPP_ROR4>(u); u = dpp_add<DPP_ROR8>(u);
        const f32x2 mean = f32x2{__uint_as_float(__builtin_amdgcn_readlane(__float_as_uint(u), 0)), __uint_as_float(__builtin_amdgcn_readlane(__float_as_uint(u), 32))} * (1.f / DM);
        f32x2 v2 = f32x2{__uint_as_float(__builtin_amdgcn_readlane(__float_as_uint(u), 16)), __uint_as_float(__builtin_amdgcn_readlane(__float_as_uint(u), 48))} * (1.f / DM) - mean * mean;
        if (__builtin_expect(!(mean[0] * mean[0] <= v2[0] && mean[1] * mean[1] <= v2[1]), 0)) {
          f32x2 sq = {0.f, 0.f};
          #pragma unroll
          for (int e = 0; e < 8; ++e) {
            const f32x2 d0 = f32x2{lo16(xs[e >> 2][e & 3]), lo16(xs[2 + (e >> 2)][e & 3])} - mean, d1 = f32x2{hi16(xs[e >> 2][e & 3]), hi16(xs[2 + (e >> 2)][e & 3])} - mean;
            sq = __builtin_elementwise_fma(d1, d1, __builtin_elementwise_fma(d0, d0, sq));
          }
          v2 = f32x2{wave_sum(sq[0]), wave_sum(sq[1])} * (1.f / DM);
        }
        const float var = (lane & 2u) ? v2[1] : v2[0];
        const float val = (lane & 1u) ? rbf(__builtin_amdgcn_sqrtf(rbf(rbf(var) + 1e-5f))) : rbf((lane & 2u) ? mean[1] : mean[0]);
        if (lane < 4u) __hip_atomic_store((gu64*)(p.ws + WS_RST) + (8u * (y * 32u + hd) + 2u * wid) * 2u + lane, gran(val), RLX_AGENT);
      }
      FQ(5);
#define SWQ 0
#define SWQ0 0x400000
      const unsigned rb0 = 16u * y + 4u * (hd >> 3), ch = 16u * (hd & 7u) + (tid >> 4);
      asm volatile("global_load_dwordx2 a[216:217], %0, %1" :: "v"(tid * 8u), "s"(p.c) : "memory");
      #pragma unroll
      for (int i = 0; i < 4; ++i) asm volatile("global_load_dwordx4 a[%2:%2 + 3], %0, %1" :: "v"(((rb0 + i) * 16u + l16) * 2048u + ch * 16u), "s"(p.x), "n"(200 + 4 * i) : "memory");
      PAGE(5, "adaln_fr");
      unsigned lane2 = lane; asm volatile("" : "+v"(lane2));
#define lane lane2
    unsigned wo0 = (32u * wid + r8) * 2048u + m * 256u + 64u * hi + 16u * ((lane & 3u) ^ (2u * (r8 >> 2)));
    const __amdgpu_buffer_rsrc_t srdt = __builtin_amdgcn_make_buffer_rsrc((void*)((const char*)p.wo + (size_t)kg * (DM * DM * 2)), (short)0, DM * DM * 2, SRD_WORD3);
    const __amdgpu_buffer_rsrc_t srdp = __builtin_amdgcn_make_buffer_rsrc((void*)((const char*)(y < 4u ? p.kc : p.vc) + (size_t)hd * (SEQ * 256) + (y & 3u) * 4096u), (short)0, (int)(KVPAR && (b & 1u) ? 0u : (KVPAR ? 0x400000u : 0u) + SEQ * 256 - (y & 3u) * 4096u), SRD_WORD3);
    unsigned vg0 = (256u * wid + (lane >> 1)) * (DFF * 2u) + sl * 32u + 16u * (lane & 1u);
    unsigned vdn = (16u * sl + hi) * 2048u + 512u * wid + 16u * ((lane & 31u) ^ (2u * hi));
    const unsigned mc0 = 2u * DM + 64u * ((idx >> 2) * 8u + (b & 7u)) + 16u * (idx & 3u);
    const unsigned md0 = (256u * wid + (lane >> 1)) * (NMOD * 2u) + mc0 * 2u + 16u * (lane & 1u);
    unsigned sd0 = ldsb + 131072u + 8192u + 512u * wid + 16u * g;
    fl = md0;
    unsigned rd0 = ldsb + 131072u + 7168u + 8192u * ((lane >> 2) & 3u) + 256u * wid + 4u * (lane & 3u);
    unsigned ta0 = m0t + 256u * g + 32u * ((lane >> 2) & 3u) + 8u * (lane & 3u);
    const unsigned c8 = (b * 4u + wid) * 8192u, cd = moe ? sl * 32768u + wid * 8192u : 0u, cw = (idx * 4u + wid) * 16384u;
    const unsigned cm = moe ? ((sl & 3u) * 4u + wid) * 64u * (DFF * 2u) + (sl >> 2) * 128u : 0u;
    unsigned vxs = BOX * (l16 >> 1) + 64u * (l16 & 1u) + 16u * g;
    const unsigned sxs = (unsigned)WS_XIN + (y * 128u + wid * 32u) * BOX + (m >> 1) * 1024u + kg * 256u + (m & 1u) * 128u;
    unsigned vx0 = ((256u * y + 8u * hd + 2u * wid + ((lane >> 2) & 1u)) * DM + 256u * hi + 128u * (l16 >> 3) + 16u * (g & 1u) + 4u * (lane & 3u) + 32u * yp2) * 2u;
    unsigned tlo = 0, thi = 0;
#define tab(ln, lo, hi2) asm("v_writelane_b32 %0, %2, " #ln "\n\tv_writelane_b32 %1, %3, " #ln : "+v"(tlo), "+v"(thi) : "s"(lo), "s"(hi2))
#define tabp(ln, q, off) do { const unsigned long long a_ = (size_t)(q) + (off); tab(ln, (unsigned)a_, (unsigned)(a_ >> 32)); } while (0)
#define TORD 0
#define TAB_X(n)  tabp(n, p.x, ((y * 32u + hd) * 2u + (wid & 1u)) * 8192u)
#define TAB_U(n)  tabp(n, p.wu, moe ? c8 : 0u)
#define TAB_O(n)  tabp(n, p.wo, c8)
#define TAB_D(n)  tabp(n, p.wd, cd)
    TAB_X(1); TAB_U(2); TAB_O(3); TAB_D(4);
    tabp(5, p.wo, kg * 2097152u + cw); tabp(6, p.wo, kg * 2097152u + cw + 8192u); tabp(7, p.wg, cm);
    tabp(17, p.wmod, 0u); tab(18, NP_FILL == 8 ? sin + 20480u : (unsigned)WS_XIN + ((y * 32u + kg * 8u + (m >> 1)) * 4u + wid) * BOX + 20480u + (m & 1u) * 8192u, (unsigned)WS_OFL + y * 16384u + kg * 128u);
    tab(19, (unsigned)WS_OFL + (y * 32u + hd) * 512u + wid * 128u, (unsigned)WS_OFL + (y * 32u + kg * 8u) * 512u + wid * 128u);
    tabp(8, p.wg, 0u); tabp(9, p.wu, 0u); tabp(10, p.wd, 0u);
    tab(11, m, m * 4u * BOX); tab(12, (y & 1u) * 128u, 128u - (y & 1u) * 128u);
    tab(13, sxl, (unsigned)WS_PFL + (y * 32u + hd) * 512u + kg * 128u); tab(14, sxs, sxf);
    tab(15, (unsigned)WS_MODT + (mc0 - 2u * DM + 4u * wid) * 4u, (mc0 >> 10) == 4u ? 0x3f800000u : 0u);
    tab(16, (unsigned)WS_PFL + y * 16384u + (hd * 4u + wid) * 4u, (unsigned)WS_PFL + (y * 32u + hd) * 512u + wid * 128u);
    const char* psig = p.ws + WS_CNT + y * 8192u + hd * 8u;
    const char* pcnt = p.ws + WS_CNT + y * 8192u + hd * 256u;
    tabp(20, p.wr, 0u); tab(21, (unsigned)WS_PART + b * 8192u + wid * 2048u, 0u);
    tabp(25, p.out, 0u); tab(26, 256u * y + 8u * hd + 2u * wid, (y & 1u) * 256u); tab(27, (unsigned)WS_KEYS + b * 8u + wid * 4096u, (unsigned)WS_KEYS + (b & 7u) * 2048u);
    tab(28, (unsigned)WS_PART + ((b & 7u) * 32u + idx) * 32u + wid * 262144u, ((b & 7u) * 32u + idx) * 8u);
    tab(31, (128u * m + 32u * (y >> 1) + 8u * (y & 1u)) * 2048u + 512u * wid, (unsigned)WS_U + hd * 512u + y * 32u); tab(32, (unsigned)WS_U + hd * 512u, 8192u * kg + 6656u + 64u * m);
    tab(33, (unsigned)WS_PX + (y * 32u + hd) * 1024u + wid * 256u, (unsigned)WS_PX + y * 32768u + (8u * hd + 2u * wid) * 4u); tab(34, (256u * y + 64u * wid) * 2048u + 64u * hd, (unsigned)WS_PK + b * 8u + wid * 4096u);
    tab(35, (unsigned)WS_PK + (b & 7u) * 2048u, 256u * y + 8u * hd + 2u * wid);
      asm volatile("" : "+v"(tlo), "+v"(thi), "+v"(wo0), "+v"(vg0), "+v"(vdn), "+v"(ta0), "+v"(vxs), "+v"(vx0), "+v"(rd0), "+v"(fl), "+v"(sd0), "+v"(kb0), "+v"(vvb), "+v"(vwb), "+v"(kd0), "+v"(vd0), "+v"(wd0), "+v"(hq0), "+v"(oq0));
#undef lane
      float* par = (float*)(lds + 131072);
      u32x2 mp[8]; u32x4 rs;
      auto look = [&]() __attribute__((always_inline)) {
        #pragma unroll
        for (int k = 0; k < 8; ++k) asm volatile("global_load_dwordx2 %0, %1, %2 offset:%3 sc1" : "=v"(mp[k]) : "v"(((wid >> 1) * 16u + 2u * (hd & 7u) + (wid & 1u)) * 4096u + lane * 8u), "s"(p.ws + WS_MOD), "n"(512 * k) : "memory");
        asm volatile("global_load_dwordx4 %0, %1, %2 sc1" : "=v"(rs) : "v"(((rb0 + wid) * 16u + l16) * 16u), "s"(p.ws + WS_RST) : "memory");
      };
#define LOOKED "+v"(mp[0]), "+v"(mp[1]), "+v"(mp[2]), "+v"(mp[3]), "+v"(mp[4]), "+v"(mp[5]), "+v"(mp[6]), "+v"(mp[7]), "+v"(rs)
      look();
      #pragma unroll
      for (int q = 0; q < 14; ++q)
        asm volatile("s_mov_b32 m0, %0\n\ts_nop 0\n\tbuffer_load_dwordx4 %1, %2, %3 offen lds" :: "s"(m0b + 65536u + 16384u * (q >> 2) + 1024u * (q & 3)), "v"(wd0), "s"(srdw),
                     "s"(0x80000u * ((2u * rho + (q >> 2)) & 15u) + 65536u * ((q >> 1) & 1) + 128u * (q & 1)) : "m0", "memory");
#define KVT 0
#define KV0 0
      #pragma unroll
      for (int j = 0; j < KVT; ++j) asm volatile("buffer_load_dword a255, %0, %1, %2 offen" :: "v"(lane < 32u ? lane * 128u : 0x80000000u), "s"(srdp), "s"((KV0 + wid + 4u * j) * 16384u) : "memory");
      asm volatile("s_waitcnt vmcnt(%0)" :: "n"(27 + KVT) : "memory");
      { const u32x4 cs = XV(4); *(u32x2*)(lds + 131072 + 8192 + tid * 8u) = u32x2{cvtpk(silu(lo16(cs[0])), silu(hi16(cs[0]))), cvtpk(silu(lo16(cs[1])), silu(hi16(cs[1])))}; }
      asm volatile("s_waitcnt vmcnt(%9)" : LOOKED : "n"(14 + KVT) : "memory");
      for (unsigned spins = 0;; ++spins) {
        unsigned bl = (rs[0] ^ tl) | (rs[2] ^ tl), bh = (rs[1] ^ th) | (rs[3] ^ th);
        #pragma unroll
        for (int k = 0; k < 8; ++k) { bl |= mp[k][0] ^ tl; bh |= mp[k][1] ^ th; }
        if (__all(((bl & 0xffu) | bh) == 0u) || spins > SPIN_CAP) break;
        __builtin_amdgcn_s_sleep(ASLEEP);
        look();
        asm volatile("s_waitcnt vmcnt(0)" : LOOKED :: "memory");
      }
      {
        float v = 0.f;
        #pragma unroll
        for (int k = 0; k < 8; ++k) v += __uint_as_float(mp[k][0] & 0xffffff00u);
        v = rbf(v); par[tid] = (tid < 128u ? v : rbf(1.f + v)) * QSCALE;
        const float rd = __builtin_amdgcn_rcpf(hi16(rs[2]));
        if (lane < 16u) *(f32x2*)(par + 256u + 8u * lane + 2u * wid) = f32x2{-hi16(rs[0]) * rd, rd};
      }
      PAGE(6, "adaln_fr");
      asm volatile("s_waitcnt lgkmcnt(0)\n\ts_barrier" ::: "memory");
      asm volatile("buffer_load_dword a255, %0, %1, %2 offen" TNT :: "v"(tq0), "s"(srdw), "s"(0x200000u) : "memory");
      FQ(0);
      {
        const float* pc = par + 8u * (tid >> 4);
        const f32x4 a0 = *(const f32x4*)pc, a1 = *(const f32x4*)(pc + 4), c0 = *(const f32x4*)(pc + 128), c1 = *(const f32x4*)(pc + 132);
        const f32x4 t0 = *(const f32x4*)(par + 256u + 8u * l16), t1 = *(const f32x4*)(par + 260u + 8u * l16);
        #pragma unroll
        for (int i = 0; i < 4; ++i) {
          const float mn = i < 2 ? t0[2 * i] : t1[2 * i - 4], rd = i < 2 ? t0[2 * i + 1] : t1[2 * i - 3];
          u32x4 o = XV(i);
          #pragma unroll
          for (int e = 0; e < 4; ++e) {
            const f32x2 sc = e < 2 ? f32x2{c0[2 * e], c0[2 * e + 1]} : f32x2{c1[2 * e - 4], c1[2 * e - 3]}, sh = e < 2 ? f32x2{a0[2 * e], a0[2 * e + 1]} : f32x2{a1[2 * e - 4], a1[2 * e - 3]};
            o[e] = cvtpk2(__builtin_elementwise_fma(__builtin_elementwise_fma(unpk(o[e]), f32x2{rd, rd}, f32x2{mn, mn}), sc, sh));
          }
          st16_wt(p.ws + WS_H + (rb0 + i) * 32768u + ch * 256u + 16u * l16, o);
        }
      }
      FQ(1);
    asm volatile(FA_BODY
      : FA_OUTS
      : [m0b] "s"(m0b), [flag] "s"(flag), [srdz] "s"(srdz), [srdw] "s"(srdw), [srdk] "s"(srdk), [srdv] "s"(srdv), [m0t] "s"(m0t), [srdt] "s"(srdt), [srdp] "s"(srdp),
        [tag] "s"(tag), [pcnt] "s"(pcnt), [psig] "s"(psig), [wid] "s"(wid), [px] "s"(p.x), [pct] "s"(pct), [sin] "s"(sin), [tlo] "v"(tlo), [thi] "v"(thi), [wbytes] "s"(WBYTES)
      : FA_CLOBBERS);
  }
  if (__builtin_expect(got != 0u, 0)) {
    unsigned lane; asm volatile("v_mbcnt_lo_u32_b32 %0, -1, 0\n\tv_mbcnt_hi_u32_b32 %0, -1, %0" : "=v"(lane));
    const unsigned tid = wid * 64u + lane, n = lane & 15u, g = lane >> 4;
    const unsigned row = 256u * y + 8u * hd + 2u * wid + ((n >> 2) & 1u);
    const char* par = lds;
    unsigned* kb = (unsigned*)(lds + 14400);
    asm volatile("s_waitcnt vmcnt(0)" ::: "memory");
    if (row == k1 || row == k2) {
      char* pub = p.ws + WS_XIN + ((y * 32u + hd) * 4u + (row == k2 && (k1 >> 3) == (k2 >> 3) ? 2u : 0u)) * BOX + 16384u + (g >> 1) * 512u + ((n >> 3) * 8u + (g & 1u) * 4u + (n & 3u)) * 16u;
      #pragma unroll
      for (int k = 0; k < 4; ++k) {
        const int q = 4 * (k >> 1) + (k & 1);
        st16_wt(pub + (k & 1) * BOX + (((k >> 1) * 2u) ^ yp2) * 128u, u32x4{cvtpk(x1q[q][0], x1q[q][1]), cvtpk(x1q[q][2], x1q[q][3]), cvtpk(x1q[q + 2][0], x1q[q + 2][1]), cvtpk(x1q[q + 2][2], x1q[q + 2][3])});
      }
      if ((lane & ~4u) == 0u) __hip_atomic_store((gu64*)(p.ws + WS_MD) + (row == k2), ((unsigned long long)(__float_as_uint(rdrow) | 1u) << 32) | __float_as_uint(mrow), RLX_AGENT);
    } else if ((lat >> lane) & 1u) {
      char* o = (char*)p.out + row * 2048u + 512u * (g >> 1) + 256u * (n >> 3) + 32u * (g & 1u) + 64u * (n & 1u) + 16u * ((n >> 1) & 1u);
      #pragma unroll
      for (int q = 0; q < 4; ++q) st16_wt(o + 1024 * (q & 1) + (((q >> 1) * 2u) ^ yp2) * 64u, yo[q]);
    }
    const unsigned un = moe ? 32u * wid + (lane & 31u) : tid & 127u;
    const u32x4 lsb = *(const u32x4*)(par + un * 16u), lc1 = *(const u32x4*)(par + 2048 + un * 16u);
    u32x4 xa, ha; u32x2 md;
    const bool second = (moe ? lane >> 5 : wid >> 1) != 0u;
    const unsigned rw = second ? k2 : k1, bx = (rw >> 3) * 4u + (second && (k1 >> 3) == (k2 >> 3) ? 2u : 0u) + (un >> 6);
    for (unsigned spins = 0; spins < RETRY_CAP; ++spins) {
      asm volatile("buffer_load_dwordx4 %0, %2, %4, 0 offen sc1\n\tbuffer_load_dwordx2 %1, %3, %4, 0 offen sc1\n\ts_waitcnt vmcnt(0)" : "=&v"(xa), "=&v"(md)
                   : "v"((unsigned)WS_XIN + bx * BOX + 16384u + (un & 63u) * 16u), "v"((unsigned)WS_MD + (second ? 8u : 0u)), "s"(srdz) : "memory");
      if (!__any(max(max(xa[0], xa[1]), max(xa[2], xa[3])) == 0xffffffffu || md[1] == 0u)) break;
    }
    #pragma unroll
    for (int e = 0; e < 4; ++e) ha[e] = cvtpk2(__builtin_elementwise_fma((unpk(xa[e]) - __uint_as_float(md[0])) * __uint_as_float(md[1]), unpk(lc1[e]), unpk(lsb[e])));
    float* part = (float*)(p.ws + WS_PART);
    if (!moe) {
      const unsigned pp = (b - MOE_WGS) * 64u + lane, sq = pp & 1u, nn = 128u * (pp >> 8) + 16u * (2u * ((pp >> 6) & 3u) + ((pp >> 1) & 1u)) + ((pp >> 2) & 15u);
      const unsigned ne = 512u * (nn >> 8) + 256u * ((nn >> 6) & 1u) + 128u * ((nn >> 7) & 1u) + 64u * ((nn >> 4) & 1u) + 16u * ((nn >> 2) & 3u) + 8u * ((nn >> 5) & 1u) + 2u * (nn & 3u);
      char* rows = lds + 16384 + tid * 16u;
      *(u32x4*)rows = ha; *(u32x4*)(rows + 4096) = xa;
      const f32x4 w0 = *(const f32x4*)(par + 10240 + un * 16u), w1 = *(const f32x4*)(par + 12288 + un * 16u);
      float dot = 0.f;
      #pragma unroll
      for (int e = 0; e < 4; ++e) dot = fmaf(hi16(xa[e]) - __uint_as_float(md[0]), (e < 2 ? w0 : w1)[2 * (e & 1) + 1], fmaf(lo16(xa[e]) - __uint_as_float(md[0]), (e < 2 ? w0 : w1)[2 * (e & 1)], dot));
      dot = wave_sum(dot) * __uint_as_float(md[1]);
      if (lane == 0) kb[16u + wid] = __float_as_uint(dot);
      const float* pb = part + (size_t)(tid >> 4) * 2048u + (b - MOE_WGS) * 64u + (tid & 15u) * 4u;
      u32x4 v[14];
      for (unsigned spins = 0;; ++spins) {
        #pragma unroll
        for (int k = 0; k < 14; ++k) v[k] = ld16_sc1(pb + (size_t)k * 32768u);
        asm volatile("s_waitcnt vmcnt(0)" : "+v"(v[0]), "+v"(v[1]), "+v"(v[2]), "+v"(v[3]), "+v"(v[4]), "+v"(v[5]), "+v"(v[6]), "+v"(v[7]),
                     "+v"(v[8]), "+v"(v[9]), "+v"(v[10]), "+v"(v[11]), "+v"(v[12]), "+v"(v[13]) :: "memory");
        u32x4 t = v[0];
        #pragma unroll
        for (int k = 1; k < 14; ++k) t &= v[k];
        if (__all((t[0] & t[1] & t[2] & t[3] & 3u) == 3u) || spins > SPIN_CAP) break;
      }
      f32x4 sum = __builtin_bit_cast(f32x4, v[0]);
      #pragma unroll
      for (int k = 1; k < 14; ++k) sum += __builtin_bit_cast(f32x4, v[k]);
      const float t0 = swap32_add(sum[0], sum[2]), t1 = swap32_add(sum[1], sum[3]), u = swap16_add(t0, t1);
      float* rd2 = (float*)(lds + 24576);
      rd2[wid * 64u + (lane & 15u) * 4u + g] = u;
      asm volatile("s_waitcnt lgkmcnt(0)\n\ts_barrier" ::: "memory");
      if (wid == 0) {
        const float xf = bf2f(*(const u16*)(lds + 20480 + sq * 2048u + ne)), hf = bf2f(*(const u16*)(lds + 16384 + sq * 2048u + ne)), gbv = bf2f(*(const u16*)(par + 4096 + ne));
        const f32x4 sw = *(const f32x4*)(par + 14384);
        const float rsel = rbf(__builtin_amdgcn_rcpf(1.f + __builtin_amdgcn_exp2f(rbf(__uint_as_float(kb[16u + 2u * sq]) + __uint_as_float(kb[17u + 2u * sq]) + ((sw[0] + sw[1]) + (sw[2] + sw[3]))) * -1.44269504089f)));
        const float yy = (rd2[lane] + rd2[64u + lane]) + (rd2[128u + lane] + rd2[192u + lane]);
        asm volatile("global_store_short %0, %1, off sc1" :: "v"(p.out + (sq ? k2 : k1) * DM + nn), "v"((unsigned)f2bf(fmaf(gbv, fmaf(yy, rsel, hf), xf))) : "memory");
      }
    } else {
      f32x4 dg = {0.f, 0.f, 0.f, 0.f}, du = {0.f, 0.f, 0.f, 0.f};
      char* hs = lds + 16384 + 1024u * wid;
      *(u32x4*)(hs + 16u * lane) = ha;
      #pragma unroll
      for (int ks = 0; ks < 8; ++ks) {
        const bf16x8 hf = *(const bf16x8*)(hs + (lane & 1u) * 512u + 16u * g + 64 * ks);
        dg = __builtin_amdgcn_mfma_f32_16x16x32_bf16(__builtin_bit_cast(bf16x8, wf[ks]), hf, dg, 0, 0, 0);
        du = __builtin_amdgcn_mfma_f32_16x16x32_bf16(__builtin_bit_cast(bf16x8, wf[8 + ks]), hf, du, 0, 0, 0);
      }
      f32x4* red = (f32x4*)(lds + 24576);
      if ((lane & 15u) < 2u) { red[wid * 16u + (lane & 1u) * 4u + g] = dg; red[wid * 16u + 8u + (lane & 1u) * 4u + g] = du; }
      asm volatile("s_waitcnt vmcnt(0) lgkmcnt(0)\n\ts_barrier" ::: "memory");
      const float* rf = (const float*)red + ((lane & 1u) * 4u + g) * 4u + ((lane >> 1) & 3u);
      const float gg = rbf((rf[0] + rf[64]) + (rf[128] + rf[192])), uu = rbf((rf[32] + rf[96]) + (rf[160] + rf[224]));
      const unsigned a = __float_as_uint(rbf(rbf(gg * __builtin_amdgcn_rcpf(1.f + __builtin_amdgcn_exp2f(gg * -1.44269504089f))) * uu));
      const unsigned o2 = __builtin_amdgcn_update_dpp(0u, a, DPP_XOR2, 0xf, 0xf, true);
      const unsigned pr = (lane & 2u) ? ((o2 >> 16) | a) : ((a >> 16) | o2);
      const unsigned o4 = __builtin_amdgcn_update_dpp(0u, pr, DPP_ROR4, 0xf, 0xf, true);
      const u32x2 actp = (lane & 4u) ? u32x2{o4, pr} : u32x2{pr, o4};
      u32x4 af;
      #pragma unroll
      for (int e = 0; e < 2; ++e) {
        const auto r = __builtin_amdgcn_permlane32_swap(actp[e], actp[e], false, false), q = __builtin_amdgcn_permlane16_swap(r[0], r[1], false, false);
        af[e] = q[0]; af[2 + e] = q[1];
      }
      f32x4 yv[2] = {{0.f, 0.f, 0.f, 0.f}, {0.f, 0.f, 0.f, 0.f}};
      #pragma unroll
      for (int t = 0; t < 4; ++t) {
        const u32x4 am = (n >> 1) == 2u * t + (g >> 1) ? af : u32x4{0u, 0u, 0u, 0u};
        #pragma unroll
        for (int u = 0; u < 2; ++u) yv[u] = __builtin_amdgcn_mfma_f32_16x16x32_bf16(__builtin_bit_cast(bf16x8, am), __builtin_bit_cast(bf16x8, wf[16 + 4 * u + t]), yv[u], 0, 0, 0);
      }
      #pragma unroll
      for (int u = 0; u < 2; ++u) st16_wt(part + (size_t)b * 2048u + (2u * wid + u) * 256u + lane * 4u, __builtin_bit_cast(u32x4, yv[u]) | 3u);
    }
  }
  if (b == 255u && wid == 0u) __hip_atomic_store((gu64*)(p.ws + WS_EP), tag, RLX_AGENT);
  asm volatile("adaln_text_end:");
}
}

extern "C" void kernel_launch(void* const* d_in, const int* in_sizes, int n_in, void* d_out, int out_size, void* d_ws, size_t ws_size, hipStream_t stream) {
  (void)in_sizes; (void)n_in; (void)out_size;
  if (ws_size < WS_END) return;
  const u16* x    = (const u16*)d_in[0];
  const u16* kc   = (const u16*)d_in[1];
  const u16* vc   = (const u16*)d_in[2];
  const u16* wq   = (const u16*)d_in[3];
  const u16* wo   = (const u16*)d_in[4];
  const u16* wr   = (const u16*)d_in[6];
  const u16* wg   = (const u16*)d_in[7];
  const u16* wu   = (const u16*)d_in[8];
  const u16* wd   = (const u16*)d_in[9];
  const u16* c    = (const u16*)d_in[10];
  const u16* wmod = (const u16*)d_in[11];
  u16* out = (u16*)d_out;
  char* ws = (char*)d_ws;
  fa::Args fr;
  fr.x = x; fr.wmod = wmod; fr.wq = wq; fr.kc = kc; fr.vc = vc; fr.wo = wo; fr.wg = wg; fr.wu = wu; fr.wd = wd; fr.ws = ws; fr.out = out; fr.c = c; fr.wr = wr;
  hipFuncSetAttribute((const void*)fa::fattn_k, hipFuncAttributeMaxDynamicSharedMemorySize, (int)fa::LDS_BYTES);
  hipLaunchKernelGGL(fa::fattn_k, dim3(256), dim3(256), fa::LDS_BYTES, stream, fr);
}
```

```cpp
#include <hip/hip_runtime.h>
#include <hip/hip_bf16.h>
#include <utility>
#include <cstdint>

#pragma clang fp contract(off)
typedef float    f32x16 __attribute__((ext_vector_type(16)));
typedef float    f32x2  __attribute__((ext_vector_type(2)));
typedef float    f32x4  __attribute__((ext_vector_type(4)));
typedef __bf16   bf16x8 __attribute__((ext_vector_type(8)));
typedef __bf16   bf16x4 __attribute__((ext_vector_type(4)));
typedef __bf16   bf16x2 __attribute__((ext_vector_type(2)));
typedef unsigned u32x4  __attribute__((ext_vector_type(4)));
typedef unsigned u32x2  __attribute__((ext_vector_type(2)));
typedef short    s16x4  __attribute__((ext_vector_type(4)));
typedef unsigned short u16;
template <int... Is> using iseq = std::integer_sequence<int, Is...>;
template <int N> using make_iseq = std::make_integer_sequence<int, N>;
#define DI __device__ __forceinline__
#define TAILB
#define PACE2
#define SELB

constexpr int SEQ = 2048, DM = 1024, NHEAD = 32, DHEAD = 128, DQ = NHEAD * DHEAD, DFF = 3584, NMOD = 6 * DM;
constexpr size_t WS_EP    = 0;
constexpr size_t WS_RST   = 32768;
constexpr size_t WS_CNT   = 65536;
constexpr size_t WS_MOD   = 131072;
constexpr size_t WS_Z     = 262144;
constexpr size_t WS_MODT  = WS_Z;
constexpr size_t WS_XFL   = WS_Z + 16384;
#define KEY_REP 8u
constexpr size_t WS_KEYS  = WS_Z + 32768;
constexpr unsigned Z_SHARE = 2560;
static_assert(KEY_REP <= 8 && Z_SHARE * 256 == 655360, "zeroed area");
constexpr size_t WS_MD    = WS_Z + 49152;
constexpr size_t WS_PFL   = WS_Z + 65536;
constexpr size_t WS_OFL   = WS_Z + 196608;
constexpr size_t WS_PK    = WS_Z + 327680;
constexpr size_t WS_U     = WS_Z + 344064;
constexpr size_t WS_PX    = WS_Z + 393216;
constexpr size_t WS_H     = 1048576;
constexpr size_t WS_PART  = WS_H  + (size_t)SEQ * DM * 2;
constexpr size_t WS_XIN   = WS_PART + (size_t)256 * 2 * DM * 4;
#define BOXV 38016
constexpr unsigned BOX    = BOXV;
constexpr size_t WS_END   = WS_XIN + (size_t)BOX * (SEQ / 2);

DI float bf2f(u16 b) { return __uint_as_float((unsigned)b << 16); }
DI float rbf(float x) { return (float)(__bf16)x; }
DI u16   f2bf(float x) { return __builtin_bit_cast(u16, (__bf16)x); }
DI float lo16(unsigned w) { return __uint_as_float(w << 16); }
DI float hi16(unsigned w) { return __uint_as_float(w & 0xffff0000u); }
DI unsigned cvtpk(float lo, float hi) {
  f32x2 v = {lo, hi}; bf16x2 b = __builtin_convertvector(v, bf16x2); return __builtin_bit_cast(unsigned, b);
}
template <int CTRL> DI float dpp_add(float v) { return v + __uint_as_float(__builtin_amdgcn_update_dpp(0u, __float_as_uint(v), CTRL, 0xf, 0xf, true)); }
DI float swap32_add(float a, float b) {
  auto r = __builtin_amdgcn_permlane32_swap(__float_as_uint(a), __float_as_uint(b), false, false); return __uint_as_float(r[0]) + __uint_as_float(r[1]);
}
DI float swap16_add(float a, float b) {
  auto r = __builtin_amdgcn_permlane16_swap(__float_as_uint(a), __float_as_uint(b), false, false); return __uint_as_float(r[0]) + __uint_as_float(r[1]);
}
constexpr int DPP_XOR1 = 0xB1, DPP_XOR2 = 0x4E, DPP_ROR4 = 0x124, DPP_ROR8 = 0x128;
DI float wave_sum(float v) {
  v = dpp_add<DPP_XOR1>(v); v = dpp_add<DPP_XOR2>(v); v = dpp_add<DPP_ROR4>(v); v = dpp_add<DPP_ROR8>(v);
  v = swap16_add(v, v); return swap32_add(v, v);
}

DI void st16_wt(void* p, u32x4 v) { asm volatile("global_store_dwordx4 %0, %1, off sc1\n\ts_nop 1" :: "v"(p), "v"(v) : "memory"); }

DI f32x2 unpk(unsigned w) { return f32x2{lo16(w), hi16(w)}; }
DI unsigned cvtpk2(f32x2 v) { return __builtin_bit_cast(unsigned, __builtin_convertvector(v, bf16x2)); }
DI f32x2 rb2(f32x2 v) { return f32x2{__uint_as_float(cvtpk(0.f, v[0])), __uint_as_float(cvtpk(0.f, v[1]))}; }
DI unsigned ln_pair(float x0, float x1, float m, float rden, float c0, float c1, float s0, float s1) {
  const unsigned d = cvtpk2(f32x2{x0, x1} - m), l = cvtpk2(unpk(d) * rden), t = cvtpk2(unpk(l) * f32x2{c0, c1});
  return cvtpk2(unpk(t) + f32x2{s0, s1});
}
#define GAS __attribute__((address_space(1)))
#define LAS __attribute__((address_space(3)))
typedef GAS unsigned long long gu64;
typedef short v4i16_t __attribute__((ext_vector_type(4)));
DI bf16x4 lds_tr(unsigned a) { return __builtin_bit_cast(bf16x4, __builtin_amdgcn_ds_read_tr16_b64_v4i16((LAS v4i16_t*)a)); }
#define RLX_AGENT __ATOMIC_RELAXED, __HIP_MEMORY_SCOPE_AGENT
constexpr unsigned SPIN_CAP = 1u << 16;

static_assert(BOX == 38016, "box size");
#define NP_FILL 8
#define GOT_WAIT "8"
#define NORM 0
#define UNITP 1
#define INS 0
#define XP2 0
#define FA_CLOBBERS "v0", "v1", "v2", "v3", "v4", "v5", "v6", "v7", "v8", "v9", "v10", "v11", "v12", "v13", "v14", "v15", "v16", "v17", "v18", "v19", "v20", "v21", "v22", "v23", "v24", "v25", "v26", "v27", "v28", "v29", "v30", "v31", "v32", "v33", "v34", "v35", "v36", "v37", "v38", "v39", "v40", "v41", "v42", "v43", "v44", "v45", "v46", "v47", "v48", "v49", "v50", "v51", "v52", "v53", "v54", "v55", "v56", "v57", "v58", "v59", "v60", "v61", "v62", "v63", "v64", "v65", "v66", "v67", "v68", "v69", "v70", "v71", "v72", "v73", "v74", "v75", "v76", "v77", "v78", "v79", "v80", "v81", "v82", "v83", "v84", "v85", "v86", "v87", "v88", "v89", "v90", "v91", "v92", "v93", "v94", "v95", "v96", "v97", "v98", "v99", "v100", "v101", "v102", "v103", "v104", "v105", "v106", "v107", "v108", "v109", "v110", "v111", "v112", "v113", "v114", "v115", "v116", "v117", "v118", "v119", "v120", "v121", "v122", "v123", "v124", "v125", "v126", "v127", "v128", "v129", "v130", "v131", "v132", "v133", "v134", "v135", "v136", "v137", "v138", "v139", "v140", "v141", "v142", "v143", "v144", "v145", "v146", "v147", "v148", "v149", "v150", "v151", "v152", "v153", "v154", "v155", "v156", "v157", "v158", "v159", "v160", "v161", "v162", "v163", "v164", "v165", "v166", "v167", "v168", "v169", "v170", "v171", "v172", "v173", "v174", "v175", "v176", "v177", "v178", "v179", "v180", "v181", "v182", "v183", "v184", "v185", "v186", "v187", "v188", "v189", "v190", "v191", "v192", "v193", "v194", "v195", "v196", "v197", "v198", "v199", "v200", "v201", "v202", "v203", "v204", "v205", "v206", "v207", "v208", "v209", "v210", "v211", "v212", "v213", "v214", "v215", "v216", "v217", "v218", "v219", "v220", "v221", "v222", "v223", "v224", "v230", "v231", "v232", "v233", "v234", "v235", "v237", "a0", "a1", "a2", "a3", "a4", "a5", "a6", "a7", "a8", "a9", "a10", "a11", "a12", "a13", "a14", "a15", "a16", "a17", "a18", "a19", "a20", "a21", "a22", "a23", "a24", "a25", "a26", "a27", "a28", "a29", "a30", "a31", "a32", "a33", "a34", "a35", "a36", "a37", "a38", "a39", "a40", "a41", "a42", "a43", "a44", "a45", "a46", "a47", "a48", "a49", "a50", "a51", "a52", "a53", "a54", "a55", "a56", "a57", "a58", "a59", "a60", "a61", "a62", "a63", "a64", "a65", "a66", "a67", "a68", "a69", "a70", "a71", "a72", "a73", "a74", "a75", "a76", "a77", "a78", "a79", "a80", "a81", "a82", "a83", "a84", "a85", "a86", "a87", "a88", "a89", "a90", "a91", "a92", "a93", "a94", "a95", "a96", "a97", "a98", "a99", "a100", "a101", "a102", "a103", "a104", "a105", "a106", "a107", "a108", "a109", "a110", "a111", "a112", "a113", "a114", "a115", "a116", "a117", "a118", "a119", "a120", "a121", "a122", "a123", "a124", "a125", "a126", "a127", "a128", "a129", "a130", "a131", "a132", "a133", "a134", "a135", "a136", "a137", "a138", "a139", "a140", "a141", "a142", "a143", "a144", "a145", "a146", "a147", "a148", "a149", "a150", "a151", "a152", "a153", "a154", "a155", "a156", "a157", "a158", "a159", "a160", "a161", "a162", "a163", "a164", "a165", "a166", "a167", "a168", "a169", "a170", "a171", "a172", "a173", "a174", "a175", "a176", "a177", "a178", "a179", "a180", "a181", "a182", "a183", "a184", "a185", "a186", "a187", "a188", "a189", "a190", "a191", "a192", "a193", "a194", "a195", "a196", "a197", "a198", "a199", "a200", "a201", "a202", "a203", "a204", "a205", "a206", "a207", "a208", "a209", "a210", "a211", "a212", "a213", "a214", "a215", "a216", "a217", "a218", "a219", "a220", "a221", "a222", "a223", "a224", "a225", "a226", "a227", "a228", "a229", "a230", "a231", "a232", "a233", "a234", "a235", "a236", "a237", "a238", "a239", "a240", "a241", "a242", "a243", "a244", "a245", "a246", "a247", "a248", "a249", "a250", "a251", "a252", "a253", "a254", "a255", "s40", "s41", "s42", "s43", "s44", "s45", "s46", "s47", "s48", "s49", "s50", "s51", "s52", "s53", "s54", "s55", "s56", "s57", "s58", "s59", "s60", "s61", "s62", "s63", "s64", "s65", "s66", "s67", "s68", "s69", "vcc", "m0", "scc", "memory"
#define EARLY2 0
#define SHW 1
#define QROT 0
#define SKV 0
#define KVPAR 0
#define QT2 0
#define QSTAG 0
#define ATAIL 1
static_assert(WS_MODT == 262144 && WS_XFL == 278528 && WS_U == 606208 && WS_XIN == 7340032, "workspace layout");
#define FA_OUTS [got] "=&s"(got), [k1] "=&s"(k1), [k2] "=&s"(k2), [lat] "=&s"(lat), "+{v229}"(fl), "+{v225}"(vwb), "+{v226}"(wd0), "+{v227}"(hq0), "+{v228}"(sd0), "+{v236}"(tq0), [kb0] "+v"(kb0), [vvb] "+v"(vvb), [kd0] "+v"(kd0), [vd0] "+v"(vd0), [oq0] "+v"(oq0), [rd0] "+v"(rd0), [wo0] "+v"(wo0), [vg0] "+v"(vg0), [vx0] "+v"(vx0), [vdn] "+v"(vdn), [ta0] "+v"(ta0), [vxs] "+v"(vxs), [vct] "+v"(vct)
#define FA_BODY \
"s_branch adaln_body\n" \
".p2align 12\n" \
"adaln_pg7:\n" \
"adaln_body:\n" \
"s_cbranch_execz adaln_fr\n" \
"s_nop 0\n" \
"s_mov_b32 s55, 0x80000\n" \
"v_add_u32_e32 v219, 0x8000, v227\n" \
"v_add_u32_e32 v220, 0x10000, v227\n" \
"v_add_u32_e32 v221, 0x18000, v227\n" \
"s_mov_b32 s50, 0\n" \
"s_movk_i32 s53, 0x1000\n" \
"s_mov_b32 s52, 0\n" \
"s_nop 0\n" \
"s_mov_b32 s49, 1572864\n" \
"v_add_u32_e32 v216, 0xfffffc80, v226\n" \
"v_add_u32_e32 v217, 0xf800, v226\n" \
"v_add_u32_e32 v218, 0xf480, v226\n" \
"s_add_u32 s40, %[m0b], 0\n" \
"s_nop 0\n" \
"s_add_u32 s44, %[m0b], 65536\n" \
"s_add_u32 s41, %[m0b], 16384\n" \
"s_add_u32 s45, %[m0b], 81920\n" \
"s_add_u32 s42, %[m0b], 32768\n" \
"s_add_u32 s46, %[m0b], 98304\n" \
"s_add_u32 s43, %[m0b], 49152\n" \
"s_add_u32 s47, %[m0b], 114688\n" \
"v_xor_b32_e64 v208, 64, %[kb0]\n" \
"v_xor_b32_e32 v209, 0x80, %[kb0]\n" \
"v_xor_b32_e32 v210, 0xc0, %[kb0]\n" \
"v_xor_b32_e64 v212, 64, %[kd0]\n" \
"v_xor_b32_e32 v213, 0x80, %[kd0]\n" \
"v_xor_b32_e32 v214, 0xc0, %[kd0]\n" \
"v_xor_b32_e64 v211, 32, %[vvb]\n" \
"v_add_u32_e32 v215, 0xfffffc80, %[vd0]\n" \
"v_readlane_b32 s64, %[tlo], 17\n" \
"v_readlane_b32 s65, %[thi], 17\n" \
"s_mov_b32 s66, 12582912\n" \
"s_mov_b32 s67, 0x20000\n" \
"v_mov_b64_e32 v[0:1], 0\n" \
"v_mov_b64_e32 v[2:3], 0\n" \
"v_mov_b64_e32 v[4:5], 0\n" \
"v_mov_b64_e32 v[6:7], 0\n" \
"v_mov_b64_e32 v[8:9], 0\n" \
"v_mov_b64_e32 v[10:11], 0\n" \
"v_mov_b64_e32 v[12:13], 0\n" \
"v_mov_b64_e32 v[14:15], 0\n" \
"v_mov_b64_e32 v[16:17], 0\n" \
"v_mov_b64_e32 v[18:19], 0\n" \
"v_mov_b64_e32 v[20:21], 0\n" \
"v_mov_b64_e32 v[22:23], 0\n" \
"v_mov_b64_e32 v[24:25], 0\n" \
"v_mov_b64_e32 v[26:27], 0\n" \
"v_mov_b64_e32 v[28:29], 0\n" \
"v_mov_b64_e32 v[30:31], 0\n" \
"v_mov_b64_e32 v[32:33], 0\n" \
"v_mov_b64_e32 v[34:35], 0\n" \
"v_mov_b64_e32 v[36:37], 0\n" \
"v_mov_b64_e32 v[38:39], 0\n" \
"v_mov_b64_e32 v[40:41], 0\n" \
"v_mov_b64_e32 v[42:43], 0\n" \
"v_mov_b64_e32 v[44:45], 0\n" \
"v_mov_b64_e32 v[46:47], 0\n" \
"v_mov_b64_e32 v[48:49], 0\n" \
"v_mov_b64_e32 v[50:51], 0\n" \
"v_mov_b64_e32 v[52:53], 0\n" \
"v_mov_b64_e32 v[54:55], 0\n" \
"v_mov_b64_e32 v[56:57], 0\n" \
"v_mov_b64_e32 v[58:59], 0\n" \
"v_mov_b64_e32 v[60:61], 0\n" \
"v_mov_b64_e32 v[62:63], 0\n" \
"v_mov_b64_e32 v[64:65], 0\n" \
"v_mov_b64_e32 v[66:67], 0\n" \
"v_mov_b64_e32 v[68:69], 0\n" \
"v_mov_b64_e32 v[70:71], 0\n" \
"v_mov_b64_e32 v[72:73], 0\n" \
"v_mov_b64_e32 v[74:75], 0\n" \
"v_mov_b64_e32 v[76:77], 0\n" \
"v_mov_b64_e32 v[78:79], 0\n" \
"v_mov_b64_e32 v[80:81], 0\n" \
"v_mov_b64_e32 v[82:83], 0\n" \
"v_mov_b64_e32 v[84:85], 0\n" \
"v_mov_b64_e32 v[86:87], 0\n" \
"v_mov_b64_e32 v[88:89], 0\n" \
"v_mov_b64_e32 v[90:91], 0\n" \
"v_mov_b64_e32 v[92:93], 0\n" \
"v_mov_b64_e32 v[94:95], 0\n" \
"v_mov_b64_e32 v[96:97], 0\n" \
"v_mov_b64_e32 v[98:99], 0\n" \
"v_mov_b64_e32 v[100:101], 0\n" \
"v_mov_b64_e32 v[102:103], 0\n" \
"v_mov_b64_e32 v[104:105], 0\n" \
"v_mov_b64_e32 v[106:107], 0\n" \
"v_mov_b64_e32 v[108:109], 0\n" \
"v_mov_b64_e32 v[110:111], 0\n" \
"v_mov_b64_e32 v[112:113], 0\n" \
"v_mov_b64_e32 v[114:115], 0\n" \
"v_mov_b64_e32 v[116:117], 0\n" \
"v_mov_b64_e32 v[118:119], 0\n" \
"v_mov_b64_e32 v[120:121], 0\n" \
"v_mov_b64_e32 v[122:123], 0\n" \
"v_mov_b64_e32 v[124:125], 0\n" \
"v_mov_b64_e32 v[126:127], 0\n" \
"s_mov_b32 s51, 0x10000\n" \
"v_mbcnt_lo_u32_b32 v232, -1, 0\n" \
"v_mov_b64_e32 v[234:235], %[tag]\n" \
"v_lshlrev_b32_e32 v233, 3, v232\n" \
"v_lshlrev_b32_e32 v232, 8, v232\n" \
"s_waitcnt vmcnt(0)\n" \
"s_barrier\n" \
"s_cmp_lg_u32 %[wid], 0\n" \
"s_cbranch_scc1 Lwalk_%=\n" \
"s_mov_b32 exec_hi, 0\n" \
"global_store_dwordx2 v232, v[234:235], %[psig] sc1\n" \
"s_mov_b32 exec_hi, -1\n" \
"Lgo_%=:\n" \
"s_nop 0\n" \
"ds_read_b64_tr_b16 v[128:129], v225 offset:0\n" \
"ds_read_b64_tr_b16 v[130:131], v225 offset:256\n" \
"ds_read_b64_tr_b16 v[132:133], v225 offset:8192\n" \
"ds_read_b64_tr_b16 v[134:135], v225 offset:8448\n" \
"ds_read_b64_tr_b16 v[136:137], v225 offset:8\n" \
"ds_read_b64_tr_b16 v[138:139], v225 offset:264\n" \
"ds_read_b64_tr_b16 v[140:141], v225 offset:8200\n" \
"ds_read_b64_tr_b16 v[142:143], v225 offset:8456\n" \
"ds_read_b64_tr_b16 v[144:145], v225 offset:512\n" \
"ds_read_b64_tr_b16 v[146:147], v225 offset:768\n" \
"ds_read_b64_tr_b16 v[148:149], v225 offset:8704\n" \
"ds_read_b64_tr_b16 v[150:151], v225 offset:8960\n" \
"ds_read_b64_tr_b16 v[152:153], v225 offset:520\n" \
"ds_read_b64_tr_b16 v[154:155], v225 offset:776\n" \
"ds_read_b64_tr_b16 v[156:157], v225 offset:8712\n" \
"ds_read_b64_tr_b16 v[158:159], v225 offset:8968\n" \
"s_mov_b32 s60, 4194304\n" \
"v_and_b32_e32 v233, 56, v233\n" \
"s_lshl_b32 s61, %[wid], 6\n" \
"v_add_u32_e64 v233, s61, v233\n" \
"Lpoll_%=:\n" \
"global_load_dwordx2 v[230:231], v233, %[pcnt] sc1\n" \
"s_waitcnt vmcnt(0)\n" \
"v_cmp_ne_u64_e32 vcc, v[230:231], v[234:235]\n" \
"s_sub_u32 s51, s51, 1\n" \
"s_cbranch_scc1 Lpd_%=\n" \
"s_nop 3\n" \
"s_cbranch_vccnz Lpoll_%=\n" \
"Lpd_%=:\n" \
"buffer_load_dwordx4 a[128:131], v227, %[srdz], s50 offen offset:0 sc1\n" \
"buffer_load_dwordx4 a[132:135], v219, %[srdz], s50 offen offset:0 sc1\n" \
"buffer_load_dwordx4 a[136:139], v220, %[srdz], s50 offen offset:0 sc1\n" \
"buffer_load_dwordx4 a[140:143], v221, %[srdz], s50 offen offset:0 sc1\n" \
"buffer_load_dwordx4 a[144:147], v227, %[srdz], s50 offen offset:1024 sc1\n" \
"buffer_load_dwordx4 a[148:151], v219, %[srdz], s50 offen offset:1024 sc1\n" \
"buffer_load_dwordx4 a[152:155], v220, %[srdz], s50 offen offset:1024 sc1\n" \
"buffer_load_dwordx4 a[156:159], v221, %[srdz], s50 offen offset:1024 sc1\n" \
"buffer_load_dwordx4 a[160:163], v227, %[srdz], s50 offen offset:2048 sc1\n" \
"buffer_load_dwordx4 a[164:167], v219, %[srdz], s50 offen offset:2048 sc1\n" \
"buffer_load_dwordx4 a[168:171], v220, %[srdz], s50 offen offset:2048 sc1\n" \
"buffer_load_dwordx4 a[172:175], v221, %[srdz], s50 offen offset:2048 sc1\n" \
"buffer_load_dwordx4 a[176:179], v227, %[srdz], s50 offen offset:3072 sc1\n" \
"buffer_load_dwordx4 a[180:183], v219, %[srdz], s50 offen offset:3072 sc1\n" \
"buffer_load_dwordx4 a[184:187], v220, %[srdz], s50 offen offset:3072 sc1\n" \
"buffer_load_dwordx4 a[188:191], v221, %[srdz], s50 offen offset:3072 sc1\n" \
"s_add_u32 s50, s50, s53\n" \
"s_nop 0\n" \
"buffer_load_dwordx4 a[192:195], v227, %[srdz], s50 offen offset:0 sc1\n" \
"buffer_load_dwordx4 a[196:199], v219, %[srdz], s50 offen offset:0 sc1\n" \
"buffer_load_dwordx4 a[200:203], v220, %[srdz], s50 offen offset:0 sc1\n" \
"buffer_load_dwordx4 a[204:207], v221, %[srdz], s50 offen offset:0 sc1\n" \
"buffer_load_dwordx4 a[208:211], v227, %[srdz], s50 offen offset:1024 sc1\n" \
"buffer_load_dwordx4 a[212:215], v219, %[srdz], s50 offen offset:1024 sc1\n" \
"buffer_load_dwordx4 a[216:219], v220, %[srdz], s50 offen offset:1024 sc1\n" \
"buffer_load_dwordx4 a[220:223], v221, %[srdz], s50 offen offset:1024 sc1\n" \
"s_mov_b32 s51, 3\n" \
"s_waitcnt vmcnt(16) lgkmcnt(0)\n" \
"Lq_%=:\n" \
"v_mfma_f32_16x16x32_bf16 v[0:3], v[128:131], a[128:131], v[0:3]\n" \
"ds_read_b64_tr_b16 v[160:161], v225 offset:1024\n" \
"v_mfma_f32_16x16x32_bf16 v[0:3], v[132:135], a[144:147], v[0:3]\n" \
"ds_read_b64_tr_b16 v[162:163], v225 offset:1280\n" \
"v_mfma_f32_16x16x32_bf16 v[32:35], v[128:131], a[132:135], v[32:35]\n" \
"s_mov_b32 m0, s47\n" \
"s_nop 0\n" \
"buffer_load_dwordx4 v217, %[srdw], s49 offen offset:2048 lds\n" \
"v_mfma_f32_16x16x32_bf16 v[32:35], v[132:135], a[148:151], v[32:35]\n" \
"ds_read_b64_tr_b16 v[164:165], v225 offset:9216\n" \
"v_mfma_f32_16x16x32_bf16 v[64:67], v[128:131], a[136:139], v[64:67]\n" \
"ds_read_b64_tr_b16 v[166:167], v225 offset:9472\n" \
"v_mfma_f32_16x16x32_bf16 v[64:67], v[132:135], a[152:155], v[64:67]\n" \
"v_mfma_f32_16x16x32_bf16 v[96:99], v[128:131], a[140:143], v[96:99]\n" \
"ds_read_b64_tr_b16 v[168:169], v225 offset:1032\n" \
"v_mfma_f32_16x16x32_bf16 v[96:99], v[132:135], a[156:159], v[96:99]\n" \
"ds_read_b64_tr_b16 v[170:171], v225 offset:1288\n" \
"v_mfma_f32_16x16x32_bf16 v[4:7], v[136:139], a[128:131], v[4:7]\n" \
"buffer_load_dwordx4 v218, %[srdw], s49 offen offset:3072 lds\n" \
"v_mfma_f32_16x16x32_bf16 v[4:7], v[140:143], a[144:147], v[4:7]\n" \
"ds_read_b64_tr_b16 v[172:173], v225 offset:9224\n" \
"v_mfma_f32_16x16x32_bf16 v[36:39], v[136:139], a[132:135], v[36:39]\n" \
"ds_read_b64_tr_b16 v[174:175], v225 offset:9480\n" \
"v_mfma_f32_16x16x32_bf16 v[36:39], v[140:143], a[148:151], v[36:39]\n" \
"v_mfma_f32_16x16x32_bf16 v[68:71], v[136:139], a[136:139], v[68:71]\n" \
"ds_read_b64_tr_b16 v[176:177], v225 offset:1536\n" \
"v_mfma_f32_16x16x32_bf16 v[68:71], v[140:143], a[152:155], v[68:71]\n" \
"ds_read_b64_tr_b16 v[178:179], v225 offset:1792\n" \
"v_mfma_f32_16x16x32_bf16 v[100:103], v[136:139], a[140:143], v[100:103]\n" \
"buffer_load_dwordx4 a[224:227], v227, %[srdz], s50 offen offset:2048 sc1\n" \
"v_mfma_f32_16x16x32_bf16 v[100:103], v[140:143], a[156:159], v[100:103]\n" \
"ds_read_b64_tr_b16 v[180:181], v225 offset:9728\n" \
"v_mfma_f32_16x16x32_bf16 v[8:11], v[144:147], a[128:131], v[8:11]\n" \
"ds_read_b64_tr_b16 v[182:183], v225 offset:9984\n" \
"v_mfma_f32_16x16x32_bf16 v[8:11], v[148:151], a[144:147], v[8:11]\n" \
"v_mfma_f32_16x16x32_bf16 v[40:43], v[144:147], a[132:135], v[40:43]\n" \
"ds_read_b64_tr_b16 v[184:185], v225 offset:1544\n" \
"v_mfma_f32_16x16x32_bf16 v[40:43], v[148:151], a[148:151], v[40:43]\n" \
"ds_read_b64_tr_b16 v[186:187], v225 offset:1800\n" \
"v_mfma_f32_16x16x32_bf16 v[72:75], v[144:147], a[136:139], v[72:75]\n" \
"buffer_load_dwordx4 a[228:231], v219, %[srdz], s50 offen offset:2048 sc1\n" \
"v_mfma_f32_16x16x32_bf16 v[72:75], v[148:151], a[152:155], v[72:75]\n" \
"ds_read_b64_tr_b16 v[188:189], v225 offset:9736\n" \
"v_mfma_f32_16x16x32_bf16 v[104:107], v[144:147], a[140:143], v[104:107]\n" \
"ds_read_b64_tr_b16 v[190:191], v225 offset:9992\n" \
"v_mfma_f32_16x16x32_bf16 v[104:107], v[148:151], a[156:159], v[104:107]\n" \
"v_mfma_f32_16x16x32_bf16 v[12:15], v[152:155], a[128:131], v[12:15]\n" \
"buffer_load_dwordx4 a[232:235], v220, %[srdz], s50 offen offset:2048 sc1\n" \
"v_mfma_f32_16x16x32_bf16 v[12:15], v[156:159], a[144:147], v[12:15]\n" \
"v_mfma_f32_16x16x32_bf16 v[44:47], v[152:155], a[132:135], v[44:47]\n" \
"v_mfma_f32_16x16x32_bf16 v[44:47], v[156:159], a[148:151], v[44:47]\n" \
"v_mfma_f32_16x16x32_bf16 v[76:79], v[152:155], a[136:139], v[76:79]\n" \
"buffer_load_dwordx4 a[236:239], v221, %[srdz], s50 offen offset:2048 sc1\n" \
"v_mfma_f32_16x16x32_bf16 v[76:79], v[156:159], a[152:155], v[76:79]\n" \
"v_mfma_f32_16x16x32_bf16 v[108:111], v[152:155], a[140:143], v[108:111]\n" \
"v_mfma_f32_16x16x32_bf16 v[108:111], v[156:159], a[156:159], v[108:111]\n" \
"s_waitcnt vmcnt(29) lgkmcnt(0)\n" \
"s_barrier\n" \
"v_mfma_f32_16x16x32_bf16 v[16:19], v[160:163], a[128:131], v[16:19]\n" \
"ds_read_b64_tr_b16 v[128:129], v225 offset:16384\n" \
"v_mfma_f32_16x16x32_bf16 v[16:19], v[164:167], a[144:147], v[16:19]\n" \
"ds_read_b64_tr_b16 v[130:131], v225 offset:16640\n" \
"v_mfma_f32_16x16x32_bf16 v[48:51], v[160:163], a[132:135], v[48:51]\n" \
"s_add_u32 s49, s49, s55\n" \
"s_mov_b32 m0, s44\n" \
"s_nop 0\n" \
"buffer_load_dwordx4 v226, %[srdw], s49 offen offset:0 lds\n" \
"v_mfma_f32_16x16x32_bf16 v[48:51], v[164:167], a[148:151], v[48:51]\n" \
"ds_read_b64_tr_b16 v[132:133], v225 offset:24576\n" \
"v_mfma_f32_16x16x32_bf16 v[80:83], v[160:163], a[136:139], v[80:83]\n" \
"ds_read_b64_tr_b16 v[134:135], v225 offset:24832\n" \
"v_mfma_f32_16x16x32_bf16 v[80:83], v[164:167], a[152:155], v[80:83]\n" \
"v_mfma_f32_16x16x32_bf16 v[112:115], v[160:163], a[140:143], v[112:115]\n" \
"ds_read_b64_tr_b16 v[136:137], v225 offset:16392\n" \
"v_mfma_f32_16x16x32_bf16 v[112:115], v[164:167], a[156:159], v[112:115]\n" \
"ds_read_b64_tr_b16 v[138:139], v225 offset:16648\n" \
"v_mfma_f32_16x16x32_bf16 v[20:23], v[168:171], a[128:131], v[20:23]\n" \
"buffer_load_dwordx4 v216, %[srdw], s49 offen offset:1024 lds\n" \
"v_mfma_f32_16x16x32_bf16 v[20:23], v[172:175], a[144:147], v[20:23]\n" \
"ds_read_b64_tr_b16 v[140:141], v225 offset:24584\n" \
"v_mfma_f32_16x16x32_bf16 v[52:55], v[168:171], a[132:135], v[52:55]\n" \
"ds_read_b64_tr_b16 v[142:143], v225 offset:24840\n" \
"v_mfma_f32_16x16x32_bf16 v[52:55], v[172:175], a[148:151], v[52:55]\n" \
"v_mfma_f32_16x16x32_bf16 v[84:87], v[168:171], a[136:139], v[84:87]\n" \
"ds_read_b64_tr_b16 v[144:145], v225 offset:16896\n" \
"v_mfma_f32_16x16x32_bf16 v[84:87], v[172:175], a[152:155], v[84:87]\n" \
"ds_read_b64_tr_b16 v[146:147], v225 offset:17152\n" \
"v_mfma_f32_16x16x32_bf16 v[116:119], v[168:171], a[140:143], v[116:119]\n" \
"buffer_load_dwordx4 a[240:243], v227, %[srdz], s50 offen offset:3072 sc1\n" \
"v_mfma_f32_16x16x32_bf16 v[116:119], v[172:175], a[156:159], v[116:119]\n" \
"ds_read_b64_tr_b16 v[148:149], v225 offset:25088\n" \
"v_mfma_f32_16x16x32_bf16 v[24:27], v[176:179], a[128:131], v[24:27]\n" \
"ds_read_b64_tr_b16 v[150:151], v225 offset:25344\n" \
"v_mfma_f32_16x16x32_bf16 v[24:27], v[180:183], a[144:147], v[24:27]\n" \
"v_mfma_f32_16x16x32_bf16 v[56:59], v[176:179], a[132:135], v[56:59]\n" \
"ds_read_b64_tr_b16 v[152:153], v225 offset:16904\n" \
"v_mfma_f32_16x16x32_bf16 v[56:59], v[180:183], a[148:151], v[56:59]\n" \
"ds_read_b64_tr_b16 v[154:155], v225 offset:17160\n" \
"v_mfma_f32_16x16x32_bf16 v[88:91], v[176:179], a[136:139], v[88:91]\n" \
"buffer_load_dwordx4 a[244:247], v219, %[srdz], s50 offen offset:3072 sc1\n" \
"v_mfma_f32_16x16x32_bf16 v[88:91], v[180:183], a[152:155], v[88:91]\n" \
"ds_read_b64_tr_b16 v[156:157], v225 offset:25096\n" \
"v_mfma_f32_16x16x32_bf16 v[120:123], v[176:179], a[140:143], v[120:123]\n" \
"ds_read_b64_tr_b16 v[158:159], v225 offset:25352\n" \
"v_mfma_f32_16x16x32_bf16 v[120:123], v[180:183], a[156:159], v[120:123]\n" \
"v_mfma_f32_16x16x32_bf16 v[28:31], v[184:187], a[128:131], v[28:31]\n" \
"buffer_load_dwordx4 a[248:251], v220, %[srdz], s50 offen offset:3072 sc1\n" \
"v_mfma_f32_16x16x32_bf16 v[28:31], v[188:191], a[144:147], v[28:31]\n" \
"v_mfma_f32_16x16x32_bf16 v[60:63], v[184:187], a[132:135], v[60:63]\n" \
"v_mfma_f32_16x16x32_bf16 v[60:63], v[188:191], a[148:151], v[60:63]\n" \
"v_mfma_f32_16x16x32_bf16 v[92:95], v[184:187], a[136:139], v[92:95]\n" \
"buffer_load_dwordx4 a[252:255], v221, %[srdz], s50 offen offset:3072 sc1\n" \
"v_mfma_f32_16x16x32_bf16 v[92:95], v[188:191], a[152:155], v[92:95]\n" \
"v_mfma_f32_16x16x32_bf16 v[124:127], v[184:187], a[140:143], v[124:127]\n" \
"v_mfma_f32_16x16x32_bf16 v[124:127], v[188:191], a[156:159], v[124:127]\n" \
"s_waitcnt vmcnt(20) lgkmcnt(0)\n" \
"v_mfma_f32_16x16x32_bf16 v[0:3], v[128:131], a[160:163], v[0:3]\n" \
"ds_read_b64_tr_b16 v[160:161], v225 offset:17408\n" \
"v_mfma_f32_16x16x32_bf16 v[0:3], v[132:135], a[176:179], v[0:3]\n" \
"ds_read_b64_tr_b16 v[162:163], v225 offset:17664\n" \
"v_mfma_f32_16x16x32_bf16 v[32:35], v[128:131], a[164:167], v[32:35]\n" \
"s_mov_b32 m0, s44\n" \
"s_nop 0\n" \
"buffer_load_dwordx4 v217, %[srdw], s49 offen offset:2048 lds\n" \
"v_mfma_f32_16x16x32_bf16 v[32:35], v[132:135], a[180:183], v[32:35]\n" \
"ds_read_b64_tr_b16 v[164:165], v225 offset:25600\n" \
"v_mfma_f32_16x16x32_bf16 v[64:67], v[128:131], a[168:171], v[64:67]\n" \
"ds_read_b64_tr_b16 v[166:167], v225 offset:25856\n" \
"v_mfma_f32_16x16x32_bf16 v[64:67], v[132:135], a[184:187], v[64:67]\n" \
"v_mfma_f32_16x16x32_bf16 v[96:99], v[128:131], a[172:175], v[96:99]\n" \
"ds_read_b64_tr_b16 v[168:169], v225 offset:17416\n" \
"v_mfma_f32_16x16x32_bf16 v[96:99], v[132:135], a[188:191], v[96:99]\n" \
"ds_read_b64_tr_b16 v[170:171], v225 offset:17672\n" \
"v_mfma_f32_16x16x32_bf16 v[4:7], v[136:139], a[160:163], v[4:7]\n" \
"buffer_load_dwordx4 v218, %[srdw], s49 offen offset:3072 lds\n" \
"v_mfma_f32_16x16x32_bf16 v[4:7], v[140:143], a[176:179], v[4:7]\n" \
"ds_read_b64_tr_b16 v[172:173], v225 offset:25608\n" \
"v_mfma_f32_16x16x32_bf16 v[36:39], v[136:139], a[164:167], v[36:39]\n" \
"ds_read_b64_tr_b16 v[174:175], v225 offset:25864\n" \
"v_mfma_f32_16x16x32_bf16 v[36:39], v[140:143], a[180:183], v[36:39]\n" \
"v_mfma_f32_16x16x32_bf16 v[68:71], v[136:139], a[168:171], v[68:71]\n" \
"ds_read_b64_tr_b16 v[176:177], v225 offset:17920\n" \
"v_mfma_f32_16x16x32_bf16 v[68:71], v[140:143], a[184:187], v[68:71]\n" \
"ds_read_b64_tr_b16 v[178:179], v225 offset:18176\n" \
"v_mfma_f32_16x16x32_bf16 v[100:103], v[136:139], a[172:175], v[100:103]\n" \
"s_add_u32 s50, s50, s53\n" \
"buffer_load_dwordx4 a[128:131], v227, %[srdz], s50 offen offset:0 sc1\n" \
"v_mfma_f32_16x16x32_bf16 v[100:103], v[140:143], a[188:191], v[100:103]\n" \
"ds_read_b64_tr_b16 v[180:181], v225 offset:26112\n" \
"v_mfma_f32_16x16x32_bf16 v[8:11], v[144:147], a[160:163], v[8:11]\n" \
"ds_read_b64_tr_b16 v[182:183], v225 offset:26368\n" \
"v_mfma_f32_16x16x32_bf16 v[8:11], v[148:151], a[176:179], v[8:11]\n" \
"v_mfma_f32_16x16x32_bf16 v[40:43], v[144:147], a[164:167], v[40:43]\n" \
"ds_read_b64_tr_b16 v[184:185], v225 offset:17928\n" \
"v_mfma_f32_16x16x32_bf16 v[40:43], v[148:151], a[180:183], v[40:43]\n" \
"ds_read_b64_tr_b16 v[186:187], v225 offset:18184\n" \
"v_mfma_f32_16x16x32_bf16 v[72:75], v[144:147], a[168:171], v[72:75]\n" \
"buffer_load_dwordx4 a[132:135], v219, %[srdz], s50 offen offset:0 sc1\n" \
"v_mfma_f32_16x16x32_bf16 v[72:75], v[148:151], a[184:187], v[72:75]\n" \
"ds_read_b64_tr_b16 v[188:189], v225 offset:26120\n" \
"v_mfma_f32_16x16x32_bf16 v[104:107], v[144:147], a[172:175], v[104:107]\n" \
"ds_read_b64_tr_b16 v[190:191], v225 offset:26376\n" \
"v_mfma_f32_16x16x32_bf16 v[104:107], v[148:151], a[188:191], v[104:107]\n" \
"v_mfma_f32_16x16x32_bf16 v[12:15], v[152:155], a[160:163], v[12:15]\n" \
"buffer_load_dwordx4 a[136:139], v220, %[srdz], s50 offen offset:0 sc1\n" \
"v_mfma_f32_16x16x32_bf16 v[12:15], v[156:159], a[176:179], v[12:15]\n" \
"v_mfma_f32_16x16x32_bf16 v[44:47], v[152:155], a[164:167], v[44:47]\n" \
"v_mfma_f32_16x16x32_bf16 v[44:47], v[156:159], a[180:183], v[44:47]\n" \
"v_mfma_f32_16x16x32_bf16 v[76:79], v[152:155], a[168:171], v[76:79]\n" \
"buffer_load_dwordx4 a[140:143], v221, %[srdz], s50 offen offset:0 sc1\n" \
"v_mfma_f32_16x16x32_bf16 v[76:79], v[156:159], a[184:187], v[76:79]\n" \
"v_mfma_f32_16x16x32_bf16 v[108:111], v[152:155], a[172:175], v[108:111]\n" \
"v_mfma_f32_16x16x32_bf16 v[108:111], v[156:159], a[188:191], v[108:111]\n" \
"s_waitcnt vmcnt(29) lgkmcnt(0)\n" \
"s_barrier\n" \
"v_mfma_f32_16x16x32_bf16 v[16:19], v[160:163], a[160:163], v[16:19]\n" \
"ds_read_b64_tr_b16 v[128:129], v225 offset:32768\n" \
"v_mfma_f32_16x16x32_bf16 v[16:19], v[164:167], a[176:179], v[16:19]\n" \
"ds_read_b64_tr_b16 v[130:131], v225 offset:33024\n" \
"v_mfma_f32_16x16x32_bf16 v[48:51], v[160:163], a[164:167], v[48:51]\n" \
"s_add_u32 s49, s49, s55\n" \
"s_mov_b32 m0, s45\n" \
"s_nop 0\n" \
"buffer_load_dwordx4 v226, %[srdw], s49 offen offset:0 lds\n" \
"v_mfma_f32_16x16x32_bf16 v[48:51], v[164:167], a[180:183], v[48:51]\n" \
"ds_read_b64_tr_b16 v[132:133], v225 offset:40960\n" \
"v_mfma_f32_16x16x32_bf16 v[80:83], v[160:163], a[168:171], v[80:83]\n" \
"ds_read_b64_tr_b16 v[134:135], v225 offset:41216\n" \
"v_mfma_f32_16x16x32_bf16 v[80:83], v[164:167], a[184:187], v[80:83]\n" \
"v_mfma_f32_16x16x32_bf16 v[112:115], v[160:163], a[172:175], v[112:115]\n" \
"ds_read_b64_tr_b16 v[136:137], v225 offset:32776\n" \
"v_mfma_f32_16x16x32_bf16 v[112:115], v[164:167], a[188:191], v[112:115]\n" \
"ds_read_b64_tr_b16 v[138:139], v225 offset:33032\n" \
"v_mfma_f32_16x16x32_bf16 v[20:23], v[168:171], a[160:163], v[20:23]\n" \
"buffer_load_dwordx4 v216, %[srdw], s49 offen offset:1024 lds\n" \
"v_mfma_f32_16x16x32_bf16 v[20:23], v[172:175], a[176:179], v[20:23]\n" \
"ds_read_b64_tr_b16 v[140:141], v225 offset:40968\n" \
"v_mfma_f32_16x16x32_bf16 v[52:55], v[168:171], a[164:167], v[52:55]\n" \
"ds_read_b64_tr_b16 v[142:143], v225 offset:41224\n" \
"v_mfma_f32_16x16x32_bf16 v[52:55], v[172:175], a[180:183], v[52:55]\n" \
"v_mfma_f32_16x16x32_bf16 v[84:87], v[168:171], a[168:171], v[84:87]\n" \
"ds_read_b64_tr_b16 v[144:145], v225 offset:33280\n" \
"v_mfma_f32_16x16x32_bf16 v[84:87], v[172:175], a[184:187], v[84:87]\n" \
"ds_read_b64_tr_b16 v[146:147], v225 offset:33536\n" \
"v_mfma_f32_16x16x32_bf16 v[116:119], v[168:171], a[172:175], v[116:119]\n" \
"buffer_load_dwordx4 a[144:147], v227, %[srdz], s50 offen offset:1024 sc1\n" \
"v_mfma_f32_16x16x32_bf16 v[116:119], v[172:175], a[188:191], v[116:119]\n" \
"ds_read_b64_tr_b16 v[148:149], v225 offset:41472\n" \
"v_mfma_f32_16x16x32_bf16 v[24:27], v[176:179], a[160:163], v[24:27]\n" \
"ds_read_b64_tr_b16 v[150:151], v225 offset:41728\n" \
"v_mfma_f32_16x16x32_bf16 v[24:27], v[180:183], a[176:179], v[24:27]\n" \
"v_mfma_f32_16x16x32_bf16 v[56:59], v[176:179], a[164:167], v[56:59]\n" \
"ds_read_b64_tr_b16 v[152:153], v225 offset:33288\n" \
"v_mfma_f32_16x16x32_bf16 v[56:59], v[180:183], a[180:183], v[56:59]\n" \
"ds_read_b64_tr_b16 v[154:155], v225 offset:33544\n" \
"v_mfma_f32_16x16x32_bf16 v[88:91], v[176:179], a[168:171], v[88:91]\n" \
"buffer_load_dwordx4 a[148:151], v219, %[srdz], s50 offen offset:1024 sc1\n" \
"v_mfma_f32_16x16x32_bf16 v[88:91], v[180:183], a[184:187], v[88:91]\n" \
"ds_read_b64_tr_b16 v[156:157], v225 offset:41480\n" \
"v_mfma_f32_16x16x32_bf16 v[120:123], v[176:179], a[172:175], v[120:123]\n" \
"ds_read_b64_tr_b16 v[158:159], v225 offset:41736\n" \
"v_mfma_f32_16x16x32_bf16 v[120:123], v[180:183], a[188:191], v[120:123]\n" \
"v_mfma_f32_16x16x32_bf16 v[28:31], v[184:187], a[160:163], v[28:31]\n" \
"buffer_load_dwordx4 a[152:155], v220, %[srdz], s50 offen offset:1024 sc1\n" \
"v_mfma_f32_16x16x32_bf16 v[28:31], v[188:191], a[176:179], v[28:31]\n" \
"v_mfma_f32_16x16x32_bf16 v[60:63], v[184:187], a[164:167], v[60:63]\n" \
"v_mfma_f32_16x16x32_bf16 v[60:63], v[188:191], a[180:183], v[60:63]\n" \
"v_mfma_f32_16x16x32_bf16 v[92:95], v[184:187], a[168:171], v[92:95]\n" \
"buffer_load_dwordx4 a[156:159], v221, %[srdz], s50 offen offset:1024 sc1\n" \
"v_mfma_f32_16x16x32_bf16 v[92:95], v[188:191], a[184:187], v[92:95]\n" \
"v_mfma_f32_16x16x32_bf16 v[124:127], v[184:187], a[172:175], v[124:127]\n" \
"v_mfma_f32_16x16x32_bf16 v[124:127], v[188:191], a[188:191], v[124:127]\n" \
"s_waitcnt vmcnt(24) lgkmcnt(0)\n" \
"v_mfma_f32_16x16x32_bf16 v[0:3], v[128:131], a[192:195], v[0:3]\n" \
"ds_read_b64_tr_b16 v[160:161], v225 offset:33792\n" \
"v_mfma_f32_16x16x32_bf16 v[0:3], v[132:135], a[208:211], v[0:3]\n" \
"ds_read_b64_tr_b16 v[162:163], v225 offset:34048\n" \
"v_mfma_f32_16x16x32_bf16 v[32:35], v[128:131], a[196:199], v[32:35]\n" \
"s_mov_b32 m0, s45\n" \
"s_nop 0\n" \
"buffer_load_dwordx4 v217, %[srdw], s49 offen offset:2048 lds\n" \
"v_mfma_f32_16x16x32_bf16 v[32:35], v[132:135], a[212:215], v[32:35]\n" \
"ds_read_b64_tr_b16 v[164:165], v225 offset:41984\n" \
"v_mfma_f32_16x16x32_bf16 v[64:67], v[128:131], a[200:203], v[64:67]\n" \
"ds_read_b64_tr_b16 v[166:167], v225 offset:42240\n" \
"v_mfma_f32_16x16x32_bf16 v[64:67], v[132:135], a[216:219], v[64:67]\n" \
"v_mfma_f32_16x16x32_bf16 v[96:99], v[128:131], a[204:207], v[96:99]\n" \
"ds_read_b64_tr_b16 v[168:169], v225 offset:33800\n" \
"v_mfma_f32_16x16x32_bf16 v[96:99], v[132:135], a[220:223], v[96:99]\n" \
"ds_read_b64_tr_b16 v[170:171], v225 offset:34056\n" \
"v_mfma_f32_16x16x32_bf16 v[4:7], v[136:139], a[192:195], v[4:7]\n" \
"buffer_load_dwordx4 v218, %[srdw], s49 offen offset:3072 lds\n" \
"v_mfma_f32_16x16x32_bf16 v[4:7], v[140:143], a[208:211], v[4:7]\n" \
"ds_read_b64_tr_b16 v[172:173], v225 offset:41992\n" \
"v_mfma_f32_16x16x32_bf16 v[36:39], v[136:139], a[196:199], v[36:39]\n" \
"ds_read_b64_tr_b16 v[174:175], v225 offset:42248\n" \
"v_mfma_f32_16x16x32_bf16 v[36:39], v[140:143], a[212:215], v[36:39]\n" \
"v_mfma_f32_16x16x32_bf16 v[68:71], v[136:139], a[200:203], v[68:71]\n" \
"ds_read_b64_tr_b16 v[176:177], v225 offset:34304\n" \
"v_mfma_f32_16x16x32_bf16 v[68:71], v[140:143], a[216:219], v[68:71]\n" \
"ds_read_b64_tr_b16 v[178:179], v225 offset:34560\n" \
"v_mfma_f32_16x16x32_bf16 v[100:103], v[136:139], a[204:207], v[100:103]\n" \
"buffer_load_dwordx4 a[160:163], v227, %[srdz], s50 offen offset:2048 sc1\n" \
"v_mfma_f32_16x16x32_bf16 v[100:103], v[140:143], a[220:223], v[100:103]\n" \
"ds_read_b64_tr_b16 v[180:181], v225 offset:42496\n" \
"v_mfma_f32_16x16x32_bf16 v[8:11], v[144:147], a[192:195], v[8:11]\n" \
"ds_read_b64_tr_b16 v[182:183], v225 offset:42752\n" \
"v_mfma_f32_16x16x32_bf16 v[8:11], v[148:151], a[208:211], v[8:11]\n" \
"v_mfma_f32_16x16x32_bf16 v[40:43], v[144:147], a[196:199], v[40:43]\n" \
"ds_read_b64_tr_b16 v[184:185], v225 offset:34312\n" \
"v_mfma_f32_16x16x32_bf16 v[40:43], v[148:151], a[212:215], v[40:43]\n" \
"ds_read_b64_tr_b16 v[186:187], v225 offset:34568\n" \
"v_mfma_f32_16x16x32_bf16 v[72:75], v[144:147], a[200:203], v[72:75]\n" \
"buffer_load_dwordx4 a[164:167], v219, %[srdz], s50 offen offset:2048 sc1\n" \
"v_mfma_f32_16x16x32_bf16 v[72:75], v[148:151], a[216:219], v[72:75]\n" \
"ds_read_b64_tr_b16 v[188:189], v225 offset:42504\n" \
"v_mfma_f32_16x16x32_bf16 v[104:107], v[144:147], a[204:207], v[104:107]\n" \
"ds_read_b64_tr_b16 v[190:191], v225 offset:42760\n" \
"v_mfma_f32_16x16x32_bf16 v[104:107], v[148:151], a[220:223], v[104:107]\n" \
"v_mfma_f32_16x16x32_bf16 v[12:15], v[152:155], a[192:195], v[12:15]\n" \
"buffer_load_dwordx4 a[168:171], v220, %[srdz], s50 offen offset:2048 sc1\n" \
"v_mfma_f32_16x16x32_bf16 v[12:15], v[156:159], a[208:211], v[12:15]\n" \
"v_mfma_f32_16x16x32_bf16 v[44:47], v[152:155], a[196:199], v[44:47]\n" \
"v_mfma_f32_16x16x32_bf16 v[44:47], v[156:159], a[212:215], v[44:47]\n" \
"v_mfma_f32_16x16x32_bf16 v[76:79], v[152:155], a[200:203], v[76:79]\n" \
"buffer_load_dwordx4 a[172:175], v221, %[srdz], s50 offen offset:2048 sc1\n" \
"v_mfma_f32_16x16x32_bf16 v[76:79], v[156:159], a[216:219], v[76:79]\n" \
"v_mfma_f32_16x16x32_bf16 v[108:111], v[152:155], a[204:207], v[108:111]\n" \
"v_mfma_f32_16x16x32_bf16 v[108:111], v[156:159], a[220:223], v[108:111]\n" \
"s_waitcnt vmcnt(28) lgkmcnt(0)\n" \
"s_barrier\n" \
"v_mfma_f32_16x16x32_bf16 v[16:19], v[160:163], a[192:195], v[16:19]\n" \
"ds_read_b64_tr_b16 v[128:129], v225 offset:49152\n" \
"v_mfma_f32_16x16x32_bf16 v[16:19], v[164:167], a[208:211], v[16:19]\n" \
"ds_read_b64_tr_b16 v[130:131], v225 offset:49408\n" \
"v_mfma_f32_16x16x32_bf16 v[48:51], v[160:163], a[196:199], v[48:51]\n" \
"s_add_u32 s49, s49, s55\n" \
"s_mov_b32 m0, s46\n" \
"s_nop 0\n" \
"buffer_load_dwordx4 v226, %[srdw], s49 offen offset:0 lds\n" \
"v_mfma_f32_16x16x32_bf16 v[48:51], v[164:167], a[212:215], v[48:51]\n" \
"ds_read_b64_tr_b16 v[132:133], v225 offset:57344\n" \
"v_mfma_f32_16x16x32_bf16 v[80:83], v[160:163], a[200:203], v[80:83]\n" \
"ds_read_b64_tr_b16 v[134:135], v225 offset:57600\n" \
"v_mfma_f32_16x16x32_bf16 v[80:83], v[164:167], a[216:219], v[80:83]\n" \
"v_mfma_f32_16x16x32_bf16 v[112:115], v[160:163], a[204:207], v[112:115]\n" \
"ds_read_b64_tr_b16 v[136:137], v225 offset:49160\n" \
"v_mfma_f32_16x16x32_bf16 v[112:115], v[164:167], a[220:223], v[112:115]\n" \
"ds_read_b64_tr_b16 v[138:139], v225 offset:49416\n" \
"v_mfma_f32_16x16x32_bf16 v[20:23], v[168:171], a[192:195], v[20:23]\n" \
"buffer_load_dwordx4 v216, %[srdw], s49 offen offset:1024 lds\n" \
"v_mfma_f32_16x16x32_bf16 v[20:23], v[172:175], a[208:211], v[20:23]\n" \
"ds_read_b64_tr_b16 v[140:141], v225 offset:57352\n" \
"v_mfma_f32_16x16x32_bf16 v[52:55], v[168:171], a[196:199], v[52:55]\n" \
"ds_read_b64_tr_b16 v[142:143], v225 offset:57608\n" \
"v_mfma_f32_16x16x32_bf16 v[52:55], v[172:175], a[212:215], v[52:55]\n" \
"v_mfma_f32_16x16x32_bf16 v[84:87], v[168:171], a[200:203], v[84:87]\n" \
"ds_read_b64_tr_b16 v[144:145], v225 offset:49664\n" \
"v_mfma_f32_16x16x32_bf16 v[84:87], v[172:175], a[216:219], v[84:87]\n" \
"ds_read_b64_tr_b16 v[146:147], v225 offset:49920\n" \
"v_mfma_f32_16x16x32_bf16 v[116:119], v[168:171], a[204:207], v[116:119]\n" \
"buffer_load_dwordx4 a[176:179], v227, %[srdz], s50 offen offset:3072 sc1\n" \
"v_mfma_f32_16x16x32_bf16 v[116:119], v[172:175], a[220:223], v[116:119]\n" \
"ds_read_b64_tr_b16 v[148:149], v225 offset:57856\n" \
"v_mfma_f32_16x16x32_bf16 v[24:27], v[176:179], a[192:195], v[24:27]\n" \
"ds_read_b64_tr_b16 v[150:151], v225 offset:58112\n" \
"v_mfma_f32_16x16x32_bf16 v[24:27], v[180:183], a[208:211], v[24:27]\n" \
"v_mfma_f32_16x16x32_bf16 v[56:59], v[176:179], a[196:199], v[56:59]\n" \
"ds_read_b64_tr_b16 v[152:153], v225 offset:49672\n" \
"v_mfma_f32_16x16x32_bf16 v[56:59], v[180:183], a[212:215], v[56:59]\n" \
"ds_read_b64_tr_b16 v[154:155], v225 offset:49928\n" \
"v_mfma_f32_16x16x32_bf16 v[88:91], v[176:179], a[200:203], v[88:91]\n" \
"buffer_load_dwordx4 a[180:183], v219, %[srdz], s50 offen offset:3072 sc1\n" \
"v_mfma_f32_16x16x32_bf16 v[88:91], v[180:183], a[216:219], v[88:91]\n" \
"ds_read_b64_tr_b16 v[156:157], v225 offset:57864\n" \
"v_mfma_f32_16x16x32_bf16 v[120:123], v[176:179], a[204:207], v[120:123]\n" \
"ds_read_b64_tr_b16 v[158:159], v225 offset:58120\n" \
"v_mfma_f32_16x16x32_bf16 v[120:123], v[180:183], a[220:223], v[120:123]\n" \
"v_mfma_f32_16x16x32_bf16 v[28:31], v[184:187], a[192:195], v[28:31]\n" \
"buffer_load_dwordx4 a[184:187], v220, %[srdz], s50 offen offset:3072 sc1\n" \
"v_mfma_f32_16x16x32_bf16 v[28:31], v[188:191], a[208:211], v[28:31]\n" \
"v_mfma_f32_16x16x32_bf16 v[60:63], v[184:187], a[196:199], v[60:63]\n" \
"v_mfma_f32_16x16x32_bf16 v[60:63], v[188:191], a[212:215], v[60:63]\n" \
"v_mfma_f32_16x16x32_bf16 v[92:95], v[184:187], a[200:203], v[92:95]\n" \
"buffer_load_dwordx4 a[188:191], v221, %[srdz], s50 offen offset:3072 sc1\n" \
"v_mfma_f32_16x16x32_bf16 v[92:95], v[188:191], a[216:219], v[92:95]\n" \
"v_mfma_f32_16x16x32_bf16 v[124:127], v[184:187], a[204:207], v[124:127]\n" \
"v_mfma_f32_16x16x32_bf16 v[124:127], v[188:191], a[220:223], v[124:127]\n" \
"s_waitcnt vmcnt(24) lgkmcnt(0)\n" \
"v_mfma_f32_16x16x32_bf16 v[0:3], v[128:131], a[224:227], v[0:3]\n" \
"ds_read_b64_tr_b16 v[160:161], v225 offset:50176\n" \
"v_mfma_f32_16x16x32_bf16 v[0:3], v[132:135], a[240:243], v[0:3]\n" \
"ds_read_b64_tr_b16 v[162:163], v225 offset:50432\n" \
"v_mfma_f32_16x16x32_bf16 v[32:35], v[128:131], a[228:231], v[32:35]\n" \
"s_mov_b32 m0, s46\n" \
"s_nop 0\n" \
"buffer_load_dwordx4 v217, %[srdw], s49 offen offset:2048 lds\n" \
"v_mfma_f32_16x16x32_bf16 v[32:35], v[132:135], a[244:247], v[32:35]\n" \
"ds_read_b64_tr_b16 v[164:165], v225 offset:58368\n" \
"v_mfma_f32_16x16x32_bf16 v[64:67], v[128:131], a[232:235], v[64:67]\n" \
"ds_read_b64_tr_b16 v[166:167], v225 offset:58624\n" \
"v_mfma_f32_16x16x32_bf16 v[64:67], v[132:135], a[248:251], v[64:67]\n" \
"v_mfma_f32_16x16x32_bf16 v[96:99], v[128:131], a[236:239], v[96:99]\n" \
"ds_read_b64_tr_b16 v[168:169], v225 offset:50184\n" \
"v_mfma_f32_16x16x32_bf16 v[96:99], v[132:135], a[252:255], v[96:99]\n" \
"ds_read_b64_tr_b16 v[170:171], v225 offset:50440\n" \
"v_mfma_f32_16x16x32_bf16 v[4:7], v[136:139], a[224:227], v[4:7]\n" \
"buffer_load_dwordx4 v218, %[srdw], s49 offen offset:3072 lds\n" \
"v_mfma_f32_16x16x32_bf16 v[4:7], v[140:143], a[240:243], v[4:7]\n" \
"ds_read_b64_tr_b16 v[172:173], v225 offset:58376\n" \
"v_mfma_f32_16x16x32_bf16 v[36:39], v[136:139], a[228:231], v[36:39]\n" \
"ds_read_b64_tr_b16 v[174:175], v225 offset:58632\n" \
"v_mfma_f32_16x16x32_bf16 v[36:39], v[140:143], a[244:247], v[36:39]\n" \
"v_mfma_f32_16x16x32_bf16 v[68:71], v[136:139], a[232:235], v[68:71]\n" \
"ds_read_b64_tr_b16 v[176:177], v225 offset:50688\n" \
"v_mfma_f32_16x16x32_bf16 v[68:71], v[140:143], a[248:251], v[68:71]\n" \
"ds_read_b64_tr_b16 v[178:179], v225 offset:50944\n" \
"v_mfma_f32_16x16x32_bf16 v[100:103], v[136:139], a[236:239], v[100:103]\n" \
"s_add_u32 s50, s50, s53\n" \
"buffer_load_dwordx4 a[192:195], v227, %[srdz], s50 offen offset:0 sc1\n" \
"v_mfma_f32_16x16x32_bf16 v[100:103], v[140:143], a[252:255], v[100:103]\n" \
"ds_read_b64_tr_b16 v[180:181], v225 offset:58880\n" \
"v_mfma_f32_16x16x32_bf16 v[8:11], v[144:147], a[224:227], v[8:11]\n" \
"ds_read_b64_tr_b16 v[182:183], v225 offset:59136\n" \
"v_mfma_f32_16x16x32_bf16 v[8:11], v[148:151], a[240:243], v[8:11]\n" \
"v_mfma_f32_16x16x32_bf16 v[40:43], v[144:147], a[228:231], v[40:43]\n" \
"ds_read_b64_tr_b16 v[184:185], v225 offset:50696\n" \
"v_mfma_f32_16x16x32_bf16 v[40:43], v[148:151], a[244:247], v[40:43]\n" \
"ds_read_b64_tr_b16 v[186:187], v225 offset:50952\n" \
"v_mfma_f32_16x16x32_bf16 v[72:75], v[144:147], a[232:235], v[72:75]\n" \
"buffer_load_dwordx4 a[196:199], v219, %[srdz], s50 offen offset:0 sc1\n" \
"v_mfma_f32_16x16x32_bf16 v[72:75], v[148:151], a[248:251], v[72:75]\n" \
"ds_read_b64_tr_b16 v[188:189], v225 offset:58888\n" \
"v_mfma_f32_16x16x32_bf16 v[104:107], v[144:147], a[236:239], v[104:107]\n" \
"ds_read_b64_tr_b16 v[190:191], v225 offset:59144\n" \
"v_mfma_f32_16x16x32_bf16 v[104:107], v[148:151], a[252:255], v[104:107]\n" \
"v_mfma_f32_16x16x32_bf16 v[12:15], v[152:155], a[224:227], v[12:15]\n" \
"buffer_load_dwordx4 a[200:203], v220, %[srdz], s50 offen offset:0 sc1\n" \
"v_mfma_f32_16x16x32_bf16 v[12:15], v[156:159], a[240:243], v[12:15]\n" \
"v_mfma_f32_16x16x32_bf16 v[44:47], v[152:155], a[228:231], v[44:47]\n" \
"buffer_load_dword v237, v236, %[srdw], s60 offen\n" \
"s_add_u32 s60, s60, 0x200000\n" \
"v_mfma_f32_16x16x32_bf16 v[44:47], v[156:159], a[244:247], v[44:47]\n" \
"v_mfma_f32_16x16x32_bf16 v[76:79], v[152:155], a[232:235], v[76:79]\n" \
"buffer_load_dwordx4 a[204:207], v221, %[srdz], s50 offen offset:0 sc1\n" \
"v_mfma_f32_16x16x32_bf16 v[76:79], v[156:159], a[248:251], v[76:79]\n" \
".if . - adaln_body != 4096\n" \
".error \"size model\"\n" \
".endif\n" \
"Lk0_%=:\n" \
"s_cbranch_execz Lk1_%=\n" \
"s_nop 0\n" \
"v_mfma_f32_16x16x32_bf16 v[108:111], v[152:155], a[236:239], v[108:111]\n" \
"v_mfma_f32_16x16x32_bf16 v[108:111], v[156:159], a[252:255], v[108:111]\n" \
"s_waitcnt vmcnt(29) lgkmcnt(0)\n" \
"s_barrier\n" \
"v_mfma_f32_16x16x32_bf16 v[16:19], v[160:163], a[224:227], v[16:19]\n" \
"ds_read_b64_tr_b16 v[128:129], v225 offset:0\n" \
"v_mfma_f32_16x16x32_bf16 v[16:19], v[164:167], a[240:243], v[16:19]\n" \
"ds_read_b64_tr_b16 v[130:131], v225 offset:256\n" \
"v_mfma_f32_16x16x32_bf16 v[48:51], v[160:163], a[228:231], v[48:51]\n" \
"s_add_u32 s49, s49, s55\n" \
"s_mov_b32 m0, s47\n" \
"s_nop 0\n" \
"buffer_load_dwordx4 v226, %[srdw], s49 offen offset:0 lds\n" \
"v_mfma_f32_16x16x32_bf16 v[48:51], v[164:167], a[244:247], v[48:51]\n" \
"ds_read_b64_tr_b16 v[132:133], v225 offset:8192\n" \
"v_mfma_f32_16x16x32_bf16 v[80:83], v[160:163], a[232:235], v[80:83]\n" \
"ds_read_b64_tr_b16 v[134:135], v225 offset:8448\n" \
"v_mfma_f32_16x16x32_bf16 v[80:83], v[164:167], a[248:251], v[80:83]\n" \
"v_mfma_f32_16x16x32_bf16 v[112:115], v[160:163], a[236:239], v[112:115]\n" \
"ds_read_b64_tr_b16 v[136:137], v225 offset:8\n" \
"v_mfma_f32_16x16x32_bf16 v[112:115], v[164:167], a[252:255], v[112:115]\n" \
"ds_read_b64_tr_b16 v[138:139], v225 offset:264\n" \
"v_mfma_f32_16x16x32_bf16 v[20:23], v[168:171], a[224:227], v[20:23]\n" \
"buffer_load_dwordx4 v216, %[srdw], s49 offen offset:1024 lds\n" \
"v_mfma_f32_16x16x32_bf16 v[20:23], v[172:175], a[240:243], v[20:23]\n" \
"ds_read_b64_tr_b16 v[140:141], v225 offset:8200\n" \
"v_mfma_f32_16x16x32_bf16 v[52:55], v[168:171], a[228:231], v[52:55]\n" \
"ds_read_b64_tr_b16 v[142:143], v225 offset:8456\n" \
"v_mfma_f32_16x16x32_bf16 v[52:55], v[172:175], a[244:247], v[52:55]\n" \
"v_mfma_f32_16x16x32_bf16 v[84:87], v[168:171], a[232:235], v[84:87]\n" \
"ds_read_b64_tr_b16 v[144:145], v225 offset:512\n" \
"v_mfma_f32_16x16x32_bf16 v[84:87], v[172:175], a[248:251], v[84:87]\n" \
"ds_read_b64_tr_b16 v[146:147], v225 offset:768\n" \
"v_mfma_f32_16x16x32_bf16 v[116:119], v[168:171], a[236:239], v[116:119]\n" \
"buffer_load_dwordx4 a[208:211], v227, %[srdz], s50 offen offset:1024 sc1\n" \
"v_mfma_f32_16x16x32_bf16 v[116:119], v[172:175], a[252:255], v[116:119]\n" \
"ds_read_b64_tr_b16 v[148:149], v225 offset:8704\n" \
"v_mfma_f32_16x16x32_bf16 v[24:27], v[176:179], a[224:227], v[24:27]\n" \
"ds_read_b64_tr_b16 v[150:151], v225 offset:8960\n" \
"v_mfma_f32_16x16x32_bf16 v[24:27], v[180:183], a[240:243], v[24:27]\n" \
"v_mfma_f32_16x16x32_bf16 v[56:59], v[176:179], a[228:231], v[56:59]\n" \
"ds_read_b64_tr_b16 v[152:153], v225 offset:520\n" \
"v_mfma_f32_16x16x32_bf16 v[56:59], v[180:183], a[244:247], v[56:59]\n" \
"ds_read_b64_tr_b16 v[154:155], v225 offset:776\n" \
"v_mfma_f32_16x16x32_bf16 v[88:91], v[176:179], a[232:235], v[88:91]\n" \
"buffer_load_dwordx4 a[212:215], v219, %[srdz], s50 offen offset:1024 sc1\n" \
"v_mfma_f32_16x16x32_bf16 v[88:91], v[180:183], a[248:251], v[88:91]\n" \
"ds_read_b64_tr_b16 v[156:157], v225 offset:8712\n" \
"v_mfma_f32_16x16x32_bf16 v[120:123], v[176:179], a[236:239], v[120:123]\n" \
"ds_read_b64_tr_b16 v[158:159], v225 offset:8968\n" \
"v_mfma_f32_16x16x32_bf16 v[120:123], v[180:183], a[252:255], v[120:123]\n" \
"v_mfma_f32_16x16x32_bf16 v[28:31], v[184:187], a[224:227], v[28:31]\n" \
"buffer_load_dwordx4 a[216:219], v220, %[srdz], s50 offen offset:1024 sc1\n" \
"v_mfma_f32_16x16x32_bf16 v[28:31], v[188:191], a[240:243], v[28:31]\n" \
"v_mfma_f32_16x16x32_bf16 v[60:63], v[184:187], a[228:231], v[60:63]\n" \
"v_mfma_f32_16x16x32_bf16 v[60:63], v[188:191], a[244:247], v[60:63]\n" \
"v_mfma_f32_16x16x32_bf16 v[92:95], v[184:187], a[232:235], v[92:95]\n" \
"buffer_load_dwordx4 a[220:223], v221, %[srdz], s50 offen offset:1024 sc1\n" \
"v_mfma_f32_16x16x32_bf16 v[92:95], v[188:191], a[248:251], v[92:95]\n" \
"v_mfma_f32_16x16x32_bf16 v[124:127], v[184:187], a[236:239], v[124:127]\n" \
"v_mfma_f32_16x16x32_bf16 v[124:127], v[188:191], a[252:255], v[124:127]\n" \
"s_waitcnt vmcnt(25) lgkmcnt(0)\n" \
"s_sub_u32 s51, s51, 1\n" \
"s_cmp_lg_u32 s51, 0\n" \
"s_cbranch_scc1 Lq_%=\n" \
"s_nop 0\n" \
"v_mfma_f32_16x16x32_bf16 v[0:3], v[128:131], a[128:131], v[0:3]\n" \
"ds_read_b64_tr_b16 v[160:161], v225 offset:1024\n" \
"v_mfma_f32_16x16x32_bf16 v[0:3], v[132:135], a[144:147], v[0:3]\n" \
"ds_read_b64_tr_b16 v[162:163], v225 offset:1280\n" \
"v_mfma_f32_16x16x32_bf16 v[32:35], v[128:131], a[132:135], v[32:35]\n" \
"s_mov_b32 m0, s47\n" \
"s_nop 0\n" \
"buffer_load_dwordx4 v217, %[srdw], s49 offen offset:2048 lds\n" \
"v_mfma_f32_16x16x32_bf16 v[32:35], v[132:135], a[148:151], v[32:35]\n" \
"ds_read_b64_tr_b16 v[164:165], v225 offset:9216\n" \
"v_mfma_f32_16x16x32_bf16 v[64:67], v[128:131], a[136:139], v[64:67]\n" \
"ds_read_b64_tr_b16 v[166:167], v225 offset:9472\n" \
"v_mfma_f32_16x16x32_bf16 v[64:67], v[132:135], a[152:155], v[64:67]\n" \
"v_mfma_f32_16x16x32_bf16 v[96:99], v[128:131], a[140:143], v[96:99]\n" \
"ds_read_b64_tr_b16 v[168:169], v225 offset:1032\n" \
"v_mfma_f32_16x16x32_bf16 v[96:99], v[132:135], a[156:159], v[96:99]\n" \
"ds_read_b64_tr_b16 v[170:171], v225 offset:1288\n" \
"v_mfma_f32_16x16x32_bf16 v[4:7], v[136:139], a[128:131], v[4:7]\n" \
"buffer_load_dwordx4 v218, %[srdw], s49 offen offset:3072 lds\n" \
"v_mfma_f32_16x16x32_bf16 v[4:7], v[140:143], a[144:147], v[4:7]\n" \
"ds_read_b64_tr_b16 v[172:173], v225 offset:9224\n" \
"v_mfma_f32_16x16x32_bf16 v[36:39], v[136:139], a[132:135], v[36:39]\n" \
"ds_read_b64_tr_b16 v[174:175], v225 offset:9480\n" \
"v_mfma_f32_16x16x32_bf16 v[36:39], v[140:143], a[148:151], v[36:39]\n" \
"v_mfma_f32_16x16x32_bf16 v[68:71], v[136:139], a[136:139], v[68:71]\n" \
"ds_read_b64_tr_b16 v[176:177], v225 offset:1536\n" \
"v_mfma_f32_16x16x32_bf16 v[68:71], v[140:143], a[152:155], v[68:71]\n" \
"ds_read_b64_tr_b16 v[178:179], v225 offset:1792\n" \
"v_mfma_f32_16x16x32_bf16 v[100:103], v[136:139], a[140:143], v[100:103]\n" \
"buffer_load_dwordx4 a[224:227], v227, %[srdz], s50 offen offset:2048 sc1\n" \
"v_mfma_f32_16x16x32_bf16 v[100:103], v[140:143], a[156:159], v[100:103]\n" \
"ds_read_b64_tr_b16 v[180:181], v225 offset:9728\n" \
"v_mfma_f32_16x16x32_bf16 v[8:11], v[144:147], a[128:131], v[8:11]\n" \
"ds_read_b64_tr_b16 v[182:183], v225 offset:9984\n" \
"v_mfma_f32_16x16x32_bf16 v[8:11], v[148:151], a[144:147], v[8:11]\n" \
"v_mfma_f32_16x16x32_bf16 v[40:43], v[144:147], a[132:135], v[40:43]\n" \
"ds_read_b64_tr_b16 v[184:185], v225 offset:1544\n" \
"v_mfma_f32_16x16x32_bf16 v[40:43], v[148:151], a[148:151], v[40:43]\n" \
"ds_read_b64_tr_b16 v[186:187], v225 offset:1800\n" \
"v_mfma_f32_16x16x32_bf16 v[72:75], v[144:147], a[136:139], v[72:75]\n" \
"buffer_load_dwordx4 a[228:231], v219, %[srdz], s50 offen offset:2048 sc1\n" \
"v_mfma_f32_16x16x32_bf16 v[72:75], v[148:151], a[152:155], v[72:75]\n" \
"ds_read_b64_tr_b16 v[188:189], v225 offset:9736\n" \
"v_mfma_f32_16x16x32_bf16 v[104:107], v[144:147], a[140:143], v[104:107]\n" \
"ds_read_b64_tr_b16 v[190:191], v225 offset:9992\n" \
"v_mfma_f32_16x16x32_bf16 v[104:107], v[148:151], a[156:159], v[104:107]\n" \
"v_mfma_f32_16x16x32_bf16 v[12:15], v[152:155], a[128:131], v[12:15]\n" \
"buffer_load_dwordx4 a[232:235], v220, %[srdz], s50 offen offset:2048 sc1\n" \
"v_mfma_f32_16x16x32_bf16 v[12:15], v[156:159], a[144:147], v[12:15]\n" \
"v_mfma_f32_16x16x32_bf16 v[44:47], v[152:155], a[132:135], v[44:47]\n" \
"v_mfma_f32_16x16x32_bf16 v[44:47], v[156:159], a[148:151], v[44:47]\n" \
"v_mfma_f32_16x16x32_bf16 v[76:79], v[152:155], a[136:139], v[76:79]\n" \
"buffer_load_dwordx4 a[236:239], v221, %[srdz], s50 offen offset:2048 sc1\n" \
"v_mfma_f32_16x16x32_bf16 v[76:79], v[156:159], a[152:155], v[76:79]\n" \
"v_mfma_f32_16x16x32_bf16 v[108:111], v[152:155], a[140:143], v[108:111]\n" \
"v_mfma_f32_16x16x32_bf16 v[108:111], v[156:159], a[156:159], v[108:111]\n" \
"s_waitcnt vmcnt(29) lgkmcnt(0)\n" \
"s_barrier\n" \
"v_mfma_f32_16x16x32_bf16 v[16:19], v[160:163], a[128:131], v[16:19]\n" \
"ds_read_b64_tr_b16 v[128:129], v225 offset:16384\n" \
"v_mfma_f32_16x16x32_bf16 v[16:19], v[164:167], a[144:147], v[16:19]\n" \
"ds_read_b64_tr_b16 v[130:131], v225 offset:16640\n" \
"v_mfma_f32_16x16x32_bf16 v[48:51], v[160:163], a[132:135], v[48:51]\n" \
"s_mov_b32 s48, 16384\n" \
"s_mov_b32 m0, s44\n" \
"s_nop 0\n" \
"buffer_load_dwordx4 %[vd0], %[srdv], s48 offen offset:0 lds\n" \
"v_mfma_f32_16x16x32_bf16 v[48:51], v[164:167], a[148:151], v[48:51]\n" \
"ds_read_b64_tr_b16 v[132:133], v225 offset:24576\n" \
"v_mfma_f32_16x16x32_bf16 v[80:83], v[160:163], a[136:139], v[80:83]\n" \
"ds_read_b64_tr_b16 v[134:135], v225 offset:24832\n" \
"v_mfma_f32_16x16x32_bf16 v[80:83], v[164:167], a[152:155], v[80:83]\n" \
"v_mfma_f32_16x16x32_bf16 v[112:115], v[160:163], a[140:143], v[112:115]\n" \
"ds_read_b64_tr_b16 v[136:137], v225 offset:16392\n" \
"v_mfma_f32_16x16x32_bf16 v[112:115], v[164:167], a[156:159], v[112:115]\n" \
"ds_read_b64_tr_b16 v[138:139], v225 offset:16648\n" \
"v_mfma_f32_16x16x32_bf16 v[20:23], v[168:171], a[128:131], v[20:23]\n" \
"buffer_load_dwordx4 v215, %[srdv], s48 offen offset:1024 lds\n" \
"v_mfma_f32_16x16x32_bf16 v[20:23], v[172:175], a[144:147], v[20:23]\n" \
"ds_read_b64_tr_b16 v[140:141], v225 offset:24584\n" \
"v_mfma_f32_16x16x32_bf16 v[52:55], v[168:171], a[132:135], v[52:55]\n" \
"ds_read_b64_tr_b16 v[142:143], v225 offset:24840\n" \
"v_mfma_f32_16x16x32_bf16 v[52:55], v[172:175], a[148:151], v[52:55]\n" \
"v_mfma_f32_16x16x32_bf16 v[84:87], v[168:171], a[136:139], v[84:87]\n" \
"ds_read_b64_tr_b16 v[144:145], v225 offset:16896\n" \
"v_mfma_f32_16x16x32_bf16 v[84:87], v[172:175], a[152:155], v[84:87]\n" \
"ds_read_b64_tr_b16 v[146:147], v225 offset:17152\n" \
"v_mfma_f32_16x16x32_bf16 v[116:119], v[168:171], a[140:143], v[116:119]\n" \
"buffer_load_dwordx4 a[240:243], v227, %[srdz], s50 offen offset:3072 sc1\n" \
"v_mfma_f32_16x16x32_bf16 v[116:119], v[172:175], a[156:159], v[116:119]\n" \
"ds_read_b64_tr_b16 v[148:149], v225 offset:25088\n" \
"v_mfma_f32_16x16x32_bf16 v[24:27], v[176:179], a[128:131], v[24:27]\n" \
"ds_read_b64_tr_b16 v[150:151], v225 offset:25344\n" \
"v_mfma_f32_16x16x32_bf16 v[24:27], v[180:183], a[144:147], v[24:27]\n" \
"v_mfma_f32_16x16x32_bf16 v[56:59], v[176:179], a[132:135], v[56:59]\n" \
"ds_read_b64_tr_b16 v[152:153], v225 offset:16904\n" \
"v_mfma_f32_16x16x32_bf16 v[56:59], v[180:183], a[148:151], v[56:59]\n" \
"ds_read_b64_tr_b16 v[154:155], v225 offset:17160\n" \
"v_mfma_f32_16x16x32_bf16 v[88:91], v[176:179], a[136:139], v[88:91]\n" \
"buffer_load_dwordx4 a[244:247], v219, %[srdz], s50 offen offset:3072 sc1\n" \
"v_mfma_f32_16x16x32_bf16 v[88:91], v[180:183], a[152:155], v[88:91]\n" \
"ds_read_b64_tr_b16 v[156:157], v225 offset:25096\n" \
"v_mfma_f32_16x16x32_bf16 v[120:123], v[176:179], a[140:143], v[120:123]\n" \
"ds_read_b64_tr_b16 v[158:159], v225 offset:25352\n" \
"v_mfma_f32_16x16x32_bf16 v[120:123], v[180:183], a[156:159], v[120:123]\n" \
"v_mfma_f32_16x16x32_bf16 v[28:31], v[184:187], a[128:131], v[28:31]\n" \
"buffer_load_dwordx4 a[248:251], v220, %[srdz], s50 offen offset:3072 sc1\n" \
"v_mfma_f32_16x16x32_bf16 v[28:31], v[188:191], a[144:147], v[28:31]\n" \
"v_mfma_f32_16x16x32_bf16 v[60:63], v[184:187], a[132:135], v[60:63]\n" \
"v_mfma_f32_16x16x32_bf16 v[60:63], v[188:191], a[148:151], v[60:63]\n" \
"v_mfma_f32_16x16x32_bf16 v[92:95], v[184:187], a[136:139], v[92:95]\n" \
"buffer_load_dwordx4 a[252:255], v221, %[srdz], s50 offen offset:3072 sc1\n" \
"v_mfma_f32_16x16x32_bf16 v[92:95], v[188:191], a[152:155], v[92:95]\n" \
"v_mfma_f32_16x16x32_bf16 v[124:127], v[184:187], a[140:143], v[124:127]\n" \
"v_mfma_f32_16x16x32_bf16 v[124:127], v[188:191], a[156:159], v[124:127]\n" \
"s_waitcnt vmcnt(25) lgkmcnt(0)\n" \
"v_mfma_f32_16x16x32_bf16 v[0:3], v[128:131], a[160:163], v[0:3]\n" \
"ds_read_b64_tr_b16 v[160:161], v225 offset:17408\n" \
"v_mfma_f32_16x16x32_bf16 v[0:3], v[132:135], a[176:179], v[0:3]\n" \
"ds_read_b64_tr_b16 v[162:163], v225 offset:17664\n" \
"v_mfma_f32_16x16x32_bf16 v[32:35], v[128:131], a[164:167], v[32:35]\n" \
"s_mov_b32 s48, 16384\n" \
"s_mov_b32 m0, s44\n" \
"s_nop 0\n" \
"buffer_load_dwordx4 %[vd0], %[srdv], s48 offen offset:2048 lds\n" \
"v_mfma_f32_16x16x32_bf16 v[32:35], v[132:135], a[180:183], v[32:35]\n" \
"ds_read_b64_tr_b16 v[164:165], v225 offset:25600\n" \
"v_mfma_f32_16x16x32_bf16 v[64:67], v[128:131], a[168:171], v[64:67]\n" \
"ds_read_b64_tr_b16 v[166:167], v225 offset:25856\n" \
"v_mfma_f32_16x16x32_bf16 v[64:67], v[132:135], a[184:187], v[64:67]\n" \
"v_mfma_f32_16x16x32_bf16 v[96:99], v[128:131], a[172:175], v[96:99]\n" \
"ds_read_b64_tr_b16 v[168:169], v225 offset:17416\n" \
"v_mfma_f32_16x16x32_bf16 v[96:99], v[132:135], a[188:191], v[96:99]\n" \
"ds_read_b64_tr_b16 v[170:171], v225 offset:17672\n" \
"v_mfma_f32_16x16x32_bf16 v[4:7], v[136:139], a[160:163], v[4:7]\n" \
"buffer_load_dwordx4 v215, %[srdv], s48 offen offset:3072 lds\n" \
"v_mfma_f32_16x16x32_bf16 v[4:7], v[140:143], a[176:179], v[4:7]\n" \
"ds_read_b64_tr_b16 v[172:173], v225 offset:25608\n" \
"v_mfma_f32_16x16x32_bf16 v[36:39], v[136:139], a[164:167], v[36:39]\n" \
"ds_read_b64_tr_b16 v[174:175], v225 offset:25864\n" \
"v_mfma_f32_16x16x32_bf16 v[36:39], v[140:143], a[180:183], v[36:39]\n" \
"v_mfma_f32_16x16x32_bf16 v[68:71], v[136:139], a[168:171], v[68:71]\n" \
"ds_read_b64_tr_b16 v[176:177], v225 offset:17920\n" \
"v_mfma_f32_16x16x32_bf16 v[68:71], v[140:143], a[184:187], v[68:71]\n" \
"ds_read_b64_tr_b16 v[178:179], v225 offset:18176\n" \
"v_mfma_f32_16x16x32_bf16 v[100:103], v[136:139], a[172:175], v[100:103]\n" \
"s_mov_b32 s48, 0\n" \
"s_mov_b32 m0, s40\n" \
"s_nop 0\n" \
"buffer_load_dwordx4 %[kd0], %[srdk], s48 offen offset:0 lds\n" \
"v_mfma_f32_16x16x32_bf16 v[100:103], v[140:143], a[188:191], v[100:103]\n" \
"ds_read_b64_tr_b16 v[180:181], v225 offset:26112\n" \
"v_mfma_f32_16x16x32_bf16 v[8:11], v[144:147], a[160:163], v[8:11]\n" \
"ds_read_b64_tr_b16 v[182:183], v225 offset:26368\n" \
"v_mfma_f32_16x16x32_bf16 v[8:11], v[148:151], a[176:179], v[8:11]\n" \
"v_mfma_f32_16x16x32_bf16 v[40:43], v[144:147], a[164:167], v[40:43]\n" \
"ds_read_b64_tr_b16 v[184:185], v225 offset:17928\n" \
"v_mfma_f32_16x16x32_bf16 v[40:43], v[148:151], a[180:183], v[40:43]\n" \
"ds_read_b64_tr_b16 v[186:187], v225 offset:18184\n" \
"v_mfma_f32_16x16x32_bf16 v[72:75], v[144:147], a[168:171], v[72:75]\n" \
"buffer_load_dwordx4 v212, %[srdk], s48 offen offset:1024 lds\n" \
"v_mfma_f32_16x16x32_bf16 v[72:75], v[148:151], a[184:187], v[72:75]\n" \
"ds_read_b64_tr_b16 v[188:189], v225 offset:26120\n" \
"v_mfma_f32_16x16x32_bf16 v[104:107], v[144:147], a[172:175], v[104:107]\n" \
"ds_read_b64_tr_b16 v[190:191], v225 offset:26376\n" \
"v_mfma_f32_16x16x32_bf16 v[104:107], v[148:151], a[188:191], v[104:107]\n" \
"v_mfma_f32_16x16x32_bf16 v[12:15], v[152:155], a[160:163], v[12:15]\n" \
"buffer_load_dwordx4 v213, %[srdk], s48 offen offset:2048 lds\n" \
"v_mfma_f32_16x16x32_bf16 v[12:15], v[156:159], a[176:179], v[12:15]\n" \
"v_mfma_f32_16x16x32_bf16 v[44:47], v[152:155], a[164:167], v[44:47]\n" \
"v_mfma_f32_16x16x32_bf16 v[44:47], v[156:159], a[180:183], v[44:47]\n" \
"v_mfma_f32_16x16x32_bf16 v[76:79], v[152:155], a[168:171], v[76:79]\n" \
"buffer_load_dwordx4 v214, %[srdk], s48 offen offset:3072 lds\n" \
"v_mfma_f32_16x16x32_bf16 v[76:79], v[156:159], a[184:187], v[76:79]\n" \
"v_mfma_f32_16x16x32_bf16 v[108:111], v[152:155], a[172:175], v[108:111]\n" \
"v_mfma_f32_16x16x32_bf16 v[108:111], v[156:159], a[188:191], v[108:111]\n" \
"s_waitcnt vmcnt(29) lgkmcnt(0)\n" \
"s_barrier\n" \
"v_mfma_f32_16x16x32_bf16 v[16:19], v[160:163], a[160:163], v[16:19]\n" \
"ds_read_b64_tr_b16 v[128:129], v225 offset:32768\n" \
"v_mfma_f32_16x16x32_bf16 v[16:19], v[164:167], a[176:179], v[16:19]\n" \
"ds_read_b64_tr_b16 v[130:131], v225 offset:33024\n" \
"v_mfma_f32_16x16x32_bf16 v[48:51], v[160:163], a[164:167], v[48:51]\n" \
"s_mov_b32 s48, 32768\n" \
"s_mov_b32 m0, s45\n" \
"s_nop 0\n" \
"buffer_load_dwordx4 %[vd0], %[srdv], s48 offen offset:0 lds\n" \
"v_mfma_f32_16x16x32_bf16 v[48:51], v[164:167], a[180:183], v[48:51]\n" \
"ds_read_b64_tr_b16 v[132:133], v225 offset:40960\n" \
"v_mfma_f32_16x16x32_bf16 v[80:83], v[160:163], a[168:171], v[80:83]\n" \
"ds_read_b64_tr_b16 v[134:135], v225 offset:41216\n" \
"v_mfma_f32_16x16x32_bf16 v[80:83], v[164:167], a[184:187], v[80:83]\n" \
"v_mfma_f32_16x16x32_bf16 v[112:115], v[160:163], a[172:175], v[112:115]\n" \
"ds_read_b64_tr_b16 v[136:137], v225 offset:32776\n" \
"v_mfma_f32_16x16x32_bf16 v[112:115], v[164:167], a[188:191], v[112:115]\n" \
"ds_read_b64_tr_b16 v[138:139], v225 offset:33032\n" \
"v_mfma_f32_16x16x32_bf16 v[20:23], v[168:171], a[160:163], v[20:23]\n" \
"buffer_load_dwordx4 v215, %[srdv], s48 offen offset:1024 lds\n" \
"v_mfma_f32_16x16x32_bf16 v[20:23], v[172:175], a[176:179], v[20:23]\n" \
"ds_read_b64_tr_b16 v[140:141], v225 offset:40968\n" \
"v_mfma_f32_16x16x32_bf16 v[52:55], v[168:171], a[164:167], v[52:55]\n" \
"ds_read_b64_tr_b16 v[142:143], v225 offset:41224\n" \
"v_mfma_f32_16x16x32_bf16 v[52:55], v[172:175], a[180:183], v[52:55]\n" \
"v_mfma_f32_16x16x32_bf16 v[84:87], v[168:171], a[168:171], v[84:87]\n" \
"ds_read_b64_tr_b16 v[144:145], v225 offset:33280\n" \
"v_mfma_f32_16x16x32_bf16 v[84:87], v[172:175], a[184:187], v[84:87]\n" \
"ds_read_b64_tr_b16 v[146:147], v225 offset:33536\n" \
"v_mfma_f32_16x16x32_bf16 v[116:119], v[168:171], a[172:175], v[116:119]\n" \
"s_mov_b32 s48, 16384\n" \
"s_mov_b32 m0, s41\n" \
"s_nop 0\n" \
"buffer_load_dwordx4 %[kd0], %[srdk], s48 offen offset:0 lds\n" \
"v_mfma_f32_16x16x32_bf16 v[116:119], v[172:175], a[188:191], v[116:119]\n" \
"ds_read_b64_tr_b16 v[148:149], v225 offset:41472\n" \
"v_mfma_f32_16x16x32_bf16 v[24:27], v[176:179], a[160:163], v[24:27]\n" \
"ds_read_b64_tr_b16 v[150:151], v225 offset:41728\n" \
"v_mfma_f32_16x16x32_bf16 v[24:27], v[180:183], a[176:179], v[24:27]\n" \
"v_mfma_f32_16x16x32_bf16 v[56:59], v[176:179], a[164:167], v[56:59]\n" \
"ds_read_b64_tr_b16 v[152:153], v225 offset:33288\n" \
"v_mfma_f32_16x16x32_bf16 v[56:59], v[180:183], a[180:183], v[56:59]\n" \
"ds_read_b64_tr_b16 v[154:155], v225 offset:33544\n" \
"v_mfma_f32_16x16x32_bf16 v[88:91], v[176:179], a[168:171], v[88:91]\n" \
"buffer_load_dwordx4 v212, %[srdk], s48 offen offset:1024 lds\n" \
"v_mfma_f32_16x16x32_bf16 v[88:91], v[180:183], a[184:187], v[88:91]\n" \
"ds_read_b64_tr_b16 v[156:157], v225 offset:41480\n" \
"v_mfma_f32_16x16x32_bf16 v[120:123], v[176:179], a[172:175], v[120:123]\n" \
"ds_read_b64_tr_b16 v[158:159], v225 offset:41736\n" \
"v_mfma_f32_16x16x32_bf16 v[120:123], v[180:183], a[188:191], v[120:123]\n" \
"v_mfma_f32_16x16x32_bf16 v[28:31], v[184:187], a[160:163], v[28:31]\n" \
"buffer_load_dwordx4 v213, %[srdk], s48 offen offset:2048 lds\n" \
"v_mfma_f32_16x16x32_bf16 v[28:31], v[188:191], a[176:179], v[28:31]\n" \
"v_mfma_f32_16x16x32_bf16 v[60:63], v[184:187], a[164:167], v[60:63]\n" \
"v_mfma_f32_16x16x32_bf16 v[60:63], v[188:191], a[180:183], v[60:63]\n" \
"v_mfma_f32_16x16x32_bf16 v[92:95], v[184:187], a[168:171], v[92:95]\n" \
"buffer_load_dwordx4 v214, %[srdk], s48 offen offset:3072 lds\n" \
"v_mfma_f32_16x16x32_bf16 v[92:95], v[188:191], a[184:187], v[92:95]\n" \
"v_mfma_f32_16x16x32_bf16 v[124:127], v[184:187], a[172:175], v[124:127]\n" \
"v_mfma_f32_16x16x32_bf16 v[124:127], v[188:191], a[188:191], v[124:127]\n" \
"s_waitcnt vmcnt(24) lgkmcnt(0)\n" \
"v_mfma_f32_16x16x32_bf16 v[0:3], v[128:131], a[192:195], v[0:3]\n" \
"ds_read_b64_tr_b16 v[160:161], v225 offset:33792\n" \
"v_mfma_f32_16x16x32_bf16 v[0:3], v[132:135], a[208:211], v[0:3]\n" \
"ds_read_b64_tr_b16 v[162:163], v225 offset:34048\n" \
"v_mfma_f32_16x16x32_bf16 v[32:35], v[128:131], a[196:199], v[32:35]\n" \
"s_mov_b32 s48, 32768\n" \
"s_mov_b32 m0, s45\n" \
"s_nop 0\n" \
"buffer_load_dwordx4 %[vd0], %[srdv], s48 offen offset:2048 lds\n" \
"v_mfma_f32_16x16x32_bf16 v[32:35], v[132:135], a[212:215], v[32:35]\n" \
"ds_read_b64_tr_b16 v[164:165], v225 offset:41984\n" \
"v_mfma_f32_16x16x32_bf16 v[64:67], v[128:131], a[200:203], v[64:67]\n" \
"ds_read_b64_tr_b16 v[166:167], v225 offset:42240\n" \
"v_mfma_f32_16x16x32_bf16 v[64:67], v[132:135], a[216:219], v[64:67]\n" \
"v_mfma_f32_16x16x32_bf16 v[96:99], v[128:131], a[204:207], v[96:99]\n" \
"ds_read_b64_tr_b16 v[168:169], v225 offset:33800\n" \
"v_mfma_f32_16x16x32_bf16 v[96:99], v[132:135], a[220:223], v[96:99]\n" \
"ds_read_b64_tr_b16 v[170:171], v225 offset:34056\n" \
"v_mfma_f32_16x16x32_bf16 v[4:7], v[136:139], a[192:195], v[4:7]\n" \
"buffer_load_dwordx4 v215, %[srdv], s48 offen offset:3072 lds\n" \
"v_mfma_f32_16x16x32_bf16 v[4:7], v[140:143], a[208:211], v[4:7]\n" \
"ds_read_b64_tr_b16 v[172:173], v225 offset:41992\n" \
"v_mfma_f32_16x16x32_bf16 v[36:39], v[136:139], a[196:199], v[36:39]\n" \
"ds_read_b64_tr_b16 v[174:175], v225 offset:42248\n" \
"v_mfma_f32_16x16x32_bf16 v[36:39], v[140:143], a[212:215], v[36:39]\n" \
"v_mfma_f32_16x16x32_bf16 v[68:71], v[136:139], a[200:203], v[68:71]\n" \
"ds_read_b64_tr_b16 v[176:177], v225 offset:34304\n" \
"v_mfma_f32_16x16x32_bf16 v[68:71], v[140:143], a[216:219], v[68:71]\n" \
"ds_read_b64_tr_b16 v[178:179], v225 offset:34560\n" \
"v_mfma_f32_16x16x32_bf16 v[100:103], v[136:139], a[204:207], v[100:103]\n" \
"s_mov_b32 s48, 32768\n" \
"s_mov_b32 m0, s42\n" \
"s_nop 0\n" \
"buffer_load_dwordx4 %[kd0], %[srdk], s48 offen offset:0 lds\n" \
"v_mfma_f32_16x16x32_bf16 v[100:103], v[140:143], a[220:223], v[100:103]\n" \
"ds_read_b64_tr_b16 v[180:181], v225 offset:42496\n" \
"v_mfma_f32_16x16x32_bf16 v[8:11], v[144:147], a[192:195], v[8:11]\n" \
"ds_read_b64_tr_b16 v[182:183], v225 offset:42752\n" \
"v_mfma_f32_16x16x32_bf16 v[8:11], v[148:151], a[208:211], v[8:11]\n" \
"v_mfma_f32_16x16x32_bf16 v[40:43], v[144:147], a[196:199], v[40:43]\n" \
"ds_read_b64_tr_b16 v[184:185], v225 offset:34312\n" \
"v_mfma_f32_16x16x32_bf16 v[40:43], v[148:151], a[212:215], v[40:43]\n" \
"ds_read_b64_tr_b16 v[186:187], v225 offset:34568\n" \
"v_mfma_f32_16x16x32_bf16 v[72:75], v[144:147], a[200:203], v[72:75]\n" \
"buffer_load_dwordx4 v212, %[srdk], s48 offen offset:1024 lds\n" \
"v_mfma_f32_16x16x32_bf16 v[72:75], v[148:151], a[216:219], v[72:75]\n" \
"ds_read_b64_tr_b16 v[188:189], v225 offset:42504\n" \
"v_mfma_f32_16x16x32_bf16 v[104:107], v[144:147], a[204:207], v[104:107]\n" \
"ds_read_b64_tr_b16 v[190:191], v225 offset:42760\n" \
"v_mfma_f32_16x16x32_bf16 v[104:107], v[148:151], a[220:223], v[104:107]\n" \
"v_mfma_f32_16x16x32_bf16 v[12:15], v[152:155], a[192:195], v[12:15]\n" \
"buffer_load_dwordx4 v213, %[srdk], s48 offen offset:2048 lds\n" \
"v_mfma_f32_16x16x32_bf16 v[12:15], v[156:159], a[208:211], v[12:15]\n" \
"v_mfma_f32_16x16x32_bf16 v[44:47], v[152:155], a[196:199], v[44:47]\n" \
"v_mfma_f32_16x16x32_bf16 v[44:47], v[156:159], a[212:215], v[44:47]\n" \
"v_mfma_f32_16x16x32_bf16 v[76:79], v[152:155], a[200:203], v[76:79]\n" \
"buffer_load_dwordx4 v214, %[srdk], s48 offen offset:3072 lds\n" \
"v_mfma_f32_16x16x32_bf16 v[76:79], v[156:159], a[216:219], v[76:79]\n" \
"v_mfma_f32_16x16x32_bf16 v[108:111], v[152:155], a[204:207], v[108:111]\n" \
"v_mfma_f32_16x16x32_bf16 v[108:111], v[156:159], a[220:223], v[108:111]\n" \
"s_waitcnt vmcnt(28) lgkmcnt(0)\n" \
"s_barrier\n" \
"v_mfma_f32_16x16x32_bf16 v[16:19], v[160:163], a[192:195], v[16:19]\n" \
"ds_read_b64_tr_b16 v[128:129], v225 offset:49152\n" \
"s_mov_b32 s54, 0x4000\n" \
"v_mfma_f32_16x16x32_bf16 v[16:19], v[164:167], a[208:211], v[16:19]\n" \
"ds_read_b64_tr_b16 v[130:131], v225 offset:49408\n" \
"v_mfma_f32_16x16x32_bf16 v[48:51], v[160:163], a[196:199], v[48:51]\n" \
"s_mov_b32 s48, 49152\n" \
"s_mov_b32 m0, s46\n" \
"s_nop 0\n" \
"buffer_load_dwordx4 %[vd0], %[srdv], s48 offen offset:0 lds\n" \
"v_mbcnt_lo_u32_b32 v231, -1, 0\n" \
"v_mfma_f32_16x16x32_bf16 v[48:51], v[164:167], a[212:215], v[48:51]\n" \
"ds_read_b64_tr_b16 v[132:133], v225 offset:57344\n" \
"v_mfma_f32_16x16x32_bf16 v[80:83], v[160:163], a[200:203], v[80:83]\n" \
"ds_read_b64_tr_b16 v[134:135], v225 offset:57600\n" \
"v_mfma_f32_16x16x32_bf16 v[80:83], v[164:167], a[216:219], v[80:83]\n" \
"v_mbcnt_hi_u32_b32 v231, -1, v231\n" \
"v_mfma_f32_16x16x32_bf16 v[112:115], v[160:163], a[204:207], v[112:115]\n" \
"ds_read_b64_tr_b16 v[136:137], v225 offset:49160\n" \
"v_mfma_f32_16x16x32_bf16 v[112:115], v[164:167], a[220:223], v[112:115]\n" \
"ds_read_b64_tr_b16 v[138:139], v225 offset:49416\n" \
"v_mfma_f32_16x16x32_bf16 v[20:23], v[168:171], a[192:195], v[20:23]\n" \
"buffer_load_dwordx4 v215, %[srdv], s48 offen offset:1024 lds\n" \
"v_lshrrev_b32_e32 v232, 4, v231\n" \
"v_mfma_f32_16x16x32_bf16 v[20:23], v[172:175], a[208:211], v[20:23]\n" \
"ds_read_b64_tr_b16 v[140:141], v225 offset:57352\n" \
"v_mfma_f32_16x16x32_bf16 v[52:55], v[168:171], a[196:199], v[52:55]\n" \
"ds_read_b64_tr_b16 v[142:143], v225 offset:57608\n" \
"v_xor_b32_e64 v232, v232, v231\n" \
"v_mfma_f32_16x16x32_bf16 v[52:55], v[172:175], a[212:215], v[52:55]\n" \
"v_mfma_f32_16x16x32_bf16 v[84:87], v[168:171], a[200:203], v[84:87]\n" \
"ds_read_b64_tr_b16 v[144:145], v225 offset:49664\n" \
"v_mfma_f32_16x16x32_bf16 v[84:87], v[172:175], a[216:219], v[84:87]\n" \
"ds_read_b64_tr_b16 v[146:147], v225 offset:49920\n" \
"v_bfe_i32 v232, v232, 1, 1\n" \
"v_mfma_f32_16x16x32_bf16 v[116:119], v[168:171], a[204:207], v[116:119]\n" \
"s_mov_b32 s48, 49152\n" \
"s_mov_b32 m0, s43\n" \
"s_nop 0\n" \
"buffer_load_dwordx4 %[kd0], %[srdk], s48 offen offset:0 lds\n" \
"v_mfma_f32_16x16x32_bf16 v[116:119], v[172:175], a[220:223], v[116:119]\n" \
"ds_read_b64_tr_b16 v[148:149], v225 offset:57856\n" \
"v_mfma_f32_16x16x32_bf16 v[24:27], v[176:179], a[192:195], v[24:27]\n" \
"ds_read_b64_tr_b16 v[150:151], v225 offset:58112\n" \
"v_not_b32_e64 v232, v232\n" \
"v_mfma_f32_16x16x32_bf16 v[24:27], v[180:183], a[208:211], v[24:27]\n" \
"v_mfma_f32_16x16x32_bf16 v[56:59], v[176:179], a[196:199], v[56:59]\n" \
"ds_read_b64_tr_b16 v[152:153], v225 offset:49672\n" \
"v_mfma_f32_16x16x32_bf16 v[56:59], v[180:183], a[212:215], v[56:59]\n" \
"ds_read_b64_tr_b16 v[154:155], v225 offset:49928\n" \
"v_and_b32_e32 v230, 0x3f803f80, v232\n" \
"v_mfma_f32_16x16x32_bf16 v[88:91], v[176:179], a[200:203], v[88:91]\n" \
"buffer_load_dwordx4 v212, %[srdk], s48 offen offset:1024 lds\n" \
"v_mfma_f32_16x16x32_bf16 v[88:91], v[180:183], a[216:219], v[88:91]\n" \
"ds_read_b64_tr_b16 v[156:157], v225 offset:57864\n" \
"v_readlane_b32 s62, %[tlo], 18\n" \
"v_mfma_f32_16x16x32_bf16 v[120:123], v[176:179], a[204:207], v[120:123]\n" \
"ds_read_b64_tr_b16 v[158:159], v225 offset:58120\n" \
"v_mfma_f32_16x16x32_bf16 v[120:123], v[180:183], a[220:223], v[120:123]\n" \
"v_mfma_f32_16x16x32_bf16 v[28:31], v[184:187], a[192:195], v[28:31]\n" \
"buffer_load_dwordx4 v213, %[srdk], s48 offen offset:2048 lds\n" \
"v_readlane_b32 s61, %[tlo], 21\n" \
"v_mfma_f32_16x16x32_bf16 v[28:31], v[188:191], a[208:211], v[28:31]\n" \
"v_mfma_f32_16x16x32_bf16 v[60:63], v[184:187], a[196:199], v[60:63]\n" \
"v_mfma_f32_16x16x32_bf16 v[60:63], v[188:191], a[212:215], v[60:63]\n" \
"v_lshlrev_b32_e64 v224, 7, v231\n" \
"v_mfma_f32_16x16x32_bf16 v[92:95], v[184:187], a[200:203], v[92:95]\n" \
"buffer_load_dwordx4 v214, %[srdk], s48 offen offset:3072 lds\n" \
"v_mfma_f32_16x16x32_bf16 v[92:95], v[188:191], a[216:219], v[92:95]\n" \
"v_mfma_f32_16x16x32_bf16 v[124:127], v[184:187], a[204:207], v[124:127]\n" \
"v_mfma_f32_16x16x32_bf16 v[124:127], v[188:191], a[220:223], v[124:127]\n" \
"s_waitcnt vmcnt(24) lgkmcnt(0)\n" \
"v_mfma_f32_16x16x32_bf16 v[0:3], v[128:131], a[224:227], v[0:3]\n" \
"ds_read_b64_tr_b16 v[160:161], v225 offset:50176\n" \
"v_accvgpr_write_b32 a192, v230\n" \
"v_mfma_f32_16x16x32_bf16 v[0:3], v[132:135], a[240:243], v[0:3]\n" \
"ds_read_b64_tr_b16 v[162:163], v225 offset:50432\n" \
"v_mfma_f32_16x16x32_bf16 v[32:35], v[128:131], a[228:231], v[32:35]\n" \
"s_mov_b32 s48, 49152\n" \
"s_mov_b32 m0, s46\n" \
"s_nop 0\n" \
"buffer_load_dwordx4 %[vd0], %[srdv], s48 offen offset:2048 lds\n" \
"v_accvgpr_write_b32 a193, v230\n" \
"v_mfma_f32_16x16x32_bf16 v[32:35], v[132:135], a[244:247], v[32:35]\n" \
"ds_read_b64_tr_b16 v[164:165], v225 offset:58368\n" \
"v_mfma_f32_16x16x32_bf16 v[64:67], v[128:131], a[232:235], v[64:67]\n" \
"ds_read_b64_tr_b16 v[166:167], v225 offset:58624\n" \
"v_accvgpr_write_b32 a194, v230\n" \
"v_mfma_f32_16x16x32_bf16 v[64:67], v[132:135], a[248:251], v[64:67]\n" \
"v_mfma_f32_16x16x32_bf16 v[96:99], v[128:131], a[236:239], v[96:99]\n" \
"ds_read_b64_tr_b16 v[168:169], v225 offset:50184\n" \
"v_accvgpr_write_b32 a195, v230\n" \
"v_mfma_f32_16x16x32_bf16 v[96:99], v[132:135], a[252:255], v[96:99]\n" \
"ds_read_b64_tr_b16 v[170:171], v225 offset:50440\n" \
"v_mfma_f32_16x16x32_bf16 v[4:7], v[136:139], a[224:227], v[4:7]\n" \
"buffer_load_dwordx4 v215, %[srdv], s48 offen offset:3072 lds\n" \
"v_accvgpr_write_b32 a196, -1\n" \
"v_mfma_f32_16x16x32_bf16 v[4:7], v[140:143], a[240:243], v[4:7]\n" \
"ds_read_b64_tr_b16 v[172:173], v225 offset:58376\n" \
"v_mfma_f32_16x16x32_bf16 v[36:39], v[136:139], a[228:231], v[36:39]\n" \
"ds_read_b64_tr_b16 v[174:175], v225 offset:58632\n" \
"v_accvgpr_write_b32 a197, -1\n" \
"v_mfma_f32_16x16x32_bf16 v[36:39], v[140:143], a[244:247], v[36:39]\n" \
"v_mfma_f32_16x16x32_bf16 v[68:71], v[136:139], a[232:235], v[68:71]\n" \
"ds_read_b64_tr_b16 v[176:177], v225 offset:50688\n" \
"v_accvgpr_write_b32 a198, -1\n" \
"v_mfma_f32_16x16x32_bf16 v[68:71], v[140:143], a[248:251], v[68:71]\n" \
"ds_read_b64_tr_b16 v[178:179], v225 offset:50944\n" \
"v_mfma_f32_16x16x32_bf16 v[100:103], v[136:139], a[236:239], v[100:103]\n" \
"v_accvgpr_write_b32 a199, -1\n" \
"v_mfma_f32_16x16x32_bf16 v[100:103], v[140:143], a[252:255], v[100:103]\n" \
"ds_read_b64_tr_b16 v[180:181], v225 offset:58880\n" \
"v_mfma_f32_16x16x32_bf16 v[8:11], v[144:147], a[224:227], v[8:11]\n" \
"ds_read_b64_tr_b16 v[182:183], v225 offset:59136\n" \
"v_lshrrev_b32_e32 v231, 3, v224\n" \
"v_mfma_f32_16x16x32_bf16 v[8:11], v[148:151], a[240:243], v[8:11]\n" \
"v_mfma_f32_16x16x32_bf16 v[40:43], v[144:147], a[228:231], v[40:43]\n" \
"ds_read_b64_tr_b16 v[184:185], v225 offset:50696\n" \
"v_mov_b32_e64 v232, 0\n" \
"v_mfma_f32_16x16x32_bf16 v[40:43], v[148:151], a[244:247], v[40:43]\n" \
"ds_read_b64_tr_b16 v[186:187], v225 offset:50952\n" \
"v_mfma_f32_16x16x32_bf16 v[72:75], v[144:147], a[232:235], v[72:75]\n" \
"v_mov_b32_e64 v233, 0\n" \
"v_mfma_f32_16x16x32_bf16 v[72:75], v[148:151], a[248:251], v[72:75]\n" \
"ds_read_b64_tr_b16 v[188:189], v225 offset:58888\n" \
"v_mfma_f32_16x16x32_bf16 v[104:107], v[144:147], a[236:239], v[104:107]\n" \
"ds_read_b64_tr_b16 v[190:191], v225 offset:59144\n" \
"v_mov_b32_e64 v234, 0\n" \
"v_mfma_f32_16x16x32_bf16 v[104:107], v[148:151], a[252:255], v[104:107]\n" \
"v_mfma_f32_16x16x32_bf16 v[12:15], v[152:155], a[224:227], v[12:15]\n" \
"v_mov_b32_e64 v235, 0\n" \
"v_mfma_f32_16x16x32_bf16 v[12:15], v[156:159], a[240:243], v[12:15]\n" \
"v_mfma_f32_16x16x32_bf16 v[44:47], v[152:155], a[228:231], v[44:47]\n" \
"buffer_store_dwordx4 v[232:235], v231, %[srdz], s61 offen offset:0 sc1\n" \
"v_mfma_f32_16x16x32_bf16 v[44:47], v[156:159], a[244:247], v[44:47]\n" \
"v_mfma_f32_16x16x32_bf16 v[76:79], v[152:155], a[232:235], v[76:79]\n" \
"buffer_store_dwordx4 v[232:235], v231, %[srdz], s61 offen offset:1024 sc1\n" \
"v_mfma_f32_16x16x32_bf16 v[76:79], v[156:159], a[248:251], v[76:79]\n" \
"v_mfma_f32_16x16x32_bf16 v[108:111], v[152:155], a[236:239], v[108:111]\n" \
"v_mfma_f32_16x16x32_bf16 v[108:111], v[156:159], a[252:255], v[108:111]\n" \
"s_waitcnt vmcnt(22) lgkmcnt(0)\n" \
"s_barrier\n" \
"v_mfma_f32_16x16x32_bf16 v[16:19], v[160:163], a[224:227], v[16:19]\n" \
"ds_read_b128 a[64:67], %[kb0] offset:0\n" \
"v_mfma_f32_16x16x32_bf16 v[16:19], v[164:167], a[240:243], v[16:19]\n" \
"v_mfma_f32_16x16x32_bf16 v[20:23], v[168:171], a[224:227], v[20:23]\n" \
"v_cvt_pk_bf16_f32 v230, v0, v1\n" \
"v_cvt_pk_bf16_f32 v231, v2, v3\n" \
"v_cvt_pk_bf16_f32 v232, v4, v5\n" \
"ds_read_b128 a[68:71], v208 offset:0\n" \
"v_mfma_f32_16x16x32_bf16 v[20:23], v[172:175], a[240:243], v[20:23]\n" \
"v_cvt_pk_bf16_f32 v233, v6, v7\n" \
"v_accvgpr_write_b32 a0, v230\n" \
"v_accvgpr_write_b32 a1, v231\n" \
"v_mfma_f32_16x16x32_bf16 v[24:27], v[176:179], a[224:227], v[24:27]\n" \
"v_accvgpr_write_b32 a2, v232\n" \
"v_accvgpr_write_b32 a3, v233\n" \
"v_cvt_pk_bf16_f32 v234, v8, v9\n" \
"ds_read_b128 a[72:75], v209 offset:0\n" \
"v_mfma_f32_16x16x32_bf16 v[24:27], v[180:183], a[240:243], v[24:27]\n" \
"v_cvt_pk_bf16_f32 v235, v10, v11\n" \
"v_cvt_pk_bf16_f32 v236, v12, v13\n" \
"v_cvt_pk_bf16_f32 v237, v14, v15\n" \
"v_mfma_f32_16x16x32_bf16 v[28:31], v[184:187], a[224:227], v[28:31]\n" \
"v_accvgpr_write_b32 a4, v234\n" \
"v_accvgpr_write_b32 a5, v235\n" \
"v_accvgpr_write_b32 a6, v236\n" \
".if . - adaln_body != 8192\n" \
".error \"size model\"\n" \
".endif\n" \
"Lk1_%=:\n" \
"s_cbranch_execz Lk2_%=\n" \
"s_nop 0\n" \
"ds_read_b128 a[76:79], v210 offset:0\n" \
"v_mfma_f32_16x16x32_bf16 v[28:31], v[188:191], a[240:243], v[28:31]\n" \
"v_accvgpr_write_b32 a7, v237\n" \
"v_cvt_pk_bf16_f32 v230, v32, v33\n" \
"v_cvt_pk_bf16_f32 v231, v34, v35\n" \
"v_mfma_f32_16x16x32_bf16 v[48:51], v[160:163], a[228:231], v[48:51]\n" \
"v_cvt_pk_bf16_f32 v232, v36, v37\n" \
"v_cvt_pk_bf16_f32 v233, v38, v39\n" \
"v_accvgpr_write_b32 a16, v230\n" \
"ds_read_b128 a[80:83], %[kb0] offset:4096\n" \
"v_mfma_f32_16x16x32_bf16 v[48:51], v[164:167], a[244:247], v[48:51]\n" \
"v_accvgpr_write_b32 a17, v231\n" \
"v_accvgpr_write_b32 a18, v232\n" \
"v_accvgpr_write_b32 a19, v233\n" \
"v_mfma_f32_16x16x32_bf16 v[52:55], v[168:171], a[228:231], v[52:55]\n" \
"v_cvt_pk_bf16_f32 v234, v40, v41\n" \
"v_cvt_pk_bf16_f32 v235, v42, v43\n" \
"v_cvt_pk_bf16_f32 v236, v44, v45\n" \
"ds_read_b128 a[84:87], v208 offset:4096\n" \
"v_mfma_f32_16x16x32_bf16 v[52:55], v[172:175], a[244:247], v[52:55]\n" \
"v_cvt_pk_bf16_f32 v237, v46, v47\n" \
"v_accvgpr_write_b32 a20, v234\n" \
"v_accvgpr_write_b32 a21, v235\n" \
"v_mfma_f32_16x16x32_bf16 v[56:59], v[176:179], a[228:231], v[56:59]\n" \
"v_accvgpr_write_b32 a22, v236\n" \
"v_accvgpr_write_b32 a23, v237\n" \
"v_cvt_pk_bf16_f32 v230, v16, v17\n" \
"ds_read_b128 a[88:91], v209 offset:4096\n" \
"v_mfma_f32_16x16x32_bf16 v[56:59], v[180:183], a[244:247], v[56:59]\n" \
"v_cvt_pk_bf16_f32 v231, v18, v19\n" \
"v_cvt_pk_bf16_f32 v232, v20, v21\n" \
"v_cvt_pk_bf16_f32 v233, v22, v23\n" \
"v_mfma_f32_16x16x32_bf16 v[60:63], v[184:187], a[228:231], v[60:63]\n" \
"v_accvgpr_write_b32 a8, v230\n" \
"v_accvgpr_write_b32 a9, v231\n" \
"v_accvgpr_write_b32 a10, v232\n" \
"ds_read_b128 a[92:95], v210 offset:4096\n" \
"v_mfma_f32_16x16x32_bf16 v[60:63], v[188:191], a[244:247], v[60:63]\n" \
"v_accvgpr_write_b32 a11, v233\n" \
"v_cvt_pk_bf16_f32 v234, v24, v25\n" \
"v_cvt_pk_bf16_f32 v235, v26, v27\n" \
"v_mfma_f32_16x16x32_bf16 v[80:83], v[160:163], a[232:235], v[80:83]\n" \
"v_cvt_pk_bf16_f32 v236, v28, v29\n" \
"v_cvt_pk_bf16_f32 v237, v30, v31\n" \
"v_accvgpr_write_b32 a12, v234\n" \
"v_mfma_f32_16x16x32_bf16 v[80:83], v[164:167], a[248:251], v[80:83]\n" \
"v_accvgpr_write_b32 a13, v235\n" \
"v_accvgpr_write_b32 a14, v236\n" \
"v_accvgpr_write_b32 a15, v237\n" \
"v_mfma_f32_16x16x32_bf16 v[84:87], v[168:171], a[232:235], v[84:87]\n" \
"v_cvt_pk_bf16_f32 v230, v64, v65\n" \
"v_cvt_pk_bf16_f32 v231, v66, v67\n" \
"v_cvt_pk_bf16_f32 v232, v68, v69\n" \
"v_mfma_f32_16x16x32_bf16 v[84:87], v[172:175], a[248:251], v[84:87]\n" \
"v_cvt_pk_bf16_f32 v233, v70, v71\n" \
"v_accvgpr_write_b32 a32, v230\n" \
"v_accvgpr_write_b32 a33, v231\n" \
"v_mfma_f32_16x16x32_bf16 v[88:91], v[176:179], a[232:235], v[88:91]\n" \
"v_accvgpr_write_b32 a34, v232\n" \
"v_accvgpr_write_b32 a35, v233\n" \
"v_cvt_pk_bf16_f32 v234, v72, v73\n" \
"v_mfma_f32_16x16x32_bf16 v[88:91], v[180:183], a[248:251], v[88:91]\n" \
"v_cvt_pk_bf16_f32 v235, v74, v75\n" \
"v_cvt_pk_bf16_f32 v236, v76, v77\n" \
"v_cvt_pk_bf16_f32 v237, v78, v79\n" \
"v_mfma_f32_16x16x32_bf16 v[92:95], v[184:187], a[232:235], v[92:95]\n" \
"v_accvgpr_write_b32 a36, v234\n" \
"v_accvgpr_write_b32 a37, v235\n" \
"v_accvgpr_write_b32 a38, v236\n" \
"v_mfma_f32_16x16x32_bf16 v[92:95], v[188:191], a[248:251], v[92:95]\n" \
"v_accvgpr_write_b32 a39, v237\n" \
"v_cvt_pk_bf16_f32 v230, v48, v49\n" \
"v_cvt_pk_bf16_f32 v231, v50, v51\n" \
"v_mfma_f32_16x16x32_bf16 v[112:115], v[160:163], a[236:239], v[112:115]\n" \
"v_cvt_pk_bf16_f32 v232, v52, v53\n" \
"v_cvt_pk_bf16_f32 v233, v54, v55\n" \
"v_accvgpr_write_b32 a24, v230\n" \
"v_mfma_f32_16x16x32_bf16 v[112:115], v[164:167], a[252:255], v[112:115]\n" \
"v_accvgpr_write_b32 a25, v231\n" \
"v_accvgpr_write_b32 a26, v232\n" \
"v_accvgpr_write_b32 a27, v233\n" \
"v_mfma_f32_16x16x32_bf16 v[116:119], v[168:171], a[236:239], v[116:119]\n" \
"v_cvt_pk_bf16_f32 v234, v56, v57\n" \
"v_cvt_pk_bf16_f32 v235, v58, v59\n" \
"v_cvt_pk_bf16_f32 v236, v60, v61\n" \
"v_mfma_f32_16x16x32_bf16 v[116:119], v[172:175], a[252:255], v[116:119]\n" \
"v_cvt_pk_bf16_f32 v237, v62, v63\n" \
"v_accvgpr_write_b32 a28, v234\n" \
"v_accvgpr_write_b32 a29, v235\n" \
"v_mfma_f32_16x16x32_bf16 v[120:123], v[176:179], a[236:239], v[120:123]\n" \
"v_accvgpr_write_b32 a30, v236\n" \
"v_accvgpr_write_b32 a31, v237\n" \
"v_cvt_pk_bf16_f32 v230, v96, v97\n" \
"v_mfma_f32_16x16x32_bf16 v[120:123], v[180:183], a[252:255], v[120:123]\n" \
"v_cvt_pk_bf16_f32 v231, v98, v99\n" \
"v_cvt_pk_bf16_f32 v232, v100, v101\n" \
"v_cvt_pk_bf16_f32 v233, v102, v103\n" \
"v_mfma_f32_16x16x32_bf16 v[124:127], v[184:187], a[236:239], v[124:127]\n" \
"v_accvgpr_write_b32 a48, v230\n" \
"v_accvgpr_write_b32 a49, v231\n" \
"v_accvgpr_write_b32 a50, v232\n" \
"v_mfma_f32_16x16x32_bf16 v[124:127], v[188:191], a[252:255], v[124:127]\n" \
"v_accvgpr_write_b32 a51, v233\n" \
"s_waitcnt vmcnt(63) lgkmcnt(0)\n" \
"s_nop 0\n" \
"s_mov_b32 s48, 65536\n" \
"s_mov_b32 s60, 0\n" \
"s_waitcnt lgkmcnt(0)\n" \
"v_mfma_f32_16x16x32_bf16 v[128:131], a[64:67], a[0:3], 0\n" \
"ds_read_b128 a[96:99], %[kb0] offset:8192\n" \
"v_cvt_pk_bf16_f32 v234, v80, v81\n" \
"v_cvt_pk_bf16_f32 v235, v82, v83\n" \
"v_mfma_f32_16x16x32_bf16 v[128:131], a[68:71], a[4:7], v[128:131]\n" \
"ds_read_b128 a[100:103], v208 offset:8192\n" \
"v_cvt_pk_bf16_f32 v236, v84, v85\n" \
"v_cvt_pk_bf16_f32 v237, v86, v87\n" \
"v_mfma_f32_16x16x32_bf16 v[128:131], a[72:75], a[8:11], v[128:131]\n" \
"ds_read_b128 a[104:107], v209 offset:8192\n" \
"v_accvgpr_write_b32 a40, v234\n" \
"v_accvgpr_write_b32 a41, v235\n" \
"v_mfma_f32_16x16x32_bf16 v[128:131], a[76:79], a[12:15], v[128:131]\n" \
"ds_read_b128 a[108:111], v210 offset:8192\n" \
"v_accvgpr_write_b32 a42, v236\n" \
"v_accvgpr_write_b32 a43, v237\n" \
"v_mfma_f32_16x16x32_bf16 v[132:135], a[80:83], a[0:3], 0\n" \
"ds_read_b128 a[112:115], %[kb0] offset:12288\n" \
"v_cvt_pk_bf16_f32 v230, v88, v89\n" \
"v_cvt_pk_bf16_f32 v231, v90, v91\n" \
"v_mfma_f32_16x16x32_bf16 v[132:135], a[84:87], a[4:7], v[132:135]\n" \
"ds_read_b128 a[116:119], v208 offset:12288\n" \
"v_cvt_pk_bf16_f32 v232, v92, v93\n" \
"v_cvt_pk_bf16_f32 v233, v94, v95\n" \
"v_mfma_f32_16x16x32_bf16 v[132:135], a[88:91], a[8:11], v[132:135]\n" \
"ds_read_b128 a[120:123], v209 offset:12288\n" \
"v_accvgpr_write_b32 a44, v230\n" \
"v_accvgpr_write_b32 a45, v231\n" \
"v_mfma_f32_16x16x32_bf16 v[132:135], a[92:95], a[12:15], v[132:135]\n" \
"ds_read_b128 a[124:127], v210 offset:12288\n" \
"v_accvgpr_write_b32 a46, v232\n" \
"v_accvgpr_write_b32 a47, v233\n" \
"v_mfma_f32_16x16x32_bf16 v[136:139], a[64:67], a[16:19], 0\n" \
"ds_read_b64_tr_b16 a[128:129], %[vvb] offset:0\n" \
"v_cvt_pk_bf16_f32 v234, v104, v105\n" \
"v_cvt_pk_bf16_f32 v235, v106, v107\n" \
"v_mfma_f32_16x16x32_bf16 v[136:139], a[68:71], a[20:23], v[136:139]\n" \
"ds_read_b64_tr_b16 a[130:131], %[vvb] offset:4096\n" \
"v_cvt_pk_bf16_f32 v236, v108, v109\n" \
"v_cvt_pk_bf16_f32 v237, v110, v111\n" \
"v_mfma_f32_16x16x32_bf16 v[136:139], a[72:75], a[24:27], v[136:139]\n" \
"ds_read_b64_tr_b16 a[132:133], v211 offset:0\n" \
"v_accvgpr_write_b32 a52, v234\n" \
"v_accvgpr_write_b32 a53, v235\n" \
"v_mfma_f32_16x16x32_bf16 v[136:139], a[76:79], a[28:31], v[136:139]\n" \
"ds_read_b64_tr_b16 a[134:135], v211 offset:4096\n" \
"v_accvgpr_write_b32 a54, v236\n" \
"v_accvgpr_write_b32 a55, v237\n" \
"v_mfma_f32_16x16x32_bf16 v[140:143], a[80:83], a[16:19], 0\n" \
"ds_read_b64_tr_b16 a[136:137], %[vvb] offset:512\n" \
"v_cvt_pk_bf16_f32 v230, v112, v113\n" \
"v_cvt_pk_bf16_f32 v231, v114, v115\n" \
"v_mfma_f32_16x16x32_bf16 v[140:143], a[84:87], a[20:23], v[140:143]\n" \
"ds_read_b64_tr_b16 a[138:139], %[vvb] offset:4608\n" \
"v_cvt_pk_bf16_f32 v232, v116, v117\n" \
"v_cvt_pk_bf16_f32 v233, v118, v119\n" \
"v_mfma_f32_16x16x32_bf16 v[140:143], a[88:91], a[24:27], v[140:143]\n" \
"ds_read_b64_tr_b16 a[140:141], v211 offset:512\n" \
"v_accvgpr_write_b32 a56, v230\n" \
"v_accvgpr_write_b32 a57, v231\n" \
"v_mfma_f32_16x16x32_bf16 v[140:143], a[92:95], a[28:31], v[140:143]\n" \
"ds_read_b64_tr_b16 a[142:143], v211 offset:4608\n" \
"v_accvgpr_write_b32 a58, v232\n" \
"v_accvgpr_write_b32 a59, v233\n" \
"v_mfma_f32_16x16x32_bf16 v[144:147], a[64:67], a[32:35], 0\n" \
"ds_read_b64_tr_b16 a[144:145], %[vvb] offset:1024\n" \
"v_cvt_pk_bf16_f32 v234, v120, v121\n" \
"v_cvt_pk_bf16_f32 v235, v122, v123\n" \
"v_mfma_f32_16x16x32_bf16 v[144:147], a[68:71], a[36:39], v[144:147]\n" \
"ds_read_b64_tr_b16 a[146:147], %[vvb] offset:5120\n" \
"v_cvt_pk_bf16_f32 v236, v124, v125\n" \
"v_cvt_pk_bf16_f32 v237, v126, v127\n" \
"v_mfma_f32_16x16x32_bf16 v[144:147], a[72:75], a[40:43], v[144:147]\n" \
"ds_read_b64_tr_b16 a[148:149], v211 offset:1024\n" \
"v_accvgpr_write_b32 a60, v234\n" \
"v_accvgpr_write_b32 a61, v235\n" \
"v_mfma_f32_16x16x32_bf16 v[144:147], a[76:79], a[44:47], v[144:147]\n" \
"ds_read_b64_tr_b16 a[150:151], v211 offset:5120\n" \
"v_accvgpr_write_b32 a62, v236\n" \
"v_accvgpr_write_b32 a63, v237\n" \
"v_mfma_f32_16x16x32_bf16 v[148:151], a[80:83], a[32:35], 0\n" \
"ds_read_b64_tr_b16 a[152:153], %[vvb] offset:1536\n" \
"v_max3_f32 v192, v128, v129, v130\n" \
"v_max3_f32 v193, v131, v132, v133\n" \
"v_mfma_f32_16x16x32_bf16 v[148:151], a[84:87], a[36:39], v[148:151]\n" \
"ds_read_b64_tr_b16 a[154:155], %[vvb] offset:5632\n" \
"v_max3_f32 v192, v192, v134, v135\n" \
"v_max_f32_e64 v225, v192, v193\n" \
"v_mfma_f32_16x16x32_bf16 v[148:151], a[88:91], a[40:43], v[148:151]\n" \
"ds_read_b64_tr_b16 a[156:157], v211 offset:1536\n" \
"v_max3_f32 v192, v136, v137, v138\n" \
"v_max3_f32 v193, v139, v140, v141\n" \
"v_mfma_f32_16x16x32_bf16 v[148:151], a[92:95], a[44:47], v[148:151]\n" \
"ds_read_b64_tr_b16 a[158:159], v211 offset:5632\n" \
"v_max3_f32 v192, v192, v142, v143\n" \
"v_max_f32_e64 v226, v192, v193\n" \
"v_mfma_f32_16x16x32_bf16 v[152:155], a[64:67], a[48:51], 0\n" \
"ds_read_b128 a[200:203], v228 offset:0\n" \
"s_mov_b32 s44, %[m0t]\n" \
"s_mov_b32 s45, 0\n" \
"v_mfma_f32_16x16x32_bf16 v[152:155], a[68:71], a[52:55], v[152:155]\n" \
"ds_read_b128 a[204:207], v228 offset:64\n" \
"v_mfma_f32_16x16x32_bf16 v[152:155], a[72:75], a[56:59], v[152:155]\n" \
"ds_read_b128 a[208:211], v228 offset:128\n" \
"v_mfma_f32_16x16x32_bf16 v[152:155], a[76:79], a[60:63], v[152:155]\n" \
"ds_read_b128 a[212:215], v228 offset:192\n" \
"v_mfma_f32_16x16x32_bf16 v[156:159], a[80:83], a[48:51], 0\n" \
"ds_read_b128 a[216:219], v228 offset:256\n" \
"v_mfma_f32_16x16x32_bf16 v[156:159], a[84:87], a[52:55], v[156:159]\n" \
"ds_read_b128 a[220:223], v228 offset:320\n" \
"v_mfma_f32_16x16x32_bf16 v[156:159], a[88:91], a[56:59], v[156:159]\n" \
"ds_read_b128 a[224:227], v228 offset:384\n" \
"v_max3_f32 v192, v144, v145, v146\n" \
"v_max3_f32 v193, v147, v148, v149\n" \
"v_mfma_f32_16x16x32_bf16 v[156:159], a[92:95], a[60:63], v[156:159]\n" \
"ds_read_b128 a[228:231], v228 offset:448\n" \
"v_max3_f32 v192, v192, v150, v151\n" \
"v_max_f32_e64 v227, v192, v193\n" \
"s_nop 3\n" \
"s_nop 0\n" \
"v_max3_f32 v230, v152, v153, v154\n" \
"v_max3_f32 v231, v155, v156, v157\n" \
"v_max3_f32 v230, v230, v158, v159\n" \
"v_max_f32_e32 v228, v230, v231\n" \
"v_mov_b32_e32 v232, v225\n" \
"v_mov_b32_e32 v233, v226\n" \
"v_mov_b32_e32 v234, v227\n" \
"v_mov_b32_e32 v235, v228\n" \
"s_nop 0\n" \
"v_permlane16_swap_b32_e32 v225, v232\n" \
"v_permlane16_swap_b32_e32 v226, v233\n" \
"v_permlane16_swap_b32_e32 v227, v234\n" \
"v_permlane16_swap_b32_e32 v228, v235\n" \
"v_max_f32_e32 v225, v225, v232\n" \
"v_max_f32_e32 v226, v226, v233\n" \
"v_max_f32_e32 v227, v227, v234\n" \
"v_max_f32_e32 v228, v228, v235\n" \
"v_mov_b32_e32 v232, v225\n" \
"v_mov_b32_e32 v233, v226\n" \
"v_mov_b32_e32 v234, v227\n" \
"v_mov_b32_e32 v235, v228\n" \
"s_nop 0\n" \
"v_permlane32_swap_b32_e32 v225, v232\n" \
"v_permlane32_swap_b32_e32 v226, v233\n" \
"v_permlane32_swap_b32_e32 v227, v234\n" \
"v_permlane32_swap_b32_e32 v228, v235\n" \
"v_max_f32_e32 v225, v225, v232\n" \
"v_max_f32_e32 v226, v226, v233\n" \
"v_max_f32_e32 v227, v227, v234\n" \
"v_max_f32_e64 v228, v228, v235\n" \
"Lhavem_%=:\n" \
"s_nop 3\n" \
"v_mov_b64_e32 v[176:177], 0\n" \
"v_mov_b64_e32 v[178:179], 0\n" \
"v_mov_b64_e32 v[180:181], 0\n" \
"v_mov_b64_e32 v[182:183], 0\n" \
"v_sub_f32_e32 v160, 0, v225\n" \
"v_sub_f32_e32 v161, 0, v225\n" \
"v_sub_f32_e32 v162, 0, v225\n" \
"v_sub_f32_e32 v163, 0, v225\n" \
"v_sub_f32_e32 v164, 0, v226\n" \
"v_sub_f32_e32 v165, 0, v226\n" \
"v_sub_f32_e32 v166, 0, v226\n" \
"v_sub_f32_e32 v167, 0, v226\n" \
"v_sub_f32_e32 v168, 0, v227\n" \
"v_sub_f32_e32 v169, 0, v227\n" \
"v_sub_f32_e32 v170, 0, v227\n" \
"v_sub_f32_e32 v171, 0, v227\n" \
"v_sub_f32_e32 v172, 0, v228\n" \
"v_sub_f32_e32 v173, 0, v228\n" \
"v_sub_f32_e32 v174, 0, v228\n" \
"v_sub_f32_e32 v175, 0, v228\n" \
"v_sub_f32_e32 v128, v128, v225\n" \
"v_sub_f32_e32 v129, v129, v225\n" \
"v_sub_f32_e32 v130, v130, v225\n" \
"v_sub_f32_e32 v131, v131, v225\n" \
"v_sub_f32_e32 v132, v132, v225\n" \
"v_sub_f32_e32 v133, v133, v225\n" \
"v_sub_f32_e32 v134, v134, v225\n" \
"v_sub_f32_e32 v135, v135, v225\n" \
"v_sub_f32_e32 v136, v136, v226\n" \
"v_sub_f32_e32 v137, v137, v226\n" \
"v_sub_f32_e32 v138, v138, v226\n" \
"v_sub_f32_e32 v139, v139, v226\n" \
"v_sub_f32_e32 v140, v140, v226\n" \
"v_sub_f32_e32 v141, v141, v226\n" \
"v_sub_f32_e32 v142, v142, v226\n" \
"v_sub_f32_e32 v143, v143, v226\n" \
"v_sub_f32_e32 v144, v144, v227\n" \
"v_sub_f32_e32 v145, v145, v227\n" \
"v_sub_f32_e32 v146, v146, v227\n" \
"v_sub_f32_e32 v147, v147, v227\n" \
"v_sub_f32_e32 v148, v148, v227\n" \
"v_sub_f32_e32 v149, v149, v227\n" \
"v_sub_f32_e32 v150, v150, v227\n" \
"v_sub_f32_e32 v151, v151, v227\n" \
"v_sub_f32_e32 v152, v152, v228\n" \
"v_sub_f32_e32 v153, v153, v228\n" \
"v_sub_f32_e32 v154, v154, v228\n" \
"v_sub_f32_e32 v155, v155, v228\n" \
"v_sub_f32_e32 v156, v156, v228\n" \
"v_sub_f32_e32 v157, v157, v228\n" \
"v_sub_f32_e32 v158, v158, v228\n" \
"v_sub_f32_e32 v159, v159, v228\n" \
"v_exp_f32_e32 v128, v128\n" \
"v_exp_f32_e32 v129, v129\n" \
"v_exp_f32_e32 v130, v130\n" \
"v_exp_f32_e32 v131, v131\n" \
"v_exp_f32_e32 v132, v132\n" \
"v_exp_f32_e32 v133, v133\n" \
"v_exp_f32_e32 v134, v134\n" \
"v_exp_f32_e32 v135, v135\n" \
"v_exp_f32_e32 v136, v136\n" \
"v_exp_f32_e32 v137, v137\n" \
"v_exp_f32_e32 v138, v138\n" \
"v_exp_f32_e32 v139, v139\n" \
"v_exp_f32_e32 v140, v140\n" \
"v_exp_f32_e32 v141, v141\n" \
"v_exp_f32_e32 v142, v142\n" \
"v_exp_f32_e32 v143, v143\n" \
"v_exp_f32_e32 v144, v144\n" \
"v_exp_f32_e32 v145, v145\n" \
"v_exp_f32_e32 v146, v146\n" \
"v_exp_f32_e32 v147, v147\n" \
"v_exp_f32_e32 v148, v148\n" \
"v_exp_f32_e32 v149, v149\n" \
"v_exp_f32_e32 v150, v150\n" \
"v_exp_f32_e32 v151, v151\n" \
"v_exp_f32_e32 v152, v152\n" \
"v_exp_f32_e32 v153, v153\n" \
"v_exp_f32_e32 v154, v154\n" \
"v_exp_f32_e64 v155, v155\n" \
"v_bfe_i32 v228, v224, 7, 1\n" \
"v_and_b32_e32 v228, 0xaaaa, v228\n" \
"v_or_b32_e32 v228, 0x4444, v228\n" \
"v_cvt_pk_bf16_f32 v184, v128, v129\n" \
"v_cvt_pk_bf16_f32 v185, v130, v131\n" \
"v_cvt_pk_bf16_f32 v186, v132, v133\n" \
"v_cvt_pk_bf16_f32 v187, v134, v135\n" \
"v_cvt_pk_bf16_f32 v188, v136, v137\n" \
"v_cvt_pk_bf16_f32 v189, v138, v139\n" \
"v_cvt_pk_bf16_f32 v190, v140, v141\n" \
"v_cvt_pk_bf16_f32 v191, v142, v143\n" \
"v_cvt_pk_bf16_f32 v216, v144, v145\n" \
"v_cvt_pk_bf16_f32 v217, v146, v147\n" \
"v_cvt_pk_bf16_f32 v218, v148, v149\n" \
"v_cvt_pk_bf16_f32 v219, v150, v151\n" \
"v_lshrrev_b32_e64 v235, 3, v224\n" \
"s_waitcnt vmcnt(2) lgkmcnt(0)\n" \
"s_barrier\n" \
"v_mfma_f32_16x16x32_bf16 v[128:131], a[96:99], a[0:3], v[160:163]\n" \
"v_exp_f32_e64 v156, v156\n" \
"v_mfma_f32_16x16x32_bf16 v[128:131], a[100:103], a[4:7], v[128:131]\n" \
"s_add_u32 m0, %[m0b], 114688\n" \
"v_mfma_f32_16x16x32_bf16 v[128:131], a[104:107], a[8:11], v[128:131]\n" \
"v_exp_f32_e64 v157, v157\n" \
"v_mfma_f32_16x16x32_bf16 v[128:131], a[108:111], a[12:15], v[128:131]\n" \
"v_cvt_pk_bf16_f32 v220, v152, v153\n" \
"v_cvt_pk_bf16_f32 v221, v154, v155\n" \
"v_mfma_f32_16x16x32_bf16 v[132:135], a[112:115], a[0:3], v[160:163]\n" \
"v_exp_f32_e64 v158, v158\n" \
"v_mfma_f32_16x16x32_bf16 v[132:135], a[116:119], a[4:7], v[132:135]\n" \
"v_mfma_f32_16x16x32_bf16 v[132:135], a[120:123], a[8:11], v[132:135]\n" \
"buffer_load_dwordx4 %[vd0], %[srdv], s48 offen offset:0 lds\n" \
"v_mfma_f32_16x16x32_bf16 v[132:135], a[124:127], a[12:15], v[132:135]\n" \
"v_exp_f32_e64 v159, v159\n" \
"v_mfma_f32_16x16x32_bf16 v[0:3], a[128:131], v[184:187], 0\n" \
"ds_read_b128 a[64:67], %[kb0] offset:16384\n" \
"v_mfma_f32_16x16x32_bf16 v[32:35], a[128:131], v[188:191], 0\n" \
"v_exp_f32_e64 v128, v128\n" \
"v_mfma_f32_16x16x32_bf16 v[4:7], a[132:135], v[184:187], 0\n" \
"v_cvt_pk_bf16_f32 v222, v156, v157\n" \
"v_cvt_pk_bf16_f32 v223, v158, v159\n" \
"v_mfma_f32_16x16x32_bf16 v[36:39], a[132:135], v[188:191], 0\n" \
"v_exp_f32_e64 v129, v129\n" \
"v_mfma_f32_16x16x32_bf16 v[8:11], a[136:139], v[184:187], 0\n" \
"ds_read_b128 a[68:71], v208 offset:16384\n" \
"v_mfma_f32_16x16x32_bf16 v[40:43], a[136:139], v[188:191], 0\n" \
"v_exp_f32_e64 v130, v130\n" \
"v_mfma_f32_16x16x32_bf16 v[12:15], a[140:143], v[184:187], 0\n" \
"ds_read_b128 a[72:75], v209 offset:16384\n" \
"v_mfma_f32_16x16x32_bf16 v[44:47], a[140:143], v[188:191], 0\n" \
"v_exp_f32_e64 v131, v131\n" \
"v_smfmac_f32_16x16x64_bf16 v[176:179], a[192:195], v[184:191], v228\n" \
"v_mfma_f32_16x16x32_bf16 v[136:139], a[96:99], a[16:19], v[164:167]\n" \
"v_exp_f32_e64 v132, v132\n" \
"v_mfma_f32_16x16x32_bf16 v[136:139], a[100:103], a[20:23], v[136:139]\n" \
"ds_read_b128 a[76:79], v210 offset:16384\n" \
"v_mfma_f32_16x16x32_bf16 v[136:139], a[104:107], a[24:27], v[136:139]\n" \
"v_exp_f32_e64 v133, v133\n" \
"v_mfma_f32_16x16x32_bf16 v[136:139], a[108:111], a[28:31], v[136:139]\n" \
"v_cvt_pk_bf16_f32 v192, v128, v129\n" \
"v_cvt_pk_bf16_f32 v193, v130, v131\n" \
"v_mfma_f32_16x16x32_bf16 v[140:143], a[112:115], a[16:19], v[164:167]\n" \
"v_exp_f32_e64 v134, v134\n" \
"v_mfma_f32_16x16x32_bf16 v[140:143], a[116:119], a[20:23], v[140:143]\n" \
"ds_read_b128 a[80:83], %[kb0] offset:20480\n" \
"v_mfma_f32_16x16x32_bf16 v[140:143], a[120:123], a[24:27], v[140:143]\n" \
"buffer_load_dwordx4 v215, %[srdv], s48 offen offset:1024 lds\n" \
"v_mfma_f32_16x16x32_bf16 v[140:143], a[124:127], a[28:31], v[140:143]\n" \
"v_exp_f32_e64 v135, v135\n" \
"v_mfma_f32_16x16x32_bf16 v[16:19], a[144:147], v[184:187], 0\n" \
"ds_read_b128 a[84:87], v208 offset:20480\n" \
"v_mfma_f32_16x16x32_bf16 v[48:51], a[144:147], v[188:191], 0\n" \
"v_exp_f32_e64 v136, v136\n" \
"v_mfma_f32_16x16x32_bf16 v[20:23], a[148:151], v[184:187], 0\n" \
"v_cvt_pk_bf16_f32 v194, v132, v133\n" \
"v_cvt_pk_bf16_f32 v195, v134, v135\n" \
"v_mfma_f32_16x16x32_bf16 v[52:55], a[148:151], v[188:191], 0\n" \
"v_exp_f32_e64 v137, v137\n" \
"v_mfma_f32_16x16x32_bf16 v[24:27], a[152:155], v[184:187], 0\n" \
"v_exp_f32_e64 v138, v138\n" \
"v_mfma_f32_16x16x32_bf16 v[56:59], a[152:155], v[188:191], 0\n" \
"ds_read_b128 a[88:91], v209 offset:20480\n" \
"v_mfma_f32_16x16x32_bf16 v[28:31], a[156:159], v[184:187], 0\n" \
"v_exp_f32_e64 v139, v139\n" \
"v_mfma_f32_16x16x32_bf16 v[60:63], a[156:159], v[188:191], 0\n" \
"v_mfma_f32_16x16x32_bf16 v[144:147], a[96:99], a[32:35], v[168:171]\n" \
"v_exp_f32_e64 v140, v140\n" \
"v_mfma_f32_16x16x32_bf16 v[144:147], a[100:103], a[36:39], v[144:147]\n" \
"ds_read_b128 a[92:95], v210 offset:20480\n" \
"v_mfma_f32_16x16x32_bf16 v[144:147], a[104:107], a[40:43], v[144:147]\n" \
"v_exp_f32_e64 v141, v141\n" \
"v_mfma_f32_16x16x32_bf16 v[144:147], a[108:111], a[44:47], v[144:147]\n" \
"v_cvt_pk_bf16_f32 v196, v136, v137\n" \
"v_cvt_pk_bf16_f32 v197, v138, v139\n" \
"v_mfma_f32_16x16x32_bf16 v[148:151], a[112:115], a[32:35], v[168:171]\n" \
"v_exp_f32_e64 v142, v142\n" \
"v_mfma_f32_16x16x32_bf16 v[148:151], a[116:119], a[36:39], v[148:151]\n" \
"v_mfma_f32_16x16x32_bf16 v[148:151], a[120:123], a[40:43], v[148:151]\n" \
"buffer_load_dwordx4 %[vd0], %[srdv], s48 offen offset:2048 lds\n" \
"v_mfma_f32_16x16x32_bf16 v[148:151], a[124:127], a[44:47], v[148:151]\n" \
"v_exp_f32_e64 v143, v143\n" \
"v_mfma_f32_16x16x32_bf16 v[64:67], a[128:131], v[216:219], 0\n" \
"ds_read_b64_tr_b16 a[160:161], %[vvb] offset:8192\n" \
"v_mfma_f32_16x16x32_bf16 v[96:99], a[128:131], v[220:223], 0\n" \
"v_exp_f32_e64 v144, v144\n" \
"v_mfma_f32_16x16x32_bf16 v[68:71], a[132:135], v[216:219], 0\n" \
"v_cvt_pk_bf16_f32 v198, v140, v141\n" \
"v_cvt_pk_bf16_f32 v199, v142, v143\n" \
"v_mfma_f32_16x16x32_bf16 v[100:103], a[132:135], v[220:223], 0\n" \
"v_exp_f32_e64 v145, v145\n" \
"v_mfma_f32_16x16x32_bf16 v[72:75], a[136:139], v[216:219], 0\n" \
"ds_read_b64_tr_b16 a[162:163], %[vvb] offset:12288\n" \
"ds_read_b64_tr_b16 a[164:165], v211 offset:8192\n" \
"v_mfma_f32_16x16x32_bf16 v[104:107], a[136:139], v[220:223], 0\n" \
"v_exp_f32_e64 v146, v146\n" \
"v_mfma_f32_16x16x32_bf16 v[76:79], a[140:143], v[216:219], 0\n" \
"ds_read_b64_tr_b16 a[166:167], v211 offset:12288\n" \
"v_mfma_f32_16x16x32_bf16 v[108:111], a[140:143], v[220:223], 0\n" \
"v_exp_f32_e64 v147, v147\n" \
"v_smfmac_f32_16x16x64_bf16 v[180:183], a[192:195], v[216:223], v228\n" \
"ds_read_b64_tr_b16 a[168:169], %[vvb] offset:8704\n" \
"v_mfma_f32_16x16x32_bf16 v[152:155], a[96:99], a[48:51], v[172:175]\n" \
"v_exp_f32_e64 v148, v148\n" \
"v_mfma_f32_16x16x32_bf16 v[152:155], a[100:103], a[52:55], v[152:155]\n" \
"ds_read_b64_tr_b16 a[170:171], %[vvb] offset:12800\n" \
"v_mfma_f32_16x16x32_bf16 v[152:155], a[104:107], a[56:59], v[152:155]\n" \
"v_exp_f32_e64 v149, v149\n" \
"v_mfma_f32_16x16x32_bf16 v[152:155], a[108:111], a[60:63], v[152:155]\n" \
"v_cvt_pk_bf16_f32 v200, v144, v145\n" \
"v_cvt_pk_bf16_f32 v201, v146, v147\n" \
"v_mfma_f32_16x16x32_bf16 v[156:159], a[112:115], a[48:51], v[172:175]\n" \
"v_exp_f32_e64 v150, v150\n" \
"v_mfma_f32_16x16x32_bf16 v[156:159], a[116:119], a[52:55], v[156:159]\n" \
"ds_read_b64_tr_b16 a[172:173], v211 offset:8704\n" \
"v_mfma_f32_16x16x32_bf16 v[156:159], a[120:123], a[56:59], v[156:159]\n" \
"buffer_load_dwordx4 v215, %[srdv], s48 offen offset:3072 lds\n" \
"v_mfma_f32_16x16x32_bf16 v[156:159], a[124:127], a[60:63], v[156:159]\n" \
"v_exp_f32_e64 v151, v151\n" \
"v_mfma_f32_16x16x32_bf16 v[80:83], a[144:147], v[216:219], 0\n" \
"ds_read_b64_tr_b16 a[174:175], v211 offset:12800\n" \
"ds_read_b64_tr_b16 a[176:177], %[vvb] offset:9216\n" \
"v_mfma_f32_16x16x32_bf16 v[112:115], a[144:147], v[220:223], 0\n" \
"v_exp_f32_e64 v152, v152\n" \
"v_mfma_f32_16x16x32_bf16 v[84:87], a[148:151], v[216:219], 0\n" \
"v_cvt_pk_bf16_f32 v202, v148, v149\n" \
"v_cvt_pk_bf16_f32 v203, v150, v151\n" \
"v_mfma_f32_16x16x32_bf16 v[116:119], a[148:151], v[220:223], 0\n" \
"v_exp_f32_e64 v153, v153\n" \
"v_mfma_f32_16x16x32_bf16 v[88:91], a[152:155], v[216:219], 0\n" \
"v_exp_f32_e64 v154, v154\n" \
"v_mfma_f32_16x16x32_bf16 v[120:123], a[152:155], v[220:223], 0\n" \
"ds_read_b64_tr_b16 a[178:179], %[vvb] offset:13312\n" \
"v_mfma_f32_16x16x32_bf16 v[92:95], a[156:159], v[216:219], 0\n" \
"v_exp_f32_e64 v155, v155\n" \
"v_mov_b64_e32 v[216:217], 0\n" \
"v_mov_b64_e32 v[218:219], 0\n" \
"v_mfma_f32_16x16x32_bf16 v[124:127], a[156:159], v[220:223], 0\n" \
"ds_read_b64_tr_b16 a[180:181], v211 offset:9216\n" \
"s_waitcnt lgkmcnt(13)\n" \
"s_nop 0\n" \
"LmA_%=:\n" \
"v_mfma_f32_16x16x32_bf16 v[128:131], a[64:67], a[0:3], v[160:163]\n" \
"v_exp_f32_e64 v156, v156\n" \
"v_mfma_f32_16x16x32_bf16 v[128:131], a[68:71], a[4:7], v[128:131]\n" \
"ds_read_b64_tr_b16 a[182:183], v211 offset:13312\n" \
"s_add_u32 m0, %[m0b], 0\n" \
"v_mfma_f32_16x16x32_bf16 v[128:131], a[72:75], a[8:11], v[128:131]\n" \
"v_exp_f32_e32 v157, v157\n" \
"v_mfma_f32_16x16x32_bf16 v[128:131], a[76:79], a[12:15], v[128:131]\n" \
"v_cvt_pk_bf16_f32 v204, v152, v153\n" \
"v_cvt_pk_bf16_f32 v205, v154, v155\n" \
"v_mfma_f32_16x16x32_bf16 v[132:135], a[80:83], a[0:3], v[160:163]\n" \
"v_exp_f32_e64 v158, v158\n" \
"v_mfma_f32_16x16x32_bf16 v[132:135], a[84:87], a[4:7], v[132:135]\n" \
"ds_read_b64_tr_b16 a[184:185], %[vvb] offset:9728\n" \
"s_waitcnt lgkmcnt(5)\n" \
"v_mfma_f32_16x16x32_bf16 v[132:135], a[88:91], a[8:11], v[132:135]\n" \
"buffer_load_dwordx4 %[kd0], %[srdk], s48 offen offset:0 lds\n" \
"v_mfma_f32_16x16x32_bf16 v[132:135], a[92:95], a[12:15], v[132:135]\n" \
"v_exp_f32_e32 v159, v159\n" \
"v_mfma_f32_16x16x32_bf16 v[64:67], a[128:131], v[216:219], v[64:67]\n" \
"ds_read_b64_tr_b16 a[186:187], %[vvb] offset:13824\n" \
"ds_read_b64_tr_b16 a[188:189], v211 offset:9728\n" \
"v_mfma_f32_16x16x32_bf16 v[64:67], a[160:163], v[200:203], v[64:67]\n" \
"v_exp_f32_e64 v128, v128\n" \
"v_mfma_f32_16x16x32_bf16 v[68:71], a[132:135], v[216:219], v[68:71]\n" \
"v_cvt_pk_bf16_f32 v206, v156, v157\n" \
"v_cvt_pk_bf16_f32 v207, v158, v159\n" \
"v_mfma_f32_16x16x32_bf16 v[68:71], a[164:167], v[200:203], v[68:71]\n" \
"v_exp_f32_e64 v129, v129\n" \
"v_mfma_f32_16x16x32_bf16 v[72:75], a[136:139], v[216:219], v[72:75]\n" \
"ds_read_b64_tr_b16 a[190:191], v211 offset:13824\n" \
"ds_read_b64_tr_b16 a[128:129], %[vvb] offset:16384\n" \
"s_add_u32 s60, s60, 1\n" \
"s_nop 0\n" \
"v_readlane_b32 s58, %[tlo], s60\n" \
"v_mfma_f32_16x16x32_bf16 v[72:75], a[168:171], v[200:203], v[72:75]\n" \
"v_exp_f32_e64 v130, v130\n" \
"v_mfma_f32_16x16x32_bf16 v[76:79], a[140:143], v[216:219], v[76:79]\n" \
"ds_read_b64_tr_b16 a[130:131], %[vvb] offset:20480\n" \
"ds_read_b64_tr_b16 a[132:133], v211 offset:16384\n" \
"v_readlane_b32 s59, %[thi], s60\n" \
"v_mfma_f32_16x16x32_bf16 v[76:79], a[172:175], v[200:203], v[76:79]\n" \
"v_exp_f32_e64 v131, v131\n" \
"v_smfmac_f32_16x16x64_bf16 v[176:179], a[192:195], v[192:199], v228\n" \
"ds_read_b64_tr_b16 a[134:135], v211 offset:20480\n" \
"ds_read_b64_tr_b16 a[136:137], %[vvb] offset:16896\n" \
"v_mfma_f32_16x16x32_bf16 v[136:139], a[64:67], a[16:19], v[164:167]\n" \
"v_exp_f32_e64 v132, v132\n" \
"v_mfma_f32_16x16x32_bf16 v[136:139], a[68:71], a[20:23], v[136:139]\n" \
"ds_read_b64_tr_b16 a[138:139], %[vvb] offset:20992\n" \
"ds_read_b64_tr_b16 a[140:141], v211 offset:16896\n" \
"v_mfma_f32_16x16x32_bf16 v[136:139], a[72:75], a[24:27], v[136:139]\n" \
"v_exp_f32_e64 v133, v133\n" \
"v_mfma_f32_16x16x32_bf16 v[136:139], a[76:79], a[28:31], v[136:139]\n" \
"v_cvt_pk_bf16_f32 v184, v128, v129\n" \
"v_cvt_pk_bf16_f32 v185, v130, v131\n" \
"v_mfma_f32_16x16x32_bf16 v[140:143], a[80:83], a[16:19], v[164:167]\n" \
"v_exp_f32_e64 v134, v134\n" \
"v_mfma_f32_16x16x32_bf16 v[140:143], a[84:87], a[20:23], v[140:143]\n" \
"ds_read_b64_tr_b16 a[142:143], v211 offset:20992\n" \
"s_waitcnt lgkmcnt(8)\n" \
"v_mfma_f32_16x16x32_bf16 v[140:143], a[88:91], a[24:27], v[140:143]\n" \
"buffer_load_dwordx4 v212, %[srdk], s48 offen offset:1024 lds\n" \
"v_mfma_f32_16x16x32_bf16 v[140:143], a[92:95], a[28:31], v[140:143]\n" \
"v_exp_f32_e32 v135, v135\n" \
"v_mfma_f32_16x16x32_bf16 v[80:83], a[144:147], v[216:219], v[80:83]\n" \
"ds_read_b64_tr_b16 a[144:145], %[vvb] offset:17408\n" \
"ds_read_b64_tr_b16 a[146:147], %[vvb] offset:21504\n" \
"v_mfma_f32_16x16x32_bf16 v[80:83], a[176:179], v[200:203], v[80:83]\n" \
"v_exp_f32_e64 v136, v136\n" \
"v_mfma_f32_16x16x32_bf16 v[84:87], a[148:151], v[216:219], v[84:87]\n" \
"v_cvt_pk_bf16_f32 v186, v132, v133\n" \
"v_cvt_pk_bf16_f32 v187, v134, v135\n" \
"v_mfma_f32_16x16x32_bf16 v[84:87], a[180:183], v[200:203], v[84:87]\n" \
"v_exp_f32_e64 v137, v137\n" \
"v_mfma_f32_16x16x32_bf16 v[88:91], a[152:155], v[216:219], v[88:91]\n" \
".if . - adaln_body != 12288\n" \
".error \"size model\"\n" \
".endif\n" \
"Lk2_%=:\n" \
"s_cbranch_execz Lk3_%=\n" \
"s_nop 0\n" \
"v_exp_f32_e64 v138, v138\n" \
"v_mfma_f32_16x16x32_bf16 v[88:91], a[184:187], v[200:203], v[88:91]\n" \
"ds_read_b64_tr_b16 a[148:149], v211 offset:17408\n" \
"ds_read_b64_tr_b16 a[150:151], v211 offset:21504\n" \
"v_mfma_f32_16x16x32_bf16 v[92:95], a[156:159], v[216:219], v[92:95]\n" \
"v_exp_f32_e64 v139, v139\n" \
"v_mfma_f32_16x16x32_bf16 v[92:95], a[188:191], v[200:203], v[92:95]\n" \
"ds_read_b64_tr_b16 a[152:153], %[vvb] offset:17920\n" \
"v_mfma_f32_16x16x32_bf16 v[144:147], a[64:67], a[32:35], v[168:171]\n" \
"v_exp_f32_e64 v140, v140\n" \
"v_mfma_f32_16x16x32_bf16 v[144:147], a[68:71], a[36:39], v[144:147]\n" \
"ds_read_b64_tr_b16 a[154:155], %[vvb] offset:22016\n" \
"ds_read_b64_tr_b16 a[156:157], v211 offset:17920\n" \
"v_mfma_f32_16x16x32_bf16 v[144:147], a[72:75], a[40:43], v[144:147]\n" \
"v_exp_f32_e64 v141, v141\n" \
"v_mfma_f32_16x16x32_bf16 v[144:147], a[76:79], a[44:47], v[144:147]\n" \
"v_cvt_pk_bf16_f32 v188, v136, v137\n" \
"v_cvt_pk_bf16_f32 v189, v138, v139\n" \
"v_mfma_f32_16x16x32_bf16 v[148:151], a[80:83], a[32:35], v[168:171]\n" \
"v_exp_f32_e64 v142, v142\n" \
"v_mfma_f32_16x16x32_bf16 v[148:151], a[84:87], a[36:39], v[148:151]\n" \
"ds_read_b64_tr_b16 a[158:159], v211 offset:22016\n" \
"s_waitcnt lgkmcnt(8)\n" \
"v_mfma_f32_16x16x32_bf16 v[148:151], a[88:91], a[40:43], v[148:151]\n" \
"buffer_load_dwordx4 v213, %[srdk], s48 offen offset:2048 lds\n" \
"v_mfma_f32_16x16x32_bf16 v[148:151], a[92:95], a[44:47], v[148:151]\n" \
"v_exp_f32_e32 v143, v143\n" \
"v_mfma_f32_16x16x32_bf16 v[0:3], a[160:163], v[192:195], v[0:3]\n" \
"ds_read_b128 a[96:99], %[kb0] offset:24576\n" \
"v_mfma_f32_16x16x32_bf16 v[0:3], a[128:131], v[184:187], v[0:3]\n" \
"v_exp_f32_e64 v144, v144\n" \
"v_mfma_f32_16x16x32_bf16 v[4:7], a[164:167], v[192:195], v[4:7]\n" \
"v_cvt_pk_bf16_f32 v190, v140, v141\n" \
"v_cvt_pk_bf16_f32 v191, v142, v143\n" \
"v_mfma_f32_16x16x32_bf16 v[4:7], a[132:135], v[184:187], v[4:7]\n" \
"v_exp_f32_e64 v145, v145\n" \
"v_mfma_f32_16x16x32_bf16 v[8:11], a[168:171], v[192:195], v[8:11]\n" \
"ds_read_b128 a[100:103], v208 offset:24576\n" \
"v_mfma_f32_16x16x32_bf16 v[8:11], a[136:139], v[184:187], v[8:11]\n" \
"v_exp_f32_e64 v146, v146\n" \
"v_mfma_f32_16x16x32_bf16 v[12:15], a[172:175], v[192:195], v[12:15]\n" \
"ds_read_b128 a[104:107], v209 offset:24576\n" \
"v_mfma_f32_16x16x32_bf16 v[12:15], a[140:143], v[184:187], v[12:15]\n" \
"v_exp_f32_e64 v147, v147\n" \
"v_smfmac_f32_16x16x64_bf16 v[180:183], a[192:195], v[200:207], v228\n" \
"ds_read_b128 a[108:111], v210 offset:24576\n" \
"v_mfma_f32_16x16x32_bf16 v[152:155], a[64:67], a[48:51], v[172:175]\n" \
"v_exp_f32_e64 v148, v148\n" \
"v_mfma_f32_16x16x32_bf16 v[152:155], a[68:71], a[52:55], v[152:155]\n" \
"ds_read_b128 a[112:115], %[kb0] offset:28672\n" \
"v_mfma_f32_16x16x32_bf16 v[152:155], a[72:75], a[56:59], v[152:155]\n" \
"v_exp_f32_e64 v149, v149\n" \
"v_mfma_f32_16x16x32_bf16 v[152:155], a[76:79], a[60:63], v[152:155]\n" \
"v_cvt_pk_bf16_f32 v216, v144, v145\n" \
"v_cvt_pk_bf16_f32 v217, v146, v147\n" \
"v_mfma_f32_16x16x32_bf16 v[156:159], a[80:83], a[48:51], v[172:175]\n" \
"v_exp_f32_e64 v150, v150\n" \
"v_mfma_f32_16x16x32_bf16 v[156:159], a[84:87], a[52:55], v[156:159]\n" \
"ds_read_b128 a[116:119], v208 offset:28672\n" \
"s_waitcnt lgkmcnt(6)\n" \
"v_mfma_f32_16x16x32_bf16 v[156:159], a[88:91], a[56:59], v[156:159]\n" \
"buffer_load_dwordx4 v214, %[srdk], s48 offen offset:3072 lds\n" \
"v_mfma_f32_16x16x32_bf16 v[156:159], a[92:95], a[60:63], v[156:159]\n" \
"v_exp_f32_e32 v151, v151\n" \
"v_mfma_f32_16x16x32_bf16 v[16:19], a[176:179], v[192:195], v[16:19]\n" \
"ds_read_b128 a[120:123], v209 offset:28672\n" \
"v_mfma_f32_16x16x32_bf16 v[16:19], a[144:147], v[184:187], v[16:19]\n" \
"v_exp_f32_e64 v152, v152\n" \
"v_mfma_f32_16x16x32_bf16 v[20:23], a[180:183], v[192:195], v[20:23]\n" \
"v_cvt_pk_bf16_f32 v218, v148, v149\n" \
"v_cvt_pk_bf16_f32 v219, v150, v151\n" \
"v_mfma_f32_16x16x32_bf16 v[20:23], a[148:151], v[184:187], v[20:23]\n" \
"v_exp_f32_e64 v153, v153\n" \
"v_mfma_f32_16x16x32_bf16 v[24:27], a[184:187], v[192:195], v[24:27]\n" \
"v_exp_f32_e64 v154, v154\n" \
"v_mfma_f32_16x16x32_bf16 v[24:27], a[152:155], v[184:187], v[24:27]\n" \
"ds_read_b128 a[124:127], v210 offset:28672\n" \
"s_add_u32 s48, s48, 0x4000\n" \
"s_mov_b32 m0, s44\n" \
"s_addk_i32 s44, 0x400\n" \
"buffer_load_dwordx4 v229, s[64:67], s45 offen offset:0 nt lds\n" \
"s_add_u32 s45, s45, 393216\n" \
"v_mfma_f32_16x16x32_bf16 v[28:31], a[188:191], v[192:195], v[28:31]\n" \
"v_exp_f32_e64 v155, v155\n" \
"v_mfma_f32_16x16x32_bf16 v[28:31], a[156:159], v[184:187], v[28:31]\n" \
"s_waitcnt vmcnt(14) lgkmcnt(2)\n" \
"s_barrier\n" \
"v_mfma_f32_16x16x32_bf16 v[128:131], a[96:99], a[0:3], v[160:163]\n" \
"v_exp_f32_e64 v156, v156\n" \
"v_mfma_f32_16x16x32_bf16 v[128:131], a[100:103], a[4:7], v[128:131]\n" \
"s_add_u32 m0, %[m0b], 65536\n" \
"v_mfma_f32_16x16x32_bf16 v[128:131], a[104:107], a[8:11], v[128:131]\n" \
"v_exp_f32_e64 v157, v157\n" \
"v_mfma_f32_16x16x32_bf16 v[128:131], a[108:111], a[12:15], v[128:131]\n" \
"v_cvt_pk_bf16_f32 v220, v152, v153\n" \
"v_cvt_pk_bf16_f32 v221, v154, v155\n" \
"v_mfma_f32_16x16x32_bf16 v[132:135], a[112:115], a[0:3], v[160:163]\n" \
"v_exp_f32_e64 v158, v158\n" \
"v_mfma_f32_16x16x32_bf16 v[132:135], a[116:119], a[4:7], v[132:135]\n" \
"s_waitcnt lgkmcnt(0)\n" \
"v_mfma_f32_16x16x32_bf16 v[132:135], a[120:123], a[8:11], v[132:135]\n" \
"buffer_load_dwordx4 %[vd0], %[srdv], s48 offen offset:0 lds\n" \
"v_mfma_f32_16x16x32_bf16 v[132:135], a[124:127], a[12:15], v[132:135]\n" \
"v_exp_f32_e32 v159, v159\n" \
"v_mfma_f32_16x16x32_bf16 v[32:35], a[160:163], v[196:199], v[32:35]\n" \
"ds_read_b128 a[64:67], %[kb0] offset:32768\n" \
"v_mfma_f32_16x16x32_bf16 v[32:35], a[128:131], v[188:191], v[32:35]\n" \
"v_exp_f32_e64 v128, v128\n" \
"v_mfma_f32_16x16x32_bf16 v[36:39], a[164:167], v[196:199], v[36:39]\n" \
"v_cvt_pk_bf16_f32 v222, v156, v157\n" \
"v_cvt_pk_bf16_f32 v223, v158, v159\n" \
"v_mfma_f32_16x16x32_bf16 v[36:39], a[132:135], v[188:191], v[36:39]\n" \
"v_exp_f32_e64 v129, v129\n" \
"v_mfma_f32_16x16x32_bf16 v[40:43], a[168:171], v[196:199], v[40:43]\n" \
"ds_read_b128 a[68:71], v208 offset:32768\n" \
"v_mfma_f32_16x16x32_bf16 v[40:43], a[136:139], v[188:191], v[40:43]\n" \
"v_exp_f32_e64 v130, v130\n" \
"v_mfma_f32_16x16x32_bf16 v[44:47], a[172:175], v[196:199], v[44:47]\n" \
"ds_read_b128 a[72:75], v209 offset:32768\n" \
"v_mfma_f32_16x16x32_bf16 v[44:47], a[140:143], v[188:191], v[44:47]\n" \
"v_exp_f32_e64 v131, v131\n" \
"v_smfmac_f32_16x16x64_bf16 v[176:179], a[192:195], v[184:191], v228\n" \
"v_mfma_f32_16x16x32_bf16 v[136:139], a[96:99], a[16:19], v[164:167]\n" \
"v_exp_f32_e64 v132, v132\n" \
"v_mfma_f32_16x16x32_bf16 v[136:139], a[100:103], a[20:23], v[136:139]\n" \
"ds_read_b128 a[76:79], v210 offset:32768\n" \
"v_mfma_f32_16x16x32_bf16 v[136:139], a[104:107], a[24:27], v[136:139]\n" \
"v_exp_f32_e64 v133, v133\n" \
"v_mfma_f32_16x16x32_bf16 v[136:139], a[108:111], a[28:31], v[136:139]\n" \
"v_cvt_pk_bf16_f32 v192, v128, v129\n" \
"v_cvt_pk_bf16_f32 v193, v130, v131\n" \
"v_mfma_f32_16x16x32_bf16 v[140:143], a[112:115], a[16:19], v[164:167]\n" \
"v_exp_f32_e64 v134, v134\n" \
"v_mfma_f32_16x16x32_bf16 v[140:143], a[116:119], a[20:23], v[140:143]\n" \
"ds_read_b128 a[80:83], %[kb0] offset:36864\n" \
"v_mfma_f32_16x16x32_bf16 v[140:143], a[120:123], a[24:27], v[140:143]\n" \
"buffer_load_dwordx4 v215, %[srdv], s48 offen offset:1024 lds\n" \
"v_mfma_f32_16x16x32_bf16 v[140:143], a[124:127], a[28:31], v[140:143]\n" \
"v_exp_f32_e64 v135, v135\n" \
"v_mfma_f32_16x16x32_bf16 v[48:51], a[176:179], v[196:199], v[48:51]\n" \
"ds_read_b128 a[84:87], v208 offset:36864\n" \
"v_mfma_f32_16x16x32_bf16 v[48:51], a[144:147], v[188:191], v[48:51]\n" \
"v_exp_f32_e64 v136, v136\n" \
"v_mfma_f32_16x16x32_bf16 v[52:55], a[180:183], v[196:199], v[52:55]\n" \
"v_cvt_pk_bf16_f32 v194, v132, v133\n" \
"v_cvt_pk_bf16_f32 v195, v134, v135\n" \
"v_mfma_f32_16x16x32_bf16 v[52:55], a[148:151], v[188:191], v[52:55]\n" \
"v_exp_f32_e64 v137, v137\n" \
"v_mfma_f32_16x16x32_bf16 v[56:59], a[184:187], v[196:199], v[56:59]\n" \
"v_exp_f32_e64 v138, v138\n" \
"v_mfma_f32_16x16x32_bf16 v[56:59], a[152:155], v[188:191], v[56:59]\n" \
"ds_read_b128 a[88:91], v209 offset:36864\n" \
"v_mfma_f32_16x16x32_bf16 v[60:63], a[188:191], v[196:199], v[60:63]\n" \
"v_exp_f32_e64 v139, v139\n" \
"v_mfma_f32_16x16x32_bf16 v[60:63], a[156:159], v[188:191], v[60:63]\n" \
"s_cmp_lg_u32 %[wid], 0\n" \
"s_cbranch_scc1 LtA3_%=\n" \
"global_load_dword v237, v224, s[58:59]\n" \
"s_add_u32 s61, s48, 49152\n" \
"s_mov_b32 exec_hi, 0\n" \
"s_nop 0\n" \
"buffer_load_dword v237, v224, %[srdp], s61 offen\n" \
"s_mov_b32 exec_hi, -1\n" \
"s_nop 0\n" \
"LtA3_%=:\n" \
"v_mfma_f32_16x16x32_bf16 v[144:147], a[96:99], a[32:35], v[168:171]\n" \
"v_exp_f32_e64 v140, v140\n" \
"v_mfma_f32_16x16x32_bf16 v[144:147], a[100:103], a[36:39], v[144:147]\n" \
"ds_read_b128 a[92:95], v210 offset:36864\n" \
"v_mfma_f32_16x16x32_bf16 v[144:147], a[104:107], a[40:43], v[144:147]\n" \
"v_exp_f32_e64 v141, v141\n" \
"v_mfma_f32_16x16x32_bf16 v[144:147], a[108:111], a[44:47], v[144:147]\n" \
"v_cvt_pk_bf16_f32 v196, v136, v137\n" \
"v_cvt_pk_bf16_f32 v197, v138, v139\n" \
"v_mfma_f32_16x16x32_bf16 v[148:151], a[112:115], a[32:35], v[168:171]\n" \
"v_exp_f32_e64 v142, v142\n" \
"v_mfma_f32_16x16x32_bf16 v[148:151], a[116:119], a[36:39], v[148:151]\n" \
"v_mfma_f32_16x16x32_bf16 v[148:151], a[120:123], a[40:43], v[148:151]\n" \
"buffer_load_dwordx4 %[vd0], %[srdv], s48 offen offset:2048 lds\n" \
"v_mfma_f32_16x16x32_bf16 v[148:151], a[124:127], a[44:47], v[148:151]\n" \
"v_exp_f32_e64 v143, v143\n" \
"v_mfma_f32_16x16x32_bf16 v[96:99], a[160:163], v[204:207], v[96:99]\n" \
"ds_read_b64_tr_b16 a[160:161], %[vvb] offset:24576\n" \
"v_mfma_f32_16x16x32_bf16 v[96:99], a[128:131], v[220:223], v[96:99]\n" \
"v_exp_f32_e64 v144, v144\n" \
"v_mfma_f32_16x16x32_bf16 v[100:103], a[164:167], v[204:207], v[100:103]\n" \
"v_cvt_pk_bf16_f32 v198, v140, v141\n" \
"v_cvt_pk_bf16_f32 v199, v142, v143\n" \
"v_mfma_f32_16x16x32_bf16 v[100:103], a[132:135], v[220:223], v[100:103]\n" \
"v_exp_f32_e64 v145, v145\n" \
"v_mfma_f32_16x16x32_bf16 v[104:107], a[168:171], v[204:207], v[104:107]\n" \
"ds_read_b64_tr_b16 a[162:163], %[vvb] offset:28672\n" \
"ds_read_b64_tr_b16 a[164:165], v211 offset:24576\n" \
"v_mfma_f32_16x16x32_bf16 v[104:107], a[136:139], v[220:223], v[104:107]\n" \
"v_exp_f32_e64 v146, v146\n" \
"v_mfma_f32_16x16x32_bf16 v[108:111], a[172:175], v[204:207], v[108:111]\n" \
"ds_read_b64_tr_b16 a[166:167], v211 offset:28672\n" \
"v_mfma_f32_16x16x32_bf16 v[108:111], a[140:143], v[220:223], v[108:111]\n" \
"v_exp_f32_e64 v147, v147\n" \
"v_smfmac_f32_16x16x64_bf16 v[180:183], a[192:195], v[216:223], v228\n" \
"ds_read_b64_tr_b16 a[168:169], %[vvb] offset:25088\n" \
"v_mfma_f32_16x16x32_bf16 v[152:155], a[96:99], a[48:51], v[172:175]\n" \
"v_exp_f32_e64 v148, v148\n" \
"v_mfma_f32_16x16x32_bf16 v[152:155], a[100:103], a[52:55], v[152:155]\n" \
"ds_read_b64_tr_b16 a[170:171], %[vvb] offset:29184\n" \
"v_mfma_f32_16x16x32_bf16 v[152:155], a[104:107], a[56:59], v[152:155]\n" \
"v_exp_f32_e64 v149, v149\n" \
"v_mfma_f32_16x16x32_bf16 v[152:155], a[108:111], a[60:63], v[152:155]\n" \
"v_cvt_pk_bf16_f32 v200, v144, v145\n" \
"v_cvt_pk_bf16_f32 v201, v146, v147\n" \
"v_mfma_f32_16x16x32_bf16 v[156:159], a[112:115], a[48:51], v[172:175]\n" \
"v_exp_f32_e64 v150, v150\n" \
"v_mfma_f32_16x16x32_bf16 v[156:159], a[116:119], a[52:55], v[156:159]\n" \
"ds_read_b64_tr_b16 a[172:173], v211 offset:25088\n" \
"v_mfma_f32_16x16x32_bf16 v[156:159], a[120:123], a[56:59], v[156:159]\n" \
"buffer_load_dwordx4 v215, %[srdv], s48 offen offset:3072 lds\n" \
"v_mfma_f32_16x16x32_bf16 v[156:159], a[124:127], a[60:63], v[156:159]\n" \
"v_exp_f32_e64 v151, v151\n" \
"v_mfma_f32_16x16x32_bf16 v[112:115], a[176:179], v[204:207], v[112:115]\n" \
"ds_read_b64_tr_b16 a[174:175], v211 offset:29184\n" \
"ds_read_b64_tr_b16 a[176:177], %[vvb] offset:25600\n" \
"v_mfma_f32_16x16x32_bf16 v[112:115], a[144:147], v[220:223], v[112:115]\n" \
"v_exp_f32_e64 v152, v152\n" \
"v_mfma_f32_16x16x32_bf16 v[116:119], a[180:183], v[204:207], v[116:119]\n" \
"v_cvt_pk_bf16_f32 v202, v148, v149\n" \
"v_cvt_pk_bf16_f32 v203, v150, v151\n" \
"v_mfma_f32_16x16x32_bf16 v[116:119], a[148:151], v[220:223], v[116:119]\n" \
"v_exp_f32_e64 v153, v153\n" \
"v_mfma_f32_16x16x32_bf16 v[120:123], a[184:187], v[204:207], v[120:123]\n" \
"v_exp_f32_e64 v154, v154\n" \
"v_mfma_f32_16x16x32_bf16 v[120:123], a[152:155], v[220:223], v[120:123]\n" \
"ds_read_b64_tr_b16 a[178:179], %[vvb] offset:29696\n" \
"v_mfma_f32_16x16x32_bf16 v[124:127], a[188:191], v[204:207], v[124:127]\n" \
"v_exp_f32_e64 v155, v155\n" \
"v_mfma_f32_16x16x32_bf16 v[124:127], a[156:159], v[220:223], v[124:127]\n" \
"ds_read_b64_tr_b16 a[180:181], v211 offset:25600\n" \
"s_waitcnt lgkmcnt(13)\n" \
"v_mfma_f32_16x16x32_bf16 v[128:131], a[64:67], a[0:3], v[160:163]\n" \
"v_exp_f32_e32 v156, v156\n" \
"v_mfma_f32_16x16x32_bf16 v[128:131], a[68:71], a[4:7], v[128:131]\n" \
"ds_read_b64_tr_b16 a[182:183], v211 offset:29696\n" \
"s_add_u32 m0, %[m0b], 16384\n" \
"v_mfma_f32_16x16x32_bf16 v[128:131], a[72:75], a[8:11], v[128:131]\n" \
"v_exp_f32_e64 v157, v157\n" \
"v_mfma_f32_16x16x32_bf16 v[128:131], a[76:79], a[12:15], v[128:131]\n" \
"v_cvt_pk_bf16_f32 v204, v152, v153\n" \
"v_cvt_pk_bf16_f32 v205, v154, v155\n" \
"v_mfma_f32_16x16x32_bf16 v[132:135], a[80:83], a[0:3], v[160:163]\n" \
"v_exp_f32_e64 v158, v158\n" \
"v_mfma_f32_16x16x32_bf16 v[132:135], a[84:87], a[4:7], v[132:135]\n" \
"ds_read_b64_tr_b16 a[184:185], %[vvb] offset:26112\n" \
"s_waitcnt lgkmcnt(5)\n" \
"v_mfma_f32_16x16x32_bf16 v[132:135], a[88:91], a[8:11], v[132:135]\n" \
"buffer_load_dwordx4 %[kd0], %[srdk], s48 offen offset:0 lds\n" \
"v_mfma_f32_16x16x32_bf16 v[132:135], a[92:95], a[12:15], v[132:135]\n" \
"v_exp_f32_e32 v159, v159\n" \
"v_mfma_f32_16x16x32_bf16 v[64:67], a[128:131], v[216:219], v[64:67]\n" \
"ds_read_b64_tr_b16 a[186:187], %[vvb] offset:30208\n" \
"ds_read_b64_tr_b16 a[188:189], v211 offset:26112\n" \
"v_mfma_f32_16x16x32_bf16 v[64:67], a[160:163], v[200:203], v[64:67]\n" \
"v_exp_f32_e64 v128, v128\n" \
"v_mfma_f32_16x16x32_bf16 v[68:71], a[132:135], v[216:219], v[68:71]\n" \
"v_cvt_pk_bf16_f32 v206, v156, v157\n" \
"v_cvt_pk_bf16_f32 v207, v158, v159\n" \
"v_mfma_f32_16x16x32_bf16 v[68:71], a[164:167], v[200:203], v[68:71]\n" \
"v_exp_f32_e64 v129, v129\n" \
"v_mfma_f32_16x16x32_bf16 v[72:75], a[136:139], v[216:219], v[72:75]\n" \
"ds_read_b64_tr_b16 a[190:191], v211 offset:30208\n" \
"ds_read_b64_tr_b16 a[128:129], %[vvb] offset:32768\n" \
"v_mfma_f32_16x16x32_bf16 v[72:75], a[168:171], v[200:203], v[72:75]\n" \
"v_exp_f32_e64 v130, v130\n" \
"v_mfma_f32_16x16x32_bf16 v[76:79], a[140:143], v[216:219], v[76:79]\n" \
"ds_read_b64_tr_b16 a[130:131], %[vvb] offset:36864\n" \
"ds_read_b64_tr_b16 a[132:133], v211 offset:32768\n" \
"v_mfma_f32_16x16x32_bf16 v[76:79], a[172:175], v[200:203], v[76:79]\n" \
"v_exp_f32_e64 v131, v131\n" \
"v_smfmac_f32_16x16x64_bf16 v[176:179], a[192:195], v[192:199], v228\n" \
"ds_read_b64_tr_b16 a[134:135], v211 offset:36864\n" \
"ds_read_b64_tr_b16 a[136:137], %[vvb] offset:33280\n" \
"v_mfma_f32_16x16x32_bf16 v[136:139], a[64:67], a[16:19], v[164:167]\n" \
"v_exp_f32_e64 v132, v132\n" \
"v_mfma_f32_16x16x32_bf16 v[136:139], a[68:71], a[20:23], v[136:139]\n" \
"ds_read_b64_tr_b16 a[138:139], %[vvb] offset:37376\n" \
"ds_read_b64_tr_b16 a[140:141], v211 offset:33280\n" \
"v_mfma_f32_16x16x32_bf16 v[136:139], a[72:75], a[24:27], v[136:139]\n" \
"v_exp_f32_e64 v133, v133\n" \
"v_mfma_f32_16x16x32_bf16 v[136:139], a[76:79], a[28:31], v[136:139]\n" \
"v_cvt_pk_bf16_f32 v184, v128, v129\n" \
"v_cvt_pk_bf16_f32 v185, v130, v131\n" \
"v_mfma_f32_16x16x32_bf16 v[140:143], a[80:83], a[16:19], v[164:167]\n" \
"v_exp_f32_e64 v134, v134\n" \
"v_mfma_f32_16x16x32_bf16 v[140:143], a[84:87], a[20:23], v[140:143]\n" \
"ds_read_b64_tr_b16 a[142:143], v211 offset:37376\n" \
"s_waitcnt lgkmcnt(8)\n" \
"v_mfma_f32_16x16x32_bf16 v[140:143], a[88:91], a[24:27], v[140:143]\n" \
"buffer_load_dwordx4 v212, %[srdk], s48 offen offset:1024 lds\n" \
"v_mfma_f32_16x16x32_bf16 v[140:143], a[92:95], a[28:31], v[140:143]\n" \
"v_exp_f32_e32 v135, v135\n" \
"v_mfma_f32_16x16x32_bf16 v[80:83], a[144:147], v[216:219], v[80:83]\n" \
"ds_read_b64_tr_b16 a[144:145], %[vvb] offset:33792\n" \
"ds_read_b64_tr_b16 a[146:147], %[vvb] offset:37888\n" \
"v_mfma_f32_16x16x32_bf16 v[80:83], a[176:179], v[200:203], v[80:83]\n" \
"v_exp_f32_e64 v136, v136\n" \
"v_mfma_f32_16x16x32_bf16 v[84:87], a[148:151], v[216:219], v[84:87]\n" \
"v_cvt_pk_bf16_f32 v186, v132, v133\n" \
"v_cvt_pk_bf16_f32 v187, v134, v135\n" \
"v_mfma_f32_16x16x32_bf16 v[84:87], a[180:183], v[200:203], v[84:87]\n" \
"v_exp_f32_e64 v137, v137\n" \
"v_mfma_f32_16x16x32_bf16 v[88:91], a[152:155], v[216:219], v[88:91]\n" \
"v_exp_f32_e64 v138, v138\n" \
"v_mfma_f32_16x16x32_bf16 v[88:91], a[184:187], v[200:203], v[88:91]\n" \
"ds_read_b64_tr_b16 a[148:149], v211 offset:33792\n" \
"ds_read_b64_tr_b16 a[150:151], v211 offset:37888\n" \
"v_mfma_f32_16x16x32_bf16 v[92:95], a[156:159], v[216:219], v[92:95]\n" \
"v_exp_f32_e64 v139, v139\n" \
"v_mfma_f32_16x16x32_bf16 v[92:95], a[188:191], v[200:203], v[92:95]\n" \
"ds_read_b64_tr_b16 a[152:153], %[vvb] offset:34304\n" \
"buffer_store_dwordx4 a[196:199], v235, %[srdz], s62 offen offset:0 sc1\n" \
"v_mfma_f32_16x16x32_bf16 v[144:147], a[64:67], a[32:35], v[168:171]\n" \
"v_exp_f32_e64 v140, v140\n" \
"v_mfma_f32_16x16x32_bf16 v[144:147], a[68:71], a[36:39], v[144:147]\n" \
"ds_read_b64_tr_b16 a[154:155], %[vvb] offset:38400\n" \
"ds_read_b64_tr_b16 a[156:157], v211 offset:34304\n" \
"v_mfma_f32_16x16x32_bf16 v[144:147], a[72:75], a[40:43], v[144:147]\n" \
"v_exp_f32_e64 v141, v141\n" \
"v_mfma_f32_16x16x32_bf16 v[144:147], a[76:79], a[44:47], v[144:147]\n" \
"v_cvt_pk_bf16_f32 v188, v136, v137\n" \
"v_cvt_pk_bf16_f32 v189, v138, v139\n" \
"v_mfma_f32_16x16x32_bf16 v[148:151], a[80:83], a[32:35], v[168:171]\n" \
"v_exp_f32_e64 v142, v142\n" \
"v_mfma_f32_16x16x32_bf16 v[148:151], a[84:87], a[36:39], v[148:151]\n" \
"ds_read_b64_tr_b16 a[158:159], v211 offset:38400\n" \
"s_waitcnt lgkmcnt(8)\n" \
"v_mfma_f32_16x16x32_bf16 v[148:151], a[88:91], a[40:43], v[148:151]\n" \
"buffer_load_dwordx4 v213, %[srdk], s48 offen offset:2048 lds\n" \
"v_mfma_f32_16x16x32_bf16 v[148:151], a[92:95], a[44:47], v[148:151]\n" \
"v_exp_f32_e32 v143, v143\n" \
"v_mfma_f32_16x16x32_bf16 v[0:3], a[160:163], v[192:195], v[0:3]\n" \
"ds_read_b128 a[96:99], %[kb0] offset:40960\n" \
"v_mfma_f32_16x16x32_bf16 v[0:3], a[128:131], v[184:187], v[0:3]\n" \
"v_exp_f32_e64 v144, v144\n" \
"v_mfma_f32_16x16x32_bf16 v[4:7], a[164:167], v[192:195], v[4:7]\n" \
"v_cvt_pk_bf16_f32 v190, v140, v141\n" \
"v_cvt_pk_bf16_f32 v191, v142, v143\n" \
"v_mfma_f32_16x16x32_bf16 v[4:7], a[132:135], v[184:187], v[4:7]\n" \
"v_exp_f32_e64 v145, v145\n" \
"v_mfma_f32_16x16x32_bf16 v[8:11], a[168:171], v[192:195], v[8:11]\n" \
"ds_read_b128 a[100:103], v208 offset:40960\n" \
"v_mfma_f32_16x16x32_bf16 v[8:11], a[136:139], v[184:187], v[8:11]\n" \
"v_exp_f32_e64 v146, v146\n" \
"v_mfma_f32_16x16x32_bf16 v[12:15], a[172:175], v[192:195], v[12:15]\n" \
"ds_read_b128 a[104:107], v209 offset:40960\n" \
"v_mfma_f32_16x16x32_bf16 v[12:15], a[140:143], v[184:187], v[12:15]\n" \
"v_exp_f32_e64 v147, v147\n" \
"v_smfmac_f32_16x16x64_bf16 v[180:183], a[192:195], v[200:207], v228\n" \
"ds_read_b128 a[108:111], v210 offset:40960\n" \
"v_mfma_f32_16x16x32_bf16 v[152:155], a[64:67], a[48:51], v[172:175]\n" \
"v_exp_f32_e64 v148, v148\n" \
"v_mfma_f32_16x16x32_bf16 v[152:155], a[68:71], a[52:55], v[152:155]\n" \
"ds_read_b128 a[112:115], %[kb0] offset:45056\n" \
"v_mfma_f32_16x16x32_bf16 v[152:155], a[72:75], a[56:59], v[152:155]\n" \
"v_exp_f32_e64 v149, v149\n" \
"v_mfma_f32_16x16x32_bf16 v[152:155], a[76:79], a[60:63], v[152:155]\n" \
"v_cvt_pk_bf16_f32 v216, v144, v145\n" \
"v_cvt_pk_bf16_f32 v217, v146, v147\n" \
"v_mfma_f32_16x16x32_bf16 v[156:159], a[80:83], a[48:51], v[172:175]\n" \
"v_exp_f32_e64 v150, v150\n" \
"v_mfma_f32_16x16x32_bf16 v[156:159], a[84:87], a[52:55], v[156:159]\n" \
"ds_read_b128 a[116:119], v208 offset:45056\n" \
"s_waitcnt lgkmcnt(6)\n" \
"v_mfma_f32_16x16x32_bf16 v[156:159], a[88:91], a[56:59], v[156:159]\n" \
"buffer_load_dwordx4 v214, %[srdk], s48 offen offset:3072 lds\n" \
"v_mfma_f32_16x16x32_bf16 v[156:159], a[92:95], a[60:63], v[156:159]\n" \
"v_exp_f32_e32 v151, v151\n" \
"v_mfma_f32_16x16x32_bf16 v[16:19], a[176:179], v[192:195], v[16:19]\n" \
"ds_read_b128 a[120:123], v209 offset:45056\n" \
"v_mfma_f32_16x16x32_bf16 v[16:19], a[144:147], v[184:187], v[16:19]\n" \
"v_exp_f32_e64 v152, v152\n" \
"v_mfma_f32_16x16x32_bf16 v[20:23], a[180:183], v[192:195], v[20:23]\n" \
"v_cvt_pk_bf16_f32 v218, v148, v149\n" \
"v_cvt_pk_bf16_f32 v219, v150, v151\n" \
"v_mfma_f32_16x16x32_bf16 v[20:23], a[148:151], v[184:187], v[20:23]\n" \
"v_exp_f32_e64 v153, v153\n" \
"v_mfma_f32_16x16x32_bf16 v[24:27], a[184:187], v[192:195], v[24:27]\n" \
"v_exp_f32_e64 v154, v154\n" \
"v_mfma_f32_16x16x32_bf16 v[24:27], a[152:155], v[184:187], v[24:27]\n" \
"ds_read_b128 a[124:127], v210 offset:45056\n" \
"s_add_u32 s48, s48, 0x4000\n" \
"v_mfma_f32_16x16x32_bf16 v[28:31], a[188:191], v[192:195], v[28:31]\n" \
"v_exp_f32_e64 v155, v155\n" \
"v_mfma_f32_16x16x32_bf16 v[28:31], a[156:159], v[184:187], v[28:31]\n" \
"s_waitcnt vmcnt(14) lgkmcnt(2)\n" \
"s_barrier\n" \
"v_mfma_f32_16x16x32_bf16 v[128:131], a[96:99], a[0:3], v[160:163]\n" \
"v_exp_f32_e64 v156, v156\n" \
"v_mfma_f32_16x16x32_bf16 v[128:131], a[100:103], a[4:7], v[128:131]\n" \
"s_add_u32 m0, %[m0b], 81920\n" \
"v_mfma_f32_16x16x32_bf16 v[128:131], a[104:107], a[8:11], v[128:131]\n" \
"v_exp_f32_e64 v157, v157\n" \
"v_mfma_f32_16x16x32_bf16 v[128:131], a[108:111], a[12:15], v[128:131]\n" \
"v_cvt_pk_bf16_f32 v220, v152, v153\n" \
"v_cvt_pk_bf16_f32 v221, v154, v155\n" \
"v_mfma_f32_16x16x32_bf16 v[132:135], a[112:115], a[0:3], v[160:163]\n" \
"v_exp_f32_e64 v158, v158\n" \
"v_mfma_f32_16x16x32_bf16 v[132:135], a[116:119], a[4:7], v[132:135]\n" \
"s_waitcnt lgkmcnt(0)\n" \
"v_mfma_f32_16x16x32_bf16 v[132:135], a[120:123], a[8:11], v[132:135]\n" \
"buffer_load_dwordx4 %[vd0], %[srdv], s48 offen offset:0 lds\n" \
"v_mfma_f32_16x16x32_bf16 v[132:135], a[124:127], a[12:15], v[132:135]\n" \
"v_exp_f32_e32 v159, v159\n" \
"v_mfma_f32_16x16x32_bf16 v[32:35], a[160:163], v[196:199], v[32:35]\n" \
"ds_read_b128 a[64:67], %[kb0] offset:49152\n" \
"v_mfma_f32_16x16x32_bf16 v[32:35], a[128:131], v[188:191], v[32:35]\n" \
"v_exp_f32_e64 v128, v128\n" \
"v_mfma_f32_16x16x32_bf16 v[36:39], a[164:167], v[196:199], v[36:39]\n" \
"v_cvt_pk_bf16_f32 v222, v156, v157\n" \
"v_cvt_pk_bf16_f32 v223, v158, v159\n" \
"v_mfma_f32_16x16x32_bf16 v[36:39], a[132:135], v[188:191], v[36:39]\n" \
"v_exp_f32_e64 v129, v129\n" \
"v_mfma_f32_16x16x32_bf16 v[40:43], a[168:171], v[196:199], v[40:43]\n" \
"ds_read_b128 a[68:71], v208 offset:49152\n" \
"v_mfma_f32_16x16x32_bf16 v[40:43], a[136:139], v[188:191], v[40:43]\n" \
"v_exp_f32_e64 v130, v130\n" \
"v_mfma_f32_16x16x32_bf16 v[44:47], a[172:175], v[196:199], v[44:47]\n" \
"ds_read_b128 a[72:75], v209 offset:49152\n" \
"v_mfma_f32_16x16x32_bf16 v[44:47], a[140:143], v[188:191], v[44:47]\n" \
"v_exp_f32_e64 v131, v131\n" \
"v_smfmac_f32_16x16x64_bf16 v[176:179], a[192:195], v[184:191], v228\n" \
"v_mfma_f32_16x16x32_bf16 v[136:139], a[96:99], a[16:19], v[164:167]\n" \
"v_exp_f32_e64 v132, v132\n" \
"v_mfma_f32_16x16x32_bf16 v[136:139], a[100:103], a[20:23], v[136:139]\n" \
"ds_read_b128 a[76:79], v210 offset:49152\n" \
"v_mfma_f32_16x16x32_bf16 v[136:139], a[104:107], a[24:27], v[136:139]\n" \
"v_exp_f32_e64 v133, v133\n" \
"v_mfma_f32_16x16x32_bf16 v[136:139], a[108:111], a[28:31], v[136:139]\n" \
"v_cvt_pk_bf16_f32 v192, v128, v129\n" \
"v_cvt_pk_bf16_f32 v193, v130, v131\n" \
"v_mfma_f32_16x16x32_bf16 v[140:143], a[112:115], a[16:19], v[164:167]\n" \
"v_exp_f32_e64 v134, v134\n" \
"v_mfma_f32_16x16x32_bf16 v[140:143], a[116:119], a[20:23], v[140:143]\n" \
"ds_read_b128 a[80:83], %[kb0] offset:53248\n" \
"v_mfma_f32_16x16x32_bf16 v[140:143], a[120:123], a[24:27], v[140:143]\n" \
"buffer_load_dwordx4 v215, %[srdv], s48 offen offset:1024 lds\n" \
"v_mfma_f32_16x16x32_bf16 v[140:143], a[124:127], a[28:31], v[140:143]\n" \
"v_exp_f32_e64 v135, v135\n" \
"v_mfma_f32_16x16x32_bf16 v[48:51], a[176:179], v[196:199], v[48:51]\n" \
"ds_read_b128 a[84:87], v208 offset:53248\n" \
"v_mfma_f32_16x16x32_bf16 v[48:51], a[144:147], v[188:191], v[48:51]\n" \
"v_exp_f32_e64 v136, v136\n" \
"v_mfma_f32_16x16x32_bf16 v[52:55], a[180:183], v[196:199], v[52:55]\n" \
"v_cvt_pk_bf16_f32 v194, v132, v133\n" \
"v_cvt_pk_bf16_f32 v195, v134, v135\n" \
"v_mfma_f32_16x16x32_bf16 v[52:55], a[148:151], v[188:191], v[52:55]\n" \
"v_exp_f32_e64 v137, v137\n" \
"v_mfma_f32_16x16x32_bf16 v[56:59], a[184:187], v[196:199], v[56:59]\n" \
"v_exp_f32_e64 v138, v138\n" \
"v_mfma_f32_16x16x32_bf16 v[56:59], a[152:155], v[188:191], v[56:59]\n" \
"ds_read_b128 a[88:91], v209 offset:53248\n" \
"v_mfma_f32_16x16x32_bf16 v[60:63], a[188:191], v[196:199], v[60:63]\n" \
"v_exp_f32_e64 v139, v139\n" \
"v_mfma_f32_16x16x32_bf16 v[60:63], a[156:159], v[188:191], v[60:63]\n" \
"s_cmp_lg_u32 %[wid], 1\n" \
"s_cbranch_scc1 LtA5_%=\n" \
"global_load_dword v237, v224, s[58:59]\n" \
"s_add_u32 s61, s48, 49152\n" \
"s_mov_b32 exec_hi, 0\n" \
"s_nop 0\n" \
"buffer_load_dword v237, v224, %[srdp], s61 offen\n" \
"s_mov_b32 exec_hi, -1\n" \
"s_nop 0\n" \
"LtA5_%=:\n" \
"v_mfma_f32_16x16x32_bf16 v[144:147], a[96:99], a[32:35], v[168:171]\n" \
"v_exp_f32_e64 v140, v140\n" \
"v_mfma_f32_16x16x32_bf16 v[144:147], a[100:103], a[36:39], v[144:147]\n" \
"ds_read_b128 a[92:95], v210 offset:53248\n" \
"v_mfma_f32_16x16x32_bf16 v[144:147], a[104:107], a[40:43], v[144:147]\n" \
"v_exp_f32_e64 v141, v141\n" \
"v_mfma_f32_16x16x32_bf16 v[144:147], a[108:111], a[44:47], v[144:147]\n" \
"v_cvt_pk_bf16_f32 v196, v136, v137\n" \
"v_cvt_pk_bf16_f32 v197, v138, v139\n" \
"v_mfma_f32_16x16x32_bf16 v[148:151], a[112:115], a[32:35], v[168:171]\n" \
"v_exp_f32_e64 v142, v142\n" \
"v_mfma_f32_16x16x32_bf16 v[148:151], a[116:119], a[36:39], v[148:151]\n" \
"v_mfma_f32_16x16x32_bf16 v[148:151], a[120:123], a[40:43], v[148:151]\n" \
"buffer_load_dwordx4 %[vd0], %[srdv], s48 offen offset:2048 lds\n" \
"v_mfma_f32_16x16x32_bf16 v[148:151], a[124:127], a[44:47], v[148:151]\n" \
"v_exp_f32_e64 v143, v143\n" \
"v_mfma_f32_16x16x32_bf16 v[96:99], a[160:163], v[204:207], v[96:99]\n" \
"ds_read_b64_tr_b16 a[160:161], %[vvb] offset:40960\n" \
"v_mfma_f32_16x16x32_bf16 v[96:99], a[128:131], v[220:223], v[96:99]\n" \
"v_exp_f32_e64 v144, v144\n" \
"v_mfma_f32_16x16x32_bf16 v[100:103], a[164:167], v[204:207], v[100:103]\n" \
"v_cvt_pk_bf16_f32 v198, v140, v141\n" \
"v_cvt_pk_bf16_f32 v199, v142, v143\n" \
"v_mfma_f32_16x16x32_bf16 v[100:103], a[132:135], v[220:223], v[100:103]\n" \
"v_exp_f32_e64 v145, v145\n" \
"v_mfma_f32_16x16x32_bf16 v[104:107], a[168:171], v[204:207], v[104:107]\n" \
"ds_read_b64_tr_b16 a[162:163], %[vvb] offset:45056\n" \
"ds_read_b64_tr_b16 a[164:165], v211 offset:40960\n" \
"v_mfma_f32_16x16x32_bf16 v[104:107], a[136:139], v[220:223], v[104:107]\n" \
"v_exp_f32_e64 v146, v146\n" \
"v_mfma_f32_16x16x32_bf16 v[108:111], a[172:175], v[204:207], v[108:111]\n" \
"ds_read_b64_tr_b16 a[166:167], v211 offset:45056\n" \
"v_mfma_f32_16x16x32_bf16 v[108:111], a[140:143], v[220:223], v[108:111]\n" \
"v_exp_f32_e64 v147, v147\n" \
"v_smfmac_f32_16x16x64_bf16 v[180:183], a[192:195], v[216:223], v228\n" \
"ds_read_b64_tr_b16 a[168:169], %[vvb] offset:41472\n" \
"v_mfma_f32_16x16x32_bf16 v[152:155], a[96:99], a[48:51], v[172:175]\n" \
"v_exp_f32_e64 v148, v148\n" \
"v_mfma_f32_16x16x32_bf16 v[152:155], a[100:103], a[52:55], v[152:155]\n" \
"ds_read_b64_tr_b16 a[170:171], %[vvb] offset:45568\n" \
"v_mfma_f32_16x16x32_bf16 v[152:155], a[104:107], a[56:59], v[152:155]\n" \
"v_exp_f32_e64 v149, v149\n" \
"v_mfma_f32_16x16x32_bf16 v[152:155], a[108:111], a[60:63], v[152:155]\n" \
"v_cvt_pk_bf16_f32 v200, v144, v145\n" \
"v_cvt_pk_bf16_f32 v201, v146, v147\n" \
"v_mfma_f32_16x16x32_bf16 v[156:159], a[112:115], a[48:51], v[172:175]\n" \
"v_exp_f32_e64 v150, v150\n" \
"v_mfma_f32_16x16x32_bf16 v[156:159], a[116:119], a[52:55], v[156:159]\n" \
"ds_read_b64_tr_b16 a[172:173], v211 offset:41472\n" \
"v_mfma_f32_16x16x32_bf16 v[156:159], a[120:123], a[56:59], v[156:159]\n" \
"buffer_load_dwordx4 v215, %[srdv], s48 offen offset:3072 lds\n" \
"v_mfma_f32_16x16x32_bf16 v[156:159], a[124:127], a[60:63], v[156:159]\n" \
"v_exp_f32_e64 v151, v151\n" \
"v_mfma_f32_16x16x32_bf16 v[112:115], a[176:179], v[204:207], v[112:115]\n" \
"ds_read_b64_tr_b16 a[174:175], v211 offset:45568\n" \
"ds_read_b64_tr_b16 a[176:177], %[vvb] offset:41984\n" \
"v_mfma_f32_16x16x32_bf16 v[112:115], a[144:147], v[220:223], v[112:115]\n" \
"v_exp_f32_e64 v152, v152\n" \
"v_mfma_f32_16x16x32_bf16 v[116:119], a[180:183], v[204:207], v[116:119]\n" \
"v_cvt_pk_bf16_f32 v202, v148, v149\n" \
".if . - adaln_body != 16384\n" \
".error \"size model\"\n" \
".endif\n" \
"Lk3_%=:\n" \
"s_cbranch_execz Lk4_%=\n" \
"s_nop 0\n" \
"v_cvt_pk_bf16_f32 v203, v150, v151\n" \
"v_mfma_f32_16x16x32_bf16 v[116:119], a[148:151], v[220:223], v[116:119]\n" \
"v_exp_f32_e64 v153, v153\n" \
"v_mfma_f32_16x16x32_bf16 v[120:123], a[184:187], v[204:207], v[120:123]\n" \
"v_exp_f32_e64 v154, v154\n" \
"v_mfma_f32_16x16x32_bf16 v[120:123], a[152:155], v[220:223], v[120:123]\n" \
"ds_read_b64_tr_b16 a[178:179], %[vvb] offset:46080\n" \
"v_mfma_f32_16x16x32_bf16 v[124:127], a[188:191], v[204:207], v[124:127]\n" \
"v_exp_f32_e64 v155, v155\n" \
"v_mfma_f32_16x16x32_bf16 v[124:127], a[156:159], v[220:223], v[124:127]\n" \
"ds_read_b64_tr_b16 a[180:181], v211 offset:41984\n" \
"s_waitcnt lgkmcnt(13)\n" \
"v_mfma_f32_16x16x32_bf16 v[128:131], a[64:67], a[0:3], v[160:163]\n" \
"v_exp_f32_e32 v156, v156\n" \
"v_mfma_f32_16x16x32_bf16 v[128:131], a[68:71], a[4:7], v[128:131]\n" \
"ds_read_b64_tr_b16 a[182:183], v211 offset:46080\n" \
"s_add_u32 m0, %[m0b], 32768\n" \
"v_mfma_f32_16x16x32_bf16 v[128:131], a[72:75], a[8:11], v[128:131]\n" \
"v_exp_f32_e64 v157, v157\n" \
"v_mfma_f32_16x16x32_bf16 v[128:131], a[76:79], a[12:15], v[128:131]\n" \
"v_cvt_pk_bf16_f32 v204, v152, v153\n" \
"v_cvt_pk_bf16_f32 v205, v154, v155\n" \
"v_mfma_f32_16x16x32_bf16 v[132:135], a[80:83], a[0:3], v[160:163]\n" \
"v_exp_f32_e64 v158, v158\n" \
"v_mfma_f32_16x16x32_bf16 v[132:135], a[84:87], a[4:7], v[132:135]\n" \
"ds_read_b64_tr_b16 a[184:185], %[vvb] offset:42496\n" \
"s_waitcnt lgkmcnt(5)\n" \
"v_mfma_f32_16x16x32_bf16 v[132:135], a[88:91], a[8:11], v[132:135]\n" \
"buffer_load_dwordx4 %[kd0], %[srdk], s48 offen offset:0 lds\n" \
"v_mfma_f32_16x16x32_bf16 v[132:135], a[92:95], a[12:15], v[132:135]\n" \
"v_exp_f32_e32 v159, v159\n" \
"v_mfma_f32_16x16x32_bf16 v[64:67], a[128:131], v[216:219], v[64:67]\n" \
"ds_read_b64_tr_b16 a[186:187], %[vvb] offset:46592\n" \
"ds_read_b64_tr_b16 a[188:189], v211 offset:42496\n" \
"v_mfma_f32_16x16x32_bf16 v[64:67], a[160:163], v[200:203], v[64:67]\n" \
"v_exp_f32_e64 v128, v128\n" \
"v_mfma_f32_16x16x32_bf16 v[68:71], a[132:135], v[216:219], v[68:71]\n" \
"v_cvt_pk_bf16_f32 v206, v156, v157\n" \
"v_cvt_pk_bf16_f32 v207, v158, v159\n" \
"v_mfma_f32_16x16x32_bf16 v[68:71], a[164:167], v[200:203], v[68:71]\n" \
"v_exp_f32_e64 v129, v129\n" \
"v_mfma_f32_16x16x32_bf16 v[72:75], a[136:139], v[216:219], v[72:75]\n" \
"ds_read_b64_tr_b16 a[190:191], v211 offset:46592\n" \
"ds_read_b64_tr_b16 a[128:129], %[vvb] offset:49152\n" \
"v_mfma_f32_16x16x32_bf16 v[72:75], a[168:171], v[200:203], v[72:75]\n" \
"v_exp_f32_e64 v130, v130\n" \
"v_mfma_f32_16x16x32_bf16 v[76:79], a[140:143], v[216:219], v[76:79]\n" \
"ds_read_b64_tr_b16 a[130:131], %[vvb] offset:53248\n" \
"ds_read_b64_tr_b16 a[132:133], v211 offset:49152\n" \
"v_mfma_f32_16x16x32_bf16 v[76:79], a[172:175], v[200:203], v[76:79]\n" \
"v_exp_f32_e64 v131, v131\n" \
"v_smfmac_f32_16x16x64_bf16 v[176:179], a[192:195], v[192:199], v228\n" \
"ds_read_b64_tr_b16 a[134:135], v211 offset:53248\n" \
"ds_read_b64_tr_b16 a[136:137], %[vvb] offset:49664\n" \
"v_mfma_f32_16x16x32_bf16 v[136:139], a[64:67], a[16:19], v[164:167]\n" \
"v_exp_f32_e64 v132, v132\n" \
"v_mfma_f32_16x16x32_bf16 v[136:139], a[68:71], a[20:23], v[136:139]\n" \
"ds_read_b64_tr_b16 a[138:139], %[vvb] offset:53760\n" \
"ds_read_b64_tr_b16 a[140:141], v211 offset:49664\n" \
"v_mfma_f32_16x16x32_bf16 v[136:139], a[72:75], a[24:27], v[136:139]\n" \
"v_exp_f32_e64 v133, v133\n" \
"v_mfma_f32_16x16x32_bf16 v[136:139], a[76:79], a[28:31], v[136:139]\n" \
"v_cvt_pk_bf16_f32 v184, v128, v129\n" \
"v_cvt_pk_bf16_f32 v185, v130, v131\n" \
"v_mfma_f32_16x16x32_bf16 v[140:143], a[80:83], a[16:19], v[164:167]\n" \
"v_exp_f32_e64 v134, v134\n" \
"v_mfma_f32_16x16x32_bf16 v[140:143], a[84:87], a[20:23], v[140:143]\n" \
"ds_read_b64_tr_b16 a[142:143], v211 offset:53760\n" \
"s_waitcnt lgkmcnt(8)\n" \
"v_mfma_f32_16x16x32_bf16 v[140:143], a[88:91], a[24:27], v[140:143]\n" \
"buffer_load_dwordx4 v212, %[srdk], s48 offen offset:1024 lds\n" \
"v_mfma_f32_16x16x32_bf16 v[140:143], a[92:95], a[28:31], v[140:143]\n" \
"v_exp_f32_e32 v135, v135\n" \
"v_mfma_f32_16x16x32_bf16 v[80:83], a[144:147], v[216:219], v[80:83]\n" \
"ds_read_b64_tr_b16 a[144:145], %[vvb] offset:50176\n" \
"ds_read_b64_tr_b16 a[146:147], %[vvb] offset:54272\n" \
"v_mfma_f32_16x16x32_bf16 v[80:83], a[176:179], v[200:203], v[80:83]\n" \
"v_exp_f32_e64 v136, v136\n" \
"v_mfma_f32_16x16x32_bf16 v[84:87], a[148:151], v[216:219], v[84:87]\n" \
"v_cvt_pk_bf16_f32 v186, v132, v133\n" \
"v_cvt_pk_bf16_f32 v187, v134, v135\n" \
"v_mfma_f32_16x16x32_bf16 v[84:87], a[180:183], v[200:203], v[84:87]\n" \
"v_exp_f32_e64 v137, v137\n" \
"v_mfma_f32_16x16x32_bf16 v[88:91], a[152:155], v[216:219], v[88:91]\n" \
"v_exp_f32_e64 v138, v138\n" \
"v_mfma_f32_16x16x32_bf16 v[88:91], a[184:187], v[200:203], v[88:91]\n" \
"ds_read_b64_tr_b16 a[148:149], v211 offset:50176\n" \
"ds_read_b64_tr_b16 a[150:151], v211 offset:54272\n" \
"v_mfma_f32_16x16x32_bf16 v[92:95], a[156:159], v[216:219], v[92:95]\n" \
"v_exp_f32_e64 v139, v139\n" \
"v_mfma_f32_16x16x32_bf16 v[92:95], a[188:191], v[200:203], v[92:95]\n" \
"ds_read_b64_tr_b16 a[152:153], %[vvb] offset:50688\n" \
"global_load_dword v237, %[vct], %[pct]\n" \
"v_mfma_f32_16x16x32_bf16 v[144:147], a[64:67], a[32:35], v[168:171]\n" \
"v_exp_f32_e64 v140, v140\n" \
"v_mfma_f32_16x16x32_bf16 v[144:147], a[68:71], a[36:39], v[144:147]\n" \
"ds_read_b64_tr_b16 a[154:155], %[vvb] offset:54784\n" \
"ds_read_b64_tr_b16 a[156:157], v211 offset:50688\n" \
"v_mfma_f32_16x16x32_bf16 v[144:147], a[72:75], a[40:43], v[144:147]\n" \
"v_exp_f32_e64 v141, v141\n" \
"v_mfma_f32_16x16x32_bf16 v[144:147], a[76:79], a[44:47], v[144:147]\n" \
"v_cvt_pk_bf16_f32 v188, v136, v137\n" \
"v_cvt_pk_bf16_f32 v189, v138, v139\n" \
"v_mfma_f32_16x16x32_bf16 v[148:151], a[80:83], a[32:35], v[168:171]\n" \
"v_exp_f32_e64 v142, v142\n" \
"v_mfma_f32_16x16x32_bf16 v[148:151], a[84:87], a[36:39], v[148:151]\n" \
"ds_read_b64_tr_b16 a[158:159], v211 offset:54784\n" \
"s_waitcnt lgkmcnt(8)\n" \
"v_mfma_f32_16x16x32_bf16 v[148:151], a[88:91], a[40:43], v[148:151]\n" \
"buffer_load_dwordx4 v213, %[srdk], s48 offen offset:2048 lds\n" \
"v_mfma_f32_16x16x32_bf16 v[148:151], a[92:95], a[44:47], v[148:151]\n" \
"v_exp_f32_e32 v143, v143\n" \
"v_mfma_f32_16x16x32_bf16 v[0:3], a[160:163], v[192:195], v[0:3]\n" \
"ds_read_b128 a[96:99], %[kb0] offset:57344\n" \
"v_mfma_f32_16x16x32_bf16 v[0:3], a[128:131], v[184:187], v[0:3]\n" \
"v_exp_f32_e64 v144, v144\n" \
"v_mfma_f32_16x16x32_bf16 v[4:7], a[164:167], v[192:195], v[4:7]\n" \
"v_cvt_pk_bf16_f32 v190, v140, v141\n" \
"v_cvt_pk_bf16_f32 v191, v142, v143\n" \
"v_mfma_f32_16x16x32_bf16 v[4:7], a[132:135], v[184:187], v[4:7]\n" \
"v_exp_f32_e64 v145, v145\n" \
"v_mfma_f32_16x16x32_bf16 v[8:11], a[168:171], v[192:195], v[8:11]\n" \
"ds_read_b128 a[100:103], v208 offset:57344\n" \
"v_mfma_f32_16x16x32_bf16 v[8:11], a[136:139], v[184:187], v[8:11]\n" \
"v_exp_f32_e64 v146, v146\n" \
"v_mfma_f32_16x16x32_bf16 v[12:15], a[172:175], v[192:195], v[12:15]\n" \
"ds_read_b128 a[104:107], v209 offset:57344\n" \
"v_mfma_f32_16x16x32_bf16 v[12:15], a[140:143], v[184:187], v[12:15]\n" \
"v_exp_f32_e64 v147, v147\n" \
"v_smfmac_f32_16x16x64_bf16 v[180:183], a[192:195], v[200:207], v228\n" \
"ds_read_b128 a[108:111], v210 offset:57344\n" \
"v_mfma_f32_16x16x32_bf16 v[152:155], a[64:67], a[48:51], v[172:175]\n" \
"v_exp_f32_e64 v148, v148\n" \
"v_mfma_f32_16x16x32_bf16 v[152:155], a[68:71], a[52:55], v[152:155]\n" \
"ds_read_b128 a[112:115], %[kb0] offset:61440\n" \
"v_mfma_f32_16x16x32_bf16 v[152:155], a[72:75], a[56:59], v[152:155]\n" \
"v_exp_f32_e64 v149, v149\n" \
"v_mfma_f32_16x16x32_bf16 v[152:155], a[76:79], a[60:63], v[152:155]\n" \
"v_cvt_pk_bf16_f32 v216, v144, v145\n" \
"v_cvt_pk_bf16_f32 v217, v146, v147\n" \
"v_mfma_f32_16x16x32_bf16 v[156:159], a[80:83], a[48:51], v[172:175]\n" \
"v_exp_f32_e64 v150, v150\n" \
"v_mfma_f32_16x16x32_bf16 v[156:159], a[84:87], a[52:55], v[156:159]\n" \
"ds_read_b128 a[116:119], v208 offset:61440\n" \
"s_waitcnt lgkmcnt(6)\n" \
"v_mfma_f32_16x16x32_bf16 v[156:159], a[88:91], a[56:59], v[156:159]\n" \
"buffer_load_dwordx4 v214, %[srdk], s48 offen offset:3072 lds\n" \
"v_mfma_f32_16x16x32_bf16 v[156:159], a[92:95], a[60:63], v[156:159]\n" \
"v_exp_f32_e32 v151, v151\n" \
"v_mfma_f32_16x16x32_bf16 v[16:19], a[176:179], v[192:195], v[16:19]\n" \
"ds_read_b128 a[120:123], v209 offset:61440\n" \
"v_mfma_f32_16x16x32_bf16 v[16:19], a[144:147], v[184:187], v[16:19]\n" \
"v_exp_f32_e64 v152, v152\n" \
"v_mfma_f32_16x16x32_bf16 v[20:23], a[180:183], v[192:195], v[20:23]\n" \
"v_cvt_pk_bf16_f32 v218, v148, v149\n" \
"v_cvt_pk_bf16_f32 v219, v150, v151\n" \
"v_mfma_f32_16x16x32_bf16 v[20:23], a[148:151], v[184:187], v[20:23]\n" \
"v_exp_f32_e64 v153, v153\n" \
"v_mfma_f32_16x16x32_bf16 v[24:27], a[184:187], v[192:195], v[24:27]\n" \
"v_exp_f32_e64 v154, v154\n" \
"v_mfma_f32_16x16x32_bf16 v[24:27], a[152:155], v[184:187], v[24:27]\n" \
"ds_read_b128 a[124:127], v210 offset:61440\n" \
"s_add_u32 s48, s48, 0x4000\n" \
"s_mov_b32 m0, s44\n" \
"s_addk_i32 s44, 0x400\n" \
"buffer_load_dwordx4 v229, s[64:67], s45 offen offset:0 nt lds\n" \
"s_add_u32 s45, s45, 393216\n" \
"v_mfma_f32_16x16x32_bf16 v[28:31], a[188:191], v[192:195], v[28:31]\n" \
"v_exp_f32_e64 v155, v155\n" \
"v_mfma_f32_16x16x32_bf16 v[28:31], a[156:159], v[184:187], v[28:31]\n" \
"s_waitcnt vmcnt(15) lgkmcnt(2)\n" \
"s_barrier\n" \
"v_mfma_f32_16x16x32_bf16 v[128:131], a[96:99], a[0:3], v[160:163]\n" \
"v_exp_f32_e64 v156, v156\n" \
"v_mfma_f32_16x16x32_bf16 v[128:131], a[100:103], a[4:7], v[128:131]\n" \
"s_add_u32 m0, %[m0b], 98304\n" \
"v_mfma_f32_16x16x32_bf16 v[128:131], a[104:107], a[8:11], v[128:131]\n" \
"v_exp_f32_e64 v157, v157\n" \
"v_mfma_f32_16x16x32_bf16 v[128:131], a[108:111], a[12:15], v[128:131]\n" \
"v_cvt_pk_bf16_f32 v220, v152, v153\n" \
"v_cvt_pk_bf16_f32 v221, v154, v155\n" \
"v_mfma_f32_16x16x32_bf16 v[132:135], a[112:115], a[0:3], v[160:163]\n" \
"v_exp_f32_e64 v158, v158\n" \
"v_mfma_f32_16x16x32_bf16 v[132:135], a[116:119], a[4:7], v[132:135]\n" \
"s_waitcnt lgkmcnt(0)\n" \
"v_mfma_f32_16x16x32_bf16 v[132:135], a[120:123], a[8:11], v[132:135]\n" \
"buffer_load_dwordx4 %[vd0], %[srdv], s48 offen offset:0 lds\n" \
"v_mfma_f32_16x16x32_bf16 v[132:135], a[124:127], a[12:15], v[132:135]\n" \
"v_exp_f32_e32 v159, v159\n" \
"v_mfma_f32_16x16x32_bf16 v[32:35], a[160:163], v[196:199], v[32:35]\n" \
"ds_read_b128 a[64:67], %[kb0] offset:0\n" \
"v_mfma_f32_16x16x32_bf16 v[32:35], a[128:131], v[188:191], v[32:35]\n" \
"v_exp_f32_e64 v128, v128\n" \
"v_mfma_f32_16x16x32_bf16 v[36:39], a[164:167], v[196:199], v[36:39]\n" \
"v_cvt_pk_bf16_f32 v222, v156, v157\n" \
"v_cvt_pk_bf16_f32 v223, v158, v159\n" \
"v_mfma_f32_16x16x32_bf16 v[36:39], a[132:135], v[188:191], v[36:39]\n" \
"v_exp_f32_e64 v129, v129\n" \
"v_mfma_f32_16x16x32_bf16 v[40:43], a[168:171], v[196:199], v[40:43]\n" \
"ds_read_b128 a[68:71], v208 offset:0\n" \
"v_mfma_f32_16x16x32_bf16 v[40:43], a[136:139], v[188:191], v[40:43]\n" \
"v_exp_f32_e64 v130, v130\n" \
"v_mfma_f32_16x16x32_bf16 v[44:47], a[172:175], v[196:199], v[44:47]\n" \
"ds_read_b128 a[72:75], v209 offset:0\n" \
"v_mfma_f32_16x16x32_bf16 v[44:47], a[140:143], v[188:191], v[44:47]\n" \
"v_exp_f32_e64 v131, v131\n" \
"v_smfmac_f32_16x16x64_bf16 v[176:179], a[192:195], v[184:191], v228\n" \
"v_mfma_f32_16x16x32_bf16 v[136:139], a[96:99], a[16:19], v[164:167]\n" \
"v_exp_f32_e64 v132, v132\n" \
"v_mfma_f32_16x16x32_bf16 v[136:139], a[100:103], a[20:23], v[136:139]\n" \
"ds_read_b128 a[76:79], v210 offset:0\n" \
"v_mfma_f32_16x16x32_bf16 v[136:139], a[104:107], a[24:27], v[136:139]\n" \
"v_exp_f32_e64 v133, v133\n" \
"v_mfma_f32_16x16x32_bf16 v[136:139], a[108:111], a[28:31], v[136:139]\n" \
"v_cvt_pk_bf16_f32 v192, v128, v129\n" \
"v_cvt_pk_bf16_f32 v193, v130, v131\n" \
"v_mfma_f32_16x16x32_bf16 v[140:143], a[112:115], a[16:19], v[164:167]\n" \
"v_exp_f32_e64 v134, v134\n" \
"v_mfma_f32_16x16x32_bf16 v[140:143], a[116:119], a[20:23], v[140:143]\n" \
"ds_read_b128 a[80:83], %[kb0] offset:4096\n" \
"v_mfma_f32_16x16x32_bf16 v[140:143], a[120:123], a[24:27], v[140:143]\n" \
"buffer_load_dwordx4 v215, %[srdv], s48 offen offset:1024 lds\n" \
"v_mfma_f32_16x16x32_bf16 v[140:143], a[124:127], a[28:31], v[140:143]\n" \
"v_exp_f32_e64 v135, v135\n" \
"v_mfma_f32_16x16x32_bf16 v[48:51], a[176:179], v[196:199], v[48:51]\n" \
"ds_read_b128 a[84:87], v208 offset:4096\n" \
"v_mfma_f32_16x16x32_bf16 v[48:51], a[144:147], v[188:191], v[48:51]\n" \
"v_exp_f32_e64 v136, v136\n" \
"v_mfma_f32_16x16x32_bf16 v[52:55], a[180:183], v[196:199], v[52:55]\n" \
"v_cvt_pk_bf16_f32 v194, v132, v133\n" \
"v_cvt_pk_bf16_f32 v195, v134, v135\n" \
"v_mfma_f32_16x16x32_bf16 v[52:55], a[148:151], v[188:191], v[52:55]\n" \
"v_exp_f32_e64 v137, v137\n" \
"v_mfma_f32_16x16x32_bf16 v[56:59], a[184:187], v[196:199], v[56:59]\n" \
"v_exp_f32_e64 v138, v138\n" \
"v_mfma_f32_16x16x32_bf16 v[56:59], a[152:155], v[188:191], v[56:59]\n" \
"ds_read_b128 a[88:91], v209 offset:4096\n" \
"v_mfma_f32_16x16x32_bf16 v[60:63], a[188:191], v[196:199], v[60:63]\n" \
"v_exp_f32_e64 v139, v139\n" \
"v_mfma_f32_16x16x32_bf16 v[60:63], a[156:159], v[188:191], v[60:63]\n" \
"s_cmp_lg_u32 %[wid], 2\n" \
"s_cbranch_scc1 LtA7_%=\n" \
"global_load_dword v237, v224, s[58:59]\n" \
"s_add_u32 s61, s48, 49152\n" \
"s_mov_b32 exec_hi, 0\n" \
"s_nop 0\n" \
"buffer_load_dword v237, v224, %[srdp], s61 offen\n" \
"s_mov_b32 exec_hi, -1\n" \
"s_nop 0\n" \
"LtA7_%=:\n" \
"v_mfma_f32_16x16x32_bf16 v[144:147], a[96:99], a[32:35], v[168:171]\n" \
"v_exp_f32_e64 v140, v140\n" \
"v_mfma_f32_16x16x32_bf16 v[144:147], a[100:103], a[36:39], v[144:147]\n" \
"ds_read_b128 a[92:95], v210 offset:4096\n" \
"v_mfma_f32_16x16x32_bf16 v[144:147], a[104:107], a[40:43], v[144:147]\n" \
"v_exp_f32_e64 v141, v141\n" \
"v_mfma_f32_16x16x32_bf16 v[144:147], a[108:111], a[44:47], v[144:147]\n" \
"v_cvt_pk_bf16_f32 v196, v136, v137\n" \
"v_cvt_pk_bf16_f32 v197, v138, v139\n" \
"v_mfma_f32_16x16x32_bf16 v[148:151], a[112:115], a[32:35], v[168:171]\n" \
"v_exp_f32_e64 v142, v142\n" \
"v_mfma_f32_16x16x32_bf16 v[148:151], a[116:119], a[36:39], v[148:151]\n" \
"v_mfma_f32_16x16x32_bf16 v[148:151], a[120:123], a[40:43], v[148:151]\n" \
"buffer_load_dwordx4 %[vd0], %[srdv], s48 offen offset:2048 lds\n" \
"v_mfma_f32_16x16x32_bf16 v[148:151], a[124:127], a[44:47], v[148:151]\n" \
"v_exp_f32_e64 v143, v143\n" \
"v_mfma_f32_16x16x32_bf16 v[96:99], a[160:163], v[204:207], v[96:99]\n" \
"ds_read_b64_tr_b16 a[160:161], %[vvb] offset:57344\n" \
"v_mfma_f32_16x16x32_bf16 v[96:99], a[128:131], v[220:223], v[96:99]\n" \
"v_exp_f32_e64 v144, v144\n" \
"v_mfma_f32_16x16x32_bf16 v[100:103], a[164:167], v[204:207], v[100:103]\n" \
"v_cvt_pk_bf16_f32 v198, v140, v141\n" \
"v_cvt_pk_bf16_f32 v199, v142, v143\n" \
"v_mfma_f32_16x16x32_bf16 v[100:103], a[132:135], v[220:223], v[100:103]\n" \
"v_exp_f32_e64 v145, v145\n" \
"v_mfma_f32_16x16x32_bf16 v[104:107], a[168:171], v[204:207], v[104:107]\n" \
"ds_read_b64_tr_b16 a[162:163], %[vvb] offset:61440\n" \
"ds_read_b64_tr_b16 a[164:165], v211 offset:57344\n" \
"v_mfma_f32_16x16x32_bf16 v[104:107], a[136:139], v[220:223], v[104:107]\n" \
"v_exp_f32_e64 v146, v146\n" \
"v_mfma_f32_16x16x32_bf16 v[108:111], a[172:175], v[204:207], v[108:111]\n" \
"ds_read_b64_tr_b16 a[166:167], v211 offset:61440\n" \
"v_mfma_f32_16x16x32_bf16 v[108:111], a[140:143], v[220:223], v[108:111]\n" \
"v_exp_f32_e64 v147, v147\n" \
"v_smfmac_f32_16x16x64_bf16 v[180:183], a[192:195], v[216:223], v228\n" \
"ds_read_b64_tr_b16 a[168:169], %[vvb] offset:57856\n" \
"v_mfma_f32_16x16x32_bf16 v[152:155], a[96:99], a[48:51], v[172:175]\n" \
"v_exp_f32_e64 v148, v148\n" \
"v_mfma_f32_16x16x32_bf16 v[152:155], a[100:103], a[52:55], v[152:155]\n" \
"ds_read_b64_tr_b16 a[170:171], %[vvb] offset:61952\n" \
"v_mfma_f32_16x16x32_bf16 v[152:155], a[104:107], a[56:59], v[152:155]\n" \
"v_exp_f32_e64 v149, v149\n" \
"v_mfma_f32_16x16x32_bf16 v[152:155], a[108:111], a[60:63], v[152:155]\n" \
"v_cvt_pk_bf16_f32 v200, v144, v145\n" \
"v_cvt_pk_bf16_f32 v201, v146, v147\n" \
"v_mfma_f32_16x16x32_bf16 v[156:159], a[112:115], a[48:51], v[172:175]\n" \
"v_exp_f32_e64 v150, v150\n" \
"v_mfma_f32_16x16x32_bf16 v[156:159], a[116:119], a[52:55], v[156:159]\n" \
"ds_read_b64_tr_b16 a[172:173], v211 offset:57856\n" \
"v_mfma_f32_16x16x32_bf16 v[156:159], a[120:123], a[56:59], v[156:159]\n" \
"buffer_load_dwordx4 v215, %[srdv], s48 offen offset:3072 lds\n" \
"v_mfma_f32_16x16x32_bf16 v[156:159], a[124:127], a[60:63], v[156:159]\n" \
"v_exp_f32_e64 v151, v151\n" \
"v_mfma_f32_16x16x32_bf16 v[112:115], a[176:179], v[204:207], v[112:115]\n" \
"ds_read_b64_tr_b16 a[174:175], v211 offset:61952\n" \
"ds_read_b64_tr_b16 a[176:177], %[vvb] offset:58368\n" \
"v_mfma_f32_16x16x32_bf16 v[112:115], a[144:147], v[220:223], v[112:115]\n" \
"v_exp_f32_e64 v152, v152\n" \
"v_mfma_f32_16x16x32_bf16 v[116:119], a[180:183], v[204:207], v[116:119]\n" \
"v_cvt_pk_bf16_f32 v202, v148, v149\n" \
"v_cvt_pk_bf16_f32 v203, v150, v151\n" \
"v_mfma_f32_16x16x32_bf16 v[116:119], a[148:151], v[220:223], v[116:119]\n" \
"v_exp_f32_e64 v153, v153\n" \
"v_mfma_f32_16x16x32_bf16 v[120:123], a[184:187], v[204:207], v[120:123]\n" \
"v_exp_f32_e64 v154, v154\n" \
"v_mfma_f32_16x16x32_bf16 v[120:123], a[152:155], v[220:223], v[120:123]\n" \
"ds_read_b64_tr_b16 a[178:179], %[vvb] offset:62464\n" \
"v_mfma_f32_16x16x32_bf16 v[124:127], a[188:191], v[204:207], v[124:127]\n" \
"v_exp_f32_e64 v155, v155\n" \
"v_mfma_f32_16x16x32_bf16 v[124:127], a[156:159], v[220:223], v[124:127]\n" \
"ds_read_b64_tr_b16 a[180:181], v211 offset:58368\n" \
"s_waitcnt lgkmcnt(13)\n" \
"v_mfma_f32_16x16x32_bf16 v[128:131], a[64:67], a[0:3], v[160:163]\n" \
"v_exp_f32_e32 v156, v156\n" \
"v_mfma_f32_16x16x32_bf16 v[128:131], a[68:71], a[4:7], v[128:131]\n" \
"ds_read_b64_tr_b16 a[182:183], v211 offset:62464\n" \
"s_add_u32 m0, %[m0b], 49152\n" \
"v_mfma_f32_16x16x32_bf16 v[128:131], a[72:75], a[8:11], v[128:131]\n" \
"v_exp_f32_e64 v157, v157\n" \
"v_mfma_f32_16x16x32_bf16 v[128:131], a[76:79], a[12:15], v[128:131]\n" \
"v_cvt_pk_bf16_f32 v204, v152, v153\n" \
"v_cvt_pk_bf16_f32 v205, v154, v155\n" \
"v_mfma_f32_16x16x32_bf16 v[132:135], a[80:83], a[0:3], v[160:163]\n" \
"v_exp_f32_e64 v158, v158\n" \
"v_mfma_f32_16x16x32_bf16 v[132:135], a[84:87], a[4:7], v[132:135]\n" \
"ds_read_b64_tr_b16 a[184:185], %[vvb] offset:58880\n" \
"s_waitcnt lgkmcnt(5)\n" \
"v_mfma_f32_16x16x32_bf16 v[132:135], a[88:91], a[8:11], v[132:135]\n" \
"buffer_load_dwordx4 %[kd0], %[srdk], s48 offen offset:0 lds\n" \
"v_mfma_f32_16x16x32_bf16 v[132:135], a[92:95], a[12:15], v[132:135]\n" \
"v_exp_f32_e32 v159, v159\n" \
"v_mfma_f32_16x16x32_bf16 v[64:67], a[128:131], v[216:219], v[64:67]\n" \
"ds_read_b64_tr_b16 a[186:187], %[vvb] offset:62976\n" \
"ds_read_b64_tr_b16 a[188:189], v211 offset:58880\n" \
"v_mfma_f32_16x16x32_bf16 v[64:67], a[160:163], v[200:203], v[64:67]\n" \
"v_exp_f32_e64 v128, v128\n" \
"v_mfma_f32_16x16x32_bf16 v[68:71], a[132:135], v[216:219], v[68:71]\n" \
"v_cvt_pk_bf16_f32 v206, v156, v157\n" \
"v_cvt_pk_bf16_f32 v207, v158, v159\n" \
"v_mfma_f32_16x16x32_bf16 v[68:71], a[164:167], v[200:203], v[68:71]\n" \
"v_exp_f32_e64 v129, v129\n" \
"v_mfma_f32_16x16x32_bf16 v[72:75], a[136:139], v[216:219], v[72:75]\n" \
"ds_read_b64_tr_b16 a[190:191], v211 offset:62976\n" \
"ds_read_b64_tr_b16 a[128:129], %[vvb] offset:0\n" \
"v_mfma_f32_16x16x32_bf16 v[72:75], a[168:171], v[200:203], v[72:75]\n" \
"v_exp_f32_e64 v130, v130\n" \
"v_mfma_f32_16x16x32_bf16 v[76:79], a[140:143], v[216:219], v[76:79]\n" \
"ds_read_b64_tr_b16 a[130:131], %[vvb] offset:4096\n" \
"ds_read_b64_tr_b16 a[132:133], v211 offset:0\n" \
"v_mfma_f32_16x16x32_bf16 v[76:79], a[172:175], v[200:203], v[76:79]\n" \
"v_exp_f32_e64 v131, v131\n" \
"v_smfmac_f32_16x16x64_bf16 v[176:179], a[192:195], v[192:199], v228\n" \
"ds_read_b64_tr_b16 a[134:135], v211 offset:4096\n" \
"ds_read_b64_tr_b16 a[136:137], %[vvb] offset:512\n" \
"v_mfma_f32_16x16x32_bf16 v[136:139], a[64:67], a[16:19], v[164:167]\n" \
"v_exp_f32_e64 v132, v132\n" \
"v_mfma_f32_16x16x32_bf16 v[136:139], a[68:71], a[20:23], v[136:139]\n" \
"ds_read_b64_tr_b16 a[138:139], %[vvb] offset:4608\n" \
"ds_read_b64_tr_b16 a[140:141], v211 offset:512\n" \
"v_mfma_f32_16x16x32_bf16 v[136:139], a[72:75], a[24:27], v[136:139]\n" \
"v_exp_f32_e64 v133, v133\n" \
"v_mfma_f32_16x16x32_bf16 v[136:139], a[76:79], a[28:31], v[136:139]\n" \
"v_cvt_pk_bf16_f32 v184, v128, v129\n" \
"v_cvt_pk_bf16_f32 v185, v130, v131\n" \
"v_mfma_f32_16x16x32_bf16 v[140:143], a[80:83], a[16:19], v[164:167]\n" \
"v_exp_f32_e64 v134, v134\n" \
"v_mfma_f32_16x16x32_bf16 v[140:143], a[84:87], a[20:23], v[140:143]\n" \
"ds_read_b64_tr_b16 a[142:143], v211 offset:4608\n" \
"s_waitcnt lgkmcnt(8)\n" \
"v_mfma_f32_16x16x32_bf16 v[140:143], a[88:91], a[24:27], v[140:143]\n" \
"buffer_load_dwordx4 v212, %[srdk], s48 offen offset:1024 lds\n" \
"v_mfma_f32_16x16x32_bf16 v[140:143], a[92:95], a[28:31], v[140:143]\n" \
"v_exp_f32_e32 v135, v135\n" \
"v_mfma_f32_16x16x32_bf16 v[80:83], a[144:147], v[216:219], v[80:83]\n" \
"ds_read_b64_tr_b16 a[144:145], %[vvb] offset:1024\n" \
"ds_read_b64_tr_b16 a[146:147], %[vvb] offset:5120\n" \
"v_mfma_f32_16x16x32_bf16 v[80:83], a[176:179], v[200:203], v[80:83]\n" \
"v_exp_f32_e64 v136, v136\n" \
"v_mfma_f32_16x16x32_bf16 v[84:87], a[148:151], v[216:219], v[84:87]\n" \
"v_cvt_pk_bf16_f32 v186, v132, v133\n" \
"v_cvt_pk_bf16_f32 v187, v134, v135\n" \
"v_mfma_f32_16x16x32_bf16 v[84:87], a[180:183], v[200:203], v[84:87]\n" \
"v_exp_f32_e64 v137, v137\n" \
"v_mfma_f32_16x16x32_bf16 v[88:91], a[152:155], v[216:219], v[88:91]\n" \
"v_exp_f32_e64 v138, v138\n" \
"v_mfma_f32_16x16x32_bf16 v[88:91], a[184:187], v[200:203], v[88:91]\n" \
"ds_read_b64_tr_b16 a[148:149], v211 offset:1024\n" \
"ds_read_b64_tr_b16 a[150:151], v211 offset:5120\n" \
"v_mfma_f32_16x16x32_bf16 v[92:95], a[156:159], v[216:219], v[92:95]\n" \
"v_exp_f32_e64 v139, v139\n" \
"v_mfma_f32_16x16x32_bf16 v[92:95], a[188:191], v[200:203], v[92:95]\n" \
"ds_read_b64_tr_b16 a[152:153], %[vvb] offset:1536\n" \
"buffer_store_dwordx4 a[196:199], v235, %[srdz], s62 offen offset:1024 sc1\n" \
"s_add_u32 s62, s62, 2048\n" \
"v_mfma_f32_16x16x32_bf16 v[144:147], a[64:67], a[32:35], v[168:171]\n" \
"v_exp_f32_e64 v140, v140\n" \
"v_mfma_f32_16x16x32_bf16 v[144:147], a[68:71], a[36:39], v[144:147]\n" \
"ds_read_b64_tr_b16 a[154:155], %[vvb] offset:5632\n" \
"ds_read_b64_tr_b16 a[156:157], v211 offset:1536\n" \
"v_mfma_f32_16x16x32_bf16 v[144:147], a[72:75], a[40:43], v[144:147]\n" \
"v_exp_f32_e64 v141, v141\n" \
"v_mfma_f32_16x16x32_bf16 v[144:147], a[76:79], a[44:47], v[144:147]\n" \
"v_cvt_pk_bf16_f32 v188, v136, v137\n" \
"v_cvt_pk_bf16_f32 v189, v138, v139\n" \
"v_mfma_f32_16x16x32_bf16 v[148:151], a[80:83], a[32:35], v[168:171]\n" \
"v_exp_f32_e64 v142, v142\n" \
"v_mfma_f32_16x16x32_bf16 v[148:151], a[84:87], a[36:39], v[148:151]\n" \
"ds_read_b64_tr_b16 a[158:159], v211 offset:5632\n" \
"s_waitcnt lgkmcnt(8)\n" \
"v_mfma_f32_16x16x32_bf16 v[148:151], a[88:91], a[40:43], v[148:151]\n" \
"buffer_load_dwordx4 v213, %[srdk], s48 offen offset:2048 lds\n" \
"v_mfma_f32_16x16x32_bf16 v[148:151], a[92:95], a[44:47], v[148:151]\n" \
"v_exp_f32_e32 v143, v143\n" \
"v_mfma_f32_16x16x32_bf16 v[0:3], a[160:163], v[192:195], v[0:3]\n" \
"ds_read_b128 a[96:99], %[kb0] offset:8192\n" \
"v_mfma_f32_16x16x32_bf16 v[0:3], a[128:131], v[184:187], v[0:3]\n" \
"v_exp_f32_e64 v144, v144\n" \
"v_mfma_f32_16x16x32_bf16 v[4:7], a[164:167], v[192:195], v[4:7]\n" \
"v_cvt_pk_bf16_f32 v190, v140, v141\n" \
"v_cvt_pk_bf16_f32 v191, v142, v143\n" \
"v_mfma_f32_16x16x32_bf16 v[4:7], a[132:135], v[184:187], v[4:7]\n" \
"v_exp_f32_e64 v145, v145\n" \
"v_mfma_f32_16x16x32_bf16 v[8:11], a[168:171], v[192:195], v[8:11]\n" \
"ds_read_b128 a[100:103], v208 offset:8192\n" \
"v_mfma_f32_16x16x32_bf16 v[8:11], a[136:139], v[184:187], v[8:11]\n" \
"v_exp_f32_e64 v146, v146\n" \
"v_mfma_f32_16x16x32_bf16 v[12:15], a[172:175], v[192:195], v[12:15]\n" \
"ds_read_b128 a[104:107], v209 offset:8192\n" \
"v_mfma_f32_16x16x32_bf16 v[12:15], a[140:143], v[184:187], v[12:15]\n" \
"v_exp_f32_e64 v147, v147\n" \
"v_smfmac_f32_16x16x64_bf16 v[180:183], a[192:195], v[200:207], v228\n" \
"ds_read_b128 a[108:111], v210 offset:8192\n" \
"v_mfma_f32_16x16x32_bf16 v[152:155], a[64:67], a[48:51], v[172:175]\n" \
"v_exp_f32_e64 v148, v148\n" \
"v_mfma_f32_16x16x32_bf16 v[152:155], a[68:71], a[52:55], v[152:155]\n" \
"ds_read_b128 a[112:115], %[kb0] offset:12288\n" \
"v_mfma_f32_16x16x32_bf16 v[152:155], a[72:75], a[56:59], v[152:155]\n" \
"v_exp_f32_e64 v149, v149\n" \
"v_mfma_f32_16x16x32_bf16 v[152:155], a[76:79], a[60:63], v[152:155]\n" \
"v_cvt_pk_bf16_f32 v216, v144, v145\n" \
"v_cvt_pk_bf16_f32 v217, v146, v147\n" \
"v_mfma_f32_16x16x32_bf16 v[156:159], a[80:83], a[48:51], v[172:175]\n" \
"v_exp_f32_e64 v150, v150\n" \
"v_mfma_f32_16x16x32_bf16 v[156:159], a[84:87], a[52:55], v[156:159]\n" \
"ds_read_b128 a[116:119], v208 offset:12288\n" \
"s_waitcnt lgkmcnt(6)\n" \
"v_mfma_f32_16x16x32_bf16 v[156:159], a[88:91], a[56:59], v[156:159]\n" \
"buffer_load_dwordx4 v214, %[srdk], s48 offen offset:3072 lds\n" \
"v_mfma_f32_16x16x32_bf16 v[156:159], a[92:95], a[60:63], v[156:159]\n" \
"v_exp_f32_e32 v151, v151\n" \
"v_mfma_f32_16x16x32_bf16 v[16:19], a[176:179], v[192:195], v[16:19]\n" \
"ds_read_b128 a[120:123], v209 offset:12288\n" \
"v_mfma_f32_16x16x32_bf16 v[16:19], a[144:147], v[184:187], v[16:19]\n" \
"v_exp_f32_e64 v152, v152\n" \
"v_mfma_f32_16x16x32_bf16 v[20:23], a[180:183], v[192:195], v[20:23]\n" \
"v_cvt_pk_bf16_f32 v218, v148, v149\n" \
"v_cvt_pk_bf16_f32 v219, v150, v151\n" \
"v_mfma_f32_16x16x32_bf16 v[20:23], a[148:151], v[184:187], v[20:23]\n" \
"v_exp_f32_e64 v153, v153\n" \
"v_mfma_f32_16x16x32_bf16 v[24:27], a[184:187], v[192:195], v[24:27]\n" \
"v_exp_f32_e64 v154, v154\n" \
"v_mfma_f32_16x16x32_bf16 v[24:27], a[152:155], v[184:187], v[24:27]\n" \
"ds_read_b128 a[124:127], v210 offset:12288\n" \
"s_add_u32 s48, s48, 0x4000\n" \
"v_mfma_f32_16x16x32_bf16 v[28:31], a[188:191], v[192:195], v[28:31]\n" \
"v_exp_f32_e64 v155, v155\n" \
"v_mfma_f32_16x16x32_bf16 v[28:31], a[156:159], v[184:187], v[28:31]\n" \
"s_waitcnt vmcnt(15) lgkmcnt(2)\n" \
"s_barrier\n" \
"v_mfma_f32_16x16x32_bf16 v[128:131], a[96:99], a[0:3], v[160:163]\n" \
"v_exp_f32_e64 v156, v156\n" \
"v_mfma_f32_16x16x32_bf16 v[128:131], a[100:103], a[4:7], v[128:131]\n" \
"s_add_u32 m0, %[m0b], 114688\n" \
"v_mfma_f32_16x16x32_bf16 v[128:131], a[104:107], a[8:11], v[128:131]\n" \
"v_exp_f32_e64 v157, v157\n" \
"v_mfma_f32_16x16x32_bf16 v[128:131], a[108:111], a[12:15], v[128:131]\n" \
"v_cvt_pk_bf16_f32 v220, v152, v153\n" \
"v_cvt_pk_bf16_f32 v221, v154, v155\n" \
"v_mfma_f32_16x16x32_bf16 v[132:135], a[112:115], a[0:3], v[160:163]\n" \
"v_exp_f32_e64 v158, v158\n" \
"v_mfma_f32_16x16x32_bf16 v[132:135], a[116:119], a[4:7], v[132:135]\n" \
"s_waitcnt lgkmcnt(0)\n" \
"v_mfma_f32_16x16x32_bf16 v[132:135], a[120:123], a[8:11], v[132:135]\n" \
"buffer_load_dwordx4 %[vd0], %[srdv], s48 offen offset:0 lds\n" \
"v_mfma_f32_16x16x32_bf16 v[132:135], a[124:127], a[12:15], v[132:135]\n" \
"v_exp_f32_e32 v159, v159\n" \
"v_mfma_f32_16x16x32_bf16 v[32:35], a[160:163], v[196:199], v[32:35]\n" \
"ds_read_b128 a[64:67], %[kb0] offset:16384\n" \
"v_mfma_f32_16x16x32_bf16 v[32:35], a[128:131], v[188:191], v[32:35]\n" \
"v_exp_f32_e64 v128, v128\n" \
"v_mfma_f32_16x16x32_bf16 v[36:39], a[164:167], v[196:199], v[36:39]\n" \
"v_cvt_pk_bf16_f32 v222, v156, v157\n" \
"v_cvt_pk_bf16_f32 v223, v158, v159\n" \
"v_mfma_f32_16x16x32_bf16 v[36:39], a[132:135], v[188:191], v[36:39]\n" \
"v_exp_f32_e64 v129, v129\n" \
"v_mfma_f32_16x16x32_bf16 v[40:43], a[168:171], v[196:199], v[40:43]\n" \
"ds_read_b128 a[68:71], v208 offset:16384\n" \
"v_mfma_f32_16x16x32_bf16 v[40:43], a[136:139], v[188:191], v[40:43]\n" \
"v_exp_f32_e64 v130, v130\n" \
"v_mfma_f32_16x16x32_bf16 v[44:47], a[172:175], v[196:199], v[44:47]\n" \
"ds_read_b128 a[72:75], v209 offset:16384\n" \
"v_mfma_f32_16x16x32_bf16 v[44:47], a[140:143], v[188:191], v[44:47]\n" \
"v_exp_f32_e64 v131, v131\n" \
"v_smfmac_f32_16x16x64_bf16 v[176:179], a[192:195], v[184:191], v228\n" \
"v_mfma_f32_16x16x32_bf16 v[136:139], a[96:99], a[16:19], v[164:167]\n" \
"v_exp_f32_e64 v132, v132\n" \
"v_mfma_f32_16x16x32_bf16 v[136:139], a[100:103], a[20:23], v[136:139]\n" \
"ds_read_b128 a[76:79], v210 offset:16384\n" \
"v_mfma_f32_16x16x32_bf16 v[136:139], a[104:107], a[24:27], v[136:139]\n" \
"v_exp_f32_e64 v133, v133\n" \
"v_mfma_f32_16x16x32_bf16 v[136:139], a[108:111], a[28:31], v[136:139]\n" \
"v_cvt_pk_bf16_f32 v192, v128, v129\n" \
"v_cvt_pk_bf16_f32 v193, v130, v131\n" \
"v_mfma_f32_16x16x32_bf16 v[140:143], a[112:115], a[16:19], v[164:167]\n" \
"v_exp_f32_e64 v134, v134\n" \
"v_mfma_f32_16x16x32_bf16 v[140:143], a[116:119], a[20:23], v[140:143]\n" \
"ds_read_b128 a[80:83], %[kb0] offset:20480\n" \
"v_mfma_f32_16x16x32_bf16 v[140:143], a[120:123], a[24:27], v[140:143]\n" \
"buffer_load_dwordx4 v215, %[srdv], s48 offen offset:1024 lds\n" \
"v_mfma_f32_16x16x32_bf16 v[140:143], a[124:127], a[28:31], v[140:143]\n" \
".if . - adaln_body != 20480\n" \
".error \"size model\"\n" \
".endif\n" \
"Lk4_%=:\n" \
"s_cbranch_execz Lk5_%=\n" \
"s_nop 0\n" \
"v_exp_f32_e64 v135, v135\n" \
"v_mfma_f32_16x16x32_bf16 v[48:51], a[176:179], v[196:199], v[48:51]\n" \
"ds_read_b128 a[84:87], v208 offset:20480\n" \
"v_mfma_f32_16x16x32_bf16 v[48:51], a[144:147], v[188:191], v[48:51]\n" \
"v_exp_f32_e64 v136, v136\n" \
"v_mfma_f32_16x16x32_bf16 v[52:55], a[180:183], v[196:199], v[52:55]\n" \
"v_cvt_pk_bf16_f32 v194, v132, v133\n" \
"v_cvt_pk_bf16_f32 v195, v134, v135\n" \
"v_mfma_f32_16x16x32_bf16 v[52:55], a[148:151], v[188:191], v[52:55]\n" \
"v_exp_f32_e64 v137, v137\n" \
"v_mfma_f32_16x16x32_bf16 v[56:59], a[184:187], v[196:199], v[56:59]\n" \
"v_exp_f32_e64 v138, v138\n" \
"v_mfma_f32_16x16x32_bf16 v[56:59], a[152:155], v[188:191], v[56:59]\n" \
"ds_read_b128 a[88:91], v209 offset:20480\n" \
"v_mfma_f32_16x16x32_bf16 v[60:63], a[188:191], v[196:199], v[60:63]\n" \
"v_exp_f32_e64 v139, v139\n" \
"v_mfma_f32_16x16x32_bf16 v[60:63], a[156:159], v[188:191], v[60:63]\n" \
"s_cmp_lg_u32 %[wid], 3\n" \
"s_cbranch_scc1 LtA1_%=\n" \
"global_load_dword v237, v224, s[58:59]\n" \
"s_add_u32 s61, s48, 49152\n" \
"s_mov_b32 exec_hi, 0\n" \
"s_nop 0\n" \
"buffer_load_dword v237, v224, %[srdp], s61 offen\n" \
"s_mov_b32 exec_hi, -1\n" \
"s_nop 0\n" \
"LtA1_%=:\n" \
"v_mfma_f32_16x16x32_bf16 v[144:147], a[96:99], a[32:35], v[168:171]\n" \
"v_exp_f32_e64 v140, v140\n" \
"v_mfma_f32_16x16x32_bf16 v[144:147], a[100:103], a[36:39], v[144:147]\n" \
"ds_read_b128 a[92:95], v210 offset:20480\n" \
"v_mfma_f32_16x16x32_bf16 v[144:147], a[104:107], a[40:43], v[144:147]\n" \
"v_exp_f32_e64 v141, v141\n" \
"v_mfma_f32_16x16x32_bf16 v[144:147], a[108:111], a[44:47], v[144:147]\n" \
"v_cvt_pk_bf16_f32 v196, v136, v137\n" \
"v_cvt_pk_bf16_f32 v197, v138, v139\n" \
"v_mfma_f32_16x16x32_bf16 v[148:151], a[112:115], a[32:35], v[168:171]\n" \
"v_exp_f32_e64 v142, v142\n" \
"v_mfma_f32_16x16x32_bf16 v[148:151], a[116:119], a[36:39], v[148:151]\n" \
"v_mfma_f32_16x16x32_bf16 v[148:151], a[120:123], a[40:43], v[148:151]\n" \
"buffer_load_dwordx4 %[vd0], %[srdv], s48 offen offset:2048 lds\n" \
"v_mfma_f32_16x16x32_bf16 v[148:151], a[124:127], a[44:47], v[148:151]\n" \
"v_exp_f32_e64 v143, v143\n" \
"v_mfma_f32_16x16x32_bf16 v[96:99], a[160:163], v[204:207], v[96:99]\n" \
"ds_read_b64_tr_b16 a[160:161], %[vvb] offset:8192\n" \
"v_mfma_f32_16x16x32_bf16 v[96:99], a[128:131], v[220:223], v[96:99]\n" \
"v_exp_f32_e64 v144, v144\n" \
"v_mfma_f32_16x16x32_bf16 v[100:103], a[164:167], v[204:207], v[100:103]\n" \
"v_cvt_pk_bf16_f32 v198, v140, v141\n" \
"v_cvt_pk_bf16_f32 v199, v142, v143\n" \
"v_mfma_f32_16x16x32_bf16 v[100:103], a[132:135], v[220:223], v[100:103]\n" \
"v_exp_f32_e64 v145, v145\n" \
"v_mfma_f32_16x16x32_bf16 v[104:107], a[168:171], v[204:207], v[104:107]\n" \
"ds_read_b64_tr_b16 a[162:163], %[vvb] offset:12288\n" \
"ds_read_b64_tr_b16 a[164:165], v211 offset:8192\n" \
"v_mfma_f32_16x16x32_bf16 v[104:107], a[136:139], v[220:223], v[104:107]\n" \
"v_exp_f32_e64 v146, v146\n" \
"v_mfma_f32_16x16x32_bf16 v[108:111], a[172:175], v[204:207], v[108:111]\n" \
"ds_read_b64_tr_b16 a[166:167], v211 offset:12288\n" \
"v_mfma_f32_16x16x32_bf16 v[108:111], a[140:143], v[220:223], v[108:111]\n" \
"v_exp_f32_e64 v147, v147\n" \
"v_smfmac_f32_16x16x64_bf16 v[180:183], a[192:195], v[216:223], v228\n" \
"ds_read_b64_tr_b16 a[168:169], %[vvb] offset:8704\n" \
"v_mfma_f32_16x16x32_bf16 v[152:155], a[96:99], a[48:51], v[172:175]\n" \
"v_exp_f32_e64 v148, v148\n" \
"v_mfma_f32_16x16x32_bf16 v[152:155], a[100:103], a[52:55], v[152:155]\n" \
"ds_read_b64_tr_b16 a[170:171], %[vvb] offset:12800\n" \
"v_mfma_f32_16x16x32_bf16 v[152:155], a[104:107], a[56:59], v[152:155]\n" \
"v_exp_f32_e64 v149, v149\n" \
"v_mfma_f32_16x16x32_bf16 v[152:155], a[108:111], a[60:63], v[152:155]\n" \
"v_cvt_pk_bf16_f32 v200, v144, v145\n" \
"v_cvt_pk_bf16_f32 v201, v146, v147\n" \
"v_mfma_f32_16x16x32_bf16 v[156:159], a[112:115], a[48:51], v[172:175]\n" \
"v_exp_f32_e64 v150, v150\n" \
"v_mfma_f32_16x16x32_bf16 v[156:159], a[116:119], a[52:55], v[156:159]\n" \
"ds_read_b64_tr_b16 a[172:173], v211 offset:8704\n" \
"v_mfma_f32_16x16x32_bf16 v[156:159], a[120:123], a[56:59], v[156:159]\n" \
"buffer_load_dwordx4 v215, %[srdv], s48 offen offset:3072 lds\n" \
"v_mfma_f32_16x16x32_bf16 v[156:159], a[124:127], a[60:63], v[156:159]\n" \
"v_exp_f32_e64 v151, v151\n" \
"v_mfma_f32_16x16x32_bf16 v[112:115], a[176:179], v[204:207], v[112:115]\n" \
"ds_read_b64_tr_b16 a[174:175], v211 offset:12800\n" \
"ds_read_b64_tr_b16 a[176:177], %[vvb] offset:9216\n" \
"v_mfma_f32_16x16x32_bf16 v[112:115], a[144:147], v[220:223], v[112:115]\n" \
"v_exp_f32_e64 v152, v152\n" \
"v_mfma_f32_16x16x32_bf16 v[116:119], a[180:183], v[204:207], v[116:119]\n" \
"v_cvt_pk_bf16_f32 v202, v148, v149\n" \
"v_cvt_pk_bf16_f32 v203, v150, v151\n" \
"v_mfma_f32_16x16x32_bf16 v[116:119], a[148:151], v[220:223], v[116:119]\n" \
"v_exp_f32_e64 v153, v153\n" \
"v_mfma_f32_16x16x32_bf16 v[120:123], a[184:187], v[204:207], v[120:123]\n" \
"v_exp_f32_e64 v154, v154\n" \
"v_mfma_f32_16x16x32_bf16 v[120:123], a[152:155], v[220:223], v[120:123]\n" \
"ds_read_b64_tr_b16 a[178:179], %[vvb] offset:13312\n" \
"v_mfma_f32_16x16x32_bf16 v[124:127], a[188:191], v[204:207], v[124:127]\n" \
"v_exp_f32_e64 v155, v155\n" \
"v_mfma_f32_16x16x32_bf16 v[124:127], a[156:159], v[220:223], v[124:127]\n" \
"ds_read_b64_tr_b16 a[180:181], v211 offset:9216\n" \
"s_waitcnt lgkmcnt(13)\n" \
"s_cmp_lg_u32 s60, 4\n" \
"s_cbranch_scc1 LmA_%=\n" \
"s_nop 0\n" \
"v_readlane_b32 s64, %[tlo], 9\n" \
"v_readlane_b32 s65, %[thi], 9\n" \
"s_mov_b32 s66, %[wbytes]\n" \
"v_mov_b32_e32 v229, %[vg0]\n" \
"s_mov_b32 s44, %[m0t]\n" \
"s_mov_b32 s45, 0\n" \
"s_cmp_lg_u32 s52, 0\n" \
"s_cbranch_scc1 LmB_%=\n" \
"s_add_u32 s62, %[sin], 8192\n" \
"s_add_u32 s63, %[sin], 28672\n" \
"s_waitcnt vmcnt(13)\n" \
"s_nop 0\n" \
"ds_read_b64_tr_b16 a[232:233], %[ta0] offset:0\n" \
"ds_read_b64_tr_b16 a[234:235], %[ta0] offset:128\n" \
"ds_read_b64_tr_b16 a[236:237], %[ta0] offset:1024\n" \
"ds_read_b64_tr_b16 a[238:239], %[ta0] offset:1152\n" \
"ds_read_b64_tr_b16 a[240:241], %[ta0] offset:2048\n" \
"ds_read_b64_tr_b16 a[242:243], %[ta0] offset:2176\n" \
"ds_read_b64_tr_b16 a[244:245], %[ta0] offset:3072\n" \
"ds_read_b64_tr_b16 a[246:247], %[ta0] offset:3200\n" \
"ds_read_b64_tr_b16 a[248:249], %[ta0] offset:4096\n" \
"ds_read_b64_tr_b16 a[250:251], %[ta0] offset:4224\n" \
"ds_read_b64_tr_b16 a[252:253], %[ta0] offset:5120\n" \
"ds_read_b64_tr_b16 a[254:255], %[ta0] offset:5248\n" \
"v_add_u32_e32 v234, %[m0t], v235\n" \
"s_waitcnt lgkmcnt(8)\n" \
"v_mfma_f32_16x16x32_bf16 v[230:233], a[232:235], a[200:203], 0\n" \
"v_mfma_f32_16x16x32_bf16 v[230:233], a[236:239], a[204:207], v[230:233]\n" \
"ds_read_b64_tr_b16 a[232:233], %[ta0] offset:6144\n" \
"ds_read_b64_tr_b16 a[234:235], %[ta0] offset:6272\n" \
"ds_read_b64_tr_b16 a[236:237], %[ta0] offset:7168\n" \
"ds_read_b64_tr_b16 a[238:239], %[ta0] offset:7296\n" \
"s_waitcnt lgkmcnt(4)\n" \
"s_nop 0\n" \
"v_mfma_f32_16x16x32_bf16 v[230:233], a[240:243], a[208:211], v[230:233]\n" \
"v_mfma_f32_16x16x32_bf16 v[230:233], a[244:247], a[212:215], v[230:233]\n" \
"v_mfma_f32_16x16x32_bf16 v[230:233], a[248:251], a[216:219], v[230:233]\n" \
"v_mfma_f32_16x16x32_bf16 v[230:233], a[252:255], a[220:223], v[230:233]\n" \
"s_waitcnt lgkmcnt(0)\n" \
"s_nop 0\n" \
"v_mfma_f32_16x16x32_bf16 v[230:233], a[232:235], a[224:227], v[230:233]\n" \
"v_mfma_f32_16x16x32_bf16 v[230:233], a[236:239], a[228:231], v[230:233]\n" \
"s_nop 7\n" \
"s_nop 0\n" \
"ds_write_b128 v234, v[230:233] offset:7168\n" \
"LmB_%=:\n" \
"v_mfma_f32_16x16x32_bf16 v[128:131], a[64:67], a[0:3], v[160:163]\n" \
"v_exp_f32_e64 v156, v156\n" \
"v_mfma_f32_16x16x32_bf16 v[128:131], a[68:71], a[4:7], v[128:131]\n" \
"ds_read_b64_tr_b16 a[182:183], v211 offset:13312\n" \
"s_add_u32 m0, %[m0b], 0\n" \
"v_mfma_f32_16x16x32_bf16 v[128:131], a[72:75], a[8:11], v[128:131]\n" \
"v_exp_f32_e32 v157, v157\n" \
"v_mfma_f32_16x16x32_bf16 v[128:131], a[76:79], a[12:15], v[128:131]\n" \
"v_cvt_pk_bf16_f32 v204, v152, v153\n" \
"v_cvt_pk_bf16_f32 v205, v154, v155\n" \
"v_mfma_f32_16x16x32_bf16 v[132:135], a[80:83], a[0:3], v[160:163]\n" \
"v_exp_f32_e64 v158, v158\n" \
"v_mfma_f32_16x16x32_bf16 v[132:135], a[84:87], a[4:7], v[132:135]\n" \
"ds_read_b64_tr_b16 a[184:185], %[vvb] offset:9728\n" \
"s_waitcnt lgkmcnt(5)\n" \
"v_mfma_f32_16x16x32_bf16 v[132:135], a[88:91], a[8:11], v[132:135]\n" \
"buffer_load_dwordx4 %[kd0], %[srdk], s48 offen offset:0 lds\n" \
"v_mfma_f32_16x16x32_bf16 v[132:135], a[92:95], a[12:15], v[132:135]\n" \
"v_exp_f32_e32 v159, v159\n" \
"v_mfma_f32_16x16x32_bf16 v[64:67], a[128:131], v[216:219], v[64:67]\n" \
"ds_read_b64_tr_b16 a[186:187], %[vvb] offset:13824\n" \
"ds_read_b64_tr_b16 a[188:189], v211 offset:9728\n" \
"v_mfma_f32_16x16x32_bf16 v[64:67], a[160:163], v[200:203], v[64:67]\n" \
"v_exp_f32_e64 v128, v128\n" \
"v_mfma_f32_16x16x32_bf16 v[68:71], a[132:135], v[216:219], v[68:71]\n" \
"v_cvt_pk_bf16_f32 v206, v156, v157\n" \
"v_cvt_pk_bf16_f32 v207, v158, v159\n" \
"v_mfma_f32_16x16x32_bf16 v[68:71], a[164:167], v[200:203], v[68:71]\n" \
"v_exp_f32_e64 v129, v129\n" \
"v_mfma_f32_16x16x32_bf16 v[72:75], a[136:139], v[216:219], v[72:75]\n" \
"ds_read_b64_tr_b16 a[190:191], v211 offset:13824\n" \
"ds_read_b64_tr_b16 a[128:129], %[vvb] offset:16384\n" \
"s_add_u32 s60, s60, 1\n" \
"s_nop 0\n" \
"v_readlane_b32 s58, %[tlo], s60\n" \
"v_mfma_f32_16x16x32_bf16 v[72:75], a[168:171], v[200:203], v[72:75]\n" \
"v_exp_f32_e64 v130, v130\n" \
"v_mfma_f32_16x16x32_bf16 v[76:79], a[140:143], v[216:219], v[76:79]\n" \
"ds_read_b64_tr_b16 a[130:131], %[vvb] offset:20480\n" \
"ds_read_b64_tr_b16 a[132:133], v211 offset:16384\n" \
"v_readlane_b32 s59, %[thi], s60\n" \
"v_mfma_f32_16x16x32_bf16 v[76:79], a[172:175], v[200:203], v[76:79]\n" \
"v_exp_f32_e64 v131, v131\n" \
"v_smfmac_f32_16x16x64_bf16 v[176:179], a[192:195], v[192:199], v228\n" \
"ds_read_b64_tr_b16 a[134:135], v211 offset:20480\n" \
"ds_read_b64_tr_b16 a[136:137], %[vvb] offset:16896\n" \
"s_cmp_eq_u32 s60, 7\n" \
"s_cselect_b32 s61, 56, 1\n" \
"v_mul_u32_u24_e64 v236, s61, v224\n" \
"v_mfma_f32_16x16x32_bf16 v[136:139], a[64:67], a[16:19], v[164:167]\n" \
"v_exp_f32_e64 v132, v132\n" \
"v_mfma_f32_16x16x32_bf16 v[136:139], a[68:71], a[20:23], v[136:139]\n" \
"ds_read_b64_tr_b16 a[138:139], %[vvb] offset:20992\n" \
"ds_read_b64_tr_b16 a[140:141], v211 offset:16896\n" \
"v_mfma_f32_16x16x32_bf16 v[136:139], a[72:75], a[24:27], v[136:139]\n" \
"v_exp_f32_e64 v133, v133\n" \
"v_mfma_f32_16x16x32_bf16 v[136:139], a[76:79], a[28:31], v[136:139]\n" \
"v_cvt_pk_bf16_f32 v184, v128, v129\n" \
"v_cvt_pk_bf16_f32 v185, v130, v131\n" \
"v_mfma_f32_16x16x32_bf16 v[140:143], a[80:83], a[16:19], v[164:167]\n" \
"v_exp_f32_e64 v134, v134\n" \
"v_mfma_f32_16x16x32_bf16 v[140:143], a[84:87], a[20:23], v[140:143]\n" \
"ds_read_b64_tr_b16 a[142:143], v211 offset:20992\n" \
"s_waitcnt lgkmcnt(8)\n" \
"v_mfma_f32_16x16x32_bf16 v[140:143], a[88:91], a[24:27], v[140:143]\n" \
"buffer_load_dwordx4 v212, %[srdk], s48 offen offset:1024 lds\n" \
"v_mfma_f32_16x16x32_bf16 v[140:143], a[92:95], a[28:31], v[140:143]\n" \
"v_exp_f32_e32 v135, v135\n" \
"v_mfma_f32_16x16x32_bf16 v[80:83], a[144:147], v[216:219], v[80:83]\n" \
"ds_read_b64_tr_b16 a[144:145], %[vvb] offset:17408\n" \
"ds_read_b64_tr_b16 a[146:147], %[vvb] offset:21504\n" \
"v_mfma_f32_16x16x32_bf16 v[80:83], a[176:179], v[200:203], v[80:83]\n" \
"v_exp_f32_e64 v136, v136\n" \
"v_mfma_f32_16x16x32_bf16 v[84:87], a[148:151], v[216:219], v[84:87]\n" \
"v_cvt_pk_bf16_f32 v186, v132, v133\n" \
"v_cvt_pk_bf16_f32 v187, v134, v135\n" \
"v_mfma_f32_16x16x32_bf16 v[84:87], a[180:183], v[200:203], v[84:87]\n" \
"v_exp_f32_e64 v137, v137\n" \
"v_mfma_f32_16x16x32_bf16 v[88:91], a[152:155], v[216:219], v[88:91]\n" \
"v_exp_f32_e64 v138, v138\n" \
"v_mfma_f32_16x16x32_bf16 v[88:91], a[184:187], v[200:203], v[88:91]\n" \
"ds_read_b64_tr_b16 a[148:149], v211 offset:17408\n" \
"ds_read_b64_tr_b16 a[150:151], v211 offset:21504\n" \
"v_mfma_f32_16x16x32_bf16 v[92:95], a[156:159], v[216:219], v[92:95]\n" \
"v_exp_f32_e64 v139, v139\n" \
"v_mfma_f32_16x16x32_bf16 v[92:95], a[188:191], v[200:203], v[92:95]\n" \
"ds_read_b64_tr_b16 a[152:153], %[vvb] offset:17920\n" \
"buffer_store_dwordx4 a[196:199], v235, %[srdz], s63 offen offset:0 sc1\n" \
"v_mfma_f32_16x16x32_bf16 v[144:147], a[64:67], a[32:35], v[168:171]\n" \
"v_exp_f32_e64 v140, v140\n" \
"v_mfma_f32_16x16x32_bf16 v[144:147], a[68:71], a[36:39], v[144:147]\n" \
"ds_read_b64_tr_b16 a[154:155], %[vvb] offset:22016\n" \
"ds_read_b64_tr_b16 a[156:157], v211 offset:17920\n" \
"v_mfma_f32_16x16x32_bf16 v[144:147], a[72:75], a[40:43], v[144:147]\n" \
"v_exp_f32_e64 v141, v141\n" \
"v_mfma_f32_16x16x32_bf16 v[144:147], a[76:79], a[44:47], v[144:147]\n" \
"v_cvt_pk_bf16_f32 v188, v136, v137\n" \
"v_cvt_pk_bf16_f32 v189, v138, v139\n" \
"v_mfma_f32_16x16x32_bf16 v[148:151], a[80:83], a[32:35], v[168:171]\n" \
"v_exp_f32_e64 v142, v142\n" \
"v_mfma_f32_16x16x32_bf16 v[148:151], a[84:87], a[36:39], v[148:151]\n" \
"ds_read_b64_tr_b16 a[158:159], v211 offset:22016\n" \
"s_waitcnt lgkmcnt(8)\n" \
"v_mfma_f32_16x16x32_bf16 v[148:151], a[88:91], a[40:43], v[148:151]\n" \
"buffer_load_dwordx4 v213, %[srdk], s48 offen offset:2048 lds\n" \
"v_mfma_f32_16x16x32_bf16 v[148:151], a[92:95], a[44:47], v[148:151]\n" \
"v_exp_f32_e32 v143, v143\n" \
"v_mfma_f32_16x16x32_bf16 v[0:3], a[160:163], v[192:195], v[0:3]\n" \
"ds_read_b128 a[96:99], %[kb0] offset:24576\n" \
"v_mfma_f32_16x16x32_bf16 v[0:3], a[128:131], v[184:187], v[0:3]\n" \
"v_exp_f32_e64 v144, v144\n" \
"v_mfma_f32_16x16x32_bf16 v[4:7], a[164:167], v[192:195], v[4:7]\n" \
"v_cvt_pk_bf16_f32 v190, v140, v141\n" \
"v_cvt_pk_bf16_f32 v191, v142, v143\n" \
"v_mfma_f32_16x16x32_bf16 v[4:7], a[132:135], v[184:187], v[4:7]\n" \
"v_exp_f32_e64 v145, v145\n" \
"v_mfma_f32_16x16x32_bf16 v[8:11], a[168:171], v[192:195], v[8:11]\n" \
"ds_read_b128 a[100:103], v208 offset:24576\n" \
"v_mfma_f32_16x16x32_bf16 v[8:11], a[136:139], v[184:187], v[8:11]\n" \
"v_exp_f32_e64 v146, v146\n" \
"v_mfma_f32_16x16x32_bf16 v[12:15], a[172:175], v[192:195], v[12:15]\n" \
"ds_read_b128 a[104:107], v209 offset:24576\n" \
"v_mfma_f32_16x16x32_bf16 v[12:15], a[140:143], v[184:187], v[12:15]\n" \
"v_exp_f32_e64 v147, v147\n" \
"v_smfmac_f32_16x16x64_bf16 v[180:183], a[192:195], v[200:207], v228\n" \
"ds_read_b128 a[108:111], v210 offset:24576\n" \
"v_mfma_f32_16x16x32_bf16 v[152:155], a[64:67], a[48:51], v[172:175]\n" \
"v_exp_f32_e64 v148, v148\n" \
"v_mfma_f32_16x16x32_bf16 v[152:155], a[68:71], a[52:55], v[152:155]\n" \
"ds_read_b128 a[112:115], %[kb0] offset:28672\n" \
"v_mfma_f32_16x16x32_bf16 v[152:155], a[72:75], a[56:59], v[152:155]\n" \
"v_exp_f32_e64 v149, v149\n" \
"v_mfma_f32_16x16x32_bf16 v[152:155], a[76:79], a[60:63], v[152:155]\n" \
"v_cvt_pk_bf16_f32 v216, v144, v145\n" \
"v_cvt_pk_bf16_f32 v217, v146, v147\n" \
"v_mfma_f32_16x16x32_bf16 v[156:159], a[80:83], a[48:51], v[172:175]\n" \
"v_exp_f32_e64 v150, v150\n" \
"v_mfma_f32_16x16x32_bf16 v[156:159], a[84:87], a[52:55], v[156:159]\n" \
"ds_read_b128 a[116:119], v208 offset:28672\n" \
"s_waitcnt lgkmcnt(6)\n" \
"v_mfma_f32_16x16x32_bf16 v[156:159], a[88:91], a[56:59], v[156:159]\n" \
"buffer_load_dwordx4 v214, %[srdk], s48 offen offset:3072 lds\n" \
"v_mfma_f32_16x16x32_bf16 v[156:159], a[92:95], a[60:63], v[156:159]\n" \
"v_exp_f32_e32 v151, v151\n" \
"v_mfma_f32_16x16x32_bf16 v[16:19], a[176:179], v[192:195], v[16:19]\n" \
"ds_read_b128 a[120:123], v209 offset:28672\n" \
"v_mfma_f32_16x16x32_bf16 v[16:19], a[144:147], v[184:187], v[16:19]\n" \
"v_exp_f32_e64 v152, v152\n" \
"v_mfma_f32_16x16x32_bf16 v[20:23], a[180:183], v[192:195], v[20:23]\n" \
"v_cvt_pk_bf16_f32 v218, v148, v149\n" \
"v_cvt_pk_bf16_f32 v219, v150, v151\n" \
"v_mfma_f32_16x16x32_bf16 v[20:23], a[148:151], v[184:187], v[20:23]\n" \
"v_exp_f32_e64 v153, v153\n" \
"v_mfma_f32_16x16x32_bf16 v[24:27], a[184:187], v[192:195], v[24:27]\n" \
"v_exp_f32_e64 v154, v154\n" \
"v_mfma_f32_16x16x32_bf16 v[24:27], a[152:155], v[184:187], v[24:27]\n" \
"ds_read_b128 a[124:127], v210 offset:28672\n" \
"s_add_u32 s48, s48, 0x4000\n" \
"s_mov_b32 m0, s44\n" \
"s_addk_i32 s44, 0x400\n" \
"buffer_load_dwordx4 v229, s[64:67], s45 offen offset:0 lds\n" \
"s_add_u32 s45, s45, 229376\n" \
"v_mfma_f32_16x16x32_bf16 v[28:31], a[188:191], v[192:195], v[28:31]\n" \
"v_exp_f32_e64 v155, v155\n" \
"v_mfma_f32_16x16x32_bf16 v[28:31], a[156:159], v[184:187], v[28:31]\n" \
"s_waitcnt vmcnt(15) lgkmcnt(2)\n" \
"s_barrier\n" \
"v_mfma_f32_16x16x32_bf16 v[128:131], a[96:99], a[0:3], v[160:163]\n" \
"v_exp_f32_e64 v156, v156\n" \
"v_mfma_f32_16x16x32_bf16 v[128:131], a[100:103], a[4:7], v[128:131]\n" \
"s_add_u32 m0, %[m0b], 65536\n" \
"v_mfma_f32_16x16x32_bf16 v[128:131], a[104:107], a[8:11], v[128:131]\n" \
"v_exp_f32_e64 v157, v157\n" \
"v_mfma_f32_16x16x32_bf16 v[128:131], a[108:111], a[12:15], v[128:131]\n" \
"v_cvt_pk_bf16_f32 v220, v152, v153\n" \
"v_cvt_pk_bf16_f32 v221, v154, v155\n" \
"v_mfma_f32_16x16x32_bf16 v[132:135], a[112:115], a[0:3], v[160:163]\n" \
"v_exp_f32_e64 v158, v158\n" \
"v_mfma_f32_16x16x32_bf16 v[132:135], a[116:119], a[4:7], v[132:135]\n" \
"s_cmp_lg_u32 s60, 5\n" \
"s_cbranch_scc1 Lsjb_%=\n" \
"ds_read_b32 v230, %[rd0]\n" \
"v_readlane_b32 s55, %[thi], 15\n" \
"v_lshrrev_b32_e32 v234, 5, v224\n" \
"s_waitcnt lgkmcnt(0)\n" \
"v_add_f32_dpp v230, v230, v230 row_ror:4 row_mask:0xf bank_mask:0xf\n" \
"s_nop 1\n" \
"v_add_f32_dpp v230, v230, v230 row_ror:8 row_mask:0xf bank_mask:0xf\n" \
"v_cvt_pk_bf16_f32 v230, v230, v230\n" \
"v_lshlrev_b32_e32 v230, 16, v230\n" \
"v_add_f32_e64 v230, s55, v230\n" \
"v_cvt_pk_bf16_f32 v230, v230, v230\n" \
"v_readlane_b32 s55, %[tlo], 15\n" \
"v_lshl_or_b32 v230, v230, 16, 1\n" \
"s_cmp_lg_u32 s52, 0\n" \
"s_cselect_b32 s55, 0x80000000, s55\n" \
"s_mov_b64 exec, 15\n" \
"buffer_store_dword v230, v234, %[srdz], s55 offen sc1\n" \
"s_mov_b64 exec, -1\n" \
"Lsjb_%=:\n" \
"v_mfma_f32_16x16x32_bf16 v[132:135], a[120:123], a[8:11], v[132:135]\n" \
"buffer_load_dwordx4 %[vd0], %[srdv], s48 offen offset:0 lds\n" \
"v_mfma_f32_16x16x32_bf16 v[132:135], a[124:127], a[12:15], v[132:135]\n" \
"v_exp_f32_e32 v159, v159\n" \
"v_mfma_f32_16x16x32_bf16 v[32:35], a[160:163], v[196:199], v[32:35]\n" \
"ds_read_b128 a[64:67], %[kb0] offset:32768\n" \
"v_mfma_f32_16x16x32_bf16 v[32:35], a[128:131], v[188:191], v[32:35]\n" \
"v_exp_f32_e64 v128, v128\n" \
"v_mfma_f32_16x16x32_bf16 v[36:39], a[164:167], v[196:199], v[36:39]\n" \
"v_cvt_pk_bf16_f32 v222, v156, v157\n" \
"v_cvt_pk_bf16_f32 v223, v158, v159\n" \
"v_mfma_f32_16x16x32_bf16 v[36:39], a[132:135], v[188:191], v[36:39]\n" \
"v_exp_f32_e64 v129, v129\n" \
"v_mfma_f32_16x16x32_bf16 v[40:43], a[168:171], v[196:199], v[40:43]\n" \
"ds_read_b128 a[68:71], v208 offset:32768\n" \
"v_mfma_f32_16x16x32_bf16 v[40:43], a[136:139], v[188:191], v[40:43]\n" \
"v_exp_f32_e64 v130, v130\n" \
"v_mfma_f32_16x16x32_bf16 v[44:47], a[172:175], v[196:199], v[44:47]\n" \
"ds_read_b128 a[72:75], v209 offset:32768\n" \
"v_mfma_f32_16x16x32_bf16 v[44:47], a[140:143], v[188:191], v[44:47]\n" \
"v_exp_f32_e64 v131, v131\n" \
"v_smfmac_f32_16x16x64_bf16 v[176:179], a[192:195], v[184:191], v228\n" \
"v_mfma_f32_16x16x32_bf16 v[136:139], a[96:99], a[16:19], v[164:167]\n" \
"v_exp_f32_e64 v132, v132\n" \
"v_mfma_f32_16x16x32_bf16 v[136:139], a[100:103], a[20:23], v[136:139]\n" \
"ds_read_b128 a[76:79], v210 offset:32768\n" \
"v_mfma_f32_16x16x32_bf16 v[136:139], a[104:107], a[24:27], v[136:139]\n" \
"v_exp_f32_e64 v133, v133\n" \
"v_mfma_f32_16x16x32_bf16 v[136:139], a[108:111], a[28:31], v[136:139]\n" \
"v_cvt_pk_bf16_f32 v192, v128, v129\n" \
"v_cvt_pk_bf16_f32 v193, v130, v131\n" \
"v_mfma_f32_16x16x32_bf16 v[140:143], a[112:115], a[16:19], v[164:167]\n" \
"v_exp_f32_e64 v134, v134\n" \
"v_mfma_f32_16x16x32_bf16 v[140:143], a[116:119], a[20:23], v[140:143]\n" \
"ds_read_b128 a[80:83], %[kb0] offset:36864\n" \
"v_mfma_f32_16x16x32_bf16 v[140:143], a[120:123], a[24:27], v[140:143]\n" \
"buffer_load_dwordx4 v215, %[srdv], s48 offen offset:1024 lds\n" \
"v_mfma_f32_16x16x32_bf16 v[140:143], a[124:127], a[28:31], v[140:143]\n" \
"v_exp_f32_e64 v135, v135\n" \
"v_mfma_f32_16x16x32_bf16 v[48:51], a[176:179], v[196:199], v[48:51]\n" \
"ds_read_b128 a[84:87], v208 offset:36864\n" \
"v_mfma_f32_16x16x32_bf16 v[48:51], a[144:147], v[188:191], v[48:51]\n" \
"v_exp_f32_e64 v136, v136\n" \
"v_mfma_f32_16x16x32_bf16 v[52:55], a[180:183], v[196:199], v[52:55]\n" \
"v_cvt_pk_bf16_f32 v194, v132, v133\n" \
"v_cvt_pk_bf16_f32 v195, v134, v135\n" \
"v_mfma_f32_16x16x32_bf16 v[52:55], a[148:151], v[188:191], v[52:55]\n" \
"v_exp_f32_e64 v137, v137\n" \
"v_mfma_f32_16x16x32_bf16 v[56:59], a[184:187], v[196:199], v[56:59]\n" \
"v_exp_f32_e64 v138, v138\n" \
"v_mfma_f32_16x16x32_bf16 v[56:59], a[152:155], v[188:191], v[56:59]\n" \
"ds_read_b128 a[88:91], v209 offset:36864\n" \
"v_mfma_f32_16x16x32_bf16 v[60:63], a[188:191], v[196:199], v[60:63]\n" \
"v_exp_f32_e64 v139, v139\n" \
"v_mfma_f32_16x16x32_bf16 v[60:63], a[156:159], v[188:191], v[60:63]\n" \
"s_cmp_lg_u32 %[wid], 0\n" \
"s_cbranch_scc1 LtB3_%=\n" \
"global_load_dword v237, v236, s[58:59]\n" \
"s_add_u32 s61, s48, 49152\n" \
"s_mov_b32 exec_hi, 0\n" \
"s_nop 0\n" \
"buffer_load_dword v237, v224, %[srdp], s61 offen\n" \
"s_mov_b32 exec_hi, -1\n" \
"s_nop 0\n" \
"LtB3_%=:\n" \
"v_mfma_f32_16x16x32_bf16 v[144:147], a[96:99], a[32:35], v[168:171]\n" \
"v_exp_f32_e64 v140, v140\n" \
"v_mfma_f32_16x16x32_bf16 v[144:147], a[100:103], a[36:39], v[144:147]\n" \
"ds_read_b128 a[92:95], v210 offset:36864\n" \
"v_mfma_f32_16x16x32_bf16 v[144:147], a[104:107], a[40:43], v[144:147]\n" \
"v_exp_f32_e64 v141, v141\n" \
"v_mfma_f32_16x16x32_bf16 v[144:147], a[108:111], a[44:47], v[144:147]\n" \
"v_cvt_pk_bf16_f32 v196, v136, v137\n" \
"v_cvt_pk_bf16_f32 v197, v138, v139\n" \
"v_mfma_f32_16x16x32_bf16 v[148:151], a[112:115], a[32:35], v[168:171]\n" \
"v_exp_f32_e64 v142, v142\n" \
"v_mfma_f32_16x16x32_bf16 v[148:151], a[116:119], a[36:39], v[148:151]\n" \
"v_mfma_f32_16x16x32_bf16 v[148:151], a[120:123], a[40:43], v[148:151]\n" \
"buffer_load_dwordx4 %[vd0], %[srdv], s48 offen offset:2048 lds\n" \
"v_mfma_f32_16x16x32_bf16 v[148:151], a[124:127], a[44:47], v[148:151]\n" \
"v_exp_f32_e64 v143, v143\n" \
"v_mfma_f32_16x16x32_bf16 v[96:99], a[160:163], v[204:207], v[96:99]\n" \
"ds_read_b64_tr_b16 a[160:161], %[vvb] offset:24576\n" \
"v_mfma_f32_16x16x32_bf16 v[96:99], a[128:131], v[220:223], v[96:99]\n" \
"v_exp_f32_e64 v144, v144\n" \
"v_mfma_f32_16x16x32_bf16 v[100:103], a[164:167], v[204:207], v[100:103]\n" \
"v_cvt_pk_bf16_f32 v198, v140, v141\n" \
"v_cvt_pk_bf16_f32 v199, v142, v143\n" \
"v_mfma_f32_16x16x32_bf16 v[100:103], a[132:135], v[220:223], v[100:103]\n" \
"v_exp_f32_e64 v145, v145\n" \
"v_mfma_f32_16x16x32_bf16 v[104:107], a[168:171], v[204:207], v[104:107]\n" \
"ds_read_b64_tr_b16 a[162:163], %[vvb] offset:28672\n" \
"ds_read_b64_tr_b16 a[164:165], v211 offset:24576\n" \
"v_mfma_f32_16x16x32_bf16 v[104:107], a[136:139], v[220:223], v[104:107]\n" \
"v_exp_f32_e64 v146, v146\n" \
"v_mfma_f32_16x16x32_bf16 v[108:111], a[172:175], v[204:207], v[108:111]\n" \
"ds_read_b64_tr_b16 a[166:167], v211 offset:28672\n" \
"v_mfma_f32_16x16x32_bf16 v[108:111], a[140:143], v[220:223], v[108:111]\n" \
"v_exp_f32_e64 v147, v147\n" \
"v_smfmac_f32_16x16x64_bf16 v[180:183], a[192:195], v[216:223], v228\n" \
"ds_read_b64_tr_b16 a[168:169], %[vvb] offset:25088\n" \
"buffer_store_dwordx4 a[196:199], v235, %[srdz], s62 offen offset:0 sc1\n" \
"v_mfma_f32_16x16x32_bf16 v[152:155], a[96:99], a[48:51], v[172:175]\n" \
"v_exp_f32_e64 v148, v148\n" \
"v_mfma_f32_16x16x32_bf16 v[152:155], a[100:103], a[52:55], v[152:155]\n" \
"ds_read_b64_tr_b16 a[170:171], %[vvb] offset:29184\n" \
"v_mfma_f32_16x16x32_bf16 v[152:155], a[104:107], a[56:59], v[152:155]\n" \
"v_exp_f32_e64 v149, v149\n" \
"v_mfma_f32_16x16x32_bf16 v[152:155], a[108:111], a[60:63], v[152:155]\n" \
"v_cvt_pk_bf16_f32 v200, v144, v145\n" \
"v_cvt_pk_bf16_f32 v201, v146, v147\n" \
"v_mfma_f32_16x16x32_bf16 v[156:159], a[112:115], a[48:51], v[172:175]\n" \
"v_exp_f32_e64 v150, v150\n" \
"v_mfma_f32_16x16x32_bf16 v[156:159], a[116:119], a[52:55], v[156:159]\n" \
"ds_read_b64_tr_b16 a[172:173], v211 offset:25088\n" \
"v_mfma_f32_16x16x32_bf16 v[156:159], a[120:123], a[56:59], v[156:159]\n" \
"buffer_load_dwordx4 v215, %[srdv], s48 offen offset:3072 lds\n" \
"v_mfma_f32_16x16x32_bf16 v[156:159], a[124:127], a[60:63], v[156:159]\n" \
"v_exp_f32_e64 v151, v151\n" \
"v_mfma_f32_16x16x32_bf16 v[112:115], a[176:179], v[204:207], v[112:115]\n" \
"ds_read_b64_tr_b16 a[174:175], v211 offset:29184\n" \
"ds_read_b64_tr_b16 a[176:177], %[vvb] offset:25600\n" \
"v_mfma_f32_16x16x32_bf16 v[112:115], a[144:147], v[220:223], v[112:115]\n" \
"v_exp_f32_e64 v152, v152\n" \
"v_mfma_f32_16x16x32_bf16 v[116:119], a[180:183], v[204:207], v[116:119]\n" \
"v_cvt_pk_bf16_f32 v202, v148, v149\n" \
"v_cvt_pk_bf16_f32 v203, v150, v151\n" \
"v_mfma_f32_16x16x32_bf16 v[116:119], a[148:151], v[220:223], v[116:119]\n" \
"v_exp_f32_e64 v153, v153\n" \
"v_mfma_f32_16x16x32_bf16 v[120:123], a[184:187], v[204:207], v[120:123]\n" \
"v_exp_f32_e64 v154, v154\n" \
"v_mfma_f32_16x16x32_bf16 v[120:123], a[152:155], v[220:223], v[120:123]\n" \
"ds_read_b64_tr_b16 a[178:179], %[vvb] offset:29696\n" \
"v_mfma_f32_16x16x32_bf16 v[124:127], a[188:191], v[204:207], v[124:127]\n" \
"v_exp_f32_e64 v155, v155\n" \
"v_mfma_f32_16x16x32_bf16 v[124:127], a[156:159], v[220:223], v[124:127]\n" \
"ds_read_b64_tr_b16 a[180:181], v211 offset:25600\n" \
"s_waitcnt lgkmcnt(13)\n" \
"v_mfma_f32_16x16x32_bf16 v[128:131], a[64:67], a[0:3], v[160:163]\n" \
"v_exp_f32_e32 v156, v156\n" \
"v_mfma_f32_16x16x32_bf16 v[128:131], a[68:71], a[4:7], v[128:131]\n" \
"ds_read_b64_tr_b16 a[182:183], v211 offset:29696\n" \
"s_add_u32 m0, %[m0b], 16384\n" \
"v_mfma_f32_16x16x32_bf16 v[128:131], a[72:75], a[8:11], v[128:131]\n" \
"v_exp_f32_e64 v157, v157\n" \
"v_mfma_f32_16x16x32_bf16 v[128:131], a[76:79], a[12:15], v[128:131]\n" \
"v_cvt_pk_bf16_f32 v204, v152, v153\n" \
"v_cvt_pk_bf16_f32 v205, v154, v155\n" \
"v_mfma_f32_16x16x32_bf16 v[132:135], a[80:83], a[0:3], v[160:163]\n" \
"v_exp_f32_e64 v158, v158\n" \
"v_mfma_f32_16x16x32_bf16 v[132:135], a[84:87], a[4:7], v[132:135]\n" \
"ds_read_b64_tr_b16 a[184:185], %[vvb] offset:26112\n" \
"s_waitcnt lgkmcnt(5)\n" \
"v_mfma_f32_16x16x32_bf16 v[132:135], a[88:91], a[8:11], v[132:135]\n" \
"buffer_load_dwordx4 %[kd0], %[srdk], s48 offen offset:0 lds\n" \
"v_mfma_f32_16x16x32_bf16 v[132:135], a[92:95], a[12:15], v[132:135]\n" \
"v_exp_f32_e32 v159, v159\n" \
"v_mfma_f32_16x16x32_bf16 v[64:67], a[128:131], v[216:219], v[64:67]\n" \
"ds_read_b64_tr_b16 a[186:187], %[vvb] offset:30208\n" \
"ds_read_b64_tr_b16 a[188:189], v211 offset:26112\n" \
"v_mfma_f32_16x16x32_bf16 v[64:67], a[160:163], v[200:203], v[64:67]\n" \
"v_exp_f32_e64 v128, v128\n" \
"v_mfma_f32_16x16x32_bf16 v[68:71], a[132:135], v[216:219], v[68:71]\n" \
"v_cvt_pk_bf16_f32 v206, v156, v157\n" \
"v_cvt_pk_bf16_f32 v207, v158, v159\n" \
"v_mfma_f32_16x16x32_bf16 v[68:71], a[164:167], v[200:203], v[68:71]\n" \
"v_exp_f32_e64 v129, v129\n" \
"v_mfma_f32_16x16x32_bf16 v[72:75], a[136:139], v[216:219], v[72:75]\n" \
"ds_read_b64_tr_b16 a[190:191], v211 offset:30208\n" \
"ds_read_b64_tr_b16 a[128:129], %[vvb] offset:32768\n" \
"v_mfma_f32_16x16x32_bf16 v[72:75], a[168:171], v[200:203], v[72:75]\n" \
"v_exp_f32_e64 v130, v130\n" \
"v_mfma_f32_16x16x32_bf16 v[76:79], a[140:143], v[216:219], v[76:79]\n" \
"ds_read_b64_tr_b16 a[130:131], %[vvb] offset:36864\n" \
"ds_read_b64_tr_b16 a[132:133], v211 offset:32768\n" \
"v_mfma_f32_16x16x32_bf16 v[76:79], a[172:175], v[200:203], v[76:79]\n" \
"v_exp_f32_e64 v131, v131\n" \
"v_smfmac_f32_16x16x64_bf16 v[176:179], a[192:195], v[192:199], v228\n" \
"ds_read_b64_tr_b16 a[134:135], v211 offset:36864\n" \
"ds_read_b64_tr_b16 a[136:137], %[vvb] offset:33280\n" \
"v_mfma_f32_16x16x32_bf16 v[136:139], a[64:67], a[16:19], v[164:167]\n" \
"v_exp_f32_e64 v132, v132\n" \
"v_mfma_f32_16x16x32_bf16 v[136:139], a[68:71], a[20:23], v[136:139]\n" \
"ds_read_b64_tr_b16 a[138:139], %[vvb] offset:37376\n" \
"ds_read_b64_tr_b16 a[140:141], v211 offset:33280\n" \
"v_mfma_f32_16x16x32_bf16 v[136:139], a[72:75], a[24:27], v[136:139]\n" \
"v_exp_f32_e64 v133, v133\n" \
"v_mfma_f32_16x16x32_bf16 v[136:139], a[76:79], a[28:31], v[136:139]\n" \
"v_cvt_pk_bf16_f32 v184, v128, v129\n" \
"v_cvt_pk_bf16_f32 v185, v130, v131\n" \
"v_mfma_f32_16x16x32_bf16 v[140:143], a[80:83], a[16:19], v[164:167]\n" \
"v_exp_f32_e64 v134, v134\n" \
"v_mfma_f32_16x16x32_bf16 v[140:143], a[84:87], a[20:23], v[140:143]\n" \
"ds_read_b64_tr_b16 a[142:143], v211 offset:37376\n" \
"s_waitcnt lgkmcnt(8)\n" \
"v_mfma_f32_16x16x32_bf16 v[140:143], a[88:91], a[24:27], v[140:143]\n" \
"buffer_load_dwordx4 v212, %[srdk], s48 offen offset:1024 lds\n" \
"v_mfma_f32_16x16x32_bf16 v[140:143], a[92:95], a[28:31], v[140:143]\n" \
"v_exp_f32_e32 v135, v135\n" \
"v_mfma_f32_16x16x32_bf16 v[80:83], a[144:147], v[216:219], v[80:83]\n" \
".if . - adaln_body != 24576\n" \
".error \"size model\"\n" \
".endif\n" \
"Lk5_%=:\n" \
"s_cbranch_execz adaln_kr\n" \
"s_nop 0\n" \
"ds_read_b64_tr_b16 a[144:145], %[vvb] offset:33792\n" \
"ds_read_b64_tr_b16 a[146:147], %[vvb] offset:37888\n" \
"v_mfma_f32_16x16x32_bf16 v[80:83], a[176:179], v[200:203], v[80:83]\n" \
"v_exp_f32_e64 v136, v136\n" \
"v_mfma_f32_16x16x32_bf16 v[84:87], a[148:151], v[216:219], v[84:87]\n" \
"v_cvt_pk_bf16_f32 v186, v132, v133\n" \
"v_cvt_pk_bf16_f32 v187, v134, v135\n" \
"v_mfma_f32_16x16x32_bf16 v[84:87], a[180:183], v[200:203], v[84:87]\n" \
"v_exp_f32_e64 v137, v137\n" \
"v_mfma_f32_16x16x32_bf16 v[88:91], a[152:155], v[216:219], v[88:91]\n" \
"v_exp_f32_e64 v138, v138\n" \
"v_mfma_f32_16x16x32_bf16 v[88:91], a[184:187], v[200:203], v[88:91]\n" \
"ds_read_b64_tr_b16 a[148:149], v211 offset:33792\n" \
"ds_read_b64_tr_b16 a[150:151], v211 offset:37888\n" \
"v_mfma_f32_16x16x32_bf16 v[92:95], a[156:159], v[216:219], v[92:95]\n" \
"v_exp_f32_e64 v139, v139\n" \
"v_mfma_f32_16x16x32_bf16 v[92:95], a[188:191], v[200:203], v[92:95]\n" \
"ds_read_b64_tr_b16 a[152:153], %[vvb] offset:34304\n" \
"buffer_store_dwordx4 a[196:199], v235, %[srdz], s63 offen offset:1024 sc1\n" \
"v_mfma_f32_16x16x32_bf16 v[144:147], a[64:67], a[32:35], v[168:171]\n" \
"v_exp_f32_e64 v140, v140\n" \
"v_mfma_f32_16x16x32_bf16 v[144:147], a[68:71], a[36:39], v[144:147]\n" \
"ds_read_b64_tr_b16 a[154:155], %[vvb] offset:38400\n" \
"ds_read_b64_tr_b16 a[156:157], v211 offset:34304\n" \
"v_mfma_f32_16x16x32_bf16 v[144:147], a[72:75], a[40:43], v[144:147]\n" \
"v_exp_f32_e64 v141, v141\n" \
"v_mfma_f32_16x16x32_bf16 v[144:147], a[76:79], a[44:47], v[144:147]\n" \
"v_cvt_pk_bf16_f32 v188, v136, v137\n" \
"v_cvt_pk_bf16_f32 v189, v138, v139\n" \
"v_mfma_f32_16x16x32_bf16 v[148:151], a[80:83], a[32:35], v[168:171]\n" \
"v_exp_f32_e64 v142, v142\n" \
"v_mfma_f32_16x16x32_bf16 v[148:151], a[84:87], a[36:39], v[148:151]\n" \
"ds_read_b64_tr_b16 a[158:159], v211 offset:38400\n" \
"s_waitcnt lgkmcnt(8)\n" \
"v_mfma_f32_16x16x32_bf16 v[148:151], a[88:91], a[40:43], v[148:151]\n" \
"buffer_load_dwordx4 v213, %[srdk], s48 offen offset:2048 lds\n" \
"v_mfma_f32_16x16x32_bf16 v[148:151], a[92:95], a[44:47], v[148:151]\n" \
"v_exp_f32_e32 v143, v143\n" \
"v_mfma_f32_16x16x32_bf16 v[0:3], a[160:163], v[192:195], v[0:3]\n" \
"ds_read_b128 a[96:99], %[kb0] offset:40960\n" \
"v_mfma_f32_16x16x32_bf16 v[0:3], a[128:131], v[184:187], v[0:3]\n" \
"v_exp_f32_e64 v144, v144\n" \
"v_mfma_f32_16x16x32_bf16 v[4:7], a[164:167], v[192:195], v[4:7]\n" \
"v_cvt_pk_bf16_f32 v190, v140, v141\n" \
"v_cvt_pk_bf16_f32 v191, v142, v143\n" \
"v_mfma_f32_16x16x32_bf16 v[4:7], a[132:135], v[184:187], v[4:7]\n" \
"v_exp_f32_e64 v145, v145\n" \
"v_mfma_f32_16x16x32_bf16 v[8:11], a[168:171], v[192:195], v[8:11]\n" \
"ds_read_b128 a[100:103], v208 offset:40960\n" \
"v_mfma_f32_16x16x32_bf16 v[8:11], a[136:139], v[184:187], v[8:11]\n" \
"v_exp_f32_e64 v146, v146\n" \
"v_mfma_f32_16x16x32_bf16 v[12:15], a[172:175], v[192:195], v[12:15]\n" \
"ds_read_b128 a[104:107], v209 offset:40960\n" \
"v_mfma_f32_16x16x32_bf16 v[12:15], a[140:143], v[184:187], v[12:15]\n" \
"v_exp_f32_e64 v147, v147\n" \
"v_smfmac_f32_16x16x64_bf16 v[180:183], a[192:195], v[200:207], v228\n" \
"ds_read_b128 a[108:111], v210 offset:40960\n" \
"v_mfma_f32_16x16x32_bf16 v[152:155], a[64:67], a[48:51], v[172:175]\n" \
"v_exp_f32_e64 v148, v148\n" \
"v_mfma_f32_16x16x32_bf16 v[152:155], a[68:71], a[52:55], v[152:155]\n" \
"ds_read_b128 a[112:115], %[kb0] offset:45056\n" \
"v_mfma_f32_16x16x32_bf16 v[152:155], a[72:75], a[56:59], v[152:155]\n" \
"v_exp_f32_e64 v149, v149\n" \
"v_mfma_f32_16x16x32_bf16 v[152:155], a[76:79], a[60:63], v[152:155]\n" \
"v_cvt_pk_bf16_f32 v216, v144, v145\n" \
"v_cvt_pk_bf16_f32 v217, v146, v147\n" \
"v_mfma_f32_16x16x32_bf16 v[156:159], a[80:83], a[48:51], v[172:175]\n" \
"v_exp_f32_e64 v150, v150\n" \
"v_mfma_f32_16x16x32_bf16 v[156:159], a[84:87], a[52:55], v[156:159]\n" \
"ds_read_b128 a[116:119], v208 offset:45056\n" \
"s_waitcnt lgkmcnt(6)\n" \
"v_mfma_f32_16x16x32_bf16 v[156:159], a[88:91], a[56:59], v[156:159]\n" \
"buffer_load_dwordx4 v214, %[srdk], s48 offen offset:3072 lds\n" \
"v_mfma_f32_16x16x32_bf16 v[156:159], a[92:95], a[60:63], v[156:159]\n" \
"v_exp_f32_e32 v151, v151\n" \
"v_mfma_f32_16x16x32_bf16 v[16:19], a[176:179], v[192:195], v[16:19]\n" \
"ds_read_b128 a[120:123], v209 offset:45056\n" \
"v_mfma_f32_16x16x32_bf16 v[16:19], a[144:147], v[184:187], v[16:19]\n" \
"v_exp_f32_e64 v152, v152\n" \
"v_mfma_f32_16x16x32_bf16 v[20:23], a[180:183], v[192:195], v[20:23]\n" \
"v_cvt_pk_bf16_f32 v218, v148, v149\n" \
"v_cvt_pk_bf16_f32 v219, v150, v151\n" \
"v_mfma_f32_16x16x32_bf16 v[20:23], a[148:151], v[184:187], v[20:23]\n" \
"v_exp_f32_e64 v153, v153\n" \
"v_mfma_f32_16x16x32_bf16 v[24:27], a[184:187], v[192:195], v[24:27]\n" \
"v_exp_f32_e64 v154, v154\n" \
"v_mfma_f32_16x16x32_bf16 v[24:27], a[152:155], v[184:187], v[24:27]\n" \
"ds_read_b128 a[124:127], v210 offset:45056\n" \
"s_add_u32 s48, s48, 0x4000\n" \
"v_mfma_f32_16x16x32_bf16 v[28:31], a[188:191], v[192:195], v[28:31]\n" \
"v_exp_f32_e64 v155, v155\n" \
"v_mfma_f32_16x16x32_bf16 v[28:31], a[156:159], v[184:187], v[28:31]\n" \
"s_waitcnt vmcnt(16) lgkmcnt(2)\n" \
"s_barrier\n" \
"v_mfma_f32_16x16x32_bf16 v[128:131], a[96:99], a[0:3], v[160:163]\n" \
"v_exp_f32_e64 v156, v156\n" \
"v_mfma_f32_16x16x32_bf16 v[128:131], a[100:103], a[4:7], v[128:131]\n" \
"s_add_u32 m0, %[m0b], 81920\n" \
"v_mfma_f32_16x16x32_bf16 v[128:131], a[104:107], a[8:11], v[128:131]\n" \
"v_exp_f32_e64 v157, v157\n" \
"v_mfma_f32_16x16x32_bf16 v[128:131], a[108:111], a[12:15], v[128:131]\n" \
"v_cvt_pk_bf16_f32 v220, v152, v153\n" \
"v_cvt_pk_bf16_f32 v221, v154, v155\n" \
"v_mfma_f32_16x16x32_bf16 v[132:135], a[112:115], a[0:3], v[160:163]\n" \
"v_exp_f32_e64 v158, v158\n" \
"v_mfma_f32_16x16x32_bf16 v[132:135], a[116:119], a[4:7], v[132:135]\n" \
"s_waitcnt lgkmcnt(0)\n" \
"v_mfma_f32_16x16x32_bf16 v[132:135], a[120:123], a[8:11], v[132:135]\n" \
"buffer_load_dwordx4 %[vd0], %[srdv], s48 offen offset:0 lds\n" \
"v_mfma_f32_16x16x32_bf16 v[132:135], a[124:127], a[12:15], v[132:135]\n" \
"v_exp_f32_e32 v159, v159\n" \
"v_mfma_f32_16x16x32_bf16 v[32:35], a[160:163], v[196:199], v[32:35]\n" \
"ds_read_b128 a[64:67], %[kb0] offset:49152\n" \
"v_mfma_f32_16x16x32_bf16 v[32:35], a[128:131], v[188:191], v[32:35]\n" \
"v_exp_f32_e64 v128, v128\n" \
"v_mfma_f32_16x16x32_bf16 v[36:39], a[164:167], v[196:199], v[36:39]\n" \
"v_cvt_pk_bf16_f32 v222, v156, v157\n" \
"v_cvt_pk_bf16_f32 v223, v158, v159\n" \
"v_mfma_f32_16x16x32_bf16 v[36:39], a[132:135], v[188:191], v[36:39]\n" \
"v_exp_f32_e64 v129, v129\n" \
"v_mfma_f32_16x16x32_bf16 v[40:43], a[168:171], v[196:199], v[40:43]\n" \
"ds_read_b128 a[68:71], v208 offset:49152\n" \
"v_mfma_f32_16x16x32_bf16 v[40:43], a[136:139], v[188:191], v[40:43]\n" \
"v_exp_f32_e64 v130, v130\n" \
"v_mfma_f32_16x16x32_bf16 v[44:47], a[172:175], v[196:199], v[44:47]\n" \
"ds_read_b128 a[72:75], v209 offset:49152\n" \
"v_mfma_f32_16x16x32_bf16 v[44:47], a[140:143], v[188:191], v[44:47]\n" \
"v_exp_f32_e64 v131, v131\n" \
"v_smfmac_f32_16x16x64_bf16 v[176:179], a[192:195], v[184:191], v228\n" \
"v_mfma_f32_16x16x32_bf16 v[136:139], a[96:99], a[16:19], v[164:167]\n" \
"v_exp_f32_e64 v132, v132\n" \
"v_mfma_f32_16x16x32_bf16 v[136:139], a[100:103], a[20:23], v[136:139]\n" \
"ds_read_b128 a[76:79], v210 offset:49152\n" \
"v_mfma_f32_16x16x32_bf16 v[136:139], a[104:107], a[24:27], v[136:139]\n" \
"v_exp_f32_e64 v133, v133\n" \
"v_mfma_f32_16x16x32_bf16 v[136:139], a[108:111], a[28:31], v[136:139]\n" \
"v_cvt_pk_bf16_f32 v192, v128, v129\n" \
"v_cvt_pk_bf16_f32 v193, v130, v131\n" \
"v_mfma_f32_16x16x32_bf16 v[140:143], a[112:115], a[16:19], v[164:167]\n" \
"v_exp_f32_e64 v134, v134\n" \
"v_mfma_f32_16x16x32_bf16 v[140:143], a[116:119], a[20:23], v[140:143]\n" \
"ds_read_b128 a[80:83], %[kb0] offset:53248\n" \
"v_mfma_f32_16x16x32_bf16 v[140:143], a[120:123], a[24:27], v[140:143]\n" \
"buffer_load_dwordx4 v215, %[srdv], s48 offen offset:1024 lds\n" \
"v_mfma_f32_16x16x32_bf16 v[140:143], a[124:127], a[28:31], v[140:143]\n" \
"v_exp_f32_e64 v135, v135\n" \
"v_mfma_f32_16x16x32_bf16 v[48:51], a[176:179], v[196:199], v[48:51]\n" \
"ds_read_b128 a[84:87], v208 offset:53248\n" \
"v_mfma_f32_16x16x32_bf16 v[48:51], a[144:147], v[188:191], v[48:51]\n" \
"v_exp_f32_e64 v136, v136\n" \
"v_mfma_f32_16x16x32_bf16 v[52:55], a[180:183], v[196:199], v[52:55]\n" \
"v_cvt_pk_bf16_f32 v194, v132, v133\n" \
"v_cvt_pk_bf16_f32 v195, v134, v135\n" \
"v_mfma_f32_16x16x32_bf16 v[52:55], a[148:151], v[188:191], v[52:55]\n" \
"v_exp_f32_e64 v137, v137\n" \
"v_mfma_f32_16x16x32_bf16 v[56:59], a[184:187], v[196:199], v[56:59]\n" \
"v_exp_f32_e64 v138, v138\n" \
"v_mfma_f32_16x16x32_bf16 v[56:59], a[152:155], v[188:191], v[56:59]\n" \
"ds_read_b128 a[88:91], v209 offset:53248\n" \
"v_mfma_f32_16x16x32_bf16 v[60:63], a[188:191], v[196:199], v[60:63]\n" \
"v_exp_f32_e64 v139, v139\n" \
"v_mfma_f32_16x16x32_bf16 v[60:63], a[156:159], v[188:191], v[60:63]\n" \
"s_cmp_lg_u32 %[wid], 1\n" \
"s_cbranch_scc1 LtB5_%=\n" \
"global_load_dword v237, v236, s[58:59]\n" \
"s_add_u32 s61, s48, 49152\n" \
"s_mov_b32 exec_hi, 0\n" \
"s_nop 0\n" \
"buffer_load_dword v237, v224, %[srdp], s61 offen\n" \
"s_mov_b32 exec_hi, -1\n" \
"s_nop 0\n" \
"LtB5_%=:\n" \
"v_mfma_f32_16x16x32_bf16 v[144:147], a[96:99], a[32:35], v[168:171]\n" \
"v_exp_f32_e64 v140, v140\n" \
"v_mfma_f32_16x16x32_bf16 v[144:147], a[100:103], a[36:39], v[144:147]\n" \
"ds_read_b128 a[92:95], v210 offset:53248\n" \
"v_mfma_f32_16x16x32_bf16 v[144:147], a[104:107], a[40:43], v[144:147]\n" \
"v_exp_f32_e64 v141, v141\n" \
"v_mfma_f32_16x16x32_bf16 v[144:147], a[108:111], a[44:47], v[144:147]\n" \
"v_cvt_pk_bf16_f32 v196, v136, v137\n" \
"v_cvt_pk_bf16_f32 v197, v138, v139\n" \
"v_mfma_f32_16x16x32_bf16 v[148:151], a[112:115], a[32:35], v[168:171]\n" \
"v_exp_f32_e64 v142, v142\n" \
"v_mfma_f32_16x16x32_bf16 v[148:151], a[116:119], a[36:39], v[148:151]\n" \
"v_mfma_f32_16x16x32_bf16 v[148:151], a[120:123], a[40:43], v[148:151]\n" \
"buffer_load_dwordx4 %[vd0], %[srdv], s48 offen offset:2048 lds\n" \
"v_mfma_f32_16x16x32_bf16 v[148:151], a[124:127], a[44:47], v[148:151]\n" \
"v_exp_f32_e64 v143, v143\n" \
"v_mfma_f32_16x16x32_bf16 v[96:99], a[160:163], v[204:207], v[96:99]\n" \
"ds_read_b64_tr_b16 a[160:161], %[vvb] offset:40960\n" \
"v_mfma_f32_16x16x32_bf16 v[96:99], a[128:131], v[220:223], v[96:99]\n" \
"v_exp_f32_e64 v144, v144\n" \
"v_mfma_f32_16x16x32_bf16 v[100:103], a[164:167], v[204:207], v[100:103]\n" \
"v_cvt_pk_bf16_f32 v198, v140, v141\n" \
"v_cvt_pk_bf16_f32 v199, v142, v143\n" \
"v_mfma_f32_16x16x32_bf16 v[100:103], a[132:135], v[220:223], v[100:103]\n" \
"v_exp_f32_e64 v145, v145\n" \
"v_mfma_f32_16x16x32_bf16 v[104:107], a[168:171], v[204:207], v[104:107]\n" \
"ds_read_b64_tr_b16 a[162:163], %[vvb] offset:45056\n" \
"ds_read_b64_tr_b16 a[164:165], v211 offset:40960\n" \
"v_mfma_f32_16x16x32_bf16 v[104:107], a[136:139], v[220:223], v[104:107]\n" \
"v_exp_f32_e64 v146, v146\n" \
"v_mfma_f32_16x16x32_bf16 v[108:111], a[172:175], v[204:207], v[108:111]\n" \
"ds_read_b64_tr_b16 a[166:167], v211 offset:45056\n" \
"v_mfma_f32_16x16x32_bf16 v[108:111], a[140:143], v[220:223], v[108:111]\n" \
"v_exp_f32_e64 v147, v147\n" \
"v_smfmac_f32_16x16x64_bf16 v[180:183], a[192:195], v[216:223], v228\n" \
"ds_read_b64_tr_b16 a[168:169], %[vvb] offset:41472\n" \
"v_mfma_f32_16x16x32_bf16 v[152:155], a[96:99], a[48:51], v[172:175]\n" \
"v_exp_f32_e64 v148, v148\n" \
"v_mfma_f32_16x16x32_bf16 v[152:155], a[100:103], a[52:55], v[152:155]\n" \
"ds_read_b64_tr_b16 a[170:171], %[vvb] offset:45568\n" \
"v_mfma_f32_16x16x32_bf16 v[152:155], a[104:107], a[56:59], v[152:155]\n" \
"v_exp_f32_e64 v149, v149\n" \
"v_mfma_f32_16x16x32_bf16 v[152:155], a[108:111], a[60:63], v[152:155]\n" \
"v_cvt_pk_bf16_f32 v200, v144, v145\n" \
"v_cvt_pk_bf16_f32 v201, v146, v147\n" \
"v_mfma_f32_16x16x32_bf16 v[156:159], a[112:115], a[48:51], v[172:175]\n" \
"v_exp_f32_e64 v150, v150\n" \
"v_mfma_f32_16x16x32_bf16 v[156:159], a[116:119], a[52:55], v[156:159]\n" \
"ds_read_b64_tr_b16 a[172:173], v211 offset:41472\n" \
"v_mfma_f32_16x16x32_bf16 v[156:159], a[120:123], a[56:59], v[156:159]\n" \
"buffer_load_dwordx4 v215, %[srdv], s48 offen offset:3072 lds\n" \
"v_mfma_f32_16x16x32_bf16 v[156:159], a[124:127], a[60:63], v[156:159]\n" \
"v_exp_f32_e64 v151, v151\n" \
"v_mfma_f32_16x16x32_bf16 v[112:115], a[176:179], v[204:207], v[112:115]\n" \
"ds_read_b64_tr_b16 a[174:175], v211 offset:45568\n" \
"ds_read_b64_tr_b16 a[176:177], %[vvb] offset:41984\n" \
"v_mfma_f32_16x16x32_bf16 v[112:115], a[144:147], v[220:223], v[112:115]\n" \
"v_exp_f32_e64 v152, v152\n" \
"v_mfma_f32_16x16x32_bf16 v[116:119], a[180:183], v[204:207], v[116:119]\n" \
"v_cvt_pk_bf16_f32 v202, v148, v149\n" \
"v_cvt_pk_bf16_f32 v203, v150, v151\n" \
"v_mfma_f32_16x16x32_bf16 v[116:119], a[148:151], v[220:223], v[116:119]\n" \
"v_exp_f32_e64 v153, v153\n" \
"v_mfma_f32_16x16x32_bf16 v[120:123], a[184:187], v[204:207], v[120:123]\n" \
"v_exp_f32_e64 v154, v154\n" \
"v_mfma_f32_16x16x32_bf16 v[120:123], a[152:155], v[220:223], v[120:123]\n" \
"ds_read_b64_tr_b16 a[178:179], %[vvb] offset:46080\n" \
"v_mfma_f32_16x16x32_bf16 v[124:127], a[188:191], v[204:207], v[124:127]\n" \
"v_exp_f32_e64 v155, v155\n" \
"v_mfma_f32_16x16x32_bf16 v[124:127], a[156:159], v[220:223], v[124:127]\n" \
"ds_read_b64_tr_b16 a[180:181], v211 offset:41984\n" \
"s_waitcnt lgkmcnt(13)\n" \
"v_mfma_f32_16x16x32_bf16 v[128:131], a[64:67], a[0:3], v[160:163]\n" \
"v_exp_f32_e32 v156, v156\n" \
"v_mfma_f32_16x16x32_bf16 v[128:131], a[68:71], a[4:7], v[128:131]\n" \
"ds_read_b64_tr_b16 a[182:183], v211 offset:46080\n" \
"s_add_u32 m0, %[m0b], 32768\n" \
"v_mfma_f32_16x16x32_bf16 v[128:131], a[72:75], a[8:11], v[128:131]\n" \
"v_exp_f32_e64 v157, v157\n" \
"v_mfma_f32_16x16x32_bf16 v[128:131], a[76:79], a[12:15], v[128:131]\n" \
"v_cvt_pk_bf16_f32 v204, v152, v153\n" \
"v_cvt_pk_bf16_f32 v205, v154, v155\n" \
"v_mfma_f32_16x16x32_bf16 v[132:135], a[80:83], a[0:3], v[160:163]\n" \
"v_exp_f32_e64 v158, v158\n" \
"v_mfma_f32_16x16x32_bf16 v[132:135], a[84:87], a[4:7], v[132:135]\n" \
"ds_read_b64_tr_b16 a[184:185], %[vvb] offset:42496\n" \
"s_waitcnt lgkmcnt(5)\n" \
"v_mfma_f32_16x16x32_bf16 v[132:135], a[88:91], a[8:11], v[132:135]\n" \
"buffer_load_dwordx4 %[kd0], %[srdk], s48 offen offset:0 lds\n" \
"v_mfma_f32_16x16x32_bf16 v[132:135], a[92:95], a[12:15], v[132:135]\n" \
"v_exp_f32_e32 v159, v159\n" \
"v_mfma_f32_16x16x32_bf16 v[64:67], a[128:131], v[216:219], v[64:67]\n" \
"ds_read_b64_tr_b16 a[186:187], %[vvb] offset:46592\n" \
"ds_read_b64_tr_b16 a[188:189], v211 offset:42496\n" \
"v_mfma_f32_16x16x32_bf16 v[64:67], a[160:163], v[200:203], v[64:67]\n" \
"v_exp_f32_e64 v128, v128\n" \
"v_mfma_f32_16x16x32_bf16 v[68:71], a[132:135], v[216:219], v[68:71]\n" \
"v_cvt_pk_bf16_f32 v206, v156, v157\n" \
"v_cvt_pk_bf16_f32 v207, v158, v159\n" \
"v_mfma_f32_16x16x32_bf16 v[68:71], a[164:167], v[200:203], v[68:71]\n" \
"v_exp_f32_e64 v129, v129\n" \
"v_mfma_f32_16x16x32_bf16 v[72:75], a[136:139], v[216:219], v[72:75]\n" \
"ds_read_b64_tr_b16 a[190:191], v211 offset:46592\n" \
"ds_read_b64_tr_b16 a[128:129], %[vvb] offset:49152\n" \
"v_mfma_f32_16x16x32_bf16 v[72:75], a[168:171], v[200:203], v[72:75]\n" \
"v_exp_f32_e64 v130, v130\n" \
"v_mfma_f32_16x16x32_bf16 v[76:79], a[140:143], v[216:219], v[76:79]\n" \
"ds_read_b64_tr_b16 a[130:131], %[vvb] offset:53248\n" \
"ds_read_b64_tr_b16 a[132:133], v211 offset:49152\n" \
"v_mfma_f32_16x16x32_bf16 v[76:79], a[172:175], v[200:203], v[76:79]\n" \
"v_exp_f32_e64 v131, v131\n" \
"v_smfmac_f32_16x16x64_bf16 v[176:179], a[192:195], v[192:199], v228\n" \
"ds_read_b64_tr_b16 a[134:135], v211 offset:53248\n" \
"ds_read_b64_tr_b16 a[136:137], %[vvb] offset:49664\n" \
"buffer_store_dwordx4 a[196:199], v235, %[srdz], s62 offen offset:1024 sc1\n" \
"v_mfma_f32_16x16x32_bf16 v[136:139], a[64:67], a[16:19], v[164:167]\n" \
"v_exp_f32_e64 v132, v132\n" \
"v_mfma_f32_16x16x32_bf16 v[136:139], a[68:71], a[20:23], v[136:139]\n" \
"ds_read_b64_tr_b16 a[138:139], %[vvb] offset:53760\n" \
"ds_read_b64_tr_b16 a[140:141], v211 offset:49664\n" \
"v_mfma_f32_16x16x32_bf16 v[136:139], a[72:75], a[24:27], v[136:139]\n" \
"v_exp_f32_e64 v133, v133\n" \
"v_mfma_f32_16x16x32_bf16 v[136:139], a[76:79], a[28:31], v[136:139]\n" \
"v_cvt_pk_bf16_f32 v184, v128, v129\n" \
"v_cvt_pk_bf16_f32 v185, v130, v131\n" \
"v_mfma_f32_16x16x32_bf16 v[140:143], a[80:83], a[16:19], v[164:167]\n" \
"v_exp_f32_e64 v134, v134\n" \
"v_mfma_f32_16x16x32_bf16 v[140:143], a[84:87], a[20:23], v[140:143]\n" \
"ds_read_b64_tr_b16 a[142:143], v211 offset:53760\n" \
"s_waitcnt lgkmcnt(8)\n" \
"v_mfma_f32_16x16x32_bf16 v[140:143], a[88:91], a[24:27], v[140:143]\n" \
"buffer_load_dwordx4 v212, %[srdk], s48 offen offset:1024 lds\n" \
"v_mfma_f32_16x16x32_bf16 v[140:143], a[92:95], a[28:31], v[140:143]\n" \
"v_exp_f32_e32 v135, v135\n" \
"v_mfma_f32_16x16x32_bf16 v[80:83], a[144:147], v[216:219], v[80:83]\n" \
"ds_read_b64_tr_b16 a[144:145], %[vvb] offset:50176\n" \
"ds_read_b64_tr_b16 a[146:147], %[vvb] offset:54272\n" \
"v_mfma_f32_16x16x32_bf16 v[80:83], a[176:179], v[200:203], v[80:83]\n" \
"v_exp_f32_e64 v136, v136\n" \
"v_mfma_f32_16x16x32_bf16 v[84:87], a[148:151], v[216:219], v[84:87]\n" \
"v_cvt_pk_bf16_f32 v186, v132, v133\n" \
"v_cvt_pk_bf16_f32 v187, v134, v135\n" \
"v_mfma_f32_16x16x32_bf16 v[84:87], a[180:183], v[200:203], v[84:87]\n" \
"v_exp_f32_e64 v137, v137\n" \
"v_mfma_f32_16x16x32_bf16 v[88:91], a[152:155], v[216:219], v[88:91]\n" \
"v_exp_f32_e64 v138, v138\n" \
"v_mfma_f32_16x16x32_bf16 v[88:91], a[184:187], v[200:203], v[88:91]\n" \
"ds_read_b64_tr_b16 a[148:149], v211 offset:50176\n" \
"ds_read_b64_tr_b16 a[150:151], v211 offset:54272\n" \
"v_mfma_f32_16x16x32_bf16 v[92:95], a[156:159], v[216:219], v[92:95]\n" \
"v_exp_f32_e64 v139, v139\n" \
"v_mfma_f32_16x16x32_bf16 v[92:95], a[188:191], v[200:203], v[92:95]\n" \
"ds_read_b64_tr_b16 a[152:153], %[vvb] offset:50688\n" \
"global_load_dword v237, %[vct], %[pct]\n" \
"v_mfma_f32_16x16x32_bf16 v[144:147], a[64:67], a[32:35], v[168:171]\n" \
"v_exp_f32_e64 v140, v140\n" \
"v_mfma_f32_16x16x32_bf16 v[144:147], a[68:71], a[36:39], v[144:147]\n" \
"ds_read_b64_tr_b16 a[154:155], %[vvb] offset:54784\n" \
"ds_read_b64_tr_b16 a[156:157], v211 offset:50688\n" \
"v_mfma_f32_16x16x32_bf16 v[144:147], a[72:75], a[40:43], v[144:147]\n" \
"v_exp_f32_e64 v141, v141\n" \
"v_mfma_f32_16x16x32_bf16 v[144:147], a[76:79], a[44:47], v[144:147]\n" \
"v_cvt_pk_bf16_f32 v188, v136, v137\n" \
"v_cvt_pk_bf16_f32 v189, v138, v139\n" \
"v_mfma_f32_16x16x32_bf16 v[148:151], a[80:83], a[32:35], v[168:171]\n" \
"v_exp_f32_e64 v142, v142\n" \
"v_mfma_f32_16x16x32_bf16 v[148:151], a[84:87], a[36:39], v[148:151]\n" \
"ds_read_b64_tr_b16 a[158:159], v211 offset:54784\n" \
"s_waitcnt lgkmcnt(8)\n" \
"v_mfma_f32_16x16x32_bf16 v[148:151], a[88:91], a[40:43], v[148:151]\n" \
"buffer_load_dwordx4 v213, %[srdk], s48 offen offset:2048 lds\n" \
"v_mfma_f32_16x16x32_bf16 v[148:151], a[92:95], a[44:47], v[148:151]\n" \
"v_exp_f32_e32 v143, v143\n" \
"v_mfma_f32_16x16x32_bf16 v[0:3], a[160:163], v[192:195], v[0:3]\n" \
"ds_read_b128 a[96:99], %[kb0] offset:57344\n" \
"v_mfma_f32_16x16x32_bf16 v[0:3], a[128:131], v[184:187], v[0:3]\n" \
"v_exp_f32_e64 v144, v144\n" \
"v_mfma_f32_16x16x32_bf16 v[4:7], a[164:167], v[192:195], v[4:7]\n" \
"v_cvt_pk_bf16_f32 v190, v140, v141\n" \
"v_cvt_pk_bf16_f32 v191, v142, v143\n" \
"v_mfma_f32_16x16x32_bf16 v[4:7], a[132:135], v[184:187], v[4:7]\n" \
"v_exp_f32_e64 v145, v145\n" \
"v_mfma_f32_16x16x32_bf16 v[8:11], a[168:171], v[192:195], v[8:11]\n" \
"ds_read_b128 a[100:103], v208 offset:57344\n" \
"v_mfma_f32_16x16x32_bf16 v[8:11], a[136:139], v[184:187], v[8:11]\n" \
"v_exp_f32_e64 v146, v146\n" \
"v_mfma_f32_16x16x32_bf16 v[12:15], a[172:175], v[192:195], v[12:15]\n" \
"ds_read_b128 a[104:107], v209 offset:57344\n" \
"v_mfma_f32_16x16x32_bf16 v[12:15], a[140:143], v[184:187], v[12:15]\n" \
"v_exp_f32_e64 v147, v147\n" \
"v_smfmac_f32_16x16x64_bf16 v[180:183], a[192:195], v[200:207], v228\n" \
"ds_read_b128 a[108:111], v210 offset:57344\n" \
"v_mfma_f32_16x16x32_bf16 v[152:155], a[64:67], a[48:51], v[172:175]\n" \
"v_exp_f32_e64 v148, v148\n" \
"v_mfma_f32_16x16x32_bf16 v[152:155], a[68:71], a[52:55], v[152:155]\n" \
"ds_read_b128 a[112:115], %[kb0] offset:61440\n" \
"v_mfma_f32_16x16x32_bf16 v[152:155], a[72:75], a[56:59], v[152:155]\n" \
"v_exp_f32_e64 v149, v149\n" \
"v_mfma_f32_16x16x32_bf16 v[152:155], a[76:79], a[60:63], v[152:155]\n" \
"v_cvt_pk_bf16_f32 v216, v144, v145\n" \
"v_cvt_pk_bf16_f32 v217, v146, v147\n" \
"v_mfma_f32_16x16x32_bf16 v[156:159], a[80:83], a[48:51], v[172:175]\n" \
"v_exp_f32_e64 v150, v150\n" \
"v_mfma_f32_16x16x32_bf16 v[156:159], a[84:87], a[52:55], v[156:159]\n" \
"ds_read_b128 a[116:119], v208 offset:61440\n" \
"s_waitcnt lgkmcnt(6)\n" \
"v_mfma_f32_16x16x32_bf16 v[156:159], a[88:91], a[56:59], v[156:159]\n" \
"buffer_load_dwordx4 v214, %[srdk], s48 offen offset:3072 lds\n" \
"v_mfma_f32_16x16x32_bf16 v[156:159], a[92:95], a[60:63], v[156:159]\n" \
"v_exp_f32_e32 v151, v151\n" \
"v_mfma_f32_16x16x32_bf16 v[16:19], a[176:179], v[192:195], v[16:19]\n" \
"ds_read_b128 a[120:123], v209 offset:61440\n" \
"v_mfma_f32_16x16x32_bf16 v[16:19], a[144:147], v[184:187], v[16:19]\n" \
"v_exp_f32_e64 v152, v152\n" \
"v_mfma_f32_16x16x32_bf16 v[20:23], a[180:183], v[192:195], v[20:23]\n" \
"v_cvt_pk_bf16_f32 v218, v148, v149\n" \
"v_cvt_pk_bf16_f32 v219, v150, v151\n" \
"v_mfma_f32_16x16x32_bf16 v[20:23], a[148:151], v[184:187], v[20:23]\n" \
"v_exp_f32_e64 v153, v153\n" \
"v_mfma_f32_16x16x32_bf16 v[24:27], a[184:187], v[192:195], v[24:27]\n" \
"v_exp_f32_e64 v154, v154\n" \
"v_mfma_f32_16x16x32_bf16 v[24:27], a[152:155], v[184:187], v[24:27]\n" \
"ds_read_b128 a[124:127], v210 offset:61440\n" \
"s_add_u32 s48, s48, 0x4000\n" \
"s_mov_b32 m0, s44\n" \
"s_addk_i32 s44, 0x400\n" \
"buffer_load_dwordx4 v229, s[64:67], s45 offen offset:0 lds\n" \
"s_add_u32 s45, s45, 229376\n" \
"v_mfma_f32_16x16x32_bf16 v[28:31], a[188:191], v[192:195], v[28:31]\n" \
"v_exp_f32_e64 v155, v155\n" \
"v_mfma_f32_16x16x32_bf16 v[28:31], a[156:159], v[184:187], v[28:31]\n" \
"s_waitcnt vmcnt(16) lgkmcnt(2)\n" \
"s_barrier\n" \
"v_mfma_f32_16x16x32_bf16 v[128:131], a[96:99], a[0:3], v[160:163]\n" \
"v_exp_f32_e64 v156, v156\n" \
"v_mfma_f32_16x16x32_bf16 v[128:131], a[100:103], a[4:7], v[128:131]\n" \
"s_add_u32 m0, %[m0b], 98304\n" \
"v_mfma_f32_16x16x32_bf16 v[128:131], a[104:107], a[8:11], v[128:131]\n" \
"v_exp_f32_e64 v157, v157\n" \
"v_mfma_f32_16x16x32_bf16 v[128:131], a[108:111], a[12:15], v[128:131]\n" \
"v_cvt_pk_bf16_f32 v220, v152, v153\n" \
"v_cvt_pk_bf16_f32 v221, v154, v155\n" \
"v_mfma_f32_16x16x32_bf16 v[132:135], a[112:115], a[0:3], v[160:163]\n" \
"v_exp_f32_e64 v158, v158\n" \
"v_mfma_f32_16x16x32_bf16 v[132:135], a[116:119], a[4:7], v[132:135]\n" \
"s_waitcnt lgkmcnt(0)\n" \
"v_mfma_f32_16x16x32_bf16 v[132:135], a[120:123], a[8:11], v[132:135]\n" \
"buffer_load_dwordx4 %[vd0], %[srdv], s48 offen offset:0 lds\n" \
"v_mfma_f32_16x16x32_bf16 v[132:135], a[124:127], a[12:15], v[132:135]\n" \
"v_exp_f32_e32 v159, v159\n" \
"v_mfma_f32_16x16x32_bf16 v[32:35], a[160:163], v[196:199], v[32:35]\n" \
"ds_read_b128 a[64:67], %[kb0] offset:0\n" \
"v_mfma_f32_16x16x32_bf16 v[32:35], a[128:131], v[188:191], v[32:35]\n" \
"v_exp_f32_e64 v128, v128\n" \
"v_mfma_f32_16x16x32_bf16 v[36:39], a[164:167], v[196:199], v[36:39]\n" \
"v_cvt_pk_bf16_f32 v222, v156, v157\n" \
"v_cvt_pk_bf16_f32 v223, v158, v159\n" \
"v_mfma_f32_16x16x32_bf16 v[36:39], a[132:135], v[188:191], v[36:39]\n" \
"v_exp_f32_e64 v129, v129\n" \
"v_mfma_f32_16x16x32_bf16 v[40:43], a[168:171], v[196:199], v[40:43]\n" \
"ds_read_b128 a[68:71], v208 offset:0\n" \
"v_mfma_f32_16x16x32_bf16 v[40:43], a[136:139], v[188:191], v[40:43]\n" \
"v_exp_f32_e64 v130, v130\n" \
"v_mfma_f32_16x16x32_bf16 v[44:47], a[172:175], v[196:199], v[44:47]\n" \
"ds_read_b128 a[72:75], v209 offset:0\n" \
"v_mfma_f32_16x16x32_bf16 v[44:47], a[140:143], v[188:191], v[44:47]\n" \
"v_exp_f32_e64 v131, v131\n" \
"v_smfmac_f32_16x16x64_bf16 v[176:179], a[192:195], v[184:191], v228\n" \
"buffer_store_dwordx4 a[196:199], v235, %[srdz], s63 offen offset:2048 sc1\n" \
"s_add_u32 s63, s63, 3072\n" \
"v_mfma_f32_16x16x32_bf16 v[136:139], a[96:99], a[16:19], v[164:167]\n" \
"v_exp_f32_e64 v132, v132\n" \
"v_mfma_f32_16x16x32_bf16 v[136:139], a[100:103], a[20:23], v[136:139]\n" \
"ds_read_b128 a[76:79], v210 offset:0\n" \
"v_mfma_f32_16x16x32_bf16 v[136:139], a[104:107], a[24:27], v[136:139]\n" \
"v_exp_f32_e64 v133, v133\n" \
"v_mfma_f32_16x16x32_bf16 v[136:139], a[108:111], a[28:31], v[136:139]\n" \
"v_cvt_pk_bf16_f32 v192, v128, v129\n" \
"v_cvt_pk_bf16_f32 v193, v130, v131\n" \
"v_mfma_f32_16x16x32_bf16 v[140:143], a[112:115], a[16:19], v[164:167]\n" \
"v_exp_f32_e64 v134, v134\n" \
"v_mfma_f32_16x16x32_bf16 v[140:143], a[116:119], a[20:23], v[140:143]\n" \
"ds_read_b128 a[80:83], %[kb0] offset:4096\n" \
"v_mfma_f32_16x16x32_bf16 v[140:143], a[120:123], a[24:27], v[140:143]\n" \
"buffer_load_dwordx4 v215, %[srdv], s48 offen offset:1024 lds\n" \
"v_mfma_f32_16x16x32_bf16 v[140:143], a[124:127], a[28:31], v[140:143]\n" \
"v_exp_f32_e64 v135, v135\n" \
"v_mfma_f32_16x16x32_bf16 v[48:51], a[176:179], v[196:199], v[48:51]\n" \
"ds_read_b128 a[84:87], v208 offset:4096\n" \
"v_mfma_f32_16x16x32_bf16 v[48:51], a[144:147], v[188:191], v[48:51]\n" \
"v_exp_f32_e64 v136, v136\n" \
"v_mfma_f32_16x16x32_bf16 v[52:55], a[180:183], v[196:199], v[52:55]\n" \
"v_cvt_pk_bf16_f32 v194, v132, v133\n" \
"v_cvt_pk_bf16_f32 v195, v134, v135\n" \
"v_mfma_f32_16x16x32_bf16 v[52:55], a[148:151], v[188:191], v[52:55]\n" \
"v_exp_f32_e64 v137, v137\n" \
"v_mfma_f32_16x16x32_bf16 v[56:59], a[184:187], v[196:199], v[56:59]\n" \
"v_exp_f32_e64 v138, v138\n" \
"v_mfma_f32_16x16x32_bf16 v[56:59], a[152:155], v[188:191], v[56:59]\n" \
"ds_read_b128 a[88:91], v209 offset:4096\n" \
"v_mfma_f32_16x16x32_bf16 v[60:63], a[188:191], v[196:199], v[60:63]\n" \
"v_exp_f32_e64 v139, v139\n" \
"v_mfma_f32_16x16x32_bf16 v[60:63], a[156:159], v[188:191], v[60:63]\n" \
"s_cmp_lg_u32 %[wid], 2\n" \
"s_cbranch_scc1 LtB7_%=\n" \
"global_load_dword v237, v236, s[58:59]\n" \
"s_add_u32 s61, s48, 49152\n" \
"s_mov_b32 exec_hi, 0\n" \
"s_nop 0\n" \
"buffer_load_dword v237, v224, %[srdp], s61 offen\n" \
"s_mov_b32 exec_hi, -1\n" \
"s_nop 0\n" \
"LtB7_%=:\n" \
"v_mfma_f32_16x16x32_bf16 v[144:147], a[96:99], a[32:35], v[168:171]\n" \
"v_exp_f32_e64 v140, v140\n" \
"v_mfma_f32_16x16x32_bf16 v[144:147], a[100:103], a[36:39], v[144:147]\n" \
"ds_read_b128 a[92:95], v210 offset:4096\n" \
"v_mfma_f32_16x16x32_bf16 v[144:147], a[104:107], a[40:43], v[144:147]\n" \
"v_exp_f32_e64 v141, v141\n" \
"v_mfma_f32_16x16x32_bf16 v[144:147], a[108:111], a[44:47], v[144:147]\n" \
"v_cvt_pk_bf16_f32 v196, v136, v137\n" \
"v_cvt_pk_bf16_f32 v197, v138, v139\n" \
"v_mfma_f32_16x16x32_bf16 v[148:151], a[112:115], a[32:35], v[168:171]\n" \
"v_exp_f32_e64 v142, v142\n" \
"v_mfma_f32_16x16x32_bf16 v[148:151], a[116:119], a[36:39], v[148:151]\n" \
"v_mfma_f32_16x16x32_bf16 v[148:151], a[120:123], a[40:43], v[148:151]\n" \
"buffer_load_dwordx4 %[vd0], %[srdv], s48 offen offset:2048 lds\n" \
"v_mfma_f32_16x16x32_bf16 v[148:151], a[124:127], a[44:47], v[148:151]\n" \
"v_exp_f32_e64 v143, v143\n" \
"v_mfma_f32_16x16x32_bf16 v[96:99], a[160:163], v[204:207], v[96:99]\n" \
"ds_read_b64_tr_b16 a[160:161], %[vvb] offset:57344\n" \
"v_mfma_f32_16x16x32_bf16 v[96:99], a[128:131], v[220:223], v[96:99]\n" \
"v_exp_f32_e64 v144, v144\n" \
"v_mfma_f32_16x16x32_bf16 v[100:103], a[164:167], v[204:207], v[100:103]\n" \
"v_cvt_pk_bf16_f32 v198, v140, v141\n" \
"v_cvt_pk_bf16_f32 v199, v142, v143\n" \
"v_mfma_f32_16x16x32_bf16 v[100:103], a[132:135], v[220:223], v[100:103]\n" \
"v_exp_f32_e64 v145, v145\n" \
"v_mfma_f32_16x16x32_bf16 v[104:107], a[168:171], v[204:207], v[104:107]\n" \
"ds_read_b64_tr_b16 a[162:163], %[vvb] offset:61440\n" \
"ds_read_b64_tr_b16 a[164:165], v211 offset:57344\n" \
"v_mfma_f32_16x16x32_bf16 v[104:107], a[136:139], v[220:223], v[104:107]\n" \
"v_exp_f32_e64 v146, v146\n" \
"v_mfma_f32_16x16x32_bf16 v[108:111], a[172:175], v[204:207], v[108:111]\n" \
"ds_read_b64_tr_b16 a[166:167], v211 offset:61440\n" \
"v_mfma_f32_16x16x32_bf16 v[108:111], a[140:143], v[220:223], v[108:111]\n" \
"v_exp_f32_e64 v147, v147\n" \
"v_smfmac_f32_16x16x64_bf16 v[180:183], a[192:195], v[216:223], v228\n" \
"ds_read_b64_tr_b16 a[168:169], %[vvb] offset:57856\n" \
"v_mfma_f32_16x16x32_bf16 v[152:155], a[96:99], a[48:51], v[172:175]\n" \
"v_exp_f32_e64 v148, v148\n" \
"v_mfma_f32_16x16x32_bf16 v[152:155], a[100:103], a[52:55], v[152:155]\n" \
"ds_read_b64_tr_b16 a[170:171], %[vvb] offset:61952\n" \
"v_mfma_f32_16x16x32_bf16 v[152:155], a[104:107], a[56:59], v[152:155]\n" \
"v_exp_f32_e64 v149, v149\n" \
"v_mfma_f32_16x16x32_bf16 v[152:155], a[108:111], a[60:63], v[152:155]\n" \
"v_cvt_pk_bf16_f32 v200, v144, v145\n" \
"v_cvt_pk_bf16_f32 v201, v146, v147\n" \
"v_mfma_f32_16x16x32_bf16 v[156:159], a[112:115], a[48:51], v[172:175]\n" \
".if . - adaln_body != 28672\n" \
".error \"size model\"\n" \
".endif\n" \
"Lk6_%=:\n" \
"s_cbranch_execz Lk7_%=\n" \
"s_nop 0\n" \
"v_exp_f32_e64 v150, v150\n" \
"v_mfma_f32_16x16x32_bf16 v[156:159], a[116:119], a[52:55], v[156:159]\n" \
"ds_read_b64_tr_b16 a[172:173], v211 offset:57856\n" \
"v_mfma_f32_16x16x32_bf16 v[156:159], a[120:123], a[56:59], v[156:159]\n" \
"buffer_load_dwordx4 v215, %[srdv], s48 offen offset:3072 lds\n" \
"v_mfma_f32_16x16x32_bf16 v[156:159], a[124:127], a[60:63], v[156:159]\n" \
"v_exp_f32_e64 v151, v151\n" \
"v_mfma_f32_16x16x32_bf16 v[112:115], a[176:179], v[204:207], v[112:115]\n" \
"ds_read_b64_tr_b16 a[174:175], v211 offset:61952\n" \
"ds_read_b64_tr_b16 a[176:177], %[vvb] offset:58368\n" \
"v_mfma_f32_16x16x32_bf16 v[112:115], a[144:147], v[220:223], v[112:115]\n" \
"v_exp_f32_e64 v152, v152\n" \
"v_mfma_f32_16x16x32_bf16 v[116:119], a[180:183], v[204:207], v[116:119]\n" \
"v_cvt_pk_bf16_f32 v202, v148, v149\n" \
"v_cvt_pk_bf16_f32 v203, v150, v151\n" \
"v_mfma_f32_16x16x32_bf16 v[116:119], a[148:151], v[220:223], v[116:119]\n" \
"v_exp_f32_e64 v153, v153\n" \
"v_mfma_f32_16x16x32_bf16 v[120:123], a[184:187], v[204:207], v[120:123]\n" \
"v_exp_f32_e64 v154, v154\n" \
"v_mfma_f32_16x16x32_bf16 v[120:123], a[152:155], v[220:223], v[120:123]\n" \
"ds_read_b64_tr_b16 a[178:179], %[vvb] offset:62464\n" \
"v_mfma_f32_16x16x32_bf16 v[124:127], a[188:191], v[204:207], v[124:127]\n" \
"v_exp_f32_e64 v155, v155\n" \
"v_mfma_f32_16x16x32_bf16 v[124:127], a[156:159], v[220:223], v[124:127]\n" \
"ds_read_b64_tr_b16 a[180:181], v211 offset:58368\n" \
"s_waitcnt lgkmcnt(13)\n" \
"v_mfma_f32_16x16x32_bf16 v[128:131], a[64:67], a[0:3], v[160:163]\n" \
"v_exp_f32_e32 v156, v156\n" \
"v_mfma_f32_16x16x32_bf16 v[128:131], a[68:71], a[4:7], v[128:131]\n" \
"ds_read_b64_tr_b16 a[182:183], v211 offset:62464\n" \
"s_add_u32 m0, %[m0b], 49152\n" \
"v_mfma_f32_16x16x32_bf16 v[128:131], a[72:75], a[8:11], v[128:131]\n" \
"v_exp_f32_e64 v157, v157\n" \
"v_mfma_f32_16x16x32_bf16 v[128:131], a[76:79], a[12:15], v[128:131]\n" \
"v_cvt_pk_bf16_f32 v204, v152, v153\n" \
"v_cvt_pk_bf16_f32 v205, v154, v155\n" \
"v_mfma_f32_16x16x32_bf16 v[132:135], a[80:83], a[0:3], v[160:163]\n" \
"v_exp_f32_e64 v158, v158\n" \
"v_mfma_f32_16x16x32_bf16 v[132:135], a[84:87], a[4:7], v[132:135]\n" \
"ds_read_b64_tr_b16 a[184:185], %[vvb] offset:58880\n" \
"s_waitcnt lgkmcnt(5)\n" \
"v_mfma_f32_16x16x32_bf16 v[132:135], a[88:91], a[8:11], v[132:135]\n" \
"buffer_load_dwordx4 %[kd0], %[srdk], s48 offen offset:0 lds\n" \
"v_mfma_f32_16x16x32_bf16 v[132:135], a[92:95], a[12:15], v[132:135]\n" \
"v_exp_f32_e32 v159, v159\n" \
"v_mfma_f32_16x16x32_bf16 v[64:67], a[128:131], v[216:219], v[64:67]\n" \
"ds_read_b64_tr_b16 a[186:187], %[vvb] offset:62976\n" \
"ds_read_b64_tr_b16 a[188:189], v211 offset:58880\n" \
"v_mfma_f32_16x16x32_bf16 v[64:67], a[160:163], v[200:203], v[64:67]\n" \
"v_exp_f32_e64 v128, v128\n" \
"v_mfma_f32_16x16x32_bf16 v[68:71], a[132:135], v[216:219], v[68:71]\n" \
"v_cvt_pk_bf16_f32 v206, v156, v157\n" \
"v_cvt_pk_bf16_f32 v207, v158, v159\n" \
"v_mfma_f32_16x16x32_bf16 v[68:71], a[164:167], v[200:203], v[68:71]\n" \
"v_exp_f32_e64 v129, v129\n" \
"v_mfma_f32_16x16x32_bf16 v[72:75], a[136:139], v[216:219], v[72:75]\n" \
"ds_read_b64_tr_b16 a[190:191], v211 offset:62976\n" \
"ds_read_b64_tr_b16 a[128:129], %[vvb] offset:0\n" \
"v_mfma_f32_16x16x32_bf16 v[72:75], a[168:171], v[200:203], v[72:75]\n" \
"v_exp_f32_e64 v130, v130\n" \
"v_mfma_f32_16x16x32_bf16 v[76:79], a[140:143], v[216:219], v[76:79]\n" \
"ds_read_b64_tr_b16 a[130:131], %[vvb] offset:4096\n" \
"ds_read_b64_tr_b16 a[132:133], v211 offset:0\n" \
"v_mfma_f32_16x16x32_bf16 v[76:79], a[172:175], v[200:203], v[76:79]\n" \
"v_exp_f32_e64 v131, v131\n" \
"v_smfmac_f32_16x16x64_bf16 v[176:179], a[192:195], v[192:199], v228\n" \
"ds_read_b64_tr_b16 a[134:135], v211 offset:4096\n" \
"ds_read_b64_tr_b16 a[136:137], %[vvb] offset:512\n" \
"v_mfma_f32_16x16x32_bf16 v[136:139], a[64:67], a[16:19], v[164:167]\n" \
"v_exp_f32_e64 v132, v132\n" \
"v_mfma_f32_16x16x32_bf16 v[136:139], a[68:71], a[20:23], v[136:139]\n" \
"ds_read_b64_tr_b16 a[138:139], %[vvb] offset:4608\n" \
"ds_read_b64_tr_b16 a[140:141], v211 offset:512\n" \
"v_mfma_f32_16x16x32_bf16 v[136:139], a[72:75], a[24:27], v[136:139]\n" \
"v_exp_f32_e64 v133, v133\n" \
"v_mfma_f32_16x16x32_bf16 v[136:139], a[76:79], a[28:31], v[136:139]\n" \
"v_cvt_pk_bf16_f32 v184, v128, v129\n" \
"v_cvt_pk_bf16_f32 v185, v130, v131\n" \
"v_mfma_f32_16x16x32_bf16 v[140:143], a[80:83], a[16:19], v[164:167]\n" \
"v_exp_f32_e64 v134, v134\n" \
"v_mfma_f32_16x16x32_bf16 v[140:143], a[84:87], a[20:23], v[140:143]\n" \
"ds_read_b64_tr_b16 a[142:143], v211 offset:4608\n" \
"s_waitcnt lgkmcnt(8)\n" \
"v_mfma_f32_16x16x32_bf16 v[140:143], a[88:91], a[24:27], v[140:143]\n" \
"buffer_load_dwordx4 v212, %[srdk], s48 offen offset:1024 lds\n" \
"v_mfma_f32_16x16x32_bf16 v[140:143], a[92:95], a[28:31], v[140:143]\n" \
"v_exp_f32_e32 v135, v135\n" \
"v_mfma_f32_16x16x32_bf16 v[80:83], a[144:147], v[216:219], v[80:83]\n" \
"ds_read_b64_tr_b16 a[144:145], %[vvb] offset:1024\n" \
"ds_read_b64_tr_b16 a[146:147], %[vvb] offset:5120\n" \
"v_mfma_f32_16x16x32_bf16 v[80:83], a[176:179], v[200:203], v[80:83]\n" \
"v_exp_f32_e64 v136, v136\n" \
"v_mfma_f32_16x16x32_bf16 v[84:87], a[148:151], v[216:219], v[84:87]\n" \
"v_cvt_pk_bf16_f32 v186, v132, v133\n" \
"v_cvt_pk_bf16_f32 v187, v134, v135\n" \
"v_mfma_f32_16x16x32_bf16 v[84:87], a[180:183], v[200:203], v[84:87]\n" \
"v_exp_f32_e64 v137, v137\n" \
"v_mfma_f32_16x16x32_bf16 v[88:91], a[152:155], v[216:219], v[88:91]\n" \
"v_exp_f32_e64 v138, v138\n" \
"v_mfma_f32_16x16x32_bf16 v[88:91], a[184:187], v[200:203], v[88:91]\n" \
"ds_read_b64_tr_b16 a[148:149], v211 offset:1024\n" \
"ds_read_b64_tr_b16 a[150:151], v211 offset:5120\n" \
"v_mfma_f32_16x16x32_bf16 v[92:95], a[156:159], v[216:219], v[92:95]\n" \
"v_exp_f32_e64 v139, v139\n" \
"v_mfma_f32_16x16x32_bf16 v[92:95], a[188:191], v[200:203], v[92:95]\n" \
"ds_read_b64_tr_b16 a[152:153], %[vvb] offset:1536\n" \
"buffer_store_dwordx4 a[196:199], v235, %[srdz], s62 offen offset:2048 sc1\n" \
"s_add_u32 s62, s62, 3072\n" \
"v_mfma_f32_16x16x32_bf16 v[144:147], a[64:67], a[32:35], v[168:171]\n" \
"v_exp_f32_e64 v140, v140\n" \
"v_mfma_f32_16x16x32_bf16 v[144:147], a[68:71], a[36:39], v[144:147]\n" \
"ds_read_b64_tr_b16 a[154:155], %[vvb] offset:5632\n" \
"ds_read_b64_tr_b16 a[156:157], v211 offset:1536\n" \
"v_mfma_f32_16x16x32_bf16 v[144:147], a[72:75], a[40:43], v[144:147]\n" \
"v_exp_f32_e64 v141, v141\n" \
"v_mfma_f32_16x16x32_bf16 v[144:147], a[76:79], a[44:47], v[144:147]\n" \
"v_cvt_pk_bf16_f32 v188, v136, v137\n" \
"v_cvt_pk_bf16_f32 v189, v138, v139\n" \
"v_mfma_f32_16x16x32_bf16 v[148:151], a[80:83], a[32:35], v[168:171]\n" \
"v_exp_f32_e64 v142, v142\n" \
"v_mfma_f32_16x16x32_bf16 v[148:151], a[84:87], a[36:39], v[148:151]\n" \
"ds_read_b64_tr_b16 a[158:159], v211 offset:5632\n" \
"s_waitcnt lgkmcnt(8)\n" \
"v_mfma_f32_16x16x32_bf16 v[148:151], a[88:91], a[40:43], v[148:151]\n" \
"buffer_load_dwordx4 v213, %[srdk], s48 offen offset:2048 lds\n" \
"v_mfma_f32_16x16x32_bf16 v[148:151], a[92:95], a[44:47], v[148:151]\n" \
"v_exp_f32_e32 v143, v143\n" \
"v_mfma_f32_16x16x32_bf16 v[0:3], a[160:163], v[192:195], v[0:3]\n" \
"ds_read_b128 a[96:99], %[kb0] offset:8192\n" \
"v_mfma_f32_16x16x32_bf16 v[0:3], a[128:131], v[184:187], v[0:3]\n" \
"v_exp_f32_e64 v144, v144\n" \
"v_mfma_f32_16x16x32_bf16 v[4:7], a[164:167], v[192:195], v[4:7]\n" \
"v_cvt_pk_bf16_f32 v190, v140, v141\n" \
"v_cvt_pk_bf16_f32 v191, v142, v143\n" \
"v_mfma_f32_16x16x32_bf16 v[4:7], a[132:135], v[184:187], v[4:7]\n" \
"v_exp_f32_e64 v145, v145\n" \
"v_mfma_f32_16x16x32_bf16 v[8:11], a[168:171], v[192:195], v[8:11]\n" \
"ds_read_b128 a[100:103], v208 offset:8192\n" \
"v_mfma_f32_16x16x32_bf16 v[8:11], a[136:139], v[184:187], v[8:11]\n" \
"v_exp_f32_e64 v146, v146\n" \
"v_mfma_f32_16x16x32_bf16 v[12:15], a[172:175], v[192:195], v[12:15]\n" \
"ds_read_b128 a[104:107], v209 offset:8192\n" \
"v_mfma_f32_16x16x32_bf16 v[12:15], a[140:143], v[184:187], v[12:15]\n" \
"v_exp_f32_e64 v147, v147\n" \
"v_smfmac_f32_16x16x64_bf16 v[180:183], a[192:195], v[200:207], v228\n" \
"ds_read_b128 a[108:111], v210 offset:8192\n" \
"v_mfma_f32_16x16x32_bf16 v[152:155], a[64:67], a[48:51], v[172:175]\n" \
"v_exp_f32_e64 v148, v148\n" \
"v_mfma_f32_16x16x32_bf16 v[152:155], a[68:71], a[52:55], v[152:155]\n" \
"ds_read_b128 a[112:115], %[kb0] offset:12288\n" \
"v_mfma_f32_16x16x32_bf16 v[152:155], a[72:75], a[56:59], v[152:155]\n" \
"v_exp_f32_e64 v149, v149\n" \
"v_mfma_f32_16x16x32_bf16 v[152:155], a[76:79], a[60:63], v[152:155]\n" \
"v_cvt_pk_bf16_f32 v216, v144, v145\n" \
"v_cvt_pk_bf16_f32 v217, v146, v147\n" \
"v_mfma_f32_16x16x32_bf16 v[156:159], a[80:83], a[48:51], v[172:175]\n" \
"v_exp_f32_e64 v150, v150\n" \
"v_mfma_f32_16x16x32_bf16 v[156:159], a[84:87], a[52:55], v[156:159]\n" \
"ds_read_b128 a[116:119], v208 offset:12288\n" \
"s_waitcnt lgkmcnt(6)\n" \
"v_mfma_f32_16x16x32_bf16 v[156:159], a[88:91], a[56:59], v[156:159]\n" \
"buffer_load_dwordx4 v214, %[srdk], s48 offen offset:3072 lds\n" \
"v_mfma_f32_16x16x32_bf16 v[156:159], a[92:95], a[60:63], v[156:159]\n" \
"v_exp_f32_e32 v151, v151\n" \
"v_mfma_f32_16x16x32_bf16 v[16:19], a[176:179], v[192:195], v[16:19]\n" \
"ds_read_b128 a[120:123], v209 offset:12288\n" \
"v_mfma_f32_16x16x32_bf16 v[16:19], a[144:147], v[184:187], v[16:19]\n" \
"v_exp_f32_e64 v152, v152\n" \
"v_mfma_f32_16x16x32_bf16 v[20:23], a[180:183], v[192:195], v[20:23]\n" \
"v_cvt_pk_bf16_f32 v218, v148, v149\n" \
"v_cvt_pk_bf16_f32 v219, v150, v151\n" \
"v_mfma_f32_16x16x32_bf16 v[20:23], a[148:151], v[184:187], v[20:23]\n" \
"v_exp_f32_e64 v153, v153\n" \
"v_mfma_f32_16x16x32_bf16 v[24:27], a[184:187], v[192:195], v[24:27]\n" \
"v_exp_f32_e64 v154, v154\n" \
"v_mfma_f32_16x16x32_bf16 v[24:27], a[152:155], v[184:187], v[24:27]\n" \
"ds_read_b128 a[124:127], v210 offset:12288\n" \
"s_add_u32 s48, s48, 0x4000\n" \
"v_mfma_f32_16x16x32_bf16 v[28:31], a[188:191], v[192:195], v[28:31]\n" \
"v_exp_f32_e64 v155, v155\n" \
"v_mfma_f32_16x16x32_bf16 v[28:31], a[156:159], v[184:187], v[28:31]\n" \
"s_waitcnt vmcnt(17) lgkmcnt(2)\n" \
"s_barrier\n" \
"v_mfma_f32_16x16x32_bf16 v[128:131], a[96:99], a[0:3], v[160:163]\n" \
"v_exp_f32_e64 v156, v156\n" \
"v_mfma_f32_16x16x32_bf16 v[128:131], a[100:103], a[4:7], v[128:131]\n" \
"s_add_u32 m0, %[m0b], 114688\n" \
"v_mfma_f32_16x16x32_bf16 v[128:131], a[104:107], a[8:11], v[128:131]\n" \
"v_exp_f32_e64 v157, v157\n" \
"v_mfma_f32_16x16x32_bf16 v[128:131], a[108:111], a[12:15], v[128:131]\n" \
"v_cvt_pk_bf16_f32 v220, v152, v153\n" \
"v_cvt_pk_bf16_f32 v221, v154, v155\n" \
"v_mfma_f32_16x16x32_bf16 v[132:135], a[112:115], a[0:3], v[160:163]\n" \
"v_exp_f32_e64 v158, v158\n" \
"v_mfma_f32_16x16x32_bf16 v[132:135], a[116:119], a[4:7], v[132:135]\n" \
"s_waitcnt lgkmcnt(0)\n" \
"v_mfma_f32_16x16x32_bf16 v[132:135], a[120:123], a[8:11], v[132:135]\n" \
"buffer_load_dwordx4 %[vd0], %[srdv], s48 offen offset:0 lds\n" \
"v_mfma_f32_16x16x32_bf16 v[132:135], a[124:127], a[12:15], v[132:135]\n" \
"v_exp_f32_e32 v159, v159\n" \
"v_mfma_f32_16x16x32_bf16 v[32:35], a[160:163], v[196:199], v[32:35]\n" \
"ds_read_b128 a[64:67], %[kb0] offset:16384\n" \
"v_mfma_f32_16x16x32_bf16 v[32:35], a[128:131], v[188:191], v[32:35]\n" \
"v_exp_f32_e64 v128, v128\n" \
"v_mfma_f32_16x16x32_bf16 v[36:39], a[164:167], v[196:199], v[36:39]\n" \
"v_cvt_pk_bf16_f32 v222, v156, v157\n" \
"v_cvt_pk_bf16_f32 v223, v158, v159\n" \
"v_mfma_f32_16x16x32_bf16 v[36:39], a[132:135], v[188:191], v[36:39]\n" \
"v_exp_f32_e64 v129, v129\n" \
"v_mfma_f32_16x16x32_bf16 v[40:43], a[168:171], v[196:199], v[40:43]\n" \
"ds_read_b128 a[68:71], v208 offset:16384\n" \
"v_mfma_f32_16x16x32_bf16 v[40:43], a[136:139], v[188:191], v[40:43]\n" \
"v_exp_f32_e64 v130, v130\n" \
"v_mfma_f32_16x16x32_bf16 v[44:47], a[172:175], v[196:199], v[44:47]\n" \
"ds_read_b128 a[72:75], v209 offset:16384\n" \
"v_mfma_f32_16x16x32_bf16 v[44:47], a[140:143], v[188:191], v[44:47]\n" \
"v_exp_f32_e64 v131, v131\n" \
"v_smfmac_f32_16x16x64_bf16 v[176:179], a[192:195], v[184:191], v228\n" \
"v_mfma_f32_16x16x32_bf16 v[136:139], a[96:99], a[16:19], v[164:167]\n" \
"v_exp_f32_e64 v132, v132\n" \
"v_mfma_f32_16x16x32_bf16 v[136:139], a[100:103], a[20:23], v[136:139]\n" \
"ds_read_b128 a[76:79], v210 offset:16384\n" \
"v_mfma_f32_16x16x32_bf16 v[136:139], a[104:107], a[24:27], v[136:139]\n" \
"v_exp_f32_e64 v133, v133\n" \
"v_mfma_f32_16x16x32_bf16 v[136:139], a[108:111], a[28:31], v[136:139]\n" \
"v_cvt_pk_bf16_f32 v192, v128, v129\n" \
"v_cvt_pk_bf16_f32 v193, v130, v131\n" \
"v_mfma_f32_16x16x32_bf16 v[140:143], a[112:115], a[16:19], v[164:167]\n" \
"v_exp_f32_e64 v134, v134\n" \
"v_mfma_f32_16x16x32_bf16 v[140:143], a[116:119], a[20:23], v[140:143]\n" \
"ds_read_b128 a[80:83], %[kb0] offset:20480\n" \
"v_mfma_f32_16x16x32_bf16 v[140:143], a[120:123], a[24:27], v[140:143]\n" \
"buffer_load_dwordx4 v215, %[srdv], s48 offen offset:1024 lds\n" \
"v_mfma_f32_16x16x32_bf16 v[140:143], a[124:127], a[28:31], v[140:143]\n" \
"v_exp_f32_e64 v135, v135\n" \
"v_mfma_f32_16x16x32_bf16 v[48:51], a[176:179], v[196:199], v[48:51]\n" \
"ds_read_b128 a[84:87], v208 offset:20480\n" \
"v_mfma_f32_16x16x32_bf16 v[48:51], a[144:147], v[188:191], v[48:51]\n" \
"v_exp_f32_e64 v136, v136\n" \
"v_mfma_f32_16x16x32_bf16 v[52:55], a[180:183], v[196:199], v[52:55]\n" \
"v_cvt_pk_bf16_f32 v194, v132, v133\n" \
"v_cvt_pk_bf16_f32 v195, v134, v135\n" \
"v_mfma_f32_16x16x32_bf16 v[52:55], a[148:151], v[188:191], v[52:55]\n" \
"v_exp_f32_e64 v137, v137\n" \
"v_mfma_f32_16x16x32_bf16 v[56:59], a[184:187], v[196:199], v[56:59]\n" \
"v_exp_f32_e64 v138, v138\n" \
"v_mfma_f32_16x16x32_bf16 v[56:59], a[152:155], v[188:191], v[56:59]\n" \
"ds_read_b128 a[88:91], v209 offset:20480\n" \
"v_mfma_f32_16x16x32_bf16 v[60:63], a[188:191], v[196:199], v[60:63]\n" \
"v_exp_f32_e64 v139, v139\n" \
"v_mfma_f32_16x16x32_bf16 v[60:63], a[156:159], v[188:191], v[60:63]\n" \
"s_cmp_lg_u32 %[wid], 3\n" \
"s_cbranch_scc1 LtB1_%=\n" \
"global_load_dword v237, v236, s[58:59]\n" \
"s_add_u32 s61, s48, 49152\n" \
"s_mov_b32 exec_hi, 0\n" \
"s_nop 0\n" \
"buffer_load_dword v237, v224, %[srdp], s61 offen\n" \
"s_mov_b32 exec_hi, -1\n" \
"s_nop 0\n" \
"LtB1_%=:\n" \
"v_mfma_f32_16x16x32_bf16 v[144:147], a[96:99], a[32:35], v[168:171]\n" \
"v_exp_f32_e64 v140, v140\n" \
"v_mfma_f32_16x16x32_bf16 v[144:147], a[100:103], a[36:39], v[144:147]\n" \
"ds_read_b128 a[92:95], v210 offset:20480\n" \
"v_mfma_f32_16x16x32_bf16 v[144:147], a[104:107], a[40:43], v[144:147]\n" \
"v_exp_f32_e64 v141, v141\n" \
"v_mfma_f32_16x16x32_bf16 v[144:147], a[108:111], a[44:47], v[144:147]\n" \
"v_cvt_pk_bf16_f32 v196, v136, v137\n" \
"v_cvt_pk_bf16_f32 v197, v138, v139\n" \
"v_mfma_f32_16x16x32_bf16 v[148:151], a[112:115], a[32:35], v[168:171]\n" \
"v_exp_f32_e64 v142, v142\n" \
"v_mfma_f32_16x16x32_bf16 v[148:151], a[116:119], a[36:39], v[148:151]\n" \
"v_mfma_f32_16x16x32_bf16 v[148:151], a[120:123], a[40:43], v[148:151]\n" \
"buffer_load_dwordx4 %[vd0], %[srdv], s48 offen offset:2048 lds\n" \
"v_mfma_f32_16x16x32_bf16 v[148:151], a[124:127], a[44:47], v[148:151]\n" \
"v_exp_f32_e64 v143, v143\n" \
"v_mfma_f32_16x16x32_bf16 v[96:99], a[160:163], v[204:207], v[96:99]\n" \
"ds_read_b64_tr_b16 a[160:161], %[vvb] offset:8192\n" \
"v_mfma_f32_16x16x32_bf16 v[96:99], a[128:131], v[220:223], v[96:99]\n" \
"v_exp_f32_e64 v144, v144\n" \
"v_mfma_f32_16x16x32_bf16 v[100:103], a[164:167], v[204:207], v[100:103]\n" \
"v_cvt_pk_bf16_f32 v198, v140, v141\n" \
"v_cvt_pk_bf16_f32 v199, v142, v143\n" \
"v_mfma_f32_16x16x32_bf16 v[100:103], a[132:135], v[220:223], v[100:103]\n" \
"v_exp_f32_e64 v145, v145\n" \
"v_mfma_f32_16x16x32_bf16 v[104:107], a[168:171], v[204:207], v[104:107]\n" \
"ds_read_b64_tr_b16 a[162:163], %[vvb] offset:12288\n" \
"ds_read_b64_tr_b16 a[164:165], v211 offset:8192\n" \
"v_mfma_f32_16x16x32_bf16 v[104:107], a[136:139], v[220:223], v[104:107]\n" \
"v_exp_f32_e64 v146, v146\n" \
"v_mfma_f32_16x16x32_bf16 v[108:111], a[172:175], v[204:207], v[108:111]\n" \
"ds_read_b64_tr_b16 a[166:167], v211 offset:12288\n" \
"v_mfma_f32_16x16x32_bf16 v[108:111], a[140:143], v[220:223], v[108:111]\n" \
"v_exp_f32_e64 v147, v147\n" \
"v_smfmac_f32_16x16x64_bf16 v[180:183], a[192:195], v[216:223], v228\n" \
"ds_read_b64_tr_b16 a[168:169], %[vvb] offset:8704\n" \
"v_mfma_f32_16x16x32_bf16 v[152:155], a[96:99], a[48:51], v[172:175]\n" \
"v_exp_f32_e64 v148, v148\n" \
"v_mfma_f32_16x16x32_bf16 v[152:155], a[100:103], a[52:55], v[152:155]\n" \
"ds_read_b64_tr_b16 a[170:171], %[vvb] offset:12800\n" \
"v_mfma_f32_16x16x32_bf16 v[152:155], a[104:107], a[56:59], v[152:155]\n" \
"v_exp_f32_e64 v149, v149\n" \
"v_mfma_f32_16x16x32_bf16 v[152:155], a[108:111], a[60:63], v[152:155]\n" \
"v_cvt_pk_bf16_f32 v200, v144, v145\n" \
"v_cvt_pk_bf16_f32 v201, v146, v147\n" \
"v_mfma_f32_16x16x32_bf16 v[156:159], a[112:115], a[48:51], v[172:175]\n" \
"v_exp_f32_e64 v150, v150\n" \
"v_mfma_f32_16x16x32_bf16 v[156:159], a[116:119], a[52:55], v[156:159]\n" \
"ds_read_b64_tr_b16 a[172:173], v211 offset:8704\n" \
"v_mfma_f32_16x16x32_bf16 v[156:159], a[120:123], a[56:59], v[156:159]\n" \
"buffer_load_dwordx4 v215, %[srdv], s48 offen offset:3072 lds\n" \
"v_mfma_f32_16x16x32_bf16 v[156:159], a[124:127], a[60:63], v[156:159]\n" \
"v_exp_f32_e64 v151, v151\n" \
"v_mfma_f32_16x16x32_bf16 v[112:115], a[176:179], v[204:207], v[112:115]\n" \
"ds_read_b64_tr_b16 a[174:175], v211 offset:12800\n" \
"ds_read_b64_tr_b16 a[176:177], %[vvb] offset:9216\n" \
"v_mfma_f32_16x16x32_bf16 v[112:115], a[144:147], v[220:223], v[112:115]\n" \
"v_exp_f32_e64 v152, v152\n" \
"v_mfma_f32_16x16x32_bf16 v[116:119], a[180:183], v[204:207], v[116:119]\n" \
"v_cvt_pk_bf16_f32 v202, v148, v149\n" \
"v_cvt_pk_bf16_f32 v203, v150, v151\n" \
"v_mfma_f32_16x16x32_bf16 v[116:119], a[148:151], v[220:223], v[116:119]\n" \
"v_exp_f32_e64 v153, v153\n" \
"v_mfma_f32_16x16x32_bf16 v[120:123], a[184:187], v[204:207], v[120:123]\n" \
"v_exp_f32_e64 v154, v154\n" \
"v_mfma_f32_16x16x32_bf16 v[120:123], a[152:155], v[220:223], v[120:123]\n" \
"ds_read_b64_tr_b16 a[178:179], %[vvb] offset:13312\n" \
"v_mfma_f32_16x16x32_bf16 v[124:127], a[188:191], v[204:207], v[124:127]\n" \
"v_exp_f32_e64 v155, v155\n" \
"v_mfma_f32_16x16x32_bf16 v[124:127], a[156:159], v[220:223], v[124:127]\n" \
"ds_read_b64_tr_b16 a[180:181], v211 offset:9216\n" \
"s_waitcnt lgkmcnt(13)\n" \
"s_cmp_lg_u32 s60, 7\n" \
"s_cbranch_scc1 LmB_%=\n" \
"s_nop 0\n" \
"v_readlane_b32 s48, %[tlo], 11\n" \
"v_readlane_b32 s58, %[tlo], 12\n" \
"v_lshlrev_b32_e64 v227, 2, v224\n" \
"v_and_b32_e32 v227, 0x1800, v227\n" \
"v_bfe_u32 v234, v224, 11, 1\n" \
"v_lshl_or_b32 v227, v234, 15, v227\n" \
"v_lshrrev_b32_e64 v234, 12, v224\n" \
"v_lshl_or_b32 v227, v234, 13, v227\n" \
"v_and_or_b32 v227, v235, 48, v227\n" \
"v_readlane_b32 s51, %[tlo], 31\n" \
"s_lshl_b32 s62, %[wid], 10\n" \
"s_add_u32 s62, s62, 262144\n" \
"s_add_u32 s55, s62, 8192\n" \
"v_readlane_b32 s68, %[tlo], 20\n" \
"v_readlane_b32 s69, %[thi], 20\n" \
"s_lshl_b32 s63, %[wid], 9\n" \
"v_lshrrev_b32_e64 v230, 1, v235\n" \
"buffer_load_dwordx4 a[200:203], v235, %[srdz], s62 offen sc1\n" \
"v_add_u32_e64 v230, s63, v230\n" \
"buffer_load_dwordx4 a[204:207], v235, %[srdz], s55 offen sc1\n" \
"s_nop 1\n" \
"global_load_dwordx2 a[208:209], v230, s[68:69]\n" \
"v_mfma_f32_16x16x32_bf16 v[128:131], a[64:67], a[0:3], v[160:163]\n" \
"v_exp_f32_e32 v156, v156\n" \
"v_mfma_f32_16x16x32_bf16 v[128:131], a[68:71], a[4:7], v[128:131]\n" \
"ds_read_b64_tr_b16 a[182:183], v211 offset:13312\n" \
"s_add_u32 m0, %[m0b], 0\n" \
"s_mov_b32 s49, s48\n" \
"v_mfma_f32_16x16x32_bf16 v[128:131], a[72:75], a[8:11], v[128:131]\n" \
"v_exp_f32_e64 v157, v157\n" \
"v_mfma_f32_16x16x32_bf16 v[128:131], a[76:79], a[12:15], v[128:131]\n" \
"v_cvt_pk_bf16_f32 v204, v152, v153\n" \
"v_cvt_pk_bf16_f32 v205, v154, v155\n" \
"v_mfma_f32_16x16x32_bf16 v[132:135], a[80:83], a[0:3], v[160:163]\n" \
"v_exp_f32_e64 v158, v158\n" \
"v_mfma_f32_16x16x32_bf16 v[132:135], a[84:87], a[4:7], v[132:135]\n" \
"ds_read_b64_tr_b16 a[184:185], %[vvb] offset:9728\n" \
"s_lshl_b32 s49, s49, 18\n" \
"s_or_b32 s49, s49, s58\n" \
"s_waitcnt lgkmcnt(5)\n" \
"v_mfma_f32_16x16x32_bf16 v[132:135], a[88:91], a[8:11], v[132:135]\n" \
"buffer_load_dwordx4 %[wo0], %[srdt], s49 offen offset:0 lds\n" \
"v_mfma_f32_16x16x32_bf16 v[132:135], a[92:95], a[12:15], v[132:135]\n" \
"v_exp_f32_e32 v159, v159\n" \
"v_mfma_f32_16x16x32_bf16 v[64:67], a[128:131], v[216:219], v[64:67]\n" \
"ds_read_b64_tr_b16 a[186:187], %[vvb] offset:13824\n" \
"ds_read_b64_tr_b16 a[188:189], v211 offset:9728\n" \
"v_readlane_b32 s60, %[thi], 11\n" \
"buffer_load_dwordx4 a[212:215], v227, %[srdt], s51 offen offset:0\n" \
"v_mfma_f32_16x16x32_bf16 v[64:67], a[160:163], v[200:203], v[64:67]\n" \
"v_exp_f32_e64 v128, v128\n" \
"v_mfma_f32_16x16x32_bf16 v[68:71], a[132:135], v[216:219], v[68:71]\n" \
"v_cvt_pk_bf16_f32 v206, v156, v157\n" \
"v_cvt_pk_bf16_f32 v207, v158, v159\n" \
"v_mfma_f32_16x16x32_bf16 v[68:71], a[164:167], v[200:203], v[68:71]\n" \
"v_exp_f32_e64 v129, v129\n" \
"v_mfma_f32_16x16x32_bf16 v[72:75], a[136:139], v[216:219], v[72:75]\n" \
"ds_read_b64_tr_b16 a[190:191], v211 offset:13824\n" \
"ds_read_b64_tr_b16 a[128:129], %[vvb] offset:16384\n" \
"v_readlane_b32 s59, %[thi], 12\n" \
"v_mfma_f32_16x16x32_bf16 v[72:75], a[168:171], v[200:203], v[72:75]\n" \
"v_exp_f32_e64 v130, v130\n" \
"v_mfma_f32_16x16x32_bf16 v[76:79], a[140:143], v[216:219], v[76:79]\n" \
"ds_read_b64_tr_b16 a[130:131], %[vvb] offset:20480\n" \
"ds_read_b64_tr_b16 a[132:133], v211 offset:16384\n" \
"v_readlane_b32 s53, %[tlo], 13\n" \
"buffer_load_dwordx4 a[216:219], v227, %[srdt], s51 offen offset:64\n" \
"v_mfma_f32_16x16x32_bf16 v[76:79], a[172:175], v[200:203], v[76:79]\n" \
"v_exp_f32_e64 v131, v131\n" \
"v_smfmac_f32_16x16x64_bf16 v[176:179], a[192:195], v[192:199], v228\n" \
"ds_read_b64_tr_b16 a[134:135], v211 offset:20480\n" \
"ds_read_b64_tr_b16 a[136:137], %[vvb] offset:16896\n" \
"v_readlane_b32 s50, %[thi], 13\n" \
"v_mfma_f32_16x16x32_bf16 v[136:139], a[64:67], a[16:19], v[164:167]\n" \
"v_exp_f32_e64 v132, v132\n" \
"v_mfma_f32_16x16x32_bf16 v[136:139], a[68:71], a[20:23], v[136:139]\n" \
"ds_read_b64_tr_b16 a[138:139], %[vvb] offset:20992\n" \
"ds_read_b64_tr_b16 a[140:141], v211 offset:16896\n" \
"v_readlane_b32 s46, %[tlo], 14\n" \
"v_add_u32_e32 v212, 0x3c00, %[wo0]\n" \
"v_mfma_f32_16x16x32_bf16 v[136:139], a[72:75], a[24:27], v[136:139]\n" \
"v_exp_f32_e64 v133, v133\n" \
"v_mfma_f32_16x16x32_bf16 v[136:139], a[76:79], a[28:31], v[136:139]\n" \
"v_cvt_pk_bf16_f32 v184, v128, v129\n" \
"v_cvt_pk_bf16_f32 v185, v130, v131\n" \
"v_mfma_f32_16x16x32_bf16 v[140:143], a[80:83], a[16:19], v[164:167]\n" \
"v_exp_f32_e64 v134, v134\n" \
"v_mfma_f32_16x16x32_bf16 v[140:143], a[84:87], a[20:23], v[140:143]\n" \
"ds_read_b64_tr_b16 a[142:143], v211 offset:20992\n" \
"v_readlane_b32 s47, %[thi], 14\n" \
"v_add_u32_e32 v213, 0x7800, %[wo0]\n" \
"buffer_load_dwordx4 a[220:223], v227, %[srdt], s51 offen offset:128\n" \
"s_waitcnt lgkmcnt(8)\n" \
"v_mfma_f32_16x16x32_bf16 v[140:143], a[88:91], a[24:27], v[140:143]\n" \
"buffer_load_dwordx4 v212, %[srdt], s49 offen offset:1024 lds\n" \
"v_mfma_f32_16x16x32_bf16 v[140:143], a[92:95], a[28:31], v[140:143]\n" \
"v_exp_f32_e32 v135, v135\n" \
"v_mfma_f32_16x16x32_bf16 v[80:83], a[144:147], v[216:219], v[80:83]\n" \
"ds_read_b64_tr_b16 a[144:145], %[vvb] offset:17408\n" \
"ds_read_b64_tr_b16 a[146:147], %[vvb] offset:21504\n" \
"v_mfma_f32_16x16x32_bf16 v[80:83], a[176:179], v[200:203], v[80:83]\n" \
"v_exp_f32_e64 v136, v136\n" \
"v_mfma_f32_16x16x32_bf16 v[84:87], a[148:151], v[216:219], v[84:87]\n" \
"v_cvt_pk_bf16_f32 v186, v132, v133\n" \
"v_cvt_pk_bf16_f32 v187, v134, v135\n" \
"v_mfma_f32_16x16x32_bf16 v[84:87], a[180:183], v[200:203], v[84:87]\n" \
"v_exp_f32_e64 v137, v137\n" \
"v_mfma_f32_16x16x32_bf16 v[88:91], a[152:155], v[216:219], v[88:91]\n" \
"v_exp_f32_e64 v138, v138\n" \
"v_mfma_f32_16x16x32_bf16 v[88:91], a[184:187], v[200:203], v[88:91]\n" \
"ds_read_b64_tr_b16 a[148:149], v211 offset:17408\n" \
"buffer_load_dwordx4 a[224:227], v227, %[srdt], s51 offen offset:192\n" \
"ds_read_b64_tr_b16 a[150:151], v211 offset:21504\n" \
"v_mfma_f32_16x16x32_bf16 v[92:95], a[156:159], v[216:219], v[92:95]\n" \
"v_exp_f32_e64 v139, v139\n" \
"v_mfma_f32_16x16x32_bf16 v[92:95], a[188:191], v[200:203], v[92:95]\n" \
"ds_read_b64_tr_b16 a[152:153], %[vvb] offset:17920\n" \
"v_mfma_f32_16x16x32_bf16 v[144:147], a[64:67], a[32:35], v[168:171]\n" \
"v_exp_f32_e64 v140, v140\n" \
"v_mfma_f32_16x16x32_bf16 v[144:147], a[68:71], a[36:39], v[144:147]\n" \
"ds_read_b64_tr_b16 a[154:155], %[vvb] offset:22016\n" \
"ds_read_b64_tr_b16 a[156:157], v211 offset:17920\n" \
"buffer_load_dwordx4 a[228:231], v227, %[srdt], s51 offen offset:256\n" \
"v_mfma_f32_16x16x32_bf16 v[144:147], a[72:75], a[40:43], v[144:147]\n" \
"v_exp_f32_e64 v141, v141\n" \
"v_mfma_f32_16x16x32_bf16 v[144:147], a[76:79], a[44:47], v[144:147]\n" \
"v_cvt_pk_bf16_f32 v188, v136, v137\n" \
"v_cvt_pk_bf16_f32 v189, v138, v139\n" \
"v_mfma_f32_16x16x32_bf16 v[148:151], a[80:83], a[32:35], v[168:171]\n" \
"v_exp_f32_e64 v142, v142\n" \
"v_mfma_f32_16x16x32_bf16 v[148:151], a[84:87], a[36:39], v[148:151]\n" \
"ds_read_b64_tr_b16 a[158:159], v211 offset:22016\n" \
"s_waitcnt lgkmcnt(8)\n" \
"v_mfma_f32_16x16x32_bf16 v[148:151], a[88:91], a[40:43], v[148:151]\n" \
"buffer_load_dwordx4 v213, %[srdt], s49 offen offset:2048 lds\n" \
"v_mfma_f32_16x16x32_bf16 v[148:151], a[92:95], a[44:47], v[148:151]\n" \
"v_exp_f32_e32 v143, v143\n" \
"v_mfma_f32_16x16x32_bf16 v[0:3], a[160:163], v[192:195], v[0:3]\n" \
"ds_read_b128 a[96:99], %[kb0] offset:24576\n" \
"buffer_load_dwordx4 a[232:235], v227, %[srdt], s51 offen offset:320\n" \
"s_waitcnt vmcnt(9)\n" \
"v_accvgpr_read_b32 v230, a208\n" \
"v_accvgpr_read_b32 v234, a204\n" \
"v_mfma_f32_16x16x32_bf16 v[0:3], a[128:131], v[184:187], v[0:3]\n" \
"v_exp_f32_e32 v144, v144\n" \
"v_accvgpr_read_b32 v225, a205\n" \
"v_accvgpr_read_b32 v226, a200\n" \
"v_mfma_f32_16x16x32_bf16 v[4:7], a[164:167], v[192:195], v[4:7]\n" \
"v_cvt_pk_bf16_f32 v190, v140, v141\n" \
"v_cvt_pk_bf16_f32 v191, v142, v143\n" \
"v_accvgpr_read_b32 v231, a201\n" \
"v_bitop3_b32 v229, v234, v225, -1 bitop3:0x80\n" \
"v_mfma_f32_16x16x32_bf16 v[4:7], a[132:135], v[184:187], v[4:7]\n" \
"v_exp_f32_e64 v145, v145\n" \
"v_bitop3_b32 v229, v226, v231, v229 bitop3:0x80\n" \
"v_lshlrev_b32_e64 v236, 16, v230\n" \
"v_mfma_f32_16x16x32_bf16 v[8:11], a[168:171], v[192:195], v[8:11]\n" \
"ds_read_b128 a[100:103], v208 offset:24576\n" \
"v_and_b32_e32 v230, 0xffff0000, v230\n" \
"v_mul_f32_e64 v234, v234, v236\n" \
"v_mfma_f32_16x16x32_bf16 v[8:11], a[136:139], v[184:187], v[8:11]\n" \
"v_exp_f32_e32 v146, v146\n" \
"v_mul_f32_e32 v225, v225, v230\n" \
"v_mul_f32_e64 v226, v226, v234\n" \
"v_mfma_f32_16x16x32_bf16 v[12:15], a[172:175], v[192:195], v[12:15]\n" \
"ds_read_b128 a[104:107], v209 offset:24576\n" \
"v_add_u32_e32 v214, 0xb400, %[wo0]\n" \
"buffer_load_dwordx4 a[236:239], v227, %[srdt], s51 offen offset:384\n" \
"v_mul_f32_e64 v231, v231, v225\n" \
"v_cvt_pk_bf16_f32 v236, v234, v225\n" \
"v_mfma_f32_16x16x32_bf16 v[12:15], a[140:143], v[184:187], v[12:15]\n" \
"v_exp_f32_e64 v147, v147\n" \
"v_cvt_pk_bf16_f32 v232, v226, v231\n" \
"v_smfmac_f32_16x16x64_bf16 v[180:183], a[192:195], v[200:207], v228\n" \
"ds_read_b128 a[108:111], v210 offset:24576\n" \
"v_accvgpr_read_b32 v230, a209\n" \
"v_accvgpr_read_b32 v234, a206\n" \
"v_mfma_f32_16x16x32_bf16 v[152:155], a[64:67], a[48:51], v[172:175]\n" \
"v_exp_f32_e64 v148, v148\n" \
"v_accvgpr_read_b32 v225, a207\n" \
"v_accvgpr_read_b32 v226, a202\n" \
"v_mfma_f32_16x16x32_bf16 v[152:155], a[68:71], a[52:55], v[152:155]\n" \
"ds_read_b128 a[112:115], %[kb0] offset:28672\n" \
"v_accvgpr_read_b32 v231, a203\n" \
"v_bitop3_b32 v229, v234, v225, v229 bitop3:0x80\n" \
"v_mfma_f32_16x16x32_bf16 v[152:155], a[72:75], a[56:59], v[152:155]\n" \
"v_exp_f32_e64 v149, v149\n" \
"v_bitop3_b32 v229, v226, v231, v229 bitop3:0x80\n" \
"v_lshlrev_b32_e64 v237, 16, v230\n" \
"v_mfma_f32_16x16x32_bf16 v[152:155], a[76:79], a[60:63], v[152:155]\n" \
"v_cvt_pk_bf16_f32 v216, v144, v145\n" \
"v_cvt_pk_bf16_f32 v217, v146, v147\n" \
"v_and_b32_e32 v230, 0xffff0000, v230\n" \
"v_mul_f32_e64 v234, v234, v237\n" \
"v_mfma_f32_16x16x32_bf16 v[156:159], a[80:83], a[48:51], v[172:175]\n" \
"v_exp_f32_e32 v150, v150\n" \
"v_mul_f32_e32 v225, v225, v230\n" \
"v_mul_f32_e64 v226, v226, v234\n" \
".if . - adaln_body != 32768\n" \
".error \"size model\"\n" \
".endif\n" \
"Lk7_%=:\n" \
"s_cbranch_execz Lk8_%=\n" \
"s_nop 0\n" \
"v_mfma_f32_16x16x32_bf16 v[156:159], a[84:87], a[52:55], v[156:159]\n" \
"ds_read_b128 a[116:119], v208 offset:28672\n" \
"buffer_load_dwordx4 a[196:199], v227, %[srdt], s51 offen offset:448\n" \
"v_mul_f32_e64 v231, v231, v225\n" \
"v_cvt_pk_bf16_f32 v237, v234, v225\n" \
"s_waitcnt lgkmcnt(6)\n" \
"v_mfma_f32_16x16x32_bf16 v[156:159], a[88:91], a[56:59], v[156:159]\n" \
"buffer_load_dwordx4 v214, %[srdt], s49 offen offset:3072 lds\n" \
"v_cvt_pk_bf16_f32 v233, v226, v231\n" \
"v_mfma_f32_16x16x32_bf16 v[156:159], a[92:95], a[60:63], v[156:159]\n" \
"v_exp_f32_e32 v151, v151\n" \
"v_lshrrev_b32_e64 v230, 1, v235\n" \
"s_add_u32 s62, %[m0t], 6144\n" \
"v_mfma_f32_16x16x32_bf16 v[16:19], a[176:179], v[192:195], v[16:19]\n" \
"ds_read_b128 a[120:123], v209 offset:28672\n" \
"v_add_u32_e32 v230, s62, v230\n" \
"v_and_b32_e32 v229, 1, v229\n" \
"v_mfma_f32_16x16x32_bf16 v[16:19], a[144:147], v[184:187], v[16:19]\n" \
"v_exp_f32_e64 v152, v152\n" \
"v_cmp_eq_u32_e64 s[68:69], 0, v229\n" \
"ds_write_b64 v230, v[232:233] offset:0\n" \
"v_mfma_f32_16x16x32_bf16 v[20:23], a[180:183], v[192:195], v[20:23]\n" \
"v_cvt_pk_bf16_f32 v218, v148, v149\n" \
"v_cvt_pk_bf16_f32 v219, v150, v151\n" \
"v_and_b32_e32 v230, 0x170, v235\n" \
"v_lshrrev_b32_e64 v234, 7, v224\n" \
"v_mfma_f32_16x16x32_bf16 v[20:23], a[148:151], v[184:187], v[20:23]\n" \
"v_exp_f32_e64 v153, v153\n" \
"v_and_or_b32 v230, v234, 8, v230\n" \
"v_lshrrev_b32_e64 v234, 12, v224\n" \
"v_mfma_f32_16x16x32_bf16 v[24:27], a[184:187], v[192:195], v[24:27]\n" \
"v_exp_f32_e64 v154, v154\n" \
"v_lshl_or_b32 v230, v234, 7, v230\n" \
"v_add_u32_e64 v230, s62, v230\n" \
"v_mfma_f32_16x16x32_bf16 v[24:27], a[152:155], v[184:187], v[24:27]\n" \
"ds_read_b128 a[124:127], v210 offset:28672\n" \
"ds_write_b64 v230, v[236:237] offset:512\n" \
"v_mfma_f32_16x16x32_bf16 v[28:31], a[188:191], v[192:195], v[28:31]\n" \
"v_exp_f32_e32 v155, v155\n" \
"s_cmp_lg_u64 s[68:69], 0\n" \
"s_cbranch_scc1 Lus_%=\n" \
"Lub_%=:\n" \
"v_lshrrev_b32_e32 v234, 1, v235\n" \
"v_mfma_f32_16x16x32_bf16 v[28:31], a[156:159], v[184:187], v[28:31]\n" \
"s_waitcnt vmcnt(15) lgkmcnt(4)\n" \
"s_barrier\n" \
"v_mfma_f32_16x16x32_bf16 v[128:131], a[96:99], a[0:3], v[160:163]\n" \
"v_exp_f32_e64 v156, v156\n" \
"v_readlane_b32 s64, %[tlo], 8\n" \
"v_readlane_b32 s65, %[thi], 8\n" \
"v_add_u32_e32 v234, s62, v234\n" \
"v_mov_b64_e32 v[232:233], 0\n" \
"v_mfma_f32_16x16x32_bf16 v[128:131], a[100:103], a[4:7], v[128:131]\n" \
"v_mbcnt_lo_u32_b32 v231, -1, 0\n" \
"ds_write_b64 v234, v[232:233] offset:1024\n" \
"s_mov_b32 s68, 0xf0000f\n" \
"v_mfma_f32_16x16x32_bf16 v[128:131], a[104:107], a[8:11], v[128:131]\n" \
"v_exp_f32_e64 v157, v157\n" \
"s_mov_b32 s69, 0xf0000f00\n" \
"v_mov_b32_e32 v234, 0x400\n" \
"v_mfma_f32_16x16x32_bf16 v[128:131], a[108:111], a[12:15], v[128:131]\n" \
"v_cvt_pk_bf16_f32 v220, v152, v153\n" \
"v_cvt_pk_bf16_f32 v221, v154, v155\n" \
"v_cndmask_b32_e64 v234, v234, 0, s[68:69]\n" \
"v_and_or_b32 v230, v235, 48, v234\n" \
"v_mfma_f32_16x16x32_bf16 v[132:135], a[112:115], a[0:3], v[160:163]\n" \
"v_exp_f32_e32 v158, v158\n" \
"v_add_u32_e32 v230, s62, v230\n" \
"v_mfma_f32_16x16x32_bf16 v[132:135], a[116:119], a[4:7], v[132:135]\n" \
"v_mbcnt_hi_u32_b32 v231, -1, v231\n" \
"s_waitcnt lgkmcnt(2)\n" \
"v_mfma_f32_16x16x32_bf16 v[132:135], a[120:123], a[8:11], v[132:135]\n" \
"s_add_u32 m0, %[m0b], 65536\n" \
"s_mov_b32 s61, 0\n" \
"buffer_load_dwordx4 %[vg0], s[64:67], s61 offen offset:0 lds\n" \
"v_mfma_f32_16x16x32_bf16 v[132:135], a[124:127], a[12:15], v[132:135]\n" \
"v_exp_f32_e64 v159, v159\n" \
"v_mfma_f32_16x16x32_bf16 v[32:35], a[160:163], v[196:199], v[32:35]\n" \
"ds_read_b128 a[64:67], %[kb0] offset:32768\n" \
"v_and_b32_e64 v226, 31, v231\n" \
"v_mfma_f32_16x16x32_bf16 v[32:35], a[128:131], v[188:191], v[32:35]\n" \
"v_exp_f32_e64 v128, v128\n" \
"v_mfma_f32_16x16x32_bf16 v[36:39], a[164:167], v[196:199], v[36:39]\n" \
"v_cvt_pk_bf16_f32 v222, v156, v157\n" \
"v_cvt_pk_bf16_f32 v223, v158, v159\n" \
"v_mfma_f32_16x16x32_bf16 v[36:39], a[132:135], v[188:191], v[36:39]\n" \
"v_exp_f32_e64 v129, v129\n" \
"v_mfma_f32_16x16x32_bf16 v[40:43], a[168:171], v[196:199], v[40:43]\n" \
"ds_read_b128 a[68:71], v208 offset:32768\n" \
"v_lshlrev_b32_e64 v226, 2, v226\n" \
"v_mfma_f32_16x16x32_bf16 v[40:43], a[136:139], v[188:191], v[40:43]\n" \
"v_exp_f32_e64 v130, v130\n" \
"v_mfma_f32_16x16x32_bf16 v[44:47], a[172:175], v[196:199], v[44:47]\n" \
"ds_read_b128 a[72:75], v209 offset:32768\n" \
"v_cmp_gt_u32_e32 vcc, 1, v231\n" \
"v_mfma_f32_16x16x32_bf16 v[44:47], a[140:143], v[188:191], v[44:47]\n" \
"v_exp_f32_e32 v131, v131\n" \
"ds_read_b128 a[200:203], v230 offset:0\n" \
"v_smfmac_f32_16x16x64_bf16 v[176:179], a[192:195], v[184:191], v228\n" \
"v_mov_b32_e32 v225, 0x80000000\n" \
"v_mfma_f32_16x16x32_bf16 v[136:139], a[96:99], a[16:19], v[164:167]\n" \
"v_exp_f32_e64 v132, v132\n" \
"v_mfma_f32_16x16x32_bf16 v[136:139], a[100:103], a[20:23], v[136:139]\n" \
"ds_read_b128 a[76:79], v210 offset:32768\n" \
"v_mfma_f32_16x16x32_bf16 v[136:139], a[104:107], a[24:27], v[136:139]\n" \
"v_exp_f32_e64 v133, v133\n" \
"v_mfma_f32_16x16x32_bf16 v[136:139], a[108:111], a[28:31], v[136:139]\n" \
"v_cvt_pk_bf16_f32 v192, v128, v129\n" \
"v_cvt_pk_bf16_f32 v193, v130, v131\n" \
"ds_read_b128 a[204:207], v230 offset:64\n" \
"v_mfma_f32_16x16x32_bf16 v[140:143], a[112:115], a[16:19], v[164:167]\n" \
"v_exp_f32_e64 v134, v134\n" \
"v_mfma_f32_16x16x32_bf16 v[140:143], a[116:119], a[20:23], v[140:143]\n" \
"ds_read_b128 a[80:83], %[kb0] offset:36864\n" \
"v_cndmask_b32_e32 v225, v225, v224, vcc\n" \
"v_mfma_f32_16x16x32_bf16 v[140:143], a[120:123], a[24:27], v[140:143]\n" \
"s_add_u32 m0, %[m0b], 66560\n" \
"s_mov_b32 s61, 229376\n" \
"buffer_load_dwordx4 %[vg0], s[64:67], s61 offen offset:0 lds\n" \
"v_mfma_f32_16x16x32_bf16 v[140:143], a[124:127], a[28:31], v[140:143]\n" \
"v_exp_f32_e32 v135, v135\n" \
"s_waitcnt vmcnt(12) lgkmcnt(3)\n" \
"v_mfma_f32_16x16x32_bf16 a[208:211], a[212:215], a[200:203], 0\n" \
"ds_read_b128 a[200:203], v230 offset:128\n" \
"v_mfma_f32_16x16x32_bf16 v[48:51], a[176:179], v[196:199], v[48:51]\n" \
"ds_read_b128 a[84:87], v208 offset:36864\n" \
"v_mfma_f32_16x16x32_bf16 v[48:51], a[144:147], v[188:191], v[48:51]\n" \
"v_exp_f32_e32 v136, v136\n" \
"v_mfma_f32_16x16x32_bf16 v[52:55], a[180:183], v[196:199], v[52:55]\n" \
"v_cvt_pk_bf16_f32 v194, v132, v133\n" \
"v_cvt_pk_bf16_f32 v195, v134, v135\n" \
"v_mfma_f32_16x16x32_bf16 v[52:55], a[148:151], v[188:191], v[52:55]\n" \
"v_exp_f32_e64 v137, v137\n" \
"v_mfma_f32_16x16x32_bf16 v[56:59], a[184:187], v[196:199], v[56:59]\n" \
"v_exp_f32_e64 v138, v138\n" \
"v_mfma_f32_16x16x32_bf16 v[56:59], a[152:155], v[188:191], v[56:59]\n" \
"ds_read_b128 a[88:91], v209 offset:36864\n" \
"s_waitcnt vmcnt(11) lgkmcnt(4)\n" \
"v_mfma_f32_16x16x32_bf16 a[208:211], a[216:219], a[204:207], a[208:211]\n" \
"ds_read_b128 a[204:207], v230 offset:192\n" \
"v_mfma_f32_16x16x32_bf16 v[60:63], a[188:191], v[196:199], v[60:63]\n" \
"v_exp_f32_e32 v139, v139\n" \
"v_mfma_f32_16x16x32_bf16 v[60:63], a[156:159], v[188:191], v[60:63]\n" \
"s_waitcnt vmcnt(10) lgkmcnt(3)\n" \
"v_mfma_f32_16x16x32_bf16 a[208:211], a[220:223], a[200:203], a[208:211]\n" \
"ds_read_b128 a[200:203], v230 offset:256\n" \
"v_mfma_f32_16x16x32_bf16 v[144:147], a[96:99], a[32:35], v[168:171]\n" \
"v_exp_f32_e32 v140, v140\n" \
"v_mfma_f32_16x16x32_bf16 v[144:147], a[100:103], a[36:39], v[144:147]\n" \
"ds_read_b128 a[92:95], v210 offset:36864\n" \
"s_sub_u32 s61, s48, 0\n" \
"s_cmp_lt_u32 s61, 4\n" \
"s_cselect_b64 vcc, -1, 0\n" \
"v_mfma_f32_16x16x32_bf16 v[144:147], a[104:107], a[40:43], v[144:147]\n" \
"v_exp_f32_e32 v141, v141\n" \
"v_mfma_f32_16x16x32_bf16 v[144:147], a[108:111], a[44:47], v[144:147]\n" \
"v_cvt_pk_bf16_f32 v196, v136, v137\n" \
"v_cvt_pk_bf16_f32 v197, v138, v139\n" \
"v_mfma_f32_16x16x32_bf16 v[148:151], a[112:115], a[32:35], v[168:171]\n" \
"v_exp_f32_e64 v142, v142\n" \
"v_mfma_f32_16x16x32_bf16 v[148:151], a[116:119], a[36:39], v[148:151]\n" \
"v_readlane_b32 s55, %[tlo], 16\n" \
"s_waitcnt vmcnt(8) lgkmcnt(2)\n" \
"v_mfma_f32_16x16x32_bf16 a[208:211], a[224:227], a[204:207], a[208:211]\n" \
"ds_read_b128 a[204:207], v230 offset:320\n" \
"v_mfma_f32_16x16x32_bf16 v[148:151], a[120:123], a[40:43], v[148:151]\n" \
"s_add_u32 m0, %[m0b], 67584\n" \
"s_mov_b32 s61, 458752\n" \
"buffer_load_dwordx4 %[vg0], s[64:67], s61 offen offset:0 lds\n" \
"v_mfma_f32_16x16x32_bf16 v[148:151], a[124:127], a[44:47], v[148:151]\n" \
"v_exp_f32_e32 v143, v143\n" \
"v_mfma_f32_16x16x32_bf16 v[96:99], a[160:163], v[204:207], v[96:99]\n" \
"ds_read_b64_tr_b16 a[160:161], %[vvb] offset:24576\n" \
"v_cmp_gt_u32_e64 s[56:57], 32, v231\n" \
"v_mfma_f32_16x16x32_bf16 v[96:99], a[128:131], v[220:223], v[96:99]\n" \
"v_exp_f32_e64 v144, v144\n" \
"v_mfma_f32_16x16x32_bf16 v[100:103], a[164:167], v[204:207], v[100:103]\n" \
"v_cvt_pk_bf16_f32 v198, v140, v141\n" \
"v_cvt_pk_bf16_f32 v199, v142, v143\n" \
"s_waitcnt vmcnt(8) lgkmcnt(3)\n" \
"v_mfma_f32_16x16x32_bf16 a[208:211], a[228:231], a[200:203], a[208:211]\n" \
"ds_read_b128 a[200:203], v230 offset:384\n" \
"v_mfma_f32_16x16x32_bf16 v[100:103], a[132:135], v[220:223], v[100:103]\n" \
"v_exp_f32_e32 v145, v145\n" \
"v_mfma_f32_16x16x32_bf16 v[104:107], a[168:171], v[204:207], v[104:107]\n" \
"ds_read_b64_tr_b16 a[162:163], %[vvb] offset:28672\n" \
"ds_read_b64_tr_b16 a[164:165], v211 offset:24576\n" \
"v_lshlrev_b32_e64 v232, 2, v224\n" \
"v_mfma_f32_16x16x32_bf16 v[104:107], a[136:139], v[220:223], v[104:107]\n" \
"v_exp_f32_e64 v146, v146\n" \
"v_mfma_f32_16x16x32_bf16 v[108:111], a[172:175], v[204:207], v[108:111]\n" \
"ds_read_b64_tr_b16 a[166:167], v211 offset:28672\n" \
"v_mov_b32_e32 v233, 0x80000000\n" \
"v_mfma_f32_16x16x32_bf16 v[108:111], a[140:143], v[220:223], v[108:111]\n" \
"v_exp_f32_e64 v147, v147\n" \
"v_smfmac_f32_16x16x64_bf16 v[180:183], a[192:195], v[216:223], v228\n" \
"ds_read_b64_tr_b16 a[168:169], %[vvb] offset:25088\n" \
"s_waitcnt vmcnt(6) lgkmcnt(6)\n" \
"v_mfma_f32_16x16x32_bf16 a[208:211], a[232:235], a[204:207], a[208:211]\n" \
"ds_read_b128 a[204:207], v230 offset:448\n" \
"v_mfma_f32_16x16x32_bf16 v[152:155], a[96:99], a[48:51], v[172:175]\n" \
"v_exp_f32_e32 v148, v148\n" \
"v_mfma_f32_16x16x32_bf16 v[152:155], a[100:103], a[52:55], v[152:155]\n" \
"ds_read_b64_tr_b16 a[170:171], %[vvb] offset:29184\n" \
"v_cndmask_b32_e64 v232, v233, v232, s[56:57]\n" \
"v_mfma_f32_16x16x32_bf16 v[152:155], a[104:107], a[56:59], v[152:155]\n" \
"v_exp_f32_e64 v149, v149\n" \
"v_mfma_f32_16x16x32_bf16 v[152:155], a[108:111], a[60:63], v[152:155]\n" \
"v_cvt_pk_bf16_f32 v200, v144, v145\n" \
"v_cvt_pk_bf16_f32 v201, v146, v147\n" \
"v_mfma_f32_16x16x32_bf16 v[156:159], a[112:115], a[48:51], v[172:175]\n" \
"v_exp_f32_e64 v150, v150\n" \
"v_mfma_f32_16x16x32_bf16 v[156:159], a[116:119], a[52:55], v[156:159]\n" \
"ds_read_b64_tr_b16 a[172:173], v211 offset:25088\n" \
"v_mov_b32_e32 v230, 1\n" \
"s_waitcnt vmcnt(5) lgkmcnt(7)\n" \
"v_mfma_f32_16x16x32_bf16 a[208:211], a[236:239], a[200:203], a[208:211]\n" \
"v_mfma_f32_16x16x32_bf16 v[156:159], a[120:123], a[56:59], v[156:159]\n" \
"s_add_u32 m0, %[m0b], 68608\n" \
"s_mov_b32 s61, 688128\n" \
"buffer_load_dwordx4 %[vg0], s[64:67], s61 offen offset:0 lds\n" \
"v_mfma_f32_16x16x32_bf16 v[156:159], a[124:127], a[60:63], v[156:159]\n" \
"v_exp_f32_e64 v151, v151\n" \
"v_mfma_f32_16x16x32_bf16 v[112:115], a[176:179], v[204:207], v[112:115]\n" \
"ds_read_b64_tr_b16 a[174:175], v211 offset:29184\n" \
"ds_read_b64_tr_b16 a[176:177], %[vvb] offset:25600\n" \
"v_mfma_f32_16x16x32_bf16 v[112:115], a[144:147], v[220:223], v[112:115]\n" \
"v_exp_f32_e64 v152, v152\n" \
"v_mfma_f32_16x16x32_bf16 v[116:119], a[180:183], v[204:207], v[116:119]\n" \
"v_cvt_pk_bf16_f32 v202, v148, v149\n" \
"v_cvt_pk_bf16_f32 v203, v150, v151\n" \
"s_waitcnt vmcnt(5) lgkmcnt(4)\n" \
"v_mfma_f32_16x16x32_bf16 a[208:211], a[196:199], a[204:207], a[208:211]\n" \
"v_mfma_f32_16x16x32_bf16 v[116:119], a[148:151], v[220:223], v[116:119]\n" \
"v_exp_f32_e32 v153, v153\n" \
"v_mfma_f32_16x16x32_bf16 v[120:123], a[184:187], v[204:207], v[120:123]\n" \
"v_exp_f32_e64 v154, v154\n" \
"v_mfma_f32_16x16x32_bf16 v[120:123], a[152:155], v[220:223], v[120:123]\n" \
"ds_read_b64_tr_b16 a[178:179], %[vvb] offset:29696\n" \
"buffer_store_dword v230, v232, %[srdz], s55 offen sc1\n" \
"v_mfma_f32_16x16x32_bf16 v[124:127], a[188:191], v[204:207], v[124:127]\n" \
"v_exp_f32_e64 v155, v155\n" \
"v_mfma_f32_16x16x32_bf16 v[124:127], a[156:159], v[220:223], v[124:127]\n" \
"ds_read_b64_tr_b16 a[180:181], v211 offset:25600\n" \
"s_waitcnt lgkmcnt(14)\n" \
"v_mfma_f32_16x16x32_bf16 v[128:131], a[64:67], a[0:3], v[160:163]\n" \
"v_exp_f32_e32 v156, v156\n" \
"v_mfma_f32_16x16x32_bf16 v[128:131], a[68:71], a[4:7], v[128:131]\n" \
"ds_read_b64_tr_b16 a[182:183], v211 offset:29696\n" \
"s_add_u32 m0, %[m0b], 16384\n" \
"s_mov_b32 s49, s48\n" \
"s_mov_b32 s68, 0xaaaaaaaa\n" \
"s_mov_b32 s69, 0xaaaaaaaa\n" \
"v_mfma_f32_16x16x32_bf16 v[128:131], a[72:75], a[8:11], v[128:131]\n" \
"v_exp_f32_e32 v157, v157\n" \
"v_mfma_f32_16x16x32_bf16 v[128:131], a[76:79], a[12:15], v[128:131]\n" \
"v_cvt_pk_bf16_f32 v204, v152, v153\n" \
"v_cvt_pk_bf16_f32 v205, v154, v155\n" \
"v_accvgpr_read_b32 v232, a208\n" \
"v_accvgpr_read_b32 v233, a209\n" \
"v_mfma_f32_16x16x32_bf16 v[132:135], a[80:83], a[0:3], v[160:163]\n" \
"v_exp_f32_e64 v158, v158\n" \
"v_mfma_f32_16x16x32_bf16 v[132:135], a[84:87], a[4:7], v[132:135]\n" \
"ds_read_b64_tr_b16 a[184:185], %[vvb] offset:26112\n" \
"s_lshl_b32 s49, s49, 18\n" \
"s_or_b32 s49, s49, s59\n" \
"v_accvgpr_read_b32 v234, a210\n" \
"v_accvgpr_read_b32 v236, a211\n" \
"s_waitcnt lgkmcnt(5)\n" \
"v_mfma_f32_16x16x32_bf16 v[132:135], a[88:91], a[8:11], v[132:135]\n" \
"buffer_load_dwordx4 %[wo0], %[srdt], s49 offen offset:0 lds\n" \
"v_mfma_f32_16x16x32_bf16 v[132:135], a[92:95], a[12:15], v[132:135]\n" \
"v_exp_f32_e32 v159, v159\n" \
"v_mfma_f32_16x16x32_bf16 v[64:67], a[128:131], v[216:219], v[64:67]\n" \
"ds_read_b64_tr_b16 a[186:187], %[vvb] offset:30208\n" \
"ds_read_b64_tr_b16 a[188:189], v211 offset:26112\n" \
"v_xor_b32_e32 v229, 0x80, %[vx0]\n" \
"v_cndmask_b32_e64 v232, v232, v233, s[68:69]\n" \
"v_cndmask_b32_e64 v234, v234, v236, s[68:69]\n" \
"v_mfma_f32_16x16x32_bf16 v[64:67], a[160:163], v[200:203], v[64:67]\n" \
"v_exp_f32_e64 v128, v128\n" \
"v_mfma_f32_16x16x32_bf16 v[68:71], a[132:135], v[216:219], v[68:71]\n" \
"v_cvt_pk_bf16_f32 v206, v156, v157\n" \
"v_cvt_pk_bf16_f32 v207, v158, v159\n" \
"s_mov_b32 s68, 0xcccccccc\n" \
"s_mov_b32 s69, 0xcccccccc\n" \
"v_mfma_f32_16x16x32_bf16 v[68:71], a[164:167], v[200:203], v[68:71]\n" \
"v_exp_f32_e64 v129, v129\n" \
"v_mfma_f32_16x16x32_bf16 v[72:75], a[136:139], v[216:219], v[72:75]\n" \
"ds_read_b64_tr_b16 a[190:191], v211 offset:30208\n" \
"ds_read_b64_tr_b16 a[128:129], %[vvb] offset:32768\n" \
"v_cndmask_b32_e64 v232, v232, v234, s[68:69]\n" \
"v_and_b32_e32 v230, 0xc0, v235\n" \
"v_mfma_f32_16x16x32_bf16 v[72:75], a[168:171], v[200:203], v[72:75]\n" \
"v_exp_f32_e64 v130, v130\n" \
"v_mfma_f32_16x16x32_bf16 v[76:79], a[140:143], v[216:219], v[76:79]\n" \
"ds_read_b64_tr_b16 a[130:131], %[vvb] offset:36864\n" \
"ds_read_b64_tr_b16 a[132:133], v211 offset:32768\n" \
"v_lshrrev_b32_e64 v233, 7, v224\n" \
"s_mul_i32 s62, %[wid], 8188\n" \
"v_mfma_f32_16x16x32_bf16 v[76:79], a[172:175], v[200:203], v[76:79]\n" \
"v_exp_f32_e64 v131, v131\n" \
"v_smfmac_f32_16x16x64_bf16 v[176:179], a[192:195], v[192:199], v228\n" \
"ds_read_b64_tr_b16 a[134:135], v211 offset:36864\n" \
"ds_read_b64_tr_b16 a[136:137], %[vvb] offset:33280\n" \
"global_load_dwordx2 a[240:241], %[vx0], %[px] offset:0\n" \
"v_mfma_f32_16x16x32_bf16 v[136:139], a[64:67], a[16:19], v[164:167]\n" \
"v_exp_f32_e64 v132, v132\n" \
"v_mfma_f32_16x16x32_bf16 v[136:139], a[68:71], a[20:23], v[136:139]\n" \
"ds_read_b64_tr_b16 a[138:139], %[vvb] offset:37376\n" \
"ds_read_b64_tr_b16 a[140:141], v211 offset:33280\n" \
"v_add_f32_dpp v232, v232, v232 quad_perm:[1,0,3,2] row_mask:0xf bank_mask:0xf\n" \
"v_and_or_b32 v230, v233, 48, v230\n" \
"v_mfma_f32_16x16x32_bf16 v[136:139], a[72:75], a[24:27], v[136:139]\n" \
"v_exp_f32_e64 v133, v133\n" \
"v_mfma_f32_16x16x32_bf16 v[136:139], a[76:79], a[28:31], v[136:139]\n" \
"v_cvt_pk_bf16_f32 v184, v128, v129\n" \
"v_cvt_pk_bf16_f32 v185, v130, v131\n" \
"s_sub_u32 s62, %[m0t], s62\n" \
"v_add_u32_e32 v230, s62, v230\n" \
"v_mfma_f32_16x16x32_bf16 v[140:143], a[80:83], a[16:19], v[164:167]\n" \
"v_exp_f32_e64 v134, v134\n" \
"v_mfma_f32_16x16x32_bf16 v[140:143], a[84:87], a[20:23], v[140:143]\n" \
"ds_read_b64_tr_b16 a[142:143], v211 offset:37376\n" \
"v_add_f32_dpp v232, v232, v232 quad_perm:[2,3,0,1] row_mask:0xf bank_mask:0xf\n" \
"s_waitcnt lgkmcnt(8)\n" \
"v_mfma_f32_16x16x32_bf16 v[140:143], a[88:91], a[24:27], v[140:143]\n" \
"buffer_load_dwordx4 v212, %[srdt], s49 offen offset:1024 lds\n" \
"v_mfma_f32_16x16x32_bf16 v[140:143], a[92:95], a[28:31], v[140:143]\n" \
"v_exp_f32_e32 v135, v135\n" \
"v_mfma_f32_16x16x32_bf16 v[80:83], a[144:147], v[216:219], v[80:83]\n" \
"ds_read_b64_tr_b16 a[144:145], %[vvb] offset:33792\n" \
"ds_read_b64_tr_b16 a[146:147], %[vvb] offset:37888\n" \
"ds_write_b32 v230, v232 offset:7680\n" \
"v_mfma_f32_16x16x32_bf16 v[80:83], a[176:179], v[200:203], v[80:83]\n" \
"v_exp_f32_e64 v136, v136\n" \
"v_mfma_f32_16x16x32_bf16 v[84:87], a[148:151], v[216:219], v[84:87]\n" \
"v_cvt_pk_bf16_f32 v186, v132, v133\n" \
"v_cvt_pk_bf16_f32 v187, v134, v135\n" \
"v_mfma_f32_16x16x32_bf16 v[84:87], a[180:183], v[200:203], v[84:87]\n" \
"v_exp_f32_e64 v137, v137\n" \
"v_mfma_f32_16x16x32_bf16 v[88:91], a[152:155], v[216:219], v[88:91]\n" \
"v_exp_f32_e64 v138, v138\n" \
"v_mfma_f32_16x16x32_bf16 v[88:91], a[184:187], v[200:203], v[88:91]\n" \
"ds_read_b64_tr_b16 a[148:149], v211 offset:33792\n" \
"ds_read_b64_tr_b16 a[150:151], v211 offset:37888\n" \
"v_mfma_f32_16x16x32_bf16 v[92:95], a[156:159], v[216:219], v[92:95]\n" \
"v_exp_f32_e64 v139, v139\n" \
"v_mfma_f32_16x16x32_bf16 v[92:95], a[188:191], v[200:203], v[92:95]\n" \
"ds_read_b64_tr_b16 a[152:153], %[vvb] offset:34304\n" \
"global_load_dwordx2 a[244:245], %[vx0], %[px] offset:1024\n" \
"v_mfma_f32_16x16x32_bf16 v[144:147], a[64:67], a[32:35], v[168:171]\n" \
"v_exp_f32_e64 v140, v140\n" \
"v_mfma_f32_16x16x32_bf16 v[144:147], a[68:71], a[36:39], v[144:147]\n" \
"ds_read_b64_tr_b16 a[154:155], %[vvb] offset:38400\n" \
"ds_read_b64_tr_b16 a[156:157], v211 offset:34304\n" \
"v_mfma_f32_16x16x32_bf16 v[144:147], a[72:75], a[40:43], v[144:147]\n" \
"v_exp_f32_e64 v141, v141\n" \
"v_mfma_f32_16x16x32_bf16 v[144:147], a[76:79], a[44:47], v[144:147]\n" \
"v_cvt_pk_bf16_f32 v188, v136, v137\n" \
"v_cvt_pk_bf16_f32 v189, v138, v139\n" \
"v_mfma_f32_16x16x32_bf16 v[148:151], a[80:83], a[32:35], v[168:171]\n" \
"v_exp_f32_e64 v142, v142\n" \
"v_mfma_f32_16x16x32_bf16 v[148:151], a[84:87], a[36:39], v[148:151]\n" \
"ds_read_b64_tr_b16 a[158:159], v211 offset:38400\n" \
"s_waitcnt lgkmcnt(9)\n" \
"v_mfma_f32_16x16x32_bf16 v[148:151], a[88:91], a[40:43], v[148:151]\n" \
"buffer_load_dwordx4 v213, %[srdt], s49 offen offset:2048 lds\n" \
"v_mfma_f32_16x16x32_bf16 v[148:151], a[92:95], a[44:47], v[148:151]\n" \
"v_exp_f32_e32 v143, v143\n" \
"v_mfma_f32_16x16x32_bf16 v[0:3], a[160:163], v[192:195], v[0:3]\n" \
"ds_read_b128 a[96:99], %[kb0] offset:40960\n" \
"v_mfma_f32_16x16x32_bf16 v[0:3], a[128:131], v[184:187], v[0:3]\n" \
"v_exp_f32_e64 v144, v144\n" \
"v_mfma_f32_16x16x32_bf16 v[4:7], a[164:167], v[192:195], v[4:7]\n" \
"v_cvt_pk_bf16_f32 v190, v140, v141\n" \
"v_cvt_pk_bf16_f32 v191, v142, v143\n" \
"v_mfma_f32_16x16x32_bf16 v[4:7], a[132:135], v[184:187], v[4:7]\n" \
"v_exp_f32_e64 v145, v145\n" \
"v_mfma_f32_16x16x32_bf16 v[8:11], a[168:171], v[192:195], v[8:11]\n" \
"ds_read_b128 a[100:103], v208 offset:40960\n" \
"v_mfma_f32_16x16x32_bf16 v[8:11], a[136:139], v[184:187], v[8:11]\n" \
"v_exp_f32_e64 v146, v146\n" \
"v_mfma_f32_16x16x32_bf16 v[12:15], a[172:175], v[192:195], v[12:15]\n" \
"ds_read_b128 a[104:107], v209 offset:40960\n" \
"v_mfma_f32_16x16x32_bf16 v[12:15], a[140:143], v[184:187], v[12:15]\n" \
"v_exp_f32_e64 v147, v147\n" \
"v_smfmac_f32_16x16x64_bf16 v[180:183], a[192:195], v[200:207], v228\n" \
"ds_read_b128 a[108:111], v210 offset:40960\n" \
"global_load_dwordx2 a[248:249], v229, %[px] offset:0\n" \
"v_mfma_f32_16x16x32_bf16 v[152:155], a[64:67], a[48:51], v[172:175]\n" \
"v_exp_f32_e64 v148, v148\n" \
"v_mfma_f32_16x16x32_bf16 v[152:155], a[68:71], a[52:55], v[152:155]\n" \
"ds_read_b128 a[112:115], %[kb0] offset:45056\n" \
"v_mfma_f32_16x16x32_bf16 v[152:155], a[72:75], a[56:59], v[152:155]\n" \
"v_exp_f32_e64 v149, v149\n" \
"v_mfma_f32_16x16x32_bf16 v[152:155], a[76:79], a[60:63], v[152:155]\n" \
"v_cvt_pk_bf16_f32 v216, v144, v145\n" \
"v_cvt_pk_bf16_f32 v217, v146, v147\n" \
"v_mfma_f32_16x16x32_bf16 v[156:159], a[80:83], a[48:51], v[172:175]\n" \
"v_exp_f32_e64 v150, v150\n" \
"v_mfma_f32_16x16x32_bf16 v[156:159], a[84:87], a[52:55], v[156:159]\n" \
"ds_read_b128 a[116:119], v208 offset:45056\n" \
"s_waitcnt lgkmcnt(6)\n" \
"v_mfma_f32_16x16x32_bf16 v[156:159], a[88:91], a[56:59], v[156:159]\n" \
"buffer_load_dwordx4 v214, %[srdt], s49 offen offset:3072 lds\n" \
"v_mfma_f32_16x16x32_bf16 v[156:159], a[92:95], a[60:63], v[156:159]\n" \
"v_exp_f32_e32 v151, v151\n" \
"v_mfma_f32_16x16x32_bf16 v[16:19], a[176:179], v[192:195], v[16:19]\n" \
"ds_read_b128 a[120:123], v209 offset:45056\n" \
"v_mfma_f32_16x16x32_bf16 v[16:19], a[144:147], v[184:187], v[16:19]\n" \
"v_exp_f32_e64 v152, v152\n" \
"v_mfma_f32_16x16x32_bf16 v[20:23], a[180:183], v[192:195], v[20:23]\n" \
"v_cvt_pk_bf16_f32 v218, v148, v149\n" \
"v_cvt_pk_bf16_f32 v219, v150, v151\n" \
"v_mfma_f32_16x16x32_bf16 v[20:23], a[148:151], v[184:187], v[20:23]\n" \
"v_exp_f32_e64 v153, v153\n" \
"v_mfma_f32_16x16x32_bf16 v[24:27], a[184:187], v[192:195], v[24:27]\n" \
"v_exp_f32_e64 v154, v154\n" \
"v_mfma_f32_16x16x32_bf16 v[24:27], a[152:155], v[184:187], v[24:27]\n" \
"ds_read_b128 a[124:127], v210 offset:45056\n" \
"global_load_dwordx2 a[252:253], v229, %[px] offset:1024\n" \
"v_mfma_f32_16x16x32_bf16 v[28:31], a[188:191], v[192:195], v[28:31]\n" \
"v_exp_f32_e64 v155, v155\n" \
"v_mfma_f32_16x16x32_bf16 v[28:31], a[156:159], v[184:187], v[28:31]\n" \
"s_waitcnt vmcnt(63) lgkmcnt(2)\n" \
"s_barrier\n" \
"v_mfma_f32_16x16x32_bf16 v[128:131], a[96:99], a[0:3], v[160:163]\n" \
"v_exp_f32_e64 v156, v156\n" \
"s_lshl_b32 s62, %[wid], 13\n" \
"s_sub_u32 s62, %[m0t], s62\n" \
"v_mfma_f32_16x16x32_bf16 v[128:131], a[100:103], a[4:7], v[128:131]\n" \
"v_lshrrev_b32_e64 v227, 5, v231\n" \
"v_and_b32_e32 v230, 0xf0, v235\n" \
"v_add_u32_e64 v230, s62, v230\n" \
"v_mfma_f32_16x16x32_bf16 v[128:131], a[104:107], a[8:11], v[128:131]\n" \
"v_exp_f32_e64 v157, v157\n" \
"v_mfma_f32_16x16x32_bf16 v[128:131], a[108:111], a[12:15], v[128:131]\n" \
"v_cvt_pk_bf16_f32 v220, v152, v153\n" \
"v_cvt_pk_bf16_f32 v221, v154, v155\n" \
"ds_read_b128 v[232:235], v230 offset:7680\n" \
"v_readlane_b32 s51, %[thi], 31\n" \
"v_mfma_f32_16x16x32_bf16 v[132:135], a[112:115], a[0:3], v[160:163]\n" \
"v_exp_f32_e64 v158, v158\n" \
"v_mfma_f32_16x16x32_bf16 v[132:135], a[116:119], a[4:7], v[132:135]\n" \
"v_lshlrev_b32_e32 v227, 10, v227\n" \
"s_waitcnt lgkmcnt(1)\n" \
"v_mfma_f32_16x16x32_bf16 v[132:135], a[120:123], a[8:11], v[132:135]\n" \
"s_add_u32 m0, %[m0b], 81920\n" \
"s_mov_b32 s61, 917504\n" \
"buffer_load_dwordx4 %[vg0], s[64:67], s61 offen offset:0 lds\n" \
"v_mfma_f32_16x16x32_bf16 v[132:135], a[124:127], a[12:15], v[132:135]\n" \
"v_exp_f32_e64 v159, v159\n" \
"v_mfma_f32_16x16x32_bf16 v[32:35], a[160:163], v[196:199], v[32:35]\n" \
"ds_read_b128 a[64:67], %[kb0] offset:49152\n" \
"v_add_u32_e32 v227, 0x10000, v227\n" \
"v_mfma_f32_16x16x32_bf16 v[32:35], a[128:131], v[188:191], v[32:35]\n" \
"v_exp_f32_e64 v128, v128\n" \
"v_mfma_f32_16x16x32_bf16 v[36:39], a[164:167], v[196:199], v[36:39]\n" \
"v_cvt_pk_bf16_f32 v222, v156, v157\n" \
"v_cvt_pk_bf16_f32 v223, v158, v159\n" \
"s_waitcnt lgkmcnt(1)\n" \
"v_add_f32_e32 v232, v232, v233\n" \
"v_add_f32_e64 v234, v234, v235\n" \
"v_mfma_f32_16x16x32_bf16 v[36:39], a[132:135], v[188:191], v[36:39]\n" \
"v_exp_f32_e64 v129, v129\n" \
"v_mfma_f32_16x16x32_bf16 v[40:43], a[168:171], v[196:199], v[40:43]\n" \
"ds_read_b128 a[68:71], v208 offset:49152\n" \
"v_sub_u32_e32 v215, %[vvb], v227\n" \
"v_add_f32_e32 v232, v232, v234\n" \
"v_lshrrev_b32_e64 v230, 6, v224\n" \
"v_mfma_f32_16x16x32_bf16 v[40:43], a[136:139], v[188:191], v[40:43]\n" \
"v_exp_f32_e64 v130, v130\n" \
"v_mfma_f32_16x16x32_bf16 v[44:47], a[172:175], v[196:199], v[44:47]\n" \
"ds_read_b128 a[72:75], v209 offset:49152\n" \
"v_xor_b32_e64 v227, 32, v215\n" \
"v_mov_b32_dpp v233, v232 quad_perm:[1,0,3,2] row_mask:0xf bank_mask:0xf\n" \
"v_mfma_f32_16x16x32_bf16 v[44:47], a[140:143], v[188:191], v[44:47]\n" \
"v_exp_f32_e64 v131, v131\n" \
"v_smfmac_f32_16x16x64_bf16 v[176:179], a[192:195], v[184:191], v228\n" \
"global_load_dwordx2 a[242:243], %[vx0], %[px] offset:64\n" \
"v_mfma_f32_16x16x32_bf16 v[136:139], a[96:99], a[16:19], v[164:167]\n" \
"v_exp_f32_e64 v132, v132\n" \
"v_mfma_f32_16x16x32_bf16 v[136:139], a[100:103], a[20:23], v[136:139]\n" \
"ds_read_b128 a[76:79], v210 offset:49152\n" \
"v_cvt_pk_bf16_f32 v232, v232, v233\n" \
"v_mfma_f32_16x16x32_bf16 v[136:139], a[104:107], a[24:27], v[136:139]\n" \
"v_exp_f32_e64 v133, v133\n" \
"v_mfma_f32_16x16x32_bf16 v[136:139], a[108:111], a[28:31], v[136:139]\n" \
"v_cvt_pk_bf16_f32 v192, v128, v129\n" \
"v_cvt_pk_bf16_f32 v193, v130, v131\n" \
"s_cmp_eq_u32 %[wid], 0\n" \
"s_cselect_b32 s51, s51, 0x80000000\n" \
"s_mov_b64 exec, 0x5555\n" \
"buffer_store_dword v232, v230, %[srdz], s51 offen sc1\n" \
"s_mov_b64 exec, -1\n" \
"v_mfma_f32_16x16x32_bf16 v[140:143], a[112:115], a[16:19], v[164:167]\n" \
"v_exp_f32_e64 v134, v134\n" \
"v_mfma_f32_16x16x32_bf16 v[140:143], a[116:119], a[20:23], v[140:143]\n" \
"ds_read_b128 a[80:83], %[kb0] offset:53248\n" \
"v_mfma_f32_16x16x32_bf16 v[140:143], a[120:123], a[24:27], v[140:143]\n" \
"s_add_u32 m0, %[m0b], 82944\n" \
"s_mov_b32 s61, 1146880\n" \
"buffer_load_dwordx4 %[vg0], s[64:67], s61 offen offset:0 lds\n" \
"v_mfma_f32_16x16x32_bf16 v[140:143], a[124:127], a[28:31], v[140:143]\n" \
"v_exp_f32_e64 v135, v135\n" \
"v_mfma_f32_16x16x32_bf16 v[48:51], a[176:179], v[196:199], v[48:51]\n" \
"ds_read_b128 a[84:87], v208 offset:53248\n" \
"v_mfma_f32_16x16x32_bf16 v[48:51], a[144:147], v[188:191], v[48:51]\n" \
"v_exp_f32_e64 v136, v136\n" \
"v_mfma_f32_16x16x32_bf16 v[52:55], a[180:183], v[196:199], v[52:55]\n" \
"v_cvt_pk_bf16_f32 v194, v132, v133\n" \
"v_cvt_pk_bf16_f32 v195, v134, v135\n" \
"v_mfma_f32_16x16x32_bf16 v[52:55], a[148:151], v[188:191], v[52:55]\n" \
"v_exp_f32_e64 v137, v137\n" \
"v_mfma_f32_16x16x32_bf16 v[56:59], a[184:187], v[196:199], v[56:59]\n" \
"v_exp_f32_e64 v138, v138\n" \
"v_mfma_f32_16x16x32_bf16 v[56:59], a[152:155], v[188:191], v[56:59]\n" \
"ds_read_b128 a[88:91], v209 offset:53248\n" \
"v_mfma_f32_16x16x32_bf16 v[60:63], a[188:191], v[196:199], v[60:63]\n" \
"v_exp_f32_e64 v139, v139\n" \
"v_mfma_f32_16x16x32_bf16 v[60:63], a[156:159], v[188:191], v[60:63]\n" \
"global_load_dwordx2 a[246:247], %[vx0], %[px] offset:1088\n" \
"v_mfma_f32_16x16x32_bf16 v[144:147], a[96:99], a[32:35], v[168:171]\n" \
"v_exp_f32_e64 v140, v140\n" \
"v_mfma_f32_16x16x32_bf16 v[144:147], a[100:103], a[36:39], v[144:147]\n" \
"ds_read_b128 a[92:95], v210 offset:53248\n" \
"v_lshrrev_b32_e64 v236, 3, v224\n" \
"v_and_b32_e32 v230, 0xb0, v236\n" \
"v_mfma_f32_16x16x32_bf16 v[144:147], a[104:107], a[40:43], v[144:147]\n" \
"v_exp_f32_e64 v141, v141\n" \
"v_mfma_f32_16x16x32_bf16 v[144:147], a[108:111], a[44:47], v[144:147]\n" \
"v_cvt_pk_bf16_f32 v196, v136, v137\n" \
"v_cvt_pk_bf16_f32 v197, v138, v139\n" \
"v_lshrrev_b32_e64 v236, 2, v236\n" \
"v_and_or_b32 v230, v236, 64, v230\n" \
"v_mfma_f32_16x16x32_bf16 v[148:151], a[112:115], a[32:35], v[168:171]\n" \
"v_exp_f32_e64 v142, v142\n" \
"v_mfma_f32_16x16x32_bf16 v[148:151], a[116:119], a[36:39], v[148:151]\n" \
"v_lshrrev_b32_e64 v236, 12, v224\n" \
"v_lshl_or_b32 v230, v236, 13, v230\n" \
"v_mfma_f32_16x16x32_bf16 v[148:151], a[120:123], a[40:43], v[148:151]\n" \
"s_add_u32 m0, %[m0b], 83968\n" \
"s_mov_b32 s61, 1376256\n" \
"buffer_load_dwordx4 %[vg0], s[64:67], s61 offen offset:0 lds\n" \
"v_mfma_f32_16x16x32_bf16 v[148:151], a[124:127], a[44:47], v[148:151]\n" \
".if . - adaln_body != 36864\n" \
".error \"size model\"\n" \
".endif\n" \
"Lk8_%=:\n" \
"s_cbranch_execz Lk9_%=\n" \
"s_nop 0\n" \
"v_exp_f32_e64 v143, v143\n" \
"v_readlane_b32 s51, %[thi], 26\n" \
"v_add_u32_e64 v230, s62, v230\n" \
"v_mfma_f32_16x16x32_bf16 v[96:99], a[160:163], v[204:207], v[96:99]\n" \
"ds_read_b64_tr_b16 a[160:161], %[vvb] offset:40960\n" \
"v_mfma_f32_16x16x32_bf16 v[96:99], a[128:131], v[220:223], v[96:99]\n" \
"v_exp_f32_e64 v144, v144\n" \
"v_mfma_f32_16x16x32_bf16 v[100:103], a[164:167], v[204:207], v[100:103]\n" \
"v_cvt_pk_bf16_f32 v198, v140, v141\n" \
"v_cvt_pk_bf16_f32 v199, v142, v143\n" \
"v_add_u32_e64 v230, s51, v230\n" \
"v_mfma_f32_16x16x32_bf16 v[100:103], a[132:135], v[220:223], v[100:103]\n" \
"v_exp_f32_e64 v145, v145\n" \
"v_mfma_f32_16x16x32_bf16 v[104:107], a[168:171], v[204:207], v[104:107]\n" \
"ds_read_b64_tr_b16 a[162:163], %[vvb] offset:45056\n" \
"ds_read_b64_tr_b16 a[164:165], v211 offset:40960\n" \
"v_xor_b32_e32 v236, 0x100, v230\n" \
"v_mfma_f32_16x16x32_bf16 v[104:107], a[136:139], v[220:223], v[104:107]\n" \
"v_exp_f32_e64 v146, v146\n" \
"v_mfma_f32_16x16x32_bf16 v[108:111], a[172:175], v[204:207], v[108:111]\n" \
"ds_read_b64_tr_b16 a[166:167], v211 offset:45056\n" \
"ds_read_b128 a[212:215], v230 offset:6656\n" \
"v_mfma_f32_16x16x32_bf16 v[108:111], a[140:143], v[220:223], v[108:111]\n" \
"v_exp_f32_e64 v147, v147\n" \
"v_smfmac_f32_16x16x64_bf16 v[180:183], a[192:195], v[216:223], v228\n" \
"ds_read_b64_tr_b16 a[168:169], %[vvb] offset:41472\n" \
"global_load_dwordx2 a[250:251], v229, %[px] offset:64\n" \
"ds_read_b128 a[216:219], v230 offset:23040\n" \
"v_mfma_f32_16x16x32_bf16 v[152:155], a[96:99], a[48:51], v[172:175]\n" \
"v_exp_f32_e64 v148, v148\n" \
"v_mfma_f32_16x16x32_bf16 v[152:155], a[100:103], a[52:55], v[152:155]\n" \
"ds_read_b64_tr_b16 a[170:171], %[vvb] offset:45568\n" \
"ds_read_b128 a[220:223], v236 offset:6656\n" \
"v_mfma_f32_16x16x32_bf16 v[152:155], a[104:107], a[56:59], v[152:155]\n" \
"v_exp_f32_e64 v149, v149\n" \
"v_mfma_f32_16x16x32_bf16 v[152:155], a[108:111], a[60:63], v[152:155]\n" \
"v_cvt_pk_bf16_f32 v200, v144, v145\n" \
"v_cvt_pk_bf16_f32 v201, v146, v147\n" \
"ds_read_b128 a[224:227], v236 offset:23040\n" \
"v_mfma_f32_16x16x32_bf16 v[156:159], a[112:115], a[48:51], v[172:175]\n" \
"v_exp_f32_e64 v150, v150\n" \
"v_mfma_f32_16x16x32_bf16 v[156:159], a[116:119], a[52:55], v[156:159]\n" \
"ds_read_b64_tr_b16 a[172:173], v211 offset:41472\n" \
"v_mfma_f32_16x16x32_bf16 v[156:159], a[120:123], a[56:59], v[156:159]\n" \
"s_add_u32 m0, %[m0b], 84992\n" \
"s_mov_b32 s61, 1605632\n" \
"buffer_load_dwordx4 %[vg0], s[64:67], s61 offen offset:0 lds\n" \
"v_mfma_f32_16x16x32_bf16 v[156:159], a[124:127], a[60:63], v[156:159]\n" \
"v_exp_f32_e64 v151, v151\n" \
"v_mfma_f32_16x16x32_bf16 v[112:115], a[176:179], v[204:207], v[112:115]\n" \
"ds_read_b64_tr_b16 a[174:175], v211 offset:45568\n" \
"ds_read_b64_tr_b16 a[176:177], %[vvb] offset:41984\n" \
"v_mfma_f32_16x16x32_bf16 v[112:115], a[144:147], v[220:223], v[112:115]\n" \
"v_exp_f32_e64 v152, v152\n" \
"v_mfma_f32_16x16x32_bf16 v[116:119], a[180:183], v[204:207], v[116:119]\n" \
"v_cvt_pk_bf16_f32 v202, v148, v149\n" \
"v_cvt_pk_bf16_f32 v203, v150, v151\n" \
"v_mfma_f32_16x16x32_bf16 v[116:119], a[148:151], v[220:223], v[116:119]\n" \
"v_exp_f32_e64 v153, v153\n" \
"v_mfma_f32_16x16x32_bf16 v[120:123], a[184:187], v[204:207], v[120:123]\n" \
"v_exp_f32_e64 v154, v154\n" \
"v_mfma_f32_16x16x32_bf16 v[120:123], a[152:155], v[220:223], v[120:123]\n" \
"ds_read_b64_tr_b16 a[178:179], %[vvb] offset:46080\n" \
"global_load_dwordx2 a[254:255], v229, %[px] offset:1088\n" \
"v_mfma_f32_16x16x32_bf16 v[124:127], a[188:191], v[204:207], v[124:127]\n" \
"v_exp_f32_e64 v155, v155\n" \
"v_mfma_f32_16x16x32_bf16 v[124:127], a[156:159], v[220:223], v[124:127]\n" \
"ds_read_b64_tr_b16 a[180:181], v211 offset:41984\n" \
"s_waitcnt lgkmcnt(14)\n" \
"v_mfma_f32_16x16x32_bf16 v[128:131], a[64:67], a[0:3], v[160:163]\n" \
"v_exp_f32_e32 v156, v156\n" \
"v_mfma_f32_16x16x32_bf16 v[128:131], a[68:71], a[4:7], v[128:131]\n" \
"ds_read_b64_tr_b16 a[182:183], v211 offset:46080\n" \
"s_add_u32 m0, %[m0b], 32768\n" \
"s_cmp_gt_u32 s48, 0\n" \
"v_mfma_f32_16x16x32_bf16 v[128:131], a[72:75], a[8:11], v[128:131]\n" \
"v_exp_f32_e32 v157, v157\n" \
"v_mfma_f32_16x16x32_bf16 v[128:131], a[76:79], a[12:15], v[128:131]\n" \
"v_cvt_pk_bf16_f32 v204, v152, v153\n" \
"v_cvt_pk_bf16_f32 v205, v154, v155\n" \
"v_mfma_f32_16x16x32_bf16 v[132:135], a[80:83], a[0:3], v[160:163]\n" \
"v_exp_f32_e64 v158, v158\n" \
"v_mfma_f32_16x16x32_bf16 v[132:135], a[84:87], a[4:7], v[132:135]\n" \
"ds_read_b64_tr_b16 a[184:185], %[vvb] offset:42496\n" \
"s_cselect_b32 s49, 0, 1\n" \
"s_lshl_b32 s49, s49, 18\n" \
"s_or_b32 s49, s49, s58\n" \
"s_waitcnt lgkmcnt(5)\n" \
"v_mfma_f32_16x16x32_bf16 v[132:135], a[88:91], a[8:11], v[132:135]\n" \
"buffer_load_dwordx4 %[wo0], %[srdt], s49 offen offset:0 lds\n" \
"v_mfma_f32_16x16x32_bf16 v[132:135], a[92:95], a[12:15], v[132:135]\n" \
"v_exp_f32_e64 v159, v159\n" \
"v_mfma_f32_16x16x32_bf16 v[64:67], a[128:131], v[216:219], v[64:67]\n" \
"ds_read_b64_tr_b16 a[186:187], %[vvb] offset:46592\n" \
"ds_read_b64_tr_b16 a[188:189], v211 offset:42496\n" \
"v_mfma_f32_16x16x32_bf16 v[64:67], a[160:163], v[200:203], v[64:67]\n" \
"v_exp_f32_e64 v128, v128\n" \
"v_mfma_f32_16x16x32_bf16 v[68:71], a[132:135], v[216:219], v[68:71]\n" \
"v_cvt_pk_bf16_f32 v206, v156, v157\n" \
"v_cvt_pk_bf16_f32 v207, v158, v159\n" \
"v_mfma_f32_16x16x32_bf16 v[68:71], a[164:167], v[200:203], v[68:71]\n" \
"v_exp_f32_e64 v129, v129\n" \
"v_mfma_f32_16x16x32_bf16 v[72:75], a[136:139], v[216:219], v[72:75]\n" \
"ds_read_b64_tr_b16 a[190:191], v211 offset:46592\n" \
"ds_read_b64_tr_b16 a[128:129], %[vvb] offset:49152\n" \
"v_mfma_f32_16x16x32_bf16 v[72:75], a[168:171], v[200:203], v[72:75]\n" \
"v_exp_f32_e64 v130, v130\n" \
"v_mfma_f32_16x16x32_bf16 v[76:79], a[140:143], v[216:219], v[76:79]\n" \
"ds_read_b64_tr_b16 a[130:131], %[vvb] offset:53248\n" \
"ds_read_b64_tr_b16 a[132:133], v211 offset:49152\n" \
"v_mfma_f32_16x16x32_bf16 v[76:79], a[172:175], v[200:203], v[76:79]\n" \
"v_exp_f32_e64 v131, v131\n" \
"v_smfmac_f32_16x16x64_bf16 v[176:179], a[192:195], v[192:199], v228\n" \
"ds_read_b64_tr_b16 a[134:135], v211 offset:53248\n" \
"ds_read_b64_tr_b16 a[136:137], %[vvb] offset:49664\n" \
"v_mfma_f32_16x16x32_bf16 v[136:139], a[64:67], a[16:19], v[164:167]\n" \
"v_exp_f32_e64 v132, v132\n" \
"v_mfma_f32_16x16x32_bf16 v[136:139], a[68:71], a[20:23], v[136:139]\n" \
"ds_read_b64_tr_b16 a[138:139], %[vvb] offset:53760\n" \
"ds_read_b64_tr_b16 a[140:141], v211 offset:49664\n" \
"v_mfma_f32_16x16x32_bf16 v[136:139], a[72:75], a[24:27], v[136:139]\n" \
"v_exp_f32_e64 v133, v133\n" \
"v_mfma_f32_16x16x32_bf16 v[136:139], a[76:79], a[28:31], v[136:139]\n" \
"v_cvt_pk_bf16_f32 v184, v128, v129\n" \
"v_cvt_pk_bf16_f32 v185, v130, v131\n" \
"v_mfma_f32_16x16x32_bf16 v[140:143], a[80:83], a[16:19], v[164:167]\n" \
"v_exp_f32_e64 v134, v134\n" \
"v_mfma_f32_16x16x32_bf16 v[140:143], a[84:87], a[20:23], v[140:143]\n" \
"ds_read_b64_tr_b16 a[142:143], v211 offset:53760\n" \
"s_waitcnt lgkmcnt(8)\n" \
"v_mfma_f32_16x16x32_bf16 v[140:143], a[88:91], a[24:27], v[140:143]\n" \
"buffer_load_dwordx4 v212, %[srdt], s49 offen offset:1024 lds\n" \
"v_mfma_f32_16x16x32_bf16 v[140:143], a[92:95], a[28:31], v[140:143]\n" \
"v_exp_f32_e32 v135, v135\n" \
"v_mfma_f32_16x16x32_bf16 v[80:83], a[144:147], v[216:219], v[80:83]\n" \
"ds_read_b64_tr_b16 a[144:145], %[vvb] offset:50176\n" \
"ds_read_b64_tr_b16 a[146:147], %[vvb] offset:54272\n" \
"v_mfma_f32_16x16x32_bf16 v[80:83], a[176:179], v[200:203], v[80:83]\n" \
"v_exp_f32_e64 v136, v136\n" \
"v_mfma_f32_16x16x32_bf16 v[84:87], a[148:151], v[216:219], v[84:87]\n" \
"v_cvt_pk_bf16_f32 v186, v132, v133\n" \
"v_cvt_pk_bf16_f32 v187, v134, v135\n" \
"v_mfma_f32_16x16x32_bf16 v[84:87], a[180:183], v[200:203], v[84:87]\n" \
"v_exp_f32_e64 v137, v137\n" \
"v_mfma_f32_16x16x32_bf16 v[88:91], a[152:155], v[216:219], v[88:91]\n" \
"v_exp_f32_e64 v138, v138\n" \
"v_mfma_f32_16x16x32_bf16 v[88:91], a[184:187], v[200:203], v[88:91]\n" \
"ds_read_b64_tr_b16 a[148:149], v211 offset:50176\n" \
"ds_read_b64_tr_b16 a[150:151], v211 offset:54272\n" \
"v_readlane_b32 s64, %[tlo], 9\n" \
"v_mfma_f32_16x16x32_bf16 v[92:95], a[156:159], v[216:219], v[92:95]\n" \
"v_exp_f32_e64 v139, v139\n" \
"v_mfma_f32_16x16x32_bf16 v[92:95], a[188:191], v[200:203], v[92:95]\n" \
"ds_read_b64_tr_b16 a[152:153], %[vvb] offset:50688\n" \
"v_readlane_b32 s65, %[thi], 9\n" \
"v_mfma_f32_16x16x32_bf16 v[144:147], a[64:67], a[32:35], v[168:171]\n" \
"v_exp_f32_e64 v140, v140\n" \
"v_mfma_f32_16x16x32_bf16 v[144:147], a[68:71], a[36:39], v[144:147]\n" \
"ds_read_b64_tr_b16 a[154:155], %[vvb] offset:54784\n" \
"ds_read_b64_tr_b16 a[156:157], v211 offset:50688\n" \
"v_mfma_f32_16x16x32_bf16 v[144:147], a[72:75], a[40:43], v[144:147]\n" \
"v_exp_f32_e64 v141, v141\n" \
"v_mfma_f32_16x16x32_bf16 v[144:147], a[76:79], a[44:47], v[144:147]\n" \
"v_cvt_pk_bf16_f32 v188, v136, v137\n" \
"v_cvt_pk_bf16_f32 v189, v138, v139\n" \
"v_mfma_f32_16x16x32_bf16 v[148:151], a[80:83], a[32:35], v[168:171]\n" \
"v_exp_f32_e64 v142, v142\n" \
"v_mfma_f32_16x16x32_bf16 v[148:151], a[84:87], a[36:39], v[148:151]\n" \
"ds_read_b64_tr_b16 a[158:159], v211 offset:54784\n" \
"s_waitcnt lgkmcnt(12)\n" \
"v_mfma_f32_16x16x32_bf16 v[148:151], a[88:91], a[40:43], v[148:151]\n" \
"buffer_load_dwordx4 v213, %[srdt], s49 offen offset:2048 lds\n" \
"v_mfma_f32_16x16x32_bf16 v[148:151], a[92:95], a[44:47], v[148:151]\n" \
"v_exp_f32_e32 v143, v143\n" \
"v_mfma_f32_16x16x32_bf16 v[0:3], a[160:163], v[192:195], v[0:3]\n" \
"ds_read_b128 a[96:99], %[kb0] offset:57344\n" \
"v_mfma_f32_16x16x32_bf16 v[0:3], a[128:131], v[184:187], v[0:3]\n" \
"v_exp_f32_e64 v144, v144\n" \
"v_mfma_f32_16x16x32_bf16 v[4:7], a[164:167], v[192:195], v[4:7]\n" \
"v_cvt_pk_bf16_f32 v190, v140, v141\n" \
"v_cvt_pk_bf16_f32 v191, v142, v143\n" \
"v_mfma_f32_16x16x32_bf16 v[4:7], a[132:135], v[184:187], v[4:7]\n" \
"v_exp_f32_e64 v145, v145\n" \
"v_mfma_f32_16x16x32_bf16 v[8:11], a[168:171], v[192:195], v[8:11]\n" \
"ds_read_b128 a[100:103], v208 offset:57344\n" \
"s_waitcnt vmcnt(3) lgkmcnt(10)\n" \
"v_mfma_f32_16x16x32_bf16 v[232:235], a[240:243], a[212:215], 0\n" \
"v_mfma_f32_16x16x32_bf16 v[8:11], a[136:139], v[184:187], v[8:11]\n" \
"v_exp_f32_e32 v146, v146\n" \
"v_mfma_f32_16x16x32_bf16 v[12:15], a[172:175], v[192:195], v[12:15]\n" \
"ds_read_b128 a[104:107], v209 offset:57344\n" \
"v_mfma_f32_16x16x32_bf16 v[232:235], a[244:247], a[216:219], v[232:235]\n" \
"v_mfma_f32_16x16x32_bf16 v[12:15], a[140:143], v[184:187], v[12:15]\n" \
"v_exp_f32_e64 v147, v147\n" \
"v_smfmac_f32_16x16x64_bf16 v[180:183], a[192:195], v[200:207], v228\n" \
"ds_read_b128 a[108:111], v210 offset:57344\n" \
"buffer_load_dword v229, v226, %[srdz], s50 offen sc1\n" \
"v_mfma_f32_16x16x32_bf16 v[232:235], a[248:251], a[220:223], v[232:235]\n" \
"v_mfma_f32_16x16x32_bf16 v[152:155], a[64:67], a[48:51], v[172:175]\n" \
"v_exp_f32_e64 v148, v148\n" \
"v_mfma_f32_16x16x32_bf16 v[152:155], a[68:71], a[52:55], v[152:155]\n" \
"ds_read_b128 a[112:115], %[kb0] offset:61440\n" \
"v_mfma_f32_16x16x32_bf16 v[232:235], a[252:255], a[224:227], v[232:235]\n" \
"v_mfma_f32_16x16x32_bf16 v[152:155], a[72:75], a[56:59], v[152:155]\n" \
"v_exp_f32_e64 v149, v149\n" \
"v_mfma_f32_16x16x32_bf16 v[152:155], a[76:79], a[60:63], v[152:155]\n" \
"v_cvt_pk_bf16_f32 v216, v144, v145\n" \
"v_cvt_pk_bf16_f32 v217, v146, v147\n" \
"v_mfma_f32_16x16x32_bf16 v[156:159], a[80:83], a[48:51], v[172:175]\n" \
"v_exp_f32_e64 v150, v150\n" \
"v_mfma_f32_16x16x32_bf16 v[156:159], a[84:87], a[52:55], v[156:159]\n" \
"ds_read_b128 a[116:119], v208 offset:61440\n" \
"s_mov_b32 s68, 0xaaaaaaaa\n" \
"s_mov_b32 s69, 0xaaaaaaaa\n" \
"s_waitcnt lgkmcnt(6)\n" \
"v_mfma_f32_16x16x32_bf16 v[156:159], a[88:91], a[56:59], v[156:159]\n" \
"buffer_load_dwordx4 v214, %[srdt], s49 offen offset:3072 lds\n" \
"v_mfma_f32_16x16x32_bf16 v[156:159], a[92:95], a[60:63], v[156:159]\n" \
"v_exp_f32_e32 v151, v151\n" \
"v_cndmask_b32_e64 v232, v232, v233, s[68:69]\n" \
"v_cndmask_b32_e64 v234, v234, v235, s[68:69]\n" \
"v_mfma_f32_16x16x32_bf16 v[16:19], a[176:179], v[192:195], v[16:19]\n" \
"ds_read_b128 a[120:123], v209 offset:61440\n" \
"v_mfma_f32_16x16x32_bf16 v[16:19], a[144:147], v[184:187], v[16:19]\n" \
"v_exp_f32_e64 v152, v152\n" \
"s_mov_b32 s68, 0xcccccccc\n" \
"s_mov_b32 s69, 0xcccccccc\n" \
"v_mfma_f32_16x16x32_bf16 v[20:23], a[180:183], v[192:195], v[20:23]\n" \
"v_cvt_pk_bf16_f32 v218, v148, v149\n" \
"v_cvt_pk_bf16_f32 v219, v150, v151\n" \
"v_mfma_f32_16x16x32_bf16 v[20:23], a[148:151], v[184:187], v[20:23]\n" \
"v_exp_f32_e64 v153, v153\n" \
"v_cndmask_b32_e64 v232, v232, v234, s[68:69]\n" \
"v_mfma_f32_16x16x32_bf16 v[24:27], a[184:187], v[192:195], v[24:27]\n" \
"v_exp_f32_e64 v154, v154\n" \
"s_mov_b32 s68, 0xf0000f\n" \
"s_mov_b32 s69, 0xf0000f00\n" \
"v_mfma_f32_16x16x32_bf16 v[24:27], a[152:155], v[184:187], v[24:27]\n" \
"ds_read_b128 a[124:127], v210 offset:61440\n" \
"v_mfma_f32_16x16x32_bf16 v[28:31], a[188:191], v[192:195], v[28:31]\n" \
"v_exp_f32_e64 v155, v155\n" \
"v_cndmask_b32_e64 %[rd0], 0, v232, s[68:69]\n" \
"v_mfma_f32_16x16x32_bf16 v[28:31], a[156:159], v[184:187], v[28:31]\n" \
"s_waitcnt vmcnt(27) lgkmcnt(2)\n" \
"s_barrier\n" \
"v_mfma_f32_16x16x32_bf16 v[128:131], a[96:99], a[0:3], v[160:163]\n" \
"v_exp_f32_e64 v156, v156\n" \
"s_mov_b32 m0, s44\n" \
"s_addk_i32 s44, 0x400\n" \
"buffer_load_dwordx4 %[vg0], s[64:67], s45 offen offset:0 lds\n" \
"s_add_u32 s45, s45, 229376\n" \
"v_mfma_f32_16x16x32_bf16 v[128:131], a[100:103], a[4:7], v[128:131]\n" \
"s_add_u32 m0, %[m0b], 98304\n" \
"s_cmp_gt_u32 s48, 0\n" \
"v_mfma_f32_16x16x32_bf16 v[128:131], a[104:107], a[8:11], v[128:131]\n" \
"v_exp_f32_e32 v157, v157\n" \
"v_mfma_f32_16x16x32_bf16 v[128:131], a[108:111], a[12:15], v[128:131]\n" \
"v_cvt_pk_bf16_f32 v220, v152, v153\n" \
"v_cvt_pk_bf16_f32 v221, v154, v155\n" \
"v_mfma_f32_16x16x32_bf16 v[132:135], a[112:115], a[0:3], v[160:163]\n" \
"v_exp_f32_e64 v158, v158\n" \
"v_mfma_f32_16x16x32_bf16 v[132:135], a[116:119], a[4:7], v[132:135]\n" \
"s_cselect_b32 s49, 0, 1\n" \
"s_lshl_b32 s49, s49, 18\n" \
"s_or_b32 s49, s49, s59\n" \
"s_waitcnt lgkmcnt(0)\n" \
"v_mfma_f32_16x16x32_bf16 v[132:135], a[120:123], a[8:11], v[132:135]\n" \
"buffer_load_dwordx4 %[wo0], %[srdt], s49 offen offset:0 lds\n" \
"v_mfma_f32_16x16x32_bf16 v[132:135], a[124:127], a[12:15], v[132:135]\n" \
"v_exp_f32_e64 v159, v159\n" \
"v_mfma_f32_16x16x32_bf16 v[32:35], a[160:163], v[196:199], v[32:35]\n" \
"ds_read_b64_tr_b16 a[196:197], v215 offset:0\n" \
"v_readlane_b32 s62, %[tlo], 32\n" \
"v_lshrrev_b32_e64 v237, 7, v224\n" \
"v_mfma_f32_16x16x32_bf16 v[32:35], a[128:131], v[188:191], v[32:35]\n" \
"v_exp_f32_e64 v128, v128\n" \
"v_mfma_f32_16x16x32_bf16 v[36:39], a[164:167], v[196:199], v[36:39]\n" \
"v_cvt_pk_bf16_f32 v222, v156, v157\n" \
"v_cvt_pk_bf16_f32 v223, v158, v159\n" \
"v_mfma_f32_16x16x32_bf16 v[36:39], a[132:135], v[188:191], v[36:39]\n" \
"v_exp_f32_e64 v129, v129\n" \
"v_mfma_f32_16x16x32_bf16 v[40:43], a[168:171], v[196:199], v[40:43]\n" \
"ds_read_b64_tr_b16 a[198:199], v215 offset:2048\n" \
"v_and_b32_e64 v237, 48, v237\n" \
"v_mfma_f32_16x16x32_bf16 v[40:43], a[136:139], v[188:191], v[40:43]\n" \
"v_exp_f32_e64 v130, v130\n" \
"v_mfma_f32_16x16x32_bf16 v[44:47], a[172:175], v[196:199], v[44:47]\n" \
"ds_read_b64_tr_b16 a[200:201], v215 offset:4096\n" \
"v_mfma_f32_16x16x32_bf16 v[44:47], a[140:143], v[188:191], v[44:47]\n" \
"v_exp_f32_e64 v131, v131\n" \
"v_smfmac_f32_16x16x64_bf16 v[176:179], a[192:195], v[184:191], v228\n" \
"v_add_u32_e64 v237, s62, v237\n" \
"v_mfma_f32_16x16x32_bf16 v[136:139], a[96:99], a[16:19], v[164:167]\n" \
"v_exp_f32_e64 v132, v132\n" \
"v_mfma_f32_16x16x32_bf16 v[136:139], a[100:103], a[20:23], v[136:139]\n" \
"ds_read_b64_tr_b16 a[202:203], v215 offset:6144\n" \
"v_mfma_f32_16x16x32_bf16 v[136:139], a[104:107], a[24:27], v[136:139]\n" \
"v_exp_f32_e64 v133, v133\n" \
"v_mfma_f32_16x16x32_bf16 v[136:139], a[108:111], a[28:31], v[136:139]\n" \
"v_cvt_pk_bf16_f32 v192, v128, v129\n" \
"v_cvt_pk_bf16_f32 v193, v130, v131\n" \
"v_mfma_f32_16x16x32_bf16 v[140:143], a[112:115], a[16:19], v[164:167]\n" \
"v_exp_f32_e64 v134, v134\n" \
"v_mfma_f32_16x16x32_bf16 v[140:143], a[116:119], a[20:23], v[140:143]\n" \
"ds_read_b64_tr_b16 a[204:205], v215 offset:8192\n" \
"v_mfma_f32_16x16x32_bf16 v[140:143], a[120:123], a[24:27], v[140:143]\n" \
"buffer_load_dwordx4 v212, %[srdt], s49 offen offset:1024 lds\n" \
"v_mfma_f32_16x16x32_bf16 v[140:143], a[124:127], a[28:31], v[140:143]\n" \
"v_exp_f32_e64 v135, v135\n" \
"v_mfma_f32_16x16x32_bf16 v[48:51], a[176:179], v[196:199], v[48:51]\n" \
"ds_read_b64_tr_b16 a[206:207], v215 offset:10240\n" \
"v_add_u32_e32 v208, 0x1000, %[oq0]\n" \
"v_mfma_f32_16x16x32_bf16 v[48:51], a[144:147], v[188:191], v[48:51]\n" \
"v_exp_f32_e64 v136, v136\n" \
"v_mfma_f32_16x16x32_bf16 v[52:55], a[180:183], v[196:199], v[52:55]\n" \
"v_cvt_pk_bf16_f32 v194, v132, v133\n" \
"v_cvt_pk_bf16_f32 v195, v134, v135\n" \
"v_mfma_f32_16x16x32_bf16 v[52:55], a[148:151], v[188:191], v[52:55]\n" \
"v_exp_f32_e64 v137, v137\n" \
"v_mfma_f32_16x16x32_bf16 v[56:59], a[184:187], v[196:199], v[56:59]\n" \
"v_exp_f32_e64 v138, v138\n" \
"v_mfma_f32_16x16x32_bf16 v[56:59], a[152:155], v[188:191], v[56:59]\n" \
"ds_read_b64_tr_b16 a[208:209], v215 offset:12288\n" \
"v_add_u32_e32 v209, 0x2000, %[oq0]\n" \
"v_mfma_f32_16x16x32_bf16 v[60:63], a[188:191], v[196:199], v[60:63]\n" \
"v_exp_f32_e64 v139, v139\n" \
"v_mfma_f32_16x16x32_bf16 v[60:63], a[156:159], v[188:191], v[60:63]\n" \
"v_add_u32_e32 v210, 0x3000, %[oq0]\n" \
"v_mfma_f32_16x16x32_bf16 v[144:147], a[96:99], a[32:35], v[168:171]\n" \
"v_exp_f32_e64 v140, v140\n" \
"v_mfma_f32_16x16x32_bf16 v[144:147], a[100:103], a[36:39], v[144:147]\n" \
"ds_read_b64_tr_b16 a[210:211], v215 offset:14336\n" \
"v_mfma_f32_16x16x32_bf16 v[144:147], a[104:107], a[40:43], v[144:147]\n" \
"v_exp_f32_e64 v141, v141\n" \
"v_mfma_f32_16x16x32_bf16 v[144:147], a[108:111], a[44:47], v[144:147]\n" \
"v_cvt_pk_bf16_f32 v196, v136, v137\n" \
"v_cvt_pk_bf16_f32 v197, v138, v139\n" \
"v_mfma_f32_16x16x32_bf16 v[148:151], a[112:115], a[32:35], v[168:171]\n" \
"v_exp_f32_e64 v142, v142\n" \
"v_mfma_f32_16x16x32_bf16 v[148:151], a[116:119], a[36:39], v[148:151]\n" \
"v_mfma_f32_16x16x32_bf16 v[148:151], a[120:123], a[40:43], v[148:151]\n" \
"buffer_load_dwordx4 v213, %[srdt], s49 offen offset:2048 lds\n" \
"v_mfma_f32_16x16x32_bf16 v[148:151], a[124:127], a[44:47], v[148:151]\n" \
"v_exp_f32_e64 v143, v143\n" \
"v_mfma_f32_16x16x32_bf16 v[96:99], a[160:163], v[204:207], v[96:99]\n" \
"ds_read_b64_tr_b16 a[160:161], %[vvb] offset:57344\n" \
"v_mfma_f32_16x16x32_bf16 v[96:99], a[128:131], v[220:223], v[96:99]\n" \
"v_exp_f32_e64 v144, v144\n" \
"v_mfma_f32_16x16x32_bf16 v[100:103], a[164:167], v[204:207], v[100:103]\n" \
"v_cvt_pk_bf16_f32 v198, v140, v141\n" \
"v_cvt_pk_bf16_f32 v199, v142, v143\n" \
"v_mfma_f32_16x16x32_bf16 v[100:103], a[132:135], v[220:223], v[100:103]\n" \
"v_exp_f32_e64 v145, v145\n" \
"v_mfma_f32_16x16x32_bf16 v[104:107], a[168:171], v[204:207], v[104:107]\n" \
"ds_read_b64_tr_b16 a[162:163], %[vvb] offset:61440\n" \
"ds_read_b64_tr_b16 a[164:165], v211 offset:57344\n" \
"v_mfma_f32_16x16x32_bf16 v[104:107], a[136:139], v[220:223], v[104:107]\n" \
"v_exp_f32_e64 v146, v146\n" \
"v_mfma_f32_16x16x32_bf16 v[108:111], a[172:175], v[204:207], v[108:111]\n" \
"ds_read_b64_tr_b16 a[166:167], v211 offset:61440\n" \
"v_mfma_f32_16x16x32_bf16 v[108:111], a[140:143], v[220:223], v[108:111]\n" \
"v_exp_f32_e64 v147, v147\n" \
"v_smfmac_f32_16x16x64_bf16 v[180:183], a[192:195], v[216:223], v228\n" \
"ds_read_b64_tr_b16 a[168:169], %[vvb] offset:57856\n" \
"v_mfma_f32_16x16x32_bf16 v[152:155], a[96:99], a[48:51], v[172:175]\n" \
"v_exp_f32_e64 v148, v148\n" \
"v_mfma_f32_16x16x32_bf16 v[152:155], a[100:103], a[52:55], v[152:155]\n" \
"ds_read_b64_tr_b16 a[170:171], %[vvb] offset:61952\n" \
"v_mfma_f32_16x16x32_bf16 v[152:155], a[104:107], a[56:59], v[152:155]\n" \
"v_exp_f32_e64 v149, v149\n" \
"v_mfma_f32_16x16x32_bf16 v[152:155], a[108:111], a[60:63], v[152:155]\n" \
"v_cvt_pk_bf16_f32 v200, v144, v145\n" \
"v_cvt_pk_bf16_f32 v201, v146, v147\n" \
"v_mfma_f32_16x16x32_bf16 v[156:159], a[112:115], a[48:51], v[172:175]\n" \
"v_exp_f32_e64 v150, v150\n" \
"v_mfma_f32_16x16x32_bf16 v[156:159], a[116:119], a[52:55], v[156:159]\n" \
"ds_read_b64_tr_b16 a[172:173], v211 offset:57856\n" \
"v_mfma_f32_16x16x32_bf16 v[156:159], a[120:123], a[56:59], v[156:159]\n" \
"buffer_load_dwordx4 v214, %[srdt], s49 offen offset:3072 lds\n" \
"v_mfma_f32_16x16x32_bf16 v[156:159], a[124:127], a[60:63], v[156:159]\n" \
"v_exp_f32_e64 v151, v151\n" \
"v_mfma_f32_16x16x32_bf16 v[112:115], a[176:179], v[204:207], v[112:115]\n" \
"ds_read_b64_tr_b16 a[174:175], v211 offset:61952\n" \
"ds_read_b64_tr_b16 a[176:177], %[vvb] offset:58368\n" \
"v_mfma_f32_16x16x32_bf16 v[112:115], a[144:147], v[220:223], v[112:115]\n" \
"v_exp_f32_e64 v152, v152\n" \
"s_mov_b32 m0, s44\n" \
"s_addk_i32 s44, 0x400\n" \
"buffer_load_dwordx4 %[vg0], s[64:67], s45 offen offset:0 lds\n" \
"s_add_u32 s45, s45, 229376\n" \
"v_mfma_f32_16x16x32_bf16 v[116:119], a[180:183], v[204:207], v[116:119]\n" \
"v_cvt_pk_bf16_f32 v202, v148, v149\n" \
"v_cvt_pk_bf16_f32 v203, v150, v151\n" \
"v_mfma_f32_16x16x32_bf16 v[116:119], a[148:151], v[220:223], v[116:119]\n" \
"v_exp_f32_e64 v153, v153\n" \
"v_mfma_f32_16x16x32_bf16 v[120:123], a[184:187], v[204:207], v[120:123]\n" \
"v_exp_f32_e64 v154, v154\n" \
"v_mfma_f32_16x16x32_bf16 v[120:123], a[152:155], v[220:223], v[120:123]\n" \
"ds_read_b64_tr_b16 a[178:179], %[vvb] offset:62464\n" \
"s_waitcnt vmcnt(7)\n" \
"v_cmp_eq_u32_e64 s[56:57], 0, v229\n" \
"s_cmp_lg_u64 s[56:57], 0\n" \
"s_cbranch_scc1 Lspk_%=\n" \
"Lbkk_%=:\n" \
"v_mfma_f32_16x16x32_bf16 v[124:127], a[188:191], v[204:207], v[124:127]\n" \
"v_exp_f32_e32 v155, v155\n" \
"v_mfma_f32_16x16x32_bf16 v[124:127], a[156:159], v[220:223], v[124:127]\n" \
"ds_read_b64_tr_b16 a[180:181], v211 offset:58368\n" \
"ds_read_b64_tr_b16 a[182:183], v211 offset:62464\n" \
"ds_read_b64_tr_b16 a[184:185], %[vvb] offset:58880\n" \
"ds_read_b64_tr_b16 a[186:187], %[vvb] offset:62976\n" \
"ds_read_b64_tr_b16 a[188:189], v211 offset:58880\n" \
"ds_read_b64_tr_b16 a[190:191], v211 offset:62976\n" \
"s_waitcnt lgkmcnt(8)\n" \
"s_nop 0\n" \
"v_smfmac_f32_16x16x64_bf16 v[176:179], a[192:195], v[192:199], v228\n" \
"v_exp_f32_e64 v156, v156\n" \
"ds_read_b64_tr_b16 a[212:213], v227 offset:0\n" \
"v_mfma_f32_16x16x32_bf16 v[0:3], a[160:163], v[192:195], v[0:3]\n" \
"v_exp_f32_e64 v157, v157\n" \
"ds_read_b64_tr_b16 a[214:215], v227 offset:2048\n" \
"v_mfma_f32_16x16x32_bf16 v[4:7], a[164:167], v[192:195], v[4:7]\n" \
"v_cvt_pk_bf16_f32 v204, v152, v153\n" \
"ds_read_b64_tr_b16 a[216:217], v227 offset:4096\n" \
"v_mfma_f32_16x16x32_bf16 v[8:11], a[168:171], v[192:195], v[8:11]\n" \
"v_cvt_pk_bf16_f32 v205, v154, v155\n" \
"ds_read_b64_tr_b16 a[218:219], v227 offset:6144\n" \
"v_mfma_f32_16x16x32_bf16 v[12:15], a[172:175], v[192:195], v[12:15]\n" \
"v_exp_f32_e64 v158, v158\n" \
"ds_read_b64_tr_b16 a[220:221], v227 offset:8192\n" \
"s_waitcnt lgkmcnt(5)\n" \
"s_nop 0\n" \
"v_mfma_f32_16x16x32_bf16 v[16:19], a[176:179], v[192:195], v[16:19]\n" \
"v_exp_f32_e64 v159, v159\n" \
"ds_read_b64_tr_b16 a[222:223], v227 offset:10240\n" \
"v_mfma_f32_16x16x32_bf16 v[20:23], a[180:183], v[192:195], v[20:23]\n" \
"v_cvt_pk_bf16_f32 v206, v156, v157\n" \
"ds_read_b64_tr_b16 a[224:225], v227 offset:12288\n" \
"v_mfma_f32_16x16x32_bf16 v[24:27], a[184:187], v[192:195], v[24:27]\n" \
"v_cvt_pk_bf16_f32 v207, v158, v159\n" \
"ds_read_b64_tr_b16 a[226:227], v227 offset:14336\n" \
"v_mfma_f32_16x16x32_bf16 v[28:31], a[188:191], v[192:195], v[28:31]\n" \
"v_add_f32_e64 v176, v176, v177\n" \
"v_smfmac_f32_16x16x64_bf16 v[180:183], a[192:195], v[200:207], v228\n" \
"v_add_f32_e32 v178, v178, v179\n" \
"v_add_f32_e32 v231, v176, v178\n" \
"v_rcp_f32_e32 v176, v176\n" \
"v_rcp_f32_e32 v178, v178\n" \
"v_mfma_f32_16x16x32_bf16 v[32:35], a[160:163], v[196:199], v[32:35]\n" \
"v_pk_mul_f32 v[0:1], v[0:1], v[176:177] op_sel_hi:[1,0]\n" \
"v_pk_mul_f32 v[2:3], v[2:3], v[176:177] op_sel_hi:[1,0]\n" \
"v_pk_mul_f32 v[4:5], v[4:5], v[176:177] op_sel_hi:[1,0]\n" \
"v_pk_mul_f32 v[6:7], v[6:7], v[176:177] op_sel_hi:[1,0]\n" \
"v_cvt_pk_bf16_f32 v0, v0, v1\n" \
"v_cvt_pk_bf16_f32 v1, v2, v3\n" \
"v_cvt_pk_bf16_f32 v2, v4, v5\n" \
"v_mfma_f32_16x16x32_bf16 v[36:39], a[164:167], v[196:199], v[36:39]\n" \
"v_cvt_pk_bf16_f32 v3, v6, v7\n" \
"s_mov_b32 s55, 0x4e800000\n" \
"v_mov_b32_e32 v236, %[flag]\n" \
"v_add_f32_e32 v180, v180, v181\n" \
"v_add_f32_e32 v182, v182, v183\n" \
"v_add_f32_e32 v231, v231, v180\n" \
"v_add_f32_e64 v231, v231, v182\n" \
"v_mfma_f32_16x16x32_bf16 v[40:43], a[168:171], v[196:199], v[40:43]\n" \
"v_rcp_f32_e64 v180, v180\n" \
"v_cmp_nle_f32_e64 s[56:57], v231, s55\n" \
"v_rcp_f32_e32 v182, v182\n" \
"s_nop 3\n" \
"s_cmp_lg_u64 s[56:57], 0\n" \
"s_cselect_b32 s55, 1, 0\n" \
"v_mov_b32_e64 v230, s55\n" \
"v_mfma_f32_16x16x32_bf16 v[44:47], a[172:175], v[196:199], v[44:47]\n" \
"s_mov_b32 s69, s55\n" \
"s_andn2_b32 s55, s55, s52\n" \
"s_cselect_b64 vcc, 0, vcc\n" \
"s_nop 0\n" \
"ds_write_b32 v236, v230\n" \
"v_and_b32_e32 v236, -16, v236\n" \
"s_cbranch_vccz Lg37_%=\n" \
"buffer_store_dwordx4 v[0:3], %[oq0], %[srdz], s60 offen offset:0 sc1\n" \
"Lg37_%=:\n" \
"v_pk_mul_f32 v[8:9], v[8:9], v[176:177] op_sel_hi:[1,0]\n" \
"v_mfma_f32_16x16x32_bf16 v[48:51], a[176:179], v[196:199], v[48:51]\n" \
"v_pk_mul_f32 v[10:11], v[10:11], v[176:177] op_sel_hi:[1,0]\n" \
"v_pk_mul_f32 v[12:13], v[12:13], v[176:177] op_sel_hi:[1,0]\n" \
"v_pk_mul_f32 v[14:15], v[14:15], v[176:177] op_sel_hi:[1,0]\n" \
"v_cvt_pk_bf16_f32 v8, v8, v9\n" \
"v_cvt_pk_bf16_f32 v9, v10, v11\n" \
"v_cvt_pk_bf16_f32 v10, v12, v13\n" \
"v_cvt_pk_bf16_f32 v11, v14, v15\n" \
"v_mfma_f32_16x16x32_bf16 v[52:55], a[180:183], v[196:199], v[52:55]\n" \
"s_cbranch_vccz Lg38_%=\n" \
"s_nop 0\n" \
"buffer_store_dwordx4 v[8:11], %[oq0], %[srdz], s60 offen offset:1024 sc1\n" \
"Lg38_%=:\n" \
"v_pk_mul_f32 v[16:17], v[16:17], v[176:177] op_sel_hi:[1,0]\n" \
"v_pk_mul_f32 v[18:19], v[18:19], v[176:177] op_sel_hi:[1,0]\n" \
"v_pk_mul_f32 v[20:21], v[20:21], v[176:177] op_sel_hi:[1,0]\n" \
"v_pk_mul_f32 v[22:23], v[22:23], v[176:177] op_sel_hi:[1,0]\n" \
"v_cvt_pk_bf16_f32 v16, v16, v17\n" \
"v_cvt_pk_bf16_f32 v17, v18, v19\n" \
"v_mfma_f32_16x16x32_bf16 v[56:59], a[184:187], v[196:199], v[56:59]\n" \
"v_cvt_pk_bf16_f32 v18, v20, v21\n" \
"v_cvt_pk_bf16_f32 v19, v22, v23\n" \
"s_cbranch_vccz Lg39_%=\n" \
"s_nop 0\n" \
"buffer_store_dwordx4 v[16:19], %[oq0], %[srdz], s60 offen offset:2048 sc1\n" \
"Lg39_%=:\n" \
"v_pk_mul_f32 v[24:25], v[24:25], v[176:177] op_sel_hi:[1,0]\n" \
"v_pk_mul_f32 v[26:27], v[26:27], v[176:177] op_sel_hi:[1,0]\n" \
"v_pk_mul_f32 v[28:29], v[28:29], v[176:177] op_sel_hi:[1,0]\n" \
"v_pk_mul_f32 v[30:31], v[30:31], v[176:177] op_sel_hi:[1,0]\n" \
"v_mfma_f32_16x16x32_bf16 v[60:63], a[188:191], v[196:199], v[60:63]\n" \
"v_cvt_pk_bf16_f32 v24, v24, v25\n" \
"v_cvt_pk_bf16_f32 v25, v26, v27\n" \
"v_cvt_pk_bf16_f32 v26, v28, v29\n" \
"v_cvt_pk_bf16_f32 v27, v30, v31\n" \
"s_cbranch_vccz Lg40_%=\n" \
"s_nop 0\n" \
"buffer_store_dwordx4 v[24:27], %[oq0], %[srdz], s60 offen offset:3072 sc1\n" \
"Lg40_%=:\n" \
"v_mfma_f32_16x16x32_bf16 v[64:67], a[128:131], v[216:219], v[64:67]\n" \
"v_pk_mul_f32 v[32:33], v[32:33], v[178:179] op_sel_hi:[1,0]\n" \
"v_pk_mul_f32 v[34:35], v[34:35], v[178:179] op_sel_hi:[1,0]\n" \
"v_pk_mul_f32 v[36:37], v[36:37], v[178:179] op_sel_hi:[1,0]\n" \
"v_mfma_f32_16x16x32_bf16 v[64:67], a[160:163], v[200:203], v[64:67]\n" \
"v_pk_mul_f32 v[38:39], v[38:39], v[178:179] op_sel_hi:[1,0]\n" \
"v_cvt_pk_bf16_f32 v32, v32, v33\n" \
"v_cvt_pk_bf16_f32 v33, v34, v35\n" \
"v_mfma_f32_16x16x32_bf16 v[68:71], a[132:135], v[216:219], v[68:71]\n" \
"v_cvt_pk_bf16_f32 v34, v36, v37\n" \
"v_cvt_pk_bf16_f32 v35, v38, v39\n" \
"s_cbranch_vccz Lg41_%=\n" \
"s_nop 0\n" \
"buffer_store_dwordx4 v[32:35], v208, %[srdz], s60 offen offset:0 sc1\n" \
"Lg41_%=:\n" \
"v_mfma_f32_16x16x32_bf16 v[68:71], a[164:167], v[200:203], v[68:71]\n" \
"v_pk_mul_f32 v[40:41], v[40:41], v[178:179] op_sel_hi:[1,0]\n" \
"v_pk_mul_f32 v[42:43], v[42:43], v[178:179] op_sel_hi:[1,0]\n" \
"v_pk_mul_f32 v[44:45], v[44:45], v[178:179] op_sel_hi:[1,0]\n" \
"v_mfma_f32_16x16x32_bf16 v[72:75], a[136:139], v[216:219], v[72:75]\n" \
"v_pk_mul_f32 v[46:47], v[46:47], v[178:179] op_sel_hi:[1,0]\n" \
"v_cvt_pk_bf16_f32 v40, v40, v41\n" \
"v_cvt_pk_bf16_f32 v41, v42, v43\n" \
"v_mfma_f32_16x16x32_bf16 v[72:75], a[168:171], v[200:203], v[72:75]\n" \
"v_cvt_pk_bf16_f32 v42, v44, v45\n" \
"v_cvt_pk_bf16_f32 v43, v46, v47\n" \
".if . - adaln_body != 40960\n" \
".error \"size model\"\n" \
".endif\n" \
"Lk9_%=:\n" \
"s_cbranch_execz Lk10_%=\n" \
"s_nop 0\n" \
"s_cbranch_vccz Lg42_%=\n" \
"s_nop 0\n" \
"buffer_store_dwordx4 v[40:43], v208, %[srdz], s60 offen offset:1024 sc1\n" \
"Lg42_%=:\n" \
"v_mfma_f32_16x16x32_bf16 v[76:79], a[140:143], v[216:219], v[76:79]\n" \
"v_pk_mul_f32 v[48:49], v[48:49], v[178:179] op_sel_hi:[1,0]\n" \
"v_pk_mul_f32 v[50:51], v[50:51], v[178:179] op_sel_hi:[1,0]\n" \
"v_pk_mul_f32 v[52:53], v[52:53], v[178:179] op_sel_hi:[1,0]\n" \
"v_mfma_f32_16x16x32_bf16 v[76:79], a[172:175], v[200:203], v[76:79]\n" \
"v_pk_mul_f32 v[54:55], v[54:55], v[178:179] op_sel_hi:[1,0]\n" \
"v_cvt_pk_bf16_f32 v48, v48, v49\n" \
"v_cvt_pk_bf16_f32 v49, v50, v51\n" \
"v_mfma_f32_16x16x32_bf16 v[80:83], a[144:147], v[216:219], v[80:83]\n" \
"v_cvt_pk_bf16_f32 v50, v52, v53\n" \
"v_cvt_pk_bf16_f32 v51, v54, v55\n" \
"s_cbranch_vccz Lg43_%=\n" \
"s_nop 0\n" \
"buffer_store_dwordx4 v[48:51], v208, %[srdz], s60 offen offset:2048 sc1\n" \
"Lg43_%=:\n" \
"v_mfma_f32_16x16x32_bf16 v[80:83], a[176:179], v[200:203], v[80:83]\n" \
"v_pk_mul_f32 v[56:57], v[56:57], v[178:179] op_sel_hi:[1,0]\n" \
"v_pk_mul_f32 v[58:59], v[58:59], v[178:179] op_sel_hi:[1,0]\n" \
"v_pk_mul_f32 v[60:61], v[60:61], v[178:179] op_sel_hi:[1,0]\n" \
"v_mfma_f32_16x16x32_bf16 v[84:87], a[148:151], v[216:219], v[84:87]\n" \
"v_pk_mul_f32 v[62:63], v[62:63], v[178:179] op_sel_hi:[1,0]\n" \
"v_cvt_pk_bf16_f32 v56, v56, v57\n" \
"v_cvt_pk_bf16_f32 v57, v58, v59\n" \
"v_mfma_f32_16x16x32_bf16 v[84:87], a[180:183], v[200:203], v[84:87]\n" \
"v_cvt_pk_bf16_f32 v58, v60, v61\n" \
"v_cvt_pk_bf16_f32 v59, v62, v63\n" \
"s_cbranch_vccz Lg44_%=\n" \
"s_nop 0\n" \
"buffer_store_dwordx4 v[56:59], v208, %[srdz], s60 offen offset:3072 sc1\n" \
"Lg44_%=:\n" \
"v_mfma_f32_16x16x32_bf16 v[88:91], a[152:155], v[216:219], v[88:91]\n" \
"v_mfma_f32_16x16x32_bf16 v[88:91], a[184:187], v[200:203], v[88:91]\n" \
"v_mfma_f32_16x16x32_bf16 v[92:95], a[156:159], v[216:219], v[92:95]\n" \
"v_mfma_f32_16x16x32_bf16 v[92:95], a[188:191], v[200:203], v[92:95]\n" \
"s_waitcnt vmcnt(14) lgkmcnt(0)\n" \
"s_bitcmp1_b32 0xf, s48\n" \
"s_cbranch_scc1 Lcw1_%=\n" \
"s_waitcnt vmcnt(6)\n" \
"Lcw1_%=:\n" \
"s_barrier\n" \
"s_nop 0\n" \
"ds_read_b128 v[232:235], v236\n" \
"v_mfma_f32_16x16x32_bf16 v[96:99], a[160:163], v[204:207], v[96:99]\n" \
"v_pk_mul_f32 v[64:65], v[64:65], v[180:181] op_sel_hi:[1,0]\n" \
"v_pk_mul_f32 v[66:67], v[66:67], v[180:181] op_sel_hi:[1,0]\n" \
"v_pk_mul_f32 v[68:69], v[68:69], v[180:181] op_sel_hi:[1,0]\n" \
"v_pk_mul_f32 v[70:71], v[70:71], v[180:181] op_sel_hi:[1,0]\n" \
"v_cvt_pk_bf16_f32 v64, v64, v65\n" \
"v_mfma_f32_16x16x32_bf16 v[100:103], a[164:167], v[204:207], v[100:103]\n" \
"v_cvt_pk_bf16_f32 v65, v66, v67\n" \
"v_cvt_pk_bf16_f32 v66, v68, v69\n" \
"v_cvt_pk_bf16_f32 v67, v70, v71\n" \
"s_cbranch_vccz Lg45_%=\n" \
"s_nop 0\n" \
"buffer_store_dwordx4 v[64:67], v209, %[srdz], s60 offen offset:0 sc1\n" \
"Lg45_%=:\n" \
"v_pk_mul_f32 v[72:73], v[72:73], v[180:181] op_sel_hi:[1,0]\n" \
"v_mfma_f32_16x16x32_bf16 v[104:107], a[168:171], v[204:207], v[104:107]\n" \
"v_pk_mul_f32 v[74:75], v[74:75], v[180:181] op_sel_hi:[1,0]\n" \
"v_pk_mul_f32 v[76:77], v[76:77], v[180:181] op_sel_hi:[1,0]\n" \
"v_pk_mul_f32 v[78:79], v[78:79], v[180:181] op_sel_hi:[1,0]\n" \
"v_cvt_pk_bf16_f32 v72, v72, v73\n" \
"v_cvt_pk_bf16_f32 v73, v74, v75\n" \
"v_mfma_f32_16x16x32_bf16 v[108:111], a[172:175], v[204:207], v[108:111]\n" \
"v_cvt_pk_bf16_f32 v74, v76, v77\n" \
"v_cvt_pk_bf16_f32 v75, v78, v79\n" \
"s_cbranch_vccz Lg46_%=\n" \
"s_nop 0\n" \
"buffer_store_dwordx4 v[72:75], v209, %[srdz], s60 offen offset:1024 sc1\n" \
"Lg46_%=:\n" \
"v_pk_mul_f32 v[80:81], v[80:81], v[180:181] op_sel_hi:[1,0]\n" \
"v_pk_mul_f32 v[82:83], v[82:83], v[180:181] op_sel_hi:[1,0]\n" \
"v_mfma_f32_16x16x32_bf16 v[112:115], a[176:179], v[204:207], v[112:115]\n" \
"v_pk_mul_f32 v[84:85], v[84:85], v[180:181] op_sel_hi:[1,0]\n" \
"v_pk_mul_f32 v[86:87], v[86:87], v[180:181] op_sel_hi:[1,0]\n" \
"v_cvt_pk_bf16_f32 v80, v80, v81\n" \
"v_cvt_pk_bf16_f32 v81, v82, v83\n" \
"v_cvt_pk_bf16_f32 v82, v84, v85\n" \
"v_mfma_f32_16x16x32_bf16 v[116:119], a[180:183], v[204:207], v[116:119]\n" \
"v_cvt_pk_bf16_f32 v83, v86, v87\n" \
"s_cbranch_vccz Lg47_%=\n" \
"s_nop 0\n" \
"buffer_store_dwordx4 v[80:83], v209, %[srdz], s60 offen offset:2048 sc1\n" \
"Lg47_%=:\n" \
"v_pk_mul_f32 v[88:89], v[88:89], v[180:181] op_sel_hi:[1,0]\n" \
"v_pk_mul_f32 v[90:91], v[90:91], v[180:181] op_sel_hi:[1,0]\n" \
"v_pk_mul_f32 v[92:93], v[92:93], v[180:181] op_sel_hi:[1,0]\n" \
"v_mfma_f32_16x16x32_bf16 v[120:123], a[184:187], v[204:207], v[120:123]\n" \
"v_pk_mul_f32 v[94:95], v[94:95], v[180:181] op_sel_hi:[1,0]\n" \
"v_cvt_pk_bf16_f32 v88, v88, v89\n" \
"v_cvt_pk_bf16_f32 v89, v90, v91\n" \
"v_cvt_pk_bf16_f32 v90, v92, v93\n" \
"v_cvt_pk_bf16_f32 v91, v94, v95\n" \
"v_mfma_f32_16x16x32_bf16 v[124:127], a[188:191], v[204:207], v[124:127]\n" \
"s_cbranch_vccz Lg48_%=\n" \
"s_nop 0\n" \
"buffer_store_dwordx4 v[88:91], v209, %[srdz], s60 offen offset:3072 sc1\n" \
"Lg48_%=:\n" \
"s_waitcnt lgkmcnt(0)\n" \
"s_nop 0\n" \
"v_or3_b32 v232, v232, v233, v234\n" \
"v_or_b32_e32 v232, v232, v235\n" \
"s_nop 0\n" \
"v_readfirstlane_b32 s55, v232\n" \
"s_nop 3\n" \
"s_andn2_b32 s55, s55, s52\n" \
"s_cbranch_scc1 Lslow_%=\n" \
"v_mfma_f32_16x16x32_bf16 v[192:195], a[196:199], v[0:3], 0\n" \
"v_mfma_f32_16x16x32_bf16 v[192:195], a[200:203], v[8:11], v[192:195]\n" \
"v_pk_mul_f32 v[96:97], v[96:97], v[182:183] op_sel_hi:[1,0]\n" \
"v_pk_mul_f32 v[98:99], v[98:99], v[182:183] op_sel_hi:[1,0]\n" \
"v_mfma_f32_16x16x32_bf16 v[192:195], a[204:207], v[16:19], v[192:195]\n" \
"v_pk_mul_f32 v[100:101], v[100:101], v[182:183] op_sel_hi:[1,0]\n" \
"v_pk_mul_f32 v[102:103], v[102:103], v[182:183] op_sel_hi:[1,0]\n" \
"s_add_u32 m0, %[m0b], 114688\n" \
"s_cmp_gt_u32 s48, 1\n" \
"s_cselect_b32 s49, 1, 2\n" \
"s_lshl_b32 s49, s49, 18\n" \
"s_or_b32 s49, s49, s58\n" \
"buffer_load_dwordx4 %[wo0], %[srdt], s49 offen offset:0 lds\n" \
"v_mfma_f32_16x16x32_bf16 v[192:195], a[208:211], v[24:27], v[192:195]\n" \
"v_cvt_pk_bf16_f32 v96, v96, v97\n" \
"v_cvt_pk_bf16_f32 v97, v98, v99\n" \
"v_mfma_f32_16x16x32_bf16 v[144:147], a[212:215], v[0:3], 0\n" \
"v_cvt_pk_bf16_f32 v98, v100, v101\n" \
"v_mfma_f32_16x16x32_bf16 v[144:147], a[216:219], v[8:11], v[144:147]\n" \
"v_cvt_pk_bf16_f32 v99, v102, v103\n" \
"s_cbranch_vccz Lg1_%=\n" \
"s_nop 0\n" \
"buffer_store_dwordx4 v[96:99], v210, %[srdz], s60 offen offset:0 sc1\n" \
"Lg1_%=:\n" \
"v_mfma_f32_16x16x32_bf16 v[144:147], a[220:223], v[16:19], v[144:147]\n" \
"ds_read_b64_tr_b16 a[228:229], v215 offset:512\n" \
"v_pk_mul_f32 v[104:105], v[104:105], v[182:183] op_sel_hi:[1,0]\n" \
"v_pk_mul_f32 v[106:107], v[106:107], v[182:183] op_sel_hi:[1,0]\n" \
"v_mfma_f32_16x16x32_bf16 v[144:147], a[224:227], v[24:27], v[144:147]\n" \
"ds_read_b64_tr_b16 a[230:231], v215 offset:2560\n" \
"v_pk_mul_f32 v[108:109], v[108:109], v[182:183] op_sel_hi:[1,0]\n" \
"v_mfma_f32_16x16x32_bf16 v[196:199], a[196:199], v[32:35], 0\n" \
"ds_read_b64_tr_b16 a[232:233], v215 offset:4608\n" \
"v_pk_mul_f32 v[110:111], v[110:111], v[182:183] op_sel_hi:[1,0]\n" \
"v_cvt_pk_bf16_f32 v104, v104, v105\n" \
"v_mfma_f32_16x16x32_bf16 v[196:199], a[200:203], v[40:43], v[196:199]\n" \
"ds_read_b64_tr_b16 a[234:235], v215 offset:6656\n" \
"v_cvt_pk_bf16_f32 v105, v106, v107\n" \
"v_cvt_pk_bf16_f32 v106, v108, v109\n" \
"v_mfma_f32_16x16x32_bf16 v[196:199], a[204:207], v[48:51], v[196:199]\n" \
"v_cvt_pk_bf16_f32 v107, v110, v111\n" \
"s_cbranch_vccz Lg2_%=\n" \
"s_nop 0\n" \
"buffer_store_dwordx4 v[104:107], v210, %[srdz], s60 offen offset:1024 sc1\n" \
"Lg2_%=:\n" \
"v_mfma_f32_16x16x32_bf16 v[196:199], a[208:211], v[56:59], v[196:199]\n" \
"v_pk_mul_f32 v[112:113], v[112:113], v[182:183] op_sel_hi:[1,0]\n" \
"v_mfma_f32_16x16x32_bf16 v[148:151], a[212:215], v[32:35], 0\n" \
"v_pk_mul_f32 v[114:115], v[114:115], v[182:183] op_sel_hi:[1,0]\n" \
"v_pk_mul_f32 v[116:117], v[116:117], v[182:183] op_sel_hi:[1,0]\n" \
"buffer_load_dwordx4 v212, %[srdt], s49 offen offset:1024 lds\n" \
"v_mfma_f32_16x16x32_bf16 v[148:151], a[216:219], v[40:43], v[148:151]\n" \
"v_pk_mul_f32 v[118:119], v[118:119], v[182:183] op_sel_hi:[1,0]\n" \
"v_cvt_pk_bf16_f32 v112, v112, v113\n" \
"v_mfma_f32_16x16x32_bf16 v[148:151], a[220:223], v[48:51], v[148:151]\n" \
"ds_read_b64_tr_b16 a[236:237], v215 offset:8704\n" \
"v_cvt_pk_bf16_f32 v113, v114, v115\n" \
"v_mfma_f32_16x16x32_bf16 v[148:151], a[224:227], v[56:59], v[148:151]\n" \
"v_cvt_pk_bf16_f32 v114, v116, v117\n" \
"v_cvt_pk_bf16_f32 v115, v118, v119\n" \
"v_mfma_f32_16x16x32_bf16 v[200:203], a[196:199], v[64:67], 0\n" \
"ds_read_b64_tr_b16 a[238:239], v215 offset:10752\n" \
"s_cbranch_vccz Lg3_%=\n" \
"s_nop 0\n" \
"buffer_store_dwordx4 v[112:115], v210, %[srdz], s60 offen offset:2048 sc1\n" \
"Lg3_%=:\n" \
"v_pk_mul_f32 v[120:121], v[120:121], v[182:183] op_sel_hi:[1,0]\n" \
"v_mfma_f32_16x16x32_bf16 v[200:203], a[200:203], v[72:75], v[200:203]\n" \
"v_pk_mul_f32 v[122:123], v[122:123], v[182:183] op_sel_hi:[1,0]\n" \
"v_pk_mul_f32 v[124:125], v[124:125], v[182:183] op_sel_hi:[1,0]\n" \
"v_mfma_f32_16x16x32_bf16 v[200:203], a[204:207], v[80:83], v[200:203]\n" \
"v_pk_mul_f32 v[126:127], v[126:127], v[182:183] op_sel_hi:[1,0]\n" \
"v_mfma_f32_16x16x32_bf16 v[200:203], a[208:211], v[88:91], v[200:203]\n" \
"v_cvt_pk_bf16_f32 v120, v120, v121\n" \
"v_cvt_pk_bf16_f32 v121, v122, v123\n" \
"v_mfma_f32_16x16x32_bf16 v[152:155], a[212:215], v[64:67], 0\n" \
"v_cvt_pk_bf16_f32 v122, v124, v125\n" \
"v_cvt_pk_bf16_f32 v123, v126, v127\n" \
"v_mfma_f32_16x16x32_bf16 v[152:155], a[216:219], v[72:75], v[152:155]\n" \
"s_cbranch_vccz Lg4_%=\n" \
"s_nop 0\n" \
"buffer_store_dwordx4 v[120:123], v210, %[srdz], s60 offen offset:3072 sc1\n" \
"Lg4_%=:\n" \
"v_mfma_f32_16x16x32_bf16 v[152:155], a[220:223], v[80:83], v[152:155]\n" \
"v_add_u32_e32 v220, 0x10000, v215\n" \
"v_mfma_f32_16x16x32_bf16 v[152:155], a[224:227], v[88:91], v[152:155]\n" \
"v_readlane_b32 s62, %[tlo], 19\n" \
"buffer_load_dwordx4 v213, %[srdt], s49 offen offset:2048 lds\n" \
"v_mfma_f32_16x16x32_bf16 v[204:207], a[196:199], v[96:99], 0\n" \
"v_add_u32_e32 v221, 0x10000, v227\n" \
"v_mfma_f32_16x16x32_bf16 v[204:207], a[200:203], v[104:107], v[204:207]\n" \
"ds_read_b64_tr_b16 a[196:197], v215 offset:12800\n" \
"v_mfma_f32_16x16x32_bf16 v[204:207], a[204:207], v[112:115], v[204:207]\n" \
"ds_read_b64_tr_b16 a[198:199], v215 offset:14848\n" \
"v_mfma_f32_16x16x32_bf16 v[204:207], a[208:211], v[120:123], v[204:207]\n" \
"ds_read_b64_tr_b16 a[200:201], v227 offset:512\n" \
"v_mfma_f32_16x16x32_bf16 v[156:159], a[212:215], v[96:99], 0\n" \
"ds_read_b64_tr_b16 a[202:203], v227 offset:2560\n" \
"v_mfma_f32_16x16x32_bf16 v[156:159], a[216:219], v[104:107], v[156:159]\n" \
"ds_read_b64_tr_b16 a[204:205], v227 offset:4608\n" \
"v_mfma_f32_16x16x32_bf16 v[156:159], a[220:223], v[112:115], v[156:159]\n" \
"ds_read_b64_tr_b16 a[206:207], v227 offset:6656\n" \
"v_mfma_f32_16x16x32_bf16 v[156:159], a[224:227], v[120:123], v[156:159]\n" \
"s_waitcnt vmcnt(63) lgkmcnt(4)\n" \
"s_nop 0\n" \
"v_mfma_f32_16x16x32_bf16 v[160:163], a[228:231], v[0:3], 0\n" \
"ds_read_b64_tr_b16 a[208:209], v227 offset:8704\n" \
"v_mfma_f32_16x16x32_bf16 v[160:163], a[232:235], v[8:11], v[160:163]\n" \
"ds_read_b64_tr_b16 a[210:211], v227 offset:10752\n" \
"s_mov_b32 s40, 606208\n" \
"buffer_load_dwordx4 a[176:179], v237, %[srdz], s40 offen sc1\n" \
"v_mfma_f32_16x16x32_bf16 v[164:167], a[228:231], v[32:35], 0\n" \
"ds_read_b64_tr_b16 a[212:213], v227 offset:12800\n" \
"v_mfma_f32_16x16x32_bf16 v[164:167], a[232:235], v[40:43], v[164:167]\n" \
"ds_read_b64_tr_b16 a[214:215], v227 offset:14848\n" \
"buffer_load_dwordx4 v214, %[srdt], s49 offen offset:3072 lds\n" \
"v_mfma_f32_16x16x32_bf16 v[168:171], a[228:231], v[64:67], 0\n" \
"v_mfma_f32_16x16x32_bf16 v[168:171], a[232:235], v[72:75], v[168:171]\n" \
"v_mfma_f32_16x16x32_bf16 v[172:175], a[228:231], v[96:99], 0\n" \
"ds_read_b64_tr_b16 a[216:217], v215 offset:16384\n" \
"v_mfma_f32_16x16x32_bf16 v[172:175], a[232:235], v[104:107], v[172:175]\n" \
"ds_read_b64_tr_b16 a[218:219], v215 offset:18432\n" \
"v_mfma_f32_16x16x32_bf16 v[160:163], a[236:239], v[16:19], v[160:163]\n" \
"ds_read_b64_tr_b16 a[220:221], v215 offset:20480\n" \
"v_mfma_f32_16x16x32_bf16 v[160:163], a[196:199], v[24:27], v[160:163]\n" \
"ds_read_b64_tr_b16 a[222:223], v215 offset:22528\n" \
"s_mov_b32 s40, 610304\n" \
"buffer_load_dwordx4 a[180:183], v237, %[srdz], s40 offen sc1\n" \
"v_mfma_f32_16x16x32_bf16 v[164:167], a[236:239], v[48:51], v[164:167]\n" \
"v_mfma_f32_16x16x32_bf16 v[164:167], a[196:199], v[56:59], v[164:167]\n" \
"v_mfma_f32_16x16x32_bf16 v[168:171], a[236:239], v[80:83], v[168:171]\n" \
"s_waitcnt vmcnt(63) lgkmcnt(0)\n" \
"s_barrier\n" \
"v_mfma_f32_16x16x32_bf16 v[168:171], a[196:199], v[88:91], v[168:171]\n" \
"v_mfma_f32_16x16x32_bf16 v[172:175], a[236:239], v[112:115], v[172:175]\n" \
"ds_read_b64_tr_b16 a[224:225], v215 offset:24576\n" \
"v_mfma_f32_16x16x32_bf16 v[172:175], a[196:199], v[120:123], v[172:175]\n" \
"s_waitcnt vmcnt(63) lgkmcnt(5)\n" \
"s_nop 0\n" \
"v_mfma_f32_16x16x32_bf16 v[176:179], a[200:203], v[0:3], 0\n" \
"ds_read_b64_tr_b16 a[226:227], v215 offset:26624\n" \
"v_mfma_f32_16x16x32_bf16 v[176:179], a[204:207], v[8:11], v[176:179]\n" \
"ds_read_b64_tr_b16 a[228:229], v215 offset:28672\n" \
"s_mov_b32 s40, 614400\n" \
"buffer_load_dwordx4 a[184:187], v237, %[srdz], s40 offen sc1\n" \
"v_mfma_f32_16x16x32_bf16 v[180:183], a[200:203], v[32:35], 0\n" \
"ds_read_b64_tr_b16 a[230:231], v215 offset:30720\n" \
"v_mfma_f32_16x16x32_bf16 v[180:183], a[204:207], v[40:43], v[180:183]\n" \
"s_add_u32 m0, %[m0b], 49152\n" \
"s_cmp_gt_u32 s48, 1\n" \
"s_cselect_b32 s49, 1, 2\n" \
"s_lshl_b32 s49, s49, 18\n" \
"s_or_b32 s49, s49, s59\n" \
"buffer_load_dwordx4 %[wo0], %[srdt], s49 offen offset:0 lds\n" \
"v_mfma_f32_16x16x32_bf16 v[184:187], a[200:203], v[64:67], 0\n" \
"v_mfma_f32_16x16x32_bf16 v[184:187], a[204:207], v[72:75], v[184:187]\n" \
"buffer_load_dwordx4 v212, %[srdt], s49 offen offset:1024 lds\n" \
"v_mfma_f32_16x16x32_bf16 v[188:191], a[200:203], v[96:99], 0\n" \
"ds_read_b64_tr_b16 a[232:233], v227 offset:16384\n" \
"v_mfma_f32_16x16x32_bf16 v[188:191], a[204:207], v[104:107], v[188:191]\n" \
"ds_read_b64_tr_b16 a[234:235], v227 offset:18432\n" \
"v_mfma_f32_16x16x32_bf16 v[176:179], a[208:211], v[16:19], v[176:179]\n" \
"ds_read_b64_tr_b16 a[236:237], v227 offset:20480\n" \
"v_mfma_f32_16x16x32_bf16 v[176:179], a[212:215], v[24:27], v[176:179]\n" \
"ds_read_b64_tr_b16 a[238:239], v227 offset:22528\n" \
"s_mov_b32 s40, 618496\n" \
"buffer_load_dwordx4 a[188:191], v237, %[srdz], s40 offen sc1\n" \
"v_mfma_f32_16x16x32_bf16 v[180:183], a[208:211], v[48:51], v[180:183]\n" \
"v_mfma_f32_16x16x32_bf16 v[180:183], a[212:215], v[56:59], v[180:183]\n" \
"v_mfma_f32_16x16x32_bf16 v[184:187], a[208:211], v[80:83], v[184:187]\n" \
"v_mfma_f32_16x16x32_bf16 v[184:187], a[212:215], v[88:91], v[184:187]\n" \
"buffer_load_dwordx4 v213, %[srdt], s49 offen offset:2048 lds\n" \
"v_mfma_f32_16x16x32_bf16 v[188:191], a[208:211], v[112:115], v[188:191]\n" \
"ds_read_b64_tr_b16 a[196:197], v227 offset:24576\n" \
"v_mfma_f32_16x16x32_bf16 v[188:191], a[212:215], v[120:123], v[188:191]\n" \
"s_waitcnt vmcnt(63) lgkmcnt(5)\n" \
"s_nop 0\n" \
"v_mfma_f32_16x16x32_bf16 v[4:7], a[216:219], v[0:3], 0\n" \
"ds_read_b64_tr_b16 a[198:199], v227 offset:26624\n" \
"v_mfma_f32_16x16x32_bf16 v[4:7], a[220:223], v[8:11], v[4:7]\n" \
"ds_read_b64_tr_b16 a[200:201], v227 offset:28672\n" \
"v_mfma_f32_16x16x32_bf16 v[36:39], a[216:219], v[32:35], 0\n" \
"ds_read_b64_tr_b16 a[202:203], v227 offset:30720\n" \
"v_mfma_f32_16x16x32_bf16 v[36:39], a[220:223], v[40:43], v[36:39]\n" \
"v_mfma_f32_16x16x32_bf16 v[68:71], a[216:219], v[64:67], 0\n" \
"buffer_load_dwordx4 v214, %[srdt], s49 offen offset:3072 lds\n" \
"v_mfma_f32_16x16x32_bf16 v[68:71], a[220:223], v[72:75], v[68:71]\n" \
"v_mfma_f32_16x16x32_bf16 v[100:103], a[216:219], v[96:99], 0\n" \
"ds_read_b64_tr_b16 a[204:205], v215 offset:16896\n" \
"v_mfma_f32_16x16x32_bf16 v[100:103], a[220:223], v[104:107], v[100:103]\n" \
"ds_read_b64_tr_b16 a[206:207], v215 offset:18944\n" \
"v_mfma_f32_16x16x32_bf16 v[4:7], a[224:227], v[16:19], v[4:7]\n" \
"ds_read_b64_tr_b16 a[208:209], v215 offset:20992\n" \
"v_mfma_f32_16x16x32_bf16 v[4:7], a[228:231], v[24:27], v[4:7]\n" \
"ds_read_b64_tr_b16 a[210:211], v215 offset:23040\n" \
"v_mfma_f32_16x16x32_bf16 v[36:39], a[224:227], v[48:51], v[36:39]\n" \
"v_mfma_f32_16x16x32_bf16 v[36:39], a[228:231], v[56:59], v[36:39]\n" \
"v_mfma_f32_16x16x32_bf16 v[68:71], a[224:227], v[80:83], v[68:71]\n" \
"v_mfma_f32_16x16x32_bf16 v[68:71], a[228:231], v[88:91], v[68:71]\n" \
"v_mfma_f32_16x16x32_bf16 v[100:103], a[224:227], v[112:115], v[100:103]\n" \
"ds_read_b64_tr_b16 a[212:213], v215 offset:25088\n" \
"v_mfma_f32_16x16x32_bf16 v[100:103], a[228:231], v[120:123], v[100:103]\n" \
"s_waitcnt vmcnt(63) lgkmcnt(5)\n" \
"s_nop 0\n" \
"v_mfma_f32_16x16x32_bf16 v[12:15], a[232:235], v[0:3], 0\n" \
"ds_read_b64_tr_b16 a[214:215], v215 offset:27136\n" \
"s_cmp_gt_u32 s48, 0\n" \
"s_cselect_b32 s50, 0, 1\n" \
"s_mul_i32 s50, s50, 152064\n" \
"buffer_load_dwordx4 a[64:67], %[oq0], %[srdz], s50 offen offset:0 sc1\n" \
"v_mfma_f32_16x16x32_bf16 v[12:15], a[236:239], v[8:11], v[12:15]\n" \
"ds_read_b64_tr_b16 a[216:217], v215 offset:29184\n" \
"v_mfma_f32_16x16x32_bf16 v[44:47], a[232:235], v[32:35], 0\n" \
"ds_read_b64_tr_b16 a[218:219], v215 offset:31232\n" \
"buffer_load_dwordx4 a[68:71], %[oq0], %[srdz], s50 offen offset:1024 sc1\n" \
"v_mfma_f32_16x16x32_bf16 v[44:47], a[236:239], v[40:43], v[44:47]\n" \
"v_mfma_f32_16x16x32_bf16 v[76:79], a[232:235], v[64:67], 0\n" \
"buffer_load_dwordx4 a[80:83], v208, %[srdz], s50 offen offset:0 sc1\n" \
"v_mfma_f32_16x16x32_bf16 v[76:79], a[236:239], v[72:75], v[76:79]\n" \
"v_mfma_f32_16x16x32_bf16 v[108:111], a[232:235], v[96:99], 0\n" \
"ds_read_b64_tr_b16 a[220:221], v227 offset:16896\n" \
"buffer_load_dwordx4 a[84:87], v208, %[srdz], s50 offen offset:1024 sc1\n" \
"v_mfma_f32_16x16x32_bf16 v[108:111], a[236:239], v[104:107], v[108:111]\n" \
"ds_read_b64_tr_b16 a[222:223], v227 offset:18944\n" \
"v_mfma_f32_16x16x32_bf16 v[12:15], a[196:199], v[16:19], v[12:15]\n" \
"ds_read_b64_tr_b16 a[224:225], v227 offset:25088\n" \
"buffer_load_dwordx4 a[96:99], v209, %[srdz], s50 offen offset:0 sc1\n" \
"v_mfma_f32_16x16x32_bf16 v[12:15], a[200:203], v[24:27], v[12:15]\n" \
"ds_read_b64_tr_b16 a[226:227], v227 offset:27136\n" \
"v_mfma_f32_16x16x32_bf16 v[44:47], a[196:199], v[48:51], v[44:47]\n" \
"ds_read_b64_tr_b16 a[228:229], v227 offset:20992\n" \
"buffer_load_dwordx4 a[100:103], v209, %[srdz], s50 offen offset:1024 sc1\n" \
"v_mfma_f32_16x16x32_bf16 v[44:47], a[200:203], v[56:59], v[44:47]\n" \
"ds_read_b64_tr_b16 a[230:231], v227 offset:23040\n" \
"v_mfma_f32_16x16x32_bf16 v[76:79], a[196:199], v[80:83], v[76:79]\n" \
"ds_read_b64_tr_b16 a[232:233], v227 offset:29184\n" \
"buffer_load_dwordx4 a[112:115], v210, %[srdz], s50 offen offset:0 sc1\n" \
"v_mfma_f32_16x16x32_bf16 v[76:79], a[200:203], v[88:91], v[76:79]\n" \
"ds_read_b64_tr_b16 a[234:235], v227 offset:31232\n" \
"v_mfma_f32_16x16x32_bf16 v[108:111], a[196:199], v[112:115], v[108:111]\n" \
"ds_read_b64_tr_b16 a[236:237], v215 offset:32768\n" \
"buffer_load_dwordx4 a[116:119], v210, %[srdz], s50 offen offset:1024 sc1\n" \
"v_mfma_f32_16x16x32_bf16 v[108:111], a[200:203], v[120:123], v[108:111]\n" \
"s_waitcnt vmcnt(63) lgkmcnt(13)\n" \
"s_nop 0\n" \
"v_mfma_f32_16x16x32_bf16 v[20:23], a[204:207], v[0:3], 0\n" \
"ds_read_b64_tr_b16 a[238:239], v215 offset:34816\n" \
"buffer_load_dwordx4 a[72:75], %[oq0], %[srdz], s50 offen offset:2048 sc1\n" \
"v_mfma_f32_16x16x32_bf16 v[20:23], a[208:211], v[8:11], v[20:23]\n" \
"ds_read_b64_tr_b16 a[196:197], v215 offset:36864\n" \
"v_mfma_f32_16x16x32_bf16 v[52:55], a[204:207], v[32:35], 0\n" \
"ds_read_b64_tr_b16 a[198:199], v215 offset:38912\n" \
"buffer_load_dwordx4 a[76:79], %[oq0], %[srdz], s50 offen offset:3072 sc1\n" \
"v_mfma_f32_16x16x32_bf16 v[52:55], a[208:211], v[40:43], v[52:55]\n" \
"v_mfma_f32_16x16x32_bf16 v[84:87], a[204:207], v[64:67], 0\n" \
"s_waitcnt vmcnt(63) lgkmcnt(4)\n" \
"s_barrier\n" \
"v_mfma_f32_16x16x32_bf16 v[84:87], a[208:211], v[72:75], v[84:87]\n" \
"buffer_load_dwordx4 a[88:91], v208, %[srdz], s50 offen offset:2048 sc1\n" \
"v_mfma_f32_16x16x32_bf16 v[116:119], a[204:207], v[96:99], 0\n" \
"ds_read_b64_tr_b16 a[200:201], v215 offset:40960\n" \
"v_mfma_f32_16x16x32_bf16 v[116:119], a[208:211], v[104:107], v[116:119]\n" \
"ds_read_b64_tr_b16 a[202:203], v215 offset:43008\n" \
"buffer_load_dwordx4 a[92:95], v208, %[srdz], s50 offen offset:3072 sc1\n" \
"v_mfma_f32_16x16x32_bf16 v[20:23], a[212:215], v[16:19], v[20:23]\n" \
"ds_read_b64_tr_b16 a[204:205], v215 offset:45056\n" \
"v_mfma_f32_16x16x32_bf16 v[20:23], a[216:219], v[24:27], v[20:23]\n" \
"ds_read_b64_tr_b16 a[206:207], v215 offset:47104\n" \
"buffer_load_dwordx4 a[104:107], v209, %[srdz], s50 offen offset:2048 sc1\n" \
"s_waitcnt vmcnt(15)\n" \
"s_nop 0\n" \
"v_mfma_f32_16x16x32_bf16 v[52:55], a[212:215], v[48:51], v[52:55]\n" \
"v_mfma_f32_16x16x32_bf16 v[216:219], a[176:179], v[0:3], 0\n" \
"v_mfma_f32_16x16x32_bf16 v[216:219], a[180:183], v[8:11], v[216:219]\n" \
"v_mfma_f32_16x16x32_bf16 v[216:219], a[184:187], v[16:19], v[216:219]\n" \
"v_mfma_f32_16x16x32_bf16 v[216:219], a[188:191], v[24:27], v[216:219]\n" \
"v_mfma_f32_16x16x32_bf16 v[52:55], a[216:219], v[56:59], v[52:55]\n" \
"buffer_load_dwordx4 a[108:111], v209, %[srdz], s50 offen offset:3072 sc1\n" \
"v_mfma_f32_16x16x32_bf16 v[84:87], a[212:215], v[80:83], v[84:87]\n" \
"v_mfma_f32_16x16x32_bf16 v[84:87], a[216:219], v[88:91], v[84:87]\n" \
"buffer_load_dwordx4 a[120:123], v210, %[srdz], s50 offen offset:2048 sc1\n" \
"v_mfma_f32_16x16x32_bf16 v[116:119], a[212:215], v[112:115], v[116:119]\n" \
"v_mfma_f32_16x16x32_bf16 v[232:235], a[176:179], v[32:35], 0\n" \
"v_mfma_f32_16x16x32_bf16 v[232:235], a[180:183], v[40:43], v[232:235]\n" \
"v_mfma_f32_16x16x32_bf16 v[232:235], a[184:187], v[48:51], v[232:235]\n" \
"v_mfma_f32_16x16x32_bf16 v[232:235], a[188:191], v[56:59], v[232:235]\n" \
"v_mfma_f32_16x16x32_bf16 v[116:119], a[216:219], v[120:123], v[116:119]\n" \
"v_mov_b32_dpp v231, v216 quad_perm:[0,1,2,3] row_mask:0x1 bank_mask:0xf\n" \
"s_waitcnt vmcnt(63) lgkmcnt(10)\n" \
"s_nop 0\n" \
"v_mfma_f32_16x16x32_bf16 v[28:31], a[220:223], v[0:3], 0\n" \
"buffer_load_dwordx4 a[124:127], v210, %[srdz], s50 offen offset:3072 sc1\n" \
"v_mfma_f32_16x16x32_bf16 v[28:31], a[228:231], v[8:11], v[28:31]\n" \
"s_sub_u32 s61, s48, 4\n" \
"s_cmp_lt_u32 s61, 2\n" \
"s_cselect_b64 vcc, -1, 0\n" \
"s_nop 0\n" \
"v_mfma_f32_16x16x32_bf16 v[60:63], a[220:223], v[32:35], 0\n" \
"s_cbranch_vccz Lg5_%=\n" \
"s_nop 0\n" \
"buffer_store_dwordx4 v[0:3], %[oq0], %[srdz], s60 offen offset:0 sc1\n" \
"Lg5_%=:\n" \
"v_mfma_f32_16x16x32_bf16 v[216:219], a[176:179], v[64:67], 0\n" \
"v_mfma_f32_16x16x32_bf16 v[216:219], a[180:183], v[72:75], v[216:219]\n" \
"v_mfma_f32_16x16x32_bf16 v[216:219], a[184:187], v[80:83], v[216:219]\n" \
"v_mfma_f32_16x16x32_bf16 v[216:219], a[188:191], v[88:91], v[216:219]\n" \
"v_mfma_f32_16x16x32_bf16 v[60:63], a[228:231], v[40:43], v[60:63]\n" \
"v_mov_b32_dpp v231, v232 quad_perm:[0,1,2,3] row_mask:0x2 bank_mask:0xf\n" \
"v_mfma_f32_16x16x32_bf16 v[92:95], a[220:223], v[64:67], 0\n" \
"s_cmp_gt_u32 s48, 1\n" \
"s_cselect_b32 s50, 1, 2\n" \
"s_mul_i32 s50, s50, 152064\n" \
"buffer_load_dwordx4 a[0:3], %[oq0], %[srdz], s50 offen offset:0 sc1\n" \
"v_mfma_f32_16x16x32_bf16 v[92:95], a[228:231], v[72:75], v[92:95]\n" \
"v_mfma_f32_16x16x32_bf16 v[124:127], a[220:223], v[96:99], 0\n" \
"ds_read_b64_tr_b16 a[208:209], v227 offset:32768\n" \
"v_mfma_f32_16x16x32_bf16 v[232:235], a[176:179], v[96:99], 0\n" \
"v_mfma_f32_16x16x32_bf16 v[232:235], a[180:183], v[104:107], v[232:235]\n" \
"v_mfma_f32_16x16x32_bf16 v[232:235], a[184:187], v[112:115], v[232:235]\n" \
"v_mfma_f32_16x16x32_bf16 v[232:235], a[188:191], v[120:123], v[232:235]\n" \
"v_mfma_f32_16x16x32_bf16 v[124:127], a[228:231], v[104:107], v[124:127]\n" \
"ds_read_b64_tr_b16 a[210:211], v227 offset:34816\n" \
"v_mov_b32_dpp v231, v216 quad_perm:[0,1,2,3] row_mask:0x4 bank_mask:0xf\n" \
"s_waitcnt vmcnt(15) lgkmcnt(6)\n" \
"s_nop 0\n" \
"v_mfma_f32_16x16x32_bf16 v[128:131], a[236:239], a[64:67], v[192:195]\n" \
"ds_read_b64_tr_b16 a[212:213], v227 offset:36864\n" \
"v_mfma_f32_16x16x32_bf16 v[128:131], a[196:199], a[68:71], v[128:131]\n" \
"ds_read_b64_tr_b16 a[214:215], v227 offset:38912\n" \
"s_waitcnt vmcnt(13) lgkmcnt(15)\n" \
"s_nop 0\n" \
"v_mfma_f32_16x16x32_bf16 v[132:135], a[236:239], a[80:83], v[196:199]\n" \
"s_cbranch_vccz Lg6_%=\n" \
"s_nop 0\n" \
"buffer_store_dwordx4 v[8:11], %[oq0], %[srdz], s60 offen offset:1024 sc1\n" \
"Lg6_%=:\n" \
"v_mfma_f32_16x16x32_bf16 v[132:135], a[196:199], a[84:87], v[132:135]\n" \
"s_waitcnt vmcnt(13) lgkmcnt(15)\n" \
"s_bitcmp1_b32 0x30, s48\n" \
"s_cbranch_scc1 Lcw2_%=\n" \
"s_waitcnt vmcnt(11)\n" \
"Lcw2_%=:\n" \
"v_mfma_f32_16x16x32_bf16 v[136:139], a[236:239], a[96:99], v[200:203]\n" \
"v_mov_b32_dpp v231, v232 quad_perm:[0,1,2,3] row_mask:0x8 bank_mask:0xf\n" \
"buffer_load_dwordx4 a[4:7], %[oq0], %[srdz], s50 offen offset:1024 sc1\n" \
"v_mfma_f32_16x16x32_bf16 v[136:139], a[196:199], a[100:103], v[136:139]\n" \
"s_waitcnt vmcnt(12) lgkmcnt(15)\n" \
"s_bitcmp1_b32 0x30, s48\n" \
"s_cbranch_scc1 Lcw3_%=\n" \
"s_waitcnt vmcnt(10)\n" \
"Lcw3_%=:\n" \
"v_mfma_f32_16x16x32_bf16 v[140:143], a[236:239], a[112:115], v[204:207]\n" \
"ds_read_b64_tr_b16 a[216:217], v227 offset:40960\n" \
"v_cmp_u_f32_e64 s[42:43], v231, v231\n" \
"v_readlane_b32 s41, %[tlo], 33\n" \
"v_mfma_f32_16x16x32_bf16 v[140:143], a[196:199], a[116:119], v[140:143]\n" \
"s_waitcnt vmcnt(10) lgkmcnt(5)\n" \
"s_bitcmp1_b32 0x30, s48\n" \
"s_cbranch_scc1 Lcw4_%=\n" \
"s_waitcnt vmcnt(8)\n" \
"Lcw4_%=:\n" \
"v_mfma_f32_16x16x32_bf16 v[128:131], a[200:203], a[72:75], v[128:131]\n" \
"ds_read_b64_tr_b16 a[218:219], v227 offset:43008\n" \
"buffer_load_dwordx4 a[8:11], %[oq0], %[srdz], s50 offen offset:2048 sc1\n" \
"v_mfma_f32_16x16x32_bf16 v[128:131], a[204:207], a[76:79], v[128:131]\n" \
"ds_read_b64_tr_b16 a[220:221], v227 offset:45056\n" \
"v_lshrrev_b32_e64 v222, 5, v224\n" \
"s_waitcnt vmcnt(9) lgkmcnt(15)\n" \
"s_bitcmp1_b32 0x30, s48\n" \
"s_cbranch_scc1 Lcw5_%=\n" \
"s_waitcnt vmcnt(7)\n" \
"Lcw5_%=:\n" \
"v_mfma_f32_16x16x32_bf16 v[132:135], a[200:203], a[88:91], v[132:135]\n" \
"ds_read_b64_tr_b16 a[222:223], v227 offset:47104\n" \
"v_mfma_f32_16x16x32_bf16 v[132:135], a[204:207], a[92:95], v[132:135]\n" \
"s_cbranch_vccz Lg7_%=\n" \
"s_nop 0\n" \
"buffer_store_dwordx4 v[16:19], %[oq0], %[srdz], s60 offen offset:2048 sc1\n" \
"Lg7_%=:\n" \
"s_waitcnt vmcnt(6) lgkmcnt(14)\n" \
"s_bitcmp1_b32 0x30, s48\n" \
"s_cbranch_scc1 Lcw6_%=\n" \
"s_waitcnt vmcnt(3)\n" \
"Lcw6_%=:\n" \
"v_mfma_f32_16x16x32_bf16 v[136:139], a[200:203], a[104:107], v[136:139]\n" \
"s_cmp_lg_u64 s[42:43], 0\n" \
"s_cbranch_scc1 Ls0_%=\n" \
"Ls0b_%=:\n" \
"v_mfma_f32_16x16x32_bf16 v[136:139], a[204:207], a[108:111], v[136:139]\n" \
"v_or_b32_e64 v231, 1, v231\n" \
"buffer_load_dwordx4 a[12:15], %[oq0], %[srdz], s50 offen offset:3072 sc1\n" \
"v_mfma_f32_16x16x32_bf16 v[140:143], a[200:203], a[120:123], v[140:143]\n" \
"ds_read_b64_tr_b16 a[228:229], v215 offset:33280\n" \
"v_mfma_f32_16x16x32_bf16 v[140:143], a[204:207], a[124:127], v[140:143]\n" \
"ds_read_b64_tr_b16 a[230:231], v215 offset:35328\n" \
"v_mfma_f32_16x16x32_bf16 v[28:31], a[224:227], v[16:19], v[28:31]\n" \
"buffer_store_dword v231, v222, %[srdz], s41 offen sc1\n" \
"v_mfma_f32_16x16x32_bf16 v[28:31], a[232:235], v[24:27], v[28:31]\n" \
"v_mfma_f32_16x16x32_bf16 v[60:63], a[224:227], v[48:51], v[60:63]\n" \
"v_mfma_f32_16x16x32_bf16 v[60:63], a[232:235], v[56:59], v[60:63]\n" \
"s_cbranch_vccz Lg8_%=\n" \
"s_nop 0\n" \
"buffer_store_dwordx4 v[24:27], %[oq0], %[srdz], s60 offen offset:3072 sc1\n" \
"Lg8_%=:\n" \
"v_mfma_f32_16x16x32_bf16 v[92:95], a[224:227], v[80:83], v[92:95]\n" \
"v_mfma_f32_16x16x32_bf16 v[92:95], a[232:235], v[88:91], v[92:95]\n" \
"v_cmp_u_f32_e64 s[44:45], v128, v132\n" \
"buffer_load_dwordx4 a[16:19], v208, %[srdz], s50 offen offset:0 sc1\n" \
"v_mfma_f32_16x16x32_bf16 v[124:127], a[224:227], v[112:115], v[124:127]\n" \
"v_mfma_f32_16x16x32_bf16 v[124:127], a[232:235], v[120:123], v[124:127]\n" \
"v_cmp_u_f32_e64 s[56:57], v136, v140\n" \
"s_or_b64 s[56:57], s[56:57], s[44:45]\n" \
"s_cbranch_scc1 Lsp1_%=\n" \
"Lbk1_%=:\n" \
"s_waitcnt vmcnt(63) lgkmcnt(2)\n" \
"s_nop 0\n" \
"v_mfma_f32_16x16x32_bf16 v[144:147], a[208:211], a[64:67], v[144:147]\n" \
"ds_read_b64_tr_b16 a[224:225], v215 offset:37376\n" \
"v_mfma_f32_16x16x32_bf16 v[144:147], a[212:215], a[68:71], v[144:147]\n" \
"ds_read_b64_tr_b16 a[226:227], v215 offset:39424\n" \
"buffer_load_dwordx4 a[20:23], v208, %[srdz], s50 offen offset:1024 sc1\n" \
"v_mfma_f32_16x16x32_bf16 v[148:151], a[208:211], a[80:83], v[148:151]\n" \
"ds_read_b64_tr_b16 a[196:197], v215 offset:41472\n" \
"v_mfma_f32_16x16x32_bf16 v[148:151], a[212:215], a[84:87], v[148:151]\n" \
"ds_read_b64_tr_b16 a[198:199], v215 offset:43520\n" \
"v_mfma_f32_16x16x32_bf16 v[152:155], a[208:211], a[96:99], v[152:155]\n" \
"ds_read_b64_tr_b16 a[200:201], v215 offset:45568\n" \
"s_cbranch_vccz Lg9_%=\n" \
"s_nop 0\n" \
"buffer_store_dwordx4 v[32:35], v208, %[srdz], s60 offen offset:0 sc1\n" \
"Lg9_%=:\n" \
"v_mfma_f32_16x16x32_bf16 v[152:155], a[212:215], a[100:103], v[152:155]\n" \
"ds_read_b64_tr_b16 a[202:203], v215 offset:47616\n" \
"v_mfma_f32_16x16x32_bf16 v[156:159], a[208:211], a[112:115], v[156:159]\n" \
"ds_read_b64_tr_b16 a[204:205], v227 offset:33280\n" \
"buffer_load_dwordx4 a[24:27], v208, %[srdz], s50 offen offset:2048 sc1\n" \
"v_mfma_f32_16x16x32_bf16 v[156:159], a[212:215], a[116:119], v[156:159]\n" \
"ds_read_b64_tr_b16 a[206:207], v227 offset:35328\n" \
"v_mfma_f32_16x16x32_bf16 v[144:147], a[216:219], a[72:75], v[144:147]\n" \
"ds_read_b64_tr_b16 a[232:233], v227 offset:37376\n" \
"v_mfma_f32_16x16x32_bf16 v[144:147], a[220:223], a[76:79], v[144:147]\n" \
"ds_read_b64_tr_b16 a[234:235], v227 offset:39424\n" \
"v_mfma_f32_16x16x32_bf16 v[148:151], a[216:219], a[88:91], v[148:151]\n" \
"buffer_load_dwordx4 a[28:31], v208, %[srdz], s50 offen offset:3072 sc1\n" \
"v_mfma_f32_16x16x32_bf16 v[148:151], a[220:223], a[92:95], v[148:151]\n" \
"v_mfma_f32_16x16x32_bf16 v[152:155], a[216:219], a[104:107], v[152:155]\n" \
"s_cbranch_vccz Lg10_%=\n" \
"s_nop 0\n" \
"buffer_store_dwordx4 v[40:43], v208, %[srdz], s60 offen offset:1024 sc1\n" \
"Lg10_%=:\n" \
"v_mfma_f32_16x16x32_bf16 v[152:155], a[220:223], a[108:111], v[152:155]\n" \
"v_mfma_f32_16x16x32_bf16 v[156:159], a[216:219], a[120:123], v[156:159]\n" \
"ds_read_b64_tr_b16 a[236:237], v227 offset:41472\n" \
"buffer_load_dwordx4 a[32:35], v209, %[srdz], s50 offen offset:0 sc1\n" \
"v_mfma_f32_16x16x32_bf16 v[156:159], a[220:223], a[124:127], v[156:159]\n" \
"s_waitcnt vmcnt(63) lgkmcnt(5)\n" \
"s_nop 0\n" \
"v_mfma_f32_16x16x32_bf16 v[160:163], a[228:231], a[64:67], v[160:163]\n" \
"ds_read_b64_tr_b16 a[238:239], v227 offset:43520\n" \
"v_mfma_f32_16x16x32_bf16 v[160:163], a[224:227], a[68:71], v[160:163]\n" \
"ds_read_b64_tr_b16 a[208:209], v227 offset:45568\n" \
"v_mfma_f32_16x16x32_bf16 v[164:167], a[228:231], a[80:83], v[164:167]\n" \
"ds_read_b64_tr_b16 a[210:211], v227 offset:47616\n" \
"buffer_load_dwordx4 a[36:39], v209, %[srdz], s50 offen offset:1024 sc1\n" \
"v_mfma_f32_16x16x32_bf16 v[164:167], a[224:227], a[84:87], v[164:167]\n" \
"v_mfma_f32_16x16x32_bf16 v[168:171], a[228:231], a[96:99], v[168:171]\n" \
"v_mfma_f32_16x16x32_bf16 v[168:171], a[224:227], a[100:103], v[168:171]\n" \
"s_cbranch_vccz Lg11_%=\n" \
"s_nop 0\n" \
"buffer_store_dwordx4 v[48:51], v208, %[srdz], s60 offen offset:2048 sc1\n" \
"Lg11_%=:\n" \
"v_mfma_f32_16x16x32_bf16 v[172:175], a[228:231], a[112:115], v[172:175]\n" \
"v_mfma_f32_16x16x32_bf16 v[172:175], a[224:227], a[116:119], v[172:175]\n" \
"buffer_load_dwordx4 a[40:43], v209, %[srdz], s50 offen offset:2048 sc1\n" \
"v_mfma_f32_16x16x32_bf16 v[160:163], a[196:199], a[72:75], v[160:163]\n" \
"v_mfma_f32_16x16x32_bf16 v[160:163], a[200:203], a[76:79], v[160:163]\n" \
"buffer_load_dwordx4 a[44:47], v209, %[srdz], s50 offen offset:3072 sc1\n" \
"v_mfma_f32_16x16x32_bf16 v[164:167], a[196:199], a[88:91], v[164:167]\n" \
"v_mfma_f32_16x16x32_bf16 v[164:167], a[200:203], a[92:95], v[164:167]\n" \
"buffer_load_dwordx4 a[48:51], v210, %[srdz], s50 offen offset:0 sc1\n" \
"v_mfma_f32_16x16x32_bf16 v[168:171], a[196:199], a[104:107], v[168:171]\n" \
"s_waitcnt vmcnt(59) lgkmcnt(0)\n" \
"s_bitcmp1_b32 0xf, s48\n" \
"s_cbranch_scc1 Lcw7_%=\n" \
"s_waitcnt vmcnt(50)\n" \
"s_bitcmp1_b32 0x30, s48\n" \
"s_cbranch_scc1 Lcw7_%=\n" \
".if . - adaln_body != 45056\n" \
".error \"size model\"\n" \
".endif\n" \
"Lk10_%=:\n" \
"s_cbranch_execz Lk11_%=\n" \
"s_nop 0\n" \
"s_waitcnt vmcnt(43)\n" \
"Lcw7_%=:\n" \
"s_barrier\n" \
"v_mfma_f32_16x16x32_bf16 v[168:171], a[200:203], a[108:111], v[168:171]\n" \
"ds_read_b64_tr_b16 a[212:213], v220 offset:32768\n" \
"s_cbranch_vccz Lg12_%=\n" \
"s_nop 0\n" \
"buffer_store_dwordx4 v[56:59], v208, %[srdz], s60 offen offset:3072 sc1\n" \
"Lg12_%=:\n" \
"v_mfma_f32_16x16x32_bf16 v[172:175], a[196:199], a[120:123], v[172:175]\n" \
"ds_read_b64_tr_b16 a[214:215], v220 offset:34816\n" \
"v_mfma_f32_16x16x32_bf16 v[172:175], a[200:203], a[124:127], v[172:175]\n" \
"s_waitcnt vmcnt(63) lgkmcnt(2)\n" \
"s_nop 0\n" \
"v_mfma_f32_16x16x32_bf16 v[176:179], a[204:207], a[64:67], v[176:179]\n" \
"ds_read_b64_tr_b16 a[216:217], v220 offset:36864\n" \
"buffer_load_dwordx4 a[52:55], v210, %[srdz], s50 offen offset:1024 sc1\n" \
"v_mfma_f32_16x16x32_bf16 v[176:179], a[232:235], a[68:71], v[176:179]\n" \
"ds_read_b64_tr_b16 a[218:219], v220 offset:38912\n" \
"v_mfma_f32_16x16x32_bf16 v[180:183], a[204:207], a[80:83], v[180:183]\n" \
"ds_read_b64_tr_b16 a[220:221], v220 offset:40960\n" \
"v_mfma_f32_16x16x32_bf16 v[180:183], a[232:235], a[84:87], v[180:183]\n" \
"ds_read_b64_tr_b16 a[222:223], v220 offset:43008\n" \
"v_mfma_f32_16x16x32_bf16 v[184:187], a[204:207], a[96:99], v[184:187]\n" \
"ds_read_b64_tr_b16 a[228:229], v220 offset:45056\n" \
"buffer_load_dwordx4 a[56:59], v210, %[srdz], s50 offen offset:2048 sc1\n" \
"v_mfma_f32_16x16x32_bf16 v[184:187], a[232:235], a[100:103], v[184:187]\n" \
"ds_read_b64_tr_b16 a[230:231], v220 offset:47104\n" \
"v_mfma_f32_16x16x32_bf16 v[188:191], a[204:207], a[112:115], v[188:191]\n" \
"ds_read_b64_tr_b16 a[224:225], v221 offset:32768\n" \
"s_cbranch_vccz Lg13_%=\n" \
"s_nop 0\n" \
"buffer_store_dwordx4 v[64:67], v209, %[srdz], s60 offen offset:0 sc1\n" \
"Lg13_%=:\n" \
"v_mfma_f32_16x16x32_bf16 v[188:191], a[232:235], a[116:119], v[188:191]\n" \
"ds_read_b64_tr_b16 a[226:227], v221 offset:34816\n" \
"v_mfma_f32_16x16x32_bf16 v[176:179], a[236:239], a[72:75], v[176:179]\n" \
"ds_read_b64_tr_b16 a[196:197], v221 offset:36864\n" \
"buffer_load_dwordx4 a[60:63], v210, %[srdz], s50 offen offset:3072 sc1\n" \
"v_mfma_f32_16x16x32_bf16 v[176:179], a[208:211], a[76:79], v[176:179]\n" \
"ds_read_b64_tr_b16 a[198:199], v221 offset:38912\n" \
"v_mfma_f32_16x16x32_bf16 v[180:183], a[236:239], a[88:91], v[180:183]\n" \
"s_cmp_gt_u32 s48, 2\n" \
"s_cselect_b32 s50, 2, 3\n" \
"s_mul_i32 s50, s50, 152064\n" \
"buffer_load_dwordx4 a[128:131], %[oq0], %[srdz], s50 offen offset:0 sc1\n" \
"v_mfma_f32_16x16x32_bf16 v[180:183], a[208:211], a[92:95], v[180:183]\n" \
"v_mfma_f32_16x16x32_bf16 v[184:187], a[236:239], a[104:107], v[184:187]\n" \
"v_mfma_f32_16x16x32_bf16 v[184:187], a[208:211], a[108:111], v[184:187]\n" \
"v_mfma_f32_16x16x32_bf16 v[188:191], a[236:239], a[120:123], v[188:191]\n" \
"ds_read_b64_tr_b16 a[200:201], v221 offset:40960\n" \
"s_cbranch_vccz Lg14_%=\n" \
"s_nop 0\n" \
"buffer_store_dwordx4 v[72:75], v209, %[srdz], s60 offen offset:1024 sc1\n" \
"Lg14_%=:\n" \
"v_mfma_f32_16x16x32_bf16 v[188:191], a[208:211], a[124:127], v[188:191]\n" \
"s_waitcnt vmcnt(63) lgkmcnt(5)\n" \
"s_nop 0\n" \
"v_mfma_f32_16x16x32_bf16 v[4:7], a[212:215], a[64:67], v[4:7]\n" \
"ds_read_b64_tr_b16 a[202:203], v221 offset:43008\n" \
"v_mfma_f32_16x16x32_bf16 v[4:7], a[216:219], a[68:71], v[4:7]\n" \
"ds_read_b64_tr_b16 a[204:205], v221 offset:45056\n" \
"s_add_u32 m0, %[m0b], 0\n" \
"s_cmp_gt_u32 s48, 2\n" \
"s_cselect_b32 s49, 2, 3\n" \
"s_lshl_b32 s49, s49, 18\n" \
"s_or_b32 s49, s49, s58\n" \
"s_nop 0\n" \
"buffer_load_dwordx4 %[wo0], %[srdt], s49 offen offset:0 lds\n" \
"v_mfma_f32_16x16x32_bf16 v[36:39], a[212:215], a[80:83], v[36:39]\n" \
"ds_read_b64_tr_b16 a[206:207], v221 offset:47104\n" \
"v_mfma_f32_16x16x32_bf16 v[36:39], a[216:219], a[84:87], v[36:39]\n" \
"buffer_load_dwordx4 a[132:135], %[oq0], %[srdz], s50 offen offset:1024 sc1\n" \
"v_mfma_f32_16x16x32_bf16 v[68:71], a[212:215], a[96:99], v[68:71]\n" \
"v_mfma_f32_16x16x32_bf16 v[68:71], a[216:219], a[100:103], v[68:71]\n" \
"v_mfma_f32_16x16x32_bf16 v[100:103], a[212:215], a[112:115], v[100:103]\n" \
"ds_read_b64_tr_b16 a[232:233], v220 offset:33280\n" \
"v_mfma_f32_16x16x32_bf16 v[100:103], a[216:219], a[116:119], v[100:103]\n" \
"ds_read_b64_tr_b16 a[234:235], v220 offset:35328\n" \
"s_cbranch_vccz Lg15_%=\n" \
"s_nop 0\n" \
"buffer_store_dwordx4 v[80:83], v209, %[srdz], s60 offen offset:2048 sc1\n" \
"Lg15_%=:\n" \
"v_mfma_f32_16x16x32_bf16 v[4:7], a[220:223], a[72:75], v[4:7]\n" \
"ds_read_b64_tr_b16 a[236:237], v220 offset:37376\n" \
"s_lshl_b32 s40, %[wid], 12\n" \
"s_nop 0\n" \
"s_mov_b32 s43, 0xffff0000\n" \
"v_mfma_f32_16x16x32_bf16 v[4:7], a[228:231], a[76:79], v[4:7]\n" \
"ds_read_b64_tr_b16 a[238:239], v220 offset:39424\n" \
"buffer_load_dwordx4 a[136:139], %[oq0], %[srdz], s50 offen offset:2048 sc1\n" \
"v_mfma_f32_16x16x32_bf16 v[36:39], a[220:223], a[88:91], v[36:39]\n" \
"v_lshrrev_b32_e32 v222, 7, v224\n" \
"s_sub_u32 s40, %[m0b], s40\n" \
"v_mfma_f32_16x16x32_bf16 v[36:39], a[228:231], a[92:95], v[36:39]\n" \
"v_readlane_b32 s41, %[tlo], 26\n" \
"v_mfma_f32_16x16x32_bf16 v[68:71], a[220:223], a[104:107], v[68:71]\n" \
"v_readlane_b32 s42, %[thi], 26\n" \
"v_mfma_f32_16x16x32_bf16 v[68:71], a[228:231], a[108:111], v[68:71]\n" \
"buffer_load_dwordx4 a[140:143], %[oq0], %[srdz], s50 offen offset:3072 sc1\n" \
"v_mfma_f32_16x16x32_bf16 v[100:103], a[220:223], a[120:123], v[100:103]\n" \
"ds_read_b64_tr_b16 a[208:209], v220 offset:41472\n" \
"v_lshrrev_b32_e64 v217, 3, v224\n" \
"v_mfma_f32_16x16x32_bf16 v[100:103], a[228:231], a[124:127], v[100:103]\n" \
"s_waitcnt vmcnt(63) lgkmcnt(5)\n" \
"s_nop 0\n" \
"v_mfma_f32_16x16x32_bf16 v[12:15], a[224:227], a[64:67], v[12:15]\n" \
"ds_read_b64_tr_b16 a[210:211], v220 offset:43520\n" \
"s_cbranch_vccz Lg16_%=\n" \
"s_nop 0\n" \
"buffer_store_dwordx4 v[88:91], v209, %[srdz], s60 offen offset:3072 sc1\n" \
"Lg16_%=:\n" \
"v_mfma_f32_16x16x32_bf16 v[12:15], a[196:199], a[68:71], v[12:15]\n" \
"ds_read_b64_tr_b16 a[212:213], v220 offset:45568\n" \
"v_lshrrev_b32_e64 v219, 5, v222\n" \
"v_mfma_f32_16x16x32_bf16 v[44:47], a[224:227], a[80:83], v[44:47]\n" \
"ds_read_b64_tr_b16 a[214:215], v220 offset:47616\n" \
"buffer_load_dwordx4 v212, %[srdt], s49 offen offset:1024 lds\n" \
"v_mfma_f32_16x16x32_bf16 v[44:47], a[196:199], a[84:87], v[44:47]\n" \
"v_bfe_u32 v235, v222, 2, 1\n" \
"v_mfma_f32_16x16x32_bf16 v[76:79], a[224:227], a[96:99], v[76:79]\n" \
"v_lshl_or_b32 v219, v219, 1, v235\n" \
"v_mfma_f32_16x16x32_bf16 v[76:79], a[196:199], a[100:103], v[76:79]\n" \
"buffer_load_dwordx4 a[144:147], v208, %[srdz], s50 offen offset:0 sc1\n" \
"v_mfma_f32_16x16x32_bf16 v[108:111], a[224:227], a[112:115], v[108:111]\n" \
"ds_read_b64_tr_b16 a[216:217], v221 offset:33280\n" \
"v_add_u32_e64 v235, s41, v235\n" \
"v_mfma_f32_16x16x32_bf16 v[108:111], a[196:199], a[116:119], v[108:111]\n" \
"ds_read_b64_tr_b16 a[218:219], v221 offset:35328\n" \
"v_sub_u32_e32 %[ta0], 0x7ff, v235\n" \
"v_mfma_f32_16x16x32_bf16 v[12:15], a[200:203], a[72:75], v[12:15]\n" \
"ds_read_b64_tr_b16 a[220:221], v221 offset:41472\n" \
"s_cbranch_vccz Lg17_%=\n" \
"s_nop 0\n" \
"buffer_store_dwordx4 v[96:99], v210, %[srdz], s60 offen offset:0 sc1\n" \
"Lg17_%=:\n" \
"v_mfma_f32_16x16x32_bf16 v[12:15], a[204:207], a[76:79], v[12:15]\n" \
"ds_read_b64_tr_b16 a[222:223], v221 offset:43520\n" \
"v_and_b32_e64 v234, 16, v222\n" \
"v_mfma_f32_16x16x32_bf16 v[44:47], a[200:203], a[88:91], v[44:47]\n" \
"ds_read_b64_tr_b16 a[228:229], v221 offset:37376\n" \
"v_and_b32_e64 v233, 1, v222\n" \
"v_mfma_f32_16x16x32_bf16 v[44:47], a[204:207], a[92:95], v[44:47]\n" \
"ds_read_b64_tr_b16 a[230:231], v221 offset:39424\n" \
"v_cmp_eq_u32_e64 s[68:69], 1, v233\n" \
"v_mfma_f32_16x16x32_bf16 v[76:79], a[200:203], a[104:107], v[76:79]\n" \
"ds_read_b64_tr_b16 a[224:225], v221 offset:45568\n" \
"s_lshl_b32 s52, %[wid], 2\n" \
"s_nop 0\n" \
"v_mfma_f32_16x16x32_bf16 v[76:79], a[204:207], a[108:111], v[76:79]\n" \
"ds_read_b64_tr_b16 a[226:227], v221 offset:47616\n" \
"v_cndmask_b32_e64 v234, v234, 32, s[68:69]\n" \
"v_mfma_f32_16x16x32_bf16 v[108:111], a[200:203], a[120:123], v[108:111]\n" \
"buffer_load_dwordx4 a[148:151], v208, %[srdz], s50 offen offset:1024 sc1\n" \
"v_mfma_f32_16x16x32_bf16 v[108:111], a[204:207], a[124:127], v[108:111]\n" \
"s_waitcnt vmcnt(63) lgkmcnt(12)\n" \
"s_nop 0\n" \
"v_mfma_f32_16x16x32_bf16 v[20:23], a[232:235], a[64:67], v[20:23]\n" \
"s_cbranch_vccz Lg18_%=\n" \
"s_nop 0\n" \
"buffer_store_dwordx4 v[104:107], v210, %[srdz], s60 offen offset:1024 sc1\n" \
"Lg18_%=:\n" \
"v_mfma_f32_16x16x32_bf16 v[20:23], a[236:239], a[68:71], v[20:23]\n" \
"s_add_u32 s52, s52, s40\n" \
"s_nop 0\n" \
"v_mfma_f32_16x16x32_bf16 v[52:55], a[232:235], a[80:83], v[52:55]\n" \
"buffer_load_dwordx4 v213, %[srdt], s49 offen offset:2048 lds\n" \
"v_mfma_f32_16x16x32_bf16 v[52:55], a[236:239], a[84:87], v[52:55]\n" \
"v_mad_u32_u24 v228, v219, 48, v234\n" \
"s_add_u32 s63, s52, 29184\n" \
"v_mfma_f32_16x16x32_bf16 v[84:87], a[232:235], a[96:99], v[84:87]\n" \
"s_waitcnt vmcnt(63) lgkmcnt(0)\n" \
"s_bitcmp1_b32 0x30, s48\n" \
"s_cbranch_scc1 Lcw8_%=\n" \
"s_waitcnt vmcnt(49)\n" \
"Lcw8_%=:\n" \
"s_barrier\n" \
"s_nop 0\n" \
"v_mfma_f32_16x16x32_bf16 v[84:87], a[236:239], a[100:103], v[84:87]\n" \
"ds_read_b64_tr_b16 a[196:197], v220 offset:49152\n" \
"v_add_u32_e64 v228, s63, v228\n" \
"v_mfma_f32_16x16x32_bf16 v[116:119], a[232:235], a[112:115], v[116:119]\n" \
"ds_read_b64_tr_b16 a[198:199], v220 offset:51200\n" \
"buffer_load_dwordx4 a[152:155], v208, %[srdz], s50 offen offset:2048 sc1\n" \
"v_mfma_f32_16x16x32_bf16 v[116:119], a[236:239], a[116:119], v[116:119]\n" \
"ds_read_b64_tr_b16 a[200:201], v220 offset:53248\n" \
"v_and_b32_e64 v216, 15, v222\n" \
"v_mfma_f32_16x16x32_bf16 v[20:23], a[208:211], a[72:75], v[20:23]\n" \
"ds_read_b64_tr_b16 a[202:203], v220 offset:55296\n" \
"v_lshrrev_b32_e64 v218, 4, v222\n" \
"v_mfma_f32_16x16x32_bf16 v[20:23], a[212:215], a[76:79], v[20:23]\n" \
"ds_read_b64_tr_b16 a[204:205], v220 offset:57344\n" \
"s_cbranch_vccz Lg19_%=\n" \
"s_nop 0\n" \
"buffer_store_dwordx4 v[112:115], v210, %[srdz], s60 offen offset:2048 sc1\n" \
"Lg19_%=:\n" \
"v_mfma_f32_16x16x32_bf16 v[52:55], a[208:211], a[88:91], v[52:55]\n" \
"ds_read_b64_tr_b16 a[206:207], v220 offset:59392\n" \
"v_lshrrev_b32_e64 v230, 3, v216\n" \
"v_mfma_f32_16x16x32_bf16 v[52:55], a[212:215], a[92:95], v[52:55]\n" \
"ds_read_b64_tr_b16 a[232:233], v220 offset:61440\n" \
"v_lshl_or_b32 v230, v230, 1, v233\n" \
"v_mfma_f32_16x16x32_bf16 v[84:87], a[208:211], a[104:107], v[84:87]\n" \
"ds_read_b64_tr_b16 a[234:235], v220 offset:63488\n" \
"s_add_u32 s63, s40, 29184\n" \
"v_mfma_f32_16x16x32_bf16 v[84:87], a[212:215], a[108:111], v[84:87]\n" \
"buffer_load_dwordx4 a[156:159], v208, %[srdz], s50 offen offset:3072 sc1\n" \
"v_mfma_f32_16x16x32_bf16 v[116:119], a[208:211], a[120:123], v[116:119]\n" \
"v_mad_u32_u24 %[vg0], v230, 48, s63\n" \
"s_add_u32 s63, s52, 25600\n" \
"v_mfma_f32_16x16x32_bf16 v[116:119], a[212:215], a[124:127], v[116:119]\n" \
"s_waitcnt vmcnt(20) lgkmcnt(4)\n" \
"s_bitcmp1_b32 0x30, s48\n" \
"s_cbranch_scc1 Lcw9_%=\n" \
"s_waitcnt vmcnt(13)\n" \
"Lcw9_%=:\n" \
"v_mfma_f32_16x16x32_bf16 v[28:31], a[216:219], a[64:67], v[28:31]\n" \
"v_lshlrev_b32_e64 v235, 6, v218\n" \
"v_mfma_f32_16x16x32_bf16 v[28:31], a[228:231], a[68:71], v[28:31]\n" \
"s_cbranch_vccz Lg20_%=\n" \
"s_nop 0\n" \
"buffer_store_dwordx4 v[120:123], v210, %[srdz], s60 offen offset:3072 sc1\n" \
"Lg20_%=:\n" \
"v_mfma_f32_16x16x32_bf16 v[60:63], a[216:219], a[80:83], v[60:63]\n" \
"v_add_u32_e32 v234, s63, v235\n" \
"s_movk_i32 s52, 272\n" \
"v_mfma_f32_16x16x32_bf16 v[60:63], a[228:231], a[84:87], v[60:63]\n" \
"buffer_load_dwordx4 v214, %[srdt], s49 offen offset:3072 lds\n" \
"v_mfma_f32_16x16x32_bf16 v[92:95], a[216:219], a[96:99], v[92:95]\n" \
"v_mad_u32_u24 %[kd0], v216, s52, v234\n" \
"v_mfma_f32_16x16x32_bf16 v[92:95], a[228:231], a[100:103], v[92:95]\n" \
"buffer_load_dwordx4 a[160:163], v209, %[srdz], s50 offen offset:0 sc1\n" \
"v_mfma_f32_16x16x32_bf16 v[124:127], a[216:219], a[112:115], v[124:127]\n" \
"ds_read_b64_tr_b16 a[236:237], v221 offset:49152\n" \
"v_bfe_u32 v234, v222, 1, 2\n" \
"v_mfma_f32_16x16x32_bf16 v[124:127], a[228:231], a[116:119], v[124:127]\n" \
"ds_read_b64_tr_b16 a[238:239], v221 offset:51200\n" \
"v_lshl_add_u32 %[vx0], v234, 4, v235\n" \
"v_mfma_f32_16x16x32_bf16 v[192:195], a[196:199], a[0:3], v[128:131]\n" \
"ds_read_b64_tr_b16 a[208:209], v221 offset:53248\n" \
"s_add_u32 s63, s40, 25600\n" \
"v_mfma_f32_16x16x32_bf16 v[192:195], a[200:203], a[4:7], v[192:195]\n" \
"ds_read_b64_tr_b16 a[210:211], v221 offset:55296\n" \
"v_add_u32_e64 %[vx0], s63, %[vx0]\n" \
"v_mfma_f32_16x16x32_bf16 v[196:199], a[196:199], a[16:19], v[132:135]\n" \
"buffer_load_dwordx4 a[164:167], v209, %[srdz], s50 offen offset:1024 sc1\n" \
"v_mfma_f32_16x16x32_bf16 v[196:199], a[200:203], a[20:23], v[196:199]\n" \
"s_movk_i32 s52, 2112\n" \
"s_nop 0\n" \
"v_mfma_f32_16x16x32_bf16 v[200:203], a[196:199], a[32:35], v[136:139]\n" \
"v_mul_u32_u24_e64 v211, s52, v216\n" \
"v_mfma_f32_16x16x32_bf16 v[200:203], a[200:203], a[36:39], v[200:203]\n" \
"v_add_u32_e32 v211, 16384, v211\n" \
"v_mfma_f32_16x16x32_bf16 v[204:207], a[196:199], a[48:51], v[140:143]\n" \
"ds_read_b64_tr_b16 a[212:213], v221 offset:57344\n" \
"s_sub_u32 s61, s48, 6\n" \
"s_cmp_lt_u32 s61, 2\n" \
"s_cselect_b64 vcc, -1, 0\n" \
"s_nop 0\n" \
"v_mov_b32_e32 v235, 2048\n" \
"v_mfma_f32_16x16x32_bf16 v[204:207], a[200:203], a[52:55], v[204:207]\n" \
"s_waitcnt vmcnt(21) lgkmcnt(5)\n" \
"s_bitcmp1_b32 0x30, s48\n" \
"s_cbranch_scc1 Lcw10_%=\n" \
"s_waitcnt vmcnt(14)\n" \
"Lcw10_%=:\n" \
"v_mfma_f32_16x16x32_bf16 v[192:195], a[204:207], a[8:11], v[192:195]\n" \
"ds_read_b64_tr_b16 a[214:215], v221 offset:59392\n" \
"s_cbranch_vccz Lg21_%=\n" \
"s_nop 0\n" \
"buffer_store_dwordx4 v[0:3], %[oq0], %[srdz], s60 offen offset:0 sc1\n" \
"Lg21_%=:\n" \
"v_mfma_f32_16x16x32_bf16 v[192:195], a[232:235], a[12:15], v[192:195]\n" \
"ds_read_b64_tr_b16 a[216:217], v221 offset:61440\n" \
"v_cmp_eq_u32_e64 s[68:69], 4, v216\n" \
"v_mfma_f32_16x16x32_bf16 v[196:199], a[204:207], a[24:27], v[196:199]\n" \
"ds_read_b64_tr_b16 a[218:219], v221 offset:63488\n" \
"s_lshl_b32 s52, %[wid], 9\n" \
"s_nop 0\n" \
"v_mfma_f32_16x16x32_bf16 v[196:199], a[232:235], a[28:31], v[196:199]\n" \
"buffer_load_dwordx4 a[168:171], v209, %[srdz], s50 offen offset:2048 sc1\n" \
"v_mfma_f32_16x16x32_bf16 v[200:203], a[204:207], a[40:43], v[200:203]\n" \
"v_cndmask_b32_e64 v235, 0, v235, s[68:69]\n" \
"v_mfma_f32_16x16x32_bf16 v[200:203], a[232:235], a[44:47], v[200:203]\n" \
"v_cmp_gt_u32_e64 s[68:69], 4, v216\n" \
"s_add_u32 s52, s52, s40\n" \
"s_nop 0\n" \
"v_mfma_f32_16x16x32_bf16 v[204:207], a[204:207], a[56:59], v[204:207]\n" \
"ds_read_b64_tr_b16 a[228:229], v220 offset:49664\n" \
"v_cndmask_b32_e64 v211, v235, v211, s[68:69]\n" \
"v_mfma_f32_16x16x32_bf16 v[204:207], a[232:235], a[60:63], v[204:207]\n" \
"ds_read_b64_tr_b16 a[230:231], v220 offset:51712\n" \
"v_lshlrev_b32_e64 v235, 4, v218\n" \
"v_mfma_f32_16x16x32_bf16 v[28:31], a[220:223], a[72:75], v[28:31]\n" \
"s_cbranch_vccz Lg22_%=\n" \
"s_nop 0\n" \
"buffer_store_dwordx4 v[8:11], %[oq0], %[srdz], s60 offen offset:1024 sc1\n" \
"Lg22_%=:\n" \
"v_mfma_f32_16x16x32_bf16 v[28:31], a[224:227], a[76:79], v[28:31]\n" \
"v_add3_u32 v211, v211, v235, s52\n" \
"v_mfma_f32_16x16x32_bf16 v[60:63], a[220:223], a[88:91], v[60:63]\n" \
"buffer_load_dwordx4 a[172:175], v209, %[srdz], s50 offen offset:3072 sc1\n" \
"v_mfma_f32_16x16x32_bf16 v[60:63], a[224:227], a[92:95], v[60:63]\n" \
"v_and_b32_e32 v216, 0x2b0, v217\n" \
"v_mfma_f32_16x16x32_bf16 v[92:95], a[220:223], a[104:107], v[92:95]\n" \
"v_lshrrev_b32_e64 v218, 2, v217\n" \
"v_mfma_f32_16x16x32_bf16 v[92:95], a[224:227], a[108:111], v[92:95]\n" \
"v_cmp_u_f32_e64 s[44:45], v192, v196\n" \
"v_and_or_b32 v216, v218, 64, v216\n" \
"s_add_u32 s52, s40, s42\n" \
"s_nop 0\n" \
"v_mfma_f32_16x16x32_bf16 v[124:127], a[220:223], a[120:123], v[124:127]\n" \
"s_add_u32 m0, %[m0b], 16384\n" \
"s_cmp_gt_u32 s48, 2\n" \
"s_cselect_b32 s49, 2, 3\n" \
"s_lshl_b32 s49, s49, 18\n" \
"s_or_b32 s49, s49, s59\n" \
"buffer_load_dwordx4 %[wo0], %[srdt], s49 offen offset:0 lds\n" \
"v_mfma_f32_16x16x32_bf16 v[124:127], a[224:227], a[124:127], v[124:127]\n" \
"v_cmp_u_f32_e64 s[56:57], v200, v204\n" \
"s_or_b64 s[56:57], s[56:57], s[44:45]\n" \
"s_cbranch_scc1 Lsp2_%=\n" \
"Lbk2_%=:\n" \
"s_waitcnt vmcnt(63) lgkmcnt(2)\n" \
"s_nop 0\n" \
"v_mfma_f32_16x16x32_bf16 v[144:147], a[236:239], a[0:3], v[144:147]\n" \
"ds_read_b64_tr_b16 a[220:221], v220 offset:53760\n" \
"v_add_u32_e64 %[kb0], s52, v216\n" \
"v_mfma_f32_16x16x32_bf16 v[144:147], a[208:211], a[4:7], v[144:147]\n" \
"ds_read_b64_tr_b16 a[222:223], v220 offset:55808\n" \
"s_cbranch_vccz Lg23_%=\n" \
"s_nop 0\n" \
"buffer_store_dwordx4 v[16:19], %[oq0], %[srdz], s60 offen offset:2048 sc1\n" \
"Lg23_%=:\n" \
"v_mfma_f32_16x16x32_bf16 v[148:151], a[236:239], a[16:19], v[148:151]\n" \
"ds_read_b64_tr_b16 a[196:197], v220 offset:57856\n" \
"v_xor_b32_e32 %[vvb], 0x100, %[kb0]\n" \
"v_mfma_f32_16x16x32_bf16 v[148:151], a[208:211], a[20:23], v[148:151]\n" \
"ds_read_b64_tr_b16 a[198:199], v220 offset:59904\n" \
"buffer_load_dwordx4 a[176:179], v210, %[srdz], s50 offen offset:0 sc1\n" \
"v_mfma_f32_16x16x32_bf16 v[152:155], a[236:239], a[32:35], v[152:155]\n" \
"ds_read_b64_tr_b16 a[200:201], v220 offset:61952\n" \
"v_and_b32_e32 v235, 0x200, v217\n" \
"v_mfma_f32_16x16x32_bf16 v[152:155], a[208:211], a[36:39], v[152:155]\n" \
"ds_read_b64_tr_b16 a[202:203], v220 offset:64000\n" \
"v_and_b32_e64 v234, 8, v222\n" \
"v_mfma_f32_16x16x32_bf16 v[156:159], a[236:239], a[48:51], v[156:159]\n" \
"ds_read_b64_tr_b16 a[204:205], v221 offset:49664\n" \
"v_lshl_or_b32 v235, v234, 5, v235\n" \
"v_mfma_f32_16x16x32_bf16 v[156:159], a[208:211], a[52:55], v[156:159]\n" \
"ds_read_b64_tr_b16 a[206:207], v221 offset:51712\n" \
"v_and_b32_e64 v234, 16, v222\n" \
"v_mfma_f32_16x16x32_bf16 v[144:147], a[212:215], a[8:11], v[144:147]\n" \
"ds_read_b64_tr_b16 a[224:225], v221 offset:53760\n" \
"v_lshl_or_b32 v235, v234, 1, v235\n" \
"v_mfma_f32_16x16x32_bf16 v[144:147], a[216:219], a[12:15], v[144:147]\n" \
"ds_read_b64_tr_b16 a[226:227], v221 offset:55808\n" \
"v_lshl_or_b32 v235, v233, 6, v235\n" \
"v_mfma_f32_16x16x32_bf16 v[148:151], a[212:215], a[24:27], v[148:151]\n" \
"s_cbranch_vccz Lg24_%=\n" \
"s_nop 0\n" \
"buffer_store_dwordx4 v[24:27], %[oq0], %[srdz], s60 offen offset:3072 sc1\n" \
"Lg24_%=:\n" \
"v_mfma_f32_16x16x32_bf16 v[148:151], a[216:219], a[28:31], v[148:151]\n" \
"v_and_b32_e64 v216, 2, v222\n" \
"v_mfma_f32_16x16x32_bf16 v[152:155], a[212:215], a[40:43], v[152:155]\n" \
"buffer_load_dwordx4 a[180:183], v210, %[srdz], s50 offen offset:1024 sc1\n" \
"v_mfma_f32_16x16x32_bf16 v[152:155], a[216:219], a[44:47], v[152:155]\n" \
"v_lshl_or_b32 v235, v216, 3, v235\n" \
"s_lshr_b32 s52, s42, 1\n" \
"s_nop 0\n" \
"v_mfma_f32_16x16x32_bf16 v[156:159], a[212:215], a[56:59], v[156:159]\n" \
"ds_read_b64_tr_b16 a[232:233], v221 offset:57856\n" \
"v_sub_u32_e32 v234, 0x7ff, %[ta0]\n" \
"v_mfma_f32_16x16x32_bf16 v[156:159], a[216:219], a[60:63], v[156:159]\n" \
"s_waitcnt vmcnt(63) lgkmcnt(5)\n" \
"s_nop 0\n" \
"v_mfma_f32_16x16x32_bf16 v[160:163], a[228:231], a[0:3], v[160:163]\n" \
"ds_read_b64_tr_b16 a[234:235], v221 offset:59904\n" \
"buffer_load_dwordx4 v212, %[srdt], s49 offen offset:1024 lds\n" \
"v_mfma_f32_16x16x32_bf16 v[160:163], a[220:223], a[4:7], v[160:163]\n" \
"ds_read_b64_tr_b16 a[236:237], v221 offset:61952\n" \
"v_lshlrev_b32_e64 v234, 11, v234\n" \
"v_mfma_f32_16x16x32_bf16 v[164:167], a[228:231], a[16:19], v[164:167]\n" \
"ds_read_b64_tr_b16 a[238:239], v221 offset:64000\n" \
"v_add3_u32 %[vd0], v234, v235, s52\n" \
"v_mfma_f32_16x16x32_bf16 v[164:167], a[220:223], a[20:23], v[164:167]\n" \
"s_cbranch_vccz Lg25_%=\n" \
"s_nop 0\n" \
"buffer_store_dwordx4 v[32:35], v208, %[srdz], s60 offen offset:0 sc1\n" \
"Lg25_%=:\n" \
"v_mfma_f32_16x16x32_bf16 v[168:171], a[228:231], a[32:35], v[168:171]\n" \
"v_mfma_f32_16x16x32_bf16 v[168:171], a[220:223], a[36:39], v[168:171]\n" \
"buffer_load_dwordx4 a[184:187], v210, %[srdz], s50 offen offset:2048 sc1\n" \
"v_mfma_f32_16x16x32_bf16 v[172:175], a[228:231], a[48:51], v[172:175]\n" \
"v_mfma_f32_16x16x32_bf16 v[172:175], a[220:223], a[52:55], v[172:175]\n" \
"buffer_load_dwordx4 a[188:191], v210, %[srdz], s50 offen offset:3072 sc1\n" \
"v_mfma_f32_16x16x32_bf16 v[160:163], a[196:199], a[8:11], v[160:163]\n" \
"v_mfma_f32_16x16x32_bf16 v[160:163], a[200:203], a[12:15], v[160:163]\n" \
"v_mfma_f32_16x16x32_bf16 v[164:167], a[196:199], a[24:27], v[164:167]\n" \
"v_mfma_f32_16x16x32_bf16 v[164:167], a[200:203], a[28:31], v[164:167]\n" \
"v_mfma_f32_16x16x32_bf16 v[168:171], a[196:199], a[40:43], v[168:171]\n" \
"s_waitcnt vmcnt(63) lgkmcnt(0)\n" \
"s_bitcmp1_b32 0x30, s48\n" \
"s_cbranch_scc1 Lcw11_%=\n" \
"s_waitcnt vmcnt(60)\n" \
"s_bitcmp1_b32 0xc0, s48\n" \
"s_cbranch_scc1 Lcw11_%=\n" \
"s_waitcnt vmcnt(55)\n" \
"Lcw11_%=:\n" \
"s_barrier\n" \
"s_nop 0\n" \
"v_mfma_f32_16x16x32_bf16 v[168:171], a[200:203], a[44:47], v[168:171]\n" \
"ds_read_b64_tr_b16 a[208:209], v215 offset:49152\n" \
"s_cbranch_vccz Lg26_%=\n" \
"s_nop 0\n" \
"buffer_store_dwordx4 v[40:43], v208, %[srdz], s60 offen offset:1024 sc1\n" \
"Lg26_%=:\n" \
"v_mfma_f32_16x16x32_bf16 v[172:175], a[196:199], a[56:59], v[172:175]\n" \
"ds_read_b64_tr_b16 a[210:211], v215 offset:51200\n" \
"v_mfma_f32_16x16x32_bf16 v[172:175], a[200:203], a[60:63], v[172:175]\n" \
"s_waitcnt vmcnt(63) lgkmcnt(2)\n" \
"s_nop 0\n" \
"v_mfma_f32_16x16x32_bf16 v[176:179], a[204:207], a[0:3], v[176:179]\n" \
"ds_read_b64_tr_b16 a[212:213], v215 offset:53248\n" \
"global_load_dword v237, %[vct], %[pct]\n" \
"v_mfma_f32_16x16x32_bf16 v[176:179], a[224:227], a[4:7], v[176:179]\n" \
"ds_read_b64_tr_b16 a[214:215], v215 offset:55296\n" \
"v_mfma_f32_16x16x32_bf16 v[180:183], a[204:207], a[16:19], v[180:183]\n" \
"ds_read_b64_tr_b16 a[216:217], v215 offset:57344\n" \
"buffer_load_dwordx4 v213, %[srdt], s49 offen offset:2048 lds\n" \
"v_mfma_f32_16x16x32_bf16 v[180:183], a[224:227], a[20:23], v[180:183]\n" \
"ds_read_b64_tr_b16 a[218:219], v215 offset:59392\n" \
"v_mfma_f32_16x16x32_bf16 v[184:187], a[204:207], a[32:35], v[184:187]\n" \
"ds_read_b64_tr_b16 a[228:229], v215 offset:61440\n" \
"v_mfma_f32_16x16x32_bf16 v[184:187], a[224:227], a[36:39], v[184:187]\n" \
"ds_read_b64_tr_b16 a[230:231], v215 offset:63488\n" \
"s_cbranch_vccz Lg27_%=\n" \
"s_nop 0\n" \
"buffer_store_dwordx4 v[48:51], v208, %[srdz], s60 offen offset:2048 sc1\n" \
"Lg27_%=:\n" \
"v_mfma_f32_16x16x32_bf16 v[188:191], a[204:207], a[48:51], v[188:191]\n" \
"ds_read_b64_tr_b16 a[220:221], v227 offset:49152\n" \
"v_mfma_f32_16x16x32_bf16 v[188:191], a[224:227], a[52:55], v[188:191]\n" \
"ds_read_b64_tr_b16 a[222:223], v227 offset:51200\n" \
"v_mfma_f32_16x16x32_bf16 v[176:179], a[232:235], a[8:11], v[176:179]\n" \
"ds_read_b64_tr_b16 a[196:197], v227 offset:53248\n" \
"v_mfma_f32_16x16x32_bf16 v[176:179], a[236:239], a[12:15], v[176:179]\n" \
"ds_read_b64_tr_b16 a[198:199], v227 offset:55296\n" \
"s_cmp_gt_u32 s48, 3\n" \
"s_cselect_b32 s50, 3, 4\n" \
"s_mul_i32 s50, s50, 152064\n" \
"buffer_load_dwordx4 a[64:67], %[oq0], %[srdz], s50 offen offset:0 sc1\n" \
"v_mfma_f32_16x16x32_bf16 v[180:183], a[232:235], a[24:27], v[180:183]\n" \
"v_mfma_f32_16x16x32_bf16 v[180:183], a[236:239], a[28:31], v[180:183]\n" \
"buffer_load_dwordx4 v214, %[srdt], s49 offen offset:3072 lds\n" \
"v_mfma_f32_16x16x32_bf16 v[184:187], a[232:235], a[40:43], v[184:187]\n" \
"v_mfma_f32_16x16x32_bf16 v[184:187], a[236:239], a[44:47], v[184:187]\n" \
"v_mfma_f32_16x16x32_bf16 v[188:191], a[232:235], a[56:59], v[188:191]\n" \
"ds_read_b64_tr_b16 a[200:201], v227 offset:57344\n" \
"s_cbranch_vccz Lg28_%=\n" \
"s_nop 0\n" \
"buffer_store_dwordx4 v[56:59], v208, %[srdz], s60 offen offset:3072 sc1\n" \
"Lg28_%=:\n" \
"v_mfma_f32_16x16x32_bf16 v[188:191], a[236:239], a[60:63], v[188:191]\n" \
"s_waitcnt vmcnt(63) lgkmcnt(5)\n" \
"s_nop 0\n" \
"v_mfma_f32_16x16x32_bf16 v[4:7], a[208:211], a[0:3], v[4:7]\n" \
"ds_read_b64_tr_b16 a[202:203], v227 offset:59392\n" \
"buffer_load_dwordx4 a[68:71], %[oq0], %[srdz], s50 offen offset:1024 sc1\n" \
"v_mfma_f32_16x16x32_bf16 v[4:7], a[212:215], a[4:7], v[4:7]\n" \
"ds_read_b64_tr_b16 a[204:205], v227 offset:61440\n" \
"v_mfma_f32_16x16x32_bf16 v[36:39], a[208:211], a[16:19], v[36:39]\n" \
"ds_read_b64_tr_b16 a[206:207], v227 offset:63488\n" \
"v_mfma_f32_16x16x32_bf16 v[36:39], a[212:215], a[20:23], v[36:39]\n" \
"v_mfma_f32_16x16x32_bf16 v[68:71], a[208:211], a[32:35], v[68:71]\n" \
"buffer_load_dwordx4 a[72:75], %[oq0], %[srdz], s50 offen offset:2048 sc1\n" \
"v_mfma_f32_16x16x32_bf16 v[68:71], a[212:215], a[36:39], v[68:71]\n" \
"v_mfma_f32_16x16x32_bf16 v[100:103], a[208:211], a[48:51], v[100:103]\n" \
"ds_read_b64_tr_b16 a[224:225], v215 offset:49664\n" \
"v_mfma_f32_16x16x32_bf16 v[100:103], a[212:215], a[52:55], v[100:103]\n" \
"ds_read_b64_tr_b16 a[226:227], v215 offset:51712\n" \
"s_cbranch_vccz Lg29_%=\n" \
"s_nop 0\n" \
"buffer_store_dwordx4 v[64:67], v209, %[srdz], s60 offen offset:0 sc1\n" \
"Lg29_%=:\n" \
"v_mfma_f32_16x16x32_bf16 v[4:7], a[216:219], a[8:11], v[4:7]\n" \
"ds_read_b64_tr_b16 a[232:233], v215 offset:53760\n" \
"v_mfma_f32_16x16x32_bf16 v[4:7], a[228:231], a[12:15], v[4:7]\n" \
"ds_read_b64_tr_b16 a[234:235], v215 offset:55808\n" \
"v_mfma_f32_16x16x32_bf16 v[36:39], a[216:219], a[24:27], v[36:39]\n" \
"v_mfma_f32_16x16x32_bf16 v[36:39], a[228:231], a[28:31], v[36:39]\n" \
"buffer_load_dwordx4 a[76:79], %[oq0], %[srdz], s50 offen offset:3072 sc1\n" \
"v_mfma_f32_16x16x32_bf16 v[68:71], a[216:219], a[40:43], v[68:71]\n" \
"v_mfma_f32_16x16x32_bf16 v[68:71], a[228:231], a[44:47], v[68:71]\n" \
"v_mfma_f32_16x16x32_bf16 v[100:103], a[216:219], a[56:59], v[100:103]\n" \
"ds_read_b64_tr_b16 a[236:237], v215 offset:57856\n" \
"v_mfma_f32_16x16x32_bf16 v[100:103], a[228:231], a[60:63], v[100:103]\n" \
"s_waitcnt vmcnt(63) lgkmcnt(5)\n" \
"s_nop 0\n" \
"v_mfma_f32_16x16x32_bf16 v[12:15], a[220:223], a[0:3], v[12:15]\n" \
"ds_read_b64_tr_b16 a[238:239], v215 offset:59904\n" \
"s_cbranch_vccz Lg30_%=\n" \
"s_nop 0\n" \
"buffer_store_dwordx4 v[72:75], v209, %[srdz], s60 offen offset:1024 sc1\n" \
"Lg30_%=:\n" \
"v_mfma_f32_16x16x32_bf16 v[12:15], a[196:199], a[4:7], v[12:15]\n" \
"ds_read_b64_tr_b16 a[208:209], v215 offset:61952\n" \
"v_mfma_f32_16x16x32_bf16 v[44:47], a[220:223], a[16:19], v[44:47]\n" \
"ds_read_b64_tr_b16 a[210:211], v215 offset:64000\n" \
"v_mfma_f32_16x16x32_bf16 v[44:47], a[196:199], a[20:23], v[44:47]\n" \
"buffer_load_dwordx4 a[80:83], v208, %[srdz], s50 offen offset:0 sc1\n" \
"v_mfma_f32_16x16x32_bf16 v[76:79], a[220:223], a[32:35], v[76:79]\n" \
"v_mfma_f32_16x16x32_bf16 v[76:79], a[196:199], a[36:39], v[76:79]\n" \
"s_add_u32 m0, %[m0b], 32768\n" \
"s_cmp_gt_u32 s48, 3\n" \
"s_cselect_b32 s49, 3, 4\n" \
"s_lshl_b32 s49, s49, 18\n" \
"s_or_b32 s49, s49, s58\n" \
"buffer_load_dwordx4 %[wo0], %[srdt], s49 offen offset:0 lds\n" \
"v_mfma_f32_16x16x32_bf16 v[108:111], a[220:223], a[48:51], v[108:111]\n" \
"ds_read_b64_tr_b16 a[212:213], v227 offset:49664\n" \
"v_mfma_f32_16x16x32_bf16 v[108:111], a[196:199], a[52:55], v[108:111]\n" \
"ds_read_b64_tr_b16 a[214:215], v227 offset:51712\n" \
"v_mfma_f32_16x16x32_bf16 v[12:15], a[200:203], a[8:11], v[12:15]\n" \
"ds_read_b64_tr_b16 a[216:217], v227 offset:57856\n" \
"v_mfma_f32_16x16x32_bf16 v[12:15], a[204:207], a[12:15], v[12:15]\n" \
"ds_read_b64_tr_b16 a[218:219], v227 offset:59904\n" \
"s_cbranch_vccz Lg31_%=\n" \
"s_nop 0\n" \
"buffer_store_dwordx4 v[80:83], v209, %[srdz], s60 offen offset:2048 sc1\n" \
"Lg31_%=:\n" \
"v_mfma_f32_16x16x32_bf16 v[44:47], a[200:203], a[24:27], v[44:47]\n" \
"ds_read_b64_tr_b16 a[228:229], v227 offset:53760\n" \
"v_mfma_f32_16x16x32_bf16 v[44:47], a[204:207], a[28:31], v[44:47]\n" \
"ds_read_b64_tr_b16 a[230:231], v227 offset:55808\n" \
"v_mfma_f32_16x16x32_bf16 v[76:79], a[200:203], a[40:43], v[76:79]\n" \
"ds_read_b64_tr_b16 a[220:221], v227 offset:61952\n" \
"buffer_load_dwordx4 a[84:87], v208, %[srdz], s50 offen offset:1024 sc1\n" \
"v_mfma_f32_16x16x32_bf16 v[76:79], a[204:207], a[44:47], v[76:79]\n" \
"ds_read_b64_tr_b16 a[222:223], v227 offset:64000\n" \
"v_mfma_f32_16x16x32_bf16 v[108:111], a[200:203], a[56:59], v[108:111]\n" \
"v_mfma_f32_16x16x32_bf16 v[108:111], a[204:207], a[60:63], v[108:111]\n" \
"s_waitcnt vmcnt(63) lgkmcnt(12)\n" \
"s_nop 0\n" \
"v_mfma_f32_16x16x32_bf16 v[20:23], a[224:227], a[0:3], v[20:23]\n" \
"v_mfma_f32_16x16x32_bf16 v[20:23], a[232:235], a[4:7], v[20:23]\n" \
"v_mfma_f32_16x16x32_bf16 v[52:55], a[224:227], a[16:19], v[52:55]\n" \
"s_cbranch_vccz Lg32_%=\n" \
"s_nop 0\n" \
"buffer_store_dwordx4 v[88:91], v209, %[srdz], s60 offen offset:3072 sc1\n" \
"Lg32_%=:\n" \
"v_mfma_f32_16x16x32_bf16 v[52:55], a[232:235], a[20:23], v[52:55]\n" \
"v_mfma_f32_16x16x32_bf16 v[84:87], a[224:227], a[32:35], v[84:87]\n" \
"s_waitcnt vmcnt(32) lgkmcnt(0)\n" \
"s_nop 0\n" \
"s_bitcmp1_b32 0xc0, s48\n" \
"s_cbranch_scc1 Lcw12_%=\n" \
"s_waitcnt vmcnt(20)\n" \
"Lcw12_%=:\n" \
"s_barrier\n" \
"s_nop 0\n" \
"v_mfma_f32_16x16x32_bf16 v[84:87], a[232:235], a[36:39], v[84:87]\n" \
"ds_read_b64_tr_b16 a[196:197], v215 offset:0\n" \
"buffer_load_dwordx4 a[88:91], v208, %[srdz], s50 offen offset:2048 sc1\n" \
"v_mfma_f32_16x16x32_bf16 v[116:119], a[224:227], a[48:51], v[116:119]\n" \
"ds_read_b64_tr_b16 a[198:199], v215 offset:2048\n" \
"v_mfma_f32_16x16x32_bf16 v[116:119], a[232:235], a[52:55], v[116:119]\n" \
"ds_read_b64_tr_b16 a[200:201], v215 offset:4096\n" \
"buffer_load_dwordx4 v212, %[srdt], s49 offen offset:1024 lds\n" \
"v_mfma_f32_16x16x32_bf16 v[20:23], a[236:239], a[8:11], v[20:23]\n" \
"ds_read_b64_tr_b16 a[202:203], v215 offset:6144\n" \
"v_mfma_f32_16x16x32_bf16 v[20:23], a[208:211], a[12:15], v[20:23]\n" \
"ds_read_b64_tr_b16 a[204:205], v215 offset:8192\n" \
"v_mfma_f32_16x16x32_bf16 v[52:55], a[236:239], a[24:27], v[52:55]\n" \
"ds_read_b64_tr_b16 a[206:207], v215 offset:10240\n" \
"v_mfma_f32_16x16x32_bf16 v[52:55], a[208:211], a[28:31], v[52:55]\n" \
"ds_read_b64_tr_b16 a[224:225], v215 offset:12288\n" \
"s_cbranch_vccz Lg33_%=\n" \
"s_nop 0\n" \
"buffer_store_dwordx4 v[96:99], v210, %[srdz], s60 offen offset:0 sc1\n" \
"Lg33_%=:\n" \
"v_mfma_f32_16x16x32_bf16 v[84:87], a[236:239], a[40:43], v[84:87]\n" \
"ds_read_b64_tr_b16 a[226:227], v215 offset:14336\n" \
"v_mfma_f32_16x16x32_bf16 v[84:87], a[208:211], a[44:47], v[84:87]\n" \
"buffer_load_dwordx4 a[92:95], v208, %[srdz], s50 offen offset:3072 sc1\n" \
"v_mfma_f32_16x16x32_bf16 v[116:119], a[236:239], a[56:59], v[116:119]\n" \
"v_mfma_f32_16x16x32_bf16 v[116:119], a[208:211], a[60:63], v[116:119]\n" \
"s_waitcnt vmcnt(25) lgkmcnt(4)\n" \
"s_nop 0\n" \
"s_bitcmp1_b32 0xc0, s48\n" \
"s_cbranch_scc1 Lcw13_%=\n" \
"s_waitcnt vmcnt(16)\n" \
"Lcw13_%=:\n" \
"v_mfma_f32_16x16x32_bf16 v[28:31], a[212:215], a[0:3], v[28:31]\n" \
"buffer_load_dwordx4 a[96:99], v209, %[srdz], s50 offen offset:0 sc1\n" \
"v_mfma_f32_16x16x32_bf16 v[28:31], a[228:231], a[4:7], v[28:31]\n" \
"v_mfma_f32_16x16x32_bf16 v[60:63], a[212:215], a[16:19], v[60:63]\n" \
"buffer_load_dwordx4 v213, %[srdt], s49 offen offset:2048 lds\n" \
"v_mfma_f32_16x16x32_bf16 v[60:63], a[228:231], a[20:23], v[60:63]\n" \
"v_mfma_f32_16x16x32_bf16 v[92:95], a[212:215], a[32:35], v[92:95]\n" \
"s_cbranch_vccz Lg34_%=\n" \
"s_nop 0\n" \
"buffer_store_dwordx4 v[104:107], v210, %[srdz], s60 offen offset:1024 sc1\n" \
"Lg34_%=:\n" \
"v_mfma_f32_16x16x32_bf16 v[92:95], a[228:231], a[36:39], v[92:95]\n" \
"v_mfma_f32_16x16x32_bf16 v[124:127], a[212:215], a[48:51], v[124:127]\n" \
"ds_read_b64_tr_b16 a[232:233], v227 offset:0\n" \
"v_mfma_f32_16x16x32_bf16 v[124:127], a[228:231], a[52:55], v[124:127]\n" \
"ds_read_b64_tr_b16 a[234:235], v227 offset:2048\n" \
"v_mfma_f32_16x16x32_bf16 v[128:131], a[196:199], a[128:131], v[192:195]\n" \
"ds_read_b64_tr_b16 a[236:237], v227 offset:4096\n" \
"v_mfma_f32_16x16x32_bf16 v[128:131], a[200:203], a[132:135], v[128:131]\n" \
"ds_read_b64_tr_b16 a[238:239], v227 offset:6144\n" \
".if . - adaln_body != 49152\n" \
".error \"size model\"\n" \
".endif\n" \
"Lk11_%=:\n" \
"s_cbranch_execz adaln_kr\n" \
"s_nop 0\n" \
"buffer_load_dwordx4 a[100:103], v209, %[srdz], s50 offen offset:1024 sc1\n" \
"v_mfma_f32_16x16x32_bf16 v[132:135], a[196:199], a[144:147], v[196:199]\n" \
"v_mfma_f32_16x16x32_bf16 v[132:135], a[200:203], a[148:151], v[132:135]\n" \
"v_mfma_f32_16x16x32_bf16 v[136:139], a[196:199], a[160:163], v[200:203]\n" \
"v_mfma_f32_16x16x32_bf16 v[136:139], a[200:203], a[164:167], v[136:139]\n" \
"s_cbranch_vccz Lg35_%=\n" \
"s_nop 0\n" \
"buffer_store_dwordx4 v[112:115], v210, %[srdz], s60 offen offset:2048 sc1\n" \
"Lg35_%=:\n" \
"v_mfma_f32_16x16x32_bf16 v[140:143], a[196:199], a[176:179], v[204:207]\n" \
"ds_read_b64_tr_b16 a[208:209], v227 offset:8192\n" \
"v_mfma_f32_16x16x32_bf16 v[140:143], a[200:203], a[180:183], v[140:143]\n" \
"s_waitcnt vmcnt(26) lgkmcnt(5)\n" \
"s_nop 0\n" \
"s_bitcmp1_b32 0xc0, s48\n" \
"s_cbranch_scc1 Lcw14_%=\n" \
"s_waitcnt vmcnt(16)\n" \
"Lcw14_%=:\n" \
"v_mfma_f32_16x16x32_bf16 v[128:131], a[204:207], a[136:139], v[128:131]\n" \
"ds_read_b64_tr_b16 a[210:211], v227 offset:10240\n" \
"buffer_load_dwordx4 a[104:107], v209, %[srdz], s50 offen offset:2048 sc1\n" \
"v_mfma_f32_16x16x32_bf16 v[128:131], a[224:227], a[140:143], v[128:131]\n" \
"ds_read_b64_tr_b16 a[212:213], v227 offset:12288\n" \
"v_mfma_f32_16x16x32_bf16 v[132:135], a[204:207], a[152:155], v[132:135]\n" \
"ds_read_b64_tr_b16 a[214:215], v227 offset:14336\n" \
"v_mfma_f32_16x16x32_bf16 v[132:135], a[224:227], a[156:159], v[132:135]\n" \
"buffer_load_dwordx4 v214, %[srdt], s49 offen offset:3072 lds\n" \
"v_mfma_f32_16x16x32_bf16 v[136:139], a[204:207], a[168:171], v[136:139]\n" \
"v_mfma_f32_16x16x32_bf16 v[136:139], a[224:227], a[172:175], v[136:139]\n" \
"v_mfma_f32_16x16x32_bf16 v[140:143], a[204:207], a[184:187], v[140:143]\n" \
"ds_read_b64_tr_b16 a[228:229], v215 offset:512\n" \
"s_cbranch_vccz Lg36_%=\n" \
"s_nop 0\n" \
"buffer_store_dwordx4 v[120:123], v210, %[srdz], s60 offen offset:3072 sc1\n" \
"Lg36_%=:\n" \
"v_mfma_f32_16x16x32_bf16 v[140:143], a[224:227], a[188:191], v[140:143]\n" \
"ds_read_b64_tr_b16 a[230:231], v215 offset:2560\n" \
"v_mfma_f32_16x16x32_bf16 v[28:31], a[216:219], a[8:11], v[28:31]\n" \
"buffer_load_dwordx4 a[108:111], v209, %[srdz], s50 offen offset:3072 sc1\n" \
"v_mfma_f32_16x16x32_bf16 v[28:31], a[220:223], a[12:15], v[28:31]\n" \
"v_mfma_f32_16x16x32_bf16 v[60:63], a[216:219], a[24:27], v[60:63]\n" \
"v_mfma_f32_16x16x32_bf16 v[60:63], a[220:223], a[28:31], v[60:63]\n" \
"v_mfma_f32_16x16x32_bf16 v[92:95], a[216:219], a[40:43], v[92:95]\n" \
"buffer_load_dwordx4 a[112:115], v210, %[srdz], s50 offen offset:0 sc1\n" \
"v_mfma_f32_16x16x32_bf16 v[92:95], a[220:223], a[44:47], v[92:95]\n" \
"v_cmp_u_f32_e64 s[44:45], v128, v132\n" \
"v_mfma_f32_16x16x32_bf16 v[124:127], a[216:219], a[56:59], v[124:127]\n" \
"v_mfma_f32_16x16x32_bf16 v[124:127], a[220:223], a[60:63], v[124:127]\n" \
"v_cmp_u_f32_e64 s[56:57], v136, v140\n" \
"s_or_b64 s[56:57], s[56:57], s[44:45]\n" \
"s_cbranch_scc1 Lsp3_%=\n" \
"Lbk3_%=:\n" \
"s_waitcnt vmcnt(63) lgkmcnt(2)\n" \
"s_nop 0\n" \
"v_mfma_f32_16x16x32_bf16 v[144:147], a[232:235], a[128:131], v[144:147]\n" \
"ds_read_b64_tr_b16 a[216:217], v215 offset:4608\n" \
"v_mfma_f32_16x16x32_bf16 v[144:147], a[236:239], a[132:135], v[144:147]\n" \
"ds_read_b64_tr_b16 a[218:219], v215 offset:6656\n" \
"v_mfma_f32_16x16x32_bf16 v[148:151], a[232:235], a[144:147], v[148:151]\n" \
"ds_read_b64_tr_b16 a[196:197], v215 offset:8704\n" \
"v_mfma_f32_16x16x32_bf16 v[148:151], a[236:239], a[148:151], v[148:151]\n" \
"ds_read_b64_tr_b16 a[198:199], v215 offset:10752\n" \
"v_mfma_f32_16x16x32_bf16 v[152:155], a[232:235], a[160:163], v[152:155]\n" \
"ds_read_b64_tr_b16 a[200:201], v215 offset:12800\n" \
"v_mfma_f32_16x16x32_bf16 v[152:155], a[236:239], a[164:167], v[152:155]\n" \
"ds_read_b64_tr_b16 a[202:203], v215 offset:14848\n" \
"v_mfma_f32_16x16x32_bf16 v[156:159], a[232:235], a[176:179], v[156:159]\n" \
"ds_read_b64_tr_b16 a[204:205], v227 offset:512\n" \
"buffer_load_dwordx4 a[116:119], v210, %[srdz], s50 offen offset:1024 sc1\n" \
"v_mfma_f32_16x16x32_bf16 v[156:159], a[236:239], a[180:183], v[156:159]\n" \
"ds_read_b64_tr_b16 a[206:207], v227 offset:2560\n" \
"v_mfma_f32_16x16x32_bf16 v[144:147], a[208:211], a[136:139], v[144:147]\n" \
"ds_read_b64_tr_b16 a[220:221], v227 offset:4608\n" \
"v_mfma_f32_16x16x32_bf16 v[144:147], a[212:215], a[140:143], v[144:147]\n" \
"ds_read_b64_tr_b16 a[222:223], v227 offset:6656\n" \
"v_mfma_f32_16x16x32_bf16 v[148:151], a[208:211], a[152:155], v[148:151]\n" \
"s_add_u32 m0, %[m0b], 98304\n" \
"s_cmp_gt_u32 s48, 4\n" \
"s_cselect_b32 s49, 4, 5\n" \
"s_lshl_b32 s49, s49, 18\n" \
"s_or_b32 s49, s49, s58\n" \
"buffer_load_dwordx4 %[wo0], %[srdt], s49 offen offset:0 lds\n" \
"v_mfma_f32_16x16x32_bf16 v[148:151], a[212:215], a[156:159], v[148:151]\n" \
"v_mfma_f32_16x16x32_bf16 v[152:155], a[208:211], a[168:171], v[152:155]\n" \
"buffer_load_dwordx4 a[120:123], v210, %[srdz], s50 offen offset:2048 sc1\n" \
"v_mfma_f32_16x16x32_bf16 v[152:155], a[212:215], a[172:175], v[152:155]\n" \
"v_mfma_f32_16x16x32_bf16 v[156:159], a[208:211], a[184:187], v[156:159]\n" \
"ds_read_b64_tr_b16 a[224:225], v227 offset:8704\n" \
"v_mfma_f32_16x16x32_bf16 v[156:159], a[212:215], a[188:191], v[156:159]\n" \
"s_waitcnt vmcnt(63) lgkmcnt(5)\n" \
"s_nop 0\n" \
"v_mfma_f32_16x16x32_bf16 v[160:163], a[228:231], a[128:131], v[160:163]\n" \
"ds_read_b64_tr_b16 a[226:227], v227 offset:10752\n" \
"v_mfma_f32_16x16x32_bf16 v[160:163], a[216:219], a[132:135], v[160:163]\n" \
"ds_read_b64_tr_b16 a[232:233], v227 offset:12800\n" \
"v_mfma_f32_16x16x32_bf16 v[164:167], a[228:231], a[144:147], v[164:167]\n" \
"ds_read_b64_tr_b16 a[234:235], v227 offset:14848\n" \
"v_mfma_f32_16x16x32_bf16 v[164:167], a[216:219], a[148:151], v[164:167]\n" \
"buffer_load_dwordx4 a[124:127], v210, %[srdz], s50 offen offset:3072 sc1\n" \
"v_mfma_f32_16x16x32_bf16 v[168:171], a[228:231], a[160:163], v[168:171]\n" \
"v_mfma_f32_16x16x32_bf16 v[168:171], a[216:219], a[164:167], v[168:171]\n" \
"v_mfma_f32_16x16x32_bf16 v[172:175], a[228:231], a[176:179], v[172:175]\n" \
"v_mfma_f32_16x16x32_bf16 v[172:175], a[216:219], a[180:183], v[172:175]\n" \
"v_mfma_f32_16x16x32_bf16 v[160:163], a[196:199], a[136:139], v[160:163]\n" \
"v_mfma_f32_16x16x32_bf16 v[160:163], a[200:203], a[140:143], v[160:163]\n" \
"buffer_load_dwordx4 v212, %[srdt], s49 offen offset:1024 lds\n" \
"v_mfma_f32_16x16x32_bf16 v[164:167], a[196:199], a[152:155], v[164:167]\n" \
"v_mfma_f32_16x16x32_bf16 v[164:167], a[200:203], a[156:159], v[164:167]\n" \
"v_mfma_f32_16x16x32_bf16 v[168:171], a[196:199], a[168:171], v[168:171]\n" \
"s_waitcnt vmcnt(30) lgkmcnt(0)\n" \
"s_nop 0\n" \
"s_bitcmp1_b32 0xc0, s48\n" \
"s_cbranch_scc1 Lcw15_%=\n" \
"s_waitcnt vmcnt(21)\n" \
"Lcw15_%=:\n" \
"s_barrier\n" \
"s_nop 0\n" \
"v_mfma_f32_16x16x32_bf16 v[168:171], a[200:203], a[172:175], v[168:171]\n" \
"ds_read_b64_tr_b16 a[236:237], v215 offset:16384\n" \
"v_mfma_f32_16x16x32_bf16 v[172:175], a[196:199], a[184:187], v[172:175]\n" \
"ds_read_b64_tr_b16 a[238:239], v215 offset:18432\n" \
"v_mfma_f32_16x16x32_bf16 v[172:175], a[200:203], a[188:191], v[172:175]\n" \
"s_waitcnt vmcnt(63) lgkmcnt(2)\n" \
"s_nop 0\n" \
"v_mfma_f32_16x16x32_bf16 v[176:179], a[204:207], a[128:131], v[176:179]\n" \
"ds_read_b64_tr_b16 a[208:209], v215 offset:20480\n" \
"s_cmp_gt_u32 s48, 4\n" \
"s_cselect_b32 s50, 4, 5\n" \
"s_mul_i32 s50, s50, 152064\n" \
"buffer_load_dwordx4 v[0:3], %[oq0], %[srdz], s50 offen offset:0 sc1\n" \
"v_mfma_f32_16x16x32_bf16 v[176:179], a[220:223], a[132:135], v[176:179]\n" \
"ds_read_b64_tr_b16 a[210:211], v215 offset:22528\n" \
"v_mfma_f32_16x16x32_bf16 v[180:183], a[204:207], a[144:147], v[180:183]\n" \
"ds_read_b64_tr_b16 a[212:213], v215 offset:24576\n" \
"v_mfma_f32_16x16x32_bf16 v[180:183], a[220:223], a[148:151], v[180:183]\n" \
"ds_read_b64_tr_b16 a[214:215], v215 offset:26624\n" \
"v_mfma_f32_16x16x32_bf16 v[184:187], a[204:207], a[160:163], v[184:187]\n" \
"ds_read_b64_tr_b16 a[228:229], v215 offset:28672\n" \
"buffer_load_dwordx4 v[8:11], %[oq0], %[srdz], s50 offen offset:1024 sc1\n" \
"v_mfma_f32_16x16x32_bf16 v[184:187], a[220:223], a[164:167], v[184:187]\n" \
"ds_read_b64_tr_b16 a[230:231], v215 offset:30720\n" \
"v_mfma_f32_16x16x32_bf16 v[188:191], a[204:207], a[176:179], v[188:191]\n" \
"ds_read_b64_tr_b16 a[216:217], v227 offset:16384\n" \
"v_mfma_f32_16x16x32_bf16 v[188:191], a[220:223], a[180:183], v[188:191]\n" \
"ds_read_b64_tr_b16 a[218:219], v227 offset:18432\n" \
"v_mfma_f32_16x16x32_bf16 v[176:179], a[224:227], a[136:139], v[176:179]\n" \
"ds_read_b64_tr_b16 a[196:197], v227 offset:20480\n" \
"buffer_load_dwordx4 v[16:19], %[oq0], %[srdz], s50 offen offset:2048 sc1\n" \
"v_mfma_f32_16x16x32_bf16 v[176:179], a[232:235], a[140:143], v[176:179]\n" \
"ds_read_b64_tr_b16 a[198:199], v227 offset:22528\n" \
"v_mfma_f32_16x16x32_bf16 v[180:183], a[224:227], a[152:155], v[180:183]\n" \
"buffer_load_dwordx4 v213, %[srdt], s49 offen offset:2048 lds\n" \
"v_mfma_f32_16x16x32_bf16 v[180:183], a[232:235], a[156:159], v[180:183]\n" \
"v_mfma_f32_16x16x32_bf16 v[184:187], a[224:227], a[168:171], v[184:187]\n" \
"buffer_load_dwordx4 v[24:27], %[oq0], %[srdz], s50 offen offset:3072 sc1\n" \
"v_mfma_f32_16x16x32_bf16 v[184:187], a[232:235], a[172:175], v[184:187]\n" \
"v_mfma_f32_16x16x32_bf16 v[188:191], a[224:227], a[184:187], v[188:191]\n" \
"ds_read_b64_tr_b16 a[200:201], v227 offset:24576\n" \
"v_mfma_f32_16x16x32_bf16 v[188:191], a[232:235], a[188:191], v[188:191]\n" \
"s_waitcnt vmcnt(63) lgkmcnt(5)\n" \
"s_nop 0\n" \
"v_mfma_f32_16x16x32_bf16 v[4:7], a[236:239], a[128:131], v[4:7]\n" \
"ds_read_b64_tr_b16 a[202:203], v227 offset:26624\n" \
"buffer_load_dwordx4 v[32:35], v208, %[srdz], s50 offen offset:0 sc1\n" \
"v_mfma_f32_16x16x32_bf16 v[4:7], a[208:211], a[132:135], v[4:7]\n" \
"ds_read_b64_tr_b16 a[204:205], v227 offset:28672\n" \
"v_mfma_f32_16x16x32_bf16 v[36:39], a[236:239], a[144:147], v[36:39]\n" \
"ds_read_b64_tr_b16 a[206:207], v227 offset:30720\n" \
"v_mfma_f32_16x16x32_bf16 v[36:39], a[208:211], a[148:151], v[36:39]\n" \
"buffer_load_dwordx4 v[40:43], v208, %[srdz], s50 offen offset:1024 sc1\n" \
"v_mfma_f32_16x16x32_bf16 v[68:71], a[236:239], a[160:163], v[68:71]\n" \
"v_mfma_f32_16x16x32_bf16 v[68:71], a[208:211], a[164:167], v[68:71]\n" \
"buffer_load_dwordx4 v[48:51], v208, %[srdz], s50 offen offset:2048 sc1\n" \
"v_mfma_f32_16x16x32_bf16 v[100:103], a[236:239], a[176:179], v[100:103]\n" \
"ds_read_b64_tr_b16 a[220:221], v215 offset:16896\n" \
"v_mfma_f32_16x16x32_bf16 v[100:103], a[208:211], a[180:183], v[100:103]\n" \
"ds_read_b64_tr_b16 a[222:223], v215 offset:18944\n" \
"v_mfma_f32_16x16x32_bf16 v[4:7], a[212:215], a[136:139], v[4:7]\n" \
"ds_read_b64_tr_b16 a[224:225], v215 offset:20992\n" \
"v_mfma_f32_16x16x32_bf16 v[4:7], a[228:231], a[140:143], v[4:7]\n" \
"ds_read_b64_tr_b16 a[226:227], v215 offset:23040\n" \
"v_mfma_f32_16x16x32_bf16 v[36:39], a[212:215], a[152:155], v[36:39]\n" \
"buffer_load_dwordx4 v214, %[srdt], s49 offen offset:3072 lds\n" \
"v_mfma_f32_16x16x32_bf16 v[36:39], a[228:231], a[156:159], v[36:39]\n" \
"v_mfma_f32_16x16x32_bf16 v[68:71], a[212:215], a[168:171], v[68:71]\n" \
"buffer_load_dwordx4 v[56:59], v208, %[srdz], s50 offen offset:3072 sc1\n" \
"v_mfma_f32_16x16x32_bf16 v[68:71], a[228:231], a[172:175], v[68:71]\n" \
"v_mfma_f32_16x16x32_bf16 v[100:103], a[212:215], a[184:187], v[100:103]\n" \
"ds_read_b64_tr_b16 a[232:233], v215 offset:25088\n" \
"v_mfma_f32_16x16x32_bf16 v[100:103], a[228:231], a[188:191], v[100:103]\n" \
"s_waitcnt vmcnt(63) lgkmcnt(5)\n" \
"s_nop 0\n" \
"v_mfma_f32_16x16x32_bf16 v[12:15], a[216:219], a[128:131], v[12:15]\n" \
"ds_read_b64_tr_b16 a[234:235], v215 offset:27136\n" \
"buffer_load_dwordx4 v[64:67], v209, %[srdz], s50 offen offset:0 sc1\n" \
"v_mfma_f32_16x16x32_bf16 v[12:15], a[196:199], a[132:135], v[12:15]\n" \
"ds_read_b64_tr_b16 a[236:237], v215 offset:29184\n" \
"v_mfma_f32_16x16x32_bf16 v[44:47], a[216:219], a[144:147], v[44:47]\n" \
"ds_read_b64_tr_b16 a[238:239], v215 offset:31232\n" \
"v_mfma_f32_16x16x32_bf16 v[44:47], a[196:199], a[148:151], v[44:47]\n" \
"buffer_load_dwordx4 v[72:75], v209, %[srdz], s50 offen offset:1024 sc1\n" \
"v_mfma_f32_16x16x32_bf16 v[76:79], a[216:219], a[160:163], v[76:79]\n" \
"v_mfma_f32_16x16x32_bf16 v[76:79], a[196:199], a[164:167], v[76:79]\n" \
"buffer_load_dwordx4 v[80:83], v209, %[srdz], s50 offen offset:2048 sc1\n" \
"v_mfma_f32_16x16x32_bf16 v[108:111], a[216:219], a[176:179], v[108:111]\n" \
"ds_read_b64_tr_b16 a[208:209], v227 offset:16896\n" \
"v_mfma_f32_16x16x32_bf16 v[108:111], a[196:199], a[180:183], v[108:111]\n" \
"ds_read_b64_tr_b16 a[210:211], v227 offset:18944\n" \
"v_mfma_f32_16x16x32_bf16 v[12:15], a[200:203], a[136:139], v[12:15]\n" \
"ds_read_b64_tr_b16 a[212:213], v227 offset:25088\n" \
"v_mfma_f32_16x16x32_bf16 v[12:15], a[204:207], a[140:143], v[12:15]\n" \
"ds_read_b64_tr_b16 a[214:215], v227 offset:27136\n" \
"v_mfma_f32_16x16x32_bf16 v[44:47], a[200:203], a[152:155], v[44:47]\n" \
"ds_read_b64_tr_b16 a[228:229], v227 offset:20992\n" \
"v_mfma_f32_16x16x32_bf16 v[44:47], a[204:207], a[156:159], v[44:47]\n" \
"ds_read_b64_tr_b16 a[230:231], v227 offset:23040\n" \
"buffer_load_dwordx4 v[88:91], v209, %[srdz], s50 offen offset:3072 sc1\n" \
"v_mfma_f32_16x16x32_bf16 v[76:79], a[200:203], a[168:171], v[76:79]\n" \
"ds_read_b64_tr_b16 a[216:217], v227 offset:29184\n" \
"v_mfma_f32_16x16x32_bf16 v[76:79], a[204:207], a[172:175], v[76:79]\n" \
"ds_read_b64_tr_b16 a[218:219], v227 offset:31232\n" \
"v_mfma_f32_16x16x32_bf16 v[108:111], a[200:203], a[184:187], v[108:111]\n" \
"buffer_load_dwordx4 v[96:99], v210, %[srdz], s50 offen offset:0 sc1\n" \
"v_mfma_f32_16x16x32_bf16 v[108:111], a[204:207], a[188:191], v[108:111]\n" \
"s_waitcnt vmcnt(63) lgkmcnt(12)\n" \
"s_nop 0\n" \
"v_mfma_f32_16x16x32_bf16 v[20:23], a[220:223], a[128:131], v[20:23]\n" \
"s_add_u32 m0, %[m0b], 114688\n" \
"s_cmp_gt_u32 s48, 5\n" \
"s_cselect_b32 s49, 5, 6\n" \
"s_lshl_b32 s49, s49, 18\n" \
"s_or_b32 s49, s49, s58\n" \
"buffer_load_dwordx4 %[wo0], %[srdt], s49 offen offset:0 lds\n" \
"v_mfma_f32_16x16x32_bf16 v[20:23], a[224:227], a[132:135], v[20:23]\n" \
"v_mfma_f32_16x16x32_bf16 v[52:55], a[220:223], a[144:147], v[52:55]\n" \
"buffer_load_dwordx4 v[104:107], v210, %[srdz], s50 offen offset:1024 sc1\n" \
"v_mfma_f32_16x16x32_bf16 v[52:55], a[224:227], a[148:151], v[52:55]\n" \
"v_mfma_f32_16x16x32_bf16 v[84:87], a[220:223], a[160:163], v[84:87]\n" \
"s_waitcnt vmcnt(24) lgkmcnt(0)\n" \
"s_barrier\n" \
"v_mfma_f32_16x16x32_bf16 v[84:87], a[224:227], a[164:167], v[84:87]\n" \
"ds_read_b64_tr_b16 a[196:197], v215 offset:32768\n" \
"v_mfma_f32_16x16x32_bf16 v[116:119], a[220:223], a[176:179], v[116:119]\n" \
"ds_read_b64_tr_b16 a[198:199], v215 offset:34816\n" \
"v_mfma_f32_16x16x32_bf16 v[116:119], a[224:227], a[180:183], v[116:119]\n" \
"ds_read_b64_tr_b16 a[200:201], v215 offset:36864\n" \
"buffer_load_dwordx4 v[112:115], v210, %[srdz], s50 offen offset:2048 sc1\n" \
"v_mfma_f32_16x16x32_bf16 v[20:23], a[232:235], a[136:139], v[20:23]\n" \
"ds_read_b64_tr_b16 a[202:203], v215 offset:38912\n" \
"v_mfma_f32_16x16x32_bf16 v[20:23], a[236:239], a[140:143], v[20:23]\n" \
"ds_read_b64_tr_b16 a[204:205], v215 offset:40960\n" \
"v_mfma_f32_16x16x32_bf16 v[52:55], a[232:235], a[152:155], v[52:55]\n" \
"ds_read_b64_tr_b16 a[206:207], v215 offset:43008\n" \
"v_mfma_f32_16x16x32_bf16 v[52:55], a[236:239], a[156:159], v[52:55]\n" \
"ds_read_b64_tr_b16 a[220:221], v215 offset:45056\n" \
"buffer_load_dwordx4 v[120:123], v210, %[srdz], s50 offen offset:3072 sc1\n" \
"v_mfma_f32_16x16x32_bf16 v[84:87], a[232:235], a[168:171], v[84:87]\n" \
"ds_read_b64_tr_b16 a[222:223], v215 offset:47104\n" \
"v_readlane_b32 s54, %[thi], 16\n" \
"v_mfma_f32_16x16x32_bf16 v[84:87], a[236:239], a[172:175], v[84:87]\n" \
"v_mfma_f32_16x16x32_bf16 v[116:119], a[232:235], a[184:187], v[116:119]\n" \
"v_mfma_f32_16x16x32_bf16 v[116:119], a[236:239], a[188:191], v[116:119]\n" \
"s_waitcnt vmcnt(23) lgkmcnt(4)\n" \
"s_nop 0\n" \
"v_mfma_f32_16x16x32_bf16 v[28:31], a[208:211], a[128:131], v[28:31]\n" \
"buffer_load_dwordx4 v212, %[srdt], s49 offen offset:1024 lds\n" \
"v_mfma_f32_16x16x32_bf16 v[28:31], a[228:231], a[132:135], v[28:31]\n" \
"v_mfma_f32_16x16x32_bf16 v[60:63], a[208:211], a[144:147], v[60:63]\n" \
"s_cmp_gt_u32 s48, 5\n" \
"s_cselect_b32 s50, 5, 6\n" \
"s_mul_i32 s50, s50, 152064\n" \
"buffer_load_dwordx4 a[0:3], %[oq0], %[srdz], s50 offen offset:0 sc1\n" \
"v_mfma_f32_16x16x32_bf16 v[60:63], a[228:231], a[148:151], v[60:63]\n" \
"v_mfma_f32_16x16x32_bf16 v[92:95], a[208:211], a[160:163], v[92:95]\n" \
"buffer_load_dword v232, v226, %[srdz], s54 offen sc1\n" \
"v_mfma_f32_16x16x32_bf16 v[92:95], a[228:231], a[164:167], v[92:95]\n" \
"v_mfma_f32_16x16x32_bf16 v[124:127], a[208:211], a[176:179], v[124:127]\n" \
"ds_read_b64_tr_b16 a[224:225], v227 offset:32768\n" \
"buffer_load_dwordx4 a[4:7], %[oq0], %[srdz], s50 offen offset:1024 sc1\n" \
"v_mfma_f32_16x16x32_bf16 v[124:127], a[228:231], a[180:183], v[124:127]\n" \
"ds_read_b64_tr_b16 a[226:227], v227 offset:34816\n" \
"v_mfma_f32_16x16x32_bf16 v[192:195], a[196:199], a[64:67], v[128:131]\n" \
"ds_read_b64_tr_b16 a[232:233], v227 offset:36864\n" \
"v_mfma_f32_16x16x32_bf16 v[192:195], a[200:203], a[68:71], v[192:195]\n" \
"ds_read_b64_tr_b16 a[234:235], v227 offset:38912\n" \
"v_mfma_f32_16x16x32_bf16 v[196:199], a[196:199], a[80:83], v[132:135]\n" \
"buffer_load_dwordx4 a[8:11], %[oq0], %[srdz], s50 offen offset:2048 sc1\n" \
"v_mfma_f32_16x16x32_bf16 v[196:199], a[200:203], a[84:87], v[196:199]\n" \
"v_mfma_f32_16x16x32_bf16 v[200:203], a[196:199], a[96:99], v[136:139]\n" \
"buffer_load_dwordx4 a[12:15], %[oq0], %[srdz], s50 offen offset:3072 sc1\n" \
"v_mfma_f32_16x16x32_bf16 v[200:203], a[200:203], a[100:103], v[200:203]\n" \
"v_mfma_f32_16x16x32_bf16 v[204:207], a[196:199], a[112:115], v[140:143]\n" \
"ds_read_b64_tr_b16 a[236:237], v227 offset:40960\n" \
"v_mfma_f32_16x16x32_bf16 v[204:207], a[200:203], a[116:119], v[204:207]\n" \
"s_waitcnt vmcnt(26) lgkmcnt(5)\n" \
"s_nop 0\n" \
"v_mfma_f32_16x16x32_bf16 v[192:195], a[204:207], a[72:75], v[192:195]\n" \
"ds_read_b64_tr_b16 a[238:239], v227 offset:43008\n" \
"v_mfma_f32_16x16x32_bf16 v[192:195], a[220:223], a[76:79], v[192:195]\n" \
"ds_read_b64_tr_b16 a[208:209], v227 offset:45056\n" \
"buffer_load_dwordx4 v213, %[srdt], s49 offen offset:2048 lds\n" \
"v_mfma_f32_16x16x32_bf16 v[196:199], a[204:207], a[88:91], v[196:199]\n" \
"ds_read_b64_tr_b16 a[210:211], v227 offset:47104\n" \
"v_mfma_f32_16x16x32_bf16 v[196:199], a[220:223], a[92:95], v[196:199]\n" \
"buffer_load_dwordx4 a[16:19], v208, %[srdz], s50 offen offset:0 sc1\n" \
"v_mfma_f32_16x16x32_bf16 v[200:203], a[204:207], a[104:107], v[200:203]\n" \
"v_mfma_f32_16x16x32_bf16 v[200:203], a[220:223], a[108:111], v[200:203]\n" \
"buffer_load_dwordx4 a[20:23], v208, %[srdz], s50 offen offset:1024 sc1\n" \
"v_mfma_f32_16x16x32_bf16 v[204:207], a[204:207], a[120:123], v[204:207]\n" \
"ds_read_b64_tr_b16 a[228:229], v215 offset:33280\n" \
"v_mfma_f32_16x16x32_bf16 v[204:207], a[220:223], a[124:127], v[204:207]\n" \
"ds_read_b64_tr_b16 a[230:231], v215 offset:35328\n" \
"v_mfma_f32_16x16x32_bf16 v[28:31], a[212:215], a[136:139], v[28:31]\n" \
"buffer_load_dwordx4 a[24:27], v208, %[srdz], s50 offen offset:2048 sc1\n" \
"v_mfma_f32_16x16x32_bf16 v[28:31], a[216:219], a[140:143], v[28:31]\n" \
"v_mfma_f32_16x16x32_bf16 v[60:63], a[212:215], a[152:155], v[60:63]\n" \
"buffer_load_dwordx4 a[28:31], v208, %[srdz], s50 offen offset:3072 sc1\n" \
"v_mfma_f32_16x16x32_bf16 v[60:63], a[216:219], a[156:159], v[60:63]\n" \
"v_mfma_f32_16x16x32_bf16 v[92:95], a[212:215], a[168:171], v[92:95]\n" \
"v_mfma_f32_16x16x32_bf16 v[92:95], a[216:219], a[172:175], v[92:95]\n" \
"v_cmp_u_f32_e64 s[44:45], v192, v196\n" \
"buffer_load_dwordx4 v214, %[srdt], s49 offen offset:3072 lds\n" \
"v_mfma_f32_16x16x32_bf16 v[124:127], a[212:215], a[184:187], v[124:127]\n" \
"v_mfma_f32_16x16x32_bf16 v[124:127], a[216:219], a[188:191], v[124:127]\n" \
"v_cmp_u_f32_e64 s[56:57], v200, v204\n" \
"s_or_b64 s[56:57], s[56:57], s[44:45]\n" \
"s_cbranch_scc1 Lsp4_%=\n" \
"Lbk4_%=:\n" \
"s_waitcnt vmcnt(63) lgkmcnt(2)\n" \
"s_nop 0\n" \
"v_mfma_f32_16x16x32_bf16 v[144:147], a[224:227], a[64:67], v[144:147]\n" \
"ds_read_b64_tr_b16 a[212:213], v215 offset:37376\n" \
"v_mfma_f32_16x16x32_bf16 v[144:147], a[232:235], a[68:71], v[144:147]\n" \
"ds_read_b64_tr_b16 a[214:215], v215 offset:39424\n" \
"buffer_load_dwordx4 a[32:35], v209, %[srdz], s50 offen offset:0 sc1\n" \
"v_mfma_f32_16x16x32_bf16 v[148:151], a[224:227], a[80:83], v[148:151]\n" \
"ds_read_b64_tr_b16 a[196:197], v215 offset:41472\n" \
"v_mfma_f32_16x16x32_bf16 v[148:151], a[232:235], a[84:87], v[148:151]\n" \
"ds_read_b64_tr_b16 a[198:199], v215 offset:43520\n" \
"v_mfma_f32_16x16x32_bf16 v[152:155], a[224:227], a[96:99], v[152:155]\n" \
"ds_read_b64_tr_b16 a[200:201], v215 offset:45568\n" \
"v_mfma_f32_16x16x32_bf16 v[152:155], a[232:235], a[100:103], v[152:155]\n" \
"ds_read_b64_tr_b16 a[202:203], v215 offset:47616\n" \
"buffer_load_dwordx4 a[36:39], v209, %[srdz], s50 offen offset:1024 sc1\n" \
"v_mfma_f32_16x16x32_bf16 v[156:159], a[224:227], a[112:115], v[156:159]\n" \
"ds_read_b64_tr_b16 a[204:205], v227 offset:33280\n" \
"v_mfma_f32_16x16x32_bf16 v[156:159], a[232:235], a[116:119], v[156:159]\n" \
"ds_read_b64_tr_b16 a[206:207], v227 offset:35328\n" \
"v_mfma_f32_16x16x32_bf16 v[144:147], a[236:239], a[72:75], v[144:147]\n" \
"ds_read_b64_tr_b16 a[216:217], v227 offset:41472\n" \
"v_mfma_f32_16x16x32_bf16 v[144:147], a[208:211], a[76:79], v[144:147]\n" \
"ds_read_b64_tr_b16 a[218:219], v227 offset:43520\n" \
"buffer_load_dwordx4 a[40:43], v209, %[srdz], s50 offen offset:2048 sc1\n" \
"v_mfma_f32_16x16x32_bf16 v[148:151], a[236:239], a[88:91], v[148:151]\n" \
"ds_read_b64_tr_b16 a[220:221], v227 offset:37376\n" \
"v_mfma_f32_16x16x32_bf16 v[148:151], a[208:211], a[92:95], v[148:151]\n" \
"ds_read_b64_tr_b16 a[222:223], v227 offset:39424\n" \
"v_mfma_f32_16x16x32_bf16 v[152:155], a[236:239], a[104:107], v[152:155]\n" \
"ds_read_b64_tr_b16 a[224:225], v227 offset:45568\n" \
"v_mfma_f32_16x16x32_bf16 v[152:155], a[208:211], a[108:111], v[152:155]\n" \
"ds_read_b64_tr_b16 a[226:227], v227 offset:47616\n" \
"buffer_load_dwordx4 a[44:47], v209, %[srdz], s50 offen offset:3072 sc1\n" \
"v_mfma_f32_16x16x32_bf16 v[156:159], a[236:239], a[120:123], v[156:159]\n" \
"v_mfma_f32_16x16x32_bf16 v[156:159], a[208:211], a[124:127], v[156:159]\n" \
"s_waitcnt vmcnt(63) lgkmcnt(12)\n" \
"s_nop 0\n" \
"v_mfma_f32_16x16x32_bf16 v[160:163], a[228:231], a[64:67], v[160:163]\n" \
"buffer_load_dwordx4 a[48:51], v210, %[srdz], s50 offen offset:0 sc1\n" \
"v_mfma_f32_16x16x32_bf16 v[160:163], a[212:215], a[68:71], v[160:163]\n" \
"v_mfma_f32_16x16x32_bf16 v[164:167], a[228:231], a[80:83], v[164:167]\n" \
"buffer_load_dwordx4 a[52:55], v210, %[srdz], s50 offen offset:1024 sc1\n" \
"v_mfma_f32_16x16x32_bf16 v[164:167], a[212:215], a[84:87], v[164:167]\n" \
"v_mfma_f32_16x16x32_bf16 v[168:171], a[228:231], a[96:99], v[168:171]\n" \
"s_waitcnt vmcnt(28) lgkmcnt(0)\n" \
"s_barrier\n" \
"v_mfma_f32_16x16x32_bf16 v[168:171], a[212:215], a[100:103], v[168:171]\n" \
"ds_read_b64_tr_b16 a[232:233], v220 offset:32768\n" \
"v_mfma_f32_16x16x32_bf16 v[172:175], a[228:231], a[112:115], v[172:175]\n" \
"ds_read_b64_tr_b16 a[234:235], v220 offset:34816\n" \
"v_mfma_f32_16x16x32_bf16 v[172:175], a[212:215], a[116:119], v[172:175]\n" \
"ds_read_b64_tr_b16 a[236:237], v220 offset:36864\n" \
"v_mfma_f32_16x16x32_bf16 v[160:163], a[196:199], a[72:75], v[160:163]\n" \
"ds_read_b64_tr_b16 a[238:239], v220 offset:38912\n" \
"v_mfma_f32_16x16x32_bf16 v[160:163], a[200:203], a[76:79], v[160:163]\n" \
"ds_read_b64_tr_b16 a[208:209], v220 offset:40960\n" \
"s_add_u32 m0, %[m0b], 49152\n" \
"s_cmp_gt_u32 s48, 6\n" \
"s_cselect_b32 s49, 6, 7\n" \
"s_lshl_b32 s49, s49, 18\n" \
"s_or_b32 s49, s49, s58\n" \
"buffer_load_dwordx4 %[wo0], %[srdt], s49 offen offset:0 lds\n" \
"v_mfma_f32_16x16x32_bf16 v[164:167], a[196:199], a[88:91], v[164:167]\n" \
"ds_read_b64_tr_b16 a[210:211], v220 offset:43008\n" \
"v_mfma_f32_16x16x32_bf16 v[164:167], a[200:203], a[92:95], v[164:167]\n" \
"ds_read_b64_tr_b16 a[228:229], v220 offset:45056\n" \
"buffer_load_dwordx4 a[56:59], v210, %[srdz], s50 offen offset:2048 sc1\n" \
"v_mfma_f32_16x16x32_bf16 v[168:171], a[196:199], a[104:107], v[168:171]\n" \
"ds_read_b64_tr_b16 a[230:231], v220 offset:47104\n" \
"v_mfma_f32_16x16x32_bf16 v[168:171], a[200:203], a[108:111], v[168:171]\n" \
"buffer_load_dwordx4 a[60:63], v210, %[srdz], s50 offen offset:3072 sc1\n" \
"v_mfma_f32_16x16x32_bf16 v[172:175], a[196:199], a[120:123], v[172:175]\n" \
"v_mfma_f32_16x16x32_bf16 v[172:175], a[200:203], a[124:127], v[172:175]\n" \
"s_waitcnt vmcnt(23) lgkmcnt(4)\n" \
"s_nop 0\n" \
"v_mfma_f32_16x16x32_bf16 v[176:179], a[204:207], a[64:67], v[176:179]\n" \
"v_mfma_f32_16x16x32_bf16 v[176:179], a[220:223], a[68:71], v[176:179]\n" \
"s_cmp_gt_u32 s48, 6\n" \
"s_cselect_b32 s50, 6, 7\n" \
"s_mul_i32 s50, s50, 152064\n" \
"buffer_load_dwordx4 a[128:131], %[oq0], %[srdz], s50 offen offset:0 sc1\n" \
"v_mfma_f32_16x16x32_bf16 v[180:183], a[204:207], a[80:83], v[180:183]\n" \
"v_mfma_f32_16x16x32_bf16 v[180:183], a[220:223], a[84:87], v[180:183]\n" \
"v_mfma_f32_16x16x32_bf16 v[184:187], a[204:207], a[96:99], v[184:187]\n" \
"v_mfma_f32_16x16x32_bf16 v[184:187], a[220:223], a[100:103], v[184:187]\n" \
"buffer_load_dwordx4 v212, %[srdt], s49 offen offset:1024 lds\n" \
"v_mfma_f32_16x16x32_bf16 v[188:191], a[204:207], a[112:115], v[188:191]\n" \
"ds_read_b64_tr_b16 a[212:213], v221 offset:32768\n" \
"v_mfma_f32_16x16x32_bf16 v[188:191], a[220:223], a[116:119], v[188:191]\n" \
"ds_read_b64_tr_b16 a[214:215], v221 offset:34816\n" \
"v_mfma_f32_16x16x32_bf16 v[128:131], a[232:235], v[0:3], v[192:195]\n" \
"ds_read_b64_tr_b16 a[196:197], v221 offset:36864\n" \
"buffer_load_dwordx4 a[132:135], %[oq0], %[srdz], s50 offen offset:1024 sc1\n" \
"v_mfma_f32_16x16x32_bf16 v[128:131], a[236:239], v[8:11], v[128:131]\n" \
"ds_read_b64_tr_b16 a[198:199], v221 offset:38912\n" \
"v_mfma_f32_16x16x32_bf16 v[132:135], a[232:235], v[32:35], v[196:199]\n" \
"buffer_load_dwordx4 a[136:139], %[oq0], %[srdz], s50 offen offset:2048 sc1\n" \
"v_mfma_f32_16x16x32_bf16 v[132:135], a[236:239], v[40:43], v[132:135]\n" \
"v_mfma_f32_16x16x32_bf16 v[136:139], a[232:235], v[64:67], v[200:203]\n" \
"v_mfma_f32_16x16x32_bf16 v[136:139], a[236:239], v[72:75], v[136:139]\n" \
"buffer_load_dwordx4 a[140:143], %[oq0], %[srdz], s50 offen offset:3072 sc1\n" \
"v_mfma_f32_16x16x32_bf16 v[140:143], a[232:235], v[96:99], v[204:207]\n" \
"ds_read_b64_tr_b16 a[200:201], v221 offset:40960\n" \
"v_mfma_f32_16x16x32_bf16 v[140:143], a[236:239], v[104:107], v[140:143]\n" \
"s_waitcnt vmcnt(26) lgkmcnt(5)\n" \
"s_nop 0\n" \
"v_mfma_f32_16x16x32_bf16 v[128:131], a[208:211], v[16:19], v[128:131]\n" \
"ds_read_b64_tr_b16 a[202:203], v221 offset:43008\n" \
"v_mfma_f32_16x16x32_bf16 v[128:131], a[228:231], v[24:27], v[128:131]\n" \
"ds_read_b64_tr_b16 a[204:205], v221 offset:45056\n" \
"v_mfma_f32_16x16x32_bf16 v[132:135], a[208:211], v[48:51], v[132:135]\n" \
"ds_read_b64_tr_b16 a[206:207], v221 offset:47104\n" \
"v_mfma_f32_16x16x32_bf16 v[132:135], a[228:231], v[56:59], v[132:135]\n" \
"buffer_load_dwordx4 a[144:147], v208, %[srdz], s50 offen offset:0 sc1\n" \
"v_mfma_f32_16x16x32_bf16 v[136:139], a[208:211], v[80:83], v[136:139]\n" \
"v_mfma_f32_16x16x32_bf16 v[136:139], a[228:231], v[88:91], v[136:139]\n" \
"buffer_load_dwordx4 v213, %[srdt], s49 offen offset:2048 lds\n" \
"v_mfma_f32_16x16x32_bf16 v[140:143], a[208:211], v[112:115], v[140:143]\n" \
"ds_read_b64_tr_b16 a[220:221], v220 offset:33280\n" \
"v_mfma_f32_16x16x32_bf16 v[140:143], a[228:231], v[120:123], v[140:143]\n" \
"ds_read_b64_tr_b16 a[222:223], v220 offset:35328\n" \
"v_mfma_f32_16x16x32_bf16 v[176:179], a[216:219], a[72:75], v[176:179]\n" \
"buffer_load_dwordx4 a[148:151], v208, %[srdz], s50 offen offset:1024 sc1\n" \
"v_mfma_f32_16x16x32_bf16 v[176:179], a[224:227], a[76:79], v[176:179]\n" \
"v_mfma_f32_16x16x32_bf16 v[180:183], a[216:219], a[88:91], v[180:183]\n" \
"buffer_load_dwordx4 a[152:155], v208, %[srdz], s50 offen offset:2048 sc1\n" \
"v_mfma_f32_16x16x32_bf16 v[180:183], a[224:227], a[92:95], v[180:183]\n" \
"v_mfma_f32_16x16x32_bf16 v[184:187], a[216:219], a[104:107], v[184:187]\n" \
"v_mfma_f32_16x16x32_bf16 v[184:187], a[224:227], a[108:111], v[184:187]\n" \
"v_cmp_u_f32_e64 s[44:45], v128, v132\n" \
"buffer_load_dwordx4 a[156:159], v208, %[srdz], s50 offen offset:3072 sc1\n" \
"v_mfma_f32_16x16x32_bf16 v[188:191], a[216:219], a[120:123], v[188:191]\n" \
"v_mfma_f32_16x16x32_bf16 v[188:191], a[224:227], a[124:127], v[188:191]\n" \
"v_cmp_u_f32_e64 s[56:57], v136, v140\n" \
"s_or_b64 s[56:57], s[56:57], s[44:45]\n" \
"s_cbranch_scc1 Lsp5_%=\n" \
"Lbk5_%=:\n" \
"s_waitcnt vmcnt(63) lgkmcnt(2)\n" \
"s_nop 0\n" \
"v_mfma_f32_16x16x32_bf16 v[144:147], a[212:215], v[0:3], v[144:147]\n" \
"ds_read_b64_tr_b16 a[216:217], v220 offset:37376\n" \
"v_mfma_f32_16x16x32_bf16 v[144:147], a[196:199], v[8:11], v[144:147]\n" \
"ds_read_b64_tr_b16 a[218:219], v220 offset:39424\n" \
"v_mfma_f32_16x16x32_bf16 v[148:151], a[212:215], v[32:35], v[148:151]\n" \
"ds_read_b64_tr_b16 a[208:209], v220 offset:41472\n" \
"v_mfma_f32_16x16x32_bf16 v[148:151], a[196:199], v[40:43], v[148:151]\n" \
"ds_read_b64_tr_b16 a[210:211], v220 offset:43520\n" \
"v_mfma_f32_16x16x32_bf16 v[152:155], a[212:215], v[64:67], v[152:155]\n" \
"ds_read_b64_tr_b16 a[224:225], v220 offset:45568\n" \
"buffer_load_dwordx4 a[160:163], v209, %[srdz], s50 offen offset:0 sc1\n" \
"v_mfma_f32_16x16x32_bf16 v[152:155], a[196:199], v[72:75], v[152:155]\n" \
"ds_read_b64_tr_b16 a[226:227], v220 offset:47616\n" \
"v_mfma_f32_16x16x32_bf16 v[156:159], a[212:215], v[96:99], v[156:159]\n" \
"ds_read_b64_tr_b16 a[228:229], v221 offset:33280\n" \
"buffer_load_dwordx4 v214, %[srdt], s49 offen offset:3072 lds\n" \
"v_mfma_f32_16x16x32_bf16 v[156:159], a[196:199], v[104:107], v[156:159]\n" \
"ds_read_b64_tr_b16 a[230:231], v221 offset:35328\n" \
"v_mfma_f32_16x16x32_bf16 v[144:147], a[200:203], v[16:19], v[144:147]\n" \
"ds_read_b64_tr_b16 a[232:233], v221 offset:41472\n" \
"v_readlane_b32 s64, %[tlo], 10\n" \
"v_readlane_b32 s65, %[thi], 10\n" \
"buffer_load_dwordx4 a[164:167], v209, %[srdz], s50 offen offset:1024 sc1\n" \
"v_mfma_f32_16x16x32_bf16 v[144:147], a[204:207], v[24:27], v[144:147]\n" \
"ds_read_b64_tr_b16 a[234:235], v221 offset:43520\n" \
"v_mfma_f32_16x16x32_bf16 v[148:151], a[200:203], v[48:51], v[148:151]\n" \
"ds_read_b64_tr_b16 a[236:237], v221 offset:37376\n" \
"v_mfma_f32_16x16x32_bf16 v[148:151], a[204:207], v[56:59], v[148:151]\n" \
"ds_read_b64_tr_b16 a[238:239], v221 offset:39424\n" \
"v_mfma_f32_16x16x32_bf16 v[152:155], a[200:203], v[80:83], v[152:155]\n" \
"ds_read_b64_tr_b16 a[212:213], v221 offset:45568\n" \
"buffer_load_dwordx4 a[168:171], v209, %[srdz], s50 offen offset:2048 sc1\n" \
"v_mfma_f32_16x16x32_bf16 v[152:155], a[204:207], v[88:91], v[152:155]\n" \
"ds_read_b64_tr_b16 a[214:215], v221 offset:47616\n" \
"v_mfma_f32_16x16x32_bf16 v[156:159], a[200:203], v[112:115], v[156:159]\n" \
"buffer_load_dwordx4 a[172:175], v209, %[srdz], s50 offen offset:3072 sc1\n" \
"v_mfma_f32_16x16x32_bf16 v[156:159], a[204:207], v[120:123], v[156:159]\n" \
"s_waitcnt vmcnt(63) lgkmcnt(12)\n" \
"s_nop 0\n" \
"v_mfma_f32_16x16x32_bf16 v[160:163], a[220:223], v[0:3], v[160:163]\n" \
"v_mfma_f32_16x16x32_bf16 v[160:163], a[216:219], v[8:11], v[160:163]\n" \
"buffer_load_dwordx4 a[176:179], v210, %[srdz], s50 offen offset:0 sc1\n" \
"v_mfma_f32_16x16x32_bf16 v[164:167], a[220:223], v[32:35], v[164:167]\n" \
"v_readlane_b32 s63, %[thi], 33\n" \
"v_lshlrev_b32_e64 v234, 3, v224\n" \
"v_mfma_f32_16x16x32_bf16 v[164:167], a[216:219], v[40:43], v[164:167]\n" \
"v_mfma_f32_16x16x32_bf16 v[168:171], a[220:223], v[64:67], v[168:171]\n" \
"v_and_b32_e32 v234, 0xc00, v234\n" \
"v_lshlrev_b32_e64 v235, 2, v224\n" \
"s_waitcnt vmcnt(25) lgkmcnt(0)\n" \
"s_barrier\n" \
"v_mfma_f32_16x16x32_bf16 v[168:171], a[216:219], v[72:75], v[168:171]\n" \
"ds_read_b64_tr_b16 a[196:197], v220 offset:49152\n" \
"v_mfma_f32_16x16x32_bf16 v[172:175], a[220:223], v[96:99], v[172:175]\n" \
"ds_read_b64_tr_b16 a[198:199], v220 offset:51200\n" \
"v_and_b32_e32 v235, 0x7000, v235\n" \
"v_bfe_u32 v230, v224, 9, 1\n" \
"v_mfma_f32_16x16x32_bf16 v[172:175], a[216:219], v[104:107], v[172:175]\n" \
"ds_read_b64_tr_b16 a[200:201], v220 offset:53248\n" \
"v_mfma_f32_16x16x32_bf16 v[160:163], a[208:211], v[16:19], v[160:163]\n" \
"ds_read_b64_tr_b16 a[202:203], v220 offset:55296\n" \
"v_lshl_or_b32 v235, v230, 2, v235\n" \
"v_readlane_b32 s52, %[thi], 35\n" \
"buffer_load_dwordx4 a[180:183], v210, %[srdz], s50 offen offset:1024 sc1\n" \
"v_mfma_f32_16x16x32_bf16 v[160:163], a[224:227], v[24:27], v[160:163]\n" \
"ds_read_b64_tr_b16 a[204:205], v220 offset:57344\n" \
"v_or_b32_e64 v234, v234, v235\n" \
"v_mfma_f32_16x16x32_bf16 v[164:167], a[208:211], v[48:51], v[164:167]\n" \
"ds_read_b64_tr_b16 a[206:207], v220 offset:59392\n" \
"v_mfma_f32_16x16x32_bf16 v[164:167], a[224:227], v[56:59], v[164:167]\n" \
"ds_read_b64_tr_b16 a[220:221], v220 offset:61440\n" \
"v_mfma_f32_16x16x32_bf16 v[168:171], a[208:211], v[80:83], v[168:171]\n" \
"ds_read_b64_tr_b16 a[222:223], v220 offset:63488\n" \
"v_mfma_f32_16x16x32_bf16 v[168:171], a[224:227], v[88:91], v[168:171]\n" \
"buffer_load_dword v235, v234, %[srdz], s63 offen sc1\n" \
"v_mfma_f32_16x16x32_bf16 v[172:175], a[208:211], v[112:115], v[172:175]\n" \
"v_mfma_f32_16x16x32_bf16 v[172:175], a[224:227], v[120:123], v[172:175]\n" \
"s_waitcnt vmcnt(63) lgkmcnt(4)\n" \
"s_nop 0\n" \
"v_mfma_f32_16x16x32_bf16 v[176:179], a[228:231], v[0:3], v[176:179]\n" \
"buffer_load_dwordx4 a[184:187], v210, %[srdz], s50 offen offset:2048 sc1\n" \
"v_mfma_f32_16x16x32_bf16 v[176:179], a[236:239], v[8:11], v[176:179]\n" \
"v_mfma_f32_16x16x32_bf16 v[180:183], a[228:231], v[32:35], v[180:183]\n" \
"buffer_load_dwordx4 a[188:191], v210, %[srdz], s50 offen offset:3072 sc1\n" \
"v_mfma_f32_16x16x32_bf16 v[180:183], a[236:239], v[40:43], v[180:183]\n" \
"v_mfma_f32_16x16x32_bf16 v[184:187], a[228:231], v[64:67], v[184:187]\n" \
"v_mfma_f32_16x16x32_bf16 v[184:187], a[236:239], v[72:75], v[184:187]\n" \
".if . - adaln_body != 53248\n" \
".error \"size model\"\n" \
".endif\n" \
"Lk12_%=:\n" \
"s_cbranch_execz Lk13_%=\n" \
"s_nop 0\n" \
"v_mfma_f32_16x16x32_bf16 v[188:191], a[228:231], v[96:99], v[188:191]\n" \
"ds_read_b64_tr_b16 a[216:217], v221 offset:49152\n" \
"s_waitcnt vmcnt(23)\n" \
"v_cmp_eq_u32_e32 vcc, 0, v232\n" \
"s_cbranch_vccnz Lspp_%=\n" \
"Lbkp_%=:\n" \
"s_nop 0\n" \
"v_mfma_f32_16x16x32_bf16 v[188:191], a[236:239], v[104:107], v[188:191]\n" \
"ds_read_b64_tr_b16 a[218:219], v221 offset:51200\n" \
"v_mfma_f32_16x16x32_bf16 v[192:195], a[196:199], a[0:3], v[128:131]\n" \
"ds_read_b64_tr_b16 a[208:209], v221 offset:53248\n" \
"v_mfma_f32_16x16x32_bf16 v[192:195], a[200:203], a[4:7], v[192:195]\n" \
"ds_read_b64_tr_b16 a[210:211], v221 offset:55296\n" \
"s_add_u32 m0, %[m0b], 0\n" \
"s_cmp_gt_u32 s48, 3\n" \
"s_cselect_b32 s49, 3, 4\n" \
"s_lshl_b32 s49, s49, 18\n" \
"s_or_b32 s49, s49, s59\n" \
"s_nop 0\n" \
"buffer_load_dwordx4 %[wo0], %[srdt], s49 offen offset:0 lds\n" \
"v_mfma_f32_16x16x32_bf16 v[196:199], a[196:199], a[16:19], v[132:135]\n" \
"v_mfma_f32_16x16x32_bf16 v[196:199], a[200:203], a[20:23], v[196:199]\n" \
"v_mfma_f32_16x16x32_bf16 v[200:203], a[196:199], a[32:35], v[136:139]\n" \
"v_mfma_f32_16x16x32_bf16 v[200:203], a[200:203], a[36:39], v[200:203]\n" \
"v_mfma_f32_16x16x32_bf16 v[204:207], a[196:199], a[48:51], v[140:143]\n" \
"ds_read_b64_tr_b16 a[224:225], v221 offset:57344\n" \
"v_mfma_f32_16x16x32_bf16 v[204:207], a[200:203], a[52:55], v[204:207]\n" \
"s_waitcnt vmcnt(21) lgkmcnt(5)\n" \
"s_nop 0\n" \
"v_mfma_f32_16x16x32_bf16 v[192:195], a[204:207], a[8:11], v[192:195]\n" \
"ds_read_b64_tr_b16 a[226:227], v221 offset:59392\n" \
"v_mfma_f32_16x16x32_bf16 v[192:195], a[220:223], a[12:15], v[192:195]\n" \
"ds_read_b64_tr_b16 a[228:229], v221 offset:61440\n" \
"v_mfma_f32_16x16x32_bf16 v[196:199], a[204:207], a[24:27], v[196:199]\n" \
"ds_read_b64_tr_b16 a[230:231], v221 offset:63488\n" \
"v_mfma_f32_16x16x32_bf16 v[196:199], a[220:223], a[28:31], v[196:199]\n" \
"buffer_load_dwordx4 v212, %[srdt], s49 offen offset:1024 lds\n" \
"v_mfma_f32_16x16x32_bf16 v[200:203], a[204:207], a[40:43], v[200:203]\n" \
"v_mfma_f32_16x16x32_bf16 v[200:203], a[220:223], a[44:47], v[200:203]\n" \
"v_mfma_f32_16x16x32_bf16 v[204:207], a[204:207], a[56:59], v[204:207]\n" \
"ds_read_b64_tr_b16 a[236:237], v220 offset:49664\n" \
"v_mfma_f32_16x16x32_bf16 v[204:207], a[220:223], a[60:63], v[204:207]\n" \
"ds_read_b64_tr_b16 a[238:239], v220 offset:51712\n" \
"v_mfma_f32_16x16x32_bf16 v[176:179], a[232:235], v[16:19], v[176:179]\n" \
"v_mfma_f32_16x16x32_bf16 v[176:179], a[212:215], v[24:27], v[176:179]\n" \
"v_mfma_f32_16x16x32_bf16 v[180:183], a[232:235], v[48:51], v[180:183]\n" \
"buffer_load_dwordx4 v213, %[srdt], s49 offen offset:2048 lds\n" \
"v_mfma_f32_16x16x32_bf16 v[180:183], a[212:215], v[56:59], v[180:183]\n" \
"v_mfma_f32_16x16x32_bf16 v[184:187], a[232:235], v[80:83], v[184:187]\n" \
"s_add_u32 m0, %[m0b], 98304\n" \
"s_mov_b32 s61, 0\n" \
"s_nop 0\n" \
"buffer_load_dwordx4 %[vdn], s[64:67], s61 offen offset:0 lds\n" \
"v_mfma_f32_16x16x32_bf16 v[184:187], a[212:215], v[88:91], v[184:187]\n" \
"v_cmp_u_f32_e64 s[44:45], v192, v196\n" \
"v_mfma_f32_16x16x32_bf16 v[188:191], a[232:235], v[112:115], v[188:191]\n" \
"v_mfma_f32_16x16x32_bf16 v[188:191], a[212:215], v[120:123], v[188:191]\n" \
"v_cmp_u_f32_e64 s[56:57], v200, v204\n" \
"s_or_b64 s[56:57], s[56:57], s[44:45]\n" \
"s_cbranch_scc1 Lsp6_%=\n" \
"Lbk6_%=:\n" \
"s_waitcnt vmcnt(63) lgkmcnt(2)\n" \
"s_nop 0\n" \
"v_mfma_f32_16x16x32_bf16 v[144:147], a[216:219], a[0:3], v[144:147]\n" \
"ds_read_b64_tr_b16 a[232:233], v220 offset:53760\n" \
"v_mfma_f32_16x16x32_bf16 v[144:147], a[208:211], a[4:7], v[144:147]\n" \
"ds_read_b64_tr_b16 a[234:235], v220 offset:55808\n" \
"v_mfma_f32_16x16x32_bf16 v[148:151], a[216:219], a[16:19], v[148:151]\n" \
"ds_read_b64_tr_b16 a[196:197], v220 offset:57856\n" \
"v_mfma_f32_16x16x32_bf16 v[148:151], a[208:211], a[20:23], v[148:151]\n" \
"ds_read_b64_tr_b16 a[198:199], v220 offset:59904\n" \
"v_mfma_f32_16x16x32_bf16 v[152:155], a[216:219], a[32:35], v[152:155]\n" \
"ds_read_b64_tr_b16 a[200:201], v220 offset:61952\n" \
"v_mfma_f32_16x16x32_bf16 v[152:155], a[208:211], a[36:39], v[152:155]\n" \
"ds_read_b64_tr_b16 a[202:203], v220 offset:64000\n" \
"v_mfma_f32_16x16x32_bf16 v[156:159], a[216:219], a[48:51], v[156:159]\n" \
"ds_read_b64_tr_b16 a[204:205], v221 offset:49664\n" \
"v_mfma_f32_16x16x32_bf16 v[156:159], a[208:211], a[52:55], v[156:159]\n" \
"ds_read_b64_tr_b16 a[206:207], v221 offset:51712\n" \
"s_add_u32 m0, %[m0b], 0\n" \
"s_nop 0\n" \
"buffer_load_dwordx4 v214, %[srdt], s49 offen offset:3072 lds\n" \
"v_mfma_f32_16x16x32_bf16 v[144:147], a[224:227], a[8:11], v[144:147]\n" \
"ds_read_b64_tr_b16 a[212:213], v221 offset:57856\n" \
"v_mfma_f32_16x16x32_bf16 v[144:147], a[228:231], a[12:15], v[144:147]\n" \
"ds_read_b64_tr_b16 a[214:215], v221 offset:59904\n" \
"v_mfma_f32_16x16x32_bf16 v[148:151], a[224:227], a[24:27], v[148:151]\n" \
"ds_read_b64_tr_b16 a[220:221], v221 offset:53760\n" \
"v_mfma_f32_16x16x32_bf16 v[148:151], a[228:231], a[28:31], v[148:151]\n" \
"ds_read_b64_tr_b16 a[222:223], v221 offset:55808\n" \
"v_mfma_f32_16x16x32_bf16 v[152:155], a[224:227], a[40:43], v[152:155]\n" \
"ds_read_b64_tr_b16 a[216:217], v221 offset:61952\n" \
"v_mfma_f32_16x16x32_bf16 v[152:155], a[228:231], a[44:47], v[152:155]\n" \
"ds_read_b64_tr_b16 a[218:219], v221 offset:64000\n" \
"v_mfma_f32_16x16x32_bf16 v[156:159], a[224:227], a[56:59], v[156:159]\n" \
"v_xor_b32_e64 v218, 64, %[vdn]\n" \
"s_add_u32 m0, %[m0b], 99328\n" \
"s_mov_b32 s61, 4096\n" \
"buffer_load_dwordx4 v218, s[64:67], s61 offen offset:0 lds\n" \
"v_mfma_f32_16x16x32_bf16 v[156:159], a[228:231], a[60:63], v[156:159]\n" \
"s_waitcnt vmcnt(63) lgkmcnt(12)\n" \
"s_nop 0\n" \
"v_mfma_f32_16x16x32_bf16 v[160:163], a[236:239], a[0:3], v[160:163]\n" \
"v_mfma_f32_16x16x32_bf16 v[160:163], a[232:235], a[4:7], v[160:163]\n" \
"v_mfma_f32_16x16x32_bf16 v[164:167], a[236:239], a[16:19], v[164:167]\n" \
"v_mfma_f32_16x16x32_bf16 v[164:167], a[232:235], a[20:23], v[164:167]\n" \
"s_add_u32 m0, %[m0b], 16384\n" \
"s_cmp_gt_u32 s48, 4\n" \
"s_cselect_b32 s49, 4, 5\n" \
"s_lshl_b32 s49, s49, 18\n" \
"s_or_b32 s49, s49, s59\n" \
"buffer_load_dwordx4 %[wo0], %[srdt], s49 offen offset:0 lds\n" \
"v_mfma_f32_16x16x32_bf16 v[168:171], a[236:239], a[32:35], v[168:171]\n" \
"s_waitcnt vmcnt(15) lgkmcnt(0)\n" \
"s_barrier\n" \
"v_mfma_f32_16x16x32_bf16 v[168:171], a[232:235], a[36:39], v[168:171]\n" \
"ds_read_b64_tr_b16 a[208:209], v215 offset:49152\n" \
"v_mfma_f32_16x16x32_bf16 v[172:175], a[236:239], a[48:51], v[172:175]\n" \
"ds_read_b64_tr_b16 a[210:211], v215 offset:51200\n" \
"v_mfma_f32_16x16x32_bf16 v[172:175], a[232:235], a[52:55], v[172:175]\n" \
"ds_read_b64_tr_b16 a[224:225], v215 offset:53248\n" \
"v_mfma_f32_16x16x32_bf16 v[160:163], a[196:199], a[8:11], v[160:163]\n" \
"ds_read_b64_tr_b16 a[226:227], v215 offset:55296\n" \
"v_mfma_f32_16x16x32_bf16 v[160:163], a[200:203], a[12:15], v[160:163]\n" \
"ds_read_b64_tr_b16 a[228:229], v215 offset:57344\n" \
"v_mfma_f32_16x16x32_bf16 v[164:167], a[196:199], a[24:27], v[164:167]\n" \
"ds_read_b64_tr_b16 a[230:231], v215 offset:59392\n" \
"v_mfma_f32_16x16x32_bf16 v[164:167], a[200:203], a[28:31], v[164:167]\n" \
"ds_read_b64_tr_b16 a[236:237], v215 offset:61440\n" \
"v_mfma_f32_16x16x32_bf16 v[168:171], a[196:199], a[40:43], v[168:171]\n" \
"ds_read_b64_tr_b16 a[238:239], v215 offset:63488\n" \
"v_mfma_f32_16x16x32_bf16 v[168:171], a[200:203], a[44:47], v[168:171]\n" \
"buffer_load_dwordx4 v212, %[srdt], s49 offen offset:1024 lds\n" \
"v_mfma_f32_16x16x32_bf16 v[172:175], a[196:199], a[56:59], v[172:175]\n" \
"v_mfma_f32_16x16x32_bf16 v[172:175], a[200:203], a[60:63], v[172:175]\n" \
"s_waitcnt vmcnt(63) lgkmcnt(4)\n" \
"s_nop 0\n" \
"v_mfma_f32_16x16x32_bf16 v[176:179], a[204:207], a[0:3], v[176:179]\n" \
"v_xor_b32_e32 v218, 0x80, %[vdn]\n" \
"s_add_u32 m0, %[m0b], 100352\n" \
"s_mov_b32 s61, 8192\n" \
"buffer_load_dwordx4 v218, s[64:67], s61 offen offset:0 lds\n" \
"v_mfma_f32_16x16x32_bf16 v[176:179], a[220:223], a[4:7], v[176:179]\n" \
"v_mfma_f32_16x16x32_bf16 v[180:183], a[204:207], a[16:19], v[180:183]\n" \
"s_waitcnt vmcnt(11)\n" \
"v_and_b32_e32 v230, 1, v235\n" \
"v_mfma_f32_16x16x32_bf16 v[180:183], a[220:223], a[20:23], v[180:183]\n" \
"v_cmp_eq_u32_e64 s[68:69], 0, v230\n" \
"v_mfma_f32_16x16x32_bf16 v[184:187], a[204:207], a[32:35], v[184:187]\n" \
"s_cmp_lg_u64 s[68:69], 0\n" \
"s_cbranch_scc1 Ls1_%=\n" \
"Ls1b_%=:\n" \
"v_add_f32_e64 v235, %[rd0], v235\n" \
"v_mfma_f32_16x16x32_bf16 v[184:187], a[220:223], a[36:39], v[184:187]\n" \
"v_bfe_u32 v234, v224, 9, 1\n" \
"v_mfma_f32_16x16x32_bf16 v[188:191], a[204:207], a[48:51], v[188:191]\n" \
"ds_read_b64_tr_b16 a[232:233], v227 offset:49152\n" \
"v_add_f32_dpp v235, v235, v235 quad_perm:[1,0,3,2] row_mask:0xf bank_mask:0xf\n" \
"v_add_u32_e64 v230, s52, v234\n" \
"v_mfma_f32_16x16x32_bf16 v[188:191], a[220:223], a[52:55], v[188:191]\n" \
"ds_read_b64_tr_b16 a[234:235], v227 offset:51200\n" \
"s_mul_i32 s63, %[wid], 4088\n" \
"v_mfma_f32_16x16x32_bf16 v[128:131], a[208:211], a[128:131], v[192:195]\n" \
"ds_read_b64_tr_b16 a[196:197], v227 offset:53248\n" \
"v_mfma_f32_16x16x32_bf16 v[128:131], a[224:227], a[132:135], v[128:131]\n" \
"ds_read_b64_tr_b16 a[198:199], v227 offset:55296\n" \
"v_add_f32_dpp v235, v235, v235 quad_perm:[2,3,0,1] row_mask:0xf bank_mask:0xf\n" \
"s_sub_u32 s63, %[m0b], s63\n" \
"s_nop 0\n" \
"v_mfma_f32_16x16x32_bf16 v[132:135], a[208:211], a[144:147], v[196:199]\n" \
"v_lshl_add_u32 v234, v234, 2, s63\n" \
"s_add_u32 m0, %[m0b], 16384\n" \
"s_nop 0\n" \
"s_nop 0\n" \
"buffer_load_dwordx4 v213, %[srdt], s49 offen offset:2048 lds\n" \
"v_mfma_f32_16x16x32_bf16 v[132:135], a[224:227], a[148:151], v[132:135]\n" \
"v_add_f32_dpp v235, v235, v235 row_ror:8 row_mask:0xf bank_mask:0xf\n" \
"v_sub_u32_e32 v230, 0x7ff, v230\n" \
"v_mfma_f32_16x16x32_bf16 v[136:139], a[208:211], a[160:163], v[200:203]\n" \
"v_mov_b32_e64 v216, v235\n" \
"v_mfma_f32_16x16x32_bf16 v[136:139], a[224:227], a[164:167], v[136:139]\n" \
"v_add_u32_e32 v234, 0xcc04, v234\n" \
"v_mfma_f32_16x16x32_bf16 v[140:143], a[208:211], a[176:179], v[204:207]\n" \
"ds_read_b64_tr_b16 a[200:201], v227 offset:57344\n" \
"v_permlane16_swap_b32_e32 v235, v216\n" \
"s_nop 0\n" \
"v_mfma_f32_16x16x32_bf16 v[140:143], a[224:227], a[180:183], v[140:143]\n" \
"s_waitcnt vmcnt(10) lgkmcnt(5)\n" \
"s_nop 0\n" \
"v_mfma_f32_16x16x32_bf16 v[128:131], a[228:231], a[136:139], v[128:131]\n" \
"ds_read_b64_tr_b16 a[202:203], v227 offset:59392\n" \
"v_add_f32_e64 v235, v235, v216\n" \
"v_mfma_f32_16x16x32_bf16 v[128:131], a[236:239], a[140:143], v[128:131]\n" \
"ds_read_b64_tr_b16 a[204:205], v227 offset:61440\n" \
"v_mov_b32_e64 v216, v235\n" \
"v_xor_b32_e32 v218, 0xc0, %[vdn]\n" \
"s_add_u32 m0, %[m0b], 101376\n" \
"s_mov_b32 s61, 12288\n" \
"buffer_load_dwordx4 v218, s[64:67], s61 offen offset:0 lds\n" \
"v_mfma_f32_16x16x32_bf16 v[132:135], a[228:231], a[152:155], v[132:135]\n" \
"ds_read_b64_tr_b16 a[206:207], v227 offset:63488\n" \
"s_nop 0\n" \
"s_nop 0\n" \
"v_mfma_f32_16x16x32_bf16 v[132:135], a[236:239], a[156:159], v[132:135]\n" \
"v_permlane32_swap_b32_e32 v235, v216\n" \
"s_nop 0\n" \
"s_add_u32 m0, %[m0b], 16384\n" \
"s_nop 0\n" \
"s_nop 0\n" \
"buffer_load_dwordx4 v214, %[srdt], s49 offen offset:3072 lds\n" \
"v_mfma_f32_16x16x32_bf16 v[136:139], a[228:231], a[168:171], v[136:139]\n" \
"v_add_f32_e64 v235, v235, v216\n" \
"v_mfma_f32_16x16x32_bf16 v[136:139], a[236:239], a[172:175], v[136:139]\n" \
"v_ashrrev_i32_e64 v216, 31, v235\n" \
"v_mfma_f32_16x16x32_bf16 v[140:143], a[228:231], a[184:187], v[140:143]\n" \
"ds_read_b64_tr_b16 a[220:221], v215 offset:49664\n" \
"v_mfma_f32_16x16x32_bf16 v[140:143], a[236:239], a[188:191], v[140:143]\n" \
"ds_read_b64_tr_b16 a[222:223], v215 offset:51712\n" \
"v_or_b32_e32 v216, 0x80000000, v216\n" \
"v_mfma_f32_16x16x32_bf16 v[176:179], a[212:215], a[8:11], v[176:179]\n" \
"v_xor_b32_e64 v235, v235, v216\n" \
"v_mfma_f32_16x16x32_bf16 v[176:179], a[216:219], a[12:15], v[176:179]\n" \
"v_and_b32_e32 v235, 0xfffff800, v235\n" \
"v_mfma_f32_16x16x32_bf16 v[180:183], a[212:215], a[24:27], v[180:183]\n" \
"v_max_u32_e32 v235, 0x800, v235\n" \
"v_mfma_f32_16x16x32_bf16 v[180:183], a[216:219], a[28:31], v[180:183]\n" \
"v_or_b32_e64 v235, v235, v230\n" \
"v_mfma_f32_16x16x32_bf16 v[184:187], a[212:215], a[40:43], v[184:187]\n" \
"ds_write_b32 v234, v235 offset:65532\n" \
"v_mfma_f32_16x16x32_bf16 v[184:187], a[216:219], a[44:47], v[184:187]\n" \
"v_cmp_u_f32_e64 s[44:45], v128, v132\n" \
"v_mfma_f32_16x16x32_bf16 v[188:191], a[212:215], a[56:59], v[188:191]\n" \
"v_mfma_f32_16x16x32_bf16 v[188:191], a[216:219], a[60:63], v[188:191]\n" \
"v_cmp_u_f32_e64 s[56:57], v136, v140\n" \
"s_or_b64 s[56:57], s[56:57], s[44:45]\n" \
"s_cbranch_scc1 Lsp7_%=\n" \
"Lbk7_%=:\n" \
"s_waitcnt vmcnt(63) lgkmcnt(3)\n" \
"s_nop 0\n" \
"v_mfma_f32_16x16x32_bf16 v[144:147], a[232:235], a[128:131], v[144:147]\n" \
"ds_read_b64_tr_b16 a[212:213], v215 offset:53760\n" \
"v_mfma_f32_16x16x32_bf16 v[144:147], a[196:199], a[132:135], v[144:147]\n" \
"ds_read_b64_tr_b16 a[214:215], v215 offset:55808\n" \
"v_mfma_f32_16x16x32_bf16 v[148:151], a[232:235], a[144:147], v[148:151]\n" \
"ds_read_b64_tr_b16 a[208:209], v215 offset:57856\n" \
"v_mfma_f32_16x16x32_bf16 v[148:151], a[196:199], a[148:151], v[148:151]\n" \
"ds_read_b64_tr_b16 a[210:211], v215 offset:59904\n" \
"s_lshl_b32 s61, %[wid], 10\n" \
"v_lshrrev_b32_e32 v231, 3, v224\n" \
"v_add_u32_e64 v231, s61, v231\n" \
"s_add_u32 m0, %[m0b], 32768\n" \
"s_cmp_gt_u32 s48, 5\n" \
"s_cselect_b32 s49, 5, 6\n" \
"s_lshl_b32 s49, s49, 18\n" \
"s_or_b32 s49, s49, s59\n" \
"buffer_load_dwordx4 %[wo0], %[srdt], s49 offen offset:0 lds\n" \
"v_mfma_f32_16x16x32_bf16 v[152:155], a[232:235], a[160:163], v[152:155]\n" \
"ds_read_b64_tr_b16 a[216:217], v215 offset:61952\n" \
"v_readlane_b32 s44, %[tlo], 20\n" \
"v_readlane_b32 s45, %[thi], 20\n" \
"v_lshrrev_b32_e64 v236, 1, v231\n" \
"v_mfma_f32_16x16x32_bf16 v[152:155], a[196:199], a[164:167], v[152:155]\n" \
"ds_read_b64_tr_b16 a[218:219], v215 offset:64000\n" \
"v_mfma_f32_16x16x32_bf16 v[156:159], a[232:235], a[176:179], v[156:159]\n" \
"ds_read_b64_tr_b16 a[224:225], v227 offset:49664\n" \
"v_mfma_f32_16x16x32_bf16 v[156:159], a[196:199], a[180:183], v[156:159]\n" \
"ds_read_b64_tr_b16 a[226:227], v227 offset:51712\n" \
"v_mfma_f32_16x16x32_bf16 v[144:147], a[200:203], a[136:139], v[144:147]\n" \
"ds_read_b64_tr_b16 a[228:229], v227 offset:53760\n" \
"v_mfma_f32_16x16x32_bf16 v[144:147], a[204:207], a[140:143], v[144:147]\n" \
"ds_read_b64_tr_b16 a[230:231], v227 offset:55808\n" \
"v_mfma_f32_16x16x32_bf16 v[148:151], a[200:203], a[152:155], v[148:151]\n" \
"s_mov_b32 s61, 262144\n" \
"buffer_load_dwordx4 v[192:195], v231, %[srdz], s61 offen offset:0 sc1\n" \
"v_mfma_f32_16x16x32_bf16 v[148:151], a[204:207], a[156:159], v[148:151]\n" \
"v_mfma_f32_16x16x32_bf16 v[152:155], a[200:203], a[168:171], v[152:155]\n" \
"buffer_load_dwordx4 v212, %[srdt], s49 offen offset:1024 lds\n" \
"v_mfma_f32_16x16x32_bf16 v[152:155], a[204:207], a[172:175], v[152:155]\n" \
"v_mfma_f32_16x16x32_bf16 v[156:159], a[200:203], a[184:187], v[156:159]\n" \
"ds_read_b64_tr_b16 a[236:237], v227 offset:57856\n" \
"v_mfma_f32_16x16x32_bf16 v[156:159], a[204:207], a[188:191], v[156:159]\n" \
"s_waitcnt vmcnt(63) lgkmcnt(5)\n" \
"s_nop 0\n" \
"v_mfma_f32_16x16x32_bf16 v[160:163], a[220:223], a[128:131], v[160:163]\n" \
"ds_read_b64_tr_b16 a[238:239], v227 offset:59904\n" \
"v_mfma_f32_16x16x32_bf16 v[160:163], a[212:215], a[132:135], v[160:163]\n" \
"ds_read_b64_tr_b16 a[232:233], v227 offset:61952\n" \
"v_mfma_f32_16x16x32_bf16 v[164:167], a[220:223], a[144:147], v[164:167]\n" \
"ds_read_b64_tr_b16 a[234:235], v227 offset:64000\n" \
"s_mov_b32 s61, 266240\n" \
"buffer_load_dwordx4 v[196:199], v231, %[srdz], s61 offen offset:0 sc1\n" \
"v_mfma_f32_16x16x32_bf16 v[164:167], a[212:215], a[148:151], v[164:167]\n" \
"v_mfma_f32_16x16x32_bf16 v[168:171], a[220:223], a[160:163], v[168:171]\n" \
"v_cvt_pk_bf16_f32 v128, v128, v129\n" \
"buffer_load_dwordx4 v213, %[srdt], s49 offen offset:2048 lds\n" \
"v_mfma_f32_16x16x32_bf16 v[168:171], a[212:215], a[164:167], v[168:171]\n" \
"v_cvt_pk_bf16_f32 v129, v130, v131\n" \
"v_mfma_f32_16x16x32_bf16 v[172:175], a[220:223], a[176:179], v[172:175]\n" \
"v_cvt_pk_bf16_f32 v130, v144, v145\n" \
"v_mfma_f32_16x16x32_bf16 v[172:175], a[212:215], a[180:183], v[172:175]\n" \
"v_cvt_pk_bf16_f32 v131, v146, v147\n" \
"v_mfma_f32_16x16x32_bf16 v[160:163], a[208:211], a[136:139], v[160:163]\n" \
"s_add_u32 s55, s46, 0\n" \
"s_nop 0\n" \
"buffer_store_dwordx4 v[128:131], %[vxs], %[srdz], s55 offen offset:0 sc1\n" \
"v_lshrrev_b32_e32 v230, 5, v224\n" \
"s_lshl_b32 s63, %[wid], 12\n" \
"v_mfma_f32_16x16x32_bf16 v[160:163], a[216:219], a[140:143], v[160:163]\n" \
"v_and_b32_e32 v230, 28, v230\n" \
"s_sub_u32 s63, %[m0b], s63\n" \
"v_mfma_f32_16x16x32_bf16 v[164:167], a[208:211], a[152:155], v[164:167]\n" \
"v_cvt_pk_bf16_f32 v132, v132, v133\n" \
"s_mov_b32 s61, 270336\n" \
"buffer_load_dwordx4 v[200:203], v231, %[srdz], s61 offen offset:0 sc1\n" \
"s_add_u32 s63, s63, 117760\n" \
"v_add_u32_e64 v230, s63, v230\n" \
"v_mfma_f32_16x16x32_bf16 v[164:167], a[216:219], a[156:159], v[164:167]\n" \
"v_cvt_pk_bf16_f32 v133, v134, v135\n" \
"v_mfma_f32_16x16x32_bf16 v[168:171], a[208:211], a[168:171], v[168:171]\n" \
"s_waitcnt vmcnt(14) lgkmcnt(0)\n" \
"s_barrier\n" \
"v_mfma_f32_16x16x32_bf16 v[168:171], a[216:219], a[172:175], v[168:171]\n" \
"ds_read_b64_tr_b16 a[196:197], v215 offset:0\n" \
"v_cvt_pk_bf16_f32 v134, v148, v149\n" \
"ds_read_b32 v235, v230 offset:0\n" \
"v_readlane_b32 s63, %[thi], 34\n" \
"v_mfma_f32_16x16x32_bf16 v[172:175], a[208:211], a[184:187], v[172:175]\n" \
"ds_read_b64_tr_b16 a[198:199], v215 offset:2048\n" \
"v_cvt_pk_bf16_f32 v135, v150, v151\n" \
"v_mfma_f32_16x16x32_bf16 v[172:175], a[216:219], a[188:191], v[172:175]\n" \
"s_waitcnt vmcnt(63) lgkmcnt(7)\n" \
"s_nop 0\n" \
"v_mfma_f32_16x16x32_bf16 v[176:179], a[224:227], a[128:131], v[176:179]\n" \
"ds_read_b64_tr_b16 a[200:201], v215 offset:4096\n" \
"v_cvt_pk_bf16_f32 v136, v136, v137\n" \
"v_mfma_f32_16x16x32_bf16 v[176:179], a[228:231], a[132:135], v[176:179]\n" \
"ds_read_b64_tr_b16 a[202:203], v215 offset:6144\n" \
"v_cvt_pk_bf16_f32 v137, v138, v139\n" \
"v_mfma_f32_16x16x32_bf16 v[180:183], a[224:227], a[144:147], v[180:183]\n" \
"ds_read_b64_tr_b16 a[204:205], v215 offset:8192\n" \
"s_add_u32 s55, s46, 304128\n" \
"buffer_store_dwordx4 v[132:135], %[vxs], %[srdz], s55 offen offset:0 sc1\n" \
"v_mfma_f32_16x16x32_bf16 v[180:183], a[228:231], a[148:151], v[180:183]\n" \
"ds_read_b64_tr_b16 a[206:207], v215 offset:10240\n" \
"v_cvt_pk_bf16_f32 v138, v152, v153\n" \
"v_mfma_f32_16x16x32_bf16 v[184:187], a[224:227], a[160:163], v[184:187]\n" \
"ds_read_b64_tr_b16 a[220:221], v215 offset:12288\n" \
"v_cvt_pk_bf16_f32 v139, v154, v155\n" \
"s_mov_b32 s61, 274432\n" \
"buffer_load_dwordx4 v[204:207], v231, %[srdz], s61 offen offset:0 sc1\n" \
"s_waitcnt lgkmcnt(6)\n" \
"s_nop 0\n" \
"v_min_u32_dpp v234, v235, v235 quad_perm:[1,0,3,2] row_mask:0xf bank_mask:0xf\n" \
"v_max_u32_dpp v235, v235, v235 quad_perm:[1,0,3,2] row_mask:0xf bank_mask:0xf\n" \
"v_mfma_f32_16x16x32_bf16 v[184:187], a[228:231], a[164:167], v[184:187]\n" \
"ds_read_b64_tr_b16 a[222:223], v215 offset:14336\n" \
"v_mfma_f32_16x16x32_bf16 v[188:191], a[224:227], a[176:179], v[188:191]\n" \
"ds_read_b64_tr_b16 a[212:213], v227 offset:0\n" \
"v_cvt_pk_bf16_f32 v140, v140, v141\n" \
"v_min_u32_dpp v216, v235, v235 quad_perm:[2,3,0,1] row_mask:0xf bank_mask:0xf\n" \
"v_max_u32_dpp v230, v234, v234 quad_perm:[2,3,0,1] row_mask:0xf bank_mask:0xf\n" \
"buffer_load_dwordx4 v214, %[srdt], s49 offen offset:3072 lds\n" \
"v_mfma_f32_16x16x32_bf16 v[188:191], a[228:231], a[180:183], v[188:191]\n" \
"ds_read_b64_tr_b16 a[214:215], v227 offset:2048\n" \
"v_cvt_pk_bf16_f32 v141, v142, v143\n" \
"v_max_u32_dpp v235, v235, v235 quad_perm:[2,3,0,1] row_mask:0xf bank_mask:0xf\n" \
"v_max_u32_e64 v234, v216, v230\n" \
"v_mfma_f32_16x16x32_bf16 v[176:179], a[236:239], a[136:139], v[176:179]\n" \
"ds_read_b64_tr_b16 a[208:209], v227 offset:4096\n" \
"s_add_u32 s55, s46, 608256\n" \
"buffer_store_dwordx4 v[136:139], %[vxs], %[srdz], s55 offen offset:0 sc1\n" \
"v_mfma_f32_16x16x32_bf16 v[176:179], a[232:235], a[140:143], v[176:179]\n" \
"ds_read_b64_tr_b16 a[210:211], v227 offset:6144\n" \
"v_cvt_pk_bf16_f32 v142, v156, v157\n" \
"v_min_u32_dpp v216, v235, v235 row_half_mirror row_mask:0xf bank_mask:0xf\n" \
"v_max_u32_dpp v230, v234, v234 row_half_mirror row_mask:0xf bank_mask:0xf\n" \
"v_mfma_f32_16x16x32_bf16 v[180:183], a[236:239], a[152:155], v[180:183]\n" \
"v_cvt_pk_bf16_f32 v143, v158, v159\n" \
"v_max_u32_dpp v235, v235, v235 row_half_mirror row_mask:0xf bank_mask:0xf\n" \
"v_max_u32_e64 v234, v216, v230\n" \
"v_mfma_f32_16x16x32_bf16 v[180:183], a[232:235], a[156:159], v[180:183]\n" \
"s_add_u32 s55, s46, 912384\n" \
"buffer_store_dwordx4 v[140:143], %[vxs], %[srdz], s55 offen offset:0 sc1\n" \
"v_lshlrev_b32_e64 v230, 4, v224\n" \
"v_mfma_f32_16x16x32_bf16 v[184:187], a[236:239], a[168:171], v[184:187]\n" \
"v_mfma_f32_16x16x32_bf16 v[184:187], a[232:235], a[172:175], v[184:187]\n" \
"global_load_dwordx2 v[232:233], v236, s[44:45]\n" \
"v_mfma_f32_16x16x32_bf16 v[188:191], a[236:239], a[184:187], v[188:191]\n" \
"ds_read_b64_tr_b16 a[216:217], v227 offset:8192\n" \
"v_mfma_f32_16x16x32_bf16 v[188:191], a[232:235], a[188:191], v[188:191]\n" \
"s_waitcnt vmcnt(63) lgkmcnt(5)\n" \
"s_nop 0\n" \
"v_mfma_f32_16x16x32_bf16 v[4:7], a[196:199], a[64:67], v[4:7]\n" \
"ds_read_b64_tr_b16 a[218:219], v227 offset:10240\n" \
"s_mov_b64 exec, 3\n" \
"s_nop 0\n" \
"buffer_atomic_or_x2 v[234:235], v230, %[srdz], s63 offen sc1\n" \
"s_mov_b64 exec, -1\n" \
"s_nop 0\n" \
"v_mfma_f32_16x16x32_bf16 v[4:7], a[200:203], a[68:71], v[4:7]\n" \
"ds_read_b64_tr_b16 a[224:225], v227 offset:12288\n" \
"v_mfma_f32_16x16x32_bf16 v[36:39], a[196:199], a[80:83], v[36:39]\n" \
"ds_read_b64_tr_b16 a[226:227], v227 offset:14336\n" \
"v_mfma_f32_16x16x32_bf16 v[36:39], a[200:203], a[84:87], v[36:39]\n" \
"v_mfma_f32_16x16x32_bf16 v[68:71], a[196:199], a[96:99], v[68:71]\n" \
"v_cvt_pk_bf16_f32 v160, v160, v161\n" \
"v_mfma_f32_16x16x32_bf16 v[68:71], a[200:203], a[100:103], v[68:71]\n" \
"v_cvt_pk_bf16_f32 v161, v162, v163\n" \
"v_mfma_f32_16x16x32_bf16 v[100:103], a[196:199], a[112:115], v[100:103]\n" \
"ds_read_b64_tr_b16 a[228:229], v215 offset:512\n" \
"v_cvt_pk_bf16_f32 v162, v176, v177\n" \
"v_mfma_f32_16x16x32_bf16 v[100:103], a[200:203], a[116:119], v[100:103]\n" \
"ds_read_b64_tr_b16 a[230:231], v215 offset:2560\n" \
"v_cvt_pk_bf16_f32 v163, v178, v179\n" \
"v_mfma_f32_16x16x32_bf16 v[4:7], a[204:207], a[72:75], v[4:7]\n" \
"ds_read_b64_tr_b16 a[236:237], v215 offset:4608\n" \
"v_mfma_f32_16x16x32_bf16 v[4:7], a[220:223], a[76:79], v[4:7]\n" \
"ds_read_b64_tr_b16 a[238:239], v215 offset:6656\n" \
"v_mfma_f32_16x16x32_bf16 v[36:39], a[204:207], a[88:91], v[36:39]\n" \
"s_add_u32 s55, s46, 4096\n" \
"buffer_store_dwordx4 v[160:163], %[vxs], %[srdz], s55 offen offset:0 sc1\n" \
"v_mfma_f32_16x16x32_bf16 v[36:39], a[220:223], a[92:95], v[36:39]\n" \
"v_cvt_pk_bf16_f32 v164, v164, v165\n" \
"v_cvt_pk_bf16_f32 v165, v166, v167\n" \
"v_mfma_f32_16x16x32_bf16 v[68:71], a[204:207], a[104:107], v[68:71]\n" \
"v_cvt_pk_bf16_f32 v166, v180, v181\n" \
"s_add_u32 m0, %[m0b], 49152\n" \
"s_cmp_gt_u32 s48, 6\n" \
"s_cselect_b32 s49, 6, 7\n" \
"s_lshl_b32 s49, s49, 18\n" \
"s_or_b32 s49, s49, s59\n" \
"buffer_load_dwordx4 %[wo0], %[srdt], s49 offen offset:0 lds\n" \
"v_mfma_f32_16x16x32_bf16 v[68:71], a[220:223], a[108:111], v[68:71]\n" \
"v_cvt_pk_bf16_f32 v167, v182, v183\n" \
"v_mfma_f32_16x16x32_bf16 v[100:103], a[204:207], a[120:123], v[100:103]\n" \
"ds_read_b64_tr_b16 a[232:233], v215 offset:8704\n" \
"v_mfma_f32_16x16x32_bf16 v[100:103], a[220:223], a[124:127], v[100:103]\n" \
"s_waitcnt vmcnt(63) lgkmcnt(5)\n" \
"s_nop 0\n" \
"v_mfma_f32_16x16x32_bf16 v[12:15], a[212:215], a[64:67], v[12:15]\n" \
"ds_read_b64_tr_b16 a[234:235], v215 offset:10752\n" \
"v_cvt_pk_bf16_f32 v168, v168, v169\n" \
"v_mfma_f32_16x16x32_bf16 v[12:15], a[208:211], a[68:71], v[12:15]\n" \
"ds_read_b64_tr_b16 a[196:197], v215 offset:12800\n" \
"s_add_u32 s55, s46, 308224\n" \
"buffer_store_dwordx4 v[164:167], %[vxs], %[srdz], s55 offen offset:0 sc1\n" \
"v_mfma_f32_16x16x32_bf16 v[44:47], a[212:215], a[80:83], v[44:47]\n" \
"ds_read_b64_tr_b16 a[198:199], v215 offset:14848\n" \
"v_cvt_pk_bf16_f32 v169, v170, v171\n" \
"v_mfma_f32_16x16x32_bf16 v[44:47], a[208:211], a[84:87], v[44:47]\n" \
"v_cvt_pk_bf16_f32 v170, v184, v185\n" \
"v_cvt_pk_bf16_f32 v171, v186, v187\n" \
"v_mfma_f32_16x16x32_bf16 v[76:79], a[212:215], a[96:99], v[76:79]\n" \
"s_add_u32 s55, s46, 612352\n" \
"buffer_store_dwordx4 v[168:171], %[vxs], %[srdz], s55 offen offset:0 sc1\n" \
"v_mfma_f32_16x16x32_bf16 v[76:79], a[208:211], a[100:103], v[76:79]\n" \
"v_mfma_f32_16x16x32_bf16 v[108:111], a[212:215], a[112:115], v[108:111]\n" \
"ds_read_b64_tr_b16 a[200:201], v227 offset:512\n" \
"v_cvt_pk_bf16_f32 v172, v172, v173\n" \
"buffer_load_dwordx4 v212, %[srdt], s49 offen offset:1024 lds\n" \
"v_mfma_f32_16x16x32_bf16 v[108:111], a[208:211], a[116:119], v[108:111]\n" \
"ds_read_b64_tr_b16 a[202:203], v227 offset:2560\n" \
"v_cvt_pk_bf16_f32 v173, v174, v175\n" \
"v_mfma_f32_16x16x32_bf16 v[12:15], a[216:219], a[72:75], v[12:15]\n" \
"ds_read_b64_tr_b16 a[204:205], v227 offset:4608\n" \
"v_cvt_pk_bf16_f32 v174, v188, v189\n" \
"v_mfma_f32_16x16x32_bf16 v[12:15], a[224:227], a[76:79], v[12:15]\n" \
"ds_read_b64_tr_b16 a[206:207], v227 offset:6656\n" \
"v_cvt_pk_bf16_f32 v175, v190, v191\n" \
"v_mfma_f32_16x16x32_bf16 v[44:47], a[216:219], a[88:91], v[44:47]\n" \
"s_add_u32 s55, s46, 916480\n" \
"buffer_store_dwordx4 v[172:175], %[vxs], %[srdz], s55 offen offset:0 sc1\n" \
"v_mfma_f32_16x16x32_bf16 v[44:47], a[224:227], a[92:95], v[44:47]\n" \
"v_mfma_f32_16x16x32_bf16 v[76:79], a[216:219], a[104:107], v[76:79]\n" \
"buffer_load_dwordx4 v213, %[srdt], s49 offen offset:2048 lds\n" \
"v_mfma_f32_16x16x32_bf16 v[76:79], a[224:227], a[108:111], v[76:79]\n" \
"v_mfma_f32_16x16x32_bf16 v[108:111], a[216:219], a[120:123], v[108:111]\n" \
"ds_read_b64_tr_b16 a[220:221], v227 offset:8704\n" \
"v_mfma_f32_16x16x32_bf16 v[108:111], a[224:227], a[124:127], v[108:111]\n" \
"s_waitcnt vmcnt(63) lgkmcnt(5)\n" \
"s_nop 0\n" \
"v_mfma_f32_16x16x32_bf16 v[20:23], a[228:231], a[64:67], v[20:23]\n" \
"ds_read_b64_tr_b16 a[222:223], v227 offset:10752\n" \
"v_mfma_f32_16x16x32_bf16 v[20:23], a[236:239], a[68:71], v[20:23]\n" \
"ds_read_b64_tr_b16 a[212:213], v227 offset:12800\n" \
"v_mfma_f32_16x16x32_bf16 v[52:55], a[228:231], a[80:83], v[52:55]\n" \
"ds_read_b64_tr_b16 a[214:215], v227 offset:14848\n" \
"v_mfma_f32_16x16x32_bf16 v[52:55], a[236:239], a[84:87], v[52:55]\n" \
"global_load_dword v237, %[vct], %[pct]\n" \
"v_mfma_f32_16x16x32_bf16 v[84:87], a[228:231], a[96:99], v[84:87]\n" \
"v_mfma_f32_16x16x32_bf16 v[84:87], a[236:239], a[100:103], v[84:87]\n" \
"v_xor_b32_e32 v218, 0x80, %[vdn]\n" \
"s_add_u32 m0, %[m0b], 114688\n" \
"s_mov_b32 s61, 16384\n" \
"buffer_load_dwordx4 v218, s[64:67], s61 offen offset:0 lds\n" \
"v_mfma_f32_16x16x32_bf16 v[116:119], a[228:231], a[112:115], v[116:119]\n" \
"v_mfma_f32_16x16x32_bf16 v[116:119], a[236:239], a[116:119], v[116:119]\n" \
"s_add_u32 m0, %[m0b], 49152\n" \
"s_nop 0\n" \
"s_nop 0\n" \
"buffer_load_dwordx4 v214, %[srdt], s49 offen offset:3072 lds\n" \
"v_mfma_f32_16x16x32_bf16 v[20:23], a[232:235], a[72:75], v[20:23]\n" \
"v_mfma_f32_16x16x32_bf16 v[20:23], a[196:199], a[76:79], v[20:23]\n" \
"v_mfma_f32_16x16x32_bf16 v[52:55], a[232:235], a[88:91], v[52:55]\n" \
"v_mfma_f32_16x16x32_bf16 v[52:55], a[196:199], a[92:95], v[52:55]\n" \
"v_mfma_f32_16x16x32_bf16 v[84:87], a[232:235], a[104:107], v[84:87]\n" \
"s_waitcnt vmcnt(24) lgkmcnt(0)\n" \
"s_barrier\n" \
"v_mfma_f32_16x16x32_bf16 v[84:87], a[196:199], a[108:111], v[84:87]\n" \
"ds_read_b64_tr_b16 a[208:209], v215 offset:16384\n" \
"v_mfma_f32_16x16x32_bf16 v[116:119], a[232:235], a[120:123], v[116:119]\n" \
"ds_read_b64_tr_b16 a[210:211], v215 offset:18432\n" \
"v_mfma_f32_16x16x32_bf16 v[116:119], a[196:199], a[124:127], v[116:119]\n" \
"s_waitcnt vmcnt(63) lgkmcnt(2)\n" \
"s_nop 0\n" \
"v_mfma_f32_16x16x32_bf16 v[28:31], a[200:203], a[64:67], v[28:31]\n" \
"ds_read_b64_tr_b16 a[216:217], v215 offset:20480\n" \
"v_mfma_f32_16x16x32_bf16 v[28:31], a[204:207], a[68:71], v[28:31]\n" \
"ds_read_b64_tr_b16 a[218:219], v215 offset:22528\n" \
"v_mfma_f32_16x16x32_bf16 v[60:63], a[200:203], a[80:83], v[60:63]\n" \
"ds_read_b64_tr_b16 a[224:225], v215 offset:24576\n" \
"v_mfma_f32_16x16x32_bf16 v[60:63], a[204:207], a[84:87], v[60:63]\n" \
"ds_read_b64_tr_b16 a[226:227], v215 offset:26624\n" \
"v_mfma_f32_16x16x32_bf16 v[92:95], a[200:203], a[96:99], v[92:95]\n" \
"ds_read_b64_tr_b16 a[228:229], v215 offset:28672\n" \
"v_xor_b32_e32 v218, 0xc0, %[vdn]\n" \
"s_add_u32 m0, %[m0b], 115712\n" \
"s_mov_b32 s61, 20480\n" \
"buffer_load_dwordx4 v218, s[64:67], s61 offen offset:0 lds\n" \
"v_mfma_f32_16x16x32_bf16 v[92:95], a[204:207], a[100:103], v[92:95]\n" \
"ds_read_b64_tr_b16 a[230:231], v215 offset:30720\n" \
"v_mfma_f32_16x16x32_bf16 v[124:127], a[200:203], a[112:115], v[124:127]\n" \
"ds_read_b64_tr_b16 a[236:237], v227 offset:16384\n" \
"v_mfma_f32_16x16x32_bf16 v[124:127], a[204:207], a[116:119], v[124:127]\n" \
"ds_read_b64_tr_b16 a[238:239], v227 offset:18432\n" \
"v_mfma_f32_16x16x32_bf16 v[28:31], a[220:223], a[72:75], v[28:31]\n" \
"ds_read_b64_tr_b16 a[232:233], v227 offset:20480\n" \
"v_mfma_f32_16x16x32_bf16 v[28:31], a[212:215], a[76:79], v[28:31]\n" \
"ds_read_b64_tr_b16 a[234:235], v227 offset:22528\n" \
"v_mfma_f32_16x16x32_bf16 v[60:63], a[220:223], a[88:91], v[60:63]\n" \
"v_mfma_f32_16x16x32_bf16 v[60:63], a[212:215], a[92:95], v[60:63]\n" \
"v_mfma_f32_16x16x32_bf16 v[92:95], a[220:223], a[104:107], v[92:95]\n" \
"v_mfma_f32_16x16x32_bf16 v[92:95], a[212:215], a[108:111], v[92:95]\n" \
"v_mfma_f32_16x16x32_bf16 v[124:127], a[220:223], a[120:123], v[124:127]\n" \
"ds_read_b64_tr_b16 a[196:197], v227 offset:24576\n" \
"v_mfma_f32_16x16x32_bf16 v[124:127], a[212:215], a[124:127], v[124:127]\n" \
"s_waitcnt vmcnt(63) lgkmcnt(5)\n" \
"s_nop 0\n" \
"v_mfma_f32_16x16x32_bf16 v[4:7], a[208:211], v[0:3], v[4:7]\n" \
"ds_read_b64_tr_b16 a[198:199], v227 offset:26624\n" \
"v_mfma_f32_16x16x32_bf16 v[4:7], a[216:219], v[8:11], v[4:7]\n" \
"ds_read_b64_tr_b16 a[200:201], v227 offset:28672\n" \
"v_mfma_f32_16x16x32_bf16 v[36:39], a[208:211], v[32:35], v[36:39]\n" \
"ds_read_b64_tr_b16 a[202:203], v227 offset:30720\n" \
"v_mfma_f32_16x16x32_bf16 v[36:39], a[216:219], v[40:43], v[36:39]\n" \
"s_waitcnt vmcnt(12)\n" \
"s_nop 0\n" \
"v_and_or_b32 v236, v192, v193, 0\n" \
"v_bitop3_b32 v236, v236, v194, v195 bitop3:0x80\n" \
"s_add_u32 m0, %[m0b], 116736\n" \
"s_mov_b32 s61, 24576\n" \
"buffer_load_dwordx4 %[vdn], s[64:67], s61 offen offset:0 lds\n" \
"v_mfma_f32_16x16x32_bf16 v[68:71], a[208:211], v[64:67], v[68:71]\n" \
".if . - adaln_body != 57344\n" \
".error \"size model\"\n" \
".endif\n" \
"Lk13_%=:\n" \
"s_cbranch_execz Lk14_%=\n" \
"s_nop 0\n" \
"v_bitop3_b32 v236, v236, v196, v197 bitop3:0x80\n" \
"v_mfma_f32_16x16x32_bf16 v[68:71], a[216:219], v[72:75], v[68:71]\n" \
"v_bitop3_b32 v236, v236, v198, v199 bitop3:0x80\n" \
"v_mfma_f32_16x16x32_bf16 v[100:103], a[208:211], v[96:99], v[100:103]\n" \
"v_bitop3_b32 v236, v236, v200, v201 bitop3:0x80\n" \
"v_mfma_f32_16x16x32_bf16 v[100:103], a[216:219], v[104:107], v[100:103]\n" \
"v_bitop3_b32 v236, v236, v202, v203 bitop3:0x80\n" \
"v_mfma_f32_16x16x32_bf16 v[4:7], a[224:227], v[16:19], v[4:7]\n" \
"v_bitop3_b32 v236, v236, v204, v205 bitop3:0x80\n" \
"v_mfma_f32_16x16x32_bf16 v[4:7], a[228:231], v[24:27], v[4:7]\n" \
"v_bitop3_b32 v236, v236, v206, v207 bitop3:0x80\n" \
"v_mfma_f32_16x16x32_bf16 v[36:39], a[224:227], v[48:51], v[36:39]\n" \
"s_mov_b32 s54, 0x07060302\n" \
"v_and_b32_e64 v236, 1, v236\n" \
"v_mfma_f32_16x16x32_bf16 v[36:39], a[228:231], v[56:59], v[36:39]\n" \
"v_and_b32_e32 v231, 0x170, v231\n" \
"v_mfma_f32_16x16x32_bf16 v[68:71], a[224:227], v[80:83], v[68:71]\n" \
"s_waitcnt vmcnt(16) lgkmcnt(15)\n" \
"s_barrier\n" \
"v_mfma_f32_16x16x32_bf16 v[68:71], a[228:231], v[88:91], v[68:71]\n" \
"ds_read_b64_tr_b16 a[204:205], v215 offset:32768\n" \
"v_cmp_eq_u32_e64 s[56:57], 0, v236\n" \
"v_mfma_f32_16x16x32_bf16 v[100:103], a[224:227], v[112:115], v[100:103]\n" \
"ds_read_b64_tr_b16 a[206:207], v215 offset:34816\n" \
"v_lshrrev_b32_e64 v236, 7, v224\n" \
"v_mfma_f32_16x16x32_bf16 v[100:103], a[228:231], v[120:123], v[100:103]\n" \
"s_waitcnt vmcnt(13) lgkmcnt(2)\n" \
"s_nop 0\n" \
"v_mfma_f32_16x16x32_bf16 v[12:15], a[236:239], v[0:3], v[12:15]\n" \
"ds_read_b64_tr_b16 a[220:221], v215 offset:36864\n" \
"v_and_or_b32 v231, v236, 8, v231\n" \
"v_mfma_f32_16x16x32_bf16 v[12:15], a[232:235], v[8:11], v[12:15]\n" \
"ds_read_b64_tr_b16 a[222:223], v215 offset:38912\n" \
"v_lshrrev_b32_e64 v236, 12, v224\n" \
"v_mfma_f32_16x16x32_bf16 v[44:47], a[236:239], v[32:35], v[44:47]\n" \
"ds_read_b64_tr_b16 a[212:213], v215 offset:40960\n" \
"v_lshl_or_b32 v231, v236, 7, v231\n" \
"v_mfma_f32_16x16x32_bf16 v[44:47], a[232:235], v[40:43], v[44:47]\n" \
"ds_read_b64_tr_b16 a[214:215], v215 offset:43008\n" \
"s_mul_i32 s61, %[wid], 3584\n" \
"s_sub_u32 s61, %[m0b], s61\n" \
"v_add_u32_e32 v231, s61, v231\n" \
"v_mfma_f32_16x16x32_bf16 v[76:79], a[236:239], v[64:67], v[76:79]\n" \
"ds_read_b64_tr_b16 a[208:209], v215 offset:45056\n" \
"s_cmp_lg_u64 s[56:57], 0\n" \
"s_cbranch_scc1 Lpars_%=\n" \
"Lparb_%=:\n" \
"v_mov_b32_e64 v234, 0\n" \
"v_mfma_f32_16x16x32_bf16 v[76:79], a[232:235], v[72:75], v[76:79]\n" \
"ds_read_b64_tr_b16 a[210:211], v215 offset:47104\n" \
"v_lshlrev_b32_e64 v184, 16, v232\n" \
"v_mfma_f32_16x16x32_bf16 v[108:111], a[236:239], v[96:99], v[108:111]\n" \
"ds_read_b64_tr_b16 a[216:217], v227 offset:32768\n" \
"v_and_b32_e32 v185, 0xffff0000, v232\n" \
"v_mfma_f32_16x16x32_bf16 v[108:111], a[232:235], v[104:107], v[108:111]\n" \
"ds_read_b64_tr_b16 a[218:219], v227 offset:34816\n" \
"v_lshlrev_b32_e32 v186, 16, v233\n" \
"v_xor_b32_e32 v218, 64, %[vdn]\n" \
"s_add_u32 m0, %[m0b], 117760\n" \
"s_mov_b32 s61, 28672\n" \
"buffer_load_dwordx4 v218, s[64:67], s61 offen offset:0 lds\n" \
"v_mfma_f32_16x16x32_bf16 v[12:15], a[196:199], v[16:19], v[12:15]\n" \
"ds_read_b64_tr_b16 a[224:225], v227 offset:36864\n" \
"v_and_b32_e32 v187, 0xffff0000, v233\n" \
"v_mfma_f32_16x16x32_bf16 v[12:15], a[200:203], v[24:27], v[12:15]\n" \
"ds_read_b64_tr_b16 a[226:227], v227 offset:38912\n" \
"v_mul_f32_e64 v188, v200, v184\n" \
"v_mfma_f32_16x16x32_bf16 v[44:47], a[196:199], v[48:51], v[44:47]\n" \
"v_mul_f32_e32 v189, v201, v185\n" \
"v_mul_f32_e32 v190, v202, v186\n" \
"v_mfma_f32_16x16x32_bf16 v[44:47], a[200:203], v[56:59], v[44:47]\n" \
"v_mul_f32_e32 v191, v203, v187\n" \
"v_mul_f32_e32 v235, v196, v184\n" \
"v_mfma_f32_16x16x32_bf16 v[76:79], a[196:199], v[80:83], v[76:79]\n" \
"v_fmac_f32_e32 v235, v197, v185\n" \
"s_nop 0\n" \
"v_mfma_f32_16x16x32_bf16 v[76:79], a[200:203], v[88:91], v[76:79]\n" \
"v_fmac_f32_e32 v235, v198, v186\n" \
"s_nop 0\n" \
"v_mfma_f32_16x16x32_bf16 v[108:111], a[196:199], v[112:115], v[108:111]\n" \
"ds_read_b64_tr_b16 a[228:229], v227 offset:40960\n" \
"v_fmac_f32_e32 v235, v199, v187\n" \
"s_nop 0\n" \
"v_mfma_f32_16x16x32_bf16 v[108:111], a[200:203], v[120:123], v[108:111]\n" \
"s_waitcnt vmcnt(63) lgkmcnt(5)\n" \
"s_nop 0\n" \
"v_mfma_f32_16x16x32_bf16 v[4:7], a[204:207], a[0:3], v[4:7]\n" \
"ds_read_b64_tr_b16 a[230:231], v227 offset:43008\n" \
"v_add_f32_e64 v184, v188, v189\n" \
"v_mfma_f32_16x16x32_bf16 v[4:7], a[220:223], a[4:7], v[4:7]\n" \
"ds_read_b64_tr_b16 a[236:237], v227 offset:45056\n" \
"v_add_f32_e64 v185, v190, v191\n" \
"v_mfma_f32_16x16x32_bf16 v[36:39], a[204:207], a[16:19], v[36:39]\n" \
"ds_read_b64_tr_b16 a[238:239], v227 offset:47104\n" \
"v_add_f32_e64 v184, v184, v185\n" \
"v_mfma_f32_16x16x32_bf16 v[36:39], a[220:223], a[20:23], v[36:39]\n" \
"s_waitcnt vmcnt(7)\n" \
"v_mov_b32_e32 v230, 1\n" \
"buffer_store_dword v230, v225, %[srdz], s62 offen offset:64 sc1\n" \
"v_mfma_f32_16x16x32_bf16 v[68:71], a[204:207], a[32:35], v[68:71]\n" \
"v_lshrrev_b32_e32 v236, 12, v224\n" \
"v_permlane32_swap_b32_e32 v184, v235\n" \
"v_mfma_f32_16x16x32_bf16 v[68:71], a[220:223], a[36:39], v[68:71]\n" \
"v_mov_b32_e64 v230, 1\n" \
"buffer_atomic_add v230, v225, %[srdz], s47 offen sc1\n" \
"v_mfma_f32_16x16x32_bf16 v[100:103], a[204:207], a[48:51], v[100:103]\n" \
"v_add_f32_e64 v184, v184, v235\n" \
"s_mul_i32 s61, %[wid], 4092\n" \
"v_mfma_f32_16x16x32_bf16 v[100:103], a[220:223], a[52:55], v[100:103]\n" \
"v_add_f32_dpp v184, v184, v184 quad_perm:[1,0,3,2] row_mask:0xf bank_mask:0xf\n" \
"s_sub_u32 s61, %[m0b], s61\n" \
"s_nop 0\n" \
"v_mfma_f32_16x16x32_bf16 v[4:7], a[212:215], a[8:11], v[4:7]\n" \
"v_add_f32_dpp v184, v184, v184 quad_perm:[2,3,0,1] row_mask:0xf bank_mask:0xf\n" \
"v_mfma_f32_16x16x32_bf16 v[4:7], a[208:211], a[12:15], v[4:7]\n" \
"v_lshl_add_u32 v236, v236, 4, s61\n" \
"v_mfma_f32_16x16x32_bf16 v[36:39], a[212:215], a[24:27], v[36:39]\n" \
"v_add_f32_dpp v184, v184, v184 row_ror:4 row_mask:0xf bank_mask:0xf\n" \
"v_mfma_f32_16x16x32_bf16 v[36:39], a[208:211], a[28:31], v[36:39]\n" \
"s_nop 0\n" \
"s_nop 0\n" \
"v_mfma_f32_16x16x32_bf16 v[68:71], a[212:215], a[40:43], v[68:71]\n" \
"s_waitcnt vmcnt(5) lgkmcnt(15)\n" \
"s_barrier\n" \
"v_mfma_f32_16x16x32_bf16 v[68:71], a[208:211], a[44:47], v[68:71]\n" \
"ds_read_b64_tr_b16 a[232:233], v215 offset:49152\n" \
"v_add_f32_dpp v184, v184, v184 row_ror:8 row_mask:0xf bank_mask:0xf\n" \
"v_mfma_f32_16x16x32_bf16 v[100:103], a[212:215], a[56:59], v[100:103]\n" \
"ds_read_b64_tr_b16 a[234:235], v215 offset:51200\n" \
"v_mov_b32_e64 v185, v184\n" \
"v_mfma_f32_16x16x32_bf16 v[100:103], a[208:211], a[60:63], v[100:103]\n" \
"s_waitcnt vmcnt(63) lgkmcnt(2)\n" \
"s_nop 0\n" \
"v_mfma_f32_16x16x32_bf16 v[12:15], a[216:219], a[0:3], v[12:15]\n" \
"ds_read_b64_tr_b16 a[196:197], v215 offset:53248\n" \
"s_nop 0\n" \
"s_nop 0\n" \
"v_mfma_f32_16x16x32_bf16 v[12:15], a[224:227], a[4:7], v[12:15]\n" \
"ds_read_b64_tr_b16 a[198:199], v215 offset:55296\n" \
"v_permlane16_swap_b32_e32 v184, v185\n" \
"s_nop 0\n" \
"v_mfma_f32_16x16x32_bf16 v[44:47], a[216:219], a[16:19], v[44:47]\n" \
"ds_read_b64_tr_b16 a[200:201], v215 offset:57344\n" \
"v_add_f32_e64 v184, v184, v185\n" \
"v_mfma_f32_16x16x32_bf16 v[44:47], a[224:227], a[20:23], v[44:47]\n" \
"ds_read_b64_tr_b16 a[202:203], v215 offset:59392\n" \
"v_bfe_u32 v186, v231, 3, 1\n" \
"v_mfma_f32_16x16x32_bf16 v[76:79], a[216:219], a[32:35], v[76:79]\n" \
"ds_read_b64_tr_b16 a[204:205], v215 offset:61440\n" \
"s_movk_i32 s61, 2040\n" \
"s_nop 0\n" \
"v_mfma_f32_16x16x32_bf16 v[76:79], a[224:227], a[36:39], v[76:79]\n" \
"ds_read_b64_tr_b16 a[206:207], v215 offset:63488\n" \
"v_mad_u32_u24 v186, v186, s61, v231\n" \
"v_mfma_f32_16x16x32_bf16 v[108:111], a[216:219], a[48:51], v[108:111]\n" \
"ds_read_b64_tr_b16 a[220:221], v227 offset:49152\n" \
"v_perm_b32 v196, v197, v196, s54\n" \
"v_mfma_f32_16x16x32_bf16 v[108:111], a[224:227], a[52:55], v[108:111]\n" \
"ds_read_b64_tr_b16 a[222:223], v227 offset:51200\n" \
"v_perm_b32 v197, v199, v198, s54\n" \
"v_mfma_f32_16x16x32_bf16 v[12:15], a[228:231], a[8:11], v[12:15]\n" \
"ds_read_b64_tr_b16 a[212:213], v227 offset:53248\n" \
"v_perm_b32 v200, v201, v200, s54\n" \
"v_mfma_f32_16x16x32_bf16 v[12:15], a[236:239], a[12:15], v[12:15]\n" \
"ds_read_b64_tr_b16 a[214:215], v227 offset:55296\n" \
"v_perm_b32 v201, v203, v202, s54\n" \
"v_mfma_f32_16x16x32_bf16 v[44:47], a[228:231], a[24:27], v[44:47]\n" \
"v_perm_b32 v204, v205, v204, s54\n" \
"v_perm_b32 v205, v207, v206, s54\n" \
"v_mfma_f32_16x16x32_bf16 v[44:47], a[236:239], a[28:31], v[44:47]\n" \
"ds_write_b32 v236, v184 offset:14368\n" \
"v_mfma_f32_16x16x32_bf16 v[76:79], a[228:231], a[40:43], v[76:79]\n" \
"ds_write_b32 v236, v234 offset:14336\n" \
"v_mfma_f32_16x16x32_bf16 v[76:79], a[236:239], a[44:47], v[76:79]\n" \
"ds_write_b128 v186, v[192:195] offset:6144\n" \
"v_mfma_f32_16x16x32_bf16 v[108:111], a[228:231], a[56:59], v[108:111]\n" \
"ds_read_b64_tr_b16 a[208:209], v227 offset:57344\n" \
"ds_write_b128 v186, v[188:191] offset:10240\n" \
"v_mfma_f32_16x16x32_bf16 v[108:111], a[236:239], a[60:63], v[108:111]\n" \
"s_waitcnt vmcnt(63) lgkmcnt(9)\n" \
"s_nop 0\n" \
"v_mfma_f32_16x16x32_bf16 v[4:7], a[232:235], a[128:131], v[4:7]\n" \
"ds_read_b64_tr_b16 a[210:211], v227 offset:59392\n" \
"ds_write_b64 v231, v[196:197] offset:0\n" \
"v_mfma_f32_16x16x32_bf16 v[4:7], a[196:199], a[132:135], v[4:7]\n" \
"ds_read_b64_tr_b16 a[216:217], v227 offset:61440\n" \
"ds_write_b64 v231, v[200:201] offset:2048\n" \
"v_mfma_f32_16x16x32_bf16 v[36:39], a[232:235], a[144:147], v[36:39]\n" \
"ds_read_b64_tr_b16 a[218:219], v227 offset:63488\n" \
"ds_write_b64 v231, v[204:205] offset:4096\n" \
"v_mfma_f32_16x16x32_bf16 v[36:39], a[196:199], a[148:151], v[36:39]\n" \
"v_mfma_f32_16x16x32_bf16 v[68:71], a[232:235], a[160:163], v[68:71]\n" \
"v_mfma_f32_16x16x32_bf16 v[68:71], a[196:199], a[164:167], v[68:71]\n" \
"v_mfma_f32_16x16x32_bf16 v[100:103], a[232:235], a[176:179], v[100:103]\n" \
"ds_read_b64_tr_b16 a[224:225], v215 offset:16896\n" \
"v_mfma_f32_16x16x32_bf16 v[100:103], a[196:199], a[180:183], v[100:103]\n" \
"ds_read_b64_tr_b16 a[226:227], v215 offset:18944\n" \
"v_mfma_f32_16x16x32_bf16 v[4:7], a[200:203], a[136:139], v[4:7]\n" \
"ds_read_b64_tr_b16 a[228:229], v215 offset:20992\n" \
"v_mfma_f32_16x16x32_bf16 v[4:7], a[204:207], a[140:143], v[4:7]\n" \
"ds_read_b64_tr_b16 a[230:231], v215 offset:23040\n" \
"v_mfma_f32_16x16x32_bf16 v[36:39], a[200:203], a[152:155], v[36:39]\n" \
"s_waitcnt vmcnt(63) lgkmcnt(4)\n" \
"s_barrier\n" \
"v_mfma_f32_16x16x32_bf16 v[36:39], a[204:207], a[156:159], v[36:39]\n" \
"v_mov_b32_e64 v192, s40\n" \
"v_mfma_f32_16x16x32_bf16 v[68:71], a[200:203], a[168:171], v[68:71]\n" \
"ds_read_b128 v[196:199], v192 offset:14368\n" \
"v_mfma_f32_16x16x32_bf16 v[68:71], a[204:207], a[172:175], v[68:71]\n" \
"ds_read_b128 v[128:131], %[kb0] offset:6144\n" \
"v_mfma_f32_16x16x32_bf16 v[100:103], a[200:203], a[184:187], v[100:103]\n" \
"ds_read_b64_tr_b16 a[236:237], v215 offset:25088\n" \
"ds_read_b128 v[132:135], %[kb0] offset:8192\n" \
"v_mfma_f32_16x16x32_bf16 v[100:103], a[204:207], a[188:191], v[100:103]\n" \
"s_waitcnt vmcnt(63) lgkmcnt(9)\n" \
"s_nop 0\n" \
"v_mfma_f32_16x16x32_bf16 v[12:15], a[220:223], a[128:131], v[12:15]\n" \
"ds_read_b64_tr_b16 a[238:239], v215 offset:27136\n" \
"ds_read_b128 v[136:139], %[kb0] offset:7168\n" \
"v_mfma_f32_16x16x32_bf16 v[12:15], a[212:215], a[132:135], v[12:15]\n" \
"ds_read_b64_tr_b16 a[232:233], v215 offset:29184\n" \
"ds_read_b128 v[140:143], %[kb0] offset:9216\n" \
"v_mfma_f32_16x16x32_bf16 v[44:47], a[220:223], a[144:147], v[44:47]\n" \
"ds_read_b64_tr_b16 a[234:235], v215 offset:31232\n" \
"ds_read_b128 v[144:147], %[vvb] offset:6144\n" \
"v_mfma_f32_16x16x32_bf16 v[44:47], a[212:215], a[148:151], v[44:47]\n" \
"ds_read_b128 v[148:151], %[vvb] offset:8192\n" \
"v_mfma_f32_16x16x32_bf16 v[76:79], a[220:223], a[160:163], v[76:79]\n" \
"ds_read_b128 v[152:155], %[vvb] offset:7168\n" \
"v_mfma_f32_16x16x32_bf16 v[76:79], a[212:215], a[164:167], v[76:79]\n" \
"ds_read_b128 v[156:159], %[vvb] offset:9216\n" \
"v_mfma_f32_16x16x32_bf16 v[108:111], a[220:223], a[176:179], v[108:111]\n" \
"ds_read_b64_tr_b16 a[196:197], v227 offset:16896\n" \
"ds_read_b128 v[160:163], %[kb0] offset:10240\n" \
"v_mfma_f32_16x16x32_bf16 v[108:111], a[212:215], a[180:183], v[108:111]\n" \
"ds_read_b64_tr_b16 a[198:199], v227 offset:18944\n" \
"ds_read_b128 v[164:167], %[kb0] offset:12288\n" \
"v_mfma_f32_16x16x32_bf16 v[12:15], a[208:211], a[136:139], v[12:15]\n" \
"ds_read_b64_tr_b16 a[200:201], v227 offset:20992\n" \
"ds_read_b128 v[168:171], %[kb0] offset:11264\n" \
"v_mfma_f32_16x16x32_bf16 v[12:15], a[216:219], a[140:143], v[12:15]\n" \
"ds_read_b64_tr_b16 a[202:203], v227 offset:23040\n" \
"ds_read_b128 v[172:175], %[kb0] offset:13312\n" \
"v_mfma_f32_16x16x32_bf16 v[44:47], a[208:211], a[152:155], v[44:47]\n" \
"ds_read_b128 v[176:179], %[vvb] offset:10240\n" \
"v_mfma_f32_16x16x32_bf16 v[44:47], a[216:219], a[156:159], v[44:47]\n" \
"ds_read_b128 v[180:183], %[vvb] offset:12288\n" \
"v_mfma_f32_16x16x32_bf16 v[76:79], a[208:211], a[168:171], v[76:79]\n" \
"ds_read_b128 v[184:187], %[vvb] offset:11264\n" \
"v_mfma_f32_16x16x32_bf16 v[76:79], a[216:219], a[172:175], v[76:79]\n" \
"ds_read_b128 v[188:191], %[vvb] offset:13312\n" \
"v_mfma_f32_16x16x32_bf16 v[108:111], a[208:211], a[184:187], v[108:111]\n" \
"ds_read_b64_tr_b16 a[204:205], v227 offset:25088\n" \
"s_waitcnt lgkmcnt(14)\n" \
"v_add_f32_e32 v223, v196, v197\n" \
"v_mfma_f32_16x16x32_bf16 v[108:111], a[216:219], a[188:191], v[108:111]\n" \
"s_waitcnt vmcnt(63) lgkmcnt(14)\n" \
"s_nop 0\n" \
"v_mfma_f32_16x16x32_bf16 v[20:23], a[224:227], v[0:3], v[20:23]\n" \
"ds_read_b64_tr_b16 a[206:207], v227 offset:27136\n" \
"v_add_f32_e64 v198, v198, v199\n" \
"v_mfma_f32_16x16x32_bf16 v[20:23], a[228:231], v[8:11], v[20:23]\n" \
"ds_read_b64_tr_b16 a[220:221], v227 offset:29184\n" \
"v_add_f32_e64 v223, v223, v198\n" \
"v_mfma_f32_16x16x32_bf16 v[52:55], a[224:227], v[32:35], v[52:55]\n" \
"ds_read_b64_tr_b16 a[222:223], v227 offset:31232\n" \
"ds_read_b128 v[192:195], %[kb0] offset:2048\n" \
"v_mfma_f32_16x16x32_bf16 v[52:55], a[228:231], v[40:43], v[52:55]\n" \
"ds_read_b128 v[196:199], %[kb0] offset:3072\n" \
"v_mfma_f32_16x16x32_bf16 v[84:87], a[224:227], v[64:67], v[84:87]\n" \
"v_cvt_pk_bf16_f32 v4, v4, v5\n" \
"ds_read_b128 v[200:203], %[vvb] offset:2048\n" \
"v_mfma_f32_16x16x32_bf16 v[84:87], a[228:231], v[72:75], v[84:87]\n" \
"v_cvt_pk_bf16_f32 v5, v6, v7\n" \
"ds_read_b128 v[204:207], %[vvb] offset:3072\n" \
"v_mfma_f32_16x16x32_bf16 v[116:119], a[224:227], v[96:99], v[116:119]\n" \
"ds_read_b64_tr_b16 a[212:213], v215 offset:33280\n" \
"v_cvt_pk_bf16_f32 v6, v12, v13\n" \
"v_mfma_f32_16x16x32_bf16 v[116:119], a[228:231], v[104:107], v[116:119]\n" \
"ds_read_b64_tr_b16 a[214:215], v215 offset:35328\n" \
"v_cvt_pk_bf16_f32 v7, v14, v15\n" \
"v_mfma_f32_16x16x32_bf16 v[20:23], a[236:239], v[16:19], v[20:23]\n" \
"ds_read_b64_tr_b16 a[208:209], v215 offset:37376\n" \
"v_mfma_f32_16x16x32_bf16 v[20:23], a[232:235], v[24:27], v[20:23]\n" \
"ds_read_b64_tr_b16 a[210:211], v215 offset:39424\n" \
"v_mfma_f32_16x16x32_bf16 v[52:55], a[236:239], v[48:51], v[52:55]\n" \
"s_add_u32 s55, s46, 8192\n" \
"buffer_store_dwordx4 v[4:7], %[vxs], %[srdz], s55 offen offset:0 sc1\n" \
"v_mfma_f32_16x16x32_bf16 v[52:55], a[232:235], v[56:59], v[52:55]\n" \
"v_cvt_pk_bf16_f32 v36, v36, v37\n" \
"v_cvt_pk_bf16_f32 v37, v38, v39\n" \
"v_mfma_f32_16x16x32_bf16 v[84:87], a[236:239], v[80:83], v[84:87]\n" \
"v_cvt_pk_bf16_f32 v38, v44, v45\n" \
"v_mfma_f32_16x16x32_bf16 v[84:87], a[232:235], v[88:91], v[84:87]\n" \
"v_cvt_pk_bf16_f32 v39, v46, v47\n" \
"v_mfma_f32_16x16x32_bf16 v[116:119], a[236:239], v[112:115], v[116:119]\n" \
"ds_read_b64_tr_b16 a[216:217], v215 offset:41472\n" \
"v_mfma_f32_16x16x32_bf16 v[116:119], a[232:235], v[120:123], v[116:119]\n" \
"s_waitcnt vmcnt(63) lgkmcnt(9)\n" \
"s_nop 0\n" \
"v_mfma_f32_16x16x32_bf16 v[28:31], a[196:199], v[0:3], v[28:31]\n" \
"ds_read_b64_tr_b16 a[218:219], v215 offset:43520\n" \
"v_cvt_pk_bf16_f32 v68, v68, v69\n" \
"v_mfma_f32_16x16x32_bf16 v[28:31], a[200:203], v[8:11], v[28:31]\n" \
"ds_read_b64_tr_b16 a[224:225], v215 offset:45568\n" \
"s_add_u32 s55, s46, 312320\n" \
"buffer_store_dwordx4 v[36:39], %[vxs], %[srdz], s55 offen offset:0 sc1\n" \
"v_mfma_f32_16x16x32_bf16 v[60:63], a[196:199], v[32:35], v[60:63]\n" \
"ds_read_b64_tr_b16 a[226:227], v215 offset:47616\n" \
"v_cvt_pk_bf16_f32 v69, v70, v71\n" \
"v_mfma_f32_16x16x32_bf16 v[60:63], a[200:203], v[40:43], v[60:63]\n" \
"v_cvt_pk_bf16_f32 v70, v76, v77\n" \
"v_cvt_pk_bf16_f32 v71, v78, v79\n" \
"v_mfma_f32_16x16x32_bf16 v[92:95], a[196:199], v[64:67], v[92:95]\n" \
"s_add_u32 s55, s46, 616448\n" \
"buffer_store_dwordx4 v[68:71], %[vxs], %[srdz], s55 offen offset:0 sc1\n" \
"v_mfma_f32_16x16x32_bf16 v[92:95], a[200:203], v[72:75], v[92:95]\n" \
"v_accvgpr_read_b32 v1, a240\n" \
"v_mfma_f32_16x16x32_bf16 v[124:127], a[196:199], v[96:99], v[124:127]\n" \
"ds_read_b64_tr_b16 a[228:229], v227 offset:33280\n" \
"v_cvt_pk_bf16_f32 v100, v100, v101\n" \
"v_mfma_f32_16x16x32_bf16 v[124:127], a[200:203], v[104:107], v[124:127]\n" \
"ds_read_b64_tr_b16 a[230:231], v227 offset:35328\n" \
"v_cvt_pk_bf16_f32 v101, v102, v103\n" \
"v_mfma_f32_16x16x32_bf16 v[28:31], a[204:207], v[16:19], v[28:31]\n" \
"ds_read_b64_tr_b16 a[236:237], v227 offset:37376\n" \
"v_cvt_pk_bf16_f32 v102, v108, v109\n" \
"v_readlane_b32 s68, %[thi], 18\n" \
"v_lshlrev_b32_e64 v226, 7, v226\n" \
"v_mfma_f32_16x16x32_bf16 v[28:31], a[220:223], v[24:27], v[28:31]\n" \
"ds_read_b64_tr_b16 a[238:239], v227 offset:39424\n" \
"v_cvt_pk_bf16_f32 v103, v110, v111\n" \
"v_mfma_f32_16x16x32_bf16 v[60:63], a[204:207], v[48:51], v[60:63]\n" \
"s_add_u32 s55, s46, 920576\n" \
"buffer_store_dwordx4 v[100:103], %[vxs], %[srdz], s55 offen offset:0 sc1\n" \
"v_mfma_f32_16x16x32_bf16 v[60:63], a[220:223], v[56:59], v[60:63]\n" \
"v_lshlrev_b32_e64 v0, 16, v1\n" \
"v_mfma_f32_16x16x32_bf16 v[92:95], a[204:207], v[80:83], v[92:95]\n" \
"v_mov_b32_e64 v230, 64\n" \
"buffer_atomic_add v230, v225, %[srdz], s47 offen sc1\n" \
"v_mfma_f32_16x16x32_bf16 v[92:95], a[220:223], v[88:91], v[92:95]\n" \
"v_and_b32_e64 v1, s43, v1\n" \
"v_mfma_f32_16x16x32_bf16 v[124:127], a[204:207], v[112:115], v[124:127]\n" \
"ds_read_b64_tr_b16 a[232:233], v227 offset:41472\n" \
"v_accvgpr_read_b32 v3, a241\n" \
"v_mfma_f32_16x16x32_bf16 v[124:127], a[220:223], v[120:123], v[124:127]\n" \
"s_waitcnt vmcnt(63) lgkmcnt(5)\n" \
"s_nop 0\n" \
"v_mfma_f32_16x16x32_bf16 v[20:23], a[212:215], a[0:3], v[20:23]\n" \
"ds_read_b64_tr_b16 a[234:235], v227 offset:43520\n" \
"v_lshlrev_b32_e64 v2, 16, v3\n" \
"v_mfma_f32_16x16x32_bf16 v[20:23], a[208:211], a[4:7], v[20:23]\n" \
"ds_read_b64_tr_b16 a[196:197], v227 offset:45568\n" \
"v_and_b32_e64 v3, s43, v3\n" \
"v_mfma_f32_16x16x32_bf16 v[52:55], a[212:215], a[16:19], v[52:55]\n" \
"ds_read_b64_tr_b16 a[198:199], v227 offset:47616\n" \
"v_accvgpr_read_b32 v9, a244\n" \
"v_mfma_f32_16x16x32_bf16 v[52:55], a[208:211], a[20:23], v[52:55]\n" \
"buffer_load_dword v216, v226, %[srdz], s68 offen offset:64 sc1\n" \
"v_mfma_f32_16x16x32_bf16 v[84:87], a[212:215], a[32:35], v[84:87]\n" \
"v_mov_b32_e32 v226, 0\n" \
"v_lshrrev_b32_e32 v217, 3, v224\n" \
"s_add_u32 s68, %[sin], 4096\n" \
"v_lshlrev_b32_e64 v8, 16, v9\n" \
"v_mfma_f32_16x16x32_bf16 v[84:87], a[208:211], a[36:39], v[84:87]\n" \
"v_and_b32_e64 v9, s43, v9\n" \
"v_mfma_f32_16x16x32_bf16 v[116:119], a[212:215], a[48:51], v[116:119]\n" \
"ds_read_b64_tr_b16 a[200:201], v215 offset:49664\n" \
"v_accvgpr_read_b32 v11, a245\n" \
"v_mfma_f32_16x16x32_bf16 v[116:119], a[208:211], a[52:55], v[116:119]\n" \
"ds_read_b64_tr_b16 a[202:203], v215 offset:51712\n" \
"v_lshlrev_b32_e64 v10, 16, v11\n" \
"v_mfma_f32_16x16x32_bf16 v[20:23], a[216:219], a[8:11], v[20:23]\n" \
"ds_read_b64_tr_b16 a[204:205], v215 offset:53760\n" \
"v_and_b32_e64 v11, s43, v11\n" \
"v_mfma_f32_16x16x32_bf16 v[20:23], a[224:227], a[12:15], v[20:23]\n" \
"ds_read_b64_tr_b16 a[206:207], v215 offset:55808\n" \
"v_accvgpr_read_b32 v33, a248\n" \
"v_mfma_f32_16x16x32_bf16 v[52:55], a[216:219], a[24:27], v[52:55]\n" \
"ds_read_b64_tr_b16 a[220:221], v215 offset:57856\n" \
"v_lshlrev_b32_e64 v32, 16, v33\n" \
"v_mfma_f32_16x16x32_bf16 v[52:55], a[224:227], a[28:31], v[52:55]\n" \
"ds_read_b64_tr_b16 a[222:223], v215 offset:59904\n" \
"v_and_b32_e64 v33, s43, v33\n" \
"v_mfma_f32_16x16x32_bf16 v[84:87], a[216:219], a[40:43], v[84:87]\n" \
"ds_read_b64_tr_b16 a[212:213], v215 offset:61952\n" \
"v_accvgpr_read_b32 v35, a249\n" \
"v_mfma_f32_16x16x32_bf16 v[84:87], a[224:227], a[44:47], v[84:87]\n" \
"ds_read_b64_tr_b16 a[214:215], v215 offset:64000\n" \
"v_lshlrev_b32_e64 v34, 16, v35\n" \
"v_mfma_f32_16x16x32_bf16 v[116:119], a[216:219], a[56:59], v[116:119]\n" \
"ds_read_b64_tr_b16 a[208:209], v227 offset:49664\n" \
"v_and_b32_e64 v35, s43, v35\n" \
"v_mfma_f32_16x16x32_bf16 v[116:119], a[224:227], a[60:63], v[116:119]\n" \
"s_waitcnt vmcnt(63) lgkmcnt(9)\n" \
"s_nop 0\n" \
"v_mfma_f32_16x16x32_bf16 v[28:31], a[228:231], a[0:3], v[28:31]\n" \
"ds_read_b64_tr_b16 a[210:211], v227 offset:51712\n" \
"v_accvgpr_read_b32 v41, a252\n" \
"v_mfma_f32_16x16x32_bf16 v[28:31], a[236:239], a[4:7], v[28:31]\n" \
"ds_read_b64_tr_b16 a[216:217], v227 offset:53760\n" \
"v_lshlrev_b32_e64 v40, 16, v41\n" \
"v_mfma_f32_16x16x32_bf16 v[60:63], a[228:231], a[16:19], v[60:63]\n" \
"ds_read_b64_tr_b16 a[218:219], v227 offset:55808\n" \
"v_and_b32_e64 v41, s43, v41\n" \
"v_mfma_f32_16x16x32_bf16 v[60:63], a[236:239], a[20:23], v[60:63]\n" \
"ds_read_b64_tr_b16 a[224:225], v227 offset:57856\n" \
"v_accvgpr_read_b32 v43, a253\n" \
"v_mfma_f32_16x16x32_bf16 v[92:95], a[228:231], a[32:35], v[92:95]\n" \
"ds_read_b64_tr_b16 a[226:227], v227 offset:59904\n" \
"v_lshlrev_b32_e64 v42, 16, v43\n" \
"v_mfma_f32_16x16x32_bf16 v[92:95], a[236:239], a[36:39], v[92:95]\n" \
"v_and_b32_e64 v43, s43, v43\n" \
"v_mfma_f32_16x16x32_bf16 v[124:127], a[228:231], a[48:51], v[124:127]\n" \
"v_readlane_b32 s52, %[tlo], 35\n" \
"v_lshrrev_b32_e64 v44, 2, v224\n" \
"v_accvgpr_read_b32 v17, a242\n" \
"v_mfma_f32_16x16x32_bf16 v[124:127], a[236:239], a[52:55], v[124:127]\n" \
"ds_read_b64_tr_b16 a[228:229], v227 offset:61952\n" \
"v_lshlrev_b32_e64 v16, 16, v17\n" \
"v_mfma_f32_16x16x32_bf16 v[28:31], a[232:235], a[8:11], v[28:31]\n" \
"ds_read_b64_tr_b16 a[230:231], v227 offset:64000\n" \
"v_and_b32_e64 v17, s43, v17\n" \
"v_mfma_f32_16x16x32_bf16 v[28:31], a[196:199], a[12:15], v[28:31]\n" \
"v_accvgpr_read_b32 v19, a243\n" \
"v_mfma_f32_16x16x32_bf16 v[60:63], a[232:235], a[24:27], v[60:63]\n" \
"v_lshlrev_b32_e64 v18, 16, v19\n" \
"v_mfma_f32_16x16x32_bf16 v[60:63], a[196:199], a[28:31], v[60:63]\n" \
"v_and_b32_e64 v19, s43, v19\n" \
"v_mfma_f32_16x16x32_bf16 v[92:95], a[232:235], a[40:43], v[92:95]\n" \
"v_accvgpr_read_b32 v25, a246\n" \
"v_mfma_f32_16x16x32_bf16 v[92:95], a[196:199], a[44:47], v[92:95]\n" \
"v_lshlrev_b32_e64 v24, 16, v25\n" \
"v_mfma_f32_16x16x32_bf16 v[124:127], a[232:235], a[56:59], v[124:127]\n" \
"buffer_load_dwordx4 v[68:71], v44, %[srdz], s52 offen offset:0 sc1\n" \
"v_mfma_f32_16x16x32_bf16 v[124:127], a[196:199], a[60:63], v[124:127]\n" \
"s_waitcnt vmcnt(63) lgkmcnt(0)\n" \
"s_nop 0\n" \
"v_mfma_f32_16x16x32_bf16 v[20:23], a[200:203], a[128:131], v[20:23]\n" \
"buffer_load_dwordx4 v[76:79], v44, %[srdz], s52 offen offset:16 sc1\n" \
"v_mfma_f32_16x16x32_bf16 v[20:23], a[204:207], a[132:135], v[20:23]\n" \
"v_and_b32_e64 v25, s43, v25\n" \
"v_mfma_f32_16x16x32_bf16 v[20:23], a[220:223], a[136:139], v[20:23]\n" \
"v_accvgpr_read_b32 v27, a247\n" \
"v_mfma_f32_16x16x32_bf16 v[20:23], a[212:215], a[140:143], v[20:23]\n" \
"v_lshlrev_b32_e64 v26, 16, v27\n" \
"v_mfma_f32_16x16x32_bf16 v[28:31], a[208:211], a[128:131], v[28:31]\n" \
"v_and_b32_e64 v27, s43, v27\n" \
"v_mfma_f32_16x16x32_bf16 v[28:31], a[216:219], a[132:135], v[28:31]\n" \
"v_accvgpr_read_b32 v49, a250\n" \
"v_mfma_f32_16x16x32_bf16 v[28:31], a[224:227], a[136:139], v[28:31]\n" \
"v_lshlrev_b32_e64 v48, 16, v49\n" \
"v_mfma_f32_16x16x32_bf16 v[28:31], a[228:231], a[140:143], v[28:31]\n" \
"v_and_b32_e64 v49, s43, v49\n" \
"v_mfma_f32_16x16x32_bf16 v[52:55], a[200:203], a[144:147], v[52:55]\n" \
"v_accvgpr_read_b32 v51, a251\n" \
"v_mfma_f32_16x16x32_bf16 v[52:55], a[204:207], a[148:151], v[52:55]\n" \
"v_lshlrev_b32_e64 v50, 16, v51\n" \
"v_mfma_f32_16x16x32_bf16 v[52:55], a[220:223], a[152:155], v[52:55]\n" \
"v_and_b32_e64 v51, s43, v51\n" \
"v_mfma_f32_16x16x32_bf16 v[52:55], a[212:215], a[156:159], v[52:55]\n" \
"v_cvt_pk_bf16_f32 v20, v20, v21\n" \
"v_cvt_pk_bf16_f32 v21, v22, v23\n" \
"v_accvgpr_read_b32 v57, a254\n" \
"v_mfma_f32_16x16x32_bf16 v[60:63], a[208:211], a[144:147], v[60:63]\n" \
"v_lshlrev_b32_e64 v56, 16, v57\n" \
"v_mfma_f32_16x16x32_bf16 v[60:63], a[216:219], a[148:151], v[60:63]\n" \
"v_cvt_pk_bf16_f32 v22, v28, v29\n" \
"v_cvt_pk_bf16_f32 v23, v30, v31\n" \
"v_and_b32_e64 v57, s43, v57\n" \
"v_mfma_f32_16x16x32_bf16 v[60:63], a[224:227], a[152:155], v[60:63]\n" \
"s_add_u32 s55, s46, 12288\n" \
"buffer_store_dwordx4 v[20:23], %[vxs], %[srdz], s55 offen offset:0 sc1\n" \
"v_mfma_f32_16x16x32_bf16 v[60:63], a[228:231], a[156:159], v[60:63]\n" \
"v_mfma_f32_16x16x32_bf16 v[84:87], a[200:203], a[160:163], v[84:87]\n" \
"v_accvgpr_read_b32 v59, a255\n" \
"v_mfma_f32_16x16x32_bf16 v[84:87], a[204:207], a[164:167], v[84:87]\n" \
"v_lshlrev_b32_e64 v58, 16, v59\n" \
"v_mfma_f32_16x16x32_bf16 v[84:87], a[220:223], a[168:171], v[84:87]\n" \
"v_and_b32_e64 v59, s43, v59\n" \
"v_mfma_f32_16x16x32_bf16 v[84:87], a[212:215], a[172:175], v[84:87]\n" \
"v_cvt_pk_bf16_f32 v52, v52, v53\n" \
"v_cvt_pk_bf16_f32 v53, v54, v55\n" \
"v_mfma_f32_16x16x32_bf16 v[92:95], a[208:211], a[160:163], v[92:95]\n" \
"v_mfma_f32_16x16x32_bf16 v[92:95], a[216:219], a[164:167], v[92:95]\n" \
"v_cvt_pk_bf16_f32 v54, v60, v61\n" \
"v_cvt_pk_bf16_f32 v55, v62, v63\n" \
"v_mfma_f32_16x16x32_bf16 v[92:95], a[224:227], a[168:171], v[92:95]\n" \
"s_add_u32 s55, s46, 316416\n" \
"buffer_store_dwordx4 v[52:55], %[vxs], %[srdz], s55 offen offset:0 sc1\n" \
"v_mfma_f32_16x16x32_bf16 v[92:95], a[228:231], a[172:175], v[92:95]\n" \
"v_mfma_f32_16x16x32_bf16 v[116:119], a[200:203], a[176:179], v[116:119]\n" \
"v_mfma_f32_16x16x32_bf16 v[116:119], a[204:207], a[180:183], v[116:119]\n" \
"v_mfma_f32_16x16x32_bf16 v[116:119], a[220:223], a[184:187], v[116:119]\n" \
"v_mfma_f32_16x16x32_bf16 v[116:119], a[212:215], a[188:191], v[116:119]\n" \
"v_cvt_pk_bf16_f32 v84, v84, v85\n" \
"v_cvt_pk_bf16_f32 v85, v86, v87\n" \
"v_mfma_f32_16x16x32_bf16 v[124:127], a[208:211], a[176:179], v[124:127]\n" \
"v_mfma_f32_16x16x32_bf16 v[124:127], a[216:219], a[180:183], v[124:127]\n" \
"v_cvt_pk_bf16_f32 v86, v92, v93\n" \
"v_cvt_pk_bf16_f32 v87, v94, v95\n" \
"v_mfma_f32_16x16x32_bf16 v[124:127], a[224:227], a[184:187], v[124:127]\n" \
"s_add_u32 s55, s46, 620544\n" \
"buffer_store_dwordx4 v[84:87], %[vxs], %[srdz], s55 offen offset:0 sc1\n" \
"v_mfma_f32_16x16x32_bf16 v[124:127], a[228:231], a[188:191], v[124:127]\n" \
"v_cvt_pk_bf16_f32 v116, v116, v117\n" \
"v_cvt_pk_bf16_f32 v117, v118, v119\n" \
"s_waitcnt lgkmcnt(0)\n" \
"s_nop 4\n" \
"v_cvt_pk_bf16_f32 v118, v124, v125\n" \
"v_cvt_pk_bf16_f32 v119, v126, v127\n" \
"s_add_u32 s55, s46, 924672\n" \
"buffer_store_dwordx4 v[116:119], %[vxs], %[srdz], s55 offen offset:0 sc1\n" \
"buffer_load_dword v229, v226, %[srdz], s53 offen sc1\n" \
"v_readlane_b32 s62, %[tlo], 35\n" \
"v_lshrrev_b32_e32 v72, 2, v224\n" \
"s_add_u32 s54, %[sin], 8192\n" \
"s_add_u32 s55, %[sin], 12288\n" \
"s_movk_i32 s56, 0x1000\n" \
"s_movk_i32 s57, 0x2000\n" \
"s_movk_i32 s58, 0x3000\n" \
"v_mov_b32_e32 v226, v211\n" \
"v_bfe_u32 v221, v222, 3, 1\n" \
"v_and_b32_e32 v208, 1, v222\n" \
"v_lshl_or_b32 v221, v221, 1, v208\n" \
"s_movk_i32 s63, 272\n" \
"v_mad_u32_u24 v221, v221, s63, %[vx0]\n" \
"v_readlane_b32 s64, %[tlo], 25\n" \
"v_readlane_b32 s65, %[thi], 25\n" \
"s_mov_b32 s66, 0x400000\n" \
"s_mov_b32 s67, 0x20000\n" \
"v_readlane_b32 s48, %[tlo], 27\n" \
"v_readlane_b32 s49, %[thi], 27\n" \
"v_readlane_b32 s69, %[tlo], 21\n" \
"v_accvgpr_write_b32 a128, -1\n" \
"v_lshrrev_b32_e32 v214, 4, v224\n" \
"s_add_u32 s62, %[m0b], 65536\n" \
"v_add_u32_e32 v215, %[m0t], v214\n" \
"v_add_u32_e32 v214, s62, v214\n" \
"ds_read_b64_tr_b16 a[64:65], v214 offset:0\n" \
"v_and_b32_e32 v4, 1, v222\n" \
"ds_read_b64_tr_b16 a[66:67], v214 offset:1024\n" \
"v_bfe_u32 v5, v222, 1, 3\n" \
"ds_read_b64_tr_b16 a[96:97], v215 offset:0\n" \
"v_lshlrev_b32_e32 v4, 4, v4\n" \
"ds_read_b64_tr_b16 a[98:99], v215 offset:1024\n" \
"v_mov_b32_e32 v6, 0x3f80\n" \
"ds_read_b64_tr_b16 a[68:69], v214 offset:512\n" \
"v_lshlrev_b32_e64 v5, v5, 1\n" \
".if . - adaln_body != 61440\n" \
".error \"size model\"\n" \
".endif\n" \
"Lk14_%=:\n" \
"s_cbranch_execz Lk15_%=\n" \
"s_nop 0\n" \
"ds_read_b64_tr_b16 a[70:71], v214 offset:1536\n" \
"v_lshlrev_b32_e32 v4, v4, v6\n" \
"ds_read_b64_tr_b16 a[100:101], v215 offset:512\n" \
"v_bfe_i32 v230, v5, 0, 1\n" \
"ds_read_b64_tr_b16 a[102:103], v215 offset:1536\n" \
"v_bfe_i32 v231, v5, 1, 1\n" \
"ds_read_b64_tr_b16 a[72:73], v214 offset:16384\n" \
"v_bfe_i32 v232, v5, 2, 1\n" \
"ds_read_b64_tr_b16 a[74:75], v214 offset:17408\n" \
"v_bfe_i32 v233, v5, 3, 1\n" \
"ds_read_b64_tr_b16 a[104:105], v215 offset:4096\n" \
"v_bfe_i32 v234, v5, 4, 1\n" \
"ds_read_b64_tr_b16 a[106:107], v215 offset:5120\n" \
"v_bfe_i32 v235, v5, 5, 1\n" \
"ds_read_b64_tr_b16 a[76:77], v214 offset:16896\n" \
"v_bfe_i32 v236, v5, 6, 1\n" \
"ds_read_b64_tr_b16 a[78:79], v214 offset:17920\n" \
"v_bfe_i32 v237, v5, 7, 1\n" \
"ds_read_b64_tr_b16 a[108:109], v215 offset:4608\n" \
"v_and_b32_e32 v230, v4, v230\n" \
"ds_read_b64_tr_b16 a[110:111], v215 offset:5632\n" \
"v_and_b32_e32 v231, v4, v231\n" \
"ds_read_b64_tr_b16 a[80:81], v214 offset:2048\n" \
"v_and_b32_e32 v232, v4, v232\n" \
"ds_read_b64_tr_b16 a[82:83], v214 offset:3072\n" \
"v_and_b32_e32 v233, v4, v233\n" \
"ds_read_b64_tr_b16 a[112:113], v215 offset:2048\n" \
"v_and_b32_e32 v234, v4, v234\n" \
"ds_read_b64_tr_b16 a[114:115], v215 offset:3072\n" \
"v_and_b32_e32 v235, v4, v235\n" \
"ds_read_b64_tr_b16 a[84:85], v214 offset:2560\n" \
"v_and_b32_e32 v236, v4, v236\n" \
"ds_read_b64_tr_b16 a[86:87], v214 offset:3584\n" \
"v_and_b32_e32 v237, v4, v237\n" \
"ds_read_b64_tr_b16 a[116:117], v215 offset:2560\n" \
"ds_read_b64_tr_b16 a[118:119], v215 offset:3584\n" \
"ds_read_b64_tr_b16 a[88:89], v214 offset:18432\n" \
"ds_read_b64_tr_b16 a[90:91], v214 offset:19456\n" \
"ds_read_b64_tr_b16 a[120:121], v215 offset:6144\n" \
"ds_read_b64_tr_b16 a[122:123], v215 offset:7168\n" \
"ds_read_b64_tr_b16 a[92:93], v214 offset:18944\n" \
"ds_read_b64_tr_b16 a[94:95], v214 offset:19968\n" \
"ds_read_b64_tr_b16 a[124:125], v215 offset:6656\n" \
"ds_read_b64_tr_b16 a[126:127], v215 offset:7680\n" \
"v_lshrrev_b32_e32 v208, 1, v217\n" \
"v_and_b32_e32 v210, 24, v208\n" \
"v_bfe_u32 v215, v222, 4, 1\n" \
"v_lshl_or_b32 v210, v215, 14, v210\n" \
"v_lshl_or_b32 v210, v215, 7, v210\n" \
"v_bfe_u32 v215, v222, 2, 2\n" \
"v_lshl_or_b32 v210, v215, 9, v210\n" \
"v_lshrrev_b32_e32 v211, 5, v222\n" \
"v_xor_b32_e32 v215, v215, v211\n" \
"v_lshl_or_b32 v210, v215, 5, v210\n" \
"s_add_u32 s62, %[m0b], 98304\n" \
"v_add_u32_e32 v210, s62, v210\n" \
"v_xor_b32_e32 v211, 64, v210\n" \
"v_xor_b32_e32 v213, 0x80, v210\n" \
"v_xor_b32_e32 v214, 0xc0, v210\n" \
"v_readlane_b32 s62, %[thi], 28\n" \
"v_and_b32_e32 v100, 7, v222\n" \
"v_lshrrev_b32_e32 v103, 1, v222\n" \
"v_and_b32_e32 v101, 1, v222\n" \
"v_lshlrev_b32_e32 v103, 13, v103\n" \
"v_lshl_or_b32 v103, v101, 4, v103\n" \
"v_add_u32_e32 v100, s62, v100\n" \
"v_accvgpr_write_b32 a34, v103\n" \
"v_lshrrev_b32_e32 v101, 8, v100\n" \
"v_lshlrev_b32_e32 v101, 7, v101\n" \
"v_bfe_u32 v103, v100, 6, 2\n" \
"v_lshl_or_b32 v101, v103, 5, v101\n" \
"v_bfe_u32 v103, v100, 1, 1\n" \
"v_lshl_or_b32 v101, v103, 4, v101\n" \
"v_bfe_u32 v103, v100, 2, 4\n" \
"v_or_b32_e32 v101, v103, v101\n" \
"v_lshrrev_b32_e32 v102, 8, v101\n" \
"v_lshlrev_b32_e32 v102, 9, v102\n" \
"v_accvgpr_write_b32 a32, v101\n" \
"v_bfe_u32 v103, v101, 6, 1\n" \
"v_lshl_or_b32 v102, v103, 8, v102\n" \
"v_bfe_u32 v103, v101, 7, 1\n" \
"v_lshl_or_b32 v102, v103, 7, v102\n" \
"v_bfe_u32 v103, v101, 4, 1\n" \
"v_lshl_or_b32 v102, v103, 6, v102\n" \
"v_bfe_u32 v103, v101, 2, 2\n" \
"v_lshl_or_b32 v102, v103, 4, v102\n" \
"v_bfe_u32 v103, v101, 5, 1\n" \
"v_lshl_or_b32 v102, v103, 3, v102\n" \
"v_bfe_u32 v103, v101, 0, 2\n" \
"v_lshl_or_b32 v102, v103, 1, v102\n" \
"v_accvgpr_write_b32 a33, v102\n" \
"s_mov_b32 s51, 0x10000\n" \
"s_mov_b32 s50, 0\n" \
"s_waitcnt vmcnt(5)\n" \
"v_cmp_eq_u32_e32 vcc, 0, v216\n" \
"s_mov_b32 %[got], 0\n" \
"s_cbranch_vccnz TLf1_%=\n" \
"s_mov_b32 %[got], 1\n" \
"buffer_load_dwordx4 a[196:199], v217, %[srdz], %[sin] offen offset:0 sc1\n" \
"buffer_load_dwordx4 a[200:203], v217, %[srdz], %[sin] offen offset:1024 sc1\n" \
"buffer_load_dwordx4 a[204:207], v217, %[srdz], %[sin] offen offset:2048 sc1\n" \
"buffer_load_dwordx4 a[208:211], v217, %[srdz], %[sin] offen offset:3072 sc1\n" \
"buffer_load_dwordx4 a[212:215], v217, %[srdz], s68 offen offset:0 sc1\n" \
"buffer_load_dwordx4 a[216:219], v217, %[srdz], s68 offen offset:1024 sc1\n" \
"buffer_load_dwordx4 a[220:223], v217, %[srdz], s68 offen offset:2048 sc1\n" \
"buffer_load_dwordx4 a[224:227], v217, %[srdz], s68 offen offset:3072 sc1\n" \
"buffer_load_dwordx4 a[164:167], v217, %[srdz], s54 offen offset:0 sc1\n" \
"buffer_load_dwordx4 a[168:171], v217, %[srdz], s54 offen offset:1024 sc1\n" \
"buffer_load_dwordx4 a[172:175], v217, %[srdz], s54 offen offset:2048 sc1\n" \
"buffer_load_dwordx4 a[176:179], v217, %[srdz], s54 offen offset:3072 sc1\n" \
"TLf1_%=:\n" \
"ds_read_b64_tr_b16 a[0:1], v210 offset:0\n" \
"ds_read_b64_tr_b16 a[2:3], v213 offset:2048\n" \
"ds_read_b64_tr_b16 a[4:5], v211 offset:0\n" \
"ds_read_b64_tr_b16 a[6:7], v214 offset:2048\n" \
"ds_read_b64_tr_b16 a[8:9], v213 offset:0\n" \
"ds_read_b64_tr_b16 a[10:11], v210 offset:2048\n" \
"ds_read_b64_tr_b16 a[12:13], v214 offset:0\n" \
"ds_read_b64_tr_b16 a[14:15], v211 offset:2048\n" \
"ds_read_b64_tr_b16 a[16:17], v210 offset:256\n" \
"ds_read_b64_tr_b16 a[18:19], v213 offset:2304\n" \
"ds_read_b64_tr_b16 a[20:21], v211 offset:256\n" \
"ds_read_b64_tr_b16 a[22:23], v214 offset:2304\n" \
"ds_read_b64_tr_b16 a[24:25], v213 offset:256\n" \
"ds_read_b64_tr_b16 a[26:27], v210 offset:2304\n" \
"ds_read_b64_tr_b16 a[28:29], v214 offset:256\n" \
"ds_read_b64_tr_b16 a[30:31], v211 offset:2304\n" \
"v_mov_b64_e32 v[64:65], v[76:77]\n" \
"v_mov_b64_e32 v[66:67], v[78:79]\n" \
"v_min3_u32 v208, v64, v65, v66\n" \
"v_min3_u32 v209, v67, v68, v69\n" \
"v_min3_u32 v208, v208, v70, v71\n" \
"v_min_u32_e32 v208, v208, v209\n" \
"v_cmp_eq_u32_e32 vcc, 0, v208\n" \
"s_cbranch_vccnz TLpk_%=\n" \
"TLpkb_%=:\n" \
"v_min_u32_e32 v208, v65, v67\n" \
"v_min_u32_e32 v209, v69, v71\n" \
"v_max_u32_e32 v65, v65, v67\n" \
"v_max_u32_e32 v69, v69, v71\n" \
"v_max3_u32 v64, v208, v64, v66\n" \
"v_max3_u32 v68, v209, v68, v70\n" \
"v_min_u32_e32 v208, v65, v69\n" \
"v_max_u32_e32 v65, v65, v69\n" \
"v_max3_u32 v64, v208, v64, v68\n" \
"v_mov_b64_e32 v[68:69], v[64:65]\n" \
"v_min_u32_dpp v208, v65, v65 quad_perm:[1,0,3,2] row_mask:0xf bank_mask:0xf\n" \
"v_max_u32_dpp v209, v64, v64 quad_perm:[1,0,3,2] row_mask:0xf bank_mask:0xf\n" \
"v_max_u32_dpp v65, v65, v65 quad_perm:[1,0,3,2] row_mask:0xf bank_mask:0xf\n" \
"v_max_u32_e32 v64, v208, v209\n" \
"s_nop 0\n" \
"v_min_u32_dpp v208, v65, v65 quad_perm:[2,3,0,1] row_mask:0xf bank_mask:0xf\n" \
"v_max_u32_dpp v209, v64, v64 quad_perm:[2,3,0,1] row_mask:0xf bank_mask:0xf\n" \
"v_max_u32_dpp v65, v65, v65 quad_perm:[2,3,0,1] row_mask:0xf bank_mask:0xf\n" \
"v_max_u32_e32 v64, v208, v209\n" \
"s_nop 0\n" \
"v_min_u32_dpp v208, v65, v65 row_half_mirror row_mask:0xf bank_mask:0xf\n" \
"v_max_u32_dpp v209, v64, v64 row_half_mirror row_mask:0xf bank_mask:0xf\n" \
"v_max_u32_dpp v65, v65, v65 row_half_mirror row_mask:0xf bank_mask:0xf\n" \
"v_max_u32_e32 v64, v208, v209\n" \
"s_nop 0\n" \
"v_min_u32_dpp v208, v65, v65 row_mirror row_mask:0xf bank_mask:0xf\n" \
"v_max_u32_dpp v209, v64, v64 row_mirror row_mask:0xf bank_mask:0xf\n" \
"v_max_u32_dpp v65, v65, v65 row_mirror row_mask:0xf bank_mask:0xf\n" \
"v_max_u32_e32 v64, v208, v209\n" \
"v_mov_b32_e32 v67, v65\n" \
"v_mov_b32_e32 v66, v64\n" \
"s_nop 0\n" \
"v_permlane16_swap_b32_e32 v65, v67\n" \
"v_permlane16_swap_b32_e32 v64, v66\n" \
"v_min_u32_e32 v208, v65, v67\n" \
"v_max_u32_e32 v65, v65, v67\n" \
"v_max3_u32 v64, v208, v64, v66\n" \
"v_mov_b32_e32 v67, v65\n" \
"v_mov_b32_e32 v66, v64\n" \
"s_nop 0\n" \
"v_permlane32_swap_b32_e32 v65, v67\n" \
"v_permlane32_swap_b32_e32 v64, v66\n" \
"v_min_u32_e32 v208, v65, v67\n" \
"v_max_u32_e32 v65, v65, v67\n" \
"v_max3_u32 v64, v208, v64, v66\n" \
"v_readfirstlane_b32 s44, v65\n" \
"v_readfirstlane_b32 s45, v64\n" \
"v_cmp_ge_u32_e32 vcc, v68, v64\n" \
"s_nop 1\n" \
"v_cndmask_b32_e64 v68, v68, 0, vcc\n" \
"v_cmp_ge_u32_e32 vcc, v69, v64\n" \
"s_nop 1\n" \
"v_cndmask_b32_e32 v69, v69, v68, vcc\n" \
"v_cndmask_b32_e64 v68, v68, 0, vcc\n" \
"s_nop 0\n" \
"v_min_u32_dpp v208, v69, v69 quad_perm:[1,0,3,2] row_mask:0xf bank_mask:0xf\n" \
"v_max_u32_dpp v209, v68, v68 quad_perm:[1,0,3,2] row_mask:0xf bank_mask:0xf\n" \
"v_max_u32_dpp v69, v69, v69 quad_perm:[1,0,3,2] row_mask:0xf bank_mask:0xf\n" \
"v_max_u32_e32 v68, v208, v209\n" \
"s_nop 0\n" \
"v_min_u32_dpp v208, v69, v69 quad_perm:[2,3,0,1] row_mask:0xf bank_mask:0xf\n" \
"v_max_u32_dpp v209, v68, v68 quad_perm:[2,3,0,1] row_mask:0xf bank_mask:0xf\n" \
"v_max_u32_dpp v69, v69, v69 quad_perm:[2,3,0,1] row_mask:0xf bank_mask:0xf\n" \
"v_max_u32_e32 v68, v208, v209\n" \
"s_nop 0\n" \
"v_min_u32_dpp v208, v69, v69 row_half_mirror row_mask:0xf bank_mask:0xf\n" \
"v_max_u32_dpp v209, v68, v68 row_half_mirror row_mask:0xf bank_mask:0xf\n" \
"v_max_u32_dpp v69, v69, v69 row_half_mirror row_mask:0xf bank_mask:0xf\n" \
"v_max_u32_e32 v68, v208, v209\n" \
"s_nop 0\n" \
"v_min_u32_dpp v208, v69, v69 row_mirror row_mask:0xf bank_mask:0xf\n" \
"v_max_u32_dpp v209, v68, v68 row_mirror row_mask:0xf bank_mask:0xf\n" \
"v_max_u32_dpp v69, v69, v69 row_mirror row_mask:0xf bank_mask:0xf\n" \
"v_max_u32_e32 v68, v208, v209\n" \
"v_mov_b32_e32 v67, v69\n" \
"v_mov_b32_e32 v66, v68\n" \
"s_nop 0\n" \
"v_permlane16_swap_b32_e32 v69, v67\n" \
"v_permlane16_swap_b32_e32 v68, v66\n" \
"v_min_u32_e32 v208, v69, v67\n" \
"v_max_u32_e32 v69, v69, v67\n" \
"v_max3_u32 v68, v208, v68, v66\n" \
"v_mov_b32_e32 v67, v69\n" \
"v_mov_b32_e32 v66, v68\n" \
"s_nop 0\n" \
"v_permlane32_swap_b32_e32 v69, v67\n" \
"v_permlane32_swap_b32_e32 v68, v66\n" \
"v_min_u32_e32 v208, v69, v67\n" \
"v_max_u32_e32 v69, v69, v67\n" \
"v_max3_u32 v68, v208, v68, v66\n" \
"v_readfirstlane_b32 s46, v69\n" \
"v_readfirstlane_b32 s47, v68\n" \
"s_andn2_b32 s44, 0x7ff, s44\n" \
"s_andn2_b32 s45, 0x7ff, s45\n" \
"s_andn2_b32 s46, 0x7ff, s46\n" \
"s_andn2_b32 s47, 0x7ff, s47\n" \
"v_and_b32_e32 v213, 4, v222\n" \
"v_mov_b32_e32 v208, s44\n" \
"v_cmp_ne_u32_e32 vcc, 0, v213\n" \
"v_mov_b32_e32 v214, s45\n" \
"v_mov_b32_e32 v209, s46\n" \
"v_mov_b32_e32 v215, s47\n" \
"v_cndmask_b32_e32 v208, v208, v214, vcc\n" \
"v_cndmask_b32_e32 v209, v209, v215, vcc\n" \
"v_cmp_lt_u32_e32 vcc, 31, v222\n" \
"v_and_b32_e32 v211, 0xf0, %[kb0]\n" \
"s_lshl_b32 s62, %[wid], 9\n" \
"v_cndmask_b32_e32 v210, v208, v209, vcc\n" \
"v_lshrrev_b32_e32 %[vdn], 6, v210\n" \
"v_lshlrev_b32_e32 %[vdn], 9, %[vdn]\n" \
"v_lshlrev_b32_e32 v213, 11, v210\n" \
"v_lshrrev_b32_e32 v214, 1, v210\n" \
"v_and_b32_e32 v214, 0x80, v214\n" \
"v_lshrrev_b32_e32 v215, 1, v211\n" \
"v_and_b32_e32 v215, 56, v215\n" \
"v_add3_u32 v213, v213, v214, v215\n" \
"v_and_b32_e32 v215, 0x80, v211\n" \
"v_lshlrev_b32_e32 v215, 1, v215\n" \
"v_add3_u32 v80, v213, v215, s62\n" \
"v_xor_b32_e32 v81, 0x80, v80\n" \
"s_mov_b32 s63, 278528\n" \
"buffer_load_dword %[vxs], %[vdn], %[srdz], s63 offen offset:0 sc1\n" \
"global_load_dwordx2 v[88:89], v80, %[px] offset:0\n" \
"global_load_dwordx2 v[90:91], v80, %[px] offset:64\n" \
"global_load_dwordx2 v[92:93], v81, %[px] offset:0\n" \
"global_load_dwordx2 v[94:95], v81, %[px] offset:64\n" \
"v_and_b32_e32 v213, 0x100, v210\n" \
"s_add_u32 s63, s62, s40\n" \
"v_add3_u32 v213, v213, v211, s63\n" \
"v_xor_b32_e32 v214, 0x100, v213\n" \
"ds_read_b128 v[104:107], v213 offset:2048\n" \
"ds_read_b128 v[108:111], v214 offset:2048\n" \
"ds_read_b128 v[64:67], v213 offset:6144\n" \
"ds_read_b128 v[68:71], v213 offset:8192\n" \
"ds_read_b128 v[72:75], v214 offset:6144\n" \
"ds_read_b128 v[76:79], v214 offset:8192\n" \
"ds_read_b128 v[112:115], v213 offset:10240\n" \
"ds_read_b128 v[116:119], v213 offset:12288\n" \
"ds_read_b128 v[120:123], v214 offset:10240\n" \
"ds_read_b128 v[124:127], v214 offset:12288\n" \
"s_movk_i32 s63, 2112\n" \
"v_mad_u32_u24 %[oq0], v219, s63, v213\n" \
"v_mad_u32_u24 %[wo0], v219, s63, v214\n" \
"s_lshl_b32 s62, %[wid], 10\n" \
"s_add_u32 s62, s62, 7340032\n" \
"v_and_b32_e32 v215, 0x3b0, v217\n" \
"v_add_u32_e32 v215, s62, v215\n" \
"s_mov_b32 s63, 38016\n" \
"v_lshrrev_b32_e32 %[vct], 1, v208\n" \
"v_and_b32_e32 v208, 1, v208\n" \
"v_mad_u32_u24 %[vct], %[vct], s63, v215\n" \
"v_lshl_add_u32 %[vct], v208, 6, %[vct]\n" \
"v_lshrrev_b32_e32 %[rd0], 1, v209\n" \
"v_and_b32_e32 v209, 1, v209\n" \
"v_mad_u32_u24 %[rd0], %[rd0], s63, v215\n" \
"v_lshl_add_u32 %[rd0], v209, 6, %[rd0]\n" \
"s_waitcnt vmcnt(17)\n" \
"s_cmp_lg_u32 %[got], 0\n" \
"s_cbranch_scc1 TLrp4_%=\n" \
"s_waitcnt vmcnt(0)\n" \
"v_and_b32_e32 v208, 32, v229\n" \
"v_cmp_eq_u32_e32 vcc, 0, v208\n" \
"s_cbranch_vccz TLrl2_%=\n" \
"v_mov_b32_e32 v216, s40\n" \
"TLrq2_%=:\n" \
"buffer_load_dword v225, off, %[srdz], s53 offset:0 sc1\n" \
"s_mov_b32 s52, 10\n" \
"TLrs2_%=:\n" \
"ds_read_b32 v229, v216 offset:14336\n" \
"s_waitcnt lgkmcnt(0)\n" \
"v_and_b32_e32 v208, 32, v229\n" \
"v_cmp_eq_u32_e32 vcc, 0, v208\n" \
"s_cbranch_vccz TLrd2_%=\n" \
"s_sub_u32 s52, s52, 1\n" \
"s_cbranch_scc0 TLrs2_%=\n" \
"s_sub_u32 s51, s51, 1\n" \
"s_cselect_b32 s51, 0, s51\n" \
"s_waitcnt vmcnt(0)\n" \
"v_and_b32_e32 v208, 32, v225\n" \
"v_cmp_eq_u32_e32 vcc, 0, v208\n" \
"s_cbranch_scc1 TLrl2_%=\n" \
"s_cbranch_vccnz TLrq2_%=\n" \
"v_mov_b32_e32 v229, v225\n" \
"s_mov_b64 exec, 1\n" \
"ds_max_u32 v216, v225 offset:14336\n" \
"s_mov_b64 exec, -1\n" \
"TLrd2_%=:\n" \
"s_sleep 0\n" \
"TLrl2_%=:\n" \
"buffer_load_dwordx4 a[196:199], v217, %[srdz], %[sin] offen offset:0 sc1\n" \
"buffer_load_dwordx4 a[200:203], v217, %[srdz], %[sin] offen offset:1024 sc1\n" \
"buffer_load_dwordx4 a[204:207], v217, %[srdz], %[sin] offen offset:2048 sc1\n" \
"buffer_load_dwordx4 a[208:211], v217, %[srdz], %[sin] offen offset:3072 sc1\n" \
"buffer_load_dwordx4 a[212:215], v217, %[srdz], s68 offen offset:0 sc1\n" \
"buffer_load_dwordx4 a[216:219], v217, %[srdz], s68 offen offset:1024 sc1\n" \
"buffer_load_dwordx4 a[220:223], v217, %[srdz], s68 offen offset:2048 sc1\n" \
"buffer_load_dwordx4 a[224:227], v217, %[srdz], s68 offen offset:3072 sc1\n" \
"v_and_b32_e32 v44, 0x800, v229\n" \
"v_cmp_eq_u32_e32 vcc, 0, v44\n" \
"s_cbranch_vccnz TLrn_%=\n" \
"buffer_load_dwordx4 a[164:167], v217, %[srdz], s54 offen offset:0 sc1\n" \
"buffer_load_dwordx4 a[168:171], v217, %[srdz], s54 offen offset:1024 sc1\n" \
"buffer_load_dwordx4 a[172:175], v217, %[srdz], s54 offen offset:2048 sc1\n" \
"buffer_load_dwordx4 a[176:179], v217, %[srdz], s54 offen offset:3072 sc1\n" \
"s_branch TLrp5_%=\n" \
"TLrp4_%=:\n" \
"v_and_b32_e32 v44, 0x800, v229\n" \
"v_cmp_eq_u32_e32 vcc, 0, v44\n" \
"s_cbranch_vccnz TLrn_%=\n" \
"TLrp5_%=:\n" \
"s_mov_b32 s50, 1\n" \
"buffer_load_dwordx4 a[180:183], v217, %[srdz], s55 offen offset:0 sc1\n" \
"buffer_load_dwordx4 a[184:187], v217, %[srdz], s55 offen offset:1024 sc1\n" \
"buffer_load_dwordx4 a[188:191], v217, %[srdz], s55 offen offset:2048 sc1\n" \
"buffer_load_dwordx4 a[192:195], v217, %[srdz], s55 offen offset:3072 sc1\n" \
"TLrn_%=:\n" \
"s_waitcnt lgkmcnt(0)\n" \
"s_cmp_lg_u32 s50, 0\n" \
"s_cbranch_scc0 TLrv_%=\n" \
"s_waitcnt vmcnt(13)\n" \
"s_cmp_lg_u32 %[got], 0\n" \
"s_cbranch_scc1 TLrw_%=\n" \
"s_waitcnt vmcnt(8)\n" \
"s_branch TLrw_%=\n" \
"TLrv_%=:\n" \
"s_waitcnt vmcnt(0)\n" \
"TLrw_%=:\n" \
"v_mfma_f32_16x16x32_bf16 v[208:211], v[230:233], a[196:199], 0\n" \
"v_mfma_f32_16x16x32_bf16 v[212:215], v[230:233], a[204:207], 0\n" \
"v_mfma_f32_16x16x32_bf16 v[208:211], v[234:237], a[200:203], v[208:211]\n" \
"v_mfma_f32_16x16x32_bf16 v[212:215], v[234:237], a[208:211], v[212:215]\n" \
"v_lshlrev_b32_e32 v96, 16, v104\n" \
"v_and_b32_e32 v97, s43, v104\n" \
"v_lshlrev_b32_e32 v98, 16, v105\n" \
"v_and_b32_e32 v99, s43, v105\n" \
"v_lshlrev_b32_e32 v100, 16, v106\n" \
"v_and_b32_e32 v101, s43, v106\n" \
"v_lshlrev_b32_e32 v102, 16, v107\n" \
"v_and_b32_e32 v103, s43, v107\n" \
"v_pk_fma_f32 v[0:1], v[208:209], v[128:129], v[0:1]\n" \
"v_pk_fma_f32 v[2:3], v[210:211], v[130:131], v[2:3]\n" \
"v_pk_fma_f32 v[8:9], v[212:213], v[136:137], v[8:9]\n" \
"v_pk_fma_f32 v[10:11], v[214:215], v[138:139], v[10:11]\n" \
"v_pk_mul_f32 v[6:7], v[0:1], v[160:161]\n" \
"v_pk_mul_f32 v[12:13], v[0:1], v[0:1]\n" \
"v_pk_add_f32 v[4:5], v[0:1], v[2:3]\n" \
"v_pk_fma_f32 v[6:7], v[2:3], v[162:163], v[6:7]\n" \
"v_pk_fma_f32 v[12:13], v[2:3], v[2:3], v[12:13]\n" \
"v_pk_add_f32 v[4:5], v[4:5], v[8:9]\n" \
"v_pk_fma_f32 v[6:7], v[8:9], v[168:169], v[6:7]\n" \
"v_pk_fma_f32 v[12:13], v[8:9], v[8:9], v[12:13]\n" \
"v_pk_add_f32 v[4:5], v[4:5], v[10:11]\n" \
"v_pk_fma_f32 v[6:7], v[10:11], v[170:171], v[6:7]\n" \
"v_pk_fma_f32 v[12:13], v[10:11], v[10:11], v[12:13]\n" \
"v_mfma_f32_16x16x32_bf16 v[208:211], v[230:233], a[212:215], 0\n" \
"v_mfma_f32_16x16x32_bf16 v[212:215], v[230:233], a[220:223], 0\n" \
"v_mfma_f32_16x16x32_bf16 v[208:211], v[234:237], a[216:219], v[208:211]\n" \
"v_mfma_f32_16x16x32_bf16 v[212:215], v[234:237], a[224:227], v[212:215]\n" \
"v_lshlrev_b32_e32 v104, 16, v108\n" \
"v_and_b32_e32 v105, s43, v108\n" \
"v_lshlrev_b32_e32 v106, 16, v109\n" \
"v_and_b32_e32 v107, s43, v109\n" \
"v_lshlrev_b32_e32 v108, 16, v110\n" \
"v_and_b32_e32 v109, s43, v110\n" \
"v_lshlrev_b32_e32 v110, 16, v111\n" \
"v_and_b32_e32 v111, s43, v111\n" \
"v_pk_fma_f32 v[16:17], v[208:209], v[132:133], v[16:17]\n" \
"v_pk_fma_f32 v[18:19], v[210:211], v[134:135], v[18:19]\n" \
"v_pk_fma_f32 v[24:25], v[212:213], v[140:141], v[24:25]\n" \
"v_pk_fma_f32 v[26:27], v[214:215], v[142:143], v[26:27]\n" \
"v_pk_add_f32 v[4:5], v[4:5], v[16:17]\n" \
"v_pk_fma_f32 v[6:7], v[16:17], v[164:165], v[6:7]\n" \
"v_pk_fma_f32 v[12:13], v[16:17], v[16:17], v[12:13]\n" \
"v_pk_add_f32 v[4:5], v[4:5], v[18:19]\n" \
"v_pk_fma_f32 v[6:7], v[18:19], v[166:167], v[6:7]\n" \
"v_pk_fma_f32 v[12:13], v[18:19], v[18:19], v[12:13]\n" \
"v_pk_add_f32 v[4:5], v[4:5], v[24:25]\n" \
"v_pk_fma_f32 v[6:7], v[24:25], v[172:173], v[6:7]\n" \
"v_pk_fma_f32 v[12:13], v[24:25], v[24:25], v[12:13]\n" \
"v_pk_add_f32 v[4:5], v[4:5], v[26:27]\n" \
"v_pk_fma_f32 v[6:7], v[26:27], v[174:175], v[6:7]\n" \
"v_pk_fma_f32 v[12:13], v[26:27], v[26:27], v[12:13]\n" \
"s_cmp_lg_u32 s50, 0\n" \
"s_cbranch_scc1 TLre_%=\n" \
"v_and_b32_e32 v208, 0x800, v229\n" \
"v_cmp_eq_u32_e32 vcc, 0, v208\n" \
"s_cbranch_vccz TLrl4_%=\n" \
"v_mov_b32_e32 v216, s40\n" \
"TLrq4_%=:\n" \
"buffer_load_dword v225, off, %[srdz], s53 offset:0 sc1\n" \
"s_mov_b32 s52, 10\n" \
"TLrs4_%=:\n" \
"ds_read_b32 v229, v216 offset:14336\n" \
"s_waitcnt lgkmcnt(0)\n" \
"v_and_b32_e32 v208, 0x800, v229\n" \
"v_cmp_eq_u32_e32 vcc, 0, v208\n" \
"s_cbranch_vccz TLrd4_%=\n" \
"s_sub_u32 s52, s52, 1\n" \
"s_cbranch_scc0 TLrs4_%=\n" \
"s_sub_u32 s51, s51, 1\n" \
"s_cselect_b32 s51, 0, s51\n" \
"s_waitcnt vmcnt(0)\n" \
"v_and_b32_e32 v208, 0x800, v225\n" \
"v_cmp_eq_u32_e32 vcc, 0, v208\n" \
"s_cbranch_scc1 TLrl4_%=\n" \
"s_cbranch_vccnz TLrq4_%=\n" \
"v_mov_b32_e32 v229, v225\n" \
"s_mov_b64 exec, 1\n" \
"ds_max_u32 v216, v225 offset:14336\n" \
"s_mov_b64 exec, -1\n" \
"TLrd4_%=:\n" \
"s_sleep 4\n" \
"TLrl4_%=:\n" \
"s_mov_b32 s63, 278528\n" \
"s_nop 0\n" \
"buffer_load_dword %[vxs], %[vdn], %[srdz], s63 offen offset:0 sc1\n" \
"buffer_load_dwordx4 a[164:167], v217, %[srdz], s54 offen offset:0 sc1\n" \
"buffer_load_dwordx4 a[168:171], v217, %[srdz], s54 offen offset:1024 sc1\n" \
"buffer_load_dwordx4 a[172:175], v217, %[srdz], s54 offen offset:2048 sc1\n" \
"buffer_load_dwordx4 a[176:179], v217, %[srdz], s54 offen offset:3072 sc1\n" \
"buffer_load_dwordx4 a[180:183], v217, %[srdz], s55 offen offset:0 sc1\n" \
"buffer_load_dwordx4 a[184:187], v217, %[srdz], s55 offen offset:1024 sc1\n" \
"buffer_load_dwordx4 a[188:191], v217, %[srdz], s55 offen offset:2048 sc1\n" \
"buffer_load_dwordx4 a[192:195], v217, %[srdz], s55 offen offset:3072 sc1\n" \
"s_waitcnt vmcnt(4)\n" \
"s_branch TLrf_%=\n" \
"TLre_%=:\n" \
"s_waitcnt vmcnt(4)\n" \
"TLrf_%=:\n" \
"v_mfma_f32_16x16x32_bf16 v[208:211], v[230:233], a[164:167], 0\n" \
"v_mfma_f32_16x16x32_bf16 v[212:215], v[230:233], a[172:175], 0\n" \
"v_mfma_f32_16x16x32_bf16 v[208:211], v[234:237], a[168:171], v[208:211]\n" \
"v_mfma_f32_16x16x32_bf16 v[212:215], v[234:237], a[176:179], v[212:215]\n" \
"s_lshl_b32 s62, %[wid], 3\n" \
"v_and_b32_e32 v216, 4, v222\n" \
"s_add_u32 s62, s62, s40\n" \
"v_lshlrev_b32_e32 v229, 4, v224\n" \
"v_add_u32_e32 v216, s62, v216\n" \
"s_movk_i32 s52, 0x1000\n" \
"s_nop 0\n" \
"v_pk_fma_f32 v[32:33], v[208:209], v[144:145], v[32:33]\n" \
"v_pk_fma_f32 v[34:35], v[210:211], v[146:147], v[34:35]\n" \
"v_pk_fma_f32 v[40:41], v[212:213], v[152:153], v[40:41]\n" \
"v_pk_fma_f32 v[42:43], v[214:215], v[154:155], v[42:43]\n" \
"v_pk_mul_f32 v[36:37], v[32:33], v[176:177]\n" \
"v_pk_mul_f32 v[38:39], v[32:33], v[32:33]\n" \
"v_pk_add_f32 v[14:15], v[32:33], v[34:35]\n" \
"v_pk_fma_f32 v[36:37], v[34:35], v[178:179], v[36:37]\n" \
"v_pk_fma_f32 v[38:39], v[34:35], v[34:35], v[38:39]\n" \
"v_pk_add_f32 v[14:15], v[14:15], v[40:41]\n" \
"v_pk_fma_f32 v[36:37], v[40:41], v[184:185], v[36:37]\n" \
"v_pk_fma_f32 v[38:39], v[40:41], v[40:41], v[38:39]\n" \
"v_pk_add_f32 v[14:15], v[14:15], v[42:43]\n" \
"v_pk_fma_f32 v[36:37], v[42:43], v[186:187], v[36:37]\n" \
"v_pk_fma_f32 v[38:39], v[42:43], v[42:43], v[38:39]\n" \
"s_waitcnt vmcnt(0)\n" \
"v_and_b32_e32 v225, 7, v222\n" \
"v_lshl_add_u32 v225, v225, 2, s40\n" \
"v_and_b32_e32 v44, 0x820, %[vxs]\n" \
"v_cmp_ne_u32_e32 vcc, 0x820, v44\n" \
"s_mov_b32 s59, 0\n" \
"s_cbranch_vccnz TLcn_%=\n" \
"s_mov_b32 s59, 1\n" \
"buffer_load_dwordx4 a[128:131], %[vct], %[srdz], 0 offen offset:0 sc1\n" \
"buffer_load_dwordx4 a[132:135], %[rd0], %[srdz], 0 offen offset:0 sc1\n" \
"buffer_load_dwordx4 a[136:139], %[vct], %[srdz], s56 offen offset:0 sc1\n" \
"buffer_load_dwordx4 a[140:143], %[rd0], %[srdz], s56 offen offset:0 sc1\n" \
"buffer_load_dwordx4 a[144:147], %[vct], %[srdz], s57 offen offset:0 sc1\n" \
"buffer_load_dwordx4 a[148:151], %[rd0], %[srdz], s57 offen offset:0 sc1\n" \
"buffer_load_dwordx4 a[152:155], %[vct], %[srdz], s58 offen offset:0 sc1\n" \
"buffer_load_dwordx4 a[156:159], %[rd0], %[srdz], s58 offen offset:0 sc1\n" \
"TLcn_%=:\n" \
"v_mfma_f32_16x16x32_bf16 v[208:211], v[230:233], a[180:183], 0\n" \
"v_mfma_f32_16x16x32_bf16 v[212:215], v[230:233], a[188:191], 0\n" \
"v_mfma_f32_16x16x32_bf16 v[208:211], v[234:237], a[184:187], v[208:211]\n" \
"v_mfma_f32_16x16x32_bf16 v[212:215], v[234:237], a[192:195], v[212:215]\n" \
"v_lshlrev_b32_e32 v80, 16, v88\n" \
"v_and_b32_e32 v81, s43, v88\n" \
"v_lshlrev_b32_e32 v82, 16, v89\n" \
"v_and_b32_e32 v83, s43, v89\n" \
"v_lshlrev_b32_e32 v84, 16, v90\n" \
"v_and_b32_e32 v85, s43, v90\n" \
"v_lshlrev_b32_e32 v86, 16, v91\n" \
"v_pk_fma_f32 v[48:49], v[208:209], v[148:149], v[48:49]\n" \
"v_pk_fma_f32 v[50:51], v[210:211], v[150:151], v[50:51]\n" \
"v_pk_fma_f32 v[56:57], v[212:213], v[156:157], v[56:57]\n" \
"v_pk_fma_f32 v[58:59], v[214:215], v[158:159], v[58:59]\n" \
"v_pk_add_f32 v[14:15], v[14:15], v[48:49]\n" \
"v_pk_fma_f32 v[36:37], v[48:49], v[180:181], v[36:37]\n" \
"v_pk_fma_f32 v[38:39], v[48:49], v[48:49], v[38:39]\n" \
"v_pk_add_f32 v[14:15], v[14:15], v[50:51]\n" \
"v_pk_fma_f32 v[36:37], v[50:51], v[182:183], v[36:37]\n" \
"v_pk_fma_f32 v[38:39], v[50:51], v[50:51], v[38:39]\n" \
"v_pk_add_f32 v[14:15], v[14:15], v[56:57]\n" \
"v_pk_fma_f32 v[36:37], v[56:57], v[188:189], v[36:37]\n" \
"v_pk_fma_f32 v[38:39], v[56:57], v[56:57], v[38:39]\n" \
"v_pk_add_f32 v[14:15], v[14:15], v[58:59]\n" \
"v_pk_fma_f32 v[36:37], v[58:59], v[190:191], v[36:37]\n" \
"v_pk_fma_f32 v[38:39], v[58:59], v[58:59], v[38:39]\n" \
"v_pk_add_f32 v[4:5], v[4:5], v[14:15]\n" \
"v_pk_add_f32 v[6:7], v[6:7], v[36:37]\n" \
"v_pk_add_f32 v[12:13], v[12:13], v[38:39]\n" \
"v_add_f32_e32 v4, v4, v5\n" \
"v_add_f32_e32 v6, v6, v7\n" \
"v_add_f32_e32 v12, v12, v13\n" \
"v_and_b32_e32 v87, s43, v91\n" \
"v_mov_b32_e32 v208, v12\n" \
"v_permlane32_swap_b32_e32 v4, v6\n" \
"v_lshlrev_b32_e32 v88, 16, v92\n" \
"v_permlane32_swap_b32_e32 v12, v208\n" \
"v_add_f32_e32 v4, v4, v6\n" \
"v_add_f32_e32 v12, v12, v208\n" \
"v_and_b32_e32 v89, s43, v92\n" \
"v_lshlrev_b32_e32 v90, 16, v93\n" \
"v_permlane16_swap_b32_e32 v4, v12\n" \
"v_add_f32_e32 v4, v4, v12\n" \
"v_and_b32_e32 v91, s43, v93\n" \
"v_lshlrev_b32_e32 v92, 16, v94\n" \
"v_add_f32_dpp v4, v4, v4 quad_perm:[1,0,3,2] row_mask:0xf bank_mask:0xf\n" \
"v_and_b32_e32 v93, s43, v94\n" \
"v_lshlrev_b32_e32 v94, 16, v95\n" \
"v_add_f32_dpp v4, v4, v4 quad_perm:[2,3,0,1] row_mask:0xf bank_mask:0xf\n" \
"v_and_b32_e32 v95, s43, v95\n" \
"s_nop 0\n" \
"v_add_f32_dpp v4, v4, v4 row_ror:8 row_mask:0xf bank_mask:0xf\n" \
"v_mov_b32_e32 v12, v4\n" \
"s_nop 0\n" \
"s_nop 0\n" \
"v_permlane16_swap_b32_e32 v4, v12\n" \
"v_mov_b32_e32 v6, v4\n" \
"v_mul_f32_e32 v12, 0x3a800000, v12\n" \
"s_nop 0\n" \
"v_permlane32_swap_b32_e32 v4, v6\n" \
"v_mul_f32_e32 v218, 0x3a800000, v4\n" \
"v_mul_f32_e32 v208, v218, v218\n" \
"v_sub_f32_e32 v12, v12, v208\n" \
"v_cmp_nle_f32_e32 vcc, v208, v12\n" \
"v_add_f32_e32 v12, 0x3727c5ac, v12\n" \
"v_rsq_f32_e32 v220, v12\n" \
"v_fma_f32 v210, -v218, v223, v6\n" \
"s_mov_b64 exec, 17\n" \
"v_mul_f32_e32 v210, v210, v220\n" \
"v_ashrrev_i32_e32 v208, 31, v210\n" \
"v_or_b32_e32 v208, 0x80000000, v208\n" \
"v_xor_b32_e32 v210, v210, v208\n" \
"v_and_b32_e32 v210, 0xfffff800, v210\n" \
"v_max_u32_e32 v210, 0x800, v210\n" \
"v_or_b32_e32 v210, v210, %[ta0]\n" \
"ds_write_b32 v216, v210 offset:14400\n" \
"s_mov_b64 exec, -1\n" \
"s_cbranch_vccnz TLrare_%=\n" \
"TLrok_%=:\n" \
"s_movk_i32 s52, 0x1000\n" \
"s_waitcnt lgkmcnt(0)\n" \
"s_barrier\n" \
"ds_read_b32 v209, v225 offset:14400\n" \
"v_mov_b32_e32 v227, 1\n" \
"v_mov_b32_e32 v191, 0\n" \
"v_mov_b32_e32 v190, 0\n" \
"v_mov_b32_e32 v225, -1\n" \
"s_mov_b32 s41, 1\n" \
"s_cmp_lg_u32 %[wbytes], 0\n" \
"s_cselect_b32 s42, -8, 0\n" \
"s_waitcnt lgkmcnt(0)\n" \
"v_min_u32_dpp v208, v209, v209 quad_perm:[1,0,3,2] row_mask:0xf bank_mask:0xf\n" \
"v_max_u32_dpp v209, v209, v209 quad_perm:[1,0,3,2] row_mask:0xf bank_mask:0xf\n" \
"s_nop 1\n" \
"v_min_u32_dpp v210, v209, v209 quad_perm:[2,3,0,1] row_mask:0xf bank_mask:0xf\n" \
"v_max_u32_dpp v211, v208, v208 quad_perm:[2,3,0,1] row_mask:0xf bank_mask:0xf\n" \
"v_max_u32_dpp v209, v209, v209 quad_perm:[2,3,0,1] row_mask:0xf bank_mask:0xf\n" \
"v_max_u32_e32 v208, v210, v211\n" \
"s_nop 0\n" \
"v_min_u32_dpp v210, v209, v209 row_half_mirror row_mask:0xf bank_mask:0xf\n" \
"v_max_u32_dpp v211, v208, v208 row_half_mirror row_mask:0xf bank_mask:0xf\n" \
"v_max_u32_dpp v209, v209, v209 row_half_mirror row_mask:0xf bank_mask:0xf\n" \
"v_max_u32_e32 v208, v210, v211\n" \
"s_mov_b64 exec, 3\n" \
"buffer_atomic_or_x2 v[208:209], v229, %[srdz], s48 offen sc1\n" \
"s_mov_b64 exec, -1\n" \
"s_waitcnt vmcnt(1)\n" \
"TLcs_%=:\n" \
"v_mfma_f32_16x16x32_bf16 v[128:131], v[230:233], a[128:131], 0\n" \
"v_mfma_f32_16x16x32_bf16 v[132:135], v[230:233], a[136:139], 0\n" \
"v_mfma_f32_16x16x32_bf16 v[128:131], v[234:237], a[132:135], v[128:131]\n" \
"v_mfma_f32_16x16x32_bf16 v[132:135], v[234:237], a[140:143], v[132:135]\n" \
"v_mfma_f32_16x16x32_bf16 v[136:139], v[230:233], a[144:147], 0\n" \
"v_mfma_f32_16x16x32_bf16 v[140:143], v[230:233], a[152:155], 0\n" \
"v_mfma_f32_16x16x32_bf16 v[136:139], v[234:237], a[148:151], v[136:139]\n" \
"v_mfma_f32_16x16x32_bf16 v[140:143], v[234:237], a[156:159], v[140:143]\n" \
"s_nop 2\n" \
"v_pk_fma_f32 v[144:145], v[128:129], v[64:65], v[80:81]\n" \
"v_pk_fma_f32 v[146:147], v[130:131], v[66:67], v[82:83]\n" \
"v_pk_fma_f32 v[148:149], v[132:133], v[68:69], v[84:85]\n" \
"v_pk_fma_f32 v[150:151], v[134:135], v[70:71], v[86:87]\n" \
"v_pk_mul_f32 v[160:161], v[144:145], v[96:97]\n" \
"v_pk_mul_f32 v[162:163], v[146:147], v[98:99]\n" \
"v_pk_mul_f32 v[164:165], v[148:149], v[100:101]\n" \
"v_pk_mul_f32 v[166:167], v[150:151], v[102:103]\n" \
"v_cvt_pk_bf16_f32 v176, v160, v161\n" \
"v_cvt_pk_bf16_f32 v177, v162, v163\n" \
"v_cvt_pk_bf16_f32 v178, v164, v165\n" \
"v_cvt_pk_bf16_f32 v179, v166, v167\n" \
"ds_write_b128 %[oq0], v[176:179] offset:16384\n" \
"v_pk_fma_f32 v[152:153], v[136:137], v[72:73], v[88:89]\n" \
"v_pk_fma_f32 v[154:155], v[138:139], v[74:75], v[90:91]\n" \
"v_pk_fma_f32 v[156:157], v[140:141], v[76:77], v[92:93]\n" \
"v_pk_fma_f32 v[158:159], v[142:143], v[78:79], v[94:95]\n" \
"v_pk_mul_f32 v[168:169], v[152:153], v[104:105]\n" \
"v_pk_mul_f32 v[170:171], v[154:155], v[106:107]\n" \
"v_pk_mul_f32 v[172:173], v[156:157], v[108:109]\n" \
"v_pk_mul_f32 v[174:175], v[158:159], v[110:111]\n" \
"v_cvt_pk_bf16_f32 v180, v168, v169\n" \
"v_cvt_pk_bf16_f32 v181, v170, v171\n" \
"v_cvt_pk_bf16_f32 v182, v172, v173\n" \
"v_cvt_pk_bf16_f32 v183, v174, v175\n" \
"ds_write_b128 %[wo0], v[180:183] offset:16384\n" \
"ds_read_b128 v[4:7], v226 offset:0\n" \
"ds_read_b128 v[12:15], v226 offset:64\n" \
"ds_read_b128 v[20:23], v226 offset:128\n" \
"ds_read_b128 v[28:31], v226 offset:192\n" \
"ds_read_b128 v[36:39], v226 offset:256\n" \
"ds_read_b128 v[44:47], v226 offset:320\n" \
"ds_read_b128 v[52:55], v226 offset:384\n" \
"ds_read_b128 v[60:63], v226 offset:448\n" \
"v_cmp_u_f32_e32 vcc, v128, v132\n" \
"v_cmp_u_f32_e64 s[62:63], v136, v140\n" \
"s_or_b64 vcc, vcc, s[62:63]\n" \
"s_cbranch_scc1 TLcx_%=\n" \
"TLco_%=:\n" \
"v_pk_add_f32 v[184:185], v[144:145], v[146:147]\n" \
"v_pk_mul_f32 v[186:187], v[144:145], v[144:145]\n" \
"v_pk_mul_f32 v[188:189], v[144:145], v[112:113]\n" \
"v_pk_fma_f32 v[186:187], v[146:147], v[146:147], v[186:187]\n" \
"v_pk_fma_f32 v[188:189], v[146:147], v[114:115], v[188:189]\n" \
"v_pk_add_f32 v[184:185], v[184:185], v[148:149]\n" \
"v_pk_fma_f32 v[186:187], v[148:149], v[148:149], v[186:187]\n" \
"v_pk_fma_f32 v[188:189], v[148:149], v[116:117], v[188:189]\n" \
".if . - adaln_body != 65536\n" \
".error \"size model\"\n" \
".endif\n" \
"Lk15_%=:\n" \
"s_cbranch_execz Lk16_%=\n" \
"s_nop 0\n" \
"v_pk_add_f32 v[184:185], v[184:185], v[150:151]\n" \
"v_pk_fma_f32 v[186:187], v[150:151], v[150:151], v[186:187]\n" \
"v_pk_fma_f32 v[188:189], v[150:151], v[118:119], v[188:189]\n" \
"v_pk_add_f32 v[184:185], v[184:185], v[152:153]\n" \
"v_pk_fma_f32 v[186:187], v[152:153], v[152:153], v[186:187]\n" \
"v_pk_fma_f32 v[188:189], v[152:153], v[120:121], v[188:189]\n" \
"v_pk_add_f32 v[184:185], v[184:185], v[154:155]\n" \
"v_pk_fma_f32 v[186:187], v[154:155], v[154:155], v[186:187]\n" \
"v_pk_fma_f32 v[188:189], v[154:155], v[122:123], v[188:189]\n" \
"v_pk_add_f32 v[184:185], v[184:185], v[156:157]\n" \
"v_pk_fma_f32 v[186:187], v[156:157], v[156:157], v[186:187]\n" \
"v_pk_fma_f32 v[188:189], v[156:157], v[124:125], v[188:189]\n" \
"v_pk_add_f32 v[184:185], v[184:185], v[158:159]\n" \
"v_pk_fma_f32 v[186:187], v[158:159], v[158:159], v[186:187]\n" \
"v_pk_fma_f32 v[188:189], v[158:159], v[126:127], v[188:189]\n" \
"v_add_f32_e32 v184, v184, v185\n" \
"v_add_f32_e32 v186, v186, v187\n" \
"v_add_f32_e32 v188, v188, v189\n" \
"s_waitcnt lgkmcnt(7)\n" \
"v_mfma_f32_16x16x32_bf16 v[208:211], a[64:67], v[4:7], 0\n" \
"v_mfma_f32_16x16x32_bf16 v[212:215], a[96:99], v[4:7], 0\n" \
"v_permlane16_swap_b32_e32 v184, v186\n" \
"v_mov_b32_e32 v189, v188\n" \
"s_waitcnt lgkmcnt(6)\n" \
"v_mfma_f32_16x16x32_bf16 v[208:211], a[68:71], v[12:15], v[208:211]\n" \
"v_mfma_f32_16x16x32_bf16 v[212:215], a[100:103], v[12:15], v[212:215]\n" \
"v_add_f32_e32 v184, v184, v186\n" \
"v_permlane16_swap_b32_e32 v188, v189\n" \
"s_waitcnt lgkmcnt(5)\n" \
"v_mfma_f32_16x16x32_bf16 v[208:211], a[72:75], v[20:23], v[208:211]\n" \
"v_mfma_f32_16x16x32_bf16 v[212:215], a[104:107], v[20:23], v[212:215]\n" \
"v_add_f32_e32 v188, v188, v189\n" \
"v_add_u32_e32 v176, 1632, %[kd0]\n" \
"s_waitcnt lgkmcnt(4)\n" \
"v_mfma_f32_16x16x32_bf16 v[208:211], a[76:79], v[28:31], v[208:211]\n" \
"v_mfma_f32_16x16x32_bf16 v[212:215], a[108:111], v[28:31], v[212:215]\n" \
"v_add_f32_dpp v184, v184, v184 quad_perm:[1,0,3,2] row_mask:0xf bank_mask:0xf\n" \
"v_add_f32_dpp v188, v188, v188 quad_perm:[1,0,3,2] row_mask:0xf bank_mask:0xf\n" \
"s_waitcnt lgkmcnt(3)\n" \
"v_mfma_f32_16x16x32_bf16 v[208:211], a[80:83], v[36:39], v[208:211]\n" \
"v_mfma_f32_16x16x32_bf16 v[212:215], a[112:115], v[36:39], v[212:215]\n" \
"v_add_f32_dpp v184, v184, v184 quad_perm:[2,3,0,1] row_mask:0xf bank_mask:0xf\n" \
"v_add_f32_dpp v188, v188, v188 quad_perm:[2,3,0,1] row_mask:0xf bank_mask:0xf\n" \
"s_waitcnt lgkmcnt(2)\n" \
"v_mfma_f32_16x16x32_bf16 v[208:211], a[84:87], v[44:47], v[208:211]\n" \
"v_mfma_f32_16x16x32_bf16 v[212:215], a[116:119], v[44:47], v[212:215]\n" \
"v_add_f32_dpp v184, v184, v184 row_ror:8 row_mask:0xf bank_mask:0xf\n" \
"v_add_f32_dpp v188, v188, v188 row_ror:8 row_mask:0xf bank_mask:0xf\n" \
"s_waitcnt lgkmcnt(1)\n" \
"v_mfma_f32_16x16x32_bf16 v[208:211], a[88:91], v[52:55], v[208:211]\n" \
"v_mfma_f32_16x16x32_bf16 v[212:215], a[120:123], v[52:55], v[212:215]\n" \
"s_mov_b32 vcc_lo, 0xaaaaaaaa\n" \
"s_mov_b32 vcc_hi, 0xaaaaaaaa\n" \
"v_cndmask_b32_e32 v184, v184, v188, vcc\n" \
"s_waitcnt lgkmcnt(0)\n" \
"v_mfma_f32_16x16x32_bf16 v[208:211], a[92:95], v[60:63], v[208:211]\n" \
"v_mfma_f32_16x16x32_bf16 v[212:215], a[124:127], v[60:63], v[212:215]\n" \
"s_mov_b32 exec_lo, 0x330033\n" \
"s_mov_b32 exec_hi, 0x330033\n" \
"ds_write_b32 v228, v184 offset:0\n" \
"s_mov_b32 exec_lo, 0x3f003f\n" \
"s_mov_b32 exec_hi, 0x3f003f\n" \
"s_nop 1\n" \
"ds_write2_b32 %[kd0], v208, v209 offset0:0 offset1:4\n" \
"ds_write2_b32 %[kd0], v210, v211 offset0:8 offset1:12\n" \
"ds_write2_b32 v176, v212, v213 offset0:0 offset1:4\n" \
"ds_write2_b32 v176, v214, v215 offset0:8 offset1:12\n" \
"s_mov_b64 exec, -1\n" \
"s_waitcnt lgkmcnt(0)\n" \
"s_barrier\n" \
"ds_read_b128 v[4:7], %[vg0] offset:0\n" \
"ds_read_b128 v[12:15], %[vg0] offset:16\n" \
"ds_read_b128 v[20:23], %[vg0] offset:32\n" \
"ds_read_b128 v[28:31], v221 offset:0\n" \
"ds_read_b128 v[36:39], %[vx0] offset:1088\n" \
"ds_read_b128 v[44:47], %[vx0] offset:1360\n" \
"ds_read_b128 v[52:55], v221 offset:1632\n" \
"ds_read_b128 v[60:63], %[vx0] offset:2720\n" \
"ds_read_b128 v[128:131], %[vx0] offset:2992\n" \
"v_bfe_u32 v208, v222, 3, 1\n" \
"v_and_b32_e32 v209, 1, v222\n" \
"v_lshl_or_b32 v208, v208, 1, v209\n" \
"s_waitcnt lgkmcnt(6)\n" \
"v_pk_add_f32 v[4:5], v[4:5], v[6:7]\n" \
"v_pk_add_f32 v[12:13], v[12:13], v[14:15]\n" \
"v_pk_add_f32 v[20:21], v[20:21], v[22:23]\n" \
"v_add_f32_e32 v4, v4, v5\n" \
"v_add_f32_e32 v12, v12, v13\n" \
"v_add_f32_e32 v20, v20, v21\n" \
"v_mul_f32_e32 v4, 0x3a800000, v4\n" \
"v_mul_f32_e32 v12, 0x3a800000, v12\n" \
"v_mul_f32_e32 v209, v4, v4\n" \
"v_sub_f32_e32 v12, v12, v209\n" \
"s_waitcnt lgkmcnt(3)\n" \
"v_cmp_nle_f32_e64 s[50:51], v209, v12\n" \
"v_add_f32_e32 v12, 0x3727c5ac, v12\n" \
"v_pk_add_f32 v[28:29], v[28:29], v[30:31]\n" \
"v_rsq_f32_e32 v132, v12\n" \
"v_pk_add_f32 v[36:37], v[36:37], v[38:39]\n" \
"v_fma_f32 v20, -v4, v223, v20\n" \
"v_pk_add_f32 v[44:45], v[44:45], v[46:47]\n" \
"v_add_f32_e32 v28, v28, v29\n" \
"v_add_f32_e32 v36, v36, v37\n" \
"v_add_f32_e32 v44, v44, v45\n" \
"v_mul_f32_e32 v20, v20, v132\n" \
"s_waitcnt lgkmcnt(0)\n" \
"v_pk_add_f32 v[52:53], v[52:53], v[54:55]\n" \
"v_ashrrev_i32_e32 v209, 31, v20\n" \
"v_pk_add_f32 v[60:61], v[60:61], v[62:63]\n" \
"v_or_b32_e32 v209, 0x80000000, v209\n" \
"v_pk_add_f32 v[128:129], v[128:129], v[130:131]\n" \
"v_xor_b32_e32 v210, v20, v209\n" \
"v_add_f32_e32 v52, v52, v53\n" \
"v_and_b32_e32 v210, 0xfffff800, v210\n" \
"v_add_f32_e32 v60, v60, v61\n" \
"v_max_u32_e32 v210, 0x800, v210\n" \
"v_add_f32_e32 v128, v128, v129\n" \
"v_and_or_b32 v210, v210, v225, v208\n" \
"v_fma_f32 v28, -v4, v36, v28\n" \
"v_fma_f32 v52, -v4, v60, v52\n" \
"v_fma_f32 v28, v28, v132, v44\n" \
"v_min_u32_dpp v211, v210, v210 quad_perm:[1,0,3,2] row_mask:0xf bank_mask:0xf\n" \
"v_fma_f32 v52, v52, v132, v128\n" \
"v_max_u32_dpp v210, v210, v210 quad_perm:[1,0,3,2] row_mask:0xf bank_mask:0xf\n" \
"v_cvt_pk_bf16_f32 v28, 0, v28\n" \
"v_cvt_pk_bf16_f32 v52, 0, v52\n" \
"v_min_u32_dpp v212, v210, v210 row_ror:8 row_mask:0xf bank_mask:0xf\n" \
"v_mul_f32_e32 v214, 0xbfb8aa3b, v28\n" \
"v_max_u32_dpp v213, v211, v211 row_ror:8 row_mask:0xf bank_mask:0xf\n" \
"v_exp_f32_e32 v214, v214\n" \
"v_max_u32_dpp v210, v210, v210 row_ror:8 row_mask:0xf bank_mask:0xf\n" \
"v_add_f32_e32 v214, 1.0, v214\n" \
"v_max_u32_e32 v211, v212, v213\n" \
"v_rcp_f32_e32 v214, v214\n" \
"s_nop 0\n" \
"v_mul_f32_e32 v28, v28, v214\n" \
"v_cvt_pk_bf16_f32 v28, 0, v28\n" \
"v_mul_f32_e32 v28, v28, v52\n" \
"v_cvt_pk_bf16_f32 v28, 0, v28\n" \
"v_cndmask_b32_e32 v133, v210, v211, vcc\n" \
"v_readfirstlane_b32 s60, v210\n" \
"v_readfirstlane_b32 s61, v211\n" \
"v_xor_b32_e32 v212, v133, v208\n" \
"v_and_b32_e32 v213, 1, v212\n" \
"v_and_b32_e32 v212, 2, v212\n" \
"v_cmp_ne_u32_e64 s[54:55], 0, v213\n" \
"v_cmp_ne_u32_e64 s[56:57], 0, v212\n" \
"s_mov_b32 s62, 0x07060302\n" \
"v_mov_b32_dpp v213, v28 quad_perm:[1,0,3,2] row_mask:0xf bank_mask:0xf\n" \
"v_cndmask_b32_e64 v28, v28, v213, s[54:55]\n" \
"s_nop 1\n" \
"v_mov_b32_dpp v214, v28 row_ror:8 row_mask:0xf bank_mask:0xf\n" \
"v_cndmask_b32_e64 v28, v28, v214, s[56:57]\n" \
"s_mov_b32 vcc_lo, 0xcccccccc\n" \
"s_mov_b32 vcc_hi, 0xcccccccc\n" \
"v_mov_b32_dpp v213, v28 quad_perm:[2,3,0,1] row_mask:0xf bank_mask:0xf\n" \
"s_mov_b32 s58, 0x30003\n" \
"s_mov_b32 s59, 0xc000c\n" \
"v_perm_b32 v214, v213, v28, s62\n" \
"v_perm_b32 v215, v28, v213, s62\n" \
"v_cndmask_b32_e32 v214, v214, v215, vcc\n" \
"s_mov_b32 vcc_lo, 0xf0f0f0f0\n" \
"s_mov_b32 vcc_hi, 0xf0f0f0f0\n" \
"v_mov_b32_dpp v213, v214 row_ror:4 row_mask:0xf bank_mask:0xf\n" \
"v_cndmask_b32_e32 v164, v214, v213, vcc\n" \
"v_cndmask_b32_e32 v165, v213, v214, vcc\n" \
"v_mov_b32_e32 v166, v164\n" \
"v_mov_b32_e32 v167, v165\n" \
"s_nop 0\n" \
"v_permlane32_swap_b32_e32 v164, v166\n" \
"v_permlane32_swap_b32_e32 v165, v167\n" \
"s_nop 0\n" \
"v_permlane16_swap_b32_e32 v164, v166\n" \
"v_permlane16_swap_b32_e32 v165, v167\n" \
"v_cndmask_b32_e64 v176, 0, v164, s[58:59]\n" \
"v_cndmask_b32_e64 v177, 0, v165, s[58:59]\n" \
"v_cndmask_b32_e64 v178, 0, v166, s[58:59]\n" \
"v_cndmask_b32_e64 v179, 0, v167, s[58:59]\n" \
"s_lshl_b64 s[58:59], s[58:59], 4\n" \
"v_cndmask_b32_e64 v180, 0, v164, s[58:59]\n" \
"v_cndmask_b32_e64 v181, 0, v165, s[58:59]\n" \
"v_cndmask_b32_e64 v182, 0, v166, s[58:59]\n" \
"v_cndmask_b32_e64 v183, 0, v167, s[58:59]\n" \
"s_lshl_b64 s[58:59], s[58:59], 4\n" \
"v_mfma_f32_16x16x32_bf16 v[168:171], v[176:179], a[0:3], 0\n" \
"v_mfma_f32_16x16x32_bf16 v[172:175], v[176:179], a[16:19], 0\n" \
"v_cndmask_b32_e64 v184, 0, v164, s[58:59]\n" \
"v_cndmask_b32_e64 v185, 0, v165, s[58:59]\n" \
"v_cndmask_b32_e64 v186, 0, v166, s[58:59]\n" \
"v_cndmask_b32_e64 v187, 0, v167, s[58:59]\n" \
"s_lshl_b64 s[58:59], s[58:59], 4\n" \
"v_mfma_f32_16x16x32_bf16 v[168:171], v[180:183], a[4:7], v[168:171]\n" \
"v_mfma_f32_16x16x32_bf16 v[172:175], v[180:183], a[20:23], v[172:175]\n" \
"v_cndmask_b32_e64 v136, 0, v164, s[58:59]\n" \
"v_cndmask_b32_e64 v137, 0, v165, s[58:59]\n" \
"v_cndmask_b32_e64 v138, 0, v166, s[58:59]\n" \
"v_cndmask_b32_e64 v139, 0, v167, s[58:59]\n" \
"v_mfma_f32_16x16x32_bf16 v[168:171], v[184:187], a[8:11], v[168:171]\n" \
"v_mfma_f32_16x16x32_bf16 v[172:175], v[184:187], a[24:27], v[172:175]\n" \
"v_mfma_f32_16x16x32_bf16 v[168:171], v[136:139], a[12:15], v[168:171]\n" \
"v_mfma_f32_16x16x32_bf16 v[172:175], v[136:139], a[28:31], v[172:175]\n" \
"ds_read_b128 v[64:67], %[kb0] offset:0\n" \
"ds_read_b128 v[68:71], %[kb0] offset:1024\n" \
"ds_read_b128 v[72:75], %[vvb] offset:0\n" \
"ds_read_b128 v[76:79], %[vvb] offset:1024\n" \
"ds_read_b128 v[80:83], %[kb0] offset:4096\n" \
"ds_read_b128 v[84:87], %[kb0] offset:5120\n" \
"ds_read_b128 v[88:91], %[vvb] offset:4096\n" \
"ds_read_b128 v[92:95], %[vvb] offset:5120\n" \
"v_and_or_b32 v168, v168, s42, v227\n" \
"v_and_or_b32 v169, v169, s42, v227\n" \
"v_and_or_b32 v170, v170, s42, v227\n" \
"v_and_or_b32 v171, v171, s42, v227\n" \
"buffer_store_dwordx4 v[168:171], v217, %[srdz], s69 offen offset:0 sc1\n" \
"v_and_or_b32 v172, v172, s42, v227\n" \
"v_and_or_b32 v173, v173, s42, v227\n" \
"v_and_or_b32 v174, v174, s42, v227\n" \
"v_and_or_b32 v175, v175, s42, v227\n" \
"buffer_store_dwordx4 v[172:175], v217, %[srdz], s69 offen offset:1024 sc1\n" \
"s_movk_i32 s62, -128\n" \
"s_sub_u32 s63, 65536, s40\n" \
"v_mul_i32_i24_e64 v208, v219, s62\n" \
"v_add_u32_e32 v208, s63, v208\n" \
"v_lshl_add_u32 v209, %[oq0], 1, v208\n" \
"v_lshl_add_u32 v210, %[wo0], 1, v208\n" \
"ds_write_b128 v209, v[144:147] offset:0\n" \
"ds_write_b128 v209, v[148:151] offset:16\n" \
"ds_write_b128 v210, v[152:155] offset:0\n" \
"ds_write_b128 v210, v[156:159] offset:16\n" \
"v_mov_b32_dpp v211, v4 quad_perm:[1,0,3,2] row_mask:0xf bank_mask:0xf\n" \
"v_mov_b32_dpp v213, v132 quad_perm:[1,0,3,2] row_mask:0xf bank_mask:0xf\n" \
"v_mov_b32_dpp v215, v20 quad_perm:[1,0,3,2] row_mask:0xf bank_mask:0xf\n" \
"v_cndmask_b32_e64 v4, v4, v211, s[54:55]\n" \
"v_cndmask_b32_e64 v132, v132, v213, s[54:55]\n" \
"v_cndmask_b32_e64 v20, v20, v215, s[54:55]\n" \
"v_mov_b32_dpp v212, v4 row_ror:8 row_mask:0xf bank_mask:0xf\n" \
"v_mov_b32_dpp v214, v132 row_ror:8 row_mask:0xf bank_mask:0xf\n" \
"v_mov_b32_dpp v208, v20 row_ror:8 row_mask:0xf bank_mask:0xf\n" \
"v_cndmask_b32_e64 v4, v4, v212, s[56:57]\n" \
"v_cndmask_b32_e64 v132, v132, v214, s[56:57]\n" \
"v_cndmask_b32_e64 v20, v20, v208, s[56:57]\n" \
"v_mov_b32_e32 v126, v132\n" \
"v_and_b32_e32 v125, 3, v133\n" \
"s_bitcmp1_b32 s60, 0\n" \
"s_cselect_b32 s56, s45, s44\n" \
"s_cselect_b32 s57, s47, s46\n" \
"s_bitcmp1_b32 s60, 1\n" \
"s_cselect_b32 s60, s57, s56\n" \
"s_bitcmp1_b32 s61, 0\n" \
"s_cselect_b32 s56, s45, s44\n" \
"s_cselect_b32 s57, s47, s46\n" \
"s_bitcmp1_b32 s61, 1\n" \
"s_cselect_b32 s61, s57, s56\n" \
"s_cmp_lg_u64 s[50:51], 0\n" \
"s_cselect_b32 s53, 1, 0\n" \
"s_mov_b32 s51, 0x10000\n" \
"s_cmp_lg_u32 s41, 1\n" \
"s_cbranch_scc1 TLls_%=\n" \
"v_mul_f32_e64 v208, -v218, v220\n" \
"v_pk_fma_f32 v[128:129], v[0:1], v[220:221], v[208:209] op_sel_hi:[1,0,0]\n" \
"v_pk_fma_f32 v[130:131], v[2:3], v[220:221], v[208:209] op_sel_hi:[1,0,0]\n" \
"v_pk_fma_f32 v[132:133], v[8:9], v[220:221], v[208:209] op_sel_hi:[1,0,0]\n" \
"v_pk_fma_f32 v[134:135], v[10:11], v[220:221], v[208:209] op_sel_hi:[1,0,0]\n" \
"v_pk_fma_f32 v[136:137], v[16:17], v[220:221], v[208:209] op_sel_hi:[1,0,0]\n" \
"v_pk_fma_f32 v[138:139], v[18:19], v[220:221], v[208:209] op_sel_hi:[1,0,0]\n" \
"v_pk_fma_f32 v[140:141], v[24:25], v[220:221], v[208:209] op_sel_hi:[1,0,0]\n" \
"v_pk_fma_f32 v[142:143], v[26:27], v[220:221], v[208:209] op_sel_hi:[1,0,0]\n" \
"v_pk_fma_f32 v[144:145], v[32:33], v[220:221], v[208:209] op_sel_hi:[1,0,0]\n" \
"v_pk_fma_f32 v[146:147], v[34:35], v[220:221], v[208:209] op_sel_hi:[1,0,0]\n" \
"v_pk_fma_f32 v[148:149], v[40:41], v[220:221], v[208:209] op_sel_hi:[1,0,0]\n" \
"v_pk_fma_f32 v[150:151], v[42:43], v[220:221], v[208:209] op_sel_hi:[1,0,0]\n" \
"v_pk_fma_f32 v[152:153], v[48:49], v[220:221], v[208:209] op_sel_hi:[1,0,0]\n" \
"v_pk_fma_f32 v[154:155], v[50:51], v[220:221], v[208:209] op_sel_hi:[1,0,0]\n" \
"v_pk_fma_f32 v[156:157], v[56:57], v[220:221], v[208:209] op_sel_hi:[1,0,0]\n" \
"v_pk_fma_f32 v[158:159], v[58:59], v[220:221], v[208:209] op_sel_hi:[1,0,0]\n" \
"v_sub_u32_e32 v216, 0x7ff, %[ta0]\n" \
"v_cmp_eq_u32_e64 s[56:57], s60, v216\n" \
"v_cmp_eq_u32_e64 s[58:59], s61, v216\n" \
"s_cmp_eq_u32 %[wid], 3\n" \
"s_cselect_b32 s49, s49, 0x80000000\n" \
"v_lshrrev_b32_e32 v216, 2, v224\n" \
"s_waitcnt lgkmcnt(0)\n" \
"v_lshlrev_b32_e32 v160, 16, v192\n" \
"v_and_b32_e32 v161, s43, v192\n" \
"v_lshlrev_b32_e32 v176, 16, v64\n" \
"v_and_b32_e32 v177, s43, v64\n" \
"v_lshlrev_b32_e32 v162, 16, v193\n" \
"v_and_b32_e32 v163, s43, v193\n" \
"v_lshlrev_b32_e32 v178, 16, v65\n" \
"v_and_b32_e32 v179, s43, v65\n" \
"v_lshlrev_b32_e32 v164, 16, v196\n" \
"v_and_b32_e32 v165, s43, v196\n" \
"v_lshlrev_b32_e32 v180, 16, v68\n" \
"v_and_b32_e32 v181, s43, v68\n" \
"v_lshlrev_b32_e32 v166, 16, v197\n" \
"v_and_b32_e32 v167, s43, v197\n" \
"v_lshlrev_b32_e32 v182, 16, v69\n" \
"v_and_b32_e32 v183, s43, v69\n" \
"v_pk_fma_f32 v[128:129], v[128:129], v[160:161], v[176:177]\n" \
"v_pk_fma_f32 v[130:131], v[130:131], v[162:163], v[178:179]\n" \
"v_pk_fma_f32 v[132:133], v[132:133], v[164:165], v[180:181]\n" \
"v_pk_fma_f32 v[134:135], v[134:135], v[166:167], v[182:183]\n" \
"v_lshlrev_b32_e32 v168, 16, v80\n" \
"v_and_b32_e32 v169, s43, v80\n" \
"v_lshlrev_b32_e32 v170, 16, v81\n" \
"v_and_b32_e32 v171, s43, v81\n" \
"v_lshlrev_b32_e32 v172, 16, v84\n" \
"v_and_b32_e32 v173, s43, v84\n" \
"v_lshlrev_b32_e32 v174, 16, v85\n" \
"v_and_b32_e32 v175, s43, v85\n" \
"v_pk_fma_f32 v[128:129], v[128:129], v[168:169], v[0:1]\n" \
"v_pk_fma_f32 v[130:131], v[130:131], v[170:171], v[2:3]\n" \
"v_pk_fma_f32 v[132:133], v[132:133], v[172:173], v[8:9]\n" \
"v_pk_fma_f32 v[134:135], v[134:135], v[174:175], v[10:11]\n" \
"v_cvt_pk_bf16_f32 v128, v128, v129\n" \
"v_cvt_pk_bf16_f32 v130, v130, v131\n" \
"v_cvt_pk_bf16_f32 v132, v132, v133\n" \
"v_cvt_pk_bf16_f32 v134, v134, v135\n" \
"v_lshlrev_b32_e32 v160, 16, v194\n" \
"v_and_b32_e32 v161, s43, v194\n" \
"v_lshlrev_b32_e32 v176, 16, v66\n" \
"v_and_b32_e32 v177, s43, v66\n" \
"v_lshlrev_b32_e32 v162, 16, v195\n" \
"v_and_b32_e32 v163, s43, v195\n" \
"v_lshlrev_b32_e32 v178, 16, v67\n" \
"v_and_b32_e32 v179, s43, v67\n" \
"v_lshlrev_b32_e32 v164, 16, v198\n" \
"v_and_b32_e32 v165, s43, v198\n" \
"v_lshlrev_b32_e32 v180, 16, v70\n" \
"v_and_b32_e32 v181, s43, v70\n" \
"v_lshlrev_b32_e32 v166, 16, v199\n" \
"v_and_b32_e32 v167, s43, v199\n" \
"v_lshlrev_b32_e32 v182, 16, v71\n" \
"v_and_b32_e32 v183, s43, v71\n" \
"v_pk_fma_f32 v[136:137], v[136:137], v[160:161], v[176:177]\n" \
"v_pk_fma_f32 v[138:139], v[138:139], v[162:163], v[178:179]\n" \
"v_pk_fma_f32 v[140:141], v[140:141], v[164:165], v[180:181]\n" \
"v_pk_fma_f32 v[142:143], v[142:143], v[166:167], v[182:183]\n" \
"v_lshlrev_b32_e32 v168, 16, v82\n" \
"v_and_b32_e32 v169, s43, v82\n" \
"v_lshlrev_b32_e32 v170, 16, v83\n" \
"v_and_b32_e32 v171, s43, v83\n" \
"v_lshlrev_b32_e32 v172, 16, v86\n" \
"v_and_b32_e32 v173, s43, v86\n" \
"v_lshlrev_b32_e32 v174, 16, v87\n" \
"v_and_b32_e32 v175, s43, v87\n" \
"v_pk_fma_f32 v[136:137], v[136:137], v[168:169], v[16:17]\n" \
"v_pk_fma_f32 v[138:139], v[138:139], v[170:171], v[18:19]\n" \
"v_pk_fma_f32 v[140:141], v[140:141], v[172:173], v[24:25]\n" \
"v_pk_fma_f32 v[142:143], v[142:143], v[174:175], v[26:27]\n" \
"v_cvt_pk_bf16_f32 v136, v136, v137\n" \
"v_cvt_pk_bf16_f32 v138, v138, v139\n" \
"v_cvt_pk_bf16_f32 v140, v140, v141\n" \
"v_cvt_pk_bf16_f32 v142, v142, v143\n" \
"buffer_load_dwordx4 v[112:115], v216, %[srdz], s49 offen offset:0 sc1\n" \
"buffer_load_dwordx4 v[116:119], v216, %[srdz], s49 offen offset:16 sc1\n" \
"v_lshlrev_b32_e32 v160, 16, v200\n" \
"v_and_b32_e32 v161, s43, v200\n" \
"v_lshlrev_b32_e32 v176, 16, v72\n" \
"v_and_b32_e32 v177, s43, v72\n" \
"v_lshlrev_b32_e32 v162, 16, v201\n" \
"v_and_b32_e32 v163, s43, v201\n" \
"v_lshlrev_b32_e32 v178, 16, v73\n" \
"v_and_b32_e32 v179, s43, v73\n" \
"v_lshlrev_b32_e32 v164, 16, v204\n" \
"v_and_b32_e32 v165, s43, v204\n" \
"v_lshlrev_b32_e32 v180, 16, v76\n" \
"v_and_b32_e32 v181, s43, v76\n" \
"v_lshlrev_b32_e32 v166, 16, v205\n" \
"v_and_b32_e32 v167, s43, v205\n" \
"v_lshlrev_b32_e32 v182, 16, v77\n" \
"v_and_b32_e32 v183, s43, v77\n" \
"v_pk_fma_f32 v[144:145], v[144:145], v[160:161], v[176:177]\n" \
"v_pk_fma_f32 v[146:147], v[146:147], v[162:163], v[178:179]\n" \
"v_pk_fma_f32 v[148:149], v[148:149], v[164:165], v[180:181]\n" \
"v_pk_fma_f32 v[150:151], v[150:151], v[166:167], v[182:183]\n" \
"v_lshlrev_b32_e32 v168, 16, v88\n" \
"v_and_b32_e32 v169, s43, v88\n" \
"v_lshlrev_b32_e32 v170, 16, v89\n" \
"v_and_b32_e32 v171, s43, v89\n" \
"v_lshlrev_b32_e32 v172, 16, v92\n" \
"v_and_b32_e32 v173, s43, v92\n" \
"v_lshlrev_b32_e32 v174, 16, v93\n" \
"v_and_b32_e32 v175, s43, v93\n" \
"v_pk_fma_f32 v[144:145], v[144:145], v[168:169], v[32:33]\n" \
"v_pk_fma_f32 v[146:147], v[146:147], v[170:171], v[34:35]\n" \
"v_pk_fma_f32 v[148:149], v[148:149], v[172:173], v[40:41]\n" \
"v_pk_fma_f32 v[150:151], v[150:151], v[174:175], v[42:43]\n" \
"v_cvt_pk_bf16_f32 v144, v144, v145\n" \
"v_cvt_pk_bf16_f32 v146, v146, v147\n" \
"v_cvt_pk_bf16_f32 v148, v148, v149\n" \
"v_cvt_pk_bf16_f32 v150, v150, v151\n" \
"v_lshlrev_b32_e32 v160, 16, v202\n" \
"v_and_b32_e32 v161, s43, v202\n" \
"v_lshlrev_b32_e32 v176, 16, v74\n" \
"v_and_b32_e32 v177, s43, v74\n" \
"v_lshlrev_b32_e32 v162, 16, v203\n" \
"v_and_b32_e32 v163, s43, v203\n" \
"v_lshlrev_b32_e32 v178, 16, v75\n" \
"v_and_b32_e32 v179, s43, v75\n" \
"v_lshlrev_b32_e32 v164, 16, v206\n" \
"v_and_b32_e32 v165, s43, v206\n" \
"v_lshlrev_b32_e32 v180, 16, v78\n" \
"v_and_b32_e32 v181, s43, v78\n" \
"v_lshlrev_b32_e32 v166, 16, v207\n" \
"v_and_b32_e32 v167, s43, v207\n" \
"v_lshlrev_b32_e32 v182, 16, v79\n" \
"v_and_b32_e32 v183, s43, v79\n" \
"v_pk_fma_f32 v[152:153], v[152:153], v[160:161], v[176:177]\n" \
"v_pk_fma_f32 v[154:155], v[154:155], v[162:163], v[178:179]\n" \
"v_pk_fma_f32 v[156:157], v[156:157], v[164:165], v[180:181]\n" \
"v_pk_fma_f32 v[158:159], v[158:159], v[166:167], v[182:183]\n" \
"v_lshlrev_b32_e32 v168, 16, v90\n" \
"v_and_b32_e32 v169, s43, v90\n" \
"v_lshlrev_b32_e32 v170, 16, v91\n" \
"v_and_b32_e32 v171, s43, v91\n" \
"v_lshlrev_b32_e32 v172, 16, v94\n" \
"v_and_b32_e32 v173, s43, v94\n" \
"v_lshlrev_b32_e32 v174, 16, v95\n" \
"v_and_b32_e32 v175, s43, v95\n" \
"v_pk_fma_f32 v[152:153], v[152:153], v[168:169], v[48:49]\n" \
"v_pk_fma_f32 v[154:155], v[154:155], v[170:171], v[50:51]\n" \
"v_pk_fma_f32 v[156:157], v[156:157], v[172:173], v[56:57]\n" \
"v_pk_fma_f32 v[158:159], v[158:159], v[174:175], v[58:59]\n" \
"v_cvt_pk_bf16_f32 v152, v152, v153\n" \
"v_cvt_pk_bf16_f32 v154, v154, v155\n" \
"v_cvt_pk_bf16_f32 v156, v156, v157\n" \
"v_cvt_pk_bf16_f32 v158, v158, v159\n" \
"s_or_b64 %[lat], s[56:57], s[58:59]\n" \
"s_mov_b32 vcc_lo, 0x55555555\n" \
"s_mov_b32 vcc_hi, 0x55555555\n" \
"v_bfrev_b32_e32 v208, 1\n" \
"v_cndmask_b32_e64 v208, %[vd0], v208, %[lat]\n" \
"v_cndmask_b32_dpp v96, v136, v128, vcc quad_perm:[1,0,3,2] row_mask:0xf bank_mask:0xf\n" \
"v_cndmask_b32_dpp v97, v138, v130, vcc quad_perm:[1,0,3,2] row_mask:0xf bank_mask:0xf\n" \
"v_cndmask_b32_dpp v100, v140, v132, vcc quad_perm:[1,0,3,2] row_mask:0xf bank_mask:0xf\n" \
"v_cndmask_b32_dpp v101, v142, v134, vcc quad_perm:[1,0,3,2] row_mask:0xf bank_mask:0xf\n" \
"v_cndmask_b32_dpp v104, v152, v144, vcc quad_perm:[1,0,3,2] row_mask:0xf bank_mask:0xf\n" \
"v_cndmask_b32_dpp v105, v154, v146, vcc quad_perm:[1,0,3,2] row_mask:0xf bank_mask:0xf\n" \
"v_cndmask_b32_dpp v108, v156, v148, vcc quad_perm:[1,0,3,2] row_mask:0xf bank_mask:0xf\n" \
"v_cndmask_b32_dpp v109, v158, v150, vcc quad_perm:[1,0,3,2] row_mask:0xf bank_mask:0xf\n" \
"s_not_b64 vcc, vcc\n" \
"v_xor_b32_e32 v209, 0x80, v208\n" \
"v_cndmask_b32_dpp v98, v128, v136, vcc quad_perm:[1,0,3,2] row_mask:0xf bank_mask:0xf\n" \
"v_cndmask_b32_dpp v99, v130, v138, vcc quad_perm:[1,0,3,2] row_mask:0xf bank_mask:0xf\n" \
"v_cndmask_b32_dpp v102, v132, v140, vcc quad_perm:[1,0,3,2] row_mask:0xf bank_mask:0xf\n" \
"v_cndmask_b32_dpp v103, v134, v142, vcc quad_perm:[1,0,3,2] row_mask:0xf bank_mask:0xf\n" \
"v_cndmask_b32_dpp v106, v144, v152, vcc quad_perm:[1,0,3,2] row_mask:0xf bank_mask:0xf\n" \
"v_cndmask_b32_dpp v107, v146, v154, vcc quad_perm:[1,0,3,2] row_mask:0xf bank_mask:0xf\n" \
"v_cndmask_b32_dpp v110, v148, v156, vcc quad_perm:[1,0,3,2] row_mask:0xf bank_mask:0xf\n" \
"v_cndmask_b32_dpp v111, v150, v158, vcc quad_perm:[1,0,3,2] row_mask:0xf bank_mask:0xf\n" \
"buffer_store_dwordx4 v[96:99], v208, s[64:67], 0 offen offset:0 sc1\n" \
"buffer_store_dwordx4 v[100:103], v208, s[64:67], 0 offen offset:1024 sc1\n" \
"buffer_store_dwordx4 v[104:107], v209, s[64:67], 0 offen offset:0 sc1\n" \
"buffer_store_dwordx4 v[108:111], v209, s[64:67], 0 offen offset:1024 sc1\n" \
"TLls_%=:\n" \
"v_readlane_b32 s63, %[tlo], 28\n" \
"v_accvgpr_read_b32 v124, a32\n" \
"v_accvgpr_read_b32 v214, a33\n" \
"v_accvgpr_read_b32 v73, a34\n" \
"s_add_u32 s62, s40, 65536\n" \
"v_lshl_add_u32 v125, v125, 11, v214\n" \
"v_mov_b32_e32 v72, s40\n" \
"v_lshl_add_u32 v125, v125, 1, s62\n" \
"v_add_u32_e32 v214, s40, v214\n" \
"v_and_b32_e32 v74, 1, v222\n" \
"s_add_u32 s58, s63, 0x100000\n" \
"s_lshl_b32 s59, s41, 2\n" \
"s_sub_u32 s59, s59, %[wid]\n" \
"s_cmp_eq_u32 s59, 1\n" \
"s_cselect_b32 s59, 1, 0\n" \
"s_cbranch_scc0 TLkw_%=\n" \
"s_waitcnt vmcnt(4)\n" \
"TLk2_%=:\n" \
"v_min3_u32 v64, v112, v113, v114\n" \
"v_min3_u32 v65, v115, v116, v117\n" \
"v_min3_u32 v64, v64, v118, v119\n" \
"v_min_u32_e32 v64, v64, v65\n" \
"v_cmp_eq_u32_e32 vcc, 0, v64\n" \
"s_cbranch_vccnz TLk1_%=\n" \
"TLkw_%=:\n" \
"s_waitcnt lgkmcnt(0)\n" \
"s_barrier\n" \
"buffer_load_dwordx4 v[128:131], v73, %[srdz], s63 offen offset:0 sc1\n" \
"buffer_load_dwordx4 v[132:135], v73, %[srdz], s58 offen offset:0 sc1\n" \
"ds_read_b32 v120, v125 offset:0\n" \
"ds_read_u16 v121, v214 offset:2048\n" \
"ds_read_u16 v212, v214 offset:0\n" \
"ds_read_u16 v123, v214 offset:4096\n" \
"ds_read_b128 v[68:71], v72 offset:14384\n" \
"s_cmp_eq_u32 s59, 0\n" \
"s_cbranch_scc0 TLg1_%=\n" \
"s_waitcnt lgkmcnt(0)\n" \
"v_lshlrev_b32_e32 v121, 16, v121\n" \
"v_lshlrev_b32_e32 v212, 16, v212\n" \
"v_lshlrev_b32_e32 v123, 16, v123\n" \
"v_sub_f32_e32 v213, v120, v4\n" \
"v_mul_f32_e32 v213, v213, v126\n" \
"v_fma_f32 v127, v213, v121, v212\n" \
"v_add_f32_e32 v120, v120, v191\n" \
"v_add_f32_e32 v64, v68, v69\n" \
"v_add_f32_e32 v65, v70, v71\n" \
"v_add_f32_e32 v122, v64, v65\n" \
"v_add_f32_e32 v122, v122, v20\n" \
"v_cvt_pk_bf16_f32 v122, 0, v122\n" \
"v_mul_f32_e32 v122, 0xbfb8aa3b, v122\n" \
"v_exp_f32_e32 v122, v122\n" \
"s_nop 0\n" \
"v_add_f32_e32 v122, 1.0, v122\n" \
"v_rcp_f32_e32 v122, v122\n" \
"s_nop 0\n" \
"v_cvt_pk_bf16_f32 v122, 0, v122\n" \
"TLg2_%=:\n" \
"s_lshl_b32 s62, %[wid], 5\n" \
"v_lshrrev_b32_e32 v66, 2, v222\n" \
"s_add_u32 s62, s62, s40\n" \
"v_and_b32_e32 v66, 12, v66\n" \
"v_lshl_or_b32 v66, v74, 4, v66\n" \
"v_add_u32_e32 v66, s62, v66\n" \
"TLg3_%=:\n" \
"s_waitcnt vmcnt(0)\n" \
"v_bitop3_b32 v65, v132, v133, v134 bitop3:0x80\n" \
"v_bitop3_b32 v67, v128, v129, v130 bitop3:0x80\n" \
"v_bitop3_b32 v65, v65, v135, v131 bitop3:0x80\n" \
"v_pk_add_f32 v[128:129], v[128:129], v[132:133]\n" \
"v_bitop3_b32 v65, v65, v67, s41 bitop3:0x80\n" \
"v_pk_add_f32 v[130:131], v[130:131], v[134:135]\n" \
"v_cmp_eq_u32_e32 vcc, 0, v65\n" \
"s_nop 0\n" \
"v_permlane32_swap_b32_e32 v128, v130\n" \
"v_permlane32_swap_b32_e32 v129, v131\n" \
"v_add_f32_e32 v128, v128, v130\n" \
"v_add_f32_e32 v129, v129, v131\n" \
"s_cbranch_vccnz TLga_%=\n" \
"TLgb_%=:\n" \
"s_nop 0\n" \
"v_permlane16_swap_b32_e32 v128, v129\n" \
"v_add_f32_e32 v128, v128, v129\n" \
"s_nop 1\n" \
"v_add_f32_dpp v128, v128, v128 row_ror:8 row_mask:0xf bank_mask:0xf\n" \
"s_nop 1\n" \
"v_add_f32_dpp v128, v128, v128 row_ror:4 row_mask:0xf bank_mask:0xf\n" \
"s_nop 1\n" \
"v_add_f32_dpp v128, v128, v128 quad_perm:[2,3,0,1] row_mask:0xf bank_mask:0xf\n" \
"ds_write_b32 v66, v128 offset:29696\n" \
"v_and_b32_e32 v65, 7, v222\n" \
"v_mov_b32_e32 v74, s60\n" \
"v_lshl_add_u32 v65, v65, 2, s40\n" \
"v_mov_b32_e32 v75, s61\n" \
"s_mov_b32 vcc_lo, 0xaaaaaaaa\n" \
"s_min_u32 s56, s60, s61\n" \
"v_cndmask_b32_e32 v74, v74, v75, vcc\n" \
"s_max_u32 s57, s60, s61\n" \
"v_lshl_or_b32 v74, v74, 10, v124\n" \
"v_lshlrev_b32_e32 v74, 1, v74\n" \
"s_waitcnt lgkmcnt(0)\n" \
"s_barrier\n" \
"ds_read_b64 v[66:67], v72 offset:14432\n" \
"ds_read_b32 v68, v65 offset:29696\n" \
"ds_read_b32 v69, v65 offset:29728\n" \
"ds_read_b32 v70, v65 offset:29760\n" \
"ds_read_b32 v71, v65 offset:29792\n" \
"s_waitcnt lgkmcnt(4)\n" \
"v_readfirstlane_b32 %[k2], v66\n" \
"v_readfirstlane_b32 %[k1], v67\n" \
"s_waitcnt lgkmcnt(0)\n" \
"v_add_f32_e32 v68, v68, v69\n" \
"v_add_f32_e32 v70, v70, v71\n" \
"s_andn2_b32 %[k1], 0x7ff, %[k1]\n" \
"s_andn2_b32 %[k2], 0x7ff, %[k2]\n" \
"v_add_f32_e32 v68, v68, v70\n" \
"s_min_u32 s62, %[k1], %[k2]\n" \
"s_max_u32 s63, %[k1], %[k2]\n" \
"v_fma_f32 v68, v68, v122, v127\n" \
"s_cmp_lg_u64 s[62:63], s[56:57]\n" \
"s_cselect_b32 %[got], 2, 0\n" \
"v_fma_f32 v68, v68, v123, v120\n" \
"s_or_b32 %[got], %[got], s53\n" \
"s_cbranch_scc1 TLagain_%=\n" \
"TLfin_%=:\n" \
"s_lshr_b32 %[got], s41, 1\n" \
"s_cmp_lg_u32 %[wid], 0\n" \
"s_cselect_b32 s58, 0x80000000, 0\n" \
"v_cvt_pk_bf16_f32 v68, v68, v68\n" \
"s_mov_b64 exec, 0xff\n" \
"buffer_store_short v68, v74, s[64:67], s58 offen offset:0 sc1\n" \
"s_mov_b64 exec, -1\n" \
"s_branch Lend_%=\n" \
"s_nop 0\n" \
"TLpk_%=:\n" \
"v_readlane_b32 s62, %[tlo], 35\n" \
"v_lshrrev_b32_e32 v72, 2, v224\n" \
"s_mov_b32 s51, 0x10000\n" \
"s_nop 4\n" \
"TLpk1_%=:\n" \
"buffer_load_dwordx4 v[64:67], v72, %[srdz], s62 offen offset:0 sc1\n" \
"buffer_load_dwordx4 v[68:71], v72, %[srdz], s62 offen offset:16 sc1\n" \
"s_waitcnt vmcnt(0)\n" \
"v_min3_u32 v208, v64, v65, v66\n" \
"v_min3_u32 v209, v67, v68, v69\n" \
"v_min3_u32 v208, v208, v70, v71\n" \
"v_min_u32_e32 v208, v208, v209\n" \
"v_cmp_eq_u32_e32 vcc, 0, v208\n" \
"s_sub_u32 s51, s51, 1\n" \
"s_cbranch_scc1 TLpkb_%=\n" \
"s_cbranch_vccz TLpkb_%=\n" \
"s_sleep 8\n" \
"s_branch TLpk1_%=\n" \
"TLrare_%=:\n" \
"v_cmp_u_f32_e32 vcc, v14, v15\n" \
"s_cbranch_vccz TLvar_%=\n" \
"s_sub_u32 s52, s52, 1\n" \
"s_cbranch_scc1 TLvar_%=\n" \
"buffer_load_dwordx4 a[164:167], v217, %[srdz], s54 offen offset:0 sc1\n" \
"buffer_load_dwordx4 a[168:171], v217, %[srdz], s54 offen offset:1024 sc1\n" \
"buffer_load_dwordx4 a[172:175], v217, %[srdz], s54 offen offset:2048 sc1\n" \
"buffer_load_dwordx4 a[176:179], v217, %[srdz], s54 offen offset:3072 sc1\n" \
"buffer_load_dwordx4 a[180:183], v217, %[srdz], s55 offen offset:0 sc1\n" \
"buffer_load_dwordx4 a[184:187], v217, %[srdz], s55 offen offset:1024 sc1\n" \
"buffer_load_dwordx4 a[188:191], v217, %[srdz], s55 offen offset:2048 sc1\n" \
"buffer_load_dwordx4 a[192:195], v217, %[srdz], s55 offen offset:3072 sc1\n" \
"v_accvgpr_read_b32 v33, a248\n" \
"v_lshlrev_b32_e32 v32, 16, v33\n" \
"v_and_b32_e32 v33, s43, v33\n" \
"v_accvgpr_read_b32 v35, a249\n" \
"v_lshlrev_b32_e32 v34, 16, v35\n" \
"v_and_b32_e32 v35, s43, v35\n" \
"v_accvgpr_read_b32 v41, a252\n" \
"v_lshlrev_b32_e32 v40, 16, v41\n" \
"v_and_b32_e32 v41, s43, v41\n" \
"v_accvgpr_read_b32 v43, a253\n" \
"v_lshlrev_b32_e32 v42, 16, v43\n" \
"v_and_b32_e32 v43, s43, v43\n" \
"v_accvgpr_read_b32 v49, a250\n" \
"v_lshlrev_b32_e32 v48, 16, v49\n" \
"v_and_b32_e32 v49, s43, v49\n" \
"v_accvgpr_read_b32 v51, a251\n" \
"v_lshlrev_b32_e32 v50, 16, v51\n" \
"v_and_b32_e32 v51, s43, v51\n" \
"v_accvgpr_read_b32 v57, a254\n" \
"v_lshlrev_b32_e32 v56, 16, v57\n" \
"v_and_b32_e32 v57, s43, v57\n" \
"v_accvgpr_read_b32 v59, a255\n" \
"v_lshlrev_b32_e32 v58, 16, v59\n" \
"v_and_b32_e32 v59, s43, v59\n" \
"v_pk_mul_f32 v[6:7], v[0:1], v[160:161]\n" \
"v_pk_mul_f32 v[12:13], v[0:1], v[0:1]\n" \
"v_pk_add_f32 v[4:5], v[0:1], v[2:3]\n" \
"v_pk_fma_f32 v[6:7], v[2:3], v[162:163], v[6:7]\n" \
"v_pk_fma_f32 v[12:13], v[2:3], v[2:3], v[12:13]\n" \
"v_pk_add_f32 v[4:5], v[4:5], v[8:9]\n" \
"v_pk_fma_f32 v[6:7], v[8:9], v[168:169], v[6:7]\n" \
"v_pk_fma_f32 v[12:13], v[8:9], v[8:9], v[12:13]\n" \
"v_pk_add_f32 v[4:5], v[4:5], v[10:11]\n" \
"v_pk_fma_f32 v[6:7], v[10:11], v[170:171], v[6:7]\n" \
"v_pk_fma_f32 v[12:13], v[10:11], v[10:11], v[12:13]\n" \
"v_pk_add_f32 v[4:5], v[4:5], v[16:17]\n" \
"v_pk_fma_f32 v[6:7], v[16:17], v[164:165], v[6:7]\n" \
"v_pk_fma_f32 v[12:13], v[16:17], v[16:17], v[12:13]\n" \
"v_pk_add_f32 v[4:5], v[4:5], v[18:19]\n" \
"v_pk_fma_f32 v[6:7], v[18:19], v[166:167], v[6:7]\n" \
"v_pk_fma_f32 v[12:13], v[18:19], v[18:19], v[12:13]\n" \
"v_pk_add_f32 v[4:5], v[4:5], v[24:25]\n" \
"v_pk_fma_f32 v[6:7], v[24:25], v[172:173], v[6:7]\n" \
"v_pk_fma_f32 v[12:13], v[24:25], v[24:25], v[12:13]\n" \
"v_pk_add_f32 v[4:5], v[4:5], v[26:27]\n" \
"v_pk_fma_f32 v[6:7], v[26:27], v[174:175], v[6:7]\n" \
"v_pk_fma_f32 v[12:13], v[26:27], v[26:27], v[12:13]\n" \
"s_waitcnt vmcnt(0)\n" \
"s_nop 1\n" \
".if . - adaln_body != 69632\n" \
".error \"size model\"\n" \
".endif\n" \
"Lk16_%=:\n" \
"s_cbranch_execz adaln_kr\n" \
"s_nop 0\n" \
"v_mfma_f32_16x16x32_bf16 v[208:211], v[230:233], a[164:167], 0\n" \
"v_mfma_f32_16x16x32_bf16 v[212:215], v[230:233], a[172:175], 0\n" \
"v_mfma_f32_16x16x32_bf16 v[208:211], v[234:237], a[168:171], v[208:211]\n" \
"v_mfma_f32_16x16x32_bf16 v[212:215], v[234:237], a[176:179], v[212:215]\n" \
"s_nop 6\n" \
"v_pk_fma_f32 v[32:33], v[208:209], v[144:145], v[32:33]\n" \
"v_pk_fma_f32 v[34:35], v[210:211], v[146:147], v[34:35]\n" \
"v_pk_fma_f32 v[40:41], v[212:213], v[152:153], v[40:41]\n" \
"v_pk_fma_f32 v[42:43], v[214:215], v[154:155], v[42:43]\n" \
"v_pk_mul_f32 v[36:37], v[32:33], v[176:177]\n" \
"v_pk_mul_f32 v[38:39], v[32:33], v[32:33]\n" \
"v_pk_add_f32 v[14:15], v[32:33], v[34:35]\n" \
"v_pk_fma_f32 v[36:37], v[34:35], v[178:179], v[36:37]\n" \
"v_pk_fma_f32 v[38:39], v[34:35], v[34:35], v[38:39]\n" \
"v_pk_add_f32 v[14:15], v[14:15], v[40:41]\n" \
"v_pk_fma_f32 v[36:37], v[40:41], v[184:185], v[36:37]\n" \
"v_pk_fma_f32 v[38:39], v[40:41], v[40:41], v[38:39]\n" \
"v_pk_add_f32 v[14:15], v[14:15], v[42:43]\n" \
"v_pk_fma_f32 v[36:37], v[42:43], v[186:187], v[36:37]\n" \
"v_pk_fma_f32 v[38:39], v[42:43], v[42:43], v[38:39]\n" \
"s_nop 7\n" \
"v_mfma_f32_16x16x32_bf16 v[208:211], v[230:233], a[180:183], 0\n" \
"v_mfma_f32_16x16x32_bf16 v[212:215], v[230:233], a[188:191], 0\n" \
"v_mfma_f32_16x16x32_bf16 v[208:211], v[234:237], a[184:187], v[208:211]\n" \
"v_mfma_f32_16x16x32_bf16 v[212:215], v[234:237], a[192:195], v[212:215]\n" \
"s_nop 6\n" \
"v_pk_fma_f32 v[48:49], v[208:209], v[148:149], v[48:49]\n" \
"v_pk_fma_f32 v[50:51], v[210:211], v[150:151], v[50:51]\n" \
"v_pk_fma_f32 v[56:57], v[212:213], v[156:157], v[56:57]\n" \
"v_pk_fma_f32 v[58:59], v[214:215], v[158:159], v[58:59]\n" \
"v_pk_add_f32 v[14:15], v[14:15], v[48:49]\n" \
"v_pk_fma_f32 v[36:37], v[48:49], v[180:181], v[36:37]\n" \
"v_pk_fma_f32 v[38:39], v[48:49], v[48:49], v[38:39]\n" \
"v_pk_add_f32 v[14:15], v[14:15], v[50:51]\n" \
"v_pk_fma_f32 v[36:37], v[50:51], v[182:183], v[36:37]\n" \
"v_pk_fma_f32 v[38:39], v[50:51], v[50:51], v[38:39]\n" \
"v_pk_add_f32 v[14:15], v[14:15], v[56:57]\n" \
"v_pk_fma_f32 v[36:37], v[56:57], v[188:189], v[36:37]\n" \
"v_pk_fma_f32 v[38:39], v[56:57], v[56:57], v[38:39]\n" \
"v_pk_add_f32 v[14:15], v[14:15], v[58:59]\n" \
"v_pk_fma_f32 v[36:37], v[58:59], v[190:191], v[36:37]\n" \
"v_pk_fma_f32 v[38:39], v[58:59], v[58:59], v[38:39]\n" \
"v_pk_add_f32 v[4:5], v[4:5], v[14:15]\n" \
"v_pk_add_f32 v[6:7], v[6:7], v[36:37]\n" \
"v_pk_add_f32 v[12:13], v[12:13], v[38:39]\n" \
"v_add_f32_e32 v4, v4, v5\n" \
"v_add_f32_e32 v6, v6, v7\n" \
"v_add_f32_e32 v12, v12, v13\n" \
"v_mov_b32_e32 v208, v12\n" \
"v_permlane32_swap_b32_e32 v4, v6\n" \
"s_nop 0\n" \
"v_permlane32_swap_b32_e32 v12, v208\n" \
"v_add_f32_e32 v4, v4, v6\n" \
"v_add_f32_e32 v12, v12, v208\n" \
"s_nop 0\n" \
"s_nop 0\n" \
"v_permlane16_swap_b32_e32 v4, v12\n" \
"v_add_f32_e32 v4, v4, v12\n" \
"s_nop 1\n" \
"v_add_f32_dpp v4, v4, v4 quad_perm:[1,0,3,2] row_mask:0xf bank_mask:0xf\n" \
"s_nop 1\n" \
"v_add_f32_dpp v4, v4, v4 quad_perm:[2,3,0,1] row_mask:0xf bank_mask:0xf\n" \
"s_nop 1\n" \
"v_add_f32_dpp v4, v4, v4 row_ror:8 row_mask:0xf bank_mask:0xf\n" \
"v_mov_b32_e32 v12, v4\n" \
"s_nop 0\n" \
"s_nop 0\n" \
"v_permlane16_swap_b32_e32 v4, v12\n" \
"v_mov_b32_e32 v6, v4\n" \
"v_mul_f32_e32 v12, 0x3a800000, v12\n" \
"s_nop 0\n" \
"v_permlane32_swap_b32_e32 v4, v6\n" \
"v_mul_f32_e32 v218, 0x3a800000, v4\n" \
"v_mul_f32_e32 v208, v218, v218\n" \
"v_sub_f32_e32 v12, v12, v208\n" \
"v_cmp_nle_f32_e32 vcc, v208, v12\n" \
"v_add_f32_e32 v12, 0x3727c5ac, v12\n" \
"v_rsq_f32_e32 v220, v12\n" \
"v_fma_f32 v210, -v218, v223, v6\n" \
"s_mov_b64 exec, 17\n" \
"v_mul_f32_e32 v210, v210, v220\n" \
"v_ashrrev_i32_e32 v208, 31, v210\n" \
"v_or_b32_e32 v208, 0x80000000, v208\n" \
"v_xor_b32_e32 v210, v210, v208\n" \
"v_and_b32_e32 v210, 0xfffff800, v210\n" \
"v_max_u32_e32 v210, 0x800, v210\n" \
"v_or_b32_e32 v210, v210, %[ta0]\n" \
"ds_write_b32 v216, v210 offset:14400\n" \
"s_mov_b64 exec, -1\n" \
"s_cbranch_vccnz TLrare_%=\n" \
"s_branch TLrok_%=\n" \
"TLvar_%=:\n" \
"v_sub_f32_e32 v210, 0, v218\n" \
"v_pk_add_f32 v[212:213], v[0:1], v[210:211] op_sel_hi:[1,0]\n" \
"v_pk_mul_f32 v[214:215], v[212:213], v[212:213]\n" \
"v_pk_add_f32 v[212:213], v[2:3], v[210:211] op_sel_hi:[1,0]\n" \
"v_pk_fma_f32 v[214:215], v[212:213], v[212:213], v[214:215]\n" \
"v_pk_add_f32 v[212:213], v[8:9], v[210:211] op_sel_hi:[1,0]\n" \
"v_pk_fma_f32 v[214:215], v[212:213], v[212:213], v[214:215]\n" \
"v_pk_add_f32 v[212:213], v[10:11], v[210:211] op_sel_hi:[1,0]\n" \
"v_pk_fma_f32 v[214:215], v[212:213], v[212:213], v[214:215]\n" \
"v_pk_add_f32 v[212:213], v[16:17], v[210:211] op_sel_hi:[1,0]\n" \
"v_pk_fma_f32 v[214:215], v[212:213], v[212:213], v[214:215]\n" \
"v_pk_add_f32 v[212:213], v[18:19], v[210:211] op_sel_hi:[1,0]\n" \
"v_pk_fma_f32 v[214:215], v[212:213], v[212:213], v[214:215]\n" \
"v_pk_add_f32 v[212:213], v[24:25], v[210:211] op_sel_hi:[1,0]\n" \
"v_pk_fma_f32 v[214:215], v[212:213], v[212:213], v[214:215]\n" \
"v_pk_add_f32 v[212:213], v[26:27], v[210:211] op_sel_hi:[1,0]\n" \
"v_pk_fma_f32 v[214:215], v[212:213], v[212:213], v[214:215]\n" \
"v_pk_add_f32 v[212:213], v[32:33], v[210:211] op_sel_hi:[1,0]\n" \
"v_pk_fma_f32 v[214:215], v[212:213], v[212:213], v[214:215]\n" \
"v_pk_add_f32 v[212:213], v[34:35], v[210:211] op_sel_hi:[1,0]\n" \
"v_pk_fma_f32 v[214:215], v[212:213], v[212:213], v[214:215]\n" \
"v_pk_add_f32 v[212:213], v[40:41], v[210:211] op_sel_hi:[1,0]\n" \
"v_pk_fma_f32 v[214:215], v[212:213], v[212:213], v[214:215]\n" \
"v_pk_add_f32 v[212:213], v[42:43], v[210:211] op_sel_hi:[1,0]\n" \
"v_pk_fma_f32 v[214:215], v[212:213], v[212:213], v[214:215]\n" \
"v_pk_add_f32 v[212:213], v[48:49], v[210:211] op_sel_hi:[1,0]\n" \
"v_pk_fma_f32 v[214:215], v[212:213], v[212:213], v[214:215]\n" \
"v_pk_add_f32 v[212:213], v[50:51], v[210:211] op_sel_hi:[1,0]\n" \
"v_pk_fma_f32 v[214:215], v[212:213], v[212:213], v[214:215]\n" \
"v_pk_add_f32 v[212:213], v[56:57], v[210:211] op_sel_hi:[1,0]\n" \
"v_pk_fma_f32 v[214:215], v[212:213], v[212:213], v[214:215]\n" \
"v_pk_add_f32 v[212:213], v[58:59], v[210:211] op_sel_hi:[1,0]\n" \
"v_pk_fma_f32 v[214:215], v[212:213], v[212:213], v[214:215]\n" \
"v_add_f32_e32 v12, v214, v215\n" \
"s_nop 1\n" \
"v_add_f32_dpp v12, v12, v12 quad_perm:[1,0,3,2] row_mask:0xf bank_mask:0xf\n" \
"s_nop 1\n" \
"v_add_f32_dpp v12, v12, v12 quad_perm:[2,3,0,1] row_mask:0xf bank_mask:0xf\n" \
"s_nop 1\n" \
"v_add_f32_dpp v12, v12, v12 row_ror:8 row_mask:0xf bank_mask:0xf\n" \
"v_mov_b32_e32 v210, v12\n" \
"s_nop 0\n" \
"s_nop 0\n" \
"v_permlane16_swap_b32_e32 v12, v210\n" \
"v_add_f32_e32 v12, v12, v210\n" \
"v_mov_b32_e32 v210, v12\n" \
"s_nop 0\n" \
"s_nop 0\n" \
"v_permlane32_swap_b32_e32 v12, v210\n" \
"v_add_f32_e32 v12, v12, v210\n" \
"v_mul_f32_e32 v12, 0x3a800000, v12\n" \
"v_add_f32_e32 v12, 0x3727c5ac, v12\n" \
"v_rsq_f32_e32 v220, v12\n" \
"v_fma_f32 v210, -v218, v223, v6\n" \
"s_mov_b64 exec, 17\n" \
"v_mul_f32_e32 v210, v210, v220\n" \
"v_ashrrev_i32_e32 v208, 31, v210\n" \
"v_or_b32_e32 v208, 0x80000000, v208\n" \
"v_xor_b32_e32 v210, v210, v208\n" \
"v_and_b32_e32 v210, 0xfffff800, v210\n" \
"v_max_u32_e32 v210, 0x800, v210\n" \
"v_or_b32_e32 v210, v210, %[ta0]\n" \
"ds_write_b32 v216, v210 offset:14400\n" \
"s_mov_b64 exec, -1\n" \
"s_branch TLrok_%=\n" \
"TLcw_%=:\n" \
"s_waitcnt lgkmcnt(0)\n" \
"s_mov_b32 s63, 278528\n" \
"s_mov_b32 s51, 0x10000\n" \
"TLcw1_%=:\n" \
"buffer_load_dword %[vxs], %[vdn], %[srdz], s63 offen offset:0 sc1\n" \
"s_waitcnt vmcnt(0)\n" \
"v_and_b32_e32 v44, 0x820, %[vxs]\n" \
"v_cmp_ne_u32_e32 vcc, 0x820, v44\n" \
"s_sub_u32 s51, s51, 1\n" \
"s_cbranch_scc1 TLcw2_%=\n" \
"s_nop 3\n" \
"s_cbranch_vccz TLcw2_%=\n" \
"s_sleep 8\n" \
"s_branch TLcw1_%=\n" \
"TLcw2_%=:\n" \
"s_sleep 4\n" \
"s_mov_b32 s59, 1\n" \
"buffer_load_dwordx4 a[128:131], %[vct], %[srdz], 0 offen offset:0 sc1\n" \
"buffer_load_dwordx4 a[132:135], %[rd0], %[srdz], 0 offen offset:0 sc1\n" \
"buffer_load_dwordx4 a[136:139], %[vct], %[srdz], s56 offen offset:0 sc1\n" \
"buffer_load_dwordx4 a[140:143], %[rd0], %[srdz], s56 offen offset:0 sc1\n" \
"buffer_load_dwordx4 a[144:147], %[vct], %[srdz], s57 offen offset:0 sc1\n" \
"buffer_load_dwordx4 a[148:151], %[rd0], %[srdz], s57 offen offset:0 sc1\n" \
"buffer_load_dwordx4 a[152:155], %[vct], %[srdz], s58 offen offset:0 sc1\n" \
"buffer_load_dwordx4 a[156:159], %[rd0], %[srdz], s58 offen offset:0 sc1\n" \
"s_waitcnt vmcnt(0)\n" \
"s_branch TLcs_%=\n" \
"TLcx_%=:\n" \
"s_cmp_eq_u32 s59, 0\n" \
"s_cbranch_scc1 TLcw_%=\n" \
"s_sub_u32 s52, s52, 1\n" \
"s_cbranch_scc1 TLco_%=\n" \
"buffer_load_dwordx4 a[128:131], %[vct], %[srdz], 0 offen offset:0 sc1\n" \
"buffer_load_dwordx4 a[132:135], %[rd0], %[srdz], 0 offen offset:0 sc1\n" \
"buffer_load_dwordx4 a[136:139], %[vct], %[srdz], s56 offen offset:0 sc1\n" \
"buffer_load_dwordx4 a[140:143], %[rd0], %[srdz], s56 offen offset:0 sc1\n" \
"buffer_load_dwordx4 a[144:147], %[vct], %[srdz], s57 offen offset:0 sc1\n" \
"buffer_load_dwordx4 a[148:151], %[rd0], %[srdz], s57 offen offset:0 sc1\n" \
"buffer_load_dwordx4 a[152:155], %[vct], %[srdz], s58 offen offset:0 sc1\n" \
"buffer_load_dwordx4 a[156:159], %[rd0], %[srdz], s58 offen offset:0 sc1\n" \
"s_waitcnt vmcnt(0) lgkmcnt(0)\n" \
"s_branch TLcs_%=\n" \
"TLk1_%=:\n" \
"s_sub_u32 s51, s51, 1\n" \
"s_cbranch_scc1 TLkw_%=\n" \
"s_sleep 3\n" \
"buffer_load_dwordx4 v[112:115], v216, %[srdz], s49 offen offset:0 sc1\n" \
"buffer_load_dwordx4 v[116:119], v216, %[srdz], s49 offen offset:16 sc1\n" \
"s_waitcnt vmcnt(0)\n" \
"s_branch TLk2_%=\n" \
"TLg1_%=:\n" \
"s_waitcnt lgkmcnt(0)\n" \
"v_min_u32_e32 v64, v113, v115\n" \
"v_min_u32_e32 v65, v117, v119\n" \
"v_max_u32_e32 v113, v113, v115\n" \
"v_max_u32_e32 v117, v117, v119\n" \
"v_max3_u32 v112, v64, v112, v114\n" \
"v_max3_u32 v116, v65, v116, v118\n" \
"v_min_u32_e32 v64, v113, v117\n" \
"v_max_u32_e32 v113, v113, v117\n" \
"v_max3_u32 v112, v64, v112, v116\n" \
"s_nop 0\n" \
"v_min_u32_dpp v64, v113, v113 quad_perm:[1,0,3,2] row_mask:0xf bank_mask:0xf\n" \
"v_max_u32_dpp v65, v112, v112 quad_perm:[1,0,3,2] row_mask:0xf bank_mask:0xf\n" \
"v_max_u32_dpp v113, v113, v113 quad_perm:[1,0,3,2] row_mask:0xf bank_mask:0xf\n" \
"v_max_u32_e32 v112, v64, v65\n" \
"s_nop 0\n" \
"v_min_u32_dpp v64, v113, v113 quad_perm:[2,3,0,1] row_mask:0xf bank_mask:0xf\n" \
"v_max_u32_dpp v65, v112, v112 quad_perm:[2,3,0,1] row_mask:0xf bank_mask:0xf\n" \
"v_max_u32_dpp v113, v113, v113 quad_perm:[2,3,0,1] row_mask:0xf bank_mask:0xf\n" \
"v_max_u32_e32 v112, v64, v65\n" \
"s_nop 0\n" \
"v_min_u32_dpp v64, v113, v113 row_half_mirror row_mask:0xf bank_mask:0xf\n" \
"v_max_u32_dpp v65, v112, v112 row_half_mirror row_mask:0xf bank_mask:0xf\n" \
"v_max_u32_dpp v113, v113, v113 row_half_mirror row_mask:0xf bank_mask:0xf\n" \
"v_max_u32_e32 v112, v64, v65\n" \
"s_nop 0\n" \
"v_min_u32_dpp v64, v113, v113 row_mirror row_mask:0xf bank_mask:0xf\n" \
"v_max_u32_dpp v65, v112, v112 row_mirror row_mask:0xf bank_mask:0xf\n" \
"v_max_u32_dpp v113, v113, v113 row_mirror row_mask:0xf bank_mask:0xf\n" \
"v_max_u32_e32 v112, v64, v65\n" \
"v_mov_b32_e32 v115, v113\n" \
"v_mov_b32_e32 v114, v112\n" \
"s_nop 0\n" \
"v_permlane16_swap_b32_e32 v113, v115\n" \
"v_permlane16_swap_b32_e32 v112, v114\n" \
"v_min_u32_e32 v64, v113, v115\n" \
"v_max_u32_e32 v113, v113, v115\n" \
"v_max3_u32 v112, v64, v112, v114\n" \
"v_mov_b32_e32 v115, v113\n" \
"v_mov_b32_e32 v114, v112\n" \
"s_nop 0\n" \
"v_permlane32_swap_b32_e32 v113, v115\n" \
"v_permlane32_swap_b32_e32 v112, v114\n" \
"v_min_u32_e32 v64, v113, v115\n" \
"v_max_u32_e32 v113, v113, v115\n" \
"v_max3_u32 v112, v64, v112, v114\n" \
"s_mov_b64 exec, 1\n" \
"ds_write_b64 v72, v[112:113] offset:14432\n" \
"s_mov_b64 exec, -1\n" \
"s_branch TLg2_%=\n" \
"TLga_%=:\n" \
"s_sub_u32 s51, s51, 1\n" \
"s_cbranch_scc1 TLgb_%=\n" \
"buffer_load_dwordx4 v[128:131], v73, %[srdz], s63 offen offset:0 sc1\n" \
"buffer_load_dwordx4 v[132:135], v73, %[srdz], s58 offen offset:0 sc1\n" \
"s_branch TLg3_%=\n" \
"TLagain_%=:\n" \
"s_cmp_eq_u32 s41, 4\n" \
"s_cbranch_scc1 TLfin_%=\n" \
"s_lshl_b32 s41, s41, 1\n" \
"v_add_f32_e32 v191, v191, v4\n" \
"v_lshl_or_b32 v227, v227, 1, 1\n" \
"v_readlane_b32 s62, v191, 0\n" \
"v_readlane_b32 s63, v191, 1\n" \
"s_bitcmp1_b32 %[got], 1\n" \
"s_cselect_b32 s44, %[k1], s60\n" \
"s_cselect_b32 s45, %[k2], s61\n" \
"s_cselect_b32 s62, 0, s62\n" \
"s_cselect_b32 s63, 0, s63\n" \
"s_mov_b32 s46, s44\n" \
"s_mov_b32 s47, s45\n" \
"v_mov_b32_e32 v208, s62\n" \
"v_mov_b32_e32 v209, s63\n" \
"s_mov_b32 vcc_lo, 0xaaaaaaaa\n" \
"s_mov_b32 vcc_hi, 0xaaaaaaaa\n" \
"v_cndmask_b32_e32 v191, v208, v209, vcc\n" \
"s_mov_b32 vcc_lo, 0xf0f0f0f0\n" \
"s_mov_b32 vcc_hi, 0xf0f0f0f0\n" \
"v_cndmask_b32_e32 v190, v208, v209, vcc\n" \
"s_mov_b32 vcc_lo, 0xff00ff\n" \
"s_mov_b32 vcc_hi, 0xff00ff\n" \
"v_cndmask_b32_e64 v225, 0, -1, vcc\n" \
"v_sub_u32_e32 v216, 0x7ff, %[ta0]\n" \
"v_cmp_ne_u32_e64 s[56:57], s44, v216\n" \
"v_cmp_ne_u32_e64 s[58:59], s45, v216\n" \
"s_and_b64 s[56:57], s[56:57], s[58:59]\n" \
"s_and_b64 s[56:57], s[56:57], %[lat]\n" \
"s_mov_b64 %[lat], 0\n" \
"v_bfrev_b32_e32 v208, 1\n" \
"v_cndmask_b32_e64 v208, v208, %[vd0], s[56:57]\n" \
"v_xor_b32_e32 v209, 0x80, v208\n" \
"buffer_store_dwordx4 v[96:99], v208, s[64:67], 0 offen offset:0 sc1\n" \
"buffer_store_dwordx4 v[100:103], v208, s[64:67], 0 offen offset:1024 sc1\n" \
"buffer_store_dwordx4 v[104:107], v209, s[64:67], 0 offen offset:0 sc1\n" \
"buffer_store_dwordx4 v[108:111], v209, s[64:67], 0 offen offset:1024 sc1\n" \
"s_movk_i32 s56, 0x1000\n" \
"s_movk_i32 s57, 0x2000\n" \
"s_movk_i32 s58, 0x3000\n" \
"s_movk_i32 s52, 0x1000\n" \
"s_mov_b32 s59, 1\n" \
"s_nop 1\n" \
"v_and_b32_e32 v213, 4, v222\n" \
"v_mov_b32_e32 v208, s44\n" \
"v_cmp_ne_u32_e32 vcc, 0, v213\n" \
"v_mov_b32_e32 v214, s45\n" \
"v_mov_b32_e32 v209, s46\n" \
"v_mov_b32_e32 v215, s47\n" \
"v_cndmask_b32_e32 v208, v208, v214, vcc\n" \
"v_cndmask_b32_e32 v209, v209, v215, vcc\n" \
"v_cmp_lt_u32_e32 vcc, 31, v222\n" \
"v_and_b32_e32 v211, 0xf0, %[kb0]\n" \
"s_lshl_b32 s62, %[wid], 9\n" \
"v_cndmask_b32_e32 v210, v208, v209, vcc\n" \
"v_lshrrev_b32_e32 %[vdn], 6, v210\n" \
"v_lshlrev_b32_e32 %[vdn], 9, %[vdn]\n" \
"v_lshlrev_b32_e32 v213, 11, v210\n" \
"v_lshrrev_b32_e32 v214, 1, v210\n" \
"v_and_b32_e32 v214, 0x80, v214\n" \
"v_lshrrev_b32_e32 v215, 1, v211\n" \
"v_and_b32_e32 v215, 56, v215\n" \
"v_add3_u32 v213, v213, v214, v215\n" \
"v_and_b32_e32 v215, 0x80, v211\n" \
"v_lshlrev_b32_e32 v215, 1, v215\n" \
"v_add3_u32 v80, v213, v215, s62\n" \
"v_xor_b32_e32 v81, 0x80, v80\n" \
"s_mov_b32 s63, 278528\n" \
"buffer_load_dword %[vxs], %[vdn], %[srdz], s63 offen offset:0 sc1\n" \
"global_load_dwordx2 v[88:89], v80, %[px] offset:0\n" \
"global_load_dwordx2 v[90:91], v80, %[px] offset:64\n" \
"global_load_dwordx2 v[92:93], v81, %[px] offset:0\n" \
"global_load_dwordx2 v[94:95], v81, %[px] offset:64\n" \
"v_and_b32_e32 v213, 0x100, v210\n" \
"s_add_u32 s63, s62, s40\n" \
"v_add3_u32 v213, v213, v211, s63\n" \
"v_xor_b32_e32 v214, 0x100, v213\n" \
"ds_read_b128 v[104:107], v213 offset:2048\n" \
"ds_read_b128 v[108:111], v214 offset:2048\n" \
"ds_read_b128 v[64:67], v213 offset:6144\n" \
"ds_read_b128 v[68:71], v213 offset:8192\n" \
"ds_read_b128 v[72:75], v214 offset:6144\n" \
"ds_read_b128 v[76:79], v214 offset:8192\n" \
"ds_read_b128 v[112:115], v213 offset:10240\n" \
"ds_read_b128 v[116:119], v213 offset:12288\n" \
"ds_read_b128 v[120:123], v214 offset:10240\n" \
"ds_read_b128 v[124:127], v214 offset:12288\n" \
"s_movk_i32 s63, 2112\n" \
"v_mad_u32_u24 %[oq0], v219, s63, v213\n" \
"v_mad_u32_u24 %[wo0], v219, s63, v214\n" \
"s_lshl_b32 s62, %[wid], 10\n" \
"s_add_u32 s62, s62, 7340032\n" \
"v_and_b32_e32 v215, 0x3b0, v217\n" \
"v_add_u32_e32 v215, s62, v215\n" \
"s_mov_b32 s63, 38016\n" \
"v_lshrrev_b32_e32 %[vct], 1, v208\n" \
"v_and_b32_e32 v208, 1, v208\n" \
"v_mad_u32_u24 %[vct], %[vct], s63, v215\n" \
"v_lshl_add_u32 %[vct], v208, 6, %[vct]\n" \
"v_lshrrev_b32_e32 %[rd0], 1, v209\n" \
"v_and_b32_e32 v209, 1, v209\n" \
"v_mad_u32_u24 %[rd0], %[rd0], s63, v215\n" \
"v_lshl_add_u32 %[rd0], v209, 6, %[rd0]\n" \
"buffer_load_dwordx4 a[128:131], %[vct], %[srdz], 0 offen offset:0 sc1\n" \
"buffer_load_dwordx4 a[132:135], %[rd0], %[srdz], 0 offen offset:0 sc1\n" \
"buffer_load_dwordx4 a[136:139], %[vct], %[srdz], s56 offen offset:0 sc1\n" \
"buffer_load_dwordx4 a[140:143], %[rd0], %[srdz], s56 offen offset:0 sc1\n" \
"buffer_load_dwordx4 a[144:147], %[vct], %[srdz], s57 offen offset:0 sc1\n" \
"buffer_load_dwordx4 a[148:151], %[rd0], %[srdz], s57 offen offset:0 sc1\n" \
"buffer_load_dwordx4 a[152:155], %[vct], %[srdz], s58 offen offset:0 sc1\n" \
"buffer_load_dwordx4 a[156:159], %[rd0], %[srdz], s58 offen offset:0 sc1\n" \
"s_waitcnt vmcnt(0) lgkmcnt(0)\n" \
"v_lshlrev_b32_e32 v96, 16, v104\n" \
"v_and_b32_e32 v97, s43, v104\n" \
"v_lshlrev_b32_e32 v98, 16, v105\n" \
"v_and_b32_e32 v99, s43, v105\n" \
"v_lshlrev_b32_e32 v100, 16, v106\n" \
"v_and_b32_e32 v101, s43, v106\n" \
"v_lshlrev_b32_e32 v102, 16, v107\n" \
"v_and_b32_e32 v103, s43, v107\n" \
"v_lshlrev_b32_e32 v104, 16, v108\n" \
"v_and_b32_e32 v105, s43, v108\n" \
"v_lshlrev_b32_e32 v106, 16, v109\n" \
"v_and_b32_e32 v107, s43, v109\n" \
"v_lshlrev_b32_e32 v108, 16, v110\n" \
"v_and_b32_e32 v109, s43, v110\n" \
"v_lshlrev_b32_e32 v110, 16, v111\n" \
"v_and_b32_e32 v111, s43, v111\n" \
"v_lshlrev_b32_e32 v80, 16, v88\n" \
"v_and_b32_e32 v81, s43, v88\n" \
"v_lshlrev_b32_e32 v82, 16, v89\n" \
"v_and_b32_e32 v83, s43, v89\n" \
"v_lshlrev_b32_e32 v84, 16, v90\n" \
"v_and_b32_e32 v85, s43, v90\n" \
"v_lshlrev_b32_e32 v86, 16, v91\n" \
"v_and_b32_e32 v87, s43, v91\n" \
"v_lshlrev_b32_e32 v88, 16, v92\n" \
"v_and_b32_e32 v89, s43, v92\n" \
"v_lshlrev_b32_e32 v90, 16, v93\n" \
"v_and_b32_e32 v91, s43, v93\n" \
"v_lshlrev_b32_e32 v92, 16, v94\n" \
"v_and_b32_e32 v93, s43, v94\n" \
"v_lshlrev_b32_e32 v94, 16, v95\n" \
"v_and_b32_e32 v95, s43, v95\n" \
"v_sub_f32_e32 v80, v80, v190\n" \
"v_sub_f32_e32 v81, v81, v190\n" \
"v_sub_f32_e32 v82, v82, v190\n" \
"v_sub_f32_e32 v83, v83, v190\n" \
"v_sub_f32_e32 v84, v84, v190\n" \
"v_sub_f32_e32 v85, v85, v190\n" \
"v_sub_f32_e32 v86, v86, v190\n" \
"v_sub_f32_e32 v87, v87, v190\n" \
"v_sub_f32_e32 v88, v88, v190\n" \
"v_sub_f32_e32 v89, v89, v190\n" \
"v_sub_f32_e32 v90, v90, v190\n" \
"v_sub_f32_e32 v91, v91, v190\n" \
"v_sub_f32_e32 v92, v92, v190\n" \
"v_sub_f32_e32 v93, v93, v190\n" \
"v_sub_f32_e32 v94, v94, v190\n" \
"v_sub_f32_e32 v95, v95, v190\n" \
"s_branch TLcs_%=\n" \
"Lsp1_%=:\n" \
"s_movk_i32 s51, 0x1000\n" \
"s_cmp_gt_u32 s48, 0\n" \
"s_cselect_b32 s61, 0, 1\n" \
"s_nop 0\n" \
"s_mul_i32 s61, s61, 152064\n" \
"Lsq1_%=:\n" \
"buffer_load_dwordx4 a[64:67], %[oq0], %[srdz], s61 offen offset:0 sc1\n" \
"buffer_load_dwordx4 a[68:71], %[oq0], %[srdz], s61 offen offset:1024 sc1\n" \
"buffer_load_dwordx4 a[72:75], %[oq0], %[srdz], s61 offen offset:2048 sc1\n" \
"buffer_load_dwordx4 a[76:79], %[oq0], %[srdz], s61 offen offset:3072 sc1\n" \
"buffer_load_dwordx4 a[80:83], v208, %[srdz], s61 offen offset:0 sc1\n" \
"buffer_load_dwordx4 a[84:87], v208, %[srdz], s61 offen offset:1024 sc1\n" \
"buffer_load_dwordx4 a[88:91], v208, %[srdz], s61 offen offset:2048 sc1\n" \
"buffer_load_dwordx4 a[92:95], v208, %[srdz], s61 offen offset:3072 sc1\n" \
"buffer_load_dwordx4 a[96:99], v209, %[srdz], s61 offen offset:0 sc1\n" \
"buffer_load_dwordx4 a[100:103], v209, %[srdz], s61 offen offset:1024 sc1\n" \
"buffer_load_dwordx4 a[104:107], v209, %[srdz], s61 offen offset:2048 sc1\n" \
"buffer_load_dwordx4 a[108:111], v209, %[srdz], s61 offen offset:3072 sc1\n" \
"buffer_load_dwordx4 a[112:115], v210, %[srdz], s61 offen offset:0 sc1\n" \
"buffer_load_dwordx4 a[116:119], v210, %[srdz], s61 offen offset:1024 sc1\n" \
"buffer_load_dwordx4 a[120:123], v210, %[srdz], s61 offen offset:2048 sc1\n" \
"buffer_load_dwordx4 a[124:127], v210, %[srdz], s61 offen offset:3072 sc1\n" \
"s_waitcnt vmcnt(0)\n" \
"s_nop 0\n" \
"v_mfma_f32_16x16x32_bf16 v[128:131], a[236:239], a[64:67], v[192:195]\n" \
"v_mfma_f32_16x16x32_bf16 v[128:131], a[196:199], a[68:71], v[128:131]\n" \
"v_mfma_f32_16x16x32_bf16 v[128:131], a[200:203], a[72:75], v[128:131]\n" \
"v_mfma_f32_16x16x32_bf16 v[128:131], a[204:207], a[76:79], v[128:131]\n" \
"v_mfma_f32_16x16x32_bf16 v[132:135], a[236:239], a[80:83], v[196:199]\n" \
"v_mfma_f32_16x16x32_bf16 v[132:135], a[196:199], a[84:87], v[132:135]\n" \
"v_mfma_f32_16x16x32_bf16 v[132:135], a[200:203], a[88:91], v[132:135]\n" \
"v_mfma_f32_16x16x32_bf16 v[132:135], a[204:207], a[92:95], v[132:135]\n" \
"v_mfma_f32_16x16x32_bf16 v[136:139], a[236:239], a[96:99], v[200:203]\n" \
"v_mfma_f32_16x16x32_bf16 v[136:139], a[196:199], a[100:103], v[136:139]\n" \
"v_mfma_f32_16x16x32_bf16 v[136:139], a[200:203], a[104:107], v[136:139]\n" \
"v_mfma_f32_16x16x32_bf16 v[136:139], a[204:207], a[108:111], v[136:139]\n" \
"v_mfma_f32_16x16x32_bf16 v[140:143], a[236:239], a[112:115], v[204:207]\n" \
"v_mfma_f32_16x16x32_bf16 v[140:143], a[196:199], a[116:119], v[140:143]\n" \
"v_mfma_f32_16x16x32_bf16 v[140:143], a[200:203], a[120:123], v[140:143]\n" \
"v_mfma_f32_16x16x32_bf16 v[140:143], a[204:207], a[124:127], v[140:143]\n" \
"s_nop 7\n" \
"s_nop 0\n" \
"v_cmp_u_f32_e64 s[44:45], v128, v132\n" \
"v_cmp_u_f32_e64 s[56:57], v136, v140\n" \
"s_or_b64 s[56:57], s[56:57], s[44:45]\n" \
"s_cbranch_scc0 Lbk1_%=\n" \
"s_sub_u32 s51, s51, 1\n" \
"s_cbranch_scc0 Lsq1_%=\n" \
"s_branch Lbk1_%=\n" \
"Lsp2_%=:\n" \
"s_movk_i32 s51, 0x1000\n" \
"s_cmp_gt_u32 s48, 1\n" \
"s_cselect_b32 s61, 1, 2\n" \
"s_mul_i32 s61, s61, 152064\n" \
"Lsq2_%=:\n" \
"buffer_load_dwordx4 a[0:3], %[oq0], %[srdz], s61 offen offset:0 sc1\n" \
"buffer_load_dwordx4 a[4:7], %[oq0], %[srdz], s61 offen offset:1024 sc1\n" \
"buffer_load_dwordx4 a[8:11], %[oq0], %[srdz], s61 offen offset:2048 sc1\n" \
"buffer_load_dwordx4 a[12:15], %[oq0], %[srdz], s61 offen offset:3072 sc1\n" \
"buffer_load_dwordx4 a[16:19], v208, %[srdz], s61 offen offset:0 sc1\n" \
"buffer_load_dwordx4 a[20:23], v208, %[srdz], s61 offen offset:1024 sc1\n" \
"buffer_load_dwordx4 a[24:27], v208, %[srdz], s61 offen offset:2048 sc1\n" \
"buffer_load_dwordx4 a[28:31], v208, %[srdz], s61 offen offset:3072 sc1\n" \
"buffer_load_dwordx4 a[32:35], v209, %[srdz], s61 offen offset:0 sc1\n" \
"buffer_load_dwordx4 a[36:39], v209, %[srdz], s61 offen offset:1024 sc1\n" \
"buffer_load_dwordx4 a[40:43], v209, %[srdz], s61 offen offset:2048 sc1\n" \
"buffer_load_dwordx4 a[44:47], v209, %[srdz], s61 offen offset:3072 sc1\n" \
"buffer_load_dwordx4 a[48:51], v210, %[srdz], s61 offen offset:0 sc1\n" \
"buffer_load_dwordx4 a[52:55], v210, %[srdz], s61 offen offset:1024 sc1\n" \
"buffer_load_dwordx4 a[56:59], v210, %[srdz], s61 offen offset:2048 sc1\n" \
"buffer_load_dwordx4 a[60:63], v210, %[srdz], s61 offen offset:3072 sc1\n" \
"s_waitcnt vmcnt(0)\n" \
"s_nop 0\n" \
"v_mfma_f32_16x16x32_bf16 v[192:195], a[196:199], a[0:3], v[128:131]\n" \
"v_mfma_f32_16x16x32_bf16 v[192:195], a[200:203], a[4:7], v[192:195]\n" \
"v_mfma_f32_16x16x32_bf16 v[192:195], a[204:207], a[8:11], v[192:195]\n" \
"v_mfma_f32_16x16x32_bf16 v[192:195], a[232:235], a[12:15], v[192:195]\n" \
"v_mfma_f32_16x16x32_bf16 v[196:199], a[196:199], a[16:19], v[132:135]\n" \
"v_mfma_f32_16x16x32_bf16 v[196:199], a[200:203], a[20:23], v[196:199]\n" \
"v_mfma_f32_16x16x32_bf16 v[196:199], a[204:207], a[24:27], v[196:199]\n" \
"v_mfma_f32_16x16x32_bf16 v[196:199], a[232:235], a[28:31], v[196:199]\n" \
"v_mfma_f32_16x16x32_bf16 v[200:203], a[196:199], a[32:35], v[136:139]\n" \
"v_mfma_f32_16x16x32_bf16 v[200:203], a[200:203], a[36:39], v[200:203]\n" \
"v_mfma_f32_16x16x32_bf16 v[200:203], a[204:207], a[40:43], v[200:203]\n" \
"v_mfma_f32_16x16x32_bf16 v[200:203], a[232:235], a[44:47], v[200:203]\n" \
"v_mfma_f32_16x16x32_bf16 v[204:207], a[196:199], a[48:51], v[140:143]\n" \
"v_mfma_f32_16x16x32_bf16 v[204:207], a[200:203], a[52:55], v[204:207]\n" \
"v_mfma_f32_16x16x32_bf16 v[204:207], a[204:207], a[56:59], v[204:207]\n" \
"v_mfma_f32_16x16x32_bf16 v[204:207], a[232:235], a[60:63], v[204:207]\n" \
"s_nop 7\n" \
"s_nop 0\n" \
"v_cmp_u_f32_e64 s[44:45], v192, v196\n" \
"v_cmp_u_f32_e64 s[56:57], v200, v204\n" \
"s_or_b64 s[56:57], s[56:57], s[44:45]\n" \
"s_cbranch_scc0 Lbk2_%=\n" \
"s_sub_u32 s51, s51, 1\n" \
"s_cbranch_scc0 Lsq2_%=\n" \
"s_branch Lbk2_%=\n" \
"Lsp3_%=:\n" \
"s_movk_i32 s51, 0x1000\n" \
"s_cmp_gt_u32 s48, 2\n" \
"s_cselect_b32 s61, 2, 3\n" \
"s_mul_i32 s61, s61, 152064\n" \
"Lsq3_%=:\n" \
"buffer_load_dwordx4 a[128:131], %[oq0], %[srdz], s61 offen offset:0 sc1\n" \
"buffer_load_dwordx4 a[132:135], %[oq0], %[srdz], s61 offen offset:1024 sc1\n" \
"buffer_load_dwordx4 a[136:139], %[oq0], %[srdz], s61 offen offset:2048 sc1\n" \
"buffer_load_dwordx4 a[140:143], %[oq0], %[srdz], s61 offen offset:3072 sc1\n" \
"buffer_load_dwordx4 a[144:147], v208, %[srdz], s61 offen offset:0 sc1\n" \
"buffer_load_dwordx4 a[148:151], v208, %[srdz], s61 offen offset:1024 sc1\n" \
"buffer_load_dwordx4 a[152:155], v208, %[srdz], s61 offen offset:2048 sc1\n" \
"buffer_load_dwordx4 a[156:159], v208, %[srdz], s61 offen offset:3072 sc1\n" \
"buffer_load_dwordx4 a[160:163], v209, %[srdz], s61 offen offset:0 sc1\n" \
"buffer_load_dwordx4 a[164:167], v209, %[srdz], s61 offen offset:1024 sc1\n" \
"buffer_load_dwordx4 a[168:171], v209, %[srdz], s61 offen offset:2048 sc1\n" \
"buffer_load_dwordx4 a[172:175], v209, %[srdz], s61 offen offset:3072 sc1\n" \
"buffer_load_dwordx4 a[176:179], v210, %[srdz], s61 offen offset:0 sc1\n" \
"buffer_load_dwordx4 a[180:183], v210, %[srdz], s61 offen offset:1024 sc1\n" \
"buffer_load_dwordx4 a[184:187], v210, %[srdz], s61 offen offset:2048 sc1\n" \
"buffer_load_dwordx4 a[188:191], v210, %[srdz], s61 offen offset:3072 sc1\n" \
"s_waitcnt vmcnt(0)\n" \
"s_nop 0\n" \
"v_mfma_f32_16x16x32_bf16 v[128:131], a[196:199], a[128:131], v[192:195]\n" \
"v_mfma_f32_16x16x32_bf16 v[128:131], a[200:203], a[132:135], v[128:131]\n" \
"v_mfma_f32_16x16x32_bf16 v[128:131], a[204:207], a[136:139], v[128:131]\n" \
"v_mfma_f32_16x16x32_bf16 v[128:131], a[224:227], a[140:143], v[128:131]\n" \
"v_mfma_f32_16x16x32_bf16 v[132:135], a[196:199], a[144:147], v[196:199]\n" \
"v_mfma_f32_16x16x32_bf16 v[132:135], a[200:203], a[148:151], v[132:135]\n" \
"v_mfma_f32_16x16x32_bf16 v[132:135], a[204:207], a[152:155], v[132:135]\n" \
"v_mfma_f32_16x16x32_bf16 v[132:135], a[224:227], a[156:159], v[132:135]\n" \
"v_mfma_f32_16x16x32_bf16 v[136:139], a[196:199], a[160:163], v[200:203]\n" \
"v_mfma_f32_16x16x32_bf16 v[136:139], a[200:203], a[164:167], v[136:139]\n" \
"v_mfma_f32_16x16x32_bf16 v[136:139], a[204:207], a[168:171], v[136:139]\n" \
"v_mfma_f32_16x16x32_bf16 v[136:139], a[224:227], a[172:175], v[136:139]\n" \
"v_mfma_f32_16x16x32_bf16 v[140:143], a[196:199], a[176:179], v[204:207]\n" \
"v_mfma_f32_16x16x32_bf16 v[140:143], a[200:203], a[180:183], v[140:143]\n" \
"v_mfma_f32_16x16x32_bf16 v[140:143], a[204:207], a[184:187], v[140:143]\n" \
"v_mfma_f32_16x16x32_bf16 v[140:143], a[224:227], a[188:191], v[140:143]\n" \
"s_nop 7\n" \
"s_nop 0\n" \
"v_cmp_u_f32_e64 s[44:45], v128, v132\n" \
"v_cmp_u_f32_e64 s[56:57], v136, v140\n" \
"s_or_b64 s[56:57], s[56:57], s[44:45]\n" \
"s_cbranch_scc0 Lbk3_%=\n" \
"s_sub_u32 s51, s51, 1\n" \
"s_cbranch_scc0 Lsq3_%=\n" \
"s_branch Lbk3_%=\n" \
"Lsp4_%=:\n" \
"s_movk_i32 s51, 0x1000\n" \
"s_cmp_gt_u32 s48, 3\n" \
"s_cselect_b32 s61, 3, 4\n" \
"s_mul_i32 s61, s61, 152064\n" \
"Lsq4_%=:\n" \
"buffer_load_dwordx4 a[64:67], %[oq0], %[srdz], s61 offen offset:0 sc1\n" \
"buffer_load_dwordx4 a[68:71], %[oq0], %[srdz], s61 offen offset:1024 sc1\n" \
"buffer_load_dwordx4 a[72:75], %[oq0], %[srdz], s61 offen offset:2048 sc1\n" \
"buffer_load_dwordx4 a[76:79], %[oq0], %[srdz], s61 offen offset:3072 sc1\n" \
"buffer_load_dwordx4 a[80:83], v208, %[srdz], s61 offen offset:0 sc1\n" \
"buffer_load_dwordx4 a[84:87], v208, %[srdz], s61 offen offset:1024 sc1\n" \
"buffer_load_dwordx4 a[88:91], v208, %[srdz], s61 offen offset:2048 sc1\n" \
"buffer_load_dwordx4 a[92:95], v208, %[srdz], s61 offen offset:3072 sc1\n" \
"buffer_load_dwordx4 a[96:99], v209, %[srdz], s61 offen offset:0 sc1\n" \
"buffer_load_dwordx4 a[100:103], v209, %[srdz], s61 offen offset:1024 sc1\n" \
"buffer_load_dwordx4 a[104:107], v209, %[srdz], s61 offen offset:2048 sc1\n" \
"buffer_load_dwordx4 a[108:111], v209, %[srdz], s61 offen offset:3072 sc1\n" \
"buffer_load_dwordx4 a[112:115], v210, %[srdz], s61 offen offset:0 sc1\n" \
"buffer_load_dwordx4 a[116:119], v210, %[srdz], s61 offen offset:1024 sc1\n" \
"buffer_load_dwordx4 a[120:123], v210, %[srdz], s61 offen offset:2048 sc1\n" \
"buffer_load_dwordx4 a[124:127], v210, %[srdz], s61 offen offset:3072 sc1\n" \
"s_waitcnt vmcnt(0)\n" \
"s_nop 0\n" \
"v_mfma_f32_16x16x32_bf16 v[192:195], a[196:199], a[64:67], v[128:131]\n" \
"v_mfma_f32_16x16x32_bf16 v[192:195], a[200:203], a[68:71], v[192:195]\n" \
"v_mfma_f32_16x16x32_bf16 v[192:195], a[204:207], a[72:75], v[192:195]\n" \
"v_mfma_f32_16x16x32_bf16 v[192:195], a[220:223], a[76:79], v[192:195]\n" \
"v_mfma_f32_16x16x32_bf16 v[196:199], a[196:199], a[80:83], v[132:135]\n" \
"v_mfma_f32_16x16x32_bf16 v[196:199], a[200:203], a[84:87], v[196:199]\n" \
"v_mfma_f32_16x16x32_bf16 v[196:199], a[204:207], a[88:91], v[196:199]\n" \
"v_mfma_f32_16x16x32_bf16 v[196:199], a[220:223], a[92:95], v[196:199]\n" \
"v_mfma_f32_16x16x32_bf16 v[200:203], a[196:199], a[96:99], v[136:139]\n" \
"v_mfma_f32_16x16x32_bf16 v[200:203], a[200:203], a[100:103], v[200:203]\n" \
"v_mfma_f32_16x16x32_bf16 v[200:203], a[204:207], a[104:107], v[200:203]\n" \
"v_mfma_f32_16x16x32_bf16 v[200:203], a[220:223], a[108:111], v[200:203]\n" \
"v_mfma_f32_16x16x32_bf16 v[204:207], a[196:199], a[112:115], v[140:143]\n" \
"v_mfma_f32_16x16x32_bf16 v[204:207], a[200:203], a[116:119], v[204:207]\n" \
"v_mfma_f32_16x16x32_bf16 v[204:207], a[204:207], a[120:123], v[204:207]\n" \
"v_mfma_f32_16x16x32_bf16 v[204:207], a[220:223], a[124:127], v[204:207]\n" \
"s_nop 7\n" \
"s_nop 0\n" \
"v_cmp_u_f32_e64 s[44:45], v192, v196\n" \
"v_cmp_u_f32_e64 s[56:57], v200, v204\n" \
"s_or_b64 s[56:57], s[56:57], s[44:45]\n" \
"s_cbranch_scc0 Lbk4_%=\n" \
"s_sub_u32 s51, s51, 1\n" \
"s_cbranch_scc0 Lsq4_%=\n" \
"s_branch Lbk4_%=\n" \
"Lsp5_%=:\n" \
"s_movk_i32 s51, 0x1000\n" \
"s_cmp_gt_u32 s48, 4\n" \
"s_cselect_b32 s61, 4, 5\n" \
"s_mul_i32 s61, s61, 152064\n" \
"Lsq5_%=:\n" \
"buffer_load_dwordx4 v[0:3], %[oq0], %[srdz], s61 offen offset:0 sc1\n" \
"buffer_load_dwordx4 v[8:11], %[oq0], %[srdz], s61 offen offset:1024 sc1\n" \
"buffer_load_dwordx4 v[16:19], %[oq0], %[srdz], s61 offen offset:2048 sc1\n" \
"buffer_load_dwordx4 v[24:27], %[oq0], %[srdz], s61 offen offset:3072 sc1\n" \
"buffer_load_dwordx4 v[32:35], v208, %[srdz], s61 offen offset:0 sc1\n" \
"buffer_load_dwordx4 v[40:43], v208, %[srdz], s61 offen offset:1024 sc1\n" \
"buffer_load_dwordx4 v[48:51], v208, %[srdz], s61 offen offset:2048 sc1\n" \
"buffer_load_dwordx4 v[56:59], v208, %[srdz], s61 offen offset:3072 sc1\n" \
"buffer_load_dwordx4 v[64:67], v209, %[srdz], s61 offen offset:0 sc1\n" \
"buffer_load_dwordx4 v[72:75], v209, %[srdz], s61 offen offset:1024 sc1\n" \
"buffer_load_dwordx4 v[80:83], v209, %[srdz], s61 offen offset:2048 sc1\n" \
"buffer_load_dwordx4 v[88:91], v209, %[srdz], s61 offen offset:3072 sc1\n" \
"buffer_load_dwordx4 v[96:99], v210, %[srdz], s61 offen offset:0 sc1\n" \
"buffer_load_dwordx4 v[104:107], v210, %[srdz], s61 offen offset:1024 sc1\n" \
"buffer_load_dwordx4 v[112:115], v210, %[srdz], s61 offen offset:2048 sc1\n" \
"buffer_load_dwordx4 v[120:123], v210, %[srdz], s61 offen offset:3072 sc1\n" \
"s_waitcnt vmcnt(0)\n" \
"s_nop 0\n" \
"v_mfma_f32_16x16x32_bf16 v[128:131], a[232:235], v[0:3], v[192:195]\n" \
"v_mfma_f32_16x16x32_bf16 v[128:131], a[236:239], v[8:11], v[128:131]\n" \
"v_mfma_f32_16x16x32_bf16 v[128:131], a[208:211], v[16:19], v[128:131]\n" \
"v_mfma_f32_16x16x32_bf16 v[128:131], a[228:231], v[24:27], v[128:131]\n" \
"v_mfma_f32_16x16x32_bf16 v[132:135], a[232:235], v[32:35], v[196:199]\n" \
"v_mfma_f32_16x16x32_bf16 v[132:135], a[236:239], v[40:43], v[132:135]\n" \
"v_mfma_f32_16x16x32_bf16 v[132:135], a[208:211], v[48:51], v[132:135]\n" \
"v_mfma_f32_16x16x32_bf16 v[132:135], a[228:231], v[56:59], v[132:135]\n" \
"v_mfma_f32_16x16x32_bf16 v[136:139], a[232:235], v[64:67], v[200:203]\n" \
"v_mfma_f32_16x16x32_bf16 v[136:139], a[236:239], v[72:75], v[136:139]\n" \
"v_mfma_f32_16x16x32_bf16 v[136:139], a[208:211], v[80:83], v[136:139]\n" \
"v_mfma_f32_16x16x32_bf16 v[136:139], a[228:231], v[88:91], v[136:139]\n" \
"v_mfma_f32_16x16x32_bf16 v[140:143], a[232:235], v[96:99], v[204:207]\n" \
"v_mfma_f32_16x16x32_bf16 v[140:143], a[236:239], v[104:107], v[140:143]\n" \
"v_mfma_f32_16x16x32_bf16 v[140:143], a[208:211], v[112:115], v[140:143]\n" \
"v_mfma_f32_16x16x32_bf16 v[140:143], a[228:231], v[120:123], v[140:143]\n" \
"s_nop 7\n" \
"s_nop 0\n" \
"v_cmp_u_f32_e64 s[44:45], v128, v132\n" \
"v_cmp_u_f32_e64 s[56:57], v136, v140\n" \
"s_or_b64 s[56:57], s[56:57], s[44:45]\n" \
"s_cbranch_scc0 Lbk5_%=\n" \
"s_sub_u32 s51, s51, 1\n" \
"s_cbranch_scc0 Lsq5_%=\n" \
"s_branch Lbk5_%=\n" \
"Lsp6_%=:\n" \
"s_movk_i32 s51, 0x1000\n" \
"s_cmp_gt_u32 s48, 5\n" \
"s_cselect_b32 s61, 5, 6\n" \
"s_mul_i32 s61, s61, 152064\n" \
"Lsq6_%=:\n" \
"buffer_load_dwordx4 a[0:3], %[oq0], %[srdz], s61 offen offset:0 sc1\n" \
"buffer_load_dwordx4 a[4:7], %[oq0], %[srdz], s61 offen offset:1024 sc1\n" \
"buffer_load_dwordx4 a[8:11], %[oq0], %[srdz], s61 offen offset:2048 sc1\n" \
"buffer_load_dwordx4 a[12:15], %[oq0], %[srdz], s61 offen offset:3072 sc1\n" \
"buffer_load_dwordx4 a[16:19], v208, %[srdz], s61 offen offset:0 sc1\n" \
"buffer_load_dwordx4 a[20:23], v208, %[srdz], s61 offen offset:1024 sc1\n" \
"buffer_load_dwordx4 a[24:27], v208, %[srdz], s61 offen offset:2048 sc1\n" \
"buffer_load_dwordx4 a[28:31], v208, %[srdz], s61 offen offset:3072 sc1\n" \
"buffer_load_dwordx4 a[32:35], v209, %[srdz], s61 offen offset:0 sc1\n" \
"buffer_load_dwordx4 a[36:39], v209, %[srdz], s61 offen offset:1024 sc1\n" \
"buffer_load_dwordx4 a[40:43], v209, %[srdz], s61 offen offset:2048 sc1\n" \
"buffer_load_dwordx4 a[44:47], v209, %[srdz], s61 offen offset:3072 sc1\n" \
"buffer_load_dwordx4 a[48:51], v210, %[srdz], s61 offen offset:0 sc1\n" \
"buffer_load_dwordx4 a[52:55], v210, %[srdz], s61 offen offset:1024 sc1\n" \
"buffer_load_dwordx4 a[56:59], v210, %[srdz], s61 offen offset:2048 sc1\n" \
"buffer_load_dwordx4 a[60:63], v210, %[srdz], s61 offen offset:3072 sc1\n" \
"s_waitcnt vmcnt(0)\n" \
"s_nop 0\n" \
"v_mfma_f32_16x16x32_bf16 v[192:195], a[196:199], a[0:3], v[128:131]\n" \
"v_mfma_f32_16x16x32_bf16 v[192:195], a[200:203], a[4:7], v[192:195]\n" \
"v_mfma_f32_16x16x32_bf16 v[192:195], a[204:207], a[8:11], v[192:195]\n" \
"v_mfma_f32_16x16x32_bf16 v[192:195], a[220:223], a[12:15], v[192:195]\n" \
"v_mfma_f32_16x16x32_bf16 v[196:199], a[196:199], a[16:19], v[132:135]\n" \
"v_mfma_f32_16x16x32_bf16 v[196:199], a[200:203], a[20:23], v[196:199]\n" \
"v_mfma_f32_16x16x32_bf16 v[196:199], a[204:207], a[24:27], v[196:199]\n" \
"v_mfma_f32_16x16x32_bf16 v[196:199], a[220:223], a[28:31], v[196:199]\n" \
"v_mfma_f32_16x16x32_bf16 v[200:203], a[196:199], a[32:35], v[136:139]\n" \
"v_mfma_f32_16x16x32_bf16 v[200:203], a[200:203], a[36:39], v[200:203]\n" \
"v_mfma_f32_16x16x32_bf16 v[200:203], a[204:207], a[40:43], v[200:203]\n" \
"v_mfma_f32_16x16x32_bf16 v[200:203], a[220:223], a[44:47], v[200:203]\n" \
"v_mfma_f32_16x16x32_bf16 v[204:207], a[196:199], a[48:51], v[140:143]\n" \
"v_mfma_f32_16x16x32_bf16 v[204:207], a[200:203], a[52:55], v[204:207]\n" \
"v_mfma_f32_16x16x32_bf16 v[204:207], a[204:207], a[56:59], v[204:207]\n" \
"v_mfma_f32_16x16x32_bf16 v[204:207], a[220:223], a[60:63], v[204:207]\n" \
"s_nop 7\n" \
"s_nop 0\n" \
"v_cmp_u_f32_e64 s[44:45], v192, v196\n" \
"v_cmp_u_f32_e64 s[56:57], v200, v204\n" \
"s_or_b64 s[56:57], s[56:57], s[44:45]\n" \
"s_cbranch_scc0 Lbk6_%=\n" \
"s_sub_u32 s51, s51, 1\n" \
"s_cbranch_scc0 Lsq6_%=\n" \
"s_branch Lbk6_%=\n" \
"Lsp7_%=:\n" \
"s_movk_i32 s51, 0x1000\n" \
"s_cmp_gt_u32 s48, 6\n" \
"s_cselect_b32 s61, 6, 7\n" \
"s_mul_i32 s61, s61, 152064\n" \
"Lsq7_%=:\n" \
"buffer_load_dwordx4 a[128:131], %[oq0], %[srdz], s61 offen offset:0 sc1\n" \
"buffer_load_dwordx4 a[132:135], %[oq0], %[srdz], s61 offen offset:1024 sc1\n" \
"buffer_load_dwordx4 a[136:139], %[oq0], %[srdz], s61 offen offset:2048 sc1\n" \
"buffer_load_dwordx4 a[140:143], %[oq0], %[srdz], s61 offen offset:3072 sc1\n" \
"buffer_load_dwordx4 a[144:147], v208, %[srdz], s61 offen offset:0 sc1\n" \
"buffer_load_dwordx4 a[148:151], v208, %[srdz], s61 offen offset:1024 sc1\n" \
"buffer_load_dwordx4 a[152:155], v208, %[srdz], s61 offen offset:2048 sc1\n" \
"buffer_load_dwordx4 a[156:159], v208, %[srdz], s61 offen offset:3072 sc1\n" \
"buffer_load_dwordx4 a[160:163], v209, %[srdz], s61 offen offset:0 sc1\n" \
"buffer_load_dwordx4 a[164:167], v209, %[srdz], s61 offen offset:1024 sc1\n" \
"buffer_load_dwordx4 a[168:171], v209, %[srdz], s61 offen offset:2048 sc1\n" \
"buffer_load_dwordx4 a[172:175], v209, %[srdz], s61 offen offset:3072 sc1\n" \
"buffer_load_dwordx4 a[176:179], v210, %[srdz], s61 offen offset:0 sc1\n" \
"buffer_load_dwordx4 a[180:183], v210, %[srdz], s61 offen offset:1024 sc1\n" \
"buffer_load_dwordx4 a[184:187], v210, %[srdz], s61 offen offset:2048 sc1\n" \
"buffer_load_dwordx4 a[188:191], v210, %[srdz], s61 offen offset:3072 sc1\n" \
"s_waitcnt vmcnt(0)\n" \
"s_nop 0\n" \
"v_mfma_f32_16x16x32_bf16 v[128:131], a[208:211], a[128:131], v[192:195]\n" \
"v_mfma_f32_16x16x32_bf16 v[128:131], a[224:227], a[132:135], v[128:131]\n" \
"v_mfma_f32_16x16x32_bf16 v[128:131], a[228:231], a[136:139], v[128:131]\n" \
"v_mfma_f32_16x16x32_bf16 v[128:131], a[236:239], a[140:143], v[128:131]\n" \
"v_mfma_f32_16x16x32_bf16 v[132:135], a[208:211], a[144:147], v[196:199]\n" \
"v_mfma_f32_16x16x32_bf16 v[132:135], a[224:227], a[148:151], v[132:135]\n" \
"v_mfma_f32_16x16x32_bf16 v[132:135], a[228:231], a[152:155], v[132:135]\n" \
"v_mfma_f32_16x16x32_bf16 v[132:135], a[236:239], a[156:159], v[132:135]\n" \
"v_mfma_f32_16x16x32_bf16 v[136:139], a[208:211], a[160:163], v[200:203]\n" \
"v_mfma_f32_16x16x32_bf16 v[136:139], a[224:227], a[164:167], v[136:139]\n" \
"v_mfma_f32_16x16x32_bf16 v[136:139], a[228:231], a[168:171], v[136:139]\n" \
"v_mfma_f32_16x16x32_bf16 v[136:139], a[236:239], a[172:175], v[136:139]\n" \
"v_mfma_f32_16x16x32_bf16 v[140:143], a[208:211], a[176:179], v[204:207]\n" \
"v_mfma_f32_16x16x32_bf16 v[140:143], a[224:227], a[180:183], v[140:143]\n" \
"v_mfma_f32_16x16x32_bf16 v[140:143], a[228:231], a[184:187], v[140:143]\n" \
"v_mfma_f32_16x16x32_bf16 v[140:143], a[236:239], a[188:191], v[140:143]\n" \
"s_nop 7\n" \
"s_nop 0\n" \
"v_cmp_u_f32_e64 s[44:45], v128, v132\n" \
"v_cmp_u_f32_e64 s[56:57], v136, v140\n" \
"s_or_b64 s[56:57], s[56:57], s[44:45]\n" \
"s_cbranch_scc0 Lbk7_%=\n" \
"s_sub_u32 s51, s51, 1\n" \
"s_cbranch_scc0 Lsq7_%=\n" \
"s_branch Lbk7_%=\n" \
"Lspk_%=:\n" \
"s_nop 0\n" \
"s_mov_b32 s51, 0x10000\n" \
"Lsqk_%=:\n" \
"buffer_load_dword v229, v226, %[srdz], s50 offen sc1\n" \
"s_waitcnt vmcnt(0)\n" \
"s_nop 0\n" \
"v_cmp_eq_u32_e64 s[56:57], 0, v229\n" \
"s_cmp_lg_u64 s[56:57], 0\n" \
"s_cbranch_scc0 Lbkk_%=\n" \
"s_sub_u32 s51, s51, 1\n" \
"s_cbranch_scc0 Lsqk_%=\n" \
"s_branch Lbkk_%=\n" \
"Lspp_%=:\n" \
"s_nop 0\n" \
"s_mov_b32 s51, 0x10000\n" \
"Lsqp_%=:\n" \
"buffer_load_dword v232, v226, %[srdz], s54 offen sc1\n" \
"s_waitcnt vmcnt(0)\n" \
"v_cmp_eq_u32_e32 vcc, 0, v232\n" \
"s_cbranch_vccz Lbkp_%=\n" \
"s_sub_u32 s51, s51, 1\n" \
"s_cbranch_scc0 Lsqp_%=\n" \
"s_branch Lbkp_%=\n" \
"Lpars_%=:\n" \
"s_mov_b32 s51, 0x10000\n" \
"s_lshl_b32 s61, %[wid], 10\n" \
"v_lshrrev_b32_e32 v235, 3, v224\n" \
"v_add_u32_e64 v235, s61, v235\n" \
"Lpar1_%=:\n" \
"s_mov_b32 s61, 262144\n" \
"s_nop 0\n" \
"s_nop 0\n" \
"buffer_load_dwordx4 v[192:195], v235, %[srdz], s61 offen offset:0 sc1\n" \
"s_mov_b32 s61, 266240\n" \
"s_nop 0\n" \
"s_nop 0\n" \
"buffer_load_dwordx4 v[196:199], v235, %[srdz], s61 offen offset:0 sc1\n" \
"s_mov_b32 s61, 270336\n" \
"s_nop 0\n" \
"s_nop 0\n" \
"buffer_load_dwordx4 v[200:203], v235, %[srdz], s61 offen offset:0 sc1\n" \
"s_mov_b32 s61, 274432\n" \
"s_nop 0\n" \
"s_nop 0\n" \
"buffer_load_dwordx4 v[204:207], v235, %[srdz], s61 offen offset:0 sc1\n" \
"s_waitcnt vmcnt(0)\n" \
"s_nop 0\n" \
"v_and_or_b32 v236, v192, v193, 0\n" \
"v_bitop3_b32 v236, v236, v194, v195 bitop3:0x80\n" \
"v_bitop3_b32 v236, v236, v196, v197 bitop3:0x80\n" \
"v_bitop3_b32 v236, v236, v198, v199 bitop3:0x80\n" \
"v_bitop3_b32 v236, v236, v200, v201 bitop3:0x80\n" \
"v_bitop3_b32 v236, v236, v202, v203 bitop3:0x80\n" \
"v_bitop3_b32 v236, v236, v204, v205 bitop3:0x80\n" \
"v_bitop3_b32 v236, v236, v206, v207 bitop3:0x80\n" \
"v_and_b32_e64 v236, 1, v236\n" \
"v_cmp_eq_u32_e64 s[56:57], 0, v236\n" \
"s_cmp_lg_u64 s[56:57], 0\n" \
"s_cbranch_scc0 Lparb_%=\n" \
"s_sub_u32 s51, s51, 1\n" \
"s_cbranch_scc0 Lpar1_%=\n" \
"s_branch Lparb_%=\n" \
"Ls0_%=:\n" \
"s_movk_i32 s51, 0x1000\n" \
"s_waitcnt vmcnt(0)\n" \
"Ls01_%=:\n" \
"s_nop 0\n" \
"s_mov_b32 s40, 606208\n" \
"buffer_load_dwordx4 a[176:179], v237, %[srdz], s40 offen sc1\n" \
"s_mov_b32 s40, 610304\n" \
"buffer_load_dwordx4 a[180:183], v237, %[srdz], s40 offen sc1\n" \
"s_mov_b32 s40, 614400\n" \
"buffer_load_dwordx4 a[184:187], v237, %[srdz], s40 offen sc1\n" \
"s_mov_b32 s40, 618496\n" \
"buffer_load_dwordx4 a[188:191], v237, %[srdz], s40 offen sc1\n" \
"s_waitcnt vmcnt(0)\n" \
"s_nop 0\n" \
"v_mfma_f32_16x16x32_bf16 v[216:219], a[176:179], v[0:3], 0\n" \
"v_mfma_f32_16x16x32_bf16 v[216:219], a[180:183], v[8:11], v[216:219]\n" \
"v_mfma_f32_16x16x32_bf16 v[216:219], a[184:187], v[16:19], v[216:219]\n" \
"v_mfma_f32_16x16x32_bf16 v[216:219], a[188:191], v[24:27], v[216:219]\n" \
"s_nop 7\n" \
"s_nop 0\n" \
"v_mov_b32_dpp v231, v216 quad_perm:[0,1,2,3] row_mask:0x1 bank_mask:0xf\n" \
"v_mfma_f32_16x16x32_bf16 v[232:235], a[176:179], v[32:35], 0\n" \
"v_mfma_f32_16x16x32_bf16 v[232:235], a[180:183], v[40:43], v[232:235]\n" \
"v_mfma_f32_16x16x32_bf16 v[232:235], a[184:187], v[48:51], v[232:235]\n" \
"v_mfma_f32_16x16x32_bf16 v[232:235], a[188:191], v[56:59], v[232:235]\n" \
"s_nop 7\n" \
"s_nop 0\n" \
"v_mov_b32_dpp v231, v232 quad_perm:[0,1,2,3] row_mask:0x2 bank_mask:0xf\n" \
"v_mfma_f32_16x16x32_bf16 v[216:219], a[176:179], v[64:67], 0\n" \
"v_mfma_f32_16x16x32_bf16 v[216:219], a[180:183], v[72:75], v[216:219]\n" \
"v_mfma_f32_16x16x32_bf16 v[216:219], a[184:187], v[80:83], v[216:219]\n" \
"v_mfma_f32_16x16x32_bf16 v[216:219], a[188:191], v[88:91], v[216:219]\n" \
"s_nop 7\n" \
"s_nop 0\n" \
"v_mov_b32_dpp v231, v216 quad_perm:[0,1,2,3] row_mask:0x4 bank_mask:0xf\n" \
"v_mfma_f32_16x16x32_bf16 v[232:235], a[176:179], v[96:99], 0\n" \
"v_mfma_f32_16x16x32_bf16 v[232:235], a[180:183], v[104:107], v[232:235]\n" \
"v_mfma_f32_16x16x32_bf16 v[232:235], a[184:187], v[112:115], v[232:235]\n" \
"v_mfma_f32_16x16x32_bf16 v[232:235], a[188:191], v[120:123], v[232:235]\n" \
"s_nop 7\n" \
"s_nop 0\n" \
"v_mov_b32_dpp v231, v232 quad_perm:[0,1,2,3] row_mask:0x8 bank_mask:0xf\n" \
"v_cmp_u_f32_e64 s[42:43], v231, v231\n" \
"s_cmp_lg_u64 s[42:43], 0\n" \
"s_cbranch_scc0 Ls0b_%=\n" \
"s_sub_u32 s51, s51, 1\n" \
"s_cbranch_scc0 Ls01_%=\n" \
"s_branch Ls0b_%=\n" \
"Ls1_%=:\n" \
"s_nop 0\n" \
"s_mov_b32 s51, 0x10000\n" \
"Ls11_%=:\n" \
"buffer_load_dword v235, v234, %[srdz], s63 offen sc1\n" \
"s_waitcnt vmcnt(0)\n" \
"v_and_b32_e32 v230, 1, v235\n" \
"v_cmp_eq_u32_e64 s[68:69], 0, v230\n" \
"s_cmp_lg_u64 s[68:69], 0\n" \
"s_cbranch_scc0 Ls1b_%=\n" \
"s_sub_u32 s51, s51, 1\n" \
"s_cbranch_scc0 Ls11_%=\n" \
"s_branch Ls1b_%=\n" \
"Lus_%=:\n" \
"s_nop 0\n" \
"s_mov_b32 s51, 0x10000\n" \
"Lus1_%=:\n" \
"s_lshl_b32 s62, %[wid], 10\n" \
"s_nop 0\n" \
"s_add_u32 s62, s62, 262144\n" \
"s_add_u32 s55, s62, 8192\n" \
"v_readlane_b32 s68, %[tlo], 20\n" \
"v_readlane_b32 s69, %[thi], 20\n" \
"s_lshl_b32 s63, %[wid], 9\n" \
"v_lshrrev_b32_e32 v230, 1, v235\n" \
"buffer_load_dwordx4 a[200:203], v235, %[srdz], s62 offen sc1\n" \
"v_add_u32_e64 v230, s63, v230\n" \
"buffer_load_dwordx4 a[204:207], v235, %[srdz], s55 offen sc1\n" \
"s_nop 1\n" \
"s_nop 0\n" \
"global_load_dwordx2 a[208:209], v230, s[68:69]\n" \
"s_waitcnt vmcnt(0)\n" \
"s_nop 0\n" \
"v_accvgpr_read_b32 v230, a208\n" \
"v_accvgpr_read_b32 v234, a204\n" \
"v_accvgpr_read_b32 v225, a205\n" \
"v_accvgpr_read_b32 v226, a200\n" \
"v_accvgpr_read_b32 v231, a201\n" \
"v_bitop3_b32 v229, v234, v225, -1 bitop3:0x80\n" \
"v_bitop3_b32 v229, v226, v231, v229 bitop3:0x80\n" \
"v_lshlrev_b32_e64 v236, 16, v230\n" \
"v_and_b32_e32 v230, 0xffff0000, v230\n" \
"v_mul_f32_e32 v234, v234, v236\n" \
"v_mul_f32_e32 v225, v225, v230\n" \
"v_mul_f32_e32 v226, v226, v234\n" \
"v_mul_f32_e32 v231, v231, v225\n" \
"v_cvt_pk_bf16_f32 v236, v234, v225\n" \
"v_cvt_pk_bf16_f32 v232, v226, v231\n" \
"v_accvgpr_read_b32 v230, a209\n" \
"v_accvgpr_read_b32 v234, a206\n" \
"v_accvgpr_read_b32 v225, a207\n" \
"v_accvgpr_read_b32 v226, a202\n" \
"v_accvgpr_read_b32 v231, a203\n" \
"v_bitop3_b32 v229, v234, v225, v229 bitop3:0x80\n" \
"v_bitop3_b32 v229, v226, v231, v229 bitop3:0x80\n" \
"v_lshlrev_b32_e64 v237, 16, v230\n" \
"v_and_b32_e32 v230, 0xffff0000, v230\n" \
"v_mul_f32_e32 v234, v234, v237\n" \
"v_mul_f32_e32 v225, v225, v230\n" \
"v_mul_f32_e32 v226, v226, v234\n" \
"v_mul_f32_e32 v231, v231, v225\n" \
"v_cvt_pk_bf16_f32 v237, v234, v225\n" \
"v_cvt_pk_bf16_f32 v233, v226, v231\n" \
"v_lshrrev_b32_e64 v230, 1, v235\n" \
"s_add_u32 s62, %[m0t], 6144\n" \
"v_add_u32_e32 v230, s62, v230\n" \
"v_and_b32_e32 v229, 1, v229\n" \
"v_cmp_eq_u32_e64 s[68:69], 0, v229\n" \
"ds_write_b64 v230, v[232:233] offset:0\n" \
"v_and_b32_e32 v230, 0x170, v235\n" \
"v_lshrrev_b32_e64 v234, 7, v224\n" \
"v_and_or_b32 v230, v234, 8, v230\n" \
"v_lshrrev_b32_e64 v234, 12, v224\n" \
"v_lshl_or_b32 v230, v234, 7, v230\n" \
"v_add_u32_e64 v230, s62, v230\n" \
"ds_write_b64 v230, v[236:237] offset:512\n" \
"s_cmp_lg_u64 s[68:69], 0\n" \
"s_cbranch_scc0 Lub_%=\n" \
"s_sub_u32 s51, s51, 1\n" \
"s_cbranch_scc0 Lus1_%=\n" \
"s_branch Lub_%=\n" \
"s_nop 0\n" \
"Lwalk_%=:\n" \
"s_mov_b64 exec, 0\n" \
"s_cmp_eq_u32 %[wid], 1\n" \
"s_cbranch_scc1 Lk0_%=\n" \
"s_cmp_eq_u32 %[wid], 2\n" \
"s_cbranch_scc1 Lk6_%=\n" \
"s_branch Lk12_%=\n" \
"adaln_kr:\n" \
"s_mov_b64 exec, -1\n" \
"s_branch Lgo_%=\n" \
"Lslow_%=:\n" \
"s_mov_b32 s52, 1\n" \
"s_mov_b32 s48, 0\n" \
"s_mov_b32 s51, 32\n" \
"s_nop 0\n" \
"s_mov_b32 s62, 0x80000000\n" \
"s_mov_b32 s63, 0x80000000\n" \
"s_waitcnt vmcnt(0)\n" \
"s_nop 0\n" \
"v_readlane_b32 s64, %[tlo], 9\n" \
"v_readlane_b32 s65, %[thi], 9\n" \
"s_mov_b32 s66, %[wbytes]\n" \
"v_mov_b32_e32 v229, %[vg0]\n" \
"s_mov_b32 s44, %[m0t]\n" \
"s_mov_b32 s45, 0\n" \
"v_xor_b32_e64 v208, 64, %[kb0]\n" \
"v_xor_b32_e32 v209, 0x80, %[kb0]\n" \
"v_xor_b32_e32 v210, 0xc0, %[kb0]\n" \
"v_xor_b32_e64 v212, 64, %[kd0]\n" \
"v_xor_b32_e32 v213, 0x80, %[kd0]\n" \
"v_xor_b32_e32 v214, 0xc0, %[kd0]\n" \
"v_xor_b32_e64 v211, 32, %[vvb]\n" \
"v_add_u32_e32 v215, 0xfffffc80, %[vd0]\n" \
"s_mov_b32 s66, 0\n" \
"s_nop 0\n" \
"v_mov_b32_e32 v225, 0xff800000\n" \
"v_mov_b32_e32 v226, 0xff800000\n" \
"v_mov_b32_e32 v227, 0xff800000\n" \
"v_mov_b32_e32 v228, 0xff800000\n" \
"s_waitcnt vmcnt(0) lgkmcnt(0)\n" \
"s_barrier\n" \
"Lsl_%=:\n" \
"s_mov_b32 m0, s40\n" \
"s_nop 0\n" \
"buffer_load_dwordx4 %[kd0], %[srdk], s48 offen offset:0 lds\n" \
"buffer_load_dwordx4 v212, %[srdk], s48 offen offset:1024 lds\n" \
"buffer_load_dwordx4 v213, %[srdk], s48 offen offset:2048 lds\n" \
"buffer_load_dwordx4 v214, %[srdk], s48 offen offset:3072 lds\n" \
"s_waitcnt vmcnt(0)\n" \
"s_barrier\n" \
"ds_read_b128 a[64:67], %[kb0] offset:0\n" \
"ds_read_b128 a[68:71], v208 offset:0\n" \
"ds_read_b128 a[72:75], v209 offset:0\n" \
"ds_read_b128 a[76:79], v210 offset:0\n" \
"ds_read_b128 a[80:83], %[kb0] offset:4096\n" \
"ds_read_b128 a[84:87], v208 offset:4096\n" \
"ds_read_b128 a[88:91], v209 offset:4096\n" \
"ds_read_b128 a[92:95], v210 offset:4096\n" \
"ds_read_b128 a[96:99], %[kb0] offset:8192\n" \
"ds_read_b128 a[100:103], v208 offset:8192\n" \
"ds_read_b128 a[104:107], v209 offset:8192\n" \
"ds_read_b128 a[108:111], v210 offset:8192\n" \
"ds_read_b128 a[112:115], %[kb0] offset:12288\n" \
"ds_read_b128 a[116:119], v208 offset:12288\n" \
"ds_read_b128 a[120:123], v209 offset:12288\n" \
"ds_read_b128 a[124:127], v210 offset:12288\n" \
"s_waitcnt lgkmcnt(0)\n" \
"s_barrier\n" \
"v_mfma_f32_16x16x32_bf16 v[128:131], a[64:67], a[0:3], 0\n" \
"v_mfma_f32_16x16x32_bf16 v[128:131], a[68:71], a[4:7], v[128:131]\n" \
"v_mfma_f32_16x16x32_bf16 v[128:131], a[72:75], a[8:11], v[128:131]\n" \
"v_mfma_f32_16x16x32_bf16 v[128:131], a[76:79], a[12:15], v[128:131]\n" \
"v_mfma_f32_16x16x32_bf16 v[132:135], a[80:83], a[0:3], 0\n" \
"v_mfma_f32_16x16x32_bf16 v[132:135], a[84:87], a[4:7], v[132:135]\n" \
"v_mfma_f32_16x16x32_bf16 v[132:135], a[88:91], a[8:11], v[132:135]\n" \
"v_mfma_f32_16x16x32_bf16 v[132:135], a[92:95], a[12:15], v[132:135]\n" \
"v_mfma_f32_16x16x32_bf16 v[136:139], a[64:67], a[16:19], 0\n" \
"v_mfma_f32_16x16x32_bf16 v[136:139], a[68:71], a[20:23], v[136:139]\n" \
"v_mfma_f32_16x16x32_bf16 v[136:139], a[72:75], a[24:27], v[136:139]\n" \
"v_mfma_f32_16x16x32_bf16 v[136:139], a[76:79], a[28:31], v[136:139]\n" \
"v_mfma_f32_16x16x32_bf16 v[140:143], a[80:83], a[16:19], 0\n" \
"v_mfma_f32_16x16x32_bf16 v[140:143], a[84:87], a[20:23], v[140:143]\n" \
"v_mfma_f32_16x16x32_bf16 v[140:143], a[88:91], a[24:27], v[140:143]\n" \
"v_mfma_f32_16x16x32_bf16 v[140:143], a[92:95], a[28:31], v[140:143]\n" \
"v_mfma_f32_16x16x32_bf16 v[144:147], a[64:67], a[32:35], 0\n" \
"v_mfma_f32_16x16x32_bf16 v[144:147], a[68:71], a[36:39], v[144:147]\n" \
"v_mfma_f32_16x16x32_bf16 v[144:147], a[72:75], a[40:43], v[144:147]\n" \
"v_mfma_f32_16x16x32_bf16 v[144:147], a[76:79], a[44:47], v[144:147]\n" \
"v_mfma_f32_16x16x32_bf16 v[148:151], a[80:83], a[32:35], 0\n" \
"v_mfma_f32_16x16x32_bf16 v[148:151], a[84:87], a[36:39], v[148:151]\n" \
"v_mfma_f32_16x16x32_bf16 v[148:151], a[88:91], a[40:43], v[148:151]\n" \
"v_mfma_f32_16x16x32_bf16 v[148:151], a[92:95], a[44:47], v[148:151]\n" \
"v_mfma_f32_16x16x32_bf16 v[152:155], a[64:67], a[48:51], 0\n" \
"v_mfma_f32_16x16x32_bf16 v[152:155], a[68:71], a[52:55], v[152:155]\n" \
"v_mfma_f32_16x16x32_bf16 v[152:155], a[72:75], a[56:59], v[152:155]\n" \
"v_mfma_f32_16x16x32_bf16 v[152:155], a[76:79], a[60:63], v[152:155]\n" \
"v_mfma_f32_16x16x32_bf16 v[156:159], a[80:83], a[48:51], 0\n" \
"v_mfma_f32_16x16x32_bf16 v[156:159], a[84:87], a[52:55], v[156:159]\n" \
"v_mfma_f32_16x16x32_bf16 v[156:159], a[88:91], a[56:59], v[156:159]\n" \
"v_mfma_f32_16x16x32_bf16 v[156:159], a[92:95], a[60:63], v[156:159]\n" \
"s_nop 7\n" \
"s_nop 0\n" \
"v_max3_f32 v230, v128, v129, v130\n" \
"v_max3_f32 v231, v131, v132, v133\n" \
"v_max3_f32 v230, v230, v134, v135\n" \
"v_max3_f32 v225, v230, v231, v225\n" \
"v_max3_f32 v230, v136, v137, v138\n" \
"v_max3_f32 v231, v139, v140, v141\n" \
"v_max3_f32 v230, v230, v142, v143\n" \
"v_max3_f32 v226, v230, v231, v226\n" \
"v_max3_f32 v230, v144, v145, v146\n" \
"v_max3_f32 v231, v147, v148, v149\n" \
"v_max3_f32 v230, v230, v150, v151\n" \
"v_max3_f32 v227, v230, v231, v227\n" \
"v_max3_f32 v230, v152, v153, v154\n" \
"v_max3_f32 v231, v155, v156, v157\n" \
"v_max3_f32 v230, v230, v158, v159\n" \
"v_max3_f32 v228, v230, v231, v228\n" \
"s_nop 3\n" \
"s_nop 0\n" \
"v_mfma_f32_16x16x32_bf16 v[128:131], a[96:99], a[0:3], 0\n" \
"v_mfma_f32_16x16x32_bf16 v[128:131], a[100:103], a[4:7], v[128:131]\n" \
"v_mfma_f32_16x16x32_bf16 v[128:131], a[104:107], a[8:11], v[128:131]\n" \
"v_mfma_f32_16x16x32_bf16 v[128:131], a[108:111], a[12:15], v[128:131]\n" \
"v_mfma_f32_16x16x32_bf16 v[132:135], a[112:115], a[0:3], 0\n" \
"v_mfma_f32_16x16x32_bf16 v[132:135], a[116:119], a[4:7], v[132:135]\n" \
"v_mfma_f32_16x16x32_bf16 v[132:135], a[120:123], a[8:11], v[132:135]\n" \
"v_mfma_f32_16x16x32_bf16 v[132:135], a[124:127], a[12:15], v[132:135]\n" \
"v_mfma_f32_16x16x32_bf16 v[136:139], a[96:99], a[16:19], 0\n" \
"v_mfma_f32_16x16x32_bf16 v[136:139], a[100:103], a[20:23], v[136:139]\n" \
"v_mfma_f32_16x16x32_bf16 v[136:139], a[104:107], a[24:27], v[136:139]\n" \
"v_mfma_f32_16x16x32_bf16 v[136:139], a[108:111], a[28:31], v[136:139]\n" \
"v_mfma_f32_16x16x32_bf16 v[140:143], a[112:115], a[16:19], 0\n" \
"v_mfma_f32_16x16x32_bf16 v[140:143], a[116:119], a[20:23], v[140:143]\n" \
"v_mfma_f32_16x16x32_bf16 v[140:143], a[120:123], a[24:27], v[140:143]\n" \
"v_mfma_f32_16x16x32_bf16 v[140:143], a[124:127], a[28:31], v[140:143]\n" \
"v_mfma_f32_16x16x32_bf16 v[144:147], a[96:99], a[32:35], 0\n" \
"v_mfma_f32_16x16x32_bf16 v[144:147], a[100:103], a[36:39], v[144:147]\n" \
"v_mfma_f32_16x16x32_bf16 v[144:147], a[104:107], a[40:43], v[144:147]\n" \
"v_mfma_f32_16x16x32_bf16 v[144:147], a[108:111], a[44:47], v[144:147]\n" \
"v_mfma_f32_16x16x32_bf16 v[148:151], a[112:115], a[32:35], 0\n" \
"v_mfma_f32_16x16x32_bf16 v[148:151], a[116:119], a[36:39], v[148:151]\n" \
"v_mfma_f32_16x16x32_bf16 v[148:151], a[120:123], a[40:43], v[148:151]\n" \
"v_mfma_f32_16x16x32_bf16 v[148:151], a[124:127], a[44:47], v[148:151]\n" \
"v_mfma_f32_16x16x32_bf16 v[152:155], a[96:99], a[48:51], 0\n" \
"v_mfma_f32_16x16x32_bf16 v[152:155], a[100:103], a[52:55], v[152:155]\n" \
"v_mfma_f32_16x16x32_bf16 v[152:155], a[104:107], a[56:59], v[152:155]\n" \
"v_mfma_f32_16x16x32_bf16 v[152:155], a[108:111], a[60:63], v[152:155]\n" \
"v_mfma_f32_16x16x32_bf16 v[156:159], a[112:115], a[48:51], 0\n" \
"v_mfma_f32_16x16x32_bf16 v[156:159], a[116:119], a[52:55], v[156:159]\n" \
"v_mfma_f32_16x16x32_bf16 v[156:159], a[120:123], a[56:59], v[156:159]\n" \
"v_mfma_f32_16x16x32_bf16 v[156:159], a[124:127], a[60:63], v[156:159]\n" \
"s_nop 7\n" \
"s_nop 0\n" \
"v_max3_f32 v230, v128, v129, v130\n" \
"v_max3_f32 v231, v131, v132, v133\n" \
"v_max3_f32 v230, v230, v134, v135\n" \
"v_max3_f32 v225, v230, v231, v225\n" \
"v_max3_f32 v230, v136, v137, v138\n" \
"v_max3_f32 v231, v139, v140, v141\n" \
"v_max3_f32 v230, v230, v142, v143\n" \
"v_max3_f32 v226, v230, v231, v226\n" \
"v_max3_f32 v230, v144, v145, v146\n" \
"v_max3_f32 v231, v147, v148, v149\n" \
"v_max3_f32 v230, v230, v150, v151\n" \
"v_max3_f32 v227, v230, v231, v227\n" \
"v_max3_f32 v230, v152, v153, v154\n" \
"v_max3_f32 v231, v155, v156, v157\n" \
"v_max3_f32 v230, v230, v158, v159\n" \
"v_max3_f32 v228, v230, v231, v228\n" \
"s_nop 3\n" \
"s_add_u32 s48, s48, s54\n" \
"s_sub_u32 s51, s51, 1\n" \
"s_cmp_lg_u32 s51, 0\n" \
"s_cbranch_scc1 Lsl_%=\n" \
"s_nop 0\n" \
"v_mov_b32_e32 v232, v225\n" \
"v_mov_b32_e32 v233, v226\n" \
"v_mov_b32_e32 v234, v227\n" \
"v_mov_b32_e32 v235, v228\n" \
"s_nop 0\n" \
"v_permlane16_swap_b32_e32 v225, v232\n" \
"v_permlane16_swap_b32_e32 v226, v233\n" \
"v_permlane16_swap_b32_e32 v227, v234\n" \
"v_permlane16_swap_b32_e32 v228, v235\n" \
"v_max_f32_e32 v225, v225, v232\n" \
"v_max_f32_e32 v226, v226, v233\n" \
"v_max_f32_e32 v227, v227, v234\n" \
"v_max_f32_e32 v228, v228, v235\n" \
"v_mov_b32_e32 v232, v225\n" \
"v_mov_b32_e32 v233, v226\n" \
"v_mov_b32_e32 v234, v227\n" \
"v_mov_b32_e32 v235, v228\n" \
"s_nop 0\n" \
"v_permlane32_swap_b32_e32 v225, v232\n" \
"v_permlane32_swap_b32_e32 v226, v233\n" \
"v_permlane32_swap_b32_e32 v227, v234\n" \
"v_permlane32_swap_b32_e32 v228, v235\n" \
"v_max_f32_e32 v225, v225, v232\n" \
"v_max_f32_e32 v226, v226, v233\n" \
"v_max_f32_e32 v227, v227, v234\n" \
"v_max_f32_e32 v228, v228, v235\n" \
"s_cmp_lg_u32 s69, 0\n" \
"s_cbranch_scc1 Lnewm_%=\n" \
"v_sub_f32_e32 v225, 0, v160\n" \
"v_sub_f32_e32 v226, 0, v164\n" \
"v_sub_f32_e64 v227, 0, v168\n" \
"v_sub_f32_e32 v228, 0, v172\n" \
"Lnewm_%=:\n" \
"s_mov_b32 s48, 0\n" \
"s_mov_b32 m0, s40\n" \
"s_nop 0\n" \
"buffer_load_dwordx4 %[kd0], %[srdk], s48 offen offset:0 lds\n" \
"buffer_load_dwordx4 v212, %[srdk], s48 offen offset:1024 lds\n" \
"buffer_load_dwordx4 v213, %[srdk], s48 offen offset:2048 lds\n" \
"buffer_load_dwordx4 v214, %[srdk], s48 offen offset:3072 lds\n" \
"s_mov_b32 s48, 16384\n" \
"s_mov_b32 m0, s41\n" \
"s_nop 0\n" \
"buffer_load_dwordx4 %[kd0], %[srdk], s48 offen offset:0 lds\n" \
"buffer_load_dwordx4 v212, %[srdk], s48 offen offset:1024 lds\n" \
"buffer_load_dwordx4 v213, %[srdk], s48 offen offset:2048 lds\n" \
"buffer_load_dwordx4 v214, %[srdk], s48 offen offset:3072 lds\n" \
"s_add_u32 m0, %[m0b], 65536\n" \
"s_nop 0\n" \
"s_nop 0\n" \
"buffer_load_dwordx4 %[vd0], %[srdv], s48 offen offset:0 lds\n" \
"buffer_load_dwordx4 v215, %[srdv], s48 offen offset:1024 lds\n" \
"buffer_load_dwordx4 %[vd0], %[srdv], s48 offen offset:2048 lds\n" \
"buffer_load_dwordx4 v215, %[srdv], s48 offen offset:3072 lds\n" \
"s_mov_b32 s48, 32768\n" \
"s_mov_b32 m0, s42\n" \
"s_nop 0\n" \
"buffer_load_dwordx4 %[kd0], %[srdk], s48 offen offset:0 lds\n" \
"buffer_load_dwordx4 v212, %[srdk], s48 offen offset:1024 lds\n" \
"buffer_load_dwordx4 v213, %[srdk], s48 offen offset:2048 lds\n" \
"buffer_load_dwordx4 v214, %[srdk], s48 offen offset:3072 lds\n" \
"s_add_u32 m0, %[m0b], 81920\n" \
"s_nop 0\n" \
"s_nop 0\n" \
"buffer_load_dwordx4 %[vd0], %[srdv], s48 offen offset:0 lds\n" \
"buffer_load_dwordx4 v215, %[srdv], s48 offen offset:1024 lds\n" \
"buffer_load_dwordx4 %[vd0], %[srdv], s48 offen offset:2048 lds\n" \
"buffer_load_dwordx4 v215, %[srdv], s48 offen offset:3072 lds\n" \
"s_mov_b32 s48, 49152\n" \
"s_mov_b32 m0, s43\n" \
"s_nop 0\n" \
"buffer_load_dwordx4 %[kd0], %[srdk], s48 offen offset:0 lds\n" \
"buffer_load_dwordx4 v212, %[srdk], s48 offen offset:1024 lds\n" \
"buffer_load_dwordx4 v213, %[srdk], s48 offen offset:2048 lds\n" \
"buffer_load_dwordx4 v214, %[srdk], s48 offen offset:3072 lds\n" \
"s_add_u32 m0, %[m0b], 98304\n" \
"s_nop 0\n" \
"s_nop 0\n" \
"buffer_load_dwordx4 %[vd0], %[srdv], s48 offen offset:0 lds\n" \
"buffer_load_dwordx4 v215, %[srdv], s48 offen offset:1024 lds\n" \
"buffer_load_dwordx4 %[vd0], %[srdv], s48 offen offset:2048 lds\n" \
"buffer_load_dwordx4 v215, %[srdv], s48 offen offset:3072 lds\n" \
"s_waitcnt vmcnt(0)\n" \
"s_barrier\n" \
"ds_read_b128 a[64:67], %[kb0] offset:0\n" \
"ds_read_b128 a[68:71], v208 offset:0\n" \
"ds_read_b128 a[72:75], v209 offset:0\n" \
"ds_read_b128 a[76:79], v210 offset:0\n" \
"ds_read_b128 a[80:83], %[kb0] offset:4096\n" \
"ds_read_b128 a[84:87], v208 offset:4096\n" \
"ds_read_b128 a[88:91], v209 offset:4096\n" \
"ds_read_b128 a[92:95], v210 offset:4096\n" \
"s_mov_b32 s48, 65536\n" \
"s_mov_b32 s60, 0\n" \
"s_waitcnt lgkmcnt(0)\n" \
"v_mfma_f32_16x16x32_bf16 v[128:131], a[64:67], a[0:3], 0\n" \
"ds_read_b128 a[96:99], %[kb0] offset:8192\n" \
"v_mfma_f32_16x16x32_bf16 v[128:131], a[68:71], a[4:7], v[128:131]\n" \
"ds_read_b128 a[100:103], v208 offset:8192\n" \
"v_mfma_f32_16x16x32_bf16 v[128:131], a[72:75], a[8:11], v[128:131]\n" \
"ds_read_b128 a[104:107], v209 offset:8192\n" \
"v_mfma_f32_16x16x32_bf16 v[128:131], a[76:79], a[12:15], v[128:131]\n" \
"ds_read_b128 a[108:111], v210 offset:8192\n" \
"v_mfma_f32_16x16x32_bf16 v[132:135], a[80:83], a[0:3], 0\n" \
"ds_read_b128 a[112:115], %[kb0] offset:12288\n" \
"v_mfma_f32_16x16x32_bf16 v[132:135], a[84:87], a[4:7], v[132:135]\n" \
"ds_read_b128 a[116:119], v208 offset:12288\n" \
"v_mfma_f32_16x16x32_bf16 v[132:135], a[88:91], a[8:11], v[132:135]\n" \
"ds_read_b128 a[120:123], v209 offset:12288\n" \
"v_mfma_f32_16x16x32_bf16 v[132:135], a[92:95], a[12:15], v[132:135]\n" \
"ds_read_b128 a[124:127], v210 offset:12288\n" \
"v_mfma_f32_16x16x32_bf16 v[136:139], a[64:67], a[16:19], 0\n" \
"ds_read_b64_tr_b16 a[128:129], %[vvb] offset:0\n" \
"v_mfma_f32_16x16x32_bf16 v[136:139], a[68:71], a[20:23], v[136:139]\n" \
"ds_read_b64_tr_b16 a[130:131], %[vvb] offset:4096\n" \
"v_mfma_f32_16x16x32_bf16 v[136:139], a[72:75], a[24:27], v[136:139]\n" \
"ds_read_b64_tr_b16 a[132:133], v211 offset:0\n" \
"v_mfma_f32_16x16x32_bf16 v[136:139], a[76:79], a[28:31], v[136:139]\n" \
"ds_read_b64_tr_b16 a[134:135], v211 offset:4096\n" \
"v_mfma_f32_16x16x32_bf16 v[140:143], a[80:83], a[16:19], 0\n" \
"ds_read_b64_tr_b16 a[136:137], %[vvb] offset:512\n" \
"v_mfma_f32_16x16x32_bf16 v[140:143], a[84:87], a[20:23], v[140:143]\n" \
"ds_read_b64_tr_b16 a[138:139], %[vvb] offset:4608\n" \
"v_mfma_f32_16x16x32_bf16 v[140:143], a[88:91], a[24:27], v[140:143]\n" \
"ds_read_b64_tr_b16 a[140:141], v211 offset:512\n" \
"v_mfma_f32_16x16x32_bf16 v[140:143], a[92:95], a[28:31], v[140:143]\n" \
"ds_read_b64_tr_b16 a[142:143], v211 offset:4608\n" \
"v_mfma_f32_16x16x32_bf16 v[144:147], a[64:67], a[32:35], 0\n" \
"ds_read_b64_tr_b16 a[144:145], %[vvb] offset:1024\n" \
"v_mfma_f32_16x16x32_bf16 v[144:147], a[68:71], a[36:39], v[144:147]\n" \
"ds_read_b64_tr_b16 a[146:147], %[vvb] offset:5120\n" \
"v_mfma_f32_16x16x32_bf16 v[144:147], a[72:75], a[40:43], v[144:147]\n" \
"ds_read_b64_tr_b16 a[148:149], v211 offset:1024\n" \
"v_mfma_f32_16x16x32_bf16 v[144:147], a[76:79], a[44:47], v[144:147]\n" \
"ds_read_b64_tr_b16 a[150:151], v211 offset:5120\n" \
"v_mfma_f32_16x16x32_bf16 v[148:151], a[80:83], a[32:35], 0\n" \
"ds_read_b64_tr_b16 a[152:153], %[vvb] offset:1536\n" \
"v_mfma_f32_16x16x32_bf16 v[148:151], a[84:87], a[36:39], v[148:151]\n" \
"ds_read_b64_tr_b16 a[154:155], %[vvb] offset:5632\n" \
"v_mfma_f32_16x16x32_bf16 v[148:151], a[88:91], a[40:43], v[148:151]\n" \
"ds_read_b64_tr_b16 a[156:157], v211 offset:1536\n" \
"v_mfma_f32_16x16x32_bf16 v[148:151], a[92:95], a[44:47], v[148:151]\n" \
"ds_read_b64_tr_b16 a[158:159], v211 offset:5632\n" \
"v_mfma_f32_16x16x32_bf16 v[152:155], a[64:67], a[48:51], 0\n" \
"v_mfma_f32_16x16x32_bf16 v[152:155], a[68:71], a[52:55], v[152:155]\n" \
"v_mfma_f32_16x16x32_bf16 v[152:155], a[72:75], a[56:59], v[152:155]\n" \
"v_mfma_f32_16x16x32_bf16 v[152:155], a[76:79], a[60:63], v[152:155]\n" \
"v_mfma_f32_16x16x32_bf16 v[156:159], a[80:83], a[48:51], 0\n" \
"v_mfma_f32_16x16x32_bf16 v[156:159], a[84:87], a[52:55], v[156:159]\n" \
"v_mfma_f32_16x16x32_bf16 v[156:159], a[88:91], a[56:59], v[156:159]\n" \
"v_mfma_f32_16x16x32_bf16 v[156:159], a[92:95], a[60:63], v[156:159]\n" \
"s_branch Lhavem_%=\n" \
"s_nop 0\n" \
"Lend_%=:\n" \
"s_waitcnt lgkmcnt(0)\n" \
"s_nop 0\n" \
""

#define SRD_WORD3 0x00020000
DI u32x4 ld16_sc1(const void* p) { u32x4 v; asm volatile("global_load_dwordx4 %0, %1, off sc1" : "=v"(v) : "v"(p) : "memory"); return v; }
DI unsigned ord_key(float r, unsigned row) {
  const unsigned b = __float_as_uint(r), o = (b & 0x80000000u) ? ~b : (b | 0x80000000u);
  return max(o & 0xfffff800u, 0x800u) | (2047u - row);
}
DI void top2_merge(unsigned& a1, unsigned& a2, unsigned b1, unsigned b2) {
  const unsigned n1 = max(a1, b1), n2 = max(min(a1, b1), max(a2, b2)); a1 = n1; a2 = n2;
}
template <int CTRL> DI void top2_dpp(unsigned& k1, unsigned& k2) {
  const unsigned p1 = __builtin_amdgcn_update_dpp(0u, k1, CTRL, 0xf, 0xf, true), p2 = __builtin_amdgcn_update_dpp(0u, k2, CTRL, 0xf, 0xf, true); top2_merge(k1, k2, p1, p2);
}
DI float row_sum(float v) {
  v = dpp_add<DPP_XOR1>(v); v = dpp_add<DPP_XOR2>(v); v = dpp_add<DPP_ROR8>(v);
  v = swap16_add(v, v); return swap32_add(v, v);
}
DI unsigned h2_pair(float x0, float x1, float m, float rden, unsigned c1w, unsigned sbw) { return ln_pair(x0, x1, m, rden, lo16(c1w), hi16(c1w), lo16(sbw), hi16(sbw)); }
#define PAGE(n, nx) asm volatile("s_branch adaln_pe" #n "\n.p2align 12\nadaln_pg" #n ":\n\ts_cbranch_execz " nx "\nadaln_pe" #n ":")
constexpr int MOE_WGS = DFF / 16;
constexpr unsigned RETRY_CAP = 4096;
#define ASLEEP 2
#define KLOOK 4
#define YSPLIT 8
#define FSLEEP 14
#define KSLEEP 3
#define YEARLY 8
#define FREST 0
#define KL0 1u
#define KLS 2u
#define KST 2u
#define KCHK 8
#define KPAUSE 2
#define KEY_SEL b

#define WBYTES (moe ? DM * DFF * 2 : 0)
namespace fa {
constexpr unsigned LDS_BYTES = 163840;
constexpr float QSCALE = 0.12751743082f;
struct Args { const u16 *x, *wmod, *wq, *kc, *vc, *wo, *wg, *wu, *wd, *c, *wr; char* ws; u16* out;
};
__global__ __launch_bounds__(256, 1) void fattn_k(const Args p) {
  extern __shared__ __attribute__((aligned(16))) char lds[];
  const unsigned wid = __builtin_amdgcn_readfirstlane(threadIdx.x >> 6);
  const unsigned b = blockIdx.x, idx = b >> 3, kg = b & 3u, m = idx & 7u, hd = 8u * kg + m, y = (b & 4u) + (idx >> 3);
  const unsigned ldsb = (unsigned)(size_t)lds;
  const unsigned m0b = ldsb + 4096u * wid, m0t = ldsb + 131072u + 8192u * wid;
  const __amdgpu_buffer_rsrc_t srdz = __builtin_amdgcn_make_buffer_rsrc((void*)p.ws, (short)0, (int)WS_END, SRD_WORD3);
  const bool moe = b < MOE_WGS;
  const unsigned sl = 4u * ((b & 7u) * 7u + (idx >> 2)) + (idx & 3u), yp2 = (y & 1u) * 2u;
  const unsigned sin = (unsigned)WS_XIN + ((y * 32u + hd) * 4u + wid) * BOX;
  u32x2 xq[8]; unsigned fl, fl2, got, got2; unsigned long long tag;
  unsigned k1, k2; unsigned long long lat;
  const unsigned sxf = (unsigned)WS_XFL + (y * 4u + wid) * 512u, sxl = (unsigned)WS_XFL + (y * 4u + kg) * 512u;
#define FQ(i)
#define TQ(i)
  {
    const unsigned tid = threadIdx.x, lane = tid & 63u, l16 = lane & 15u, g = lane >> 4, hi = lane >> 5, r8 = (lane >> 2) & 7u;
    unsigned kb0 = ldsb + 256u * l16 + 16u * (g ^ l16);
    unsigned vvb = ldsb + 65536u + 2048u * hi + 288u * (g & 1u) + 64u * ((lane >> 2) & 3u) + 8u * (lane & 3u);
    unsigned vwb = ldsb + 65536u + 2048u * g + 64u * ((lane >> 2) & 3u) + 16u * (lane & 3u);
    unsigned kd0 = 4096u * wid + 256u * g + 16u * (l16 ^ g);
    unsigned vd0 = 4096u * wid + 256u * r8 + 64u * hi + 16u * ((lane & 3u) ^ (2u * (r8 >> 2))) + 1024u;
    unsigned wd0 = 8192u * (16u * wid + r8) + 64u * hi + 16u * (lane & 3u) + 4096u;
    unsigned hq0 = (unsigned)WS_H + (16u * y + 4u * wid) * 32768u + 16u * lane;
    unsigned oq0 = (unsigned)WS_XIN + ((y * 32u + kg * 8u) * 4u + wid) * BOX + 20480u + 16u * lane;
    const unsigned flag = ldsb + 49152u + 4u * wid;
    const __amdgpu_buffer_rsrc_t srdw = __builtin_amdgcn_make_buffer_rsrc((void*)((const char*)p.wq + 256u * hd - 4096), (short)0, (int)(DM * DQ * 2 - 256u * hd + 4096u), SRD_WORD3);
    const __amdgpu_buffer_rsrc_t srdk = __builtin_amdgcn_make_buffer_rsrc((void*)((const char*)p.kc + (size_t)hd * (SEQ * 256)), (short)0, SEQ * 256, SRD_WORD3);
    const __amdgpu_buffer_rsrc_t srdv = __builtin_amdgcn_make_buffer_rsrc((void*)((const char*)p.vc + (size_t)hd * (SEQ * 256) - 17408), (short)0, SEQ * 256 + 17408, SRD_WORD3);
    unsigned long long pct; unsigned clen;
    asm volatile("s_getpc_b64 %0\n\ts_mov_b32 %1, adaln_text_end@rel32@lo+4" : "=s"(pct), "=s"(clen));
    unsigned vct = min(((idx * 4u + wid) * 6u + min(lane, 5u)) * 128u, (clen - 4u) & ~3u);
#define XNT " nt"
#define WNT " nt"
      const unsigned gr = 128u * (b & 7u) + 4u * (tid >> 3), gc = 64u * (b >> 3) + 8u * (tid & 7u), gran0 = b;
      u32x4 w[4], xs[4]; u32x2 ep, cv;
#define XV(i) ({ u32x4 v_; asm volatile("v_accvgpr_read_b32 %0, a[%4]\n\tv_accvgpr_read_b32 %1, a[%4 + 1]\n\tv_accvgpr_read_b32 %2, a[%4 + 2]\n\tv_accvgpr_read_b32 %3, a[%4 + 3]" : "=v"(v_[0]), "=v"(v_[1]), "=v"(v_[2]), "=v"(v_[3]) : "n"(200 + 4 * (i))); v_; })
      asm volatile("global_load_dwordx2 %0, %1, %2" : "=v"(cv) : "v"(gr * 2u), "s"(p.c) : "memory");
      #pragma unroll
      for (int j = 0; j < 4; ++j) asm volatile("global_load_dwordx4 %0, %1, %2" WNT : "=v"(w[j]) : "v"(((gr + j) * NMOD + gc) * 2u), "s"(p.wmod) : "memory");
      #pragma unroll
      for (int q = 0; q < 4; ++q) asm volatile("global_load_dwordx4 %0, %1, %2 offset:%3" XNT : "=v"(xs[q]) : "v"((8u * (y * 32u + hd) + 2u * wid) * 2048u + 16u * lane), "s"(p.x), "n"(1024 * q) : "memory");
      asm volatile("global_load_dwordx2 %0, %1, %2 sc1" : "=v"(ep) : "v"(0u), "s"(p.ws + WS_EP) : "memory");
      {
        const unsigned zo = b * Z_SHARE + tid * 16u - (unsigned)(WS_U - WS_Z);
        u32x4 zv;
        #pragma unroll
        for (int e = 0; e < 4; ++e) { const unsigned h = (zo + 4u * e + 1u) * 2654435761u; zv[e] = zo >= 16384u ? 0u : NORM && (zo & 2048u) ? 0x3f803f80u | ((h ^ (h >> 15)) * 0x2c1b3c6du & 0x80008000u) : ~0u; }
        asm volatile("buffer_store_dwordx4 %0, %1, %2, 0 offen sc1\n\ts_nop 1" :: "v"(zv), "v"(tid < Z_SHARE / 16u ? (unsigned)WS_Z + b * Z_SHARE + tid * 16u : 0x80000000u), "s"(srdz) : "memory");
      }
      FQ(6);
      asm volatile("global_load_dword a255, %0, %1" :: "v"(vct), "s"(pct) : "memory");
#define PRET 0
      asm volatile("s_mov_b64 exec, 0\n\ts_cmp_eq_u32 %0, 1\n\ts_cbranch_scc1 adaln_pg5\n\ts_cmp_eq_u32 %0, 2\n\ts_cbranch_scc1 adaln_pg6\n\ts_cmp_eq_u32 %0, 3\n\ts_cbranch_scc1 adaln_pg7\nadaln_fr:\n\ts_mov_b64 exec, -1" :: "s"(wid) : "scc");
      FQ(7);
      float* red = (float*)(lds + 131072 + 4096);
      auto silu = [](float v) { return rbf(v * __builtin_amdgcn_rcpf(1.f + __builtin_amdgcn_exp2f(v * -1.44269504089f))); };
      asm volatile("s_waitcnt vmcnt(11)" : "+v"(cv) :: "memory");
      unsigned sp[2] = {cvtpk(silu(lo16(cv[0])), silu(hi16(cv[0]))), cvtpk(silu(lo16(cv[1])), silu(hi16(cv[1])))};
      unsigned gsa = lane < 4u ? (unsigned)WS_RST + ((8u * (y * 32u + hd) + 2u * wid) * 2u + lane) * 8u : 0x80000000u;
      unsigned gma = wid ? 0x80000000u : (unsigned)WS_MOD + (gran0 * 64u + lane) * 8u, gra = ldsb + 131072u + 4096u + lane * 4u;
      asm volatile("s_waitcnt vmcnt(7)" : "+v"(sp[0]), "+v"(sp[1]), "+v"(gsa), "+v"(gma), "+v"(gra), "+v"(w[0]), "+v"(w[1]), "+v"(w[2]), "+v"(w[3]) :: "memory");
      FQ(4);
      const unsigned rho = 0;
      unsigned tq0 = (idx * 8u + wid * 2u + ((lane >> 3) & 1u)) * 8192u + (b & 4u) * 256u + (lane & 7u) * 128u + 4096u - 256u * m;
#define WQT 0
#define KVT2 0
#define WQT2 4
#define TNT ""
      asm volatile("buffer_load_dword a255, %0, %1, 0 offen" TNT :: "v"(tq0), "s"(srdw) : "memory");
      {
        float acc[8];
        #pragma unroll
        for (int e = 0; e < 8; ++e) acc[e] = 0.f;
        #pragma unroll
        for (int j = 0; j < 2; ++j) {
          #pragma unroll
          for (int e = 0; e < 8; ++e) acc[e] = __builtin_amdgcn_fdot2_f32_bf16(__builtin_bit_cast(bf16x2, __builtin_amdgcn_perm(w[2 * j + 1][e >> 1], w[2 * j][e >> 1], (e & 1) ? 0x07060302u : 0x05040100u)), __builtin_bit_cast(bf16x2, sp[j]), acc[e], false);
        }
        float t[4], u[2];
        #pragma unroll
        for (int e = 0; e < 4; ++e) t[e] = swap32_add(acc[e], acc[e + 4]);
        #pragma unroll
        for (int e = 0; e < 2; ++e) u[e] = dpp_add<DPP_ROR8>(swap16_add(t[e], t[e + 2]));
        if ((lane & 8u) == 0) *(f32x2*)&red[wid * 64u + 8u * (tid & 7u) + 2u * g] = f32x2{u[0], u[1]};
      }
      unsigned gst, vx0r, vx1r;
      auto rowstats = [&]() __attribute__((always_inline)) {
      float stv4;
      {
        float t4[4] = {0.f, 0.f, 0.f, 0.f};
        #pragma unroll
        for (int e = 0; e < 16; ++e) {
          const unsigned xw = xs[e >> 2][e & 3]; const bf16x2 v = __builtin_bit_cast(bf16x2, xw);
          t4[2 * (e >> 3)] = __builtin_amdgcn_fdot2_f32_bf16(v, __builtin_bit_cast(bf16x2, 0x3f803f80u), t4[2 * (e >> 3)], false);
          t4[2 * (e >> 3) + 1] = __builtin_amdgcn_fdot2_f32_bf16(v, v, t4[2 * (e >> 3) + 1], false);
        }
        float u = swap16_add(swap32_add(t4[0], t4[2]), swap32_add(t4[1], t4[3]));
        u = dpp_add<DPP_XOR1>(u); u = dpp_add<DPP_XOR2>(u); u = dpp_add<DPP_ROR4>(u); u = dpp_add<DPP_ROR8>(u);
        const f32x2 mean = f32x2{__uint_as_float(__builtin_amdgcn_readlane(__float_as_uint(u), 0)), __uint_as_float(__builtin_amdgcn_readlane(__float_as_uint(u), 32))} * (1.f / DM);
        f32x2 v2 = f32x2{__uint_as_float(__builtin_amdgcn_readlane(__float_as_uint(u), 16)), __uint_as_float(__builtin_amdgcn_readlane(__float_as_uint(u), 48))} * (1.f / DM) - mean * mean;
        if (__builtin_expect(!(mean[0] * mean[0] <= v2[0] && mean[1] * mean[1] <= v2[1]), 0)) {
          f32x2 sq = {0.f, 0.f};
          #pragma unroll
          for (int e = 0; e < 8; ++e) {
            const f32x2 d0 = f32x2{lo16(xs[e >> 2][e & 3]), lo16(xs[2 + (e >> 2)][e & 3])} - mean, d1 = f32x2{hi16(xs[e >> 2][e & 3]), hi16(xs[2 + (e >> 2)][e & 3])} - mean;
            sq = __builtin_elementwise_fma(d1, d1, __builtin_elementwise_fma(d0, d0, sq));
          }
          v2 = f32x2{wave_sum(sq[0]), wave_sum(sq[1])} * (1.f / DM);
        }
        asm("s_nop 1\n\tv_readfirstlane_b32 %0, %2\n\tv_readfirstlane_b32 %1, %3" : "=&s"(vx0r), "=&s"(vx1r) : "v"(v2[0] + 1e-5f), "v"(v2[1] + 1e-5f));
        const float rs_ = __builtin_amdgcn_rcpf(rbf(__builtin_amdgcn_sqrtf(rbf(rbf((lane & 2u) ? v2[1] : v2[0]) + 1e-5f))));
        stv4 = rs_ * ((lane & 1u) ? 1.f : -rbf((lane & 2u) ? mean[1] : mean[0]));
      }
      gst = (__float_as_uint(stv4) + 0x80u) & 0xffffff00u; };
#define STP 0
      if (STP == 0) { asm volatile("s_waitcnt vmcnt(4)" : "+v"(xs[0]), "+v"(xs[1]), "+v"(xs[2]), "+v"(xs[3]) :: "memory"); rowstats(); asm volatile("" : "+v"(gst)); }
      asm volatile("s_waitcnt vmcnt(%5) lgkmcnt(0)\n\ts_barrier" : "+v"(xs[0]), "+v"(xs[1]), "+v"(xs[2]), "+v"(xs[3]), "+v"(ep) : "n"(PRET ? 3 : 2) : "memory");

      tag = (((unsigned long long)__builtin_amdgcn_readfirstlane(ep[1]) << 32) | __builtin_amdgcn_readfirstlane(ep[0])) + 1ull;
      const unsigned tl = (unsigned)tag & 0xffu, th = (unsigned)(tag >> 8);
      auto gran = [&](float v) { return ((unsigned long long)th << 32) | (((__float_as_uint(v) + 0x80u) & 0xffffff00u) | tl); };
      {
        float r0, r1, r2, r3;
        if (STP == 0) asm volatile("buffer_store_dwordx2 %0, %1, %2, 0 offen sc1" :: "v"(u32x2{gst | tl, th}), "v"(gsa), "s"(srdz) : "memory");
        asm volatile("ds_read_b32 %0, %4\n\tds_read_b32 %1, %4 offset:256\n\tds_read_b32 %2, %4 offset:512\n\tds_read_b32 %3, %4 offset:768\n\ts_waitcnt lgkmcnt(0)"
                     : "=&v"(r0), "=&v"(r1), "=&v"(r2), "=&v"(r3) : "v"(gra) : "memory");
        asm volatile("buffer_store_dwordx2 %0, %1, %2, 0 offen sc1\n\ts_nop 0" :: "v"(u32x2{((__float_as_uint((r0 + r1) + (r2 + r3)) + 0x80u) & 0xffffff00u) | tl, th}), "v"(gma), "s"(srdz) : "memory");
        if (STP == 1) { rowstats(); asm volatile("buffer_store_dwordx2 %0, %1, %2, 0 offen sc1\n\ts_nop 0" :: "v"(u32x2{gst | tl, th}), "v"(gsa), "s"(srdz) : "memory"); }
      }
      FQ(5);
#define SWQ 0
#define SWQ0 0x400000
      const unsigned rb0 = 16u * y + 4u * (hd >> 3), ch = 16u * (hd & 7u) + (tid >> 4);
      asm volatile("global_load_dwordx2 a[216:217], %0, %1" :: "v"(tid * 8u), "s"(p.c) : "memory");
      #pragma unroll
      for (int i = 0; i < 4; ++i) asm volatile("global_load_dwordx4 a[%2:%2 + 3], %0, %1" :: "v"(((rb0 + i) * 16u + l16) * 2048u + ch * 16u), "s"(p.x), "n"(200 + 4 * i) : "memory");
      PAGE(5, "adaln_fr");
      unsigned lane2 = lane; asm volatile("" : "+v"(lane2));
#define lane lane2
    unsigned wo0 = (32u * wid + r8) * 2048u + m * 256u + 64u * hi + 16u * ((lane & 3u) ^ (2u * (r8 >> 2)));
    const __amdgpu_buffer_rsrc_t srdt = __builtin_amdgcn_make_buffer_rsrc((void*)((const char*)p.wo + (size_t)kg * (DM * DM * 2)), (short)0, DM * DM * 2, SRD_WORD3);
    const __amdgpu_buffer_rsrc_t srdp = __builtin_amdgcn_make_buffer_rsrc((void*)((const char*)(y < 4u ? p.kc : p.vc) + (size_t)hd * (SEQ * 256) + (y & 3u) * 4096u), (short)0, (int)(KVPAR && (b & 1u) ? 0u : (KVPAR ? 0x400000u : 0u) + SEQ * 256 - (y & 3u) * 4096u), SRD_WORD3);
    unsigned vg0 = (256u * wid + (lane >> 1)) * (DFF * 2u) + sl * 32u + 16u * (lane & 1u);
    unsigned vdn = (16u * sl + hi) * 2048u + 512u * wid + 16u * ((lane & 31u) ^ (2u * hi));
    const unsigned mc0 = 2u * DM + 64u * ((idx >> 2) * 8u + (b & 7u)) + 16u * (idx & 3u);
    const unsigned md0 = (256u * wid + (lane >> 1)) * (NMOD * 2u) + mc0 * 2u + 16u * (lane & 1u);
    unsigned sd0 = ldsb + 131072u + 8192u + 512u * wid + 16u * g;
    fl = md0;
    unsigned rd0 = ldsb + 131072u + 7168u + 8192u * ((lane >> 2) & 3u) + 256u * wid + 4u * (lane & 3u);
    unsigned ta0 = m0t + 256u * g + 32u * ((lane >> 2) & 3u) + 8u * (lane & 3u);
    const unsigned c8 = (b * 4u + wid) * 8192u, cd = moe ? sl * 32768u + wid * 8192u : 0u, cw = (idx * 4u + wid) * 16384u;
    const unsigned cm = moe ? ((sl & 3u) * 4u + wid) * 64u * (DFF * 2u) + (sl >> 2) * 128u : 0u;
    unsigned vxs = BOX * (l16 >> 1) + 64u * (l16 & 1u) + 16u * g;
    const unsigned sxs = (unsigned)WS_XIN + (y * 128u + wid * 32u) * BOX + (m >> 1) * 1024u + kg * 256u + (m & 1u) * 128u;
    unsigned vx0 = ((256u * y + 8u * hd + 2u * wid + ((lane >> 2) & 1u)) * DM + 256u * hi + 128u * (l16 >> 3) + 16u * (g & 1u) + 4u * (lane & 3u) + 32u * yp2) * 2u;
    unsigned tlo = 0, thi = 0;
#define tab(ln, lo, hi2) asm("v_writelane_b32 %0, %2, " #ln "\n\tv_writelane_b32 %1, %3, " #ln : "+v"(tlo), "+v"(thi) : "s"(lo), "s"(hi2))
#define tabp(ln, q, off) do { const unsigned long long a_ = (size_t)(q) + (off); tab(ln, (unsigned)a_, (unsigned)(a_ >> 32)); } while (0)
#define TORD 0
#define TAB_X(n)  tabp(n, p.x, ((y * 32u + hd) * 2u + (wid & 1u)) * 8192u)
#define TAB_U(n)  tabp(n, p.wu, moe ? c8 : 0u)
#define TAB_O(n)  tabp(n, p.wo, c8)
#define TAB_D(n)  tabp(n, p.wd, cd)
    TAB_X(1); TAB_U(2); TAB_O(3); TAB_D(4);
    tabp(5, p.wo, kg * 2097152u + cw); tabp(6, p.wo, kg * 2097152u + cw + 8192u); tabp(7, p.wg, cm);
    tabp(17, p.wmod, 0u); tab(18, NP_FILL == 8 ? sin + 20480u : (unsigned)WS_XIN + ((y * 32u + kg * 8u + (m >> 1)) * 4u + wid) * BOX + 20480u + (m & 1u) * 8192u, (unsigned)WS_OFL + y * 16384u + kg * 128u);
    tab(19, (unsigned)WS_OFL + (y * 32u + hd) * 512u + wid * 128u, (unsigned)WS_OFL + (y * 32u + kg * 8u) * 512u + wid * 128u);
    tabp(8, p.wg, 0u); tabp(9, p.wu, 0u); tabp(10, p.wd, 0u);
    tab(11, m, m * 4u * BOX); tab(12, (y & 1u) * 128u, 128u - (y & 1u) * 128u);
    tab(13, sxl, (unsigned)WS_PFL + (y * 32u + hd) * 512u + kg * 128u); tab(14, sxs, sxf);
    tab(15, (unsigned)WS_MODT + (mc0 - 2u * DM + 4u * wid) * 4u, (mc0 >> 10) == 4u ? 0x3f800000u : 0u);
    tab(16, (unsigned)WS_PFL + y * 16384u + (hd * 4u + wid) * 4u, (unsigned)WS_PFL + (y * 32u + hd) * 512u + wid * 128u);
    const char* psig = p.ws + WS_CNT + y * 8192u + hd * 8u;
    const char* pcnt = p.ws + WS_CNT + y * 8192u + hd * 256u;
    tabp(20, p.wr, 0u); tab(21, (unsigned)WS_PART + b * 8192u + wid * 2048u, 0u);
    tabp(25, p.out, 0u); tab(26, 256u * y + 8u * hd + 2u * wid, (y & 1u) * 256u); tab(27, (unsigned)WS_KEYS + b * 8u + wid * 4096u, (unsigned)WS_KEYS + (b & 7u) * 2048u);
    tab(28, (unsigned)WS_PART + ((b & 7u) * 32u + idx) * 32u + wid * 262144u, ((b & 7u) * 32u + idx) * 8u);
    tab(31, (128u * m + 32u * (y >> 1) + 8u * (y & 1u)) * 2048u + 512u * wid, (unsigned)WS_U + (y >> 1) * 4096u + hd * 64u + (y & 1u) * 32u); tab(32, hd * 64u, 0u);
    tab(33, (unsigned)WS_PX + (y * 32u + hd) * 1024u + wid * 256u, (unsigned)WS_PX + y * 32768u + (8u * hd + 2u * wid) * 4u); tab(34, (256u * y + 64u * wid) * 2048u + 64u * hd, (unsigned)WS_PK + b * 8u + wid * 4096u);
    tab(35, (unsigned)WS_PK + (b & 7u) * 2048u, 256u * y + 8u * hd + 2u * wid);
      asm volatile("" : "+v"(tlo), "+v"(thi), "+v"(wo0), "+v"(vg0), "+v"(vdn), "+v"(ta0), "+v"(vxs), "+v"(vx0), "+v"(rd0), "+v"(fl), "+v"(sd0), "+v"(kb0), "+v"(vvb), "+v"(vwb), "+v"(kd0), "+v"(vd0), "+v"(wd0), "+v"(hq0), "+v"(oq0));
#undef lane
      float* par = (float*)(lds + 131072);
      u32x2 mp[8]; u32x4 rs;
      auto look = [&]() __attribute__((always_inline)) {
        #pragma unroll
        for (int k = 0; k < 8; ++k) asm volatile("global_load_dwordx2 %0, %1, %2 offset:%3 sc1" : "=v"(mp[k]) : "v"(((wid >> 1) * 16u + 2u * (hd & 7u) + (wid & 1u)) * 4096u + lane * 8u), "s"(p.ws + WS_MOD), "n"(512 * k) : "memory");
        asm volatile("global_load_dwordx4 %0, %1, %2 sc1" : "=v"(rs) : "v"(((rb0 + wid) * 16u + l16) * 16u), "s"(p.ws + WS_RST) : "memory");
      };
#define LOOKED "+v"(mp[0]), "+v"(mp[1]), "+v"(mp[2]), "+v"(mp[3]), "+v"(mp[4]), "+v"(mp[5]), "+v"(mp[6]), "+v"(mp[7]), "+v"(rs)
      look();
      #pragma unroll
      for (int q = 0; q < 14; ++q)
        asm volatile("s_mov_b32 m0, %0\n\ts_nop 0\n\tbuffer_load_dwordx4 %1, %2, %3 offen lds" :: "s"(m0b + 65536u + 16384u * (q >> 2) + 1024u * (q & 3)), "v"(wd0), "s"(srdw),
                     "s"(0x80000u * ((2u * rho + (q >> 2)) & 15u) + 65536u * ((q >> 1) & 1) + 128u * (q & 1)) : "m0", "memory");
#define KVT 0
#define KV0 0
      #pragma unroll
      for (int j = 0; j < KVT; ++j) asm volatile("buffer_load_dword a255, %0, %1, %2 offen" :: "v"(lane < 32u ? lane * 128u : 0x80000000u), "s"(srdp), "s"((KV0 + wid + 4u * j) * 16384u) : "memory");
      asm volatile("s_waitcnt vmcnt(%0)" :: "n"(27 + KVT) : "memory");
      { const u32x4 cs = XV(4); *(u32x2*)(lds + 131072 + 8192 + tid * 8u) = u32x2{cvtpk(silu(lo16(cs[0])), silu(hi16(cs[0]))), cvtpk(silu(lo16(cs[1])), silu(hi16(cs[1])))}; }
      asm volatile("s_waitcnt vmcnt(%9)" : LOOKED : "n"(14 + KVT) : "memory");
      for (unsigned spins = 0;; ++spins) {
        unsigned bl = (rs[0] ^ tl) | (rs[2] ^ tl), bh = (rs[1] ^ th) | (rs[3] ^ th);
        #pragma unroll
        for (int k = 0; k < 8; ++k) { bl |= mp[k][0] ^ tl; bh |= mp[k][1] ^ th; }
        if (__all(((bl & 0xffu) | bh) == 0u) || spins > SPIN_CAP) break;
        __builtin_amdgcn_s_sleep(ASLEEP);
        look();
        asm volatile("s_waitcnt vmcnt(0)" : LOOKED :: "memory");
      }
      {
        float v = 0.f;
        #pragma unroll
        for (int k = 0; k < 8; ++k) v += __uint_as_float(mp[k][0] & 0xffffff00u);
        v = rbf(v); par[tid] = (tid < 128u ? v : rbf(1.f + v)) * QSCALE;
        if (lane < 16u) *(u32x2*)(par + 256u + 8u * lane + 2u * wid) = u32x2{rs[0] & 0xffffff00u, rs[2] & 0xffffff00u};
      }
      PAGE(6, "adaln_fr");
      asm volatile("s_waitcnt lgkmcnt(0)\n\ts_barrier" ::: "memory");
      asm volatile("buffer_load_dword a255, %0, %1, %2 offen" TNT :: "v"(tq0), "s"(srdw), "s"(0x200000u) : "memory");
      FQ(0);
      {
        const float* pc = par + 8u * (tid >> 4);
        const f32x4 a0 = *(const f32x4*)pc, a1 = *(const f32x4*)(pc + 4), c0 = *(const f32x4*)(pc + 128), c1 = *(const f32x4*)(pc + 132);
        const f32x4 t0 = *(const f32x4*)(par + 256u + 8u * l16), t1 = *(const f32x4*)(par + 260u + 8u * l16);
        #pragma unroll
        for (int i = 0; i < 4; ++i) {
          const float mn = i < 2 ? t0[2 * i] : t1[2 * i - 4], rd = i < 2 ? t0[2 * i + 1] : t1[2 * i - 3];
          u32x4 o = XV(i);
          #pragma unroll
          for (int e = 0; e < 4; ++e) {
            const f32x2 sc = e < 2 ? f32x2{c0[2 * e], c0[2 * e + 1]} : f32x2{c1[2 * e - 4], c1[2 * e - 3]}, sh = e < 2 ? f32x2{a0[2 * e], a0[2 * e + 1]} : f32x2{a1[2 * e - 4], a1[2 * e - 3]};
            o[e] = cvtpk2(__builtin_elementwise_fma(__builtin_elementwise_fma(unpk(o[e]), f32x2{rd, rd}, f32x2{mn, mn}), sc, sh));
          }
          st16_wt(p.ws + WS_H + (rb0 + i) * 32768u + ch * 256u + 16u * l16, o);
        }
      }
      FQ(1);
    asm volatile(FA_BODY
      : FA_OUTS
      : [m0b] "s"(m0b), [flag] "s"(flag), [srdz] "s"(srdz), [srdw] "s"(srdw), [srdk] "s"(srdk), [srdv] "s"(srdv), [m0t] "s"(m0t), [srdt] "s"(srdt), [srdp] "s"(srdp),
        [tag] "s"(tag), [pcnt] "s"(pcnt), [psig] "s"(psig), [wid] "s"(wid), [px] "s"(p.x), [pct] "s"(pct), [sin] "s"(sin), [tlo] "v"(tlo), [thi] "v"(thi), [wbytes] "s"(WBYTES)
      : FA_CLOBBERS);
  }
  if (b == 255u && wid == 0u) __hip_atomic_store((gu64*)(p.ws + WS_EP), tag, RLX_AGENT);
  asm volatile("adaln_text_end:");
}
}

extern "C" void kernel_launch(void* const* d_in, const int* in_sizes, int n_in, void* d_out, int out_size, void* d_ws, size_t ws_size, hipStream_t stream) {
  (void)in_sizes; (void)n_in; (void)out_size;
  if (ws_size < WS_END) return;
  const u16* x    = (const u16*)d_in[0];
  const u16* kc   = (const u16*)d_in[1];
  const u16* vc   = (const u16*)d_in[2];
  const u16* wq   = (const u16*)d_in[3];
  const u16* wo   = (const u16*)d_in[4];
  const u16* wr   = (const u16*)d_in[6];
  const u16* wg   = (const u16*)d_in[7];
  const u16* wu   = (const u16*)d_in[8];
  const u16* wd   = (const u16*)d_in[9];
  const u16* c    = (const u16*)d_in[10];
  const u16* wmod = (const u16*)d_in[11];
  u16* out = (u16*)d_out;
  char* ws = (char*)d_ws;
  fa::Args fr;
  fr.x = x; fr.wmod = wmod; fr.wq = wq; fr.kc = kc; fr.vc = vc; fr.wo = wo; fr.wg = wg; fr.wu = wu; fr.wd = wd; fr.ws = ws; fr.out = out; fr.c = c; fr.wr = wr;
  hipFuncSetAttribute((const void*)fa::fattn_k, hipFuncAttributeMaxDynamicSharedMemorySize, (int)fa::LDS_BYTES);
  hipLaunchKernelGGL(fa::fattn_k, dim3(256), dim3(256), fa::LDS_BYTES, stream, fr);
}
```

```cpp
#include <hip/hip_runtime.h>
#include <hip/hip_bf16.h>
#include <utility>
#include <cstdint>

#pragma clang fp contract(off)
typedef float    f32x16 __attribute__((ext_vector_type(16)));
typedef float    f32x2  __attribute__((ext_vector_type(2)));
typedef float    f32x4  __attribute__((ext_vector_type(4)));
typedef __bf16   bf16x8 __attribute__((ext_vector_type(8)));
typedef __bf16   bf16x4 __attribute__((ext_vector_type(4)));
typedef __bf16   bf16x2 __attribute__((ext_vector_type(2)));
typedef unsigned u32x4  __attribute__((ext_vector_type(4)));
typedef unsigned u32x2  __attribute__((ext_vector_type(2)));
typedef short    s16x4  __attribute__((ext_vector_type(4)));
typedef unsigned short u16;
template <int... Is> using iseq = std::integer_sequence<int, Is...>;
template <int N> using make_iseq = std::make_integer_sequence<int, N>;
#define DI __device__ __forceinline__
#define TAILB
#define PACE2
#define SELB

constexpr int SEQ = 2048, DM = 1024, NHEAD = 32, DHEAD = 128, DQ = NHEAD * DHEAD, DFF = 3584, NMOD = 6 * DM;
constexpr size_t WS_EP    = 0;
constexpr size_t WS_RST   = 32768;
constexpr size_t WS_CNT   = 65536;
constexpr size_t WS_MOD   = 131072;
constexpr size_t WS_Z     = 262144;
constexpr size_t WS_MODT  = WS_Z;
constexpr size_t WS_XFL   = WS_Z + 16384;
#define KEY_REP 8u
constexpr size_t WS_KEYS  = WS_Z + 32768;
constexpr unsigned Z_SHARE = 2560;
static_assert(KEY_REP <= 8 && Z_SHARE * 256 == 655360, "zeroed area");
constexpr size_t WS_MD    = WS_Z + 49152;
constexpr size_t WS_PFL   = WS_Z + 65536;
constexpr size_t WS_OFL   = WS_Z + 196608;
constexpr size_t WS_PK    = WS_Z + 327680;
constexpr size_t WS_U     = WS_Z + 344064;
constexpr size_t WS_PX    = WS_Z + 393216;
constexpr size_t WS_H     = 1048576;
constexpr size_t WS_PART  = WS_H  + (size_t)SEQ * DM * 2;
constexpr size_t WS_XIN   = WS_PART + (size_t)256 * 2 * DM * 4;
#define BOXV 38016
constexpr unsigned BOX    = BOXV;
constexpr size_t WS_END   = WS_XIN + (size_t)BOX * (SEQ / 2);

DI float bf2f(u16 b) { return __uint_as_float((unsigned)b << 16); }
DI float rbf(float x) { return (float)(__bf16)x; }
DI u16   f2bf(float x) { return __builtin_bit_cast(u16, (__bf16)x); }
DI float lo16(unsigned w) { return __uint_as_float(w << 16); }
DI float hi16(unsigned w) { return __uint_as_float(w & 0xffff0000u); }
DI unsigned cvtpk(float lo, float hi) {
  f32x2 v = {lo, hi}; bf16x2 b = __builtin_convertvector(v, bf16x2); return __builtin_bit_cast(unsigned, b);
}
template <int CTRL> DI float dpp_add(float v) { return v + __uint_as_float(__builtin_amdgcn_update_dpp(0u, __float_as_uint(v), CTRL, 0xf, 0xf, true)); }
DI float swap32_add(float a, float b) {
  auto r = __builtin_amdgcn_permlane32_swap(__float_as_uint(a), __float_as_uint(b), false, false); return __uint_as_float(r[0]) + __uint_as_float(r[1]);
}
DI float swap16_add(float a, float b) {
  auto r = __builtin_amdgcn_permlane16_swap(__float_as_uint(a), __float_as_uint(b), false, false); return __uint_as_float(r[0]) + __uint_as_float(r[1]);
}
constexpr int DPP_XOR1 = 0xB1, DPP_XOR2 = 0x4E, DPP_ROR4 = 0x124, DPP_ROR8 = 0x128;
DI float wave_sum(float v) {
  v = dpp_add<DPP_XOR1>(v); v = dpp_add<DPP_XOR2>(v); v = dpp_add<DPP_ROR4>(v); v = dpp_add<DPP_ROR8>(v);
  v = swap16_add(v, v); return swap32_add(v, v);
}

DI void st16_wt(void* p, u32x4 v) { asm volatile("global_store_dwordx4 %0, %1, off sc1\n\ts_nop 1" :: "v"(p), "v"(v) : "memory"); }

DI f32x2 unpk(unsigned w) { return f32x2{lo16(w), hi16(w)}; }
DI unsigned cvtpk2(f32x2 v) { return __builtin_bit_cast(unsigned, __builtin_convertvector(v, bf16x2)); }
DI f32x2 rb2(f32x2 v) { return f32x2{__uint_as_float(cvtpk(0.f, v[0])), __uint_as_float(cvtpk(0.f, v[1]))}; }
DI unsigned ln_pair(float x0, float x1, float m, float rden, float c0, float c1, float s0, float s1) {
  const unsigned d = cvtpk2(f32x2{x0, x1} - m), l = cvtpk2(unpk(d) * rden), t = cvtpk2(unpk(l) * f32x2{c0, c1});
  return cvtpk2(unpk(t) + f32x2{s0, s1});
}
#define GAS __attribute__((address_space(1)))
#define LAS __attribute__((address_space(3)))
typedef GAS unsigned long long gu64;
typedef short v4i16_t __attribute__((ext_vector_type(4)));
DI bf16x4 lds_tr(unsigned a) { return __builtin_bit_cast(bf16x4, __builtin_amdgcn_ds_read_tr16_b64_v4i16((LAS v4i16_t*)a)); }
#define RLX_AGENT __ATOMIC_RELAXED, __HIP_MEMORY_SCOPE_AGENT
constexpr unsigned SPIN_CAP = 1u << 16;

static_assert(BOX == 38016, "box size");
#define NP_FILL 8
#define GOT_WAIT "8"
#define NORM 0
#define UNITP 1
#define INS 0
#define XP2 0
#define FA_CLOBBERS "v0", "v1", "v2", "v3", "v4", "v5", "v6", "v7", "v8", "v9", "v10", "v11", "v12", "v13", "v14", "v15", "v16", "v17", "v18", "v19", "v20", "v21", "v22", "v23", "v24", "v25", "v26", "v27", "v28", "v29", "v30", "v31", "v32", "v33", "v34", "v35", "v36", "v37", "v38", "v39", "v40", "v41", "v42", "v43", "v44", "v45", "v46", "v47", "v48", "v49", "v50", "v51", "v52", "v53", "v54", "v55", "v56", "v57", "v58", "v59", "v60", "v61", "v62", "v63", "v64", "v65", "v66", "v67", "v68", "v69", "v70", "v71", "v72", "v73", "v74", "v75", "v76", "v77", "v78", "v79", "v80", "v81", "v82", "v83", "v84", "v85", "v86", "v87", "v88", "v89", "v90", "v91", "v92", "v93", "v94", "v95", "v96", "v97", "v98", "v99", "v100", "v101", "v102", "v103", "v104", "v105", "v106", "v107", "v108", "v109", "v110", "v111", "v112", "v113", "v114", "v115", "v116", "v117", "v118", "v119", "v120", "v121", "v122", "v123", "v124", "v125", "v126", "v127", "v128", "v129", "v130", "v131", "v132", "v133", "v134", "v135", "v136", "v137", "v138", "v139", "v140", "v141", "v142", "v143", "v144", "v145", "v146", "v147", "v148", "v149", "v150", "v151", "v152", "v153", "v154", "v155", "v156", "v157", "v158", "v159", "v160", "v161", "v162", "v163", "v164", "v165", "v166", "v167", "v168", "v169", "v170", "v171", "v172", "v173", "v174", "v175", "v176", "v177", "v178", "v179", "v180", "v181", "v182", "v183", "v184", "v185", "v186", "v187", "v188", "v189", "v190", "v191", "v192", "v193", "v194", "v195", "v196", "v197", "v198", "v199", "v200", "v201", "v202", "v203", "v204", "v205", "v206", "v207", "v208", "v209", "v210", "v211", "v212", "v213", "v214", "v215", "v216", "v217", "v218", "v219", "v220", "v221", "v222", "v223", "v224", "v230", "v231", "v232", "v233", "v234", "v235", "v237", "a0", "a1", "a2", "a3", "a4", "a5", "a6", "a7", "a8", "a9", "a10", "a11", "a12", "a13", "a14", "a15", "a16", "a17", "a18", "a19", "a20", "a21", "a22", "a23", "a24", "a25", "a26", "a27", "a28", "a29", "a30", "a31", "a32", "a33", "a34", "a35", "a36", "a37", "a38", "a39", "a40", "a41", "a42", "a43", "a44", "a45", "a46", "a47", "a48", "a49", "a50", "a51", "a52", "a53", "a54", "a55", "a56", "a57", "a58", "a59", "a60", "a61", "a62", "a63", "a64", "a65", "a66", "a67", "a68", "a69", "a70", "a71", "a72", "a73", "a74", "a75", "a76", "a77", "a78", "a79", "a80", "a81", "a82", "a83", "a84", "a85", "a86", "a87", "a88", "a89", "a90", "a91", "a92", "a93", "a94", "a95", "a96", "a97", "a98", "a99", "a100", "a101", "a102", "a103", "a104", "a105", "a106", "a107", "a108", "a109", "a110", "a111", "a112", "a113", "a114", "a115", "a116", "a117", "a118", "a119", "a120", "a121", "a122", "a123", "a124", "a125", "a126", "a127", "a128", "a129", "a130", "a131", "a132", "a133", "a134", "a135", "a136", "a137", "a138", "a139", "a140", "a141", "a142", "a143", "a144", "a145", "a146", "a147", "a148", "a149", "a150", "a151", "a152", "a153", "a154", "a155", "a156", "a157", "a158", "a159", "a160", "a161", "a162", "a163", "a164", "a165", "a166", "a167", "a168", "a169", "a170", "a171", "a172", "a173", "a174", "a175", "a176", "a177", "a178", "a179", "a180", "a181", "a182", "a183", "a184", "a185", "a186", "a187", "a188", "a189", "a190", "a191", "a192", "a193", "a194", "a195", "a196", "a197", "a198", "a199", "a200", "a201", "a202", "a203", "a204", "a205", "a206", "a207", "a208", "a209", "a210", "a211", "a212", "a213", "a214", "a215", "a216", "a217", "a218", "a219", "a220", "a221", "a222", "a223", "a224", "a225", "a226", "a227", "a228", "a229", "a230", "a231", "a232", "a233", "a234", "a235", "a236", "a237", "a238", "a239", "a240", "a241", "a242", "a243", "a244", "a245", "a246", "a247", "a248", "a249", "a250", "a251", "a252", "a253", "a254", "a255", "s40", "s41", "s42", "s43", "s44", "s45", "s46", "s47", "s48", "s49", "s50", "s51", "s52", "s53", "s54", "s55", "s56", "s57", "s58", "s59", "s60", "s61", "s62", "s63", "s64", "s65", "s66", "s67", "s68", "s69", "vcc", "m0", "scc", "memory"
#define EARLY2 0
#define SHW 1
#define QROT 0
#define SKV 0
#define KVPAR 0
#define QT2 0
#define QSTAG 0
#define ATAIL 1
static_assert(WS_MODT == 262144 && WS_XFL == 278528 && WS_U == 606208 && WS_XIN == 7340032, "workspace layout");
#define FA_OUTS [got] "=&s"(got), [k1] "=&s"(k1), [k2] "=&s"(k2), [lat] "=&s"(lat), "+{v229}"(fl), "+{v225}"(vwb), "+{v226}"(wd0), "+{v227}"(hq0), "+{v228}"(sd0), "+{v236}"(tq0), [kb0] "+v"(kb0), [vvb] "+v"(vvb), [kd0] "+v"(kd0), [vd0] "+v"(vd0), [oq0] "+v"(oq0), [rd0] "+v"(rd0), [wo0] "+v"(wo0), [vg0] "+v"(vg0), [vx0] "+v"(vx0), [vdn] "+v"(vdn), [ta0] "+v"(ta0), [vxs] "+v"(vxs), [vct] "+v"(vct)
#define FA_BODY \
"s_branch adaln_body\n" \
".p2align 12\n" \
"adaln_pg7:\n" \
"adaln_body:\n" \
"s_cbranch_execz adaln_fr\n" \
"s_nop 0\n" \
"s_mov_b32 s55, 0x80000\n" \
"v_add_u32_e32 v219, 0x8000, v227\n" \
"v_add_u32_e32 v220, 0x10000, v227\n" \
"v_add_u32_e32 v221, 0x18000, v227\n" \
"s_mov_b32 s50, 0\n" \
"s_movk_i32 s53, 0x1000\n" \
"s_mov_b32 s52, 0\n" \
"s_nop 0\n" \
"s_mov_b32 s49, 1572864\n" \
"v_add_u32_e32 v216, 0xfffffc80, v226\n" \
"v_add_u32_e32 v217, 0xf800, v226\n" \
"v_add_u32_e32 v218, 0xf480, v226\n" \
"s_add_u32 s40, %[m0b], 0\n" \
"s_nop 0\n" \
"s_add_u32 s44, %[m0b], 65536\n" \
"s_add_u32 s41, %[m0b], 16384\n" \
"s_add_u32 s45, %[m0b], 81920\n" \
"s_add_u32 s42, %[m0b], 32768\n" \
"s_add_u32 s46, %[m0b], 98304\n" \
"s_add_u32 s43, %[m0b], 49152\n" \
"s_add_u32 s47, %[m0b], 114688\n" \
"v_xor_b32_e64 v208, 64, %[kb0]\n" \
"v_xor_b32_e32 v209, 0x80, %[kb0]\n" \
"v_xor_b32_e32 v210, 0xc0, %[kb0]\n" \
"v_xor_b32_e64 v212, 64, %[kd0]\n" \
"v_xor_b32_e32 v213, 0x80, %[kd0]\n" \
"v_xor_b32_e32 v214, 0xc0, %[kd0]\n" \
"v_xor_b32_e64 v211, 32, %[vvb]\n" \
"v_add_u32_e32 v215, 0xfffffc80, %[vd0]\n" \
"v_readlane_b32 s64, %[tlo], 17\n" \
"v_readlane_b32 s65, %[thi], 17\n" \
"s_mov_b32 s66, 12582912\n" \
"s_mov_b32 s67, 0x20000\n" \
"v_mov_b64_e32 v[0:1], 0\n" \
"v_mov_b64_e32 v[2:3], 0\n" \
"v_mov_b64_e32 v[4:5], 0\n" \
"v_mov_b64_e32 v[6:7], 0\n" \
"v_mov_b64_e32 v[8:9], 0\n" \
"v_mov_b64_e32 v[10:11], 0\n" \
"v_mov_b64_e32 v[12:13], 0\n" \
"v_mov_b64_e32 v[14:15], 0\n" \
"v_mov_b64_e32 v[16:17], 0\n" \
"v_mov_b64_e32 v[18:19], 0\n" \
"v_mov_b64_e32 v[20:21], 0\n" \
"v_mov_b64_e32 v[22:23], 0\n" \
"v_mov_b64_e32 v[24:25], 0\n" \
"v_mov_b64_e32 v[26:27], 0\n" \
"v_mov_b64_e32 v[28:29], 0\n" \
"v_mov_b64_e32 v[30:31], 0\n" \
"v_mov_b64_e32 v[32:33], 0\n" \
"v_mov_b64_e32 v[34:35], 0\n" \
"v_mov_b64_e32 v[36:37], 0\n" \
"v_mov_b64_e32 v[38:39], 0\n" \
"v_mov_b64_e32 v[40:41], 0\n" \
"v_mov_b64_e32 v[42:43], 0\n" \
"v_mov_b64_e32 v[44:45], 0\n" \
"v_mov_b64_e32 v[46:47], 0\n" \
"v_mov_b64_e32 v[48:49], 0\n" \
"v_mov_b64_e32 v[50:51], 0\n" \
"v_mov_b64_e32 v[52:53], 0\n" \
"v_mov_b64_e32 v[54:55], 0\n" \
"v_mov_b64_e32 v[56:57], 0\n" \
"v_mov_b64_e32 v[58:59], 0\n" \
"v_mov_b64_e32 v[60:61], 0\n" \
"v_mov_b64_e32 v[62:63], 0\n" \
"v_mov_b64_e32 v[64:65], 0\n" \
"v_mov_b64_e32 v[66:67], 0\n" \
"v_mov_b64_e32 v[68:69], 0\n" \
"v_mov_b64_e32 v[70:71], 0\n" \
"v_mov_b64_e32 v[72:73], 0\n" \
"v_mov_b64_e32 v[74:75], 0\n" \
"v_mov_b64_e32 v[76:77], 0\n" \
"v_mov_b64_e32 v[78:79], 0\n" \
"v_mov_b64_e32 v[80:81], 0\n" \
"v_mov_b64_e32 v[82:83], 0\n" \
"v_mov_b64_e32 v[84:85], 0\n" \
"v_mov_b64_e32 v[86:87], 0\n" \
"v_mov_b64_e32 v[88:89], 0\n" \
"v_mov_b64_e32 v[90:91], 0\n" \
"v_mov_b64_e32 v[92:93], 0\n" \
"v_mov_b64_e32 v[94:95], 0\n" \
"v_mov_b64_e32 v[96:97], 0\n" \
"v_mov_b64_e32 v[98:99], 0\n" \
"v_mov_b64_e32 v[100:101], 0\n" \
"v_mov_b64_e32 v[102:103], 0\n" \
"v_mov_b64_e32 v[104:105], 0\n" \
"v_mov_b64_e32 v[106:107], 0\n" \
"v_mov_b64_e32 v[108:109], 0\n" \
"v_mov_b64_e32 v[110:111], 0\n" \
"v_mov_b64_e32 v[112:113], 0\n" \
"v_mov_b64_e32 v[114:115], 0\n" \
"v_mov_b64_e32 v[116:117], 0\n" \
"v_mov_b64_e32 v[118:119], 0\n" \
"v_mov_b64_e32 v[120:121], 0\n" \
"v_mov_b64_e32 v[122:123], 0\n" \
"v_mov_b64_e32 v[124:125], 0\n" \
"v_mov_b64_e32 v[126:127], 0\n" \
"s_mov_b32 s51, 0x10000\n" \
"v_mbcnt_lo_u32_b32 v232, -1, 0\n" \
"v_mov_b64_e32 v[234:235], %[tag]\n" \
"v_lshlrev_b32_e32 v233, 3, v232\n" \
"v_lshlrev_b32_e32 v232, 8, v232\n" \
"s_waitcnt vmcnt(0)\n" \
"s_barrier\n" \
"s_cmp_lg_u32 %[wid], 0\n" \
"s_cbranch_scc1 Lwalk_%=\n" \
"s_mov_b32 exec_hi, 0\n" \
"global_store_dwordx2 v232, v[234:235], %[psig] sc1\n" \
"s_mov_b32 exec_hi, -1\n" \
"Lgo_%=:\n" \
"s_nop 0\n" \
"ds_read_b64_tr_b16 v[128:129], v225 offset:0\n" \
"ds_read_b64_tr_b16 v[130:131], v225 offset:256\n" \
"ds_read_b64_tr_b16 v[132:133], v225 offset:8192\n" \
"ds_read_b64_tr_b16 v[134:135], v225 offset:8448\n" \
"ds_read_b64_tr_b16 v[136:137], v225 offset:8\n" \
"ds_read_b64_tr_b16 v[138:139], v225 offset:264\n" \
"ds_read_b64_tr_b16 v[140:141], v225 offset:8200\n" \
"ds_read_b64_tr_b16 v[142:143], v225 offset:8456\n" \
"ds_read_b64_tr_b16 v[144:145], v225 offset:512\n" \
"ds_read_b64_tr_b16 v[146:147], v225 offset:768\n" \
"ds_read_b64_tr_b16 v[148:149], v225 offset:8704\n" \
"ds_read_b64_tr_b16 v[150:151], v225 offset:8960\n" \
"ds_read_b64_tr_b16 v[152:153], v225 offset:520\n" \
"ds_read_b64_tr_b16 v[154:155], v225 offset:776\n" \
"ds_read_b64_tr_b16 v[156:157], v225 offset:8712\n" \
"ds_read_b64_tr_b16 v[158:159], v225 offset:8968\n" \
"s_mov_b32 s60, 4194304\n" \
"v_and_b32_e32 v233, 56, v233\n" \
"s_lshl_b32 s61, %[wid], 6\n" \
"v_add_u32_e64 v233, s61, v233\n" \
"Lpoll_%=:\n" \
"global_load_dwordx2 v[230:231], v233, %[pcnt] sc1\n" \
"s_waitcnt vmcnt(0)\n" \
"v_cmp_ne_u64_e32 vcc, v[230:231], v[234:235]\n" \
"s_sub_u32 s51, s51, 1\n" \
"s_cbranch_scc1 Lpd_%=\n" \
"s_nop 3\n" \
"s_cbranch_vccnz Lpoll_%=\n" \
"Lpd_%=:\n" \
"buffer_load_dwordx4 a[128:131], v227, %[srdz], s50 offen offset:0 sc1\n" \
"buffer_load_dwordx4 a[132:135], v219, %[srdz], s50 offen offset:0 sc1\n" \
"buffer_load_dwordx4 a[136:139], v220, %[srdz], s50 offen offset:0 sc1\n" \
"buffer_load_dwordx4 a[140:143], v221, %[srdz], s50 offen offset:0 sc1\n" \
"buffer_load_dwordx4 a[144:147], v227, %[srdz], s50 offen offset:1024 sc1\n" \
"buffer_load_dwordx4 a[148:151], v219, %[srdz], s50 offen offset:1024 sc1\n" \
"buffer_load_dwordx4 a[152:155], v220, %[srdz], s50 offen offset:1024 sc1\n" \
"buffer_load_dwordx4 a[156:159], v221, %[srdz], s50 offen offset:1024 sc1\n" \
"buffer_load_dwordx4 a[160:163], v227, %[srdz], s50 offen offset:2048 sc1\n" \
"buffer_load_dwordx4 a[164:167], v219, %[srdz], s50 offen offset:2048 sc1\n" \
"buffer_load_dwordx4 a[168:171], v220, %[srdz], s50 offen offset:2048 sc1\n" \
"buffer_load_dwordx4 a[172:175], v221, %[srdz], s50 offen offset:2048 sc1\n" \
"buffer_load_dwordx4 a[176:179], v227, %[srdz], s50 offen offset:3072 sc1\n" \
"buffer_load_dwordx4 a[180:183], v219, %[srdz], s50 offen offset:3072 sc1\n" \
"buffer_load_dwordx4 a[184:187], v220, %[srdz], s50 offen offset:3072 sc1\n" \
"buffer_load_dwordx4 a[188:191], v221, %[srdz], s50 offen offset:3072 sc1\n" \
"s_add_u32 s50, s50, s53\n" \
"s_nop 0\n" \
"buffer_load_dwordx4 a[192:195], v227, %[srdz], s50 offen offset:0 sc1\n" \
"buffer_load_dwordx4 a[196:199], v219, %[srdz], s50 offen offset:0 sc1\n" \
"buffer_load_dwordx4 a[200:203], v220, %[srdz], s50 offen offset:0 sc1\n" \
"buffer_load_dwordx4 a[204:207], v221, %[srdz], s50 offen offset:0 sc1\n" \
"buffer_load_dwordx4 a[208:211], v227, %[srdz], s50 offen offset:1024 sc1\n" \
"buffer_load_dwordx4 a[212:215], v219, %[srdz], s50 offen offset:1024 sc1\n" \
"buffer_load_dwordx4 a[216:219], v220, %[srdz], s50 offen offset:1024 sc1\n" \
"buffer_load_dwordx4 a[220:223], v221, %[srdz], s50 offen offset:1024 sc1\n" \
"s_mov_b32 s51, 3\n" \
"s_waitcnt vmcnt(16) lgkmcnt(0)\n" \
"Lq_%=:\n" \
"v_mfma_f32_16x16x32_bf16 v[0:3], v[128:131], a[128:131], v[0:3]\n" \
"ds_read_b64_tr_b16 v[160:161], v225 offset:1024\n" \
"v_mfma_f32_16x16x32_bf16 v[0:3], v[132:135], a[144:147], v[0:3]\n" \
"ds_read_b64_tr_b16 v[162:163], v225 offset:1280\n" \
"v_mfma_f32_16x16x32_bf16 v[32:35], v[128:131], a[132:135], v[32:35]\n" \
"s_mov_b32 m0, s47\n" \
"s_nop 0\n" \
"buffer_load_dwordx4 v217, %[srdw], s49 offen offset:2048 lds\n" \
"v_mfma_f32_16x16x32_bf16 v[32:35], v[132:135], a[148:151], v[32:35]\n" \
"ds_read_b64_tr_b16 v[164:165], v225 offset:9216\n" \
"v_mfma_f32_16x16x32_bf16 v[64:67], v[128:131], a[136:139], v[64:67]\n" \
"ds_read_b64_tr_b16 v[166:167], v225 offset:9472\n" \
"v_mfma_f32_16x16x32_bf16 v[64:67], v[132:135], a[152:155], v[64:67]\n" \
"v_mfma_f32_16x16x32_bf16 v[96:99], v[128:131], a[140:143], v[96:99]\n" \
"ds_read_b64_tr_b16 v[168:169], v225 offset:1032\n" \
"v_mfma_f32_16x16x32_bf16 v[96:99], v[132:135], a[156:159], v[96:99]\n" \
"ds_read_b64_tr_b16 v[170:171], v225 offset:1288\n" \
"v_mfma_f32_16x16x32_bf16 v[4:7], v[136:139], a[128:131], v[4:7]\n" \
"buffer_load_dwordx4 v218, %[srdw], s49 offen offset:3072 lds\n" \
"v_mfma_f32_16x16x32_bf16 v[4:7], v[140:143], a[144:147], v[4:7]\n" \
"ds_read_b64_tr_b16 v[172:173], v225 offset:9224\n" \
"v_mfma_f32_16x16x32_bf16 v[36:39], v[136:139], a[132:135], v[36:39]\n" \
"ds_read_b64_tr_b16 v[174:175], v225 offset:9480\n" \
"v_mfma_f32_16x16x32_bf16 v[36:39], v[140:143], a[148:151], v[36:39]\n" \
"v_mfma_f32_16x16x32_bf16 v[68:71], v[136:139], a[136:139], v[68:71]\n" \
"ds_read_b64_tr_b16 v[176:177], v225 offset:1536\n" \
"v_mfma_f32_16x16x32_bf16 v[68:71], v[140:143], a[152:155], v[68:71]\n" \
"ds_read_b64_tr_b16 v[178:179], v225 offset:1792\n" \
"v_mfma_f32_16x16x32_bf16 v[100:103], v[136:139], a[140:143], v[100:103]\n" \
"buffer_load_dwordx4 a[224:227], v227, %[srdz], s50 offen offset:2048 sc1\n" \
"v_mfma_f32_16x16x32_bf16 v[100:103], v[140:143], a[156:159], v[100:103]\n" \
"ds_read_b64_tr_b16 v[180:181], v225 offset:9728\n" \
"v_mfma_f32_16x16x32_bf16 v[8:11], v[144:147], a[128:131], v[8:11]\n" \
"ds_read_b64_tr_b16 v[182:183], v225 offset:9984\n" \
"v_mfma_f32_16x16x32_bf16 v[8:11], v[148:151], a[144:147], v[8:11]\n" \
"v_mfma_f32_16x16x32_bf16 v[40:43], v[144:147], a[132:135], v[40:43]\n" \
"ds_read_b64_tr_b16 v[184:185], v225 offset:1544\n" \
"v_mfma_f32_16x16x32_bf16 v[40:43], v[148:151], a[148:151], v[40:43]\n" \
"ds_read_b64_tr_b16 v[186:187], v225 offset:1800\n" \
"v_mfma_f32_16x16x32_bf16 v[72:75], v[144:147], a[136:139], v[72:75]\n" \
"buffer_load_dwordx4 a[228:231], v219, %[srdz], s50 offen offset:2048 sc1\n" \
"v_mfma_f32_16x16x32_bf16 v[72:75], v[148:151], a[152:155], v[72:75]\n" \
"ds_read_b64_tr_b16 v[188:189], v225 offset:9736\n" \
"v_mfma_f32_16x16x32_bf16 v[104:107], v[144:147], a[140:143], v[104:107]\n" \
"ds_read_b64_tr_b16 v[190:191], v225 offset:9992\n" \
"v_mfma_f32_16x16x32_bf16 v[104:107], v[148:151], a[156:159], v[104:107]\n" \
"v_mfma_f32_16x16x32_bf16 v[12:15], v[152:155], a[128:131], v[12:15]\n" \
"buffer_load_dwordx4 a[232:235], v220, %[srdz], s50 offen offset:2048 sc1\n" \
"v_mfma_f32_16x16x32_bf16 v[12:15], v[156:159], a[144:147], v[12:15]\n" \
"v_mfma_f32_16x16x32_bf16 v[44:47], v[152:155], a[132:135], v[44:47]\n" \
"v_mfma_f32_16x16x32_bf16 v[44:47], v[156:159], a[148:151], v[44:47]\n" \
"v_mfma_f32_16x16x32_bf16 v[76:79], v[152:155], a[136:139], v[76:79]\n" \
"buffer_load_dwordx4 a[236:239], v221, %[srdz], s50 offen offset:2048 sc1\n" \
"v_mfma_f32_16x16x32_bf16 v[76:79], v[156:159], a[152:155], v[76:79]\n" \
"v_mfma_f32_16x16x32_bf16 v[108:111], v[152:155], a[140:143], v[108:111]\n" \
"v_mfma_f32_16x16x32_bf16 v[108:111], v[156:159], a[156:159], v[108:111]\n" \
"s_waitcnt vmcnt(29) lgkmcnt(0)\n" \
"s_barrier\n" \
"v_mfma_f32_16x16x32_bf16 v[16:19], v[160:163], a[128:131], v[16:19]\n" \
"ds_read_b64_tr_b16 v[128:129], v225 offset:16384\n" \
"v_mfma_f32_16x16x32_bf16 v[16:19], v[164:167], a[144:147], v[16:19]\n" \
"ds_read_b64_tr_b16 v[130:131], v225 offset:16640\n" \
"v_mfma_f32_16x16x32_bf16 v[48:51], v[160:163], a[132:135], v[48:51]\n" \
"s_add_u32 s49, s49, s55\n" \
"s_mov_b32 m0, s44\n" \
"s_nop 0\n" \
"buffer_load_dwordx4 v226, %[srdw], s49 offen offset:0 lds\n" \
"v_mfma_f32_16x16x32_bf16 v[48:51], v[164:167], a[148:151], v[48:51]\n" \
"ds_read_b64_tr_b16 v[132:133], v225 offset:24576\n" \
"v_mfma_f32_16x16x32_bf16 v[80:83], v[160:163], a[136:139], v[80:83]\n" \
"ds_read_b64_tr_b16 v[134:135], v225 offset:24832\n" \
"v_mfma_f32_16x16x32_bf16 v[80:83], v[164:167], a[152:155], v[80:83]\n" \
"v_mfma_f32_16x16x32_bf16 v[112:115], v[160:163], a[140:143], v[112:115]\n" \
"ds_read_b64_tr_b16 v[136:137], v225 offset:16392\n" \
"v_mfma_f32_16x16x32_bf16 v[112:115], v[164:167], a[156:159], v[112:115]\n" \
"ds_read_b64_tr_b16 v[138:139], v225 offset:16648\n" \
"v_mfma_f32_16x16x32_bf16 v[20:23], v[168:171], a[128:131], v[20:23]\n" \
"buffer_load_dwordx4 v216, %[srdw], s49 offen offset:1024 lds\n" \
"v_mfma_f32_16x16x32_bf16 v[20:23], v[172:175], a[144:147], v[20:23]\n" \
"ds_read_b64_tr_b16 v[140:141], v225 offset:24584\n" \
"v_mfma_f32_16x16x32_bf16 v[52:55], v[168:171], a[132:135], v[52:55]\n" \
"ds_read_b64_tr_b16 v[142:143], v225 offset:24840\n" \
"v_mfma_f32_16x16x32_bf16 v[52:55], v[172:175], a[148:151], v[52:55]\n" \
"v_mfma_f32_16x16x32_bf16 v[84:87], v[168:171], a[136:139], v[84:87]\n" \
"ds_read_b64_tr_b16 v[144:145], v225 offset:16896\n" \
"v_mfma_f32_16x16x32_bf16 v[84:87], v[172:175], a[152:155], v[84:87]\n" \
"ds_read_b64_tr_b16 v[146:147], v225 offset:17152\n" \
"v_mfma_f32_16x16x32_bf16 v[116:119], v[168:171], a[140:143], v[116:119]\n" \
"buffer_load_dwordx4 a[240:243], v227, %[srdz], s50 offen offset:3072 sc1\n" \
"v_mfma_f32_16x16x32_bf16 v[116:119], v[172:175], a[156:159], v[116:119]\n" \
"ds_read_b64_tr_b16 v[148:149], v225 offset:25088\n" \
"v_mfma_f32_16x16x32_bf16 v[24:27], v[176:179], a[128:131], v[24:27]\n" \
"ds_read_b64_tr_b16 v[150:151], v225 offset:25344\n" \
"v_mfma_f32_16x16x32_bf16 v[24:27], v[180:183], a[144:147], v[24:27]\n" \
"v_mfma_f32_16x16x32_bf16 v[56:59], v[176:179], a[132:135], v[56:59]\n" \
"ds_read_b64_tr_b16 v[152:153], v225 offset:16904\n" \
"v_mfma_f32_16x16x32_bf16 v[56:59], v[180:183], a[148:151], v[56:59]\n" \
"ds_read_b64_tr_b16 v[154:155], v225 offset:17160\n" \
"v_mfma_f32_16x16x32_bf16 v[88:91], v[176:179], a[136:139], v[88:91]\n" \
"buffer_load_dwordx4 a[244:247], v219, %[srdz], s50 offen offset:3072 sc1\n" \
"v_mfma_f32_16x16x32_bf16 v[88:91], v[180:183], a[152:155], v[88:91]\n" \
"ds_read_b64_tr_b16 v[156:157], v225 offset:25096\n" \
"v_mfma_f32_16x16x32_bf16 v[120:123], v[176:179], a[140:143], v[120:123]\n" \
"ds_read_b64_tr_b16 v[158:159], v225 offset:25352\n" \
"v_mfma_f32_16x16x32_bf16 v[120:123], v[180:183], a[156:159], v[120:123]\n" \
"v_mfma_f32_16x16x32_bf16 v[28:31], v[184:187], a[128:131], v[28:31]\n" \
"buffer_load_dwordx4 a[248:251], v220, %[srdz], s50 offen offset:3072 sc1\n" \
"v_mfma_f32_16x16x32_bf16 v[28:31], v[188:191], a[144:147], v[28:31]\n" \
"v_mfma_f32_16x16x32_bf16 v[60:63], v[184:187], a[132:135], v[60:63]\n" \
"v_mfma_f32_16x16x32_bf16 v[60:63], v[188:191], a[148:151], v[60:63]\n" \
"v_mfma_f32_16x16x32_bf16 v[92:95], v[184:187], a[136:139], v[92:95]\n" \
"buffer_load_dwordx4 a[252:255], v221, %[srdz], s50 offen offset:3072 sc1\n" \
"v_mfma_f32_16x16x32_bf16 v[92:95], v[188:191], a[152:155], v[92:95]\n" \
"v_mfma_f32_16x16x32_bf16 v[124:127], v[184:187], a[140:143], v[124:127]\n" \
"v_mfma_f32_16x16x32_bf16 v[124:127], v[188:191], a[156:159], v[124:127]\n" \
"s_waitcnt vmcnt(20) lgkmcnt(0)\n" \
"v_mfma_f32_16x16x32_bf16 v[0:3], v[128:131], a[160:163], v[0:3]\n" \
"ds_read_b64_tr_b16 v[160:161], v225 offset:17408\n" \
"v_mfma_f32_16x16x32_bf16 v[0:3], v[132:135], a[176:179], v[0:3]\n" \
"ds_read_b64_tr_b16 v[162:163], v225 offset:17664\n" \
"v_mfma_f32_16x16x32_bf16 v[32:35], v[128:131], a[164:167], v[32:35]\n" \
"s_mov_b32 m0, s44\n" \
"s_nop 0\n" \
"buffer_load_dwordx4 v217, %[srdw], s49 offen offset:2048 lds\n" \
"v_mfma_f32_16x16x32_bf16 v[32:35], v[132:135], a[180:183], v[32:35]\n" \
"ds_read_b64_tr_b16 v[164:165], v225 offset:25600\n" \
"v_mfma_f32_16x16x32_bf16 v[64:67], v[128:131], a[168:171], v[64:67]\n" \
"ds_read_b64_tr_b16 v[166:167], v225 offset:25856\n" \
"v_mfma_f32_16x16x32_bf16 v[64:67], v[132:135], a[184:187], v[64:67]\n" \
"v_mfma_f32_16x16x32_bf16 v[96:99], v[128:131], a[172:175], v[96:99]\n" \
"ds_read_b64_tr_b16 v[168:169], v225 offset:17416\n" \
"v_mfma_f32_16x16x32_bf16 v[96:99], v[132:135], a[188:191], v[96:99]\n" \
"ds_read_b64_tr_b16 v[170:171], v225 offset:17672\n" \
"v_mfma_f32_16x16x32_bf16 v[4:7], v[136:139], a[160:163], v[4:7]\n" \
"buffer_load_dwordx4 v218, %[srdw], s49 offen offset:3072 lds\n" \
"v_mfma_f32_16x16x32_bf16 v[4:7], v[140:143], a[176:179], v[4:7]\n" \
"ds_read_b64_tr_b16 v[172:173], v225 offset:25608\n" \
"v_mfma_f32_16x16x32_bf16 v[36:39], v[136:139], a[164:167], v[36:39]\n" \
"ds_read_b64_tr_b16 v[174:175], v225 offset:25864\n" \
"v_mfma_f32_16x16x32_bf16 v[36:39], v[140:143], a[180:183], v[36:39]\n" \
"v_mfma_f32_16x16x32_bf16 v[68:71], v[136:139], a[168:171], v[68:71]\n" \
"ds_read_b64_tr_b16 v[176:177], v225 offset:17920\n" \
"v_mfma_f32_16x16x32_bf16 v[68:71], v[140:143], a[184:187], v[68:71]\n" \
"ds_read_b64_tr_b16 v[178:179], v225 offset:18176\n" \
"v_mfma_f32_16x16x32_bf16 v[100:103], v[136:139], a[172:175], v[100:103]\n" \
"s_add_u32 s50, s50, s53\n" \
"buffer_load_dwordx4 a[128:131], v227, %[srdz], s50 offen offset:0 sc1\n" \
"v_mfma_f32_16x16x32_bf16 v[100:103], v[140:143], a[188:191], v[100:103]\n" \
"ds_read_b64_tr_b16 v[180:181], v225 offset:26112\n" \
"v_mfma_f32_16x16x32_bf16 v[8:11], v[144:147], a[160:163], v[8:11]\n" \
"ds_read_b64_tr_b16 v[182:183], v225 offset:26368\n" \
"v_mfma_f32_16x16x32_bf16 v[8:11], v[148:151], a[176:179], v[8:11]\n" \
"v_mfma_f32_16x16x32_bf16 v[40:43], v[144:147], a[164:167], v[40:43]\n" \
"ds_read_b64_tr_b16 v[184:185], v225 offset:17928\n" \
"v_mfma_f32_16x16x32_bf16 v[40:43], v[148:151], a[180:183], v[40:43]\n" \
"ds_read_b64_tr_b16 v[186:187], v225 offset:18184\n" \
"v_mfma_f32_16x16x32_bf16 v[72:75], v[144:147], a[168:171], v[72:75]\n" \
"buffer_load_dwordx4 a[132:135], v219, %[srdz], s50 offen offset:0 sc1\n" \
"v_mfma_f32_16x16x32_bf16 v[72:75], v[148:151], a[184:187], v[72:75]\n" \
"ds_read_b64_tr_b16 v[188:189], v225 offset:26120\n" \
"v_mfma_f32_16x16x32_bf16 v[104:107], v[144:147], a[172:175], v[104:107]\n" \
"ds_read_b64_tr_b16 v[190:191], v225 offset:26376\n" \
"v_mfma_f32_16x16x32_bf16 v[104:107], v[148:151], a[188:191], v[104:107]\n" \
"v_mfma_f32_16x16x32_bf16 v[12:15], v[152:155], a[160:163], v[12:15]\n" \
"buffer_load_dwordx4 a[136:139], v220, %[srdz], s50 offen offset:0 sc1\n" \
"v_mfma_f32_16x16x32_bf16 v[12:15], v[156:159], a[176:179], v[12:15]\n" \
"v_mfma_f32_16x16x32_bf16 v[44:47], v[152:155], a[164:167], v[44:47]\n" \
"v_mfma_f32_16x16x32_bf16 v[44:47], v[156:159], a[180:183], v[44:47]\n" \
"v_mfma_f32_16x16x32_bf16 v[76:79], v[152:155], a[168:171], v[76:79]\n" \
"buffer_load_dwordx4 a[140:143], v221, %[srdz], s50 offen offset:0 sc1\n" \
"v_mfma_f32_16x16x32_bf16 v[76:79], v[156:159], a[184:187], v[76:79]\n" \
"v_mfma_f32_16x16x32_bf16 v[108:111], v[152:155], a[172:175], v[108:111]\n" \
"v_mfma_f32_16x16x32_bf16 v[108:111], v[156:159], a[188:191], v[108:111]\n" \
"s_waitcnt vmcnt(29) lgkmcnt(0)\n" \
"s_barrier\n" \
"v_mfma_f32_16x16x32_bf16 v[16:19], v[160:163], a[160:163], v[16:19]\n" \
"ds_read_b64_tr_b16 v[128:129], v225 offset:32768\n" \
"v_mfma_f32_16x16x32_bf16 v[16:19], v[164:167], a[176:179], v[16:19]\n" \
"ds_read_b64_tr_b16 v[130:131], v225 offset:33024\n" \
"v_mfma_f32_16x16x32_bf16 v[48:51], v[160:163], a[164:167], v[48:51]\n" \
"s_add_u32 s49, s49, s55\n" \
"s_mov_b32 m0, s45\n" \
"s_nop 0\n" \
"buffer_load_dwordx4 v226, %[srdw], s49 offen offset:0 lds\n" \
"v_mfma_f32_16x16x32_bf16 v[48:51], v[164:167], a[180:183], v[48:51]\n" \
"ds_read_b64_tr_b16 v[132:133], v225 offset:40960\n" \
"v_mfma_f32_16x16x32_bf16 v[80:83], v[160:163], a[168:171], v[80:83]\n" \
"ds_read_b64_tr_b16 v[134:135], v225 offset:41216\n" \
"v_mfma_f32_16x16x32_bf16 v[80:83], v[164:167], a[184:187], v[80:83]\n" \
"v_mfma_f32_16x16x32_bf16 v[112:115], v[160:163], a[172:175], v[112:115]\n" \
"ds_read_b64_tr_b16 v[136:137], v225 offset:32776\n" \
"v_mfma_f32_16x16x32_bf16 v[112:115], v[164:167], a[188:191], v[112:115]\n" \
"ds_read_b64_tr_b16 v[138:139], v225 offset:33032\n" \
"v_mfma_f32_16x16x32_bf16 v[20:23], v[168:171], a[160:163], v[20:23]\n" \
"buffer_load_dwordx4 v216, %[srdw], s49 offen offset:1024 lds\n" \
"v_mfma_f32_16x16x32_bf16 v[20:23], v[172:175], a[176:179], v[20:23]\n" \
"ds_read_b64_tr_b16 v[140:141], v225 offset:40968\n" \
"v_mfma_f32_16x16x32_bf16 v[52:55], v[168:171], a[164:167], v[52:55]\n" \
"ds_read_b64_tr_b16 v[142:143], v225 offset:41224\n" \
"v_mfma_f32_16x16x32_bf16 v[52:55], v[172:175], a[180:183], v[52:55]\n" \
"v_mfma_f32_16x16x32_bf16 v[84:87], v[168:171], a[168:171], v[84:87]\n" \
"ds_read_b64_tr_b16 v[144:145], v225 offset:33280\n" \
"v_mfma_f32_16x16x32_bf16 v[84:87], v[172:175], a[184:187], v[84:87]\n" \
"ds_read_b64_tr_b16 v[146:147], v225 offset:33536\n" \
"v_mfma_f32_16x16x32_bf16 v[116:119], v[168:171], a[172:175], v[116:119]\n" \
"buffer_load_dwordx4 a[144:147], v227, %[srdz], s50 offen offset:1024 sc1\n" \
"v_mfma_f32_16x16x32_bf16 v[116:119], v[172:175], a[188:191], v[116:119]\n" \
"ds_read_b64_tr_b16 v[148:149], v225 offset:41472\n" \
"v_mfma_f32_16x16x32_bf16 v[24:27], v[176:179], a[160:163], v[24:27]\n" \
"ds_read_b64_tr_b16 v[150:151], v225 offset:41728\n" \
"v_mfma_f32_16x16x32_bf16 v[24:27], v[180:183], a[176:179], v[24:27]\n" \
"v_mfma_f32_16x16x32_bf16 v[56:59], v[176:179], a[164:167], v[56:59]\n" \
"ds_read_b64_tr_b16 v[152:153], v225 offset:33288\n" \
"v_mfma_f32_16x16x32_bf16 v[56:59], v[180:183], a[180:183], v[56:59]\n" \
"ds_read_b64_tr_b16 v[154:155], v225 offset:33544\n" \
"v_mfma_f32_16x16x32_bf16 v[88:91], v[176:179], a[168:171], v[88:91]\n" \
"buffer_load_dwordx4 a[148:151], v219, %[srdz], s50 offen offset:1024 sc1\n" \
"v_mfma_f32_16x16x32_bf16 v[88:91], v[180:183], a[184:187], v[88:91]\n" \
"ds_read_b64_tr_b16 v[156:157], v225 offset:41480\n" \
"v_mfma_f32_16x16x32_bf16 v[120:123], v[176:179], a[172:175], v[120:123]\n" \
"ds_read_b64_tr_b16 v[158:159], v225 offset:41736\n" \
"v_mfma_f32_16x16x32_bf16 v[120:123], v[180:183], a[188:191], v[120:123]\n" \
"v_mfma_f32_16x16x32_bf16 v[28:31], v[184:187], a[160:163], v[28:31]\n" \
"buffer_load_dwordx4 a[152:155], v220, %[srdz], s50 offen offset:1024 sc1\n" \
"v_mfma_f32_16x16x32_bf16 v[28:31], v[188:191], a[176:179], v[28:31]\n" \
"v_mfma_f32_16x16x32_bf16 v[60:63], v[184:187], a[164:167], v[60:63]\n" \
"v_mfma_f32_16x16x32_bf16 v[60:63], v[188:191], a[180:183], v[60:63]\n" \
"v_mfma_f32_16x16x32_bf16 v[92:95], v[184:187], a[168:171], v[92:95]\n" \
"buffer_load_dwordx4 a[156:159], v221, %[srdz], s50 offen offset:1024 sc1\n" \
"v_mfma_f32_16x16x32_bf16 v[92:95], v[188:191], a[184:187], v[92:95]\n" \
"v_mfma_f32_16x16x32_bf16 v[124:127], v[184:187], a[172:175], v[124:127]\n" \
"v_mfma_f32_16x16x32_bf16 v[124:127], v[188:191], a[188:191], v[124:127]\n" \
"s_waitcnt vmcnt(24) lgkmcnt(0)\n" \
"v_mfma_f32_16x16x32_bf16 v[0:3], v[128:131], a[192:195], v[0:3]\n" \
"ds_read_b64_tr_b16 v[160:161], v225 offset:33792\n" \
"v_mfma_f32_16x16x32_bf16 v[0:3], v[132:135], a[208:211], v[0:3]\n" \
"ds_read_b64_tr_b16 v[162:163], v225 offset:34048\n" \
"v_mfma_f32_16x16x32_bf16 v[32:35], v[128:131], a[196:199], v[32:35]\n" \
"s_mov_b32 m0, s45\n" \
"s_nop 0\n" \
"buffer_load_dwordx4 v217, %[srdw], s49 offen offset:2048 lds\n" \
"v_mfma_f32_16x16x32_bf16 v[32:35], v[132:135], a[212:215], v[32:35]\n" \
"ds_read_b64_tr_b16 v[164:165], v225 offset:41984\n" \
"v_mfma_f32_16x16x32_bf16 v[64:67], v[128:131], a[200:203], v[64:67]\n" \
"ds_read_b64_tr_b16 v[166:167], v225 offset:42240\n" \
"v_mfma_f32_16x16x32_bf16 v[64:67], v[132:135], a[216:219], v[64:67]\n" \
"v_mfma_f32_16x16x32_bf16 v[96:99], v[128:131], a[204:207], v[96:99]\n" \
"ds_read_b64_tr_b16 v[168:169], v225 offset:33800\n" \
"v_mfma_f32_16x16x32_bf16 v[96:99], v[132:135], a[220:223], v[96:99]\n" \
"ds_read_b64_tr_b16 v[170:171], v225 offset:34056\n" \
"v_mfma_f32_16x16x32_bf16 v[4:7], v[136:139], a[192:195], v[4:7]\n" \
"buffer_load_dwordx4 v218, %[srdw], s49 offen offset:3072 lds\n" \
"v_mfma_f32_16x16x32_bf16 v[4:7], v[140:143], a[208:211], v[4:7]\n" \
"ds_read_b64_tr_b16 v[172:173], v225 offset:41992\n" \
"v_mfma_f32_16x16x32_bf16 v[36:39], v[136:139], a[196:199], v[36:39]\n" \
"ds_read_b64_tr_b16 v[174:175], v225 offset:42248\n" \
"v_mfma_f32_16x16x32_bf16 v[36:39], v[140:143], a[212:215], v[36:39]\n" \
"v_mfma_f32_16x16x32_bf16 v[68:71], v[136:139], a[200:203], v[68:71]\n" \
"ds_read_b64_tr_b16 v[176:177], v225 offset:34304\n" \
"v_mfma_f32_16x16x32_bf16 v[68:71], v[140:143], a[216:219], v[68:71]\n" \
"ds_read_b64_tr_b16 v[178:179], v225 offset:34560\n" \
"v_mfma_f32_16x16x32_bf16 v[100:103], v[136:139], a[204:207], v[100:103]\n" \
"buffer_load_dwordx4 a[160:163], v227, %[srdz], s50 offen offset:2048 sc1\n" \
"v_mfma_f32_16x16x32_bf16 v[100:103], v[140:143], a[220:223], v[100:103]\n" \
"ds_read_b64_tr_b16 v[180:181], v225 offset:42496\n" \
"v_mfma_f32_16x16x32_bf16 v[8:11], v[144:147], a[192:195], v[8:11]\n" \
"ds_read_b64_tr_b16 v[182:183], v225 offset:42752\n" \
"v_mfma_f32_16x16x32_bf16 v[8:11], v[148:151], a[208:211], v[8:11]\n" \
"v_mfma_f32_16x16x32_bf16 v[40:43], v[144:147], a[196:199], v[40:43]\n" \
"ds_read_b64_tr_b16 v[184:185], v225 offset:34312\n" \
"v_mfma_f32_16x16x32_bf16 v[40:43], v[148:151], a[212:215], v[40:43]\n" \
"ds_read_b64_tr_b16 v[186:187], v225 offset:34568\n" \
"v_mfma_f32_16x16x32_bf16 v[72:75], v[144:147], a[200:203], v[72:75]\n" \
"buffer_load_dwordx4 a[164:167], v219, %[srdz], s50 offen offset:2048 sc1\n" \
"v_mfma_f32_16x16x32_bf16 v[72:75], v[148:151], a[216:219], v[72:75]\n" \
"ds_read_b64_tr_b16 v[188:189], v225 offset:42504\n" \
"v_mfma_f32_16x16x32_bf16 v[104:107], v[144:147], a[204:207], v[104:107]\n" \
"ds_read_b64_tr_b16 v[190:191], v225 offset:42760\n" \
"v_mfma_f32_16x16x32_bf16 v[104:107], v[148:151], a[220:223], v[104:107]\n" \
"v_mfma_f32_16x16x32_bf16 v[12:15], v[152:155], a[192:195], v[12:15]\n" \
"buffer_load_dwordx4 a[168:171], v220, %[srdz], s50 offen offset:2048 sc1\n" \
"v_mfma_f32_16x16x32_bf16 v[12:15], v[156:159], a[208:211], v[12:15]\n" \
"v_mfma_f32_16x16x32_bf16 v[44:47], v[152:155], a[196:199], v[44:47]\n" \
"v_mfma_f32_16x16x32_bf16 v[44:47], v[156:159], a[212:215], v[44:47]\n" \
"v_mfma_f32_16x16x32_bf16 v[76:79], v[152:155], a[200:203], v[76:79]\n" \
"buffer_load_dwordx4 a[172:175], v221, %[srdz], s50 offen offset:2048 sc1\n" \
"v_mfma_f32_16x16x32_bf16 v[76:79], v[156:159], a[216:219], v[76:79]\n" \
"v_mfma_f32_16x16x32_bf16 v[108:111], v[152:155], a[204:207], v[108:111]\n" \
"v_mfma_f32_16x16x32_bf16 v[108:111], v[156:159], a[220:223], v[108:111]\n" \
"s_waitcnt vmcnt(28) lgkmcnt(0)\n" \
"s_barrier\n" \
"v_mfma_f32_16x16x32_bf16 v[16:19], v[160:163], a[192:195], v[16:19]\n" \
"ds_read_b64_tr_b16 v[128:129], v225 offset:49152\n" \
"v_mfma_f32_16x16x32_bf16 v[16:19], v[164:167], a[208:211], v[16:19]\n" \
"ds_read_b64_tr_b16 v[130:131], v225 offset:49408\n" \
"v_mfma_f32_16x16x32_bf16 v[48:51], v[160:163], a[196:199], v[48:51]\n" \
"s_add_u32 s49, s49, s55\n" \
"s_mov_b32 m0, s46\n" \
"s_nop 0\n" \
"buffer_load_dwordx4 v226, %[srdw], s49 offen offset:0 lds\n" \
"v_mfma_f32_16x16x32_bf16 v[48:51], v[164:167], a[212:215], v[48:51]\n" \
"ds_read_b64_tr_b16 v[132:133], v225 offset:57344\n" \
"v_mfma_f32_16x16x32_bf16 v[80:83], v[160:163], a[200:203], v[80:83]\n" \
"ds_read_b64_tr_b16 v[134:135], v225 offset:57600\n" \
"v_mfma_f32_16x16x32_bf16 v[80:83], v[164:167], a[216:219], v[80:83]\n" \
"v_mfma_f32_16x16x32_bf16 v[112:115], v[160:163], a[204:207], v[112:115]\n" \
"ds_read_b64_tr_b16 v[136:137], v225 offset:49160\n" \
"v_mfma_f32_16x16x32_bf16 v[112:115], v[164:167], a[220:223], v[112:115]\n" \
"ds_read_b64_tr_b16 v[138:139], v225 offset:49416\n" \
"v_mfma_f32_16x16x32_bf16 v[20:23], v[168:171], a[192:195], v[20:23]\n" \
"buffer_load_dwordx4 v216, %[srdw], s49 offen offset:1024 lds\n" \
"v_mfma_f32_16x16x32_bf16 v[20:23], v[172:175], a[208:211], v[20:23]\n" \
"ds_read_b64_tr_b16 v[140:141], v225 offset:57352\n" \
"v_mfma_f32_16x16x32_bf16 v[52:55], v[168:171], a[196:199], v[52:55]\n" \
"ds_read_b64_tr_b16 v[142:143], v225 offset:57608\n" \
"v_mfma_f32_16x16x32_bf16 v[52:55], v[172:175], a[212:215], v[52:55]\n" \
"v_mfma_f32_16x16x32_bf16 v[84:87], v[168:171], a[200:203], v[84:87]\n" \
"ds_read_b64_tr_b16 v[144:145], v225 offset:49664\n" \
"v_mfma_f32_16x16x32_bf16 v[84:87], v[172:175], a[216:219], v[84:87]\n" \
"ds_read_b64_tr_b16 v[146:147], v225 offset:49920\n" \
"v_mfma_f32_16x16x32_bf16 v[116:119], v[168:171], a[204:207], v[116:119]\n" \
"buffer_load_dwordx4 a[176:179], v227, %[srdz], s50 offen offset:3072 sc1\n" \
"v_mfma_f32_16x16x32_bf16 v[116:119], v[172:175], a[220:223], v[116:119]\n" \
"ds_read_b64_tr_b16 v[148:149], v225 offset:57856\n" \
"v_mfma_f32_16x16x32_bf16 v[24:27], v[176:179], a[192:195], v[24:27]\n" \
"ds_read_b64_tr_b16 v[150:151], v225 offset:58112\n" \
"v_mfma_f32_16x16x32_bf16 v[24:27], v[180:183], a[208:211], v[24:27]\n" \
"v_mfma_f32_16x16x32_bf16 v[56:59], v[176:179], a[196:199], v[56:59]\n" \
"ds_read_b64_tr_b16 v[152:153], v225 offset:49672\n" \
"v_mfma_f32_16x16x32_bf16 v[56:59], v[180:183], a[212:215], v[56:59]\n" \
"ds_read_b64_tr_b16 v[154:155], v225 offset:49928\n" \
"v_mfma_f32_16x16x32_bf16 v[88:91], v[176:179], a[200:203], v[88:91]\n" \
"buffer_load_dwordx4 a[180:183], v219, %[srdz], s50 offen offset:3072 sc1\n" \
"v_mfma_f32_16x16x32_bf16 v[88:91], v[180:183], a[216:219], v[88:91]\n" \
"ds_read_b64_tr_b16 v[156:157], v225 offset:57864\n" \
"v_mfma_f32_16x16x32_bf16 v[120:123], v[176:179], a[204:207], v[120:123]\n" \
"ds_read_b64_tr_b16 v[158:159], v225 offset:58120\n" \
"v_mfma_f32_16x16x32_bf16 v[120:123], v[180:183], a[220:223], v[120:123]\n" \
"v_mfma_f32_16x16x32_bf16 v[28:31], v[184:187], a[192:195], v[28:31]\n" \
"buffer_load_dwordx4 a[184:187], v220, %[srdz], s50 offen offset:3072 sc1\n" \
"v_mfma_f32_16x16x32_bf16 v[28:31], v[188:191], a[208:211], v[28:31]\n" \
"v_mfma_f32_16x16x32_bf16 v[60:63], v[184:187], a[196:199], v[60:63]\n" \
"v_mfma_f32_16x16x32_bf16 v[60:63], v[188:191], a[212:215], v[60:63]\n" \
"v_mfma_f32_16x16x32_bf16 v[92:95], v[184:187], a[200:203], v[92:95]\n" \
"buffer_load_dwordx4 a[188:191], v221, %[srdz], s50 offen offset:3072 sc1\n" \
"v_mfma_f32_16x16x32_bf16 v[92:95], v[188:191], a[216:219], v[92:95]\n" \
"v_mfma_f32_16x16x32_bf16 v[124:127], v[184:187], a[204:207], v[124:127]\n" \
"v_mfma_f32_16x16x32_bf16 v[124:127], v[188:191], a[220:223], v[124:127]\n" \
"s_waitcnt vmcnt(24) lgkmcnt(0)\n" \
"v_mfma_f32_16x16x32_bf16 v[0:3], v[128:131], a[224:227], v[0:3]\n" \
"ds_read_b64_tr_b16 v[160:161], v225 offset:50176\n" \
"v_mfma_f32_16x16x32_bf16 v[0:3], v[132:135], a[240:243], v[0:3]\n" \
"ds_read_b64_tr_b16 v[162:163], v225 offset:50432\n" \
"v_mfma_f32_16x16x32_bf16 v[32:35], v[128:131], a[228:231], v[32:35]\n" \
"s_mov_b32 m0, s46\n" \
"s_nop 0\n" \
"buffer_load_dwordx4 v217, %[srdw], s49 offen offset:2048 lds\n" \
"v_mfma_f32_16x16x32_bf16 v[32:35], v[132:135], a[244:247], v[32:35]\n" \
"ds_read_b64_tr_b16 v[164:165], v225 offset:58368\n" \
"v_mfma_f32_16x16x32_bf16 v[64:67], v[128:131], a[232:235], v[64:67]\n" \
"ds_read_b64_tr_b16 v[166:167], v225 offset:58624\n" \
"v_mfma_f32_16x16x32_bf16 v[64:67], v[132:135], a[248:251], v[64:67]\n" \
"v_mfma_f32_16x16x32_bf16 v[96:99], v[128:131], a[236:239], v[96:99]\n" \
"ds_read_b64_tr_b16 v[168:169], v225 offset:50184\n" \
"v_mfma_f32_16x16x32_bf16 v[96:99], v[132:135], a[252:255], v[96:99]\n" \
"ds_read_b64_tr_b16 v[170:171], v225 offset:50440\n" \
"v_mfma_f32_16x16x32_bf16 v[4:7], v[136:139], a[224:227], v[4:7]\n" \
"buffer_load_dwordx4 v218, %[srdw], s49 offen offset:3072 lds\n" \
"v_mfma_f32_16x16x32_bf16 v[4:7], v[140:143], a[240:243], v[4:7]\n" \
"ds_read_b64_tr_b16 v[172:173], v225 offset:58376\n" \
"v_mfma_f32_16x16x32_bf16 v[36:39], v[136:139], a[228:231], v[36:39]\n" \
"ds_read_b64_tr_b16 v[174:175], v225 offset:58632\n" \
"v_mfma_f32_16x16x32_bf16 v[36:39], v[140:143], a[244:247], v[36:39]\n" \
"v_mfma_f32_16x16x32_bf16 v[68:71], v[136:139], a[232:235], v[68:71]\n" \
"ds_read_b64_tr_b16 v[176:177], v225 offset:50688\n" \
"v_mfma_f32_16x16x32_bf16 v[68:71], v[140:143], a[248:251], v[68:71]\n" \
"ds_read_b64_tr_b16 v[178:179], v225 offset:50944\n" \
"v_mfma_f32_16x16x32_bf16 v[100:103], v[136:139], a[236:239], v[100:103]\n" \
"s_add_u32 s50, s50, s53\n" \
"buffer_load_dwordx4 a[192:195], v227, %[srdz], s50 offen offset:0 sc1\n" \
"v_mfma_f32_16x16x32_bf16 v[100:103], v[140:143], a[252:255], v[100:103]\n" \
"ds_read_b64_tr_b16 v[180:181], v225 offset:58880\n" \
"v_mfma_f32_16x16x32_bf16 v[8:11], v[144:147], a[224:227], v[8:11]\n" \
"ds_read_b64_tr_b16 v[182:183], v225 offset:59136\n" \
"v_mfma_f32_16x16x32_bf16 v[8:11], v[148:151], a[240:243], v[8:11]\n" \
"v_mfma_f32_16x16x32_bf16 v[40:43], v[144:147], a[228:231], v[40:43]\n" \
"ds_read_b64_tr_b16 v[184:185], v225 offset:50696\n" \
"v_mfma_f32_16x16x32_bf16 v[40:43], v[148:151], a[244:247], v[40:43]\n" \
"ds_read_b64_tr_b16 v[186:187], v225 offset:50952\n" \
"v_mfma_f32_16x16x32_bf16 v[72:75], v[144:147], a[232:235], v[72:75]\n" \
"buffer_load_dwordx4 a[196:199], v219, %[srdz], s50 offen offset:0 sc1\n" \
"v_mfma_f32_16x16x32_bf16 v[72:75], v[148:151], a[248:251], v[72:75]\n" \
"ds_read_b64_tr_b16 v[188:189], v225 offset:58888\n" \
"v_mfma_f32_16x16x32_bf16 v[104:107], v[144:147], a[236:239], v[104:107]\n" \
"ds_read_b64_tr_b16 v[190:191], v225 offset:59144\n" \
"v_mfma_f32_16x16x32_bf16 v[104:107], v[148:151], a[252:255], v[104:107]\n" \
"v_mfma_f32_16x16x32_bf16 v[12:15], v[152:155], a[224:227], v[12:15]\n" \
"buffer_load_dwordx4 a[200:203], v220, %[srdz], s50 offen offset:0 sc1\n" \
"v_mfma_f32_16x16x32_bf16 v[12:15], v[156:159], a[240:243], v[12:15]\n" \
"v_mfma_f32_16x16x32_bf16 v[44:47], v[152:155], a[228:231], v[44:47]\n" \
"buffer_load_dword v237, v236, %[srdw], s60 offen\n" \
"s_add_u32 s60, s60, 0x200000\n" \
"v_mfma_f32_16x16x32_bf16 v[44:47], v[156:159], a[244:247], v[44:47]\n" \
"v_mfma_f32_16x16x32_bf16 v[76:79], v[152:155], a[232:235], v[76:79]\n" \
"buffer_load_dwordx4 a[204:207], v221, %[srdz], s50 offen offset:0 sc1\n" \
"v_mfma_f32_16x16x32_bf16 v[76:79], v[156:159], a[248:251], v[76:79]\n" \
".if . - adaln_body != 4096\n" \
".error \"size model\"\n" \
".endif\n" \
"Lk0_%=:\n" \
"s_cbranch_execz Lk1_%=\n" \
"s_nop 0\n" \
"v_mfma_f32_16x16x32_bf16 v[108:111], v[152:155], a[236:239], v[108:111]\n" \
"v_mfma_f32_16x16x32_bf16 v[108:111], v[156:159], a[252:255], v[108:111]\n" \
"s_waitcnt vmcnt(29) lgkmcnt(0)\n" \
"s_barrier\n" \
"v_mfma_f32_16x16x32_bf16 v[16:19], v[160:163], a[224:227], v[16:19]\n" \
"ds_read_b64_tr_b16 v[128:129], v225 offset:0\n" \
"v_mfma_f32_16x16x32_bf16 v[16:19], v[164:167], a[240:243], v[16:19]\n" \
"ds_read_b64_tr_b16 v[130:131], v225 offset:256\n" \
"v_mfma_f32_16x16x32_bf16 v[48:51], v[160:163], a[228:231], v[48:51]\n" \
"s_add_u32 s49, s49, s55\n" \
"s_mov_b32 m0, s47\n" \
"s_nop 0\n" \
"buffer_load_dwordx4 v226, %[srdw], s49 offen offset:0 lds\n" \
"v_mfma_f32_16x16x32_bf16 v[48:51], v[164:167], a[244:247], v[48:51]\n" \
"ds_read_b64_tr_b16 v[132:133], v225 offset:8192\n" \
"v_mfma_f32_16x16x32_bf16 v[80:83], v[160:163], a[232:235], v[80:83]\n" \
"ds_read_b64_tr_b16 v[134:135], v225 offset:8448\n" \
"v_mfma_f32_16x16x32_bf16 v[80:83], v[164:167], a[248:251], v[80:83]\n" \
"v_mfma_f32_16x16x32_bf16 v[112:115], v[160:163], a[236:239], v[112:115]\n" \
"ds_read_b64_tr_b16 v[136:137], v225 offset:8\n" \
"v_mfma_f32_16x16x32_bf16 v[112:115], v[164:167], a[252:255], v[112:115]\n" \
"ds_read_b64_tr_b16 v[138:139], v225 offset:264\n" \
"v_mfma_f32_16x16x32_bf16 v[20:23], v[168:171], a[224:227], v[20:23]\n" \
"buffer_load_dwordx4 v216, %[srdw], s49 offen offset:1024 lds\n" \
"v_mfma_f32_16x16x32_bf16 v[20:23], v[172:175], a[240:243], v[20:23]\n" \
"ds_read_b64_tr_b16 v[140:141], v225 offset:8200\n" \
"v_mfma_f32_16x16x32_bf16 v[52:55], v[168:171], a[228:231], v[52:55]\n" \
"ds_read_b64_tr_b16 v[142:143], v225 offset:8456\n" \
"v_mfma_f32_16x16x32_bf16 v[52:55], v[172:175], a[244:247], v[52:55]\n" \
"v_mfma_f32_16x16x32_bf16 v[84:87], v[168:171], a[232:235], v[84:87]\n" \
"ds_read_b64_tr_b16 v[144:145], v225 offset:512\n" \
"v_mfma_f32_16x16x32_bf16 v[84:87], v[172:175], a[248:251], v[84:87]\n" \
"ds_read_b64_tr_b16 v[146:147], v225 offset:768\n" \
"v_mfma_f32_16x16x32_bf16 v[116:119], v[168:171], a[236:239], v[116:119]\n" \
"buffer_load_dwordx4 a[208:211], v227, %[srdz], s50 offen offset:1024 sc1\n" \
"v_mfma_f32_16x16x32_bf16 v[116:119], v[172:175], a[252:255], v[116:119]\n" \
"ds_read_b64_tr_b16 v[148:149], v225 offset:8704\n" \
"v_mfma_f32_16x16x32_bf16 v[24:27], v[176:179], a[224:227], v[24:27]\n" \
"ds_read_b64_tr_b16 v[150:151], v225 offset:8960\n" \
"v_mfma_f32_16x16x32_bf16 v[24:27], v[180:183], a[240:243], v[24:27]\n" \
"v_mfma_f32_16x16x32_bf16 v[56:59], v[176:179], a[228:231], v[56:59]\n" \
"ds_read_b64_tr_b16 v[152:153], v225 offset:520\n" \
"v_mfma_f32_16x16x32_bf16 v[56:59], v[180:183], a[244:247], v[56:59]\n" \
"ds_read_b64_tr_b16 v[154:155], v225 offset:776\n" \
"v_mfma_f32_16x16x32_bf16 v[88:91], v[176:179], a[232:235], v[88:91]\n" \
"buffer_load_dwordx4 a[212:215], v219, %[srdz], s50 offen offset:1024 sc1\n" \
"v_mfma_f32_16x16x32_bf16 v[88:91], v[180:183], a[248:251], v[88:91]\n" \
"ds_read_b64_tr_b16 v[156:157], v225 offset:8712\n" \
"v_mfma_f32_16x16x32_bf16 v[120:123], v[176:179], a[236:239], v[120:123]\n" \
"ds_read_b64_tr_b16 v[158:159], v225 offset:8968\n" \
"v_mfma_f32_16x16x32_bf16 v[120:123], v[180:183], a[252:255], v[120:123]\n" \
"v_mfma_f32_16x16x32_bf16 v[28:31], v[184:187], a[224:227], v[28:31]\n" \
"buffer_load_dwordx4 a[216:219], v220, %[srdz], s50 offen offset:1024 sc1\n" \
"v_mfma_f32_16x16x32_bf16 v[28:31], v[188:191], a[240:243], v[28:31]\n" \
"v_mfma_f32_16x16x32_bf16 v[60:63], v[184:187], a[228:231], v[60:63]\n" \
"v_mfma_f32_16x16x32_bf16 v[60:63], v[188:191], a[244:247], v[60:63]\n" \
"v_mfma_f32_16x16x32_bf16 v[92:95], v[184:187], a[232:235], v[92:95]\n" \
"buffer_load_dwordx4 a[220:223], v221, %[srdz], s50 offen offset:1024 sc1\n" \
"v_mfma_f32_16x16x32_bf16 v[92:95], v[188:191], a[248:251], v[92:95]\n" \
"v_mfma_f32_16x16x32_bf16 v[124:127], v[184:187], a[236:239], v[124:127]\n" \
"v_mfma_f32_16x16x32_bf16 v[124:127], v[188:191], a[252:255], v[124:127]\n" \
"s_waitcnt vmcnt(25) lgkmcnt(0)\n" \
"s_sub_u32 s51, s51, 1\n" \
"s_cmp_lg_u32 s51, 0\n" \
"s_cbranch_scc1 Lq_%=\n" \
"s_nop 0\n" \
"v_mfma_f32_16x16x32_bf16 v[0:3], v[128:131], a[128:131], v[0:3]\n" \
"ds_read_b64_tr_b16 v[160:161], v225 offset:1024\n" \
"v_mfma_f32_16x16x32_bf16 v[0:3], v[132:135], a[144:147], v[0:3]\n" \
"ds_read_b64_tr_b16 v[162:163], v225 offset:1280\n" \
"v_mfma_f32_16x16x32_bf16 v[32:35], v[128:131], a[132:135], v[32:35]\n" \
"s_mov_b32 m0, s47\n" \
"s_nop 0\n" \
"buffer_load_dwordx4 v217, %[srdw], s49 offen offset:2048 lds\n" \
"v_mfma_f32_16x16x32_bf16 v[32:35], v[132:135], a[148:151], v[32:35]\n" \
"ds_read_b64_tr_b16 v[164:165], v225 offset:9216\n" \
"v_mfma_f32_16x16x32_bf16 v[64:67], v[128:131], a[136:139], v[64:67]\n" \
"ds_read_b64_tr_b16 v[166:167], v225 offset:9472\n" \
"v_mfma_f32_16x16x32_bf16 v[64:67], v[132:135], a[152:155], v[64:67]\n" \
"v_mfma_f32_16x16x32_bf16 v[96:99], v[128:131], a[140:143], v[96:99]\n" \
"ds_read_b64_tr_b16 v[168:169], v225 offset:1032\n" \
"v_mfma_f32_16x16x32_bf16 v[96:99], v[132:135], a[156:159], v[96:99]\n" \
"ds_read_b64_tr_b16 v[170:171], v225 offset:1288\n" \
"v_mfma_f32_16x16x32_bf16 v[4:7], v[136:139], a[128:131], v[4:7]\n" \
"buffer_load_dwordx4 v218, %[srdw], s49 offen offset:3072 lds\n" \
"v_mfma_f32_16x16x32_bf16 v[4:7], v[140:143], a[144:147], v[4:7]\n" \
"ds_read_b64_tr_b16 v[172:173], v225 offset:9224\n" \
"v_mfma_f32_16x16x32_bf16 v[36:39], v[136:139], a[132:135], v[36:39]\n" \
"ds_read_b64_tr_b16 v[174:175], v225 offset:9480\n" \
"v_mfma_f32_16x16x32_bf16 v[36:39], v[140:143], a[148:151], v[36:39]\n" \
"v_mfma_f32_16x16x32_bf16 v[68:71], v[136:139], a[136:139], v[68:71]\n" \
"ds_read_b64_tr_b16 v[176:177], v225 offset:1536\n" \
"v_mfma_f32_16x16x32_bf16 v[68:71], v[140:143], a[152:155], v[68:71]\n" \
"ds_read_b64_tr_b16 v[178:179], v225 offset:1792\n" \
"v_mfma_f32_16x16x32_bf16 v[100:103], v[136:139], a[140:143], v[100:103]\n" \
"buffer_load_dwordx4 a[224:227], v227, %[srdz], s50 offen offset:2048 sc1\n" \
"v_mfma_f32_16x16x32_bf16 v[100:103], v[140:143], a[156:159], v[100:103]\n" \
"ds_read_b64_tr_b16 v[180:181], v225 offset:9728\n" \
"v_mfma_f32_16x16x32_bf16 v[8:11], v[144:147], a[128:131], v[8:11]\n" \
"ds_read_b64_tr_b16 v[182:183], v225 offset:9984\n" \
"v_mfma_f32_16x16x32_bf16 v[8:11], v[148:151], a[144:147], v[8:11]\n" \
"v_mfma_f32_16x16x32_bf16 v[40:43], v[144:147], a[132:135], v[40:43]\n" \
"ds_read_b64_tr_b16 v[184:185], v225 offset:1544\n" \
"v_mfma_f32_16x16x32_bf16 v[40:43], v[148:151], a[148:151], v[40:43]\n" \
"ds_read_b64_tr_b16 v[186:187], v225 offset:1800\n" \
"v_mfma_f32_16x16x32_bf16 v[72:75], v[144:147], a[136:139], v[72:75]\n" \
"buffer_load_dwordx4 a[228:231], v219, %[srdz], s50 offen offset:2048 sc1\n" \
"v_mfma_f32_16x16x32_bf16 v[72:75], v[148:151], a[152:155], v[72:75]\n" \
"ds_read_b64_tr_b16 v[188:189], v225 offset:9736\n" \
"v_mfma_f32_16x16x32_bf16 v[104:107], v[144:147], a[140:143], v[104:107]\n" \
"ds_read_b64_tr_b16 v[190:191], v225 offset:9992\n" \
"v_mfma_f32_16x16x32_bf16 v[104:107], v[148:151], a[156:159], v[104:107]\n" \
"v_mfma_f32_16x16x32_bf16 v[12:15], v[152:155], a[128:131], v[12:15]\n" \
"buffer_load_dwordx4 a[232:235], v220, %[srdz], s50 offen offset:2048 sc1\n" \
"v_mfma_f32_16x16x32_bf16 v[12:15], v[156:159], a[144:147], v[12:15]\n" \
"v_mfma_f32_16x16x32_bf16 v[44:47], v[152:155], a[132:135], v[44:47]\n" \
"v_mfma_f32_16x16x32_bf16 v[44:47], v[156:159], a[148:151], v[44:47]\n" \
"v_mfma_f32_16x16x32_bf16 v[76:79], v[152:155], a[136:139], v[76:79]\n" \
"buffer_load_dwordx4 a[236:239], v221, %[srdz], s50 offen offset:2048 sc1\n" \
"v_mfma_f32_16x16x32_bf16 v[76:79], v[156:159], a[152:155], v[76:79]\n" \
"v_mfma_f32_16x16x32_bf16 v[108:111], v[152:155], a[140:143], v[108:111]\n" \
"v_mfma_f32_16x16x32_bf16 v[108:111], v[156:159], a[156:159], v[108:111]\n" \
"s_waitcnt vmcnt(29) lgkmcnt(0)\n" \
"s_barrier\n" \
"v_mfma_f32_16x16x32_bf16 v[16:19], v[160:163], a[128:131], v[16:19]\n" \
"ds_read_b64_tr_b16 v[128:129], v225 offset:16384\n" \
"v_mfma_f32_16x16x32_bf16 v[16:19], v[164:167], a[144:147], v[16:19]\n" \
"ds_read_b64_tr_b16 v[130:131], v225 offset:16640\n" \
"v_mfma_f32_16x16x32_bf16 v[48:51], v[160:163], a[132:135], v[48:51]\n" \
"s_mov_b32 s48, 16384\n" \
"s_mov_b32 m0, s44\n" \
"s_nop 0\n" \
"buffer_load_dwordx4 %[vd0], %[srdv], s48 offen offset:0 lds\n" \
"v_mfma_f32_16x16x32_bf16 v[48:51], v[164:167], a[148:151], v[48:51]\n" \
"ds_read_b64_tr_b16 v[132:133], v225 offset:24576\n" \
"v_mfma_f32_16x16x32_bf16 v[80:83], v[160:163], a[136:139], v[80:83]\n" \
"ds_read_b64_tr_b16 v[134:135], v225 offset:24832\n" \
"v_mfma_f32_16x16x32_bf16 v[80:83], v[164:167], a[152:155], v[80:83]\n" \
"v_mfma_f32_16x16x32_bf16 v[112:115], v[160:163], a[140:143], v[112:115]\n" \
"ds_read_b64_tr_b16 v[136:137], v225 offset:16392\n" \
"v_mfma_f32_16x16x32_bf16 v[112:115], v[164:167], a[156:159], v[112:115]\n" \
"ds_read_b64_tr_b16 v[138:139], v225 offset:16648\n" \
"v_mfma_f32_16x16x32_bf16 v[20:23], v[168:171], a[128:131], v[20:23]\n" \
"buffer_load_dwordx4 v215, %[srdv], s48 offen offset:1024 lds\n" \
"v_mfma_f32_16x16x32_bf16 v[20:23], v[172:175], a[144:147], v[20:23]\n" \
"ds_read_b64_tr_b16 v[140:141], v225 offset:24584\n" \
"v_mfma_f32_16x16x32_bf16 v[52:55], v[168:171], a[132:135], v[52:55]\n" \
"ds_read_b64_tr_b16 v[142:143], v225 offset:24840\n" \
"v_mfma_f32_16x16x32_bf16 v[52:55], v[172:175], a[148:151], v[52:55]\n" \
"v_mfma_f32_16x16x32_bf16 v[84:87], v[168:171], a[136:139], v[84:87]\n" \
"ds_read_b64_tr_b16 v[144:145], v225 offset:16896\n" \
"v_mfma_f32_16x16x32_bf16 v[84:87], v[172:175], a[152:155], v[84:87]\n" \
"ds_read_b64_tr_b16 v[146:147], v225 offset:17152\n" \
"v_mfma_f32_16x16x32_bf16 v[116:119], v[168:171], a[140:143], v[116:119]\n" \
"buffer_load_dwordx4 a[240:243], v227, %[srdz], s50 offen offset:3072 sc1\n" \
"v_mfma_f32_16x16x32_bf16 v[116:119], v[172:175], a[156:159], v[116:119]\n" \
"ds_read_b64_tr_b16 v[148:149], v225 offset:25088\n" \
"v_mfma_f32_16x16x32_bf16 v[24:27], v[176:179], a[128:131], v[24:27]\n" \
"ds_read_b64_tr_b16 v[150:151], v225 offset:25344\n" \
"v_mfma_f32_16x16x32_bf16 v[24:27], v[180:183], a[144:147], v[24:27]\n" \
"v_mfma_f32_16x16x32_bf16 v[56:59], v[176:179], a[132:135], v[56:59]\n" \
"ds_read_b64_tr_b16 v[152:153], v225 offset:16904\n" \
"v_mfma_f32_16x16x32_bf16 v[56:59], v[180:183], a[148:151], v[56:59]\n" \
"ds_read_b64_tr_b16 v[154:155], v225 offset:17160\n" \
"v_mfma_f32_16x16x32_bf16 v[88:91], v[176:179], a[136:139], v[88:91]\n" \
"buffer_load_dwordx4 a[244:247], v219, %[srdz], s50 offen offset:3072 sc1\n" \
"v_mfma_f32_16x16x32_bf16 v[88:91], v[180:183], a[152:155], v[88:91]\n" \
"ds_read_b64_tr_b16 v[156:157], v225 offset:25096\n" \
"v_mfma_f32_16x16x32_bf16 v[120:123], v[176:179], a[140:143], v[120:123]\n" \
"ds_read_b64_tr_b16 v[158:159], v225 offset:25352\n" \
"v_mfma_f32_16x16x32_bf16 v[120:123], v[180:183], a[156:159], v[120:123]\n" \
"v_mfma_f32_16x16x32_bf16 v[28:31], v[184:187], a[128:131], v[28:31]\n" \
"buffer_load_dwordx4 a[248:251], v220, %[srdz], s50 offen offset:3072 sc1\n" \
"v_mfma_f32_16x16x32_bf16 v[28:31], v[188:191], a[144:147], v[28:31]\n" \
"v_mfma_f32_16x16x32_bf16 v[60:63], v[184:187], a[132:135], v[60:63]\n" \
"v_mfma_f32_16x16x32_bf16 v[60:63], v[188:191], a[148:151], v[60:63]\n" \
"v_mfma_f32_16x16x32_bf16 v[92:95], v[184:187], a[136:139], v[92:95]\n" \
"buffer_load_dwordx4 a[252:255], v221, %[srdz], s50 offen offset:3072 sc1\n" \
"v_mfma_f32_16x16x32_bf16 v[92:95], v[188:191], a[152:155], v[92:95]\n" \
"v_mfma_f32_16x16x32_bf16 v[124:127], v[184:187], a[140:143], v[124:127]\n" \
"v_mfma_f32_16x16x32_bf16 v[124:127], v[188:191], a[156:159], v[124:127]\n" \
"s_waitcnt vmcnt(25) lgkmcnt(0)\n" \
"v_mfma_f32_16x16x32_bf16 v[0:3], v[128:131], a[160:163], v[0:3]\n" \
"ds_read_b64_tr_b16 v[160:161], v225 offset:17408\n" \
"v_mfma_f32_16x16x32_bf16 v[0:3], v[132:135], a[176:179], v[0:3]\n" \
"ds_read_b64_tr_b16 v[162:163], v225 offset:17664\n" \
"v_mfma_f32_16x16x32_bf16 v[32:35], v[128:131], a[164:167], v[32:35]\n" \
"s_mov_b32 s48, 16384\n" \
"s_mov_b32 m0, s44\n" \
"s_nop 0\n" \
"buffer_load_dwordx4 %[vd0], %[srdv], s48 offen offset:2048 lds\n" \
"v_mfma_f32_16x16x32_bf16 v[32:35], v[132:135], a[180:183], v[32:35]\n" \
"ds_read_b64_tr_b16 v[164:165], v225 offset:25600\n" \
"v_mfma_f32_16x16x32_bf16 v[64:67], v[128:131], a[168:171], v[64:67]\n" \
"ds_read_b64_tr_b16 v[166:167], v225 offset:25856\n" \
"v_mfma_f32_16x16x32_bf16 v[64:67], v[132:135], a[184:187], v[64:67]\n" \
"v_mfma_f32_16x16x32_bf16 v[96:99], v[128:131], a[172:175], v[96:99]\n" \
"ds_read_b64_tr_b16 v[168:169], v225 offset:17416\n" \
"v_mfma_f32_16x16x32_bf16 v[96:99], v[132:135], a[188:191], v[96:99]\n" \
"ds_read_b64_tr_b16 v[170:171], v225 offset:17672\n" \
"v_mfma_f32_16x16x32_bf16 v[4:7], v[136:139], a[160:163], v[4:7]\n" \
"buffer_load_dwordx4 v215, %[srdv], s48 offen offset:3072 lds\n" \
"v_mfma_f32_16x16x32_bf16 v[4:7], v[140:143], a[176:179], v[4:7]\n" \
"ds_read_b64_tr_b16 v[172:173], v225 offset:25608\n" \
"v_mfma_f32_16x16x32_bf16 v[36:39], v[136:139], a[164:167], v[36:39]\n" \
"ds_read_b64_tr_b16 v[174:175], v225 offset:25864\n" \
"v_mfma_f32_16x16x32_bf16 v[36:39], v[140:143], a[180:183], v[36:39]\n" \
"v_mfma_f32_16x16x32_bf16 v[68:71], v[136:139], a[168:171], v[68:71]\n" \
"ds_read_b64_tr_b16 v[176:177], v225 offset:17920\n" \
"v_mfma_f32_16x16x32_bf16 v[68:71], v[140:143], a[184:187], v[68:71]\n" \
"ds_read_b64_tr_b16 v[178:179], v225 offset:18176\n" \
"v_mfma_f32_16x16x32_bf16 v[100:103], v[136:139], a[172:175], v[100:103]\n" \
"s_mov_b32 s48, 0\n" \
"s_mov_b32 m0, s40\n" \
"s_nop 0\n" \
"buffer_load_dwordx4 %[kd0], %[srdk], s48 offen offset:0 lds\n" \
"v_mfma_f32_16x16x32_bf16 v[100:103], v[140:143], a[188:191], v[100:103]\n" \
"ds_read_b64_tr_b16 v[180:181], v225 offset:26112\n" \
"v_mfma_f32_16x16x32_bf16 v[8:11], v[144:147], a[160:163], v[8:11]\n" \
"ds_read_b64_tr_b16 v[182:183], v225 offset:26368\n" \
"v_mfma_f32_16x16x32_bf16 v[8:11], v[148:151], a[176:179], v[8:11]\n" \
"v_mfma_f32_16x16x32_bf16 v[40:43], v[144:147], a[164:167], v[40:43]\n" \
"ds_read_b64_tr_b16 v[184:185], v225 offset:17928\n" \
"v_mfma_f32_16x16x32_bf16 v[40:43], v[148:151], a[180:183], v[40:43]\n" \
"ds_read_b64_tr_b16 v[186:187], v225 offset:18184\n" \
"v_mfma_f32_16x16x32_bf16 v[72:75], v[144:147], a[168:171], v[72:75]\n" \
"buffer_load_dwordx4 v212, %[srdk], s48 offen offset:1024 lds\n" \
"v_mfma_f32_16x16x32_bf16 v[72:75], v[148:151], a[184:187], v[72:75]\n" \
"ds_read_b64_tr_b16 v[188:189], v225 offset:26120\n" \
"v_mfma_f32_16x16x32_bf16 v[104:107], v[144:147], a[172:175], v[104:107]\n" \
"ds_read_b64_tr_b16 v[190:191], v225 offset:26376\n" \
"v_mfma_f32_16x16x32_bf16 v[104:107], v[148:151], a[188:191], v[104:107]\n" \
"v_mfma_f32_16x16x32_bf16 v[12:15], v[152:155], a[160:163], v[12:15]\n" \
"buffer_load_dwordx4 v213, %[srdk], s48 offen offset:2048 lds\n" \
"v_mfma_f32_16x16x32_bf16 v[12:15], v[156:159], a[176:179], v[12:15]\n" \
"v_mfma_f32_16x16x32_bf16 v[44:47], v[152:155], a[164:167], v[44:47]\n" \
"v_mfma_f32_16x16x32_bf16 v[44:47], v[156:159], a[180:183], v[44:47]\n" \
"v_mfma_f32_16x16x32_bf16 v[76:79], v[152:155], a[168:171], v[76:79]\n" \
"buffer_load_dwordx4 v214, %[srdk], s48 offen offset:3072 lds\n" \
"v_mfma_f32_16x16x32_bf16 v[76:79], v[156:159], a[184:187], v[76:79]\n" \
"v_mfma_f32_16x16x32_bf16 v[108:111], v[152:155], a[172:175], v[108:111]\n" \
"v_mfma_f32_16x16x32_bf16 v[108:111], v[156:159], a[188:191], v[108:111]\n" \
"s_waitcnt vmcnt(29) lgkmcnt(0)\n" \
"s_barrier\n" \
"v_mfma_f32_16x16x32_bf16 v[16:19], v[160:163], a[160:163], v[16:19]\n" \
"ds_read_b64_tr_b16 v[128:129], v225 offset:32768\n" \
"v_mfma_f32_16x16x32_bf16 v[16:19], v[164:167], a[176:179], v[16:19]\n" \
"ds_read_b64_tr_b16 v[130:131], v225 offset:33024\n" \
"v_mfma_f32_16x16x32_bf16 v[48:51], v[160:163], a[164:167], v[48:51]\n" \
"s_mov_b32 s48, 32768\n" \
"s_mov_b32 m0, s45\n" \
"s_nop 0\n" \
"buffer_load_dwordx4 %[vd0], %[srdv], s48 offen offset:0 lds\n" \
"v_mfma_f32_16x16x32_bf16 v[48:51], v[164:167], a[180:183], v[48:51]\n" \
"ds_read_b64_tr_b16 v[132:133], v225 offset:40960\n" \
"v_mfma_f32_16x16x32_bf16 v[80:83], v[160:163], a[168:171], v[80:83]\n" \
"ds_read_b64_tr_b16 v[134:135], v225 offset:41216\n" \
"v_mfma_f32_16x16x32_bf16 v[80:83], v[164:167], a[184:187], v[80:83]\n" \
"v_mfma_f32_16x16x32_bf16 v[112:115], v[160:163], a[172:175], v[112:115]\n" \
"ds_read_b64_tr_b16 v[136:137], v225 offset:32776\n" \
"v_mfma_f32_16x16x32_bf16 v[112:115], v[164:167], a[188:191], v[112:115]\n" \
"ds_read_b64_tr_b16 v[138:139], v225 offset:33032\n" \
"v_mfma_f32_16x16x32_bf16 v[20:23], v[168:171], a[160:163], v[20:23]\n" \
"buffer_load_dwordx4 v215, %[srdv], s48 offen offset:1024 lds\n" \
"v_mfma_f32_16x16x32_bf16 v[20:23], v[172:175], a[176:179], v[20:23]\n" \
"ds_read_b64_tr_b16 v[140:141], v225 offset:40968\n" \
"v_mfma_f32_16x16x32_bf16 v[52:55], v[168:171], a[164:167], v[52:55]\n" \
"ds_read_b64_tr_b16 v[142:143], v225 offset:41224\n" \
"v_mfma_f32_16x16x32_bf16 v[52:55], v[172:175], a[180:183], v[52:55]\n" \
"v_mfma_f32_16x16x32_bf16 v[84:87], v[168:171], a[168:171], v[84:87]\n" \
"ds_read_b64_tr_b16 v[144:145], v225 offset:33280\n" \
"v_mfma_f32_16x16x32_bf16 v[84:87], v[172:175], a[184:187], v[84:87]\n" \
"ds_read_b64_tr_b16 v[146:147], v225 offset:33536\n" \
"v_mfma_f32_16x16x32_bf16 v[116:119], v[168:171], a[172:175], v[116:119]\n" \
"s_mov_b32 s48, 16384\n" \
"s_mov_b32 m0, s41\n" \
"s_nop 0\n" \
"buffer_load_dwordx4 %[kd0], %[srdk], s48 offen offset:0 lds\n" \
"v_mfma_f32_16x16x32_bf16 v[116:119], v[172:175], a[188:191], v[116:119]\n" \
"ds_read_b64_tr_b16 v[148:149], v225 offset:41472\n" \
"v_mfma_f32_16x16x32_bf16 v[24:27], v[176:179], a[160:163], v[24:27]\n" \
"ds_read_b64_tr_b16 v[150:151], v225 offset:41728\n" \
"v_mfma_f32_16x16x32_bf16 v[24:27], v[180:183], a[176:179], v[24:27]\n" \
"v_mfma_f32_16x16x32_bf16 v[56:59], v[176:179], a[164:167], v[56:59]\n" \
"ds_read_b64_tr_b16 v[152:153], v225 offset:33288\n" \
"v_mfma_f32_16x16x32_bf16 v[56:59], v[180:183], a[180:183], v[56:59]\n" \
"ds_read_b64_tr_b16 v[154:155], v225 offset:33544\n" \
"v_mfma_f32_16x16x32_bf16 v[88:91], v[176:179], a[168:171], v[88:91]\n" \
"buffer_load_dwordx4 v212, %[srdk], s48 offen offset:1024 lds\n" \
"v_mfma_f32_16x16x32_bf16 v[88:91], v[180:183], a[184:187], v[88:91]\n" \
"ds_read_b64_tr_b16 v[156:157], v225 offset:41480\n" \
"v_mfma_f32_16x16x32_bf16 v[120:123], v[176:179], a[172:175], v[120:123]\n" \
"ds_read_b64_tr_b16 v[158:159], v225 offset:41736\n" \
"v_mfma_f32_16x16x32_bf16 v[120:123], v[180:183], a[188:191], v[120:123]\n" \
"v_mfma_f32_16x16x32_bf16 v[28:31], v[184:187], a[160:163], v[28:31]\n" \
"buffer_load_dwordx4 v213, %[srdk], s48 offen offset:2048 lds\n" \
"v_mfma_f32_16x16x32_bf16 v[28:31], v[188:191], a[176:179], v[28:31]\n" \
"v_mfma_f32_16x16x32_bf16 v[60:63], v[184:187], a[164:167], v[60:63]\n" \
"v_mfma_f32_16x16x32_bf16 v[60:63], v[188:191], a[180:183], v[60:63]\n" \
"v_mfma_f32_16x16x32_bf16 v[92:95], v[184:187], a[168:171], v[92:95]\n" \
"buffer_load_dwordx4 v214, %[srdk], s48 offen offset:3072 lds\n" \
"v_mfma_f32_16x16x32_bf16 v[92:95], v[188:191], a[184:187], v[92:95]\n" \
"v_mfma_f32_16x16x32_bf16 v[124:127], v[184:187], a[172:175], v[124:127]\n" \
"v_mfma_f32_16x16x32_bf16 v[124:127], v[188:191], a[188:191], v[124:127]\n" \
"s_waitcnt vmcnt(24) lgkmcnt(0)\n" \
"v_mfma_f32_16x16x32_bf16 v[0:3], v[128:131], a[192:195], v[0:3]\n" \
"ds_read_b64_tr_b16 v[160:161], v225 offset:33792\n" \
"v_mfma_f32_16x16x32_bf16 v[0:3], v[132:135], a[208:211], v[0:3]\n" \
"ds_read_b64_tr_b16 v[162:163], v225 offset:34048\n" \
"v_mfma_f32_16x16x32_bf16 v[32:35], v[128:131], a[196:199], v[32:35]\n" \
"s_mov_b32 s48, 32768\n" \
"s_mov_b32 m0, s45\n" \
"s_nop 0\n" \
"buffer_load_dwordx4 %[vd0], %[srdv], s48 offen offset:2048 lds\n" \
"v_mfma_f32_16x16x32_bf16 v[32:35], v[132:135], a[212:215], v[32:35]\n" \
"ds_read_b64_tr_b16 v[164:165], v225 offset:41984\n" \
"v_mfma_f32_16x16x32_bf16 v[64:67], v[128:131], a[200:203], v[64:67]\n" \
"ds_read_b64_tr_b16 v[166:167], v225 offset:42240\n" \
"v_mfma_f32_16x16x32_bf16 v[64:67], v[132:135], a[216:219], v[64:67]\n" \
"v_mfma_f32_16x16x32_bf16 v[96:99], v[128:131], a[204:207], v[96:99]\n" \
"ds_read_b64_tr_b16 v[168:169], v225 offset:33800\n" \
"v_mfma_f32_16x16x32_bf16 v[96:99], v[132:135], a[220:223], v[96:99]\n" \
"ds_read_b64_tr_b16 v[170:171], v225 offset:34056\n" \
"v_mfma_f32_16x16x32_bf16 v[4:7], v[136:139], a[192:195], v[4:7]\n" \
"buffer_load_dwordx4 v215, %[srdv], s48 offen offset:3072 lds\n" \
"v_mfma_f32_16x16x32_bf16 v[4:7], v[140:143], a[208:211], v[4:7]\n" \
"ds_read_b64_tr_b16 v[172:173], v225 offset:41992\n" \
"v_mfma_f32_16x16x32_bf16 v[36:39], v[136:139], a[196:199], v[36:39]\n" \
"ds_read_b64_tr_b16 v[174:175], v225 offset:42248\n" \
"v_mfma_f32_16x16x32_bf16 v[36:39], v[140:143], a[212:215], v[36:39]\n" \
"v_mfma_f32_16x16x32_bf16 v[68:71], v[136:139], a[200:203], v[68:71]\n" \
"ds_read_b64_tr_b16 v[176:177], v225 offset:34304\n" \
"v_mfma_f32_16x16x32_bf16 v[68:71], v[140:143], a[216:219], v[68:71]\n" \
"ds_read_b64_tr_b16 v[178:179], v225 offset:34560\n" \
"v_mfma_f32_16x16x32_bf16 v[100:103], v[136:139], a[204:207], v[100:103]\n" \
"s_mov_b32 s48, 32768\n" \
"s_mov_b32 m0, s42\n" \
"s_nop 0\n" \
"buffer_load_dwordx4 %[kd0], %[srdk], s48 offen offset:0 lds\n" \
"v_mfma_f32_16x16x32_bf16 v[100:103], v[140:143], a[220:223], v[100:103]\n" \
"ds_read_b64_tr_b16 v[180:181], v225 offset:42496\n" \
"v_mfma_f32_16x16x32_bf16 v[8:11], v[144:147], a[192:195], v[8:11]\n" \
"ds_read_b64_tr_b16 v[182:183], v225 offset:42752\n" \
"v_mfma_f32_16x16x32_bf16 v[8:11], v[148:151], a[208:211], v[8:11]\n" \
"v_mfma_f32_16x16x32_bf16 v[40:43], v[144:147], a[196:199], v[40:43]\n" \
"ds_read_b64_tr_b16 v[184:185], v225 offset:34312\n" \
"v_mfma_f32_16x16x32_bf16 v[40:43], v[148:151], a[212:215], v[40:43]\n" \
"ds_read_b64_tr_b16 v[186:187], v225 offset:34568\n" \
"v_mfma_f32_16x16x32_bf16 v[72:75], v[144:147], a[200:203], v[72:75]\n" \
"buffer_load_dwordx4 v212, %[srdk], s48 offen offset:1024 lds\n" \
"v_mfma_f32_16x16x32_bf16 v[72:75], v[148:151], a[216:219], v[72:75]\n" \
"ds_read_b64_tr_b16 v[188:189], v225 offset:42504\n" \
"v_mfma_f32_16x16x32_bf16 v[104:107], v[144:147], a[204:207], v[104:107]\n" \
"ds_read_b64_tr_b16 v[190:191], v225 offset:42760\n" \
"v_mfma_f32_16x16x32_bf16 v[104:107], v[148:151], a[220:223], v[104:107]\n" \
"v_mfma_f32_16x16x32_bf16 v[12:15], v[152:155], a[192:195], v[12:15]\n" \
"buffer_load_dwordx4 v213, %[srdk], s48 offen offset:2048 lds\n" \
"v_mfma_f32_16x16x32_bf16 v[12:15], v[156:159], a[208:211], v[12:15]\n" \
"v_mfma_f32_16x16x32_bf16 v[44:47], v[152:155], a[196:199], v[44:47]\n" \
"v_mfma_f32_16x16x32_bf16 v[44:47], v[156:159], a[212:215], v[44:47]\n" \
"v_mfma_f32_16x16x32_bf16 v[76:79], v[152:155], a[200:203], v[76:79]\n" \
"buffer_load_dwordx4 v214, %[srdk], s48 offen offset:3072 lds\n" \
"v_mfma_f32_16x16x32_bf16 v[76:79], v[156:159], a[216:219], v[76:79]\n" \
"v_mfma_f32_16x16x32_bf16 v[108:111], v[152:155], a[204:207], v[108:111]\n" \
"v_mfma_f32_16x16x32_bf16 v[108:111], v[156:159], a[220:223], v[108:111]\n" \
"s_waitcnt vmcnt(28) lgkmcnt(0)\n" \
"s_barrier\n" \
"v_mfma_f32_16x16x32_bf16 v[16:19], v[160:163], a[192:195], v[16:19]\n" \
"ds_read_b64_tr_b16 v[128:129], v225 offset:49152\n" \
"s_mov_b32 s54, 0x4000\n" \
"v_mfma_f32_16x16x32_bf16 v[16:19], v[164:167], a[208:211], v[16:19]\n" \
"ds_read_b64_tr_b16 v[130:131], v225 offset:49408\n" \
"v_mfma_f32_16x16x32_bf16 v[48:51], v[160:163], a[196:199], v[48:51]\n" \
"s_mov_b32 s48, 49152\n" \
"s_mov_b32 m0, s46\n" \
"s_nop 0\n" \
"buffer_load_dwordx4 %[vd0], %[srdv], s48 offen offset:0 lds\n" \
"v_mbcnt_lo_u32_b32 v231, -1, 0\n" \
"v_mfma_f32_16x16x32_bf16 v[48:51], v[164:167], a[212:215], v[48:51]\n" \
"ds_read_b64_tr_b16 v[132:133], v225 offset:57344\n" \
"v_mfma_f32_16x16x32_bf16 v[80:83], v[160:163], a[200:203], v[80:83]\n" \
"ds_read_b64_tr_b16 v[134:135], v225 offset:57600\n" \
"v_mfma_f32_16x16x32_bf16 v[80:83], v[164:167], a[216:219], v[80:83]\n" \
"v_mbcnt_hi_u32_b32 v231, -1, v231\n" \
"v_mfma_f32_16x16x32_bf16 v[112:115], v[160:163], a[204:207], v[112:115]\n" \
"ds_read_b64_tr_b16 v[136:137], v225 offset:49160\n" \
"v_mfma_f32_16x16x32_bf16 v[112:115], v[164:167], a[220:223], v[112:115]\n" \
"ds_read_b64_tr_b16 v[138:139], v225 offset:49416\n" \
"v_mfma_f32_16x16x32_bf16 v[20:23], v[168:171], a[192:195], v[20:23]\n" \
"buffer_load_dwordx4 v215, %[srdv], s48 offen offset:1024 lds\n" \
"v_lshrrev_b32_e32 v232, 4, v231\n" \
"v_mfma_f32_16x16x32_bf16 v[20:23], v[172:175], a[208:211], v[20:23]\n" \
"ds_read_b64_tr_b16 v[140:141], v225 offset:57352\n" \
"v_mfma_f32_16x16x32_bf16 v[52:55], v[168:171], a[196:199], v[52:55]\n" \
"ds_read_b64_tr_b16 v[142:143], v225 offset:57608\n" \
"v_xor_b32_e64 v232, v232, v231\n" \
"v_mfma_f32_16x16x32_bf16 v[52:55], v[172:175], a[212:215], v[52:55]\n" \
"v_mfma_f32_16x16x32_bf16 v[84:87], v[168:171], a[200:203], v[84:87]\n" \
"ds_read_b64_tr_b16 v[144:145], v225 offset:49664\n" \
"v_mfma_f32_16x16x32_bf16 v[84:87], v[172:175], a[216:219], v[84:87]\n" \
"ds_read_b64_tr_b16 v[146:147], v225 offset:49920\n" \
"v_bfe_i32 v232, v232, 1, 1\n" \
"v_mfma_f32_16x16x32_bf16 v[116:119], v[168:171], a[204:207], v[116:119]\n" \
"s_mov_b32 s48, 49152\n" \
"s_mov_b32 m0, s43\n" \
"s_nop 0\n" \
"buffer_load_dwordx4 %[kd0], %[srdk], s48 offen offset:0 lds\n" \
"v_mfma_f32_16x16x32_bf16 v[116:119], v[172:175], a[220:223], v[116:119]\n" \
"ds_read_b64_tr_b16 v[148:149], v225 offset:57856\n" \
"v_mfma_f32_16x16x32_bf16 v[24:27], v[176:179], a[192:195], v[24:27]\n" \
"ds_read_b64_tr_b16 v[150:151], v225 offset:58112\n" \
"v_not_b32_e64 v232, v232\n" \
"v_mfma_f32_16x16x32_bf16 v[24:27], v[180:183], a[208:211], v[24:27]\n" \
"v_mfma_f32_16x16x32_bf16 v[56:59], v[176:179], a[196:199], v[56:59]\n" \
"ds_read_b64_tr_b16 v[152:153], v225 offset:49672\n" \
"v_mfma_f32_16x16x32_bf16 v[56:59], v[180:183], a[212:215], v[56:59]\n" \
"ds_read_b64_tr_b16 v[154:155], v225 offset:49928\n" \
"v_and_b32_e32 v230, 0x3f803f80, v232\n" \
"v_mfma_f32_16x16x32_bf16 v[88:91], v[176:179], a[200:203], v[88:91]\n" \
"buffer_load_dwordx4 v212, %[srdk], s48 offen offset:1024 lds\n" \
"v_mfma_f32_16x16x32_bf16 v[88:91], v[180:183], a[216:219], v[88:91]\n" \
"ds_read_b64_tr_b16 v[156:157], v225 offset:57864\n" \
"v_readlane_b32 s62, %[tlo], 18\n" \
"v_mfma_f32_16x16x32_bf16 v[120:123], v[176:179], a[204:207], v[120:123]\n" \
"ds_read_b64_tr_b16 v[158:159], v225 offset:58120\n" \
"v_mfma_f32_16x16x32_bf16 v[120:123], v[180:183], a[220:223], v[120:123]\n" \
"v_mfma_f32_16x16x32_bf16 v[28:31], v[184:187], a[192:195], v[28:31]\n" \
"buffer_load_dwordx4 v213, %[srdk], s48 offen offset:2048 lds\n" \
"v_readlane_b32 s61, %[tlo], 21\n" \
"v_mfma_f32_16x16x32_bf16 v[28:31], v[188:191], a[208:211], v[28:31]\n" \
"v_mfma_f32_16x16x32_bf16 v[60:63], v[184:187], a[196:199], v[60:63]\n" \
"v_mfma_f32_16x16x32_bf16 v[60:63], v[188:191], a[212:215], v[60:63]\n" \
"v_lshlrev_b32_e64 v224, 7, v231\n" \
"v_mfma_f32_16x16x32_bf16 v[92:95], v[184:187], a[200:203], v[92:95]\n" \
"buffer_load_dwordx4 v214, %[srdk], s48 offen offset:3072 lds\n" \
"v_mfma_f32_16x16x32_bf16 v[92:95], v[188:191], a[216:219], v[92:95]\n" \
"v_mfma_f32_16x16x32_bf16 v[124:127], v[184:187], a[204:207], v[124:127]\n" \
"v_mfma_f32_16x16x32_bf16 v[124:127], v[188:191], a[220:223], v[124:127]\n" \
"s_waitcnt vmcnt(24) lgkmcnt(0)\n" \
"v_mfma_f32_16x16x32_bf16 v[0:3], v[128:131], a[224:227], v[0:3]\n" \
"ds_read_b64_tr_b16 v[160:161], v225 offset:50176\n" \
"v_accvgpr_write_b32 a192, v230\n" \
"v_mfma_f32_16x16x32_bf16 v[0:3], v[132:135], a[240:243], v[0:3]\n" \
"ds_read_b64_tr_b16 v[162:163], v225 offset:50432\n" \
"v_mfma_f32_16x16x32_bf16 v[32:35], v[128:131], a[228:231], v[32:35]\n" \
"s_mov_b32 s48, 49152\n" \
"s_mov_b32 m0, s46\n" \
"s_nop 0\n" \
"buffer_load_dwordx4 %[vd0], %[srdv], s48 offen offset:2048 lds\n" \
"v_accvgpr_write_b32 a193, v230\n" \
"v_mfma_f32_16x16x32_bf16 v[32:35], v[132:135], a[244:247], v[32:35]\n" \
"ds_read_b64_tr_b16 v[164:165], v225 offset:58368\n" \
"v_mfma_f32_16x16x32_bf16 v[64:67], v[128:131], a[232:235], v[64:67]\n" \
"ds_read_b64_tr_b16 v[166:167], v225 offset:58624\n" \
"v_accvgpr_write_b32 a194, v230\n" \
"v_mfma_f32_16x16x32_bf16 v[64:67], v[132:135], a[248:251], v[64:67]\n" \
"v_mfma_f32_16x16x32_bf16 v[96:99], v[128:131], a[236:239], v[96:99]\n" \
"ds_read_b64_tr_b16 v[168:169], v225 offset:50184\n" \
"v_accvgpr_write_b32 a195, v230\n" \
"v_mfma_f32_16x16x32_bf16 v[96:99], v[132:135], a[252:255], v[96:99]\n" \
"ds_read_b64_tr_b16 v[170:171], v225 offset:50440\n" \
"v_mfma_f32_16x16x32_bf16 v[4:7], v[136:139], a[224:227], v[4:7]\n" \
"buffer_load_dwordx4 v215, %[srdv], s48 offen offset:3072 lds\n" \
"v_accvgpr_write_b32 a196, -1\n" \
"v_mfma_f32_16x16x32_bf16 v[4:7], v[140:143], a[240:243], v[4:7]\n" \
"ds_read_b64_tr_b16 v[172:173], v225 offset:58376\n" \
"v_mfma_f32_16x16x32_bf16 v[36:39], v[136:139], a[228:231], v[36:39]\n" \
"ds_read_b64_tr_b16 v[174:175], v225 offset:58632\n" \
"v_accvgpr_write_b32 a197, -1\n" \
"v_mfma_f32_16x16x32_bf16 v[36:39], v[140:143], a[244:247], v[36:39]\n" \
"v_mfma_f32_16x16x32_bf16 v[68:71], v[136:139], a[232:235], v[68:71]\n" \
"ds_read_b64_tr_b16 v[176:177], v225 offset:50688\n" \
"v_accvgpr_write_b32 a198, -1\n" \
"v_mfma_f32_16x16x32_bf16 v[68:71], v[140:143], a[248:251], v[68:71]\n" \
"ds_read_b64_tr_b16 v[178:179], v225 offset:50944\n" \
"v_mfma_f32_16x16x32_bf16 v[100:103], v[136:139], a[236:239], v[100:103]\n" \
"v_accvgpr_write_b32 a199, -1\n" \
"v_mfma_f32_16x16x32_bf16 v[100:103], v[140:143], a[252:255], v[100:103]\n" \
"ds_read_b64_tr_b16 v[180:181], v225 offset:58880\n" \
"v_mfma_f32_16x16x32_bf16 v[8:11], v[144:147], a[224:227], v[8:11]\n" \
"ds_read_b64_tr_b16 v[182:183], v225 offset:59136\n" \
"v_lshrrev_b32_e32 v231, 3, v224\n" \
"v_mfma_f32_16x16x32_bf16 v[8:11], v[148:151], a[240:243], v[8:11]\n" \
"v_mfma_f32_16x16x32_bf16 v[40:43], v[144:147], a[228:231], v[40:43]\n" \
"ds_read_b64_tr_b16 v[184:185], v225 offset:50696\n" \
"v_mov_b32_e64 v232, 0\n" \
"v_mfma_f32_16x16x32_bf16 v[40:43], v[148:151], a[244:247], v[40:43]\n" \
"ds_read_b64_tr_b16 v[186:187], v225 offset:50952\n" \
"v_mfma_f32_16x16x32_bf16 v[72:75], v[144:147], a[232:235], v[72:75]\n" \
"v_mov_b32_e64 v233, 0\n" \
"v_mfma_f32_16x16x32_bf16 v[72:75], v[148:151], a[248:251], v[72:75]\n" \
"ds_read_b64_tr_b16 v[188:189], v225 offset:58888\n" \
"v_mfma_f32_16x16x32_bf16 v[104:107], v[144:147], a[236:239], v[104:107]\n" \
"ds_read_b64_tr_b16 v[190:191], v225 offset:59144\n" \
"v_mov_b32_e64 v234, 0\n" \
"v_mfma_f32_16x16x32_bf16 v[104:107], v[148:151], a[252:255], v[104:107]\n" \
"v_mfma_f32_16x16x32_bf16 v[12:15], v[152:155], a[224:227], v[12:15]\n" \
"v_mov_b32_e64 v235, 0\n" \
"v_mfma_f32_16x16x32_bf16 v[12:15], v[156:159], a[240:243], v[12:15]\n" \
"v_mfma_f32_16x16x32_bf16 v[44:47], v[152:155], a[228:231], v[44:47]\n" \
"buffer_store_dwordx4 v[232:235], v231, %[srdz], s61 offen offset:0 sc1\n" \
"v_mfma_f32_16x16x32_bf16 v[44:47], v[156:159], a[244:247], v[44:47]\n" \
"v_mfma_f32_16x16x32_bf16 v[76:79], v[152:155], a[232:235], v[76:79]\n" \
"buffer_store_dwordx4 v[232:235], v231, %[srdz], s61 offen offset:1024 sc1\n" \
"v_mfma_f32_16x16x32_bf16 v[76:79], v[156:159], a[248:251], v[76:79]\n" \
"v_mfma_f32_16x16x32_bf16 v[108:111], v[152:155], a[236:239], v[108:111]\n" \
"v_mfma_f32_16x16x32_bf16 v[108:111], v[156:159], a[252:255], v[108:111]\n" \
"s_waitcnt vmcnt(22) lgkmcnt(0)\n" \
"s_barrier\n" \
"v_mfma_f32_16x16x32_bf16 v[16:19], v[160:163], a[224:227], v[16:19]\n" \
"ds_read_b128 a[64:67], %[kb0] offset:0\n" \
"v_mfma_f32_16x16x32_bf16 v[16:19], v[164:167], a[240:243], v[16:19]\n" \
"v_mfma_f32_16x16x32_bf16 v[20:23], v[168:171], a[224:227], v[20:23]\n" \
"v_cvt_pk_bf16_f32 v230, v0, v1\n" \
"v_cvt_pk_bf16_f32 v231, v2, v3\n" \
"v_cvt_pk_bf16_f32 v232, v4, v5\n" \
"ds_read_b128 a[68:71], v208 offset:0\n" \
"v_mfma_f32_16x16x32_bf16 v[20:23], v[172:175], a[240:243], v[20:23]\n" \
"v_cvt_pk_bf16_f32 v233, v6, v7\n" \
"v_accvgpr_write_b32 a0, v230\n" \
"v_accvgpr_write_b32 a1, v231\n" \
"v_mfma_f32_16x16x32_bf16 v[24:27], v[176:179], a[224:227], v[24:27]\n" \
"v_accvgpr_write_b32 a2, v232\n" \
"v_accvgpr_write_b32 a3, v233\n" \
"v_cvt_pk_bf16_f32 v234, v8, v9\n" \
"ds_read_b128 a[72:75], v209 offset:0\n" \
"v_mfma_f32_16x16x32_bf16 v[24:27], v[180:183], a[240:243], v[24:27]\n" \
"v_cvt_pk_bf16_f32 v235, v10, v11\n" \
"v_cvt_pk_bf16_f32 v236, v12, v13\n" \
"v_cvt_pk_bf16_f32 v237, v14, v15\n" \
"v_mfma_f32_16x16x32_bf16 v[28:31], v[184:187], a[224:227], v[28:31]\n" \
"v_accvgpr_write_b32 a4, v234\n" \
"v_accvgpr_write_b32 a5, v235\n" \
"v_accvgpr_write_b32 a6, v236\n" \
".if . - adaln_body != 8192\n" \
".error \"size model\"\n" \
".endif\n" \
"Lk1_%=:\n" \
"s_cbranch_execz Lk2_%=\n" \
"s_nop 0\n" \
"ds_read_b128 a[76:79], v210 offset:0\n" \
"v_mfma_f32_16x16x32_bf16 v[28:31], v[188:191], a[240:243], v[28:31]\n" \
"v_accvgpr_write_b32 a7, v237\n" \
"v_cvt_pk_bf16_f32 v230, v32, v33\n" \
"v_cvt_pk_bf16_f32 v231, v34, v35\n" \
"v_mfma_f32_16x16x32_bf16 v[48:51], v[160:163], a[228:231], v[48:51]\n" \
"v_cvt_pk_bf16_f32 v232, v36, v37\n" \
"v_cvt_pk_bf16_f32 v233, v38, v39\n" \
"v_accvgpr_write_b32 a16, v230\n" \
"ds_read_b128 a[80:83], %[kb0] offset:4096\n" \
"v_mfma_f32_16x16x32_bf16 v[48:51], v[164:167], a[244:247], v[48:51]\n" \
"v_accvgpr_write_b32 a17, v231\n" \
"v_accvgpr_write_b32 a18, v232\n" \
"v_accvgpr_write_b32 a19, v233\n" \
"v_mfma_f32_16x16x32_bf16 v[52:55], v[168:171], a[228:231], v[52:55]\n" \
"v_cvt_pk_bf16_f32 v234, v40, v41\n" \
"v_cvt_pk_bf16_f32 v235, v42, v43\n" \
"v_cvt_pk_bf16_f32 v236, v44, v45\n" \
"ds_read_b128 a[84:87], v208 offset:4096\n" \
"v_mfma_f32_16x16x32_bf16 v[52:55], v[172:175], a[244:247], v[52:55]\n" \
"v_cvt_pk_bf16_f32 v237, v46, v47\n" \
"v_accvgpr_write_b32 a20, v234\n" \
"v_accvgpr_write_b32 a21, v235\n" \
"v_mfma_f32_16x16x32_bf16 v[56:59], v[176:179], a[228:231], v[56:59]\n" \
"v_accvgpr_write_b32 a22, v236\n" \
"v_accvgpr_write_b32 a23, v237\n" \
"v_cvt_pk_bf16_f32 v230, v16, v17\n" \
"ds_read_b128 a[88:91], v209 offset:4096\n" \
"v_mfma_f32_16x16x32_bf16 v[56:59], v[180:183], a[244:247], v[56:59]\n" \
"v_cvt_pk_bf16_f32 v231, v18, v19\n" \
"v_cvt_pk_bf16_f32 v232, v20, v21\n" \
"v_cvt_pk_bf16_f32 v233, v22, v23\n" \
"v_mfma_f32_16x16x32_bf16 v[60:63], v[184:187], a[228:231], v[60:63]\n" \
"v_accvgpr_write_b32 a8, v230\n" \
"v_accvgpr_write_b32 a9, v231\n" \
"v_accvgpr_write_b32 a10, v232\n" \
"ds_read_b128 a[92:95], v210 offset:4096\n" \
"v_mfma_f32_16x16x32_bf16 v[60:63], v[188:191], a[244:247], v[60:63]\n" \
"v_accvgpr_write_b32 a11, v233\n" \
"v_cvt_pk_bf16_f32 v234, v24, v25\n" \
"v_cvt_pk_bf16_f32 v235, v26, v27\n" \
"v_mfma_f32_16x16x32_bf16 v[80:83], v[160:163], a[232:235], v[80:83]\n" \
"v_cvt_pk_bf16_f32 v236, v28, v29\n" \
"v_cvt_pk_bf16_f32 v237, v30, v31\n" \
"v_accvgpr_write_b32 a12, v234\n" \
"v_mfma_f32_16x16x32_bf16 v[80:83], v[164:167], a[248:251], v[80:83]\n" \
"v_accvgpr_write_b32 a13, v235\n" \
"v_accvgpr_write_b32 a14, v236\n" \
"v_accvgpr_write_b32 a15, v237\n" \
"v_mfma_f32_16x16x32_bf16 v[84:87], v[168:171], a[232:235], v[84:87]\n" \
"v_cvt_pk_bf16_f32 v230, v64, v65\n" \
"v_cvt_pk_bf16_f32 v231, v66, v67\n" \
"v_cvt_pk_bf16_f32 v232, v68, v69\n" \
"v_mfma_f32_16x16x32_bf16 v[84:87], v[172:175], a[248:251], v[84:87]\n" \
"v_cvt_pk_bf16_f32 v233, v70, v71\n" \
"v_accvgpr_write_b32 a32, v230\n" \
"v_accvgpr_write_b32 a33, v231\n" \
"v_mfma_f32_16x16x32_bf16 v[88:91], v[176:179], a[232:235], v[88:91]\n" \
"v_accvgpr_write_b32 a34, v232\n" \
"v_accvgpr_write_b32 a35, v233\n" \
"v_cvt_pk_bf16_f32 v234, v72, v73\n" \
"v_mfma_f32_16x16x32_bf16 v[88:91], v[180:183], a[248:251], v[88:91]\n" \
"v_cvt_pk_bf16_f32 v235, v74, v75\n" \
"v_cvt_pk_bf16_f32 v236, v76, v77\n" \
"v_cvt_pk_bf16_f32 v237, v78, v79\n" \
"v_mfma_f32_16x16x32_bf16 v[92:95], v[184:187], a[232:235], v[92:95]\n" \
"v_accvgpr_write_b32 a36, v234\n" \
"v_accvgpr_write_b32 a37, v235\n" \
"v_accvgpr_write_b32 a38, v236\n" \
"v_mfma_f32_16x16x32_bf16 v[92:95], v[188:191], a[248:251], v[92:95]\n" \
"v_accvgpr_write_b32 a39, v237\n" \
"v_cvt_pk_bf16_f32 v230, v48, v49\n" \
"v_cvt_pk_bf16_f32 v231, v50, v51\n" \
"v_mfma_f32_16x16x32_bf16 v[112:115], v[160:163], a[236:239], v[112:115]\n" \
"v_cvt_pk_bf16_f32 v232, v52, v53\n" \
"v_cvt_pk_bf16_f32 v233, v54, v55\n" \
"v_accvgpr_write_b32 a24, v230\n" \
"v_mfma_f32_16x16x32_bf16 v[112:115], v[164:167], a[252:255], v[112:115]\n" \
"v_accvgpr_write_b32 a25, v231\n" \
"v_accvgpr_write_b32 a26, v232\n" \
"v_accvgpr_write_b32 a27, v233\n" \
"v_mfma_f32_16x16x32_bf16 v[116:119], v[168:171], a[236:239], v[116:119]\n" \
"v_cvt_pk_bf16_f32 v234, v56, v57\n" \
"v_cvt_pk_bf16_f32 v235, v58, v59\n" \
"v_cvt_pk_bf16_f32 v236, v60, v61\n" \
"v_mfma_f32_16x16x32_bf16 v[116:119], v[172:175], a[252:255], v[116:119]\n" \
"v_cvt_pk_bf16_f32 v237, v62, v63\n" \
"v_accvgpr_write_b32 a28, v234\n" \
"v_accvgpr_write_b32 a29, v235\n" \
"v_mfma_f32_16x16x32_bf16 v[120:123], v[176:179], a[236:239], v[120:123]\n" \
"v_accvgpr_write_b32 a30, v236\n" \
"v_accvgpr_write_b32 a31, v237\n" \
"v_cvt_pk_bf16_f32 v230, v96, v97\n" \
"v_mfma_f32_16x16x32_bf16 v[120:123], v[180:183], a[252:255], v[120:123]\n" \
"v_cvt_pk_bf16_f32 v231, v98, v99\n" \
"v_cvt_pk_bf16_f32 v232, v100, v101\n" \
"v_cvt_pk_bf16_f32 v233, v102, v103\n" \
"v_mfma_f32_16x16x32_bf16 v[124:127], v[184:187], a[236:239], v[124:127]\n" \
"v_accvgpr_write_b32 a48, v230\n" \
"v_accvgpr_write_b32 a49, v231\n" \
"v_accvgpr_write_b32 a50, v232\n" \
"v_mfma_f32_16x16x32_bf16 v[124:127], v[188:191], a[252:255], v[124:127]\n" \
"v_accvgpr_write_b32 a51, v233\n" \
"s_waitcnt vmcnt(63) lgkmcnt(0)\n" \
"s_nop 0\n" \
"s_mov_b32 s48, 65536\n" \
"s_mov_b32 s60, 0\n" \
"s_waitcnt lgkmcnt(0)\n" \
"v_mfma_f32_16x16x32_bf16 v[128:131], a[64:67], a[0:3], 0\n" \
"ds_read_b128 a[96:99], %[kb0] offset:8192\n" \
"v_cvt_pk_bf16_f32 v234, v80, v81\n" \
"v_cvt_pk_bf16_f32 v235, v82, v83\n" \
"v_mfma_f32_16x16x32_bf16 v[128:131], a[68:71], a[4:7], v[128:131]\n" \
"ds_read_b128 a[100:103], v208 offset:8192\n" \
"v_cvt_pk_bf16_f32 v236, v84, v85\n" \
"v_cvt_pk_bf16_f32 v237, v86, v87\n" \
"v_mfma_f32_16x16x32_bf16 v[128:131], a[72:75], a[8:11], v[128:131]\n" \
"ds_read_b128 a[104:107], v209 offset:8192\n" \
"v_accvgpr_write_b32 a40, v234\n" \
"v_accvgpr_write_b32 a41, v235\n" \
"v_mfma_f32_16x16x32_bf16 v[128:131], a[76:79], a[12:15], v[128:131]\n" \
"ds_read_b128 a[108:111], v210 offset:8192\n" \
"v_accvgpr_write_b32 a42, v236\n" \
"v_accvgpr_write_b32 a43, v237\n" \
"v_mfma_f32_16x16x32_bf16 v[132:135], a[80:83], a[0:3], 0\n" \
"ds_read_b128 a[112:115], %[kb0] offset:12288\n" \
"v_cvt_pk_bf16_f32 v230, v88, v89\n" \
"v_cvt_pk_bf16_f32 v231, v90, v91\n" \
"v_mfma_f32_16x16x32_bf16 v[132:135], a[84:87], a[4:7], v[132:135]\n" \
"ds_read_b128 a[116:119], v208 offset:12288\n" \
"v_cvt_pk_bf16_f32 v232, v92, v93\n" \
"v_cvt_pk_bf16_f32 v233, v94, v95\n" \
"v_mfma_f32_16x16x32_bf16 v[132:135], a[88:91], a[8:11], v[132:135]\n" \
"ds_read_b128 a[120:123], v209 offset:12288\n" \
"v_accvgpr_write_b32 a44, v230\n" \
"v_accvgpr_write_b32 a45, v231\n" \
"v_mfma_f32_16x16x32_bf16 v[132:135], a[92:95], a[12:15], v[132:135]\n" \
"ds_read_b128 a[124:127], v210 offset:12288\n" \
"v_accvgpr_write_b32 a46, v232\n" \
"v_accvgpr_write_b32 a47, v233\n" \
"v_mfma_f32_16x16x32_bf16 v[136:139], a[64:67], a[16:19], 0\n" \
"ds_read_b64_tr_b16 a[128:129], %[vvb] offset:0\n" \
"v_cvt_pk_bf16_f32 v234, v104, v105\n" \
"v_cvt_pk_bf16_f32 v235, v106, v107\n" \
"v_mfma_f32_16x16x32_bf16 v[136:139], a[68:71], a[20:23], v[136:139]\n" \
"ds_read_b64_tr_b16 a[130:131], %[vvb] offset:4096\n" \
"v_cvt_pk_bf16_f32 v236, v108, v109\n" \
"v_cvt_pk_bf16_f32 v237, v110, v111\n" \
"v_mfma_f32_16x16x32_bf16 v[136:139], a[72:75], a[24:27], v[136:139]\n" \
"ds_read_b64_tr_b16 a[132:133], v211 offset:0\n" \
"v_accvgpr_write_b32 a52, v234\n" \
"v_accvgpr_write_b32 a53, v235\n" \
"v_mfma_f32_16x16x32_bf16 v[136:139], a[76:79], a[28:31], v[136:139]\n" \
"ds_read_b64_tr_b16 a[134:135], v211 offset:4096\n" \
"v_accvgpr_write_b32 a54, v236\n" \
"v_accvgpr_write_b32 a55, v237\n" \
"v_mfma_f32_16x16x32_bf16 v[140:143], a[80:83], a[16:19], 0\n" \
"ds_read_b64_tr_b16 a[136:137], %[vvb] offset:512\n" \
"v_cvt_pk_bf16_f32 v230, v112, v113\n" \
"v_cvt_pk_bf16_f32 v231, v114, v115\n" \
"v_mfma_f32_16x16x32_bf16 v[140:143], a[84:87], a[20:23], v[140:143]\n" \
"ds_read_b64_tr_b16 a[138:139], %[vvb] offset:4608\n" \
"v_cvt_pk_bf16_f32 v232, v116, v117\n" \
"v_cvt_pk_bf16_f32 v233, v118, v119\n" \
"v_mfma_f32_16x16x32_bf16 v[140:143], a[88:91], a[24:27], v[140:143]\n" \
"ds_read_b64_tr_b16 a[140:141], v211 offset:512\n" \
"v_accvgpr_write_b32 a56, v230\n" \
"v_accvgpr_write_b32 a57, v231\n" \
"v_mfma_f32_16x16x32_bf16 v[140:143], a[92:95], a[28:31], v[140:143]\n" \
"ds_read_b64_tr_b16 a[142:143], v211 offset:4608\n" \
"v_accvgpr_write_b32 a58, v232\n" \
"v_accvgpr_write_b32 a59, v233\n" \
"v_mfma_f32_16x16x32_bf16 v[144:147], a[64:67], a[32:35], 0\n" \
"ds_read_b64_tr_b16 a[144:145], %[vvb] offset:1024\n" \
"v_cvt_pk_bf16_f32 v234, v120, v121\n" \
"v_cvt_pk_bf16_f32 v235, v122, v123\n" \
"v_mfma_f32_16x16x32_bf16 v[144:147], a[68:71], a[36:39], v[144:147]\n" \
"ds_read_b64_tr_b16 a[146:147], %[vvb] offset:5120\n" \
"v_cvt_pk_bf16_f32 v236, v124, v125\n" \
"v_cvt_pk_bf16_f32 v237, v126, v127\n" \
"v_mfma_f32_16x16x32_bf16 v[144:147], a[72:75], a[40:43], v[144:147]\n" \
"ds_read_b64_tr_b16 a[148:149], v211 offset:1024\n" \
"v_accvgpr_write_b32 a60, v234\n" \
"v_accvgpr_write_b32 a61, v235\n" \
"v_mfma_f32_16x16x32_bf16 v[144:147], a[76:79], a[44:47], v[144:147]\n" \
"ds_read_b64_tr_b16 a[150:151], v211 offset:5120\n" \
"v_accvgpr_write_b32 a62, v236\n" \
"v_accvgpr_write_b32 a63, v237\n" \
"v_mfma_f32_16x16x32_bf16 v[148:151], a[80:83], a[32:35], 0\n" \
"ds_read_b64_tr_b16 a[152:153], %[vvb] offset:1536\n" \
"v_max3_f32 v192, v128, v129, v130\n" \
"v_max3_f32 v193, v131, v132, v133\n" \
"v_mfma_f32_16x16x32_bf16 v[148:151], a[84:87], a[36:39], v[148:151]\n" \
"ds_read_b64_tr_b16 a[154:155], %[vvb] offset:5632\n" \
"v_max3_f32 v192, v192, v134, v135\n" \
"v_max_f32_e64 v225, v192, v193\n" \
"v_mfma_f32_16x16x32_bf16 v[148:151], a[88:91], a[40:43], v[148:151]\n" \
"ds_read_b64_tr_b16 a[156:157], v211 offset:1536\n" \
"v_max3_f32 v192, v136, v137, v138\n" \
"v_max3_f32 v193, v139, v140, v141\n" \
"v_mfma_f32_16x16x32_bf16 v[148:151], a[92:95], a[44:47], v[148:151]\n" \
"ds_read_b64_tr_b16 a[158:159], v211 offset:5632\n" \
"v_max3_f32 v192, v192, v142, v143\n" \
"v_max_f32_e64 v226, v192, v193\n" \
"v_mfma_f32_16x16x32_bf16 v[152:155], a[64:67], a[48:51], 0\n" \
"ds_read_b128 a[200:203], v228 offset:0\n" \
"s_mov_b32 s44, %[m0t]\n" \
"s_mov_b32 s45, 0\n" \
"v_mfma_f32_16x16x32_bf16 v[152:155], a[68:71], a[52:55], v[152:155]\n" \
"ds_read_b128 a[204:207], v228 offset:64\n" \
"v_mfma_f32_16x16x32_bf16 v[152:155], a[72:75], a[56:59], v[152:155]\n" \
"ds_read_b128 a[208:211], v228 offset:128\n" \
"v_mfma_f32_16x16x32_bf16 v[152:155], a[76:79], a[60:63], v[152:155]\n" \
"ds_read_b128 a[212:215], v228 offset:192\n" \
"v_mfma_f32_16x16x32_bf16 v[156:159], a[80:83], a[48:51], 0\n" \
"ds_read_b128 a[216:219], v228 offset:256\n" \
"v_mfma_f32_16x16x32_bf16 v[156:159], a[84:87], a[52:55], v[156:159]\n" \
"ds_read_b128 a[220:223], v228 offset:320\n" \
"v_mfma_f32_16x16x32_bf16 v[156:159], a[88:91], a[56:59], v[156:159]\n" \
"ds_read_b128 a[224:227], v228 offset:384\n" \
"v_max3_f32 v192, v144, v145, v146\n" \
"v_max3_f32 v193, v147, v148, v149\n" \
"v_mfma_f32_16x16x32_bf16 v[156:159], a[92:95], a[60:63], v[156:159]\n" \
"ds_read_b128 a[228:231], v228 offset:448\n" \
"v_max3_f32 v192, v192, v150, v151\n" \
"v_max_f32_e64 v227, v192, v193\n" \
"s_nop 3\n" \
"s_nop 0\n" \
"v_max3_f32 v230, v152, v153, v154\n" \
"v_max3_f32 v231, v155, v156, v157\n" \
"v_max3_f32 v230, v230, v158, v159\n" \
"v_max_f32_e32 v228, v230, v231\n" \
"v_mov_b32_e32 v232, v225\n" \
"v_mov_b32_e32 v233, v226\n" \
"v_mov_b32_e32 v234, v227\n" \
"v_mov_b32_e32 v235, v228\n" \
"s_nop 0\n" \
"v_permlane16_swap_b32_e32 v225, v232\n" \
"v_permlane16_swap_b32_e32 v226, v233\n" \
"v_permlane16_swap_b32_e32 v227, v234\n" \
"v_permlane16_swap_b32_e32 v228, v235\n" \
"v_max_f32_e32 v225, v225, v232\n" \
"v_max_f32_e32 v226, v226, v233\n" \
"v_max_f32_e32 v227, v227, v234\n" \
"v_max_f32_e32 v228, v228, v235\n" \
"v_mov_b32_e32 v232, v225\n" \
"v_mov_b32_e32 v233, v226\n" \
"v_mov_b32_e32 v234, v227\n" \
"v_mov_b32_e32 v235, v228\n" \
"s_nop 0\n" \
"v_permlane32_swap_b32_e32 v225, v232\n" \
"v_permlane32_swap_b32_e32 v226, v233\n" \
"v_permlane32_swap_b32_e32 v227, v234\n" \
"v_permlane32_swap_b32_e32 v228, v235\n" \
"v_max_f32_e32 v225, v225, v232\n" \
"v_max_f32_e32 v226, v226, v233\n" \
"v_max_f32_e32 v227, v227, v234\n" \
"v_max_f32_e64 v228, v228, v235\n" \
"Lhavem_%=:\n" \
"s_nop 3\n" \
"v_mov_b64_e32 v[176:177], 0\n" \
"v_mov_b64_e32 v[178:179], 0\n" \
"v_mov_b64_e32 v[180:181], 0\n" \
"v_mov_b64_e32 v[182:183], 0\n" \
"v_sub_f32_e32 v160, 0, v225\n" \
"v_sub_f32_e32 v161, 0, v225\n" \
"v_sub_f32_e32 v162, 0, v225\n" \
"v_sub_f32_e32 v163, 0, v225\n" \
"v_sub_f32_e32 v164, 0, v226\n" \
"v_sub_f32_e32 v165, 0, v226\n" \
"v_sub_f32_e32 v166, 0, v226\n" \
"v_sub_f32_e32 v167, 0, v226\n" \
"v_sub_f32_e32 v168, 0, v227\n" \
"v_sub_f32_e32 v169, 0, v227\n" \
"v_sub_f32_e32 v170, 0, v227\n" \
"v_sub_f32_e32 v171, 0, v227\n" \
"v_sub_f32_e32 v172, 0, v228\n" \
"v_sub_f32_e32 v173, 0, v228\n" \
"v_sub_f32_e32 v174, 0, v228\n" \
"v_sub_f32_e32 v175, 0, v228\n" \
"v_sub_f32_e32 v128, v128, v225\n" \
"v_sub_f32_e32 v129, v129, v225\n" \
"v_sub_f32_e32 v130, v130, v225\n" \
"v_sub_f32_e32 v131, v131, v225\n" \
"v_sub_f32_e32 v132, v132, v225\n" \
"v_sub_f32_e32 v133, v133, v225\n" \
"v_sub_f32_e32 v134, v134, v225\n" \
"v_sub_f32_e32 v135, v135, v225\n" \
"v_sub_f32_e32 v136, v136, v226\n" \
"v_sub_f32_e32 v137, v137, v226\n" \
"v_sub_f32_e32 v138, v138, v226\n" \
"v_sub_f32_e32 v139, v139, v226\n" \
"v_sub_f32_e32 v140, v140, v226\n" \
"v_sub_f32_e32 v141, v141, v226\n" \
"v_sub_f32_e32 v142, v142, v226\n" \
"v_sub_f32_e32 v143, v143, v226\n" \
"v_sub_f32_e32 v144, v144, v227\n" \
"v_sub_f32_e32 v145, v145, v227\n" \
"v_sub_f32_e32 v146, v146, v227\n" \
"v_sub_f32_e32 v147, v147, v227\n" \
"v_sub_f32_e32 v148, v148, v227\n" \
"v_sub_f32_e32 v149, v149, v227\n" \
"v_sub_f32_e32 v150, v150, v227\n" \
"v_sub_f32_e32 v151, v151, v227\n" \
"v_sub_f32_e32 v152, v152, v228\n" \
"v_sub_f32_e32 v153, v153, v228\n" \
"v_sub_f32_e32 v154, v154, v228\n" \
"v_sub_f32_e32 v155, v155, v228\n" \
"v_sub_f32_e32 v156, v156, v228\n" \
"v_sub_f32_e32 v157, v157, v228\n" \
"v_sub_f32_e32 v158, v158, v228\n" \
"v_sub_f32_e32 v159, v159, v228\n" \
"v_exp_f32_e32 v128, v128\n" \
"v_exp_f32_e32 v129, v129\n" \
"v_exp_f32_e32 v130, v130\n" \
"v_exp_f32_e32 v131, v131\n" \
"v_exp_f32_e32 v132, v132\n" \
"v_exp_f32_e32 v133, v133\n" \
"v_exp_f32_e32 v134, v134\n" \
"v_exp_f32_e32 v135, v135\n" \
"v_exp_f32_e32 v136, v136\n" \
"v_exp_f32_e32 v137, v137\n" \
"v_exp_f32_e32 v138, v138\n" \
"v_exp_f32_e32 v139, v139\n" \
"v_exp_f32_e32 v140, v140\n" \
"v_exp_f32_e32 v141, v141\n" \
"v_exp_f32_e32 v142, v142\n" \
"v_exp_f32_e32 v143, v143\n" \
"v_exp_f32_e32 v144, v144\n" \
"v_exp_f32_e32 v145, v145\n" \
"v_exp_f32_e32 v146, v146\n" \
"v_exp_f32_e32 v147, v147\n" \
"v_exp_f32_e32 v148, v148\n" \
"v_exp_f32_e32 v149, v149\n" \
"v_exp_f32_e32 v150, v150\n" \
"v_exp_f32_e32 v151, v151\n" \
"v_exp_f32_e32 v152, v152\n" \
"v_exp_f32_e32 v153, v153\n" \
"v_exp_f32_e32 v154, v154\n" \
"v_exp_f32_e64 v155, v155\n" \
"v_bfe_i32 v228, v224, 7, 1\n" \
"v_and_b32_e32 v228, 0xaaaa, v228\n" \
"v_or_b32_e32 v228, 0x4444, v228\n" \
"v_cvt_pk_bf16_f32 v184, v128, v129\n" \
"v_cvt_pk_bf16_f32 v185, v130, v131\n" \
"v_cvt_pk_bf16_f32 v186, v132, v133\n" \
"v_cvt_pk_bf16_f32 v187, v134, v135\n" \
"v_cvt_pk_bf16_f32 v188, v136, v137\n" \
"v_cvt_pk_bf16_f32 v189, v138, v139\n" \
"v_cvt_pk_bf16_f32 v190, v140, v141\n" \
"v_cvt_pk_bf16_f32 v191, v142, v143\n" \
"v_cvt_pk_bf16_f32 v216, v144, v145\n" \
"v_cvt_pk_bf16_f32 v217, v146, v147\n" \
"v_cvt_pk_bf16_f32 v218, v148, v149\n" \
"v_cvt_pk_bf16_f32 v219, v150, v151\n" \
"v_lshrrev_b32_e64 v235, 3, v224\n" \
"s_waitcnt vmcnt(2) lgkmcnt(0)\n" \
"s_barrier\n" \
"v_mfma_f32_16x16x32_bf16 v[128:131], a[96:99], a[0:3], v[160:163]\n" \
"v_exp_f32_e64 v156, v156\n" \
"v_mfma_f32_16x16x32_bf16 v[128:131], a[100:103], a[4:7], v[128:131]\n" \
"s_add_u32 m0, %[m0b], 114688\n" \
"v_mfma_f32_16x16x32_bf16 v[128:131], a[104:107], a[8:11], v[128:131]\n" \
"v_exp_f32_e64 v157, v157\n" \
"v_mfma_f32_16x16x32_bf16 v[128:131], a[108:111], a[12:15], v[128:131]\n" \
"v_cvt_pk_bf16_f32 v220, v152, v153\n" \
"v_cvt_pk_bf16_f32 v221, v154, v155\n" \
"v_mfma_f32_16x16x32_bf16 v[132:135], a[112:115], a[0:3], v[160:163]\n" \
"v_exp_f32_e64 v158, v158\n" \
"v_mfma_f32_16x16x32_bf16 v[132:135], a[116:119], a[4:7], v[132:135]\n" \
"v_mfma_f32_16x16x32_bf16 v[132:135], a[120:123], a[8:11], v[132:135]\n" \
"buffer_load_dwordx4 %[vd0], %[srdv], s48 offen offset:0 lds\n" \
"v_mfma_f32_16x16x32_bf16 v[132:135], a[124:127], a[12:15], v[132:135]\n" \
"v_exp_f32_e64 v159, v159\n" \
"v_mfma_f32_16x16x32_bf16 v[0:3], a[128:131], v[184:187], 0\n" \
"ds_read_b128 a[64:67], %[kb0] offset:16384\n" \
"v_mfma_f32_16x16x32_bf16 v[32:35], a[128:131], v[188:191], 0\n" \
"v_exp_f32_e64 v128, v128\n" \
"v_mfma_f32_16x16x32_bf16 v[4:7], a[132:135], v[184:187], 0\n" \
"v_cvt_pk_bf16_f32 v222, v156, v157\n" \
"v_cvt_pk_bf16_f32 v223, v158, v159\n" \
"v_mfma_f32_16x16x32_bf16 v[36:39], a[132:135], v[188:191], 0\n" \
"v_exp_f32_e64 v129, v129\n" \
"v_mfma_f32_16x16x32_bf16 v[8:11], a[136:139], v[184:187], 0\n" \
"ds_read_b128 a[68:71], v208 offset:16384\n" \
"v_mfma_f32_16x16x32_bf16 v[40:43], a[136:139], v[188:191], 0\n" \
"v_exp_f32_e64 v130, v130\n" \
"v_mfma_f32_16x16x32_bf16 v[12:15], a[140:143], v[184:187], 0\n" \
"ds_read_b128 a[72:75], v209 offset:16384\n" \
"v_mfma_f32_16x16x32_bf16 v[44:47], a[140:143], v[188:191], 0\n" \
"v_exp_f32_e64 v131, v131\n" \
"v_smfmac_f32_16x16x64_bf16 v[176:179], a[192:195], v[184:191], v228\n" \
"v_mfma_f32_16x16x32_bf16 v[136:139], a[96:99], a[16:19], v[164:167]\n" \
"v_exp_f32_e64 v132, v132\n" \
"v_mfma_f32_16x16x32_bf16 v[136:139], a[100:103], a[20:23], v[136:139]\n" \
"ds_read_b128 a[76:79], v210 offset:16384\n" \
"v_mfma_f32_16x16x32_bf16 v[136:139], a[104:107], a[24:27], v[136:139]\n" \
"v_exp_f32_e64 v133, v133\n" \
"v_mfma_f32_16x16x32_bf16 v[136:139], a[108:111], a[28:31], v[136:139]\n" \
"v_cvt_pk_bf16_f32 v192, v128, v129\n" \
"v_cvt_pk_bf16_f32 v193, v130, v131\n" \
"v_mfma_f32_16x16x32_bf16 v[140:143], a[112:115], a[16:19], v[164:167]\n" \
"v_exp_f32_e64 v134, v134\n" \
"v_mfma_f32_16x16x32_bf16 v[140:143], a[116:119], a[20:23], v[140:143]\n" \
"ds_read_b128 a[80:83], %[kb0] offset:20480\n" \
"v_mfma_f32_16x16x32_bf16 v[140:143], a[120:123], a[24:27], v[140:143]\n" \
"buffer_load_dwordx4 v215, %[srdv], s48 offen offset:1024 lds\n" \
"v_mfma_f32_16x16x32_bf16 v[140:143], a[124:127], a[28:31], v[140:143]\n" \
"v_exp_f32_e64 v135, v135\n" \
"v_mfma_f32_16x16x32_bf16 v[16:19], a[144:147], v[184:187], 0\n" \
"ds_read_b128 a[84:87], v208 offset:20480\n" \
"v_mfma_f32_16x16x32_bf16 v[48:51], a[144:147], v[188:191], 0\n" \
"v_exp_f32_e64 v136, v136\n" \
"v_mfma_f32_16x16x32_bf16 v[20:23], a[148:151], v[184:187], 0\n" \
"v_cvt_pk_bf16_f32 v194, v132, v133\n" \
"v_cvt_pk_bf16_f32 v195, v134, v135\n" \
"v_mfma_f32_16x16x32_bf16 v[52:55], a[148:151], v[188:191], 0\n" \
"v_exp_f32_e64 v137, v137\n" \
"v_mfma_f32_16x16x32_bf16 v[24:27], a[152:155], v[184:187], 0\n" \
"v_exp_f32_e64 v138, v138\n" \
"v_mfma_f32_16x16x32_bf16 v[56:59], a[152:155], v[188:191], 0\n" \
"ds_read_b128 a[88:91], v209 offset:20480\n" \
"v_mfma_f32_16x16x32_bf16 v[28:31], a[156:159], v[184:187], 0\n" \
"v_exp_f32_e64 v139, v139\n" \
"v_mfma_f32_16x16x32_bf16 v[60:63], a[156:159], v[188:191], 0\n" \
"v_mfma_f32_16x16x32_bf16 v[144:147], a[96:99], a[32:35], v[168:171]\n" \
"v_exp_f32_e64 v140, v140\n" \
"v_mfma_f32_16x16x32_bf16 v[144:147], a[100:103], a[36:39], v[144:147]\n" \
"ds_read_b128 a[92:95], v210 offset:20480\n" \
"v_mfma_f32_16x16x32_bf16 v[144:147], a[104:107], a[40:43], v[144:147]\n" \
"v_exp_f32_e64 v141, v141\n" \
"v_mfma_f32_16x16x32_bf16 v[144:147], a[108:111], a[44:47], v[144:147]\n" \
"v_cvt_pk_bf16_f32 v196, v136, v137\n" \
"v_cvt_pk_bf16_f32 v197, v138, v139\n" \
"v_mfma_f32_16x16x32_bf16 v[148:151], a[112:115], a[32:35], v[168:171]\n" \
"v_exp_f32_e64 v142, v142\n" \
"v_mfma_f32_16x16x32_bf16 v[148:151], a[116:119], a[36:39], v[148:151]\n" \
"v_mfma_f32_16x16x32_bf16 v[148:151], a[120:123], a[40:43], v[148:151]\n" \
"buffer_load_dwordx4 %[vd0], %[srdv], s48 offen offset:2048 lds\n" \
"v_mfma_f32_16x16x32_bf16 v[148:151], a[124:127], a[44:47], v[148:151]\n" \
"v_exp_f32_e64 v143, v143\n" \
"v_mfma_f32_16x16x32_bf16 v[64:67], a[128:131], v[216:219], 0\n" \
"ds_read_b64_tr_b16 a[160:161], %[vvb] offset:8192\n" \
"v_mfma_f32_16x16x32_bf16 v[96:99], a[128:131], v[220:223], 0\n" \
"v_exp_f32_e64 v144, v144\n" \
"v_mfma_f32_16x16x32_bf16 v[68:71], a[132:135], v[216:219], 0\n" \
"v_cvt_pk_bf16_f32 v198, v140, v141\n" \
"v_cvt_pk_bf16_f32 v199, v142, v143\n" \
"v_mfma_f32_16x16x32_bf16 v[100:103], a[132:135], v[220:223], 0\n" \
"v_exp_f32_e64 v145, v145\n" \
"v_mfma_f32_16x16x32_bf16 v[72:75], a[136:139], v[216:219], 0\n" \
"ds_read_b64_tr_b16 a[162:163], %[vvb] offset:12288\n" \
"ds_read_b64_tr_b16 a[164:165], v211 offset:8192\n" \
"v_mfma_f32_16x16x32_bf16 v[104:107], a[136:139], v[220:223], 0\n" \
"v_exp_f32_e64 v146, v146\n" \
"v_mfma_f32_16x16x32_bf16 v[76:79], a[140:143], v[216:219], 0\n" \
"ds_read_b64_tr_b16 a[166:167], v211 offset:12288\n" \
"v_mfma_f32_16x16x32_bf16 v[108:111], a[140:143], v[220:223], 0\n" \
"v_exp_f32_e64 v147, v147\n" \
"v_smfmac_f32_16x16x64_bf16 v[180:183], a[192:195], v[216:223], v228\n" \
"ds_read_b64_tr_b16 a[168:169], %[vvb] offset:8704\n" \
"v_mfma_f32_16x16x32_bf16 v[152:155], a[96:99], a[48:51], v[172:175]\n" \
"v_exp_f32_e64 v148, v148\n" \
"v_mfma_f32_16x16x32_bf16 v[152:155], a[100:103], a[52:55], v[152:155]\n" \
"ds_read_b64_tr_b16 a[170:171], %[vvb] offset:12800\n" \
"v_mfma_f32_16x16x32_bf16 v[152:155], a[104:107], a[56:59], v[152:155]\n" \
"v_exp_f32_e64 v149, v149\n" \
"v_mfma_f32_16x16x32_bf16 v[152:155], a[108:111], a[60:63], v[152:155]\n" \
"v_cvt_pk_bf16_f32 v200, v144, v145\n" \
"v_cvt_pk_bf16_f32 v201, v146, v147\n" \
"v_mfma_f32_16x16x32_bf16 v[156:159], a[112:115], a[48:51], v[172:175]\n" \
"v_exp_f32_e64 v150, v150\n" \
"v_mfma_f32_16x16x32_bf16 v[156:159], a[116:119], a[52:55], v[156:159]\n" \
"ds_read_b64_tr_b16 a[172:173], v211 offset:8704\n" \
"v_mfma_f32_16x16x32_bf16 v[156:159], a[120:123], a[56:59], v[156:159]\n" \
"buffer_load_dwordx4 v215, %[srdv], s48 offen offset:3072 lds\n" \
"v_mfma_f32_16x16x32_bf16 v[156:159], a[124:127], a[60:63], v[156:159]\n" \
"v_exp_f32_e64 v151, v151\n" \
"v_mfma_f32_16x16x32_bf16 v[80:83], a[144:147], v[216:219], 0\n" \
"ds_read_b64_tr_b16 a[174:175], v211 offset:12800\n" \
"ds_read_b64_tr_b16 a[176:177], %[vvb] offset:9216\n" \
"v_mfma_f32_16x16x32_bf16 v[112:115], a[144:147], v[220:223], 0\n" \
"v_exp_f32_e64 v152, v152\n" \
"v_mfma_f32_16x16x32_bf16 v[84:87], a[148:151], v[216:219], 0\n" \
"v_cvt_pk_bf16_f32 v202, v148, v149\n" \
"v_cvt_pk_bf16_f32 v203, v150, v151\n" \
"v_mfma_f32_16x16x32_bf16 v[116:119], a[148:151], v[220:223], 0\n" \
"v_exp_f32_e64 v153, v153\n" \
"v_mfma_f32_16x16x32_bf16 v[88:91], a[152:155], v[216:219], 0\n" \
"v_exp_f32_e64 v154, v154\n" \
"v_mfma_f32_16x16x32_bf16 v[120:123], a[152:155], v[220:223], 0\n" \
"ds_read_b64_tr_b16 a[178:179], %[vvb] offset:13312\n" \
"v_mfma_f32_16x16x32_bf16 v[92:95], a[156:159], v[216:219], 0\n" \
"v_exp_f32_e64 v155, v155\n" \
"v_mov_b64_e32 v[216:217], 0\n" \
"v_mov_b64_e32 v[218:219], 0\n" \
"v_mfma_f32_16x16x32_bf16 v[124:127], a[156:159], v[220:223], 0\n" \
"ds_read_b64_tr_b16 a[180:181], v211 offset:9216\n" \
"s_waitcnt lgkmcnt(13)\n" \
"s_nop 0\n" \
"LmA_%=:\n" \
"v_mfma_f32_16x16x32_bf16 v[128:131], a[64:67], a[0:3], v[160:163]\n" \
"v_exp_f32_e64 v156, v156\n" \
"v_mfma_f32_16x16x32_bf16 v[128:131], a[68:71], a[4:7], v[128:131]\n" \
"ds_read_b64_tr_b16 a[182:183], v211 offset:13312\n" \
"s_add_u32 m0, %[m0b], 0\n" \
"v_mfma_f32_16x16x32_bf16 v[128:131], a[72:75], a[8:11], v[128:131]\n" \
"v_exp_f32_e32 v157, v157\n" \
"v_mfma_f32_16x16x32_bf16 v[128:131], a[76:79], a[12:15], v[128:131]\n" \
"v_cvt_pk_bf16_f32 v204, v152, v153\n" \
"v_cvt_pk_bf16_f32 v205, v154, v155\n" \
"v_mfma_f32_16x16x32_bf16 v[132:135], a[80:83], a[0:3], v[160:163]\n" \
"v_exp_f32_e64 v158, v158\n" \
"v_mfma_f32_16x16x32_bf16 v[132:135], a[84:87], a[4:7], v[132:135]\n" \
"ds_read_b64_tr_b16 a[184:185], %[vvb] offset:9728\n" \
"s_waitcnt lgkmcnt(5)\n" \
"v_mfma_f32_16x16x32_bf16 v[132:135], a[88:91], a[8:11], v[132:135]\n" \
"buffer_load_dwordx4 %[kd0], %[srdk], s48 offen offset:0 lds\n" \
"v_mfma_f32_16x16x32_bf16 v[132:135], a[92:95], a[12:15], v[132:135]\n" \
"v_exp_f32_e32 v159, v159\n" \
"v_mfma_f32_16x16x32_bf16 v[64:67], a[128:131], v[216:219], v[64:67]\n" \
"ds_read_b64_tr_b16 a[186:187], %[vvb] offset:13824\n" \
"ds_read_b64_tr_b16 a[188:189], v211 offset:9728\n" \
"v_mfma_f32_16x16x32_bf16 v[64:67], a[160:163], v[200:203], v[64:67]\n" \
"v_exp_f32_e64 v128, v128\n" \
"v_mfma_f32_16x16x32_bf16 v[68:71], a[132:135], v[216:219], v[68:71]\n" \
"v_cvt_pk_bf16_f32 v206, v156, v157\n" \
"v_cvt_pk_bf16_f32 v207, v158, v159\n" \
"v_mfma_f32_16x16x32_bf16 v[68:71], a[164:167], v[200:203], v[68:71]\n" \
"v_exp_f32_e64 v129, v129\n" \
"v_mfma_f32_16x16x32_bf16 v[72:75], a[136:139], v[216:219], v[72:75]\n" \
"ds_read_b64_tr_b16 a[190:191], v211 offset:13824\n" \
"ds_read_b64_tr_b16 a[128:129], %[vvb] offset:16384\n" \
"s_add_u32 s60, s60, 1\n" \
"s_nop 0\n" \
"v_readlane_b32 s58, %[tlo], s60\n" \
"v_mfma_f32_16x16x32_bf16 v[72:75], a[168:171], v[200:203], v[72:75]\n" \
"v_exp_f32_e64 v130, v130\n" \
"v_mfma_f32_16x16x32_bf16 v[76:79], a[140:143], v[216:219], v[76:79]\n" \
"ds_read_b64_tr_b16 a[130:131], %[vvb] offset:20480\n" \
"ds_read_b64_tr_b16 a[132:133], v211 offset:16384\n" \
"v_readlane_b32 s59, %[thi], s60\n" \
"v_mfma_f32_16x16x32_bf16 v[76:79], a[172:175], v[200:203], v[76:79]\n" \
"v_exp_f32_e64 v131, v131\n" \
"v_smfmac_f32_16x16x64_bf16 v[176:179], a[192:195], v[192:199], v228\n" \
"ds_read_b64_tr_b16 a[134:135], v211 offset:20480\n" \
"ds_read_b64_tr_b16 a[136:137], %[vvb] offset:16896\n" \
"v_mfma_f32_16x16x32_bf16 v[136:139], a[64:67], a[16:19], v[164:167]\n" \
"v_exp_f32_e64 v132, v132\n" \
"v_mfma_f32_16x16x32_bf16 v[136:139], a[68:71], a[20:23], v[136:139]\n" \
"ds_read_b64_tr_b16 a[138:139], %[vvb] offset:20992\n" \
"ds_read_b64_tr_b16 a[140:141], v211 offset:16896\n" \
"v_mfma_f32_16x16x32_bf16 v[136:139], a[72:75], a[24:27], v[136:139]\n" \
"v_exp_f32_e64 v133, v133\n" \
"v_mfma_f32_16x16x32_bf16 v[136:139], a[76:79], a[28:31], v[136:139]\n" \
"v_cvt_pk_bf16_f32 v184, v128, v129\n" \
"v_cvt_pk_bf16_f32 v185, v130, v131\n" \
"v_mfma_f32_16x16x32_bf16 v[140:143], a[80:83], a[16:19], v[164:167]\n" \
"v_exp_f32_e64 v134, v134\n" \
"v_mfma_f32_16x16x32_bf16 v[140:143], a[84:87], a[20:23], v[140:143]\n" \
"ds_read_b64_tr_b16 a[142:143], v211 offset:20992\n" \
"s_waitcnt lgkmcnt(8)\n" \
"v_mfma_f32_16x16x32_bf16 v[140:143], a[88:91], a[24:27], v[140:143]\n" \
"buffer_load_dwordx4 v212, %[srdk], s48 offen offset:1024 lds\n" \
"v_mfma_f32_16x16x32_bf16 v[140:143], a[92:95], a[28:31], v[140:143]\n" \
"v_exp_f32_e32 v135, v135\n" \
"v_mfma_f32_16x16x32_bf16 v[80:83], a[144:147], v[216:219], v[80:83]\n" \
"ds_read_b64_tr_b16 a[144:145], %[vvb] offset:17408\n" \
"ds_read_b64_tr_b16 a[146:147], %[vvb] offset:21504\n" \
"v_mfma_f32_16x16x32_bf16 v[80:83], a[176:179], v[200:203], v[80:83]\n" \
"v_exp_f32_e64 v136, v136\n" \
"v_mfma_f32_16x16x32_bf16 v[84:87], a[148:151], v[216:219], v[84:87]\n" \
"v_cvt_pk_bf16_f32 v186, v132, v133\n" \
"v_cvt_pk_bf16_f32 v187, v134, v135\n" \
"v_mfma_f32_16x16x32_bf16 v[84:87], a[180:183], v[200:203], v[84:87]\n" \
"v_exp_f32_e64 v137, v137\n" \
"v_mfma_f32_16x16x32_bf16 v[88:91], a[152:155], v[216:219], v[88:91]\n" \
".if . - adaln_body != 12288\n" \
".error \"size model\"\n" \
".endif\n" \
"Lk2_%=:\n" \
"s_cbranch_execz Lk3_%=\n" \
"s_nop 0\n" \
"v_exp_f32_e64 v138, v138\n" \
"v_mfma_f32_16x16x32_bf16 v[88:91], a[184:187], v[200:203], v[88:91]\n" \
"ds_read_b64_tr_b16 a[148:149], v211 offset:17408\n" \
"ds_read_b64_tr_b16 a[150:151], v211 offset:21504\n" \
"v_mfma_f32_16x16x32_bf16 v[92:95], a[156:159], v[216:219], v[92:95]\n" \
"v_exp_f32_e64 v139, v139\n" \
"v_mfma_f32_16x16x32_bf16 v[92:95], a[188:191], v[200:203], v[92:95]\n" \
"ds_read_b64_tr_b16 a[152:153], %[vvb] offset:17920\n" \
"v_mfma_f32_16x16x32_bf16 v[144:147], a[64:67], a[32:35], v[168:171]\n" \
"v_exp_f32_e64 v140, v140\n" \
"v_mfma_f32_16x16x32_bf16 v[144:147], a[68:71], a[36:39], v[144:147]\n" \
"ds_read_b64_tr_b16 a[154:155], %[vvb] offset:22016\n" \
"ds_read_b64_tr_b16 a[156:157], v211 offset:17920\n" \
"v_mfma_f32_16x16x32_bf16 v[144:147], a[72:75], a[40:43], v[144:147]\n" \
"v_exp_f32_e64 v141, v141\n" \
"v_mfma_f32_16x16x32_bf16 v[144:147], a[76:79], a[44:47], v[144:147]\n" \
"v_cvt_pk_bf16_f32 v188, v136, v137\n" \
"v_cvt_pk_bf16_f32 v189, v138, v139\n" \
"v_mfma_f32_16x16x32_bf16 v[148:151], a[80:83], a[32:35], v[168:171]\n" \
"v_exp_f32_e64 v142, v142\n" \
"v_mfma_f32_16x16x32_bf16 v[148:151], a[84:87], a[36:39], v[148:151]\n" \
"ds_read_b64_tr_b16 a[158:159], v211 offset:22016\n" \
"s_waitcnt lgkmcnt(8)\n" \
"v_mfma_f32_16x16x32_bf16 v[148:151], a[88:91], a[40:43], v[148:151]\n" \
"buffer_load_dwordx4 v213, %[srdk], s48 offen offset:2048 lds\n" \
"v_mfma_f32_16x16x32_bf16 v[148:151], a[92:95], a[44:47], v[148:151]\n" \
"v_exp_f32_e32 v143, v143\n" \
"v_mfma_f32_16x16x32_bf16 v[0:3], a[160:163], v[192:195], v[0:3]\n" \
"ds_read_b128 a[96:99], %[kb0] offset:24576\n" \
"v_mfma_f32_16x16x32_bf16 v[0:3], a[128:131], v[184:187], v[0:3]\n" \
"v_exp_f32_e64 v144, v144\n" \
"v_mfma_f32_16x16x32_bf16 v[4:7], a[164:167], v[192:195], v[4:7]\n" \
"v_cvt_pk_bf16_f32 v190, v140, v141\n" \
"v_cvt_pk_bf16_f32 v191, v142, v143\n" \
"v_mfma_f32_16x16x32_bf16 v[4:7], a[132:135], v[184:187], v[4:7]\n" \
"v_exp_f32_e64 v145, v145\n" \
"v_mfma_f32_16x16x32_bf16 v[8:11], a[168:171], v[192:195], v[8:11]\n" \
"ds_read_b128 a[100:103], v208 offset:24576\n" \
"v_mfma_f32_16x16x32_bf16 v[8:11], a[136:139], v[184:187], v[8:11]\n" \
"v_exp_f32_e64 v146, v146\n" \
"v_mfma_f32_16x16x32_bf16 v[12:15], a[172:175], v[192:195], v[12:15]\n" \
"ds_read_b128 a[104:107], v209 offset:24576\n" \
"v_mfma_f32_16x16x32_bf16 v[12:15], a[140:143], v[184:187], v[12:15]\n" \
"v_exp_f32_e64 v147, v147\n" \
"v_smfmac_f32_16x16x64_bf16 v[180:183], a[192:195], v[200:207], v228\n" \
"ds_read_b128 a[108:111], v210 offset:24576\n" \
"v_mfma_f32_16x16x32_bf16 v[152:155], a[64:67], a[48:51], v[172:175]\n" \
"v_exp_f32_e64 v148, v148\n" \
"v_mfma_f32_16x16x32_bf16 v[152:155], a[68:71], a[52:55], v[152:155]\n" \
"ds_read_b128 a[112:115], %[kb0] offset:28672\n" \
"v_mfma_f32_16x16x32_bf16 v[152:155], a[72:75], a[56:59], v[152:155]\n" \
"v_exp_f32_e64 v149, v149\n" \
"v_mfma_f32_16x16x32_bf16 v[152:155], a[76:79], a[60:63], v[152:155]\n" \
"v_cvt_pk_bf16_f32 v216, v144, v145\n" \
"v_cvt_pk_bf16_f32 v217, v146, v147\n" \
"v_mfma_f32_16x16x32_bf16 v[156:159], a[80:83], a[48:51], v[172:175]\n" \
"v_exp_f32_e64 v150, v150\n" \
"v_mfma_f32_16x16x32_bf16 v[156:159], a[84:87], a[52:55], v[156:159]\n" \
"ds_read_b128 a[116:119], v208 offset:28672\n" \
"s_waitcnt lgkmcnt(6)\n" \
"v_mfma_f32_16x16x32_bf16 v[156:159], a[88:91], a[56:59], v[156:159]\n" \
"buffer_load_dwordx4 v214, %[srdk], s48 offen offset:3072 lds\n" \
"v_mfma_f32_16x16x32_bf16 v[156:159], a[92:95], a[60:63], v[156:159]\n" \
"v_exp_f32_e32 v151, v151\n" \
"v_mfma_f32_16x16x32_bf16 v[16:19], a[176:179], v[192:195], v[16:19]\n" \
"ds_read_b128 a[120:123], v209 offset:28672\n" \
"v_mfma_f32_16x16x32_bf16 v[16:19], a[144:147], v[184:187], v[16:19]\n" \
"v_exp_f32_e64 v152, v152\n" \
"v_mfma_f32_16x16x32_bf16 v[20:23], a[180:183], v[192:195], v[20:23]\n" \
"v_cvt_pk_bf16_f32 v218, v148, v149\n" \
"v_cvt_pk_bf16_f32 v219, v150, v151\n" \
"v_mfma_f32_16x16x32_bf16 v[20:23], a[148:151], v[184:187], v[20:23]\n" \
"v_exp_f32_e64 v153, v153\n" \
"v_mfma_f32_16x16x32_bf16 v[24:27], a[184:187], v[192:195], v[24:27]\n" \
"v_exp_f32_e64 v154, v154\n" \
"v_mfma_f32_16x16x32_bf16 v[24:27], a[152:155], v[184:187], v[24:27]\n" \
"ds_read_b128 a[124:127], v210 offset:28672\n" \
"s_add_u32 s48, s48, 0x4000\n" \
"s_mov_b32 m0, s44\n" \
"s_addk_i32 s44, 0x400\n" \
"buffer_load_dwordx4 v229, s[64:67], s45 offen offset:0 nt lds\n" \
"s_add_u32 s45, s45, 393216\n" \
"v_mfma_f32_16x16x32_bf16 v[28:31], a[188:191], v[192:195], v[28:31]\n" \
"v_exp_f32_e64 v155, v155\n" \
"v_mfma_f32_16x16x32_bf16 v[28:31], a[156:159], v[184:187], v[28:31]\n" \
"s_waitcnt vmcnt(14) lgkmcnt(2)\n" \
"s_barrier\n" \
"v_mfma_f32_16x16x32_bf16 v[128:131], a[96:99], a[0:3], v[160:163]\n" \
"v_exp_f32_e64 v156, v156\n" \
"v_mfma_f32_16x16x32_bf16 v[128:131], a[100:103], a[4:7], v[128:131]\n" \
"s_add_u32 m0, %[m0b], 65536\n" \
"v_mfma_f32_16x16x32_bf16 v[128:131], a[104:107], a[8:11], v[128:131]\n" \
"v_exp_f32_e64 v157, v157\n" \
"v_mfma_f32_16x16x32_bf16 v[128:131], a[108:111], a[12:15], v[128:131]\n" \
"v_cvt_pk_bf16_f32 v220, v152, v153\n" \
"v_cvt_pk_bf16_f32 v221, v154, v155\n" \
"v_mfma_f32_16x16x32_bf16 v[132:135], a[112:115], a[0:3], v[160:163]\n" \
"v_exp_f32_e64 v158, v158\n" \
"v_mfma_f32_16x16x32_bf16 v[132:135], a[116:119], a[4:7], v[132:135]\n" \
"s_waitcnt lgkmcnt(0)\n" \
"v_mfma_f32_16x16x32_bf16 v[132:135], a[120:123], a[8:11], v[132:135]\n" \
"buffer_load_dwordx4 %[vd0], %[srdv], s48 offen offset:0 lds\n" \
"v_mfma_f32_16x16x32_bf16 v[132:135], a[124:127], a[12:15], v[132:135]\n" \
"v_exp_f32_e32 v159, v159\n" \
"v_mfma_f32_16x16x32_bf16 v[32:35], a[160:163], v[196:199], v[32:35]\n" \
"ds_read_b128 a[64:67], %[kb0] offset:32768\n" \
"v_mfma_f32_16x16x32_bf16 v[32:35], a[128:131], v[188:191], v[32:35]\n" \
"v_exp_f32_e64 v128, v128\n" \
"v_mfma_f32_16x16x32_bf16 v[36:39], a[164:167], v[196:199], v[36:39]\n" \
"v_cvt_pk_bf16_f32 v222, v156, v157\n" \
"v_cvt_pk_bf16_f32 v223, v158, v159\n" \
"v_mfma_f32_16x16x32_bf16 v[36:39], a[132:135], v[188:191], v[36:39]\n" \
"v_exp_f32_e64 v129, v129\n" \
"v_mfma_f32_16x16x32_bf16 v[40:43], a[168:171], v[196:199], v[40:43]\n" \
"ds_read_b128 a[68:71], v208 offset:32768\n" \
"v_mfma_f32_16x16x32_bf16 v[40:43], a[136:139], v[188:191], v[40:43]\n" \
"v_exp_f32_e64 v130, v130\n" \
"v_mfma_f32_16x16x32_bf16 v[44:47], a[172:175], v[196:199], v[44:47]\n" \
"ds_read_b128 a[72:75], v209 offset:32768\n" \
"v_mfma_f32_16x16x32_bf16 v[44:47], a[140:143], v[188:191], v[44:47]\n" \
"v_exp_f32_e64 v131, v131\n" \
"v_smfmac_f32_16x16x64_bf16 v[176:179], a[192:195], v[184:191], v228\n" \
"v_mfma_f32_16x16x32_bf16 v[136:139], a[96:99], a[16:19], v[164:167]\n" \
"v_exp_f32_e64 v132, v132\n" \
"v_mfma_f32_16x16x32_bf16 v[136:139], a[100:103], a[20:23], v[136:139]\n" \
"ds_read_b128 a[76:79], v210 offset:32768\n" \
"v_mfma_f32_16x16x32_bf16 v[136:139], a[104:107], a[24:27], v[136:139]\n" \
"v_exp_f32_e64 v133, v133\n" \
"v_mfma_f32_16x16x32_bf16 v[136:139], a[108:111], a[28:31], v[136:139]\n" \
"v_cvt_pk_bf16_f32 v192, v128, v129\n" \
"v_cvt_pk_bf16_f32 v193, v130, v131\n" \
"v_mfma_f32_16x16x32_bf16 v[140:143], a[112:115], a[16:19], v[164:167]\n" \
"v_exp_f32_e64 v134, v134\n" \
"v_mfma_f32_16x16x32_bf16 v[140:143], a[116:119], a[20:23], v[140:143]\n" \
"ds_read_b128 a[80:83], %[kb0] offset:36864\n" \
"v_mfma_f32_16x16x32_bf16 v[140:143], a[120:123], a[24:27], v[140:143]\n" \
"buffer_load_dwordx4 v215, %[srdv], s48 offen offset:1024 lds\n" \
"v_mfma_f32_16x16x32_bf16 v[140:143], a[124:127], a[28:31], v[140:143]\n" \
"v_exp_f32_e64 v135, v135\n" \
"v_mfma_f32_16x16x32_bf16 v[48:51], a[176:179], v[196:199], v[48:51]\n" \
"ds_read_b128 a[84:87], v208 offset:36864\n" \
"v_mfma_f32_16x16x32_bf16 v[48:51], a[144:147], v[188:191], v[48:51]\n" \
"v_exp_f32_e64 v136, v136\n" \
"v_mfma_f32_16x16x32_bf16 v[52:55], a[180:183], v[196:199], v[52:55]\n" \
"v_cvt_pk_bf16_f32 v194, v132, v133\n" \
"v_cvt_pk_bf16_f32 v195, v134, v135\n" \
"v_mfma_f32_16x16x32_bf16 v[52:55], a[148:151], v[188:191], v[52:55]\n" \
"v_exp_f32_e64 v137, v137\n" \
"v_mfma_f32_16x16x32_bf16 v[56:59], a[184:187], v[196:199], v[56:59]\n" \
"v_exp_f32_e64 v138, v138\n" \
"v_mfma_f32_16x16x32_bf16 v[56:59], a[152:155], v[188:191], v[56:59]\n" \
"ds_read_b128 a[88:91], v209 offset:36864\n" \
"v_mfma_f32_16x16x32_bf16 v[60:63], a[188:191], v[196:199], v[60:63]\n" \
"v_exp_f32_e64 v139, v139\n" \
"v_mfma_f32_16x16x32_bf16 v[60:63], a[156:159], v[188:191], v[60:63]\n" \
"s_cmp_lg_u32 %[wid], 0\n" \
"s_cbranch_scc1 LtA3_%=\n" \
"global_load_dword v237, v224, s[58:59]\n" \
"s_add_u32 s61, s48, 49152\n" \
"s_mov_b32 exec_hi, 0\n" \
"s_nop 0\n" \
"buffer_load_dword v237, v224, %[srdp], s61 offen\n" \
"s_mov_b32 exec_hi, -1\n" \
"s_nop 0\n" \
"LtA3_%=:\n" \
"v_mfma_f32_16x16x32_bf16 v[144:147], a[96:99], a[32:35], v[168:171]\n" \
"v_exp_f32_e64 v140, v140\n" \
"v_mfma_f32_16x16x32_bf16 v[144:147], a[100:103], a[36:39], v[144:147]\n" \
"ds_read_b128 a[92:95], v210 offset:36864\n" \
"v_mfma_f32_16x16x32_bf16 v[144:147], a[104:107], a[40:43], v[144:147]\n" \
"v_exp_f32_e64 v141, v141\n" \
"v_mfma_f32_16x16x32_bf16 v[144:147], a[108:111], a[44:47], v[144:147]\n" \
"v_cvt_pk_bf16_f32 v196, v136, v137\n" \
"v_cvt_pk_bf16_f32 v197, v138, v139\n" \
"v_mfma_f32_16x16x32_bf16 v[148:151], a[112:115], a[32:35], v[168:171]\n" \
"v_exp_f32_e64 v142, v142\n" \
"v_mfma_f32_16x16x32_bf16 v[148:151], a[116:119], a[36:39], v[148:151]\n" \
"v_mfma_f32_16x16x32_bf16 v[148:151], a[120:123], a[40:43], v[148:151]\n" \
"buffer_load_dwordx4 %[vd0], %[srdv], s48 offen offset:2048 lds\n" \
"v_mfma_f32_16x16x32_bf16 v[148:151], a[124:127], a[44:47], v[148:151]\n" \
"v_exp_f32_e64 v143, v143\n" \
"v_mfma_f32_16x16x32_bf16 v[96:99], a[160:163], v[204:207], v[96:99]\n" \
"ds_read_b64_tr_b16 a[160:161], %[vvb] offset:24576\n" \
"v_mfma_f32_16x16x32_bf16 v[96:99], a[128:131], v[220:223], v[96:99]\n" \
"v_exp_f32_e64 v144, v144\n" \
"v_mfma_f32_16x16x32_bf16 v[100:103], a[164:167], v[204:207], v[100:103]\n" \
"v_cvt_pk_bf16_f32 v198, v140, v141\n" \
"v_cvt_pk_bf16_f32 v199, v142, v143\n" \
"v_mfma_f32_16x16x32_bf16 v[100:103], a[132:135], v[220:223], v[100:103]\n" \
"v_exp_f32_e64 v145, v145\n" \
"v_mfma_f32_16x16x32_bf16 v[104:107], a[168:171], v[204:207], v[104:107]\n" \
"ds_read_b64_tr_b16 a[162:163], %[vvb] offset:28672\n" \
"ds_read_b64_tr_b16 a[164:165], v211 offset:24576\n" \
"v_mfma_f32_16x16x32_bf16 v[104:107], a[136:139], v[220:223], v[104:107]\n" \
"v_exp_f32_e64 v146, v146\n" \
"v_mfma_f32_16x16x32_bf16 v[108:111], a[172:175], v[204:207], v[108:111]\n" \
"ds_read_b64_tr_b16 a[166:167], v211 offset:28672\n" \
"v_mfma_f32_16x16x32_bf16 v[108:111], a[140:143], v[220:223], v[108:111]\n" \
"v_exp_f32_e64 v147, v147\n" \
"v_smfmac_f32_16x16x64_bf16 v[180:183], a[192:195], v[216:223], v228\n" \
"ds_read_b64_tr_b16 a[168:169], %[vvb] offset:25088\n" \
"v_mfma_f32_16x16x32_bf16 v[152:155], a[96:99], a[48:51], v[172:175]\n" \
"v_exp_f32_e64 v148, v148\n" \
"v_mfma_f32_16x16x32_bf16 v[152:155], a[100:103], a[52:55], v[152:155]\n" \
"ds_read_b64_tr_b16 a[170:171], %[vvb] offset:29184\n" \
"v_mfma_f32_16x16x32_bf16 v[152:155], a[104:107], a[56:59], v[152:155]\n" \
"v_exp_f32_e64 v149, v149\n" \
"v_mfma_f32_16x16x32_bf16 v[152:155], a[108:111], a[60:63], v[152:155]\n" \
"v_cvt_pk_bf16_f32 v200, v144, v145\n" \
"v_cvt_pk_bf16_f32 v201, v146, v147\n" \
"v_mfma_f32_16x16x32_bf16 v[156:159], a[112:115], a[48:51], v[172:175]\n" \
"v_exp_f32_e64 v150, v150\n" \
"v_mfma_f32_16x16x32_bf16 v[156:159], a[116:119], a[52:55], v[156:159]\n" \
"ds_read_b64_tr_b16 a[172:173], v211 offset:25088\n" \
"v_mfma_f32_16x16x32_bf16 v[156:159], a[120:123], a[56:59], v[156:159]\n" \
"buffer_load_dwordx4 v215, %[srdv], s48 offen offset:3072 lds\n" \
"v_mfma_f32_16x16x32_bf16 v[156:159], a[124:127], a[60:63], v[156:159]\n" \
"v_exp_f32_e64 v151, v151\n" \
"v_mfma_f32_16x16x32_bf16 v[112:115], a[176:179], v[204:207], v[112:115]\n" \
"ds_read_b64_tr_b16 a[174:175], v211 offset:29184\n" \
"ds_read_b64_tr_b16 a[176:177], %[vvb] offset:25600\n" \
"v_mfma_f32_16x16x32_bf16 v[112:115], a[144:147], v[220:223], v[112:115]\n" \
"v_exp_f32_e64 v152, v152\n" \
"v_mfma_f32_16x16x32_bf16 v[116:119], a[180:183], v[204:207], v[116:119]\n" \
"v_cvt_pk_bf16_f32 v202, v148, v149\n" \
"v_cvt_pk_bf16_f32 v203, v150, v151\n" \
"v_mfma_f32_16x16x32_bf16 v[116:119], a[148:151], v[220:223], v[116:119]\n" \
"v_exp_f32_e64 v153, v153\n" \
"v_mfma_f32_16x16x32_bf16 v[120:123], a[184:187], v[204:207], v[120:123]\n" \
"v_exp_f32_e64 v154, v154\n" \
"v_mfma_f32_16x16x32_bf16 v[120:123], a[152:155], v[220:223], v[120:123]\n" \
"ds_read_b64_tr_b16 a[178:179], %[vvb] offset:29696\n" \
"v_mfma_f32_16x16x32_bf16 v[124:127], a[188:191], v[204:207], v[124:127]\n" \
"v_exp_f32_e64 v155, v155\n" \
"v_mfma_f32_16x16x32_bf16 v[124:127], a[156:159], v[220:223], v[124:127]\n" \
"ds_read_b64_tr_b16 a[180:181], v211 offset:25600\n" \
"s_waitcnt lgkmcnt(13)\n" \
"v_mfma_f32_16x16x32_bf16 v[128:131], a[64:67], a[0:3], v[160:163]\n" \
"v_exp_f32_e32 v156, v156\n" \
"v_mfma_f32_16x16x32_bf16 v[128:131], a[68:71], a[4:7], v[128:131]\n" \
"ds_read_b64_tr_b16 a[182:183], v211 offset:29696\n" \
"s_add_u32 m0, %[m0b], 16384\n" \
"v_mfma_f32_16x16x32_bf16 v[128:131], a[72:75], a[8:11], v[128:131]\n" \
"v_exp_f32_e64 v157, v157\n" \
"v_mfma_f32_16x16x32_bf16 v[128:131], a[76:79], a[12:15], v[128:131]\n" \
"v_cvt_pk_bf16_f32 v204, v152, v153\n" \
"v_cvt_pk_bf16_f32 v205, v154, v155\n" \
"v_mfma_f32_16x16x32_bf16 v[132:135], a[80:83], a[0:3], v[160:163]\n" \
"v_exp_f32_e64 v158, v158\n" \
"v_mfma_f32_16x16x32_bf16 v[132:135], a[84:87], a[4:7], v[132:135]\n" \
"ds_read_b64_tr_b16 a[184:185], %[vvb] offset:26112\n" \
"s_waitcnt lgkmcnt(5)\n" \
"v_mfma_f32_16x16x32_bf16 v[132:135], a[88:91], a[8:11], v[132:135]\n" \
"buffer_load_dwordx4 %[kd0], %[srdk], s48 offen offset:0 lds\n" \
"v_mfma_f32_16x16x32_bf16 v[132:135], a[92:95], a[12:15], v[132:135]\n" \
"v_exp_f32_e32 v159, v159\n" \
"v_mfma_f32_16x16x32_bf16 v[64:67], a[128:131], v[216:219], v[64:67]\n" \
"ds_read_b64_tr_b16 a[186:187], %[vvb] offset:30208\n" \
"ds_read_b64_tr_b16 a[188:189], v211 offset:26112\n" \
"v_mfma_f32_16x16x32_bf16 v[64:67], a[160:163], v[200:203], v[64:67]\n" \
"v_exp_f32_e64 v128, v128\n" \
"v_mfma_f32_16x16x32_bf16 v[68:71], a[132:135], v[216:219], v[68:71]\n" \
"v_cvt_pk_bf16_f32 v206, v156, v157\n" \
"v_cvt_pk_bf16_f32 v207, v158, v159\n" \
"v_mfma_f32_16x16x32_bf16 v[68:71], a[164:167], v[200:203], v[68:71]\n" \
"v_exp_f32_e64 v129, v129\n" \
"v_mfma_f32_16x16x32_bf16 v[72:75], a[136:139], v[216:219], v[72:75]\n" \
"ds_read_b64_tr_b16 a[190:191], v211 offset:30208\n" \
"ds_read_b64_tr_b16 a[128:129], %[vvb] offset:32768\n" \
"v_mfma_f32_16x16x32_bf16 v[72:75], a[168:171], v[200:203], v[72:75]\n" \
"v_exp_f32_e64 v130, v130\n" \
"v_mfma_f32_16x16x32_bf16 v[76:79], a[140:143], v[216:219], v[76:79]\n" \
"ds_read_b64_tr_b16 a[130:131], %[vvb] offset:36864\n" \
"ds_read_b64_tr_b16 a[132:133], v211 offset:32768\n" \
"v_mfma_f32_16x16x32_bf16 v[76:79], a[172:175], v[200:203], v[76:79]\n" \
"v_exp_f32_e64 v131, v131\n" \
"v_smfmac_f32_16x16x64_bf16 v[176:179], a[192:195], v[192:199], v228\n" \
"ds_read_b64_tr_b16 a[134:135], v211 offset:36864\n" \
"ds_read_b64_tr_b16 a[136:137], %[vvb] offset:33280\n" \
"v_mfma_f32_16x16x32_bf16 v[136:139], a[64:67], a[16:19], v[164:167]\n" \
"v_exp_f32_e64 v132, v132\n" \
"v_mfma_f32_16x16x32_bf16 v[136:139], a[68:71], a[20:23], v[136:139]\n" \
"ds_read_b64_tr_b16 a[138:139], %[vvb] offset:37376\n" \
"ds_read_b64_tr_b16 a[140:141], v211 offset:33280\n" \
"v_mfma_f32_16x16x32_bf16 v[136:139], a[72:75], a[24:27], v[136:139]\n" \
"v_exp_f32_e64 v133, v133\n" \
"v_mfma_f32_16x16x32_bf16 v[136:139], a[76:79], a[28:31], v[136:139]\n" \
"v_cvt_pk_bf16_f32 v184, v128, v129\n" \
"v_cvt_pk_bf16_f32 v185, v130, v131\n" \
"v_mfma_f32_16x16x32_bf16 v[140:143], a[80:83], a[16:19], v[164:167]\n" \
"v_exp_f32_e64 v134, v134\n" \
"v_mfma_f32_16x16x32_bf16 v[140:143], a[84:87], a[20:23], v[140:143]\n" \
"ds_read_b64_tr_b16 a[142:143], v211 offset:37376\n" \
"s_waitcnt lgkmcnt(8)\n" \
"v_mfma_f32_16x16x32_bf16 v[140:143], a[88:91], a[24:27], v[140:143]\n" \
"buffer_load_dwordx4 v212, %[srdk], s48 offen offset:1024 lds\n" \
"v_mfma_f32_16x16x32_bf16 v[140:143], a[92:95], a[28:31], v[140:143]\n" \
"v_exp_f32_e32 v135, v135\n" \
"v_mfma_f32_16x16x32_bf16 v[80:83], a[144:147], v[216:219], v[80:83]\n" \
"ds_read_b64_tr_b16 a[144:145], %[vvb] offset:33792\n" \
"ds_read_b64_tr_b16 a[146:147], %[vvb] offset:37888\n" \
"v_mfma_f32_16x16x32_bf16 v[80:83], a[176:179], v[200:203], v[80:83]\n" \
"v_exp_f32_e64 v136, v136\n" \
"v_mfma_f32_16x16x32_bf16 v[84:87], a[148:151], v[216:219], v[84:87]\n" \
"v_cvt_pk_bf16_f32 v186, v132, v133\n" \
"v_cvt_pk_bf16_f32 v187, v134, v135\n" \
"v_mfma_f32_16x16x32_bf16 v[84:87], a[180:183], v[200:203], v[84:87]\n" \
"v_exp_f32_e64 v137, v137\n" \
"v_mfma_f32_16x16x32_bf16 v[88:91], a[152:155], v[216:219], v[88:91]\n" \
"v_exp_f32_e64 v138, v138\n" \
"v_mfma_f32_16x16x32_bf16 v[88:91], a[184:187], v[200:203], v[88:91]\n" \
"ds_read_b64_tr_b16 a[148:149], v211 offset:33792\n" \
"ds_read_b64_tr_b16 a[150:151], v211 offset:37888\n" \
"v_mfma_f32_16x16x32_bf16 v[92:95], a[156:159], v[216:219], v[92:95]\n" \
"v_exp_f32_e64 v139, v139\n" \
"v_mfma_f32_16x16x32_bf16 v[92:95], a[188:191], v[200:203], v[92:95]\n" \
"ds_read_b64_tr_b16 a[152:153], %[vvb] offset:34304\n" \
"buffer_store_dwordx4 a[196:199], v235, %[srdz], s62 offen offset:0 sc1\n" \
"v_mfma_f32_16x16x32_bf16 v[144:147], a[64:67], a[32:35], v[168:171]\n" \
"v_exp_f32_e64 v140, v140\n" \
"v_mfma_f32_16x16x32_bf16 v[144:147], a[68:71], a[36:39], v[144:147]\n" \
"ds_read_b64_tr_b16 a[154:155], %[vvb] offset:38400\n" \
"ds_read_b64_tr_b16 a[156:157], v211 offset:34304\n" \
"v_mfma_f32_16x16x32_bf16 v[144:147], a[72:75], a[40:43], v[144:147]\n" \
"v_exp_f32_e64 v141, v141\n" \
"v_mfma_f32_16x16x32_bf16 v[144:147], a[76:79], a[44:47], v[144:147]\n" \
"v_cvt_pk_bf16_f32 v188, v136, v137\n" \
"v_cvt_pk_bf16_f32 v189, v138, v139\n" \
"v_mfma_f32_16x16x32_bf16 v[148:151], a[80:83], a[32:35], v[168:171]\n" \
"v_exp_f32_e64 v142, v142\n" \
"v_mfma_f32_16x16x32_bf16 v[148:151], a[84:87], a[36:39], v[148:151]\n" \
"ds_read_b64_tr_b16 a[158:159], v211 offset:38400\n" \
"s_waitcnt lgkmcnt(8)\n" \
"v_mfma_f32_16x16x32_bf16 v[148:151], a[88:91], a[40:43], v[148:151]\n" \
"buffer_load_dwordx4 v213, %[srdk], s48 offen offset:2048 lds\n" \
"v_mfma_f32_16x16x32_bf16 v[148:151], a[92:95], a[44:47], v[148:151]\n" \
"v_exp_f32_e32 v143, v143\n" \
"v_mfma_f32_16x16x32_bf16 v[0:3], a[160:163], v[192:195], v[0:3]\n" \
"ds_read_b128 a[96:99], %[kb0] offset:40960\n" \
"v_mfma_f32_16x16x32_bf16 v[0:3], a[128:131], v[184:187], v[0:3]\n" \
"v_exp_f32_e64 v144, v144\n" \
"v_mfma_f32_16x16x32_bf16 v[4:7], a[164:167], v[192:195], v[4:7]\n" \
"v_cvt_pk_bf16_f32 v190, v140, v141\n" \
"v_cvt_pk_bf16_f32 v191, v142, v143\n" \
"v_mfma_f32_16x16x32_bf16 v[4:7], a[132:135], v[184:187], v[4:7]\n" \
"v_exp_f32_e64 v145, v145\n" \
"v_mfma_f32_16x16x32_bf16 v[8:11], a[168:171], v[192:195], v[8:11]\n" \
"ds_read_b128 a[100:103], v208 offset:40960\n" \
"v_mfma_f32_16x16x32_bf16 v[8:11], a[136:139], v[184:187], v[8:11]\n" \
"v_exp_f32_e64 v146, v146\n" \
"v_mfma_f32_16x16x32_bf16 v[12:15], a[172:175], v[192:195], v[12:15]\n" \
"ds_read_b128 a[104:107], v209 offset:40960\n" \
"v_mfma_f32_16x16x32_bf16 v[12:15], a[140:143], v[184:187], v[12:15]\n" \
"v_exp_f32_e64 v147, v147\n" \
"v_smfmac_f32_16x16x64_bf16 v[180:183], a[192:195], v[200:207], v228\n" \
"ds_read_b128 a[108:111], v210 offset:40960\n" \
"v_mfma_f32_16x16x32_bf16 v[152:155], a[64:67], a[48:51], v[172:175]\n" \
"v_exp_f32_e64 v148, v148\n" \
"v_mfma_f32_16x16x32_bf16 v[152:155], a[68:71], a[52:55], v[152:155]\n" \
"ds_read_b128 a[112:115], %[kb0] offset:45056\n" \
"v_mfma_f32_16x16x32_bf16 v[152:155], a[72:75], a[56:59], v[152:155]\n" \
"v_exp_f32_e64 v149, v149\n" \
"v_mfma_f32_16x16x32_bf16 v[152:155], a[76:79], a[60:63], v[152:155]\n" \
"v_cvt_pk_bf16_f32 v216, v144, v145\n" \
"v_cvt_pk_bf16_f32 v217, v146, v147\n" \
"v_mfma_f32_16x16x32_bf16 v[156:159], a[80:83], a[48:51], v[172:175]\n" \
"v_exp_f32_e64 v150, v150\n" \
"v_mfma_f32_16x16x32_bf16 v[156:159], a[84:87], a[52:55], v[156:159]\n" \
"ds_read_b128 a[116:119], v208 offset:45056\n" \
"s_waitcnt lgkmcnt(6)\n" \
"v_mfma_f32_16x16x32_bf16 v[156:159], a[88:91], a[56:59], v[156:159]\n" \
"buffer_load_dwordx4 v214, %[srdk], s48 offen offset:3072 lds\n" \
"v_mfma_f32_16x16x32_bf16 v[156:159], a[92:95], a[60:63], v[156:159]\n" \
"v_exp_f32_e32 v151, v151\n" \
"v_mfma_f32_16x16x32_bf16 v[16:19], a[176:179], v[192:195], v[16:19]\n" \
"ds_read_b128 a[120:123], v209 offset:45056\n" \
"v_mfma_f32_16x16x32_bf16 v[16:19], a[144:147], v[184:187], v[16:19]\n" \
"v_exp_f32_e64 v152, v152\n" \
"v_mfma_f32_16x16x32_bf16 v[20:23], a[180:183], v[192:195], v[20:23]\n" \
"v_cvt_pk_bf16_f32 v218, v148, v149\n" \
"v_cvt_pk_bf16_f32 v219, v150, v151\n" \
"v_mfma_f32_16x16x32_bf16 v[20:23], a[148:151], v[184:187], v[20:23]\n" \
"v_exp_f32_e64 v153, v153\n" \
"v_mfma_f32_16x16x32_bf16 v[24:27], a[184:187], v[192:195], v[24:27]\n" \
"v_exp_f32_e64 v154, v154\n" \
"v_mfma_f32_16x16x32_bf16 v[24:27], a[152:155], v[184:187], v[24:27]\n" \
"ds_read_b128 a[124:127], v210 offset:45056\n" \
"s_add_u32 s48, s48, 0x4000\n" \
"v_mfma_f32_16x16x32_bf16 v[28:31], a[188:191], v[192:195], v[28:31]\n" \
"v_exp_f32_e64 v155, v155\n" \
"v_mfma_f32_16x16x32_bf16 v[28:31], a[156:159], v[184:187], v[28:31]\n" \
"s_waitcnt vmcnt(14) lgkmcnt(2)\n" \
"s_barrier\n" \
"v_mfma_f32_16x16x32_bf16 v[128:131], a[96:99], a[0:3], v[160:163]\n" \
"v_exp_f32_e64 v156, v156\n" \
"v_mfma_f32_16x16x32_bf16 v[128:131], a[100:103], a[4:7], v[128:131]\n" \
"s_add_u32 m0, %[m0b], 81920\n" \
"v_mfma_f32_16x16x32_bf16 v[128:131], a[104:107], a[8:11], v[128:131]\n" \
"v_exp_f32_e64 v157, v157\n" \
"v_mfma_f32_16x16x32_bf16 v[128:131], a[108:111], a[12:15], v[128:131]\n" \
"v_cvt_pk_bf16_f32 v220, v152, v153\n" \
"v_cvt_pk_bf16_f32 v221, v154, v155\n" \
"v_mfma_f32_16x16x32_bf16 v[132:135], a[112:115], a[0:3], v[160:163]\n" \
"v_exp_f32_e64 v158, v158\n" \
"v_mfma_f32_16x16x32_bf16 v[132:135], a[116:119], a[4:7], v[132:135]\n" \
"s_waitcnt lgkmcnt(0)\n" \
"v_mfma_f32_16x16x32_bf16 v[132:135], a[120:123], a[8:11], v[132:135]\n" \
"buffer_load_dwordx4 %[vd0], %[srdv], s48 offen offset:0 lds\n" \
"v_mfma_f32_16x16x32_bf16 v[132:135], a[124:127], a[12:15], v[132:135]\n" \
"v_exp_f32_e32 v159, v159\n" \
"v_mfma_f32_16x16x32_bf16 v[32:35], a[160:163], v[196:199], v[32:35]\n" \
"ds_read_b128 a[64:67], %[kb0] offset:49152\n" \
"v_mfma_f32_16x16x32_bf16 v[32:35], a[128:131], v[188:191], v[32:35]\n" \
"v_exp_f32_e64 v128, v128\n" \
"v_mfma_f32_16x16x32_bf16 v[36:39], a[164:167], v[196:199], v[36:39]\n" \
"v_cvt_pk_bf16_f32 v222, v156, v157\n" \
"v_cvt_pk_bf16_f32 v223, v158, v159\n" \
"v_mfma_f32_16x16x32_bf16 v[36:39], a[132:135], v[188:191], v[36:39]\n" \
"v_exp_f32_e64 v129, v129\n" \
"v_mfma_f32_16x16x32_bf16 v[40:43], a[168:171], v[196:199], v[40:43]\n" \
"ds_read_b128 a[68:71], v208 offset:49152\n" \
"v_mfma_f32_16x16x32_bf16 v[40:43], a[136:139], v[188:191], v[40:43]\n" \
"v_exp_f32_e64 v130, v130\n" \
"v_mfma_f32_16x16x32_bf16 v[44:47], a[172:175], v[196:199], v[44:47]\n" \
"ds_read_b128 a[72:75], v209 offset:49152\n" \
"v_mfma_f32_16x16x32_bf16 v[44:47], a[140:143], v[188:191], v[44:47]\n" \
"v_exp_f32_e64 v131, v131\n" \
"v_smfmac_f32_16x16x64_bf16 v[176:179], a[192:195], v[184:191], v228\n" \
"v_mfma_f32_16x16x32_bf16 v[136:139], a[96:99], a[16:19], v[164:167]\n" \
"v_exp_f32_e64 v132, v132\n" \
"v_mfma_f32_16x16x32_bf16 v[136:139], a[100:103], a[20:23], v[136:139]\n" \
"ds_read_b128 a[76:79], v210 offset:49152\n" \
"v_mfma_f32_16x16x32_bf16 v[136:139], a[104:107], a[24:27], v[136:139]\n" \
"v_exp_f32_e64 v133, v133\n" \
"v_mfma_f32_16x16x32_bf16 v[136:139], a[108:111], a[28:31], v[136:139]\n" \
"v_cvt_pk_bf16_f32 v192, v128, v129\n" \
"v_cvt_pk_bf16_f32 v193, v130, v131\n" \
"v_mfma_f32_16x16x32_bf16 v[140:143], a[112:115], a[16:19], v[164:167]\n" \
"v_exp_f32_e64 v134, v134\n" \
"v_mfma_f32_16x16x32_bf16 v[140:143], a[116:119], a[20:23], v[140:143]\n" \
"ds_read_b128 a[80:83], %[kb0] offset:53248\n" \
"v_mfma_f32_16x16x32_bf16 v[140:143], a[120:123], a[24:27], v[140:143]\n" \
"buffer_load_dwordx4 v215, %[srdv], s48 offen offset:1024 lds\n" \
"v_mfma_f32_16x16x32_bf16 v[140:143], a[124:127], a[28:31], v[140:143]\n" \
"v_exp_f32_e64 v135, v135\n" \
"v_mfma_f32_16x16x32_bf16 v[48:51], a[176:179], v[196:199], v[48:51]\n" \
"ds_read_b128 a[84:87], v208 offset:53248\n" \
"v_mfma_f32_16x16x32_bf16 v[48:51], a[144:147], v[188:191], v[48:51]\n" \
"v_exp_f32_e64 v136, v136\n" \
"v_mfma_f32_16x16x32_bf16 v[52:55], a[180:183], v[196:199], v[52:55]\n" \
"v_cvt_pk_bf16_f32 v194, v132, v133\n" \
"v_cvt_pk_bf16_f32 v195, v134, v135\n" \
"v_mfma_f32_16x16x32_bf16 v[52:55], a[148:151], v[188:191], v[52:55]\n" \
"v_exp_f32_e64 v137, v137\n" \
"v_mfma_f32_16x16x32_bf16 v[56:59], a[184:187], v[196:199], v[56:59]\n" \
"v_exp_f32_e64 v138, v138\n" \
"v_mfma_f32_16x16x32_bf16 v[56:59], a[152:155], v[188:191], v[56:59]\n" \
"ds_read_b128 a[88:91], v209 offset:53248\n" \
"v_mfma_f32_16x16x32_bf16 v[60:63], a[188:191], v[196:199], v[60:63]\n" \
"v_exp_f32_e64 v139, v139\n" \
"v_mfma_f32_16x16x32_bf16 v[60:63], a[156:159], v[188:191], v[60:63]\n" \
"s_cmp_lg_u32 %[wid], 1\n" \
"s_cbranch_scc1 LtA5_%=\n" \
"global_load_dword v237, v224, s[58:59]\n" \
"s_add_u32 s61, s48, 49152\n" \
"s_mov_b32 exec_hi, 0\n" \
"s_nop 0\n" \
"buffer_load_dword v237, v224, %[srdp], s61 offen\n" \
"s_mov_b32 exec_hi, -1\n" \
"s_nop 0\n" \
"LtA5_%=:\n" \
"v_mfma_f32_16x16x32_bf16 v[144:147], a[96:99], a[32:35], v[168:171]\n" \
"v_exp_f32_e64 v140, v140\n" \
"v_mfma_f32_16x16x32_bf16 v[144:147], a[100:103], a[36:39], v[144:147]\n" \
"ds_read_b128 a[92:95], v210 offset:53248\n" \
"v_mfma_f32_16x16x32_bf16 v[144:147], a[104:107], a[40:43], v[144:147]\n" \
"v_exp_f32_e64 v141, v141\n" \
"v_mfma_f32_16x16x32_bf16 v[144:147], a[108:111], a[44:47], v[144:147]\n" \
"v_cvt_pk_bf16_f32 v196, v136, v137\n" \
"v_cvt_pk_bf16_f32 v197, v138, v139\n" \
"v_mfma_f32_16x16x32_bf16 v[148:151], a[112:115], a[32:35], v[168:171]\n" \
"v_exp_f32_e64 v142, v142\n" \
"v_mfma_f32_16x16x32_bf16 v[148:151], a[116:119], a[36:39], v[148:151]\n" \
"v_mfma_f32_16x16x32_bf16 v[148:151], a[120:123], a[40:43], v[148:151]\n" \
"buffer_load_dwordx4 %[vd0], %[srdv], s48 offen offset:2048 lds\n" \
"v_mfma_f32_16x16x32_bf16 v[148:151], a[124:127], a[44:47], v[148:151]\n" \
"v_exp_f32_e64 v143, v143\n" \
"v_mfma_f32_16x16x32_bf16 v[96:99], a[160:163], v[204:207], v[96:99]\n" \
"ds_read_b64_tr_b16 a[160:161], %[vvb] offset:40960\n" \
"v_mfma_f32_16x16x32_bf16 v[96:99], a[128:131], v[220:223], v[96:99]\n" \
"v_exp_f32_e64 v144, v144\n" \
"v_mfma_f32_16x16x32_bf16 v[100:103], a[164:167], v[204:207], v[100:103]\n" \
"v_cvt_pk_bf16_f32 v198, v140, v141\n" \
"v_cvt_pk_bf16_f32 v199, v142, v143\n" \
"v_mfma_f32_16x16x32_bf16 v[100:103], a[132:135], v[220:223], v[100:103]\n" \
"v_exp_f32_e64 v145, v145\n" \
"v_mfma_f32_16x16x32_bf16 v[104:107], a[168:171], v[204:207], v[104:107]\n" \
"ds_read_b64_tr_b16 a[162:163], %[vvb] offset:45056\n" \
"ds_read_b64_tr_b16 a[164:165], v211 offset:40960\n" \
"v_mfma_f32_16x16x32_bf16 v[104:107], a[136:139], v[220:223], v[104:107]\n" \
"v_exp_f32_e64 v146, v146\n" \
"v_mfma_f32_16x16x32_bf16 v[108:111], a[172:175], v[204:207], v[108:111]\n" \
"ds_read_b64_tr_b16 a[166:167], v211 offset:45056\n" \
"v_mfma_f32_16x16x32_bf16 v[108:111], a[140:143], v[220:223], v[108:111]\n" \
"v_exp_f32_e64 v147, v147\n" \
"v_smfmac_f32_16x16x64_bf16 v[180:183], a[192:195], v[216:223], v228\n" \
"ds_read_b64_tr_b16 a[168:169], %[vvb] offset:41472\n" \
"v_mfma_f32_16x16x32_bf16 v[152:155], a[96:99], a[48:51], v[172:175]\n" \
"v_exp_f32_e64 v148, v148\n" \
"v_mfma_f32_16x16x32_bf16 v[152:155], a[100:103], a[52:55], v[152:155]\n" \
"ds_read_b64_tr_b16 a[170:171], %[vvb] offset:45568\n" \
"v_mfma_f32_16x16x32_bf16 v[152:155], a[104:107], a[56:59], v[152:155]\n" \
"v_exp_f32_e64 v149, v149\n" \
"v_mfma_f32_16x16x32_bf16 v[152:155], a[108:111], a[60:63], v[152:155]\n" \
"v_cvt_pk_bf16_f32 v200, v144, v145\n" \
"v_cvt_pk_bf16_f32 v201, v146, v147\n" \
"v_mfma_f32_16x16x32_bf16 v[156:159], a[112:115], a[48:51], v[172:175]\n" \
"v_exp_f32_e64 v150, v150\n" \
"v_mfma_f32_16x16x32_bf16 v[156:159], a[116:119], a[52:55], v[156:159]\n" \
"ds_read_b64_tr_b16 a[172:173], v211 offset:41472\n" \
"v_mfma_f32_16x16x32_bf16 v[156:159], a[120:123], a[56:59], v[156:159]\n" \
"buffer_load_dwordx4 v215, %[srdv], s48 offen offset:3072 lds\n" \
"v_mfma_f32_16x16x32_bf16 v[156:159], a[124:127], a[60:63], v[156:159]\n" \
"v_exp_f32_e64 v151, v151\n" \
"v_mfma_f32_16x16x32_bf16 v[112:115], a[176:179], v[204:207], v[112:115]\n" \
"ds_read_b64_tr_b16 a[174:175], v211 offset:45568\n" \
"ds_read_b64_tr_b16 a[176:177], %[vvb] offset:41984\n" \
"v_mfma_f32_16x16x32_bf16 v[112:115], a[144:147], v[220:223], v[112:115]\n" \
"v_exp_f32_e64 v152, v152\n" \
"v_mfma_f32_16x16x32_bf16 v[116:119], a[180:183], v[204:207], v[116:119]\n" \
"v_cvt_pk_bf16_f32 v202, v148, v149\n" \
".if . - adaln_body != 16384\n" \
".error \"size model\"\n" \
".endif\n" \
"Lk3_%=:\n" \
"s_cbranch_execz Lk4_%=\n" \
"s_nop 0\n" \
"v_cvt_pk_bf16_f32 v203, v150, v151\n" \
"v_mfma_f32_16x16x32_bf16 v[116:119], a[148:151], v[220:223], v[116:119]\n" \
"v_exp_f32_e64 v153, v153\n" \
"v_mfma_f32_16x16x32_bf16 v[120:123], a[184:187], v[204:207], v[120:123]\n" \
"v_exp_f32_e64 v154, v154\n" \
"v_mfma_f32_16x16x32_bf16 v[120:123], a[152:155], v[220:223], v[120:123]\n" \
"ds_read_b64_tr_b16 a[178:179], %[vvb] offset:46080\n" \
"v_mfma_f32_16x16x32_bf16 v[124:127], a[188:191], v[204:207], v[124:127]\n" \
"v_exp_f32_e64 v155, v155\n" \
"v_mfma_f32_16x16x32_bf16 v[124:127], a[156:159], v[220:223], v[124:127]\n" \
"ds_read_b64_tr_b16 a[180:181], v211 offset:41984\n" \
"s_waitcnt lgkmcnt(13)\n" \
"v_mfma_f32_16x16x32_bf16 v[128:131], a[64:67], a[0:3], v[160:163]\n" \
"v_exp_f32_e32 v156, v156\n" \
"v_mfma_f32_16x16x32_bf16 v[128:131], a[68:71], a[4:7], v[128:131]\n" \
"ds_read_b64_tr_b16 a[182:183], v211 offset:46080\n" \
"s_add_u32 m0, %[m0b], 32768\n" \
"v_mfma_f32_16x16x32_bf16 v[128:131], a[72:75], a[8:11], v[128:131]\n" \
"v_exp_f32_e64 v157, v157\n" \
"v_mfma_f32_16x16x32_bf16 v[128:131], a[76:79], a[12:15], v[128:131]\n" \
"v_cvt_pk_bf16_f32 v204, v152, v153\n" \
"v_cvt_pk_bf16_f32 v205, v154, v155\n" \
"v_mfma_f32_16x16x32_bf16 v[132:135], a[80:83], a[0:3], v[160:163]\n" \
"v_exp_f32_e64 v158, v158\n" \
"v_mfma_f32_16x16x32_bf16 v[132:135], a[84:87], a[4:7], v[132:135]\n" \
"ds_read_b64_tr_b16 a[184:185], %[vvb] offset:42496\n" \
"s_waitcnt lgkmcnt(5)\n" \
"v_mfma_f32_16x16x32_bf16 v[132:135], a[88:91], a[8:11], v[132:135]\n" \
"buffer_load_dwordx4 %[kd0], %[srdk], s48 offen offset:0 lds\n" \
"v_mfma_f32_16x16x32_bf16 v[132:135], a[92:95], a[12:15], v[132:135]\n" \
"v_exp_f32_e32 v159, v159\n" \
"v_mfma_f32_16x16x32_bf16 v[64:67], a[128:131], v[216:219], v[64:67]\n" \
"ds_read_b64_tr_b16 a[186:187], %[vvb] offset:46592\n" \
"ds_read_b64_tr_b16 a[188:189], v211 offset:42496\n" \
"v_mfma_f32_16x16x32_bf16 v[64:67], a[160:163], v[200:203], v[64:67]\n" \
"v_exp_f32_e64 v128, v128\n" \
"v_mfma_f32_16x16x32_bf16 v[68:71], a[132:135], v[216:219], v[68:71]\n" \
"v_cvt_pk_bf16_f32 v206, v156, v157\n" \
"v_cvt_pk_bf16_f32 v207, v158, v159\n" \
"v_mfma_f32_16x16x32_bf16 v[68:71], a[164:167], v[200:203], v[68:71]\n" \
"v_exp_f32_e64 v129, v129\n" \
"v_mfma_f32_16x16x32_bf16 v[72:75], a[136:139], v[216:219], v[72:75]\n" \
"ds_read_b64_tr_b16 a[190:191], v211 offset:46592\n" \
"ds_read_b64_tr_b16 a[128:129], %[vvb] offset:49152\n" \
"v_mfma_f32_16x16x32_bf16 v[72:75], a[168:171], v[200:203], v[72:75]\n" \
"v_exp_f32_e64 v130, v130\n" \
"v_mfma_f32_16x16x32_bf16 v[76:79], a[140:143], v[216:219], v[76:79]\n" \
"ds_read_b64_tr_b16 a[130:131], %[vvb] offset:53248\n" \
"ds_read_b64_tr_b16 a[132:133], v211 offset:49152\n" \
"v_mfma_f32_16x16x32_bf16 v[76:79], a[172:175], v[200:203], v[76:79]\n" \
"v_exp_f32_e64 v131, v131\n" \
"v_smfmac_f32_16x16x64_bf16 v[176:179], a[192:195], v[192:199], v228\n" \
"ds_read_b64_tr_b16 a[134:135], v211 offset:53248\n" \
"ds_read_b64_tr_b16 a[136:137], %[vvb] offset:49664\n" \
"v_mfma_f32_16x16x32_bf16 v[136:139], a[64:67], a[16:19], v[164:167]\n" \
"v_exp_f32_e64 v132, v132\n" \
"v_mfma_f32_16x16x32_bf16 v[136:139], a[68:71], a[20:23], v[136:139]\n" \
"ds_read_b64_tr_b16 a[138:139], %[vvb] offset:53760\n" \
"ds_read_b64_tr_b16 a[140:141], v211 offset:49664\n" \
"v_mfma_f32_16x16x32_bf16 v[136:139], a[72:75], a[24:27], v[136:139]\n" \
"v_exp_f32_e64 v133, v133\n" \
"v_mfma_f32_16x16x32_bf16 v[136:139], a[76:79], a[28:31], v[136:139]\n" \
"v_cvt_pk_bf16_f32 v184, v128, v129\n" \
"v_cvt_pk_bf16_f32 v185, v130, v131\n" \
"v_mfma_f32_16x16x32_bf16 v[140:143], a[80:83], a[16:19], v[164:167]\n" \
"v_exp_f32_e64 v134, v134\n" \
"v_mfma_f32_16x16x32_bf16 v[140:143], a[84:87], a[20:23], v[140:143]\n" \
"ds_read_b64_tr_b16 a[142:143], v211 offset:53760\n" \
"s_waitcnt lgkmcnt(8)\n" \
"v_mfma_f32_16x16x32_bf16 v[140:143], a[88:91], a[24:27], v[140:143]\n" \
"buffer_load_dwordx4 v212, %[srdk], s48 offen offset:1024 lds\n" \
"v_mfma_f32_16x16x32_bf16 v[140:143], a[92:95], a[28:31], v[140:143]\n" \
"v_exp_f32_e32 v135, v135\n" \
"v_mfma_f32_16x16x32_bf16 v[80:83], a[144:147], v[216:219], v[80:83]\n" \
"ds_read_b64_tr_b16 a[144:145], %[vvb] offset:50176\n" \
"ds_read_b64_tr_b16 a[146:147], %[vvb] offset:54272\n" \
"v_mfma_f32_16x16x32_bf16 v[80:83], a[176:179], v[200:203], v[80:83]\n" \
"v_exp_f32_e64 v136, v136\n" \
"v_mfma_f32_16x16x32_bf16 v[84:87], a[148:151], v[216:219], v[84:87]\n" \
"v_cvt_pk_bf16_f32 v186, v132, v133\n" \
"v_cvt_pk_bf16_f32 v187, v134, v135\n" \
"v_mfma_f32_16x16x32_bf16 v[84:87], a[180:183], v[200:203], v[84:87]\n" \
"v_exp_f32_e64 v137, v137\n" \
"v_mfma_f32_16x16x32_bf16 v[88:91], a[152:155], v[216:219], v[88:91]\n" \
"v_exp_f32_e64 v138, v138\n" \
"v_mfma_f32_16x16x32_bf16 v[88:91], a[184:187], v[200:203], v[88:91]\n" \
"ds_read_b64_tr_b16 a[148:149], v211 offset:50176\n" \
"ds_read_b64_tr_b16 a[150:151], v211 offset:54272\n" \
"v_mfma_f32_16x16x32_bf16 v[92:95], a[156:159], v[216:219], v[92:95]\n" \
"v_exp_f32_e64 v139, v139\n" \
"v_mfma_f32_16x16x32_bf16 v[92:95], a[188:191], v[200:203], v[92:95]\n" \
"ds_read_b64_tr_b16 a[152:153], %[vvb] offset:50688\n" \
"global_load_dword v237, %[vct], %[pct]\n" \
"v_mfma_f32_16x16x32_bf16 v[144:147], a[64:67], a[32:35], v[168:171]\n" \
"v_exp_f32_e64 v140, v140\n" \
"v_mfma_f32_16x16x32_bf16 v[144:147], a[68:71], a[36:39], v[144:147]\n" \
"ds_read_b64_tr_b16 a[154:155], %[vvb] offset:54784\n" \
"ds_read_b64_tr_b16 a[156:157], v211 offset:50688\n" \
"v_mfma_f32_16x16x32_bf16 v[144:147], a[72:75], a[40:43], v[144:147]\n" \
"v_exp_f32_e64 v141, v141\n" \
"v_mfma_f32_16x16x32_bf16 v[144:147], a[76:79], a[44:47], v[144:147]\n" \
"v_cvt_pk_bf16_f32 v188, v136, v137\n" \
"v_cvt_pk_bf16_f32 v189, v138, v139\n" \
"v_mfma_f32_16x16x32_bf16 v[148:151], a[80:83], a[32:35], v[168:171]\n" \
"v_exp_f32_e64 v142, v142\n" \
"v_mfma_f32_16x16x32_bf16 v[148:151], a[84:87], a[36:39], v[148:151]\n" \
"ds_read_b64_tr_b16 a[158:159], v211 offset:54784\n" \
"s_waitcnt lgkmcnt(8)\n" \
"v_mfma_f32_16x16x32_bf16 v[148:151], a[88:91], a[40:43], v[148:151]\n" \
"buffer_load_dwordx4 v213, %[srdk], s48 offen offset:2048 lds\n" \
"v_mfma_f32_16x16x32_bf16 v[148:151], a[92:95], a[44:47], v[148:151]\n" \
"v_exp_f32_e32 v143, v143\n" \
"v_mfma_f32_16x16x32_bf16 v[0:3], a[160:163], v[192:195], v[0:3]\n" \
"ds_read_b128 a[96:99], %[kb0] offset:57344\n" \
"v_mfma_f32_16x16x32_bf16 v[0:3], a[128:131], v[184:187], v[0:3]\n" \
"v_exp_f32_e64 v144, v144\n" \
"v_mfma_f32_16x16x32_bf16 v[4:7], a[164:167], v[192:195], v[4:7]\n" \
"v_cvt_pk_bf16_f32 v190, v140, v141\n" \
"v_cvt_pk_bf16_f32 v191, v142, v143\n" \
"v_mfma_f32_16x16x32_bf16 v[4:7], a[132:135], v[184:187], v[4:7]\n" \
"v_exp_f32_e64 v145, v145\n" \
"v_mfma_f32_16x16x32_bf16 v[8:11], a[168:171], v[192:195], v[8:11]\n" \
"ds_read_b128 a[100:103], v208 offset:57344\n" \
"v_mfma_f32_16x16x32_bf16 v[8:11], a[136:139], v[184:187], v[8:11]\n" \
"v_exp_f32_e64 v146, v146\n" \
"v_mfma_f32_16x16x32_bf16 v[12:15], a[172:175], v[192:195], v[12:15]\n" \
"ds_read_b128 a[104:107], v209 offset:57344\n" \
"v_mfma_f32_16x16x32_bf16 v[12:15], a[140:143], v[184:187], v[12:15]\n" \
"v_exp_f32_e64 v147, v147\n" \
"v_smfmac_f32_16x16x64_bf16 v[180:183], a[192:195], v[200:207], v228\n" \
"ds_read_b128 a[108:111], v210 offset:57344\n" \
"v_mfma_f32_16x16x32_bf16 v[152:155], a[64:67], a[48:51], v[172:175]\n" \
"v_exp_f32_e64 v148, v148\n" \
"v_mfma_f32_16x16x32_bf16 v[152:155], a[68:71], a[52:55], v[152:155]\n" \
"ds_read_b128 a[112:115], %[kb0] offset:61440\n" \
"v_mfma_f32_16x16x32_bf16 v[152:155], a[72:75], a[56:59], v[152:155]\n" \
"v_exp_f32_e64 v149, v149\n" \
"v_mfma_f32_16x16x32_bf16 v[152:155], a[76:79], a[60:63], v[152:155]\n" \
"v_cvt_pk_bf16_f32 v216, v144, v145\n" \
"v_cvt_pk_bf16_f32 v217, v146, v147\n" \
"v_mfma_f32_16x16x32_bf16 v[156:159], a[80:83], a[48:51], v[172:175]\n" \
"v_exp_f32_e64 v150, v150\n" \
"v_mfma_f32_16x16x32_bf16 v[156:159], a[84:87], a[52:55], v[156:159]\n" \
"ds_read_b128 a[116:119], v208 offset:61440\n" \
"s_waitcnt lgkmcnt(6)\n" \
"v_mfma_f32_16x16x32_bf16 v[156:159], a[88:91], a[56:59], v[156:159]\n" \
"buffer_load_dwordx4 v214, %[srdk], s48 offen offset:3072 lds\n" \
"v_mfma_f32_16x16x32_bf16 v[156:159], a[92:95], a[60:63], v[156:159]\n" \
"v_exp_f32_e32 v151, v151\n" \
"v_mfma_f32_16x16x32_bf16 v[16:19], a[176:179], v[192:195], v[16:19]\n" \
"ds_read_b128 a[120:123], v209 offset:61440\n" \
"v_mfma_f32_16x16x32_bf16 v[16:19], a[144:147], v[184:187], v[16:19]\n" \
"v_exp_f32_e64 v152, v152\n" \
"v_mfma_f32_16x16x32_bf16 v[20:23], a[180:183], v[192:195], v[20:23]\n" \
"v_cvt_pk_bf16_f32 v218, v148, v149\n" \
"v_cvt_pk_bf16_f32 v219, v150, v151\n" \
"v_mfma_f32_16x16x32_bf16 v[20:23], a[148:151], v[184:187], v[20:23]\n" \
"v_exp_f32_e64 v153, v153\n" \
"v_mfma_f32_16x16x32_bf16 v[24:27], a[184:187], v[192:195], v[24:27]\n" \
"v_exp_f32_e64 v154, v154\n" \
"v_mfma_f32_16x16x32_bf16 v[24:27], a[152:155], v[184:187], v[24:27]\n" \
"ds_read_b128 a[124:127], v210 offset:61440\n" \
"s_add_u32 s48, s48, 0x4000\n" \
"s_mov_b32 m0, s44\n" \
"s_addk_i32 s44, 0x400\n" \
"buffer_load_dwordx4 v229, s[64:67], s45 offen offset:0 nt lds\n" \
"s_add_u32 s45, s45, 393216\n" \
"v_mfma_f32_16x16x32_bf16 v[28:31], a[188:191], v[192:195], v[28:31]\n" \
"v_exp_f32_e64 v155, v155\n" \
"v_mfma_f32_16x16x32_bf16 v[28:31], a[156:159], v[184:187], v[28:31]\n" \
"s_waitcnt vmcnt(15) lgkmcnt(2)\n" \
"s_barrier\n" \
"v_mfma_f32_16x16x32_bf16 v[128:131], a[96:99], a[0:3], v[160:163]\n" \
"v_exp_f32_e64 v156, v156\n" \
"v_mfma_f32_16x16x32_bf16 v[128:131], a[100:103], a[4:7], v[128:131]\n" \
"s_add_u32 m0, %[m0b], 98304\n" \
"v_mfma_f32_16x16x32_bf16 v[128:131], a[104:107], a[8:11], v[128:131]\n" \
"v_exp_f32_e64 v157, v157\n" \
"v_mfma_f32_16x16x32_bf16 v[128:131], a[108:111], a[12:15], v[128:131]\n" \
"v_cvt_pk_bf16_f32 v220, v152, v153\n" \
"v_cvt_pk_bf16_f32 v221, v154, v155\n" \
"v_mfma_f32_16x16x32_bf16 v[132:135], a[112:115], a[0:3], v[160:163]\n" \
"v_exp_f32_e64 v158, v158\n" \
"v_mfma_f32_16x16x32_bf16 v[132:135], a[116:119], a[4:7], v[132:135]\n" \
"s_waitcnt lgkmcnt(0)\n" \
"v_mfma_f32_16x16x32_bf16 v[132:135], a[120:123], a[8:11], v[132:135]\n" \
"buffer_load_dwordx4 %[vd0], %[srdv], s48 offen offset:0 lds\n" \
"v_mfma_f32_16x16x32_bf16 v[132:135], a[124:127], a[12:15], v[132:135]\n" \
"v_exp_f32_e32 v159, v159\n" \
"v_mfma_f32_16x16x32_bf16 v[32:35], a[160:163], v[196:199], v[32:35]\n" \
"ds_read_b128 a[64:67], %[kb0] offset:0\n" \
"v_mfma_f32_16x16x32_bf16 v[32:35], a[128:131], v[188:191], v[32:35]\n" \
"v_exp_f32_e64 v128, v128\n" \
"v_mfma_f32_16x16x32_bf16 v[36:39], a[164:167], v[196:199], v[36:39]\n" \
"v_cvt_pk_bf16_f32 v222, v156, v157\n" \
"v_cvt_pk_bf16_f32 v223, v158, v159\n" \
"v_mfma_f32_16x16x32_bf16 v[36:39], a[132:135], v[188:191], v[36:39]\n" \
"v_exp_f32_e64 v129, v129\n" \
"v_mfma_f32_16x16x32_bf16 v[40:43], a[168:171], v[196:199], v[40:43]\n" \
"ds_read_b128 a[68:71], v208 offset:0\n" \
"v_mfma_f32_16x16x32_bf16 v[40:43], a[136:139], v[188:191], v[40:43]\n" \
"v_exp_f32_e64 v130, v130\n" \
"v_mfma_f32_16x16x32_bf16 v[44:47], a[172:175], v[196:199], v[44:47]\n" \
"ds_read_b128 a[72:75], v209 offset:0\n" \
"v_mfma_f32_16x16x32_bf16 v[44:47], a[140:143], v[188:191], v[44:47]\n" \
"v_exp_f32_e64 v131, v131\n" \
"v_smfmac_f32_16x16x64_bf16 v[176:179], a[192:195], v[184:191], v228\n" \
"v_mfma_f32_16x16x32_bf16 v[136:139], a[96:99], a[16:19], v[164:167]\n" \
"v_exp_f32_e64 v132, v132\n" \
"v_mfma_f32_16x16x32_bf16 v[136:139], a[100:103], a[20:23], v[136:139]\n" \
"ds_read_b128 a[76:79], v210 offset:0\n" \
"v_mfma_f32_16x16x32_bf16 v[136:139], a[104:107], a[24:27], v[136:139]\n" \
"v_exp_f32_e64 v133, v133\n" \
"v_mfma_f32_16x16x32_bf16 v[136:139], a[108:111], a[28:31], v[136:139]\n" \
"v_cvt_pk_bf16_f32 v192, v128, v129\n" \
"v_cvt_pk_bf16_f32 v193, v130, v131\n" \
"v_mfma_f32_16x16x32_bf16 v[140:143], a[112:115], a[16:19], v[164:167]\n" \
"v_exp_f32_e64 v134, v134\n" \
"v_mfma_f32_16x16x32_bf16 v[140:143], a[116:119], a[20:23], v[140:143]\n" \
"ds_read_b128 a[80:83], %[kb0] offset:4096\n" \
"v_mfma_f32_16x16x32_bf16 v[140:143], a[120:123], a[24:27], v[140:143]\n" \
"buffer_load_dwordx4 v215, %[srdv], s48 offen offset:1024 lds\n" \
"v_mfma_f32_16x16x32_bf16 v[140:143], a[124:127], a[28:31], v[140:143]\n" \
"v_exp_f32_e64 v135, v135\n" \
"v_mfma_f32_16x16x32_bf16 v[48:51], a[176:179], v[196:199], v[48:51]\n" \
"ds_read_b128 a[84:87], v208 offset:4096\n" \
"v_mfma_f32_16x16x32_bf16 v[48:51], a[144:147], v[188:191], v[48:51]\n" \
"v_exp_f32_e64 v136, v136\n" \
"v_mfma_f32_16x16x32_bf16 v[52:55], a[180:183], v[196:199], v[52:55]\n" \
"v_cvt_pk_bf16_f32 v194, v132, v133\n" \
"v_cvt_pk_bf16_f32 v195, v134, v135\n" \
"v_mfma_f32_16x16x32_bf16 v[52:55], a[148:151], v[188:191], v[52:55]\n" \
"v_exp_f32_e64 v137, v137\n" \
"v_mfma_f32_16x16x32_bf16 v[56:59], a[184:187], v[196:199], v[56:59]\n" \
"v_exp_f32_e64 v138, v138\n" \
"v_mfma_f32_16x16x32_bf16 v[56:59], a[152:155], v[188:191], v[56:59]\n" \
"ds_read_b128 a[88:91], v209 offset:4096\n" \
"v_mfma_f32_16x16x32_bf16 v[60:63], a[188:191], v[196:199], v[60:63]\n" \
"v_exp_f32_e64 v139, v139\n" \
"v_mfma_f32_16x16x32_bf16 v[60:63], a[156:159], v[188:191], v[60:63]\n" \
"s_cmp_lg_u32 %[wid], 2\n" \
"s_cbranch_scc1 LtA7_%=\n" \
"global_load_dword v237, v224, s[58:59]\n" \
"s_add_u32 s61, s48, 49152\n" \
"s_mov_b32 exec_hi, 0\n" \
"s_nop 0\n" \
"buffer_load_dword v237, v224, %[srdp], s61 offen\n" \
"s_mov_b32 exec_hi, -1\n" \
"s_nop 0\n" \
"LtA7_%=:\n" \
"v_mfma_f32_16x16x32_bf16 v[144:147], a[96:99], a[32:35], v[168:171]\n" \
"v_exp_f32_e64 v140, v140\n" \
"v_mfma_f32_16x16x32_bf16 v[144:147], a[100:103], a[36:39], v[144:147]\n" \
"ds_read_b128 a[92:95], v210 offset:4096\n" \
"v_mfma_f32_16x16x32_bf16 v[144:147], a[104:107], a[40:43], v[144:147]\n" \
"v_exp_f32_e64 v141, v141\n" \
"v_mfma_f32_16x16x32_bf16 v[144:147], a[108:111], a[44:47], v[144:147]\n" \
"v_cvt_pk_bf16_f32 v196, v136, v137\n" \
"v_cvt_pk_bf16_f32 v197, v138, v139\n" \
"v_mfma_f32_16x16x32_bf16 v[148:151], a[112:115], a[32:35], v[168:171]\n" \
"v_exp_f32_e64 v142, v142\n" \
"v_mfma_f32_16x16x32_bf16 v[148:151], a[116:119], a[36:39], v[148:151]\n" \
"v_mfma_f32_16x16x32_bf16 v[148:151], a[120:123], a[40:43], v[148:151]\n" \
"buffer_load_dwordx4 %[vd0], %[srdv], s48 offen offset:2048 lds\n" \
"v_mfma_f32_16x16x32_bf16 v[148:151], a[124:127], a[44:47], v[148:151]\n" \
"v_exp_f32_e64 v143, v143\n" \
"v_mfma_f32_16x16x32_bf16 v[96:99], a[160:163], v[204:207], v[96:99]\n" \
"ds_read_b64_tr_b16 a[160:161], %[vvb] offset:57344\n" \
"v_mfma_f32_16x16x32_bf16 v[96:99], a[128:131], v[220:223], v[96:99]\n" \
"v_exp_f32_e64 v144, v144\n" \
"v_mfma_f32_16x16x32_bf16 v[100:103], a[164:167], v[204:207], v[100:103]\n" \
"v_cvt_pk_bf16_f32 v198, v140, v141\n" \
"v_cvt_pk_bf16_f32 v199, v142, v143\n" \
"v_mfma_f32_16x16x32_bf16 v[100:103], a[132:135], v[220:223], v[100:103]\n" \
"v_exp_f32_e64 v145, v145\n" \
"v_mfma_f32_16x16x32_bf16 v[104:107], a[168:171], v[204:207], v[104:107]\n" \
"ds_read_b64_tr_b16 a[162:163], %[vvb] offset:61440\n" \
"ds_read_b64_tr_b16 a[164:165], v211 offset:57344\n" \
"v_mfma_f32_16x16x32_bf16 v[104:107], a[136:139], v[220:223], v[104:107]\n" \
"v_exp_f32_e64 v146, v146\n" \
"v_mfma_f32_16x16x32_bf16 v[108:111], a[172:175], v[204:207], v[108:111]\n" \
"ds_read_b64_tr_b16 a[166:167], v211 offset:61440\n" \
"v_mfma_f32_16x16x32_bf16 v[108:111], a[140:143], v[220:223], v[108:111]\n" \
"v_exp_f32_e64 v147, v147\n" \
"v_smfmac_f32_16x16x64_bf16 v[180:183], a[192:195], v[216:223], v228\n" \
"ds_read_b64_tr_b16 a[168:169], %[vvb] offset:57856\n" \
"v_mfma_f32_16x16x32_bf16 v[152:155], a[96:99], a[48:51], v[172:175]\n" \
"v_exp_f32_e64 v148, v148\n" \
"v_mfma_f32_16x16x32_bf16 v[152:155], a[100:103], a[52:55], v[152:155]\n" \
"ds_read_b64_tr_b16 a[170:171], %[vvb] offset:61952\n" \
"v_mfma_f32_16x16x32_bf16 v[152:155], a[104:107], a[56:59], v[152:155]\n" \
"v_exp_f32_e64 v149, v149\n" \
"v_mfma_f32_16x16x32_bf16 v[152:155], a[108:111], a[60:63], v[152:155]\n" \
"v_cvt_pk_bf16_f32 v200, v144, v145\n" \
"v_cvt_pk_bf16_f32 v201, v146, v147\n" \
"v_mfma_f32_16x16x32_bf16 v[156:159], a[112:115], a[48:51], v[172:175]\n" \
"v_exp_f32_e64 v150, v150\n" \
"v_mfma_f32_16x16x32_bf16 v[156:159], a[116:119], a[52:55], v[156:159]\n" \
"ds_read_b64_tr_b16 a[172:173], v211 offset:57856\n" \
"v_mfma_f32_16x16x32_bf16 v[156:159], a[120:123], a[56:59], v[156:159]\n" \
"buffer_load_dwordx4 v215, %[srdv], s48 offen offset:3072 lds\n" \
"v_mfma_f32_16x16x32_bf16 v[156:159], a[124:127], a[60:63], v[156:159]\n" \
"v_exp_f32_e64 v151, v151\n" \
"v_mfma_f32_16x16x32_bf16 v[112:115], a[176:179], v[204:207], v[112:115]\n" \
"ds_read_b64_tr_b16 a[174:175], v211 offset:61952\n" \
"ds_read_b64_tr_b16 a[176:177], %[vvb] offset:58368\n" \
"v_mfma_f32_16x16x32_bf16 v[112:115], a[144:147], v[220:223], v[112:115]\n" \
"v_exp_f32_e64 v152, v152\n" \
"v_mfma_f32_16x16x32_bf16 v[116:119], a[180:183], v[204:207], v[116:119]\n" \
"v_cvt_pk_bf16_f32 v202, v148, v149\n" \
"v_cvt_pk_bf16_f32 v203, v150, v151\n" \
"v_mfma_f32_16x16x32_bf16 v[116:119], a[148:151], v[220:223], v[116:119]\n" \
"v_exp_f32_e64 v153, v153\n" \
"v_mfma_f32_16x16x32_bf16 v[120:123], a[184:187], v[204:207], v[120:123]\n" \
"v_exp_f32_e64 v154, v154\n" \
"v_mfma_f32_16x16x32_bf16 v[120:123], a[152:155], v[220:223], v[120:123]\n" \
"ds_read_b64_tr_b16 a[178:179], %[vvb] offset:62464\n" \
"v_mfma_f32_16x16x32_bf16 v[124:127], a[188:191], v[204:207], v[124:127]\n" \
"v_exp_f32_e64 v155, v155\n" \
"v_mfma_f32_16x16x32_bf16 v[124:127], a[156:159], v[220:223], v[124:127]\n" \
"ds_read_b64_tr_b16 a[180:181], v211 offset:58368\n" \
"s_waitcnt lgkmcnt(13)\n" \
"v_mfma_f32_16x16x32_bf16 v[128:131], a[64:67], a[0:3], v[160:163]\n" \
"v_exp_f32_e32 v156, v156\n" \
"v_mfma_f32_16x16x32_bf16 v[128:131], a[68:71], a[4:7], v[128:131]\n" \
"ds_read_b64_tr_b16 a[182:183], v211 offset:62464\n" \
"s_add_u32 m0, %[m0b], 49152\n" \
"v_mfma_f32_16x16x32_bf16 v[128:131], a[72:75], a[8:11], v[128:131]\n" \
"v_exp_f32_e64 v157, v157\n" \
"v_mfma_f32_16x16x32_bf16 v[128:131], a[76:79], a[12:15], v[128:131]\n" \
"v_cvt_pk_bf16_f32 v204, v152, v153\n" \
"v_cvt_pk_bf16_f32 v205, v154, v155\n" \
"v_mfma_f32_16x16x32_bf16 v[132:135], a[80:83], a[0:3], v[160:163]\n" \
"v_exp_f32_e64 v158, v158\n" \
"v_mfma_f32_16x16x32_bf16 v[132:135], a[84:87], a[4:7], v[132:135]\n" \
"ds_read_b64_tr_b16 a[184:185], %[vvb] offset:58880\n" \
"s_waitcnt lgkmcnt(5)\n" \
"v_mfma_f32_16x16x32_bf16 v[132:135], a[88:91], a[8:11], v[132:135]\n" \
"buffer_load_dwordx4 %[kd0], %[srdk], s48 offen offset:0 lds\n" \
"v_mfma_f32_16x16x32_bf16 v[132:135], a[92:95], a[12:15], v[132:135]\n" \
"v_exp_f32_e32 v159, v159\n" \
"v_mfma_f32_16x16x32_bf16 v[64:67], a[128:131], v[216:219], v[64:67]\n" \
"ds_read_b64_tr_b16 a[186:187], %[vvb] offset:62976\n" \
"ds_read_b64_tr_b16 a[188:189], v211 offset:58880\n" \
"v_mfma_f32_16x16x32_bf16 v[64:67], a[160:163], v[200:203], v[64:67]\n" \
"v_exp_f32_e64 v128, v128\n" \
"v_mfma_f32_16x16x32_bf16 v[68:71], a[132:135], v[216:219], v[68:71]\n" \
"v_cvt_pk_bf16_f32 v206, v156, v157\n" \
"v_cvt_pk_bf16_f32 v207, v158, v159\n" \
"v_mfma_f32_16x16x32_bf16 v[68:71], a[164:167], v[200:203], v[68:71]\n" \
"v_exp_f32_e64 v129, v129\n" \
"v_mfma_f32_16x16x32_bf16 v[72:75], a[136:139], v[216:219], v[72:75]\n" \
"ds_read_b64_tr_b16 a[190:191], v211 offset:62976\n" \
"ds_read_b64_tr_b16 a[128:129], %[vvb] offset:0\n" \
"v_mfma_f32_16x16x32_bf16 v[72:75], a[168:171], v[200:203], v[72:75]\n" \
"v_exp_f32_e64 v130, v130\n" \
"v_mfma_f32_16x16x32_bf16 v[76:79], a[140:143], v[216:219], v[76:79]\n" \
"ds_read_b64_tr_b16 a[130:131], %[vvb] offset:4096\n" \
"ds_read_b64_tr_b16 a[132:133], v211 offset:0\n" \
"v_mfma_f32_16x16x32_bf16 v[76:79], a[172:175], v[200:203], v[76:79]\n" \
"v_exp_f32_e64 v131, v131\n" \
"v_smfmac_f32_16x16x64_bf16 v[176:179], a[192:195], v[192:199], v228\n" \
"ds_read_b64_tr_b16 a[134:135], v211 offset:4096\n" \
"ds_read_b64_tr_b16 a[136:137], %[vvb] offset:512\n" \
"v_mfma_f32_16x16x32_bf16 v[136:139], a[64:67], a[16:19], v[164:167]\n" \
"v_exp_f32_e64 v132, v132\n" \
"v_mfma_f32_16x16x32_bf16 v[136:139], a[68:71], a[20:23], v[136:139]\n" \
"ds_read_b64_tr_b16 a[138:139], %[vvb] offset:4608\n" \
"ds_read_b64_tr_b16 a[140:141], v211 offset:512\n" \
"v_mfma_f32_16x16x32_bf16 v[136:139], a[72:75], a[24:27], v[136:139]\n" \
"v_exp_f32_e64 v133, v133\n" \
"v_mfma_f32_16x16x32_bf16 v[136:139], a[76:79], a[28:31], v[136:139]\n" \
"v_cvt_pk_bf16_f32 v184, v128, v129\n" \
"v_cvt_pk_bf16_f32 v185, v130, v131\n" \
"v_mfma_f32_16x16x32_bf16 v[140:143], a[80:83], a[16:19], v[164:167]\n" \
"v_exp_f32_e64 v134, v134\n" \
"v_mfma_f32_16x16x32_bf16 v[140:143], a[84:87], a[20:23], v[140:143]\n" \
"ds_read_b64_tr_b16 a[142:143], v211 offset:4608\n" \
"s_waitcnt lgkmcnt(8)\n" \
"v_mfma_f32_16x16x32_bf16 v[140:143], a[88:91], a[24:27], v[140:143]\n" \
"buffer_load_dwordx4 v212, %[srdk], s48 offen offset:1024 lds\n" \
"v_mfma_f32_16x16x32_bf16 v[140:143], a[92:95], a[28:31], v[140:143]\n" \
"v_exp_f32_e32 v135, v135\n" \
"v_mfma_f32_16x16x32_bf16 v[80:83], a[144:147], v[216:219], v[80:83]\n" \
"ds_read_b64_tr_b16 a[144:145], %[vvb] offset:1024\n" \
"ds_read_b64_tr_b16 a[146:147], %[vvb] offset:5120\n" \
"v_mfma_f32_16x16x32_bf16 v[80:83], a[176:179], v[200:203], v[80:83]\n" \
"v_exp_f32_e64 v136, v136\n" \
"v_mfma_f32_16x16x32_bf16 v[84:87], a[148:151], v[216:219], v[84:87]\n" \
"v_cvt_pk_bf16_f32 v186, v132, v133\n" \
"v_cvt_pk_bf16_f32 v187, v134, v135\n" \
"v_mfma_f32_16x16x32_bf16 v[84:87], a[180:183], v[200:203], v[84:87]\n" \
"v_exp_f32_e64 v137, v137\n" \
"v_mfma_f32_16x16x32_bf16 v[88:91], a[152:155], v[216:219], v[88:91]\n" \
"v_exp_f32_e64 v138, v138\n" \
"v_mfma_f32_16x16x32_bf16 v[88:91], a[184:187], v[200:203], v[88:91]\n" \
"ds_read_b64_tr_b16 a[148:149], v211 offset:1024\n" \
"ds_read_b64_tr_b16 a[150:151], v211 offset:5120\n" \
"v_mfma_f32_16x16x32_bf16 v[92:95], a[156:159], v[216:219], v[92:95]\n" \
"v_exp_f32_e64 v139, v139\n" \
"v_mfma_f32_16x16x32_bf16 v[92:95], a[188:191], v[200:203], v[92:95]\n" \
"ds_read_b64_tr_b16 a[152:153], %[vvb] offset:1536\n" \
"buffer_store_dwordx4 a[196:199], v235, %[srdz], s62 offen offset:1024 sc1\n" \
"s_add_u32 s62, s62, 2048\n" \
"v_mfma_f32_16x16x32_bf16 v[144:147], a[64:67], a[32:35], v[168:171]\n" \
"v_exp_f32_e64 v140, v140\n" \
"v_mfma_f32_16x16x32_bf16 v[144:147], a[68:71], a[36:39], v[144:147]\n" \
"ds_read_b64_tr_b16 a[154:155], %[vvb] offset:5632\n" \
"ds_read_b64_tr_b16 a[156:157], v211 offset:1536\n" \
"v_mfma_f32_16x16x32_bf16 v[144:147], a[72:75], a[40:43], v[144:147]\n" \
"v_exp_f32_e64 v141, v141\n" \
"v_mfma_f32_16x16x32_bf16 v[144:147], a[76:79], a[44:47], v[144:147]\n" \
"v_cvt_pk_bf16_f32 v188, v136, v137\n" \
"v_cvt_pk_bf16_f32 v189, v138, v139\n" \
"v_mfma_f32_16x16x32_bf16 v[148:151], a[80:83], a[32:35], v[168:171]\n" \
"v_exp_f32_e64 v142, v142\n" \
"v_mfma_f32_16x16x32_bf16 v[148:151], a[84:87], a[36:39], v[148:151]\n" \
"ds_read_b64_tr_b16 a[158:159], v211 offset:5632\n" \
"s_waitcnt lgkmcnt(8)\n" \
"v_mfma_f32_16x16x32_bf16 v[148:151], a[88:91], a[40:43], v[148:151]\n" \
"buffer_load_dwordx4 v213, %[srdk], s48 offen offset:2048 lds\n" \
"v_mfma_f32_16x16x32_bf16 v[148:151], a[92:95], a[44:47], v[148:151]\n" \
"v_exp_f32_e32 v143, v143\n" \
"v_mfma_f32_16x16x32_bf16 v[0:3], a[160:163], v[192:195], v[0:3]\n" \
"ds_read_b128 a[96:99], %[kb0] offset:8192\n" \
"v_mfma_f32_16x16x32_bf16 v[0:3], a[128:131], v[184:187], v[0:3]\n" \
"v_exp_f32_e64 v144, v144\n" \
"v_mfma_f32_16x16x32_bf16 v[4:7], a[164:167], v[192:195], v[4:7]\n" \
"v_cvt_pk_bf16_f32 v190, v140, v141\n" \
"v_cvt_pk_bf16_f32 v191, v142, v143\n" \
"v_mfma_f32_16x16x32_bf16 v[4:7], a[132:135], v[184:187], v[4:7]\n" \
"v_exp_f32_e64 v145, v145\n" \
"v_mfma_f32_16x16x32_bf16 v[8:11], a[168:171], v[192:195], v[8:11]\n" \
"ds_read_b128 a[100:103], v208 offset:8192\n" \
"v_mfma_f32_16x16x32_bf16 v[8:11], a[136:139], v[184:187], v[8:11]\n" \
"v_exp_f32_e64 v146, v146\n" \
"v_mfma_f32_16x16x32_bf16 v[12:15], a[172:175], v[192:195], v[12:15]\n" \
"ds_read_b128 a[104:107], v209 offset:8192\n" \
"v_mfma_f32_16x16x32_bf16 v[12:15], a[140:143], v[184:187], v[12:15]\n" \
"v_exp_f32_e64 v147, v147\n" \
"v_smfmac_f32_16x16x64_bf16 v[180:183], a[192:195], v[200:207], v228\n" \
"ds_read_b128 a[108:111], v210 offset:8192\n" \
"v_mfma_f32_16x16x32_bf16 v[152:155], a[64:67], a[48:51], v[172:175]\n" \
"v_exp_f32_e64 v148, v148\n" \
"v_mfma_f32_16x16x32_bf16 v[152:155], a[68:71], a[52:55], v[152:155]\n" \
"ds_read_b128 a[112:115], %[kb0] offset:12288\n" \
"v_mfma_f32_16x16x32_bf16 v[152:155], a[72:75], a[56:59], v[152:155]\n" \
"v_exp_f32_e64 v149, v149\n" \
"v_mfma_f32_16x16x32_bf16 v[152:155], a[76:79], a[60:63], v[152:155]\n" \
"v_cvt_pk_bf16_f32 v216, v144, v145\n" \
"v_cvt_pk_bf16_f32 v217, v146, v147\n" \
"v_mfma_f32_16x16x32_bf16 v[156:159], a[80:83], a[48:51], v[172:175]\n" \
"v_exp_f32_e64 v150, v150\n" \
"v_mfma_f32_16x16x32_bf16 v[156:159], a[84:87], a[52:55], v[156:159]\n" \
"ds_read_b128 a[116:119], v208 offset:12288\n" \
"s_waitcnt lgkmcnt(6)\n" \
"v_mfma_f32_16x16x32_bf16 v[156:159], a[88:91], a[56:59], v[156:159]\n" \
"buffer_load_dwordx4 v214, %[srdk], s48 offen offset:3072 lds\n" \
"v_mfma_f32_16x16x32_bf16 v[156:159], a[92:95], a[60:63], v[156:159]\n" \
"v_exp_f32_e32 v151, v151\n" \
"v_mfma_f32_16x16x32_bf16 v[16:19], a[176:179], v[192:195], v[16:19]\n" \
"ds_read_b128 a[120:123], v209 offset:12288\n" \
"v_mfma_f32_16x16x32_bf16 v[16:19], a[144:147], v[184:187], v[16:19]\n" \
"v_exp_f32_e64 v152, v152\n" \
"v_mfma_f32_16x16x32_bf16 v[20:23], a[180:183], v[192:195], v[20:23]\n" \
"v_cvt_pk_bf16_f32 v218, v148, v149\n" \
"v_cvt_pk_bf16_f32 v219, v150, v151\n" \
"v_mfma_f32_16x16x32_bf16 v[20:23], a[148:151], v[184:187], v[20:23]\n" \
"v_exp_f32_e64 v153, v153\n" \
"v_mfma_f32_16x16x32_bf16 v[24:27], a[184:187], v[192:195], v[24:27]\n" \
"v_exp_f32_e64 v154, v154\n" \
"v_mfma_f32_16x16x32_bf16 v[24:27], a[152:155], v[184:187], v[24:27]\n" \
"ds_read_b128 a[124:127], v210 offset:12288\n" \
"s_add_u32 s48, s48, 0x4000\n" \
"v_mfma_f32_16x16x32_bf16 v[28:31], a[188:191], v[192:195], v[28:31]\n" \
"v_exp_f32_e64 v155, v155\n" \
"v_mfma_f32_16x16x32_bf16 v[28:31], a[156:159], v[184:187], v[28:31]\n" \
"s_waitcnt vmcnt(15) lgkmcnt(2)\n" \
"s_barrier\n" \
"v_mfma_f32_16x16x32_bf16 v[128:131], a[96:99], a[0:3], v[160:163]\n" \
"v_exp_f32_e64 v156, v156\n" \
"v_mfma_f32_16x16x32_bf16 v[128:131], a[100:103], a[4:7], v[128:131]\n" \
"s_add_u32 m0, %[m0b], 114688\n" \
"v_mfma_f32_16x16x32_bf16 v[128:131], a[104:107], a[8:11], v[128:131]\n" \
"v_exp_f32_e64 v157, v157\n" \
"v_mfma_f32_16x16x32_bf16 v[128:131], a[108:111], a[12:15], v[128:131]\n" \
"v_cvt_pk_bf16_f32 v220, v152, v153\n" \
"v_cvt_pk_bf16_f32 v221, v154, v155\n" \
"v_mfma_f32_16x16x32_bf16 v[132:135], a[112:115], a[0:3], v[160:163]\n" \
"v_exp_f32_e64 v158, v158\n" \
"v_mfma_f32_16x16x32_bf16 v[132:135], a[116:119], a[4:7], v[132:135]\n" \
"s_waitcnt lgkmcnt(0)\n" \
"v_mfma_f32_16x16x32_bf16 v[132:135], a[120:123], a[8:11], v[132:135]\n" \
"buffer_load_dwordx4 %[vd0], %[srdv], s48 offen offset:0 lds\n" \
"v_mfma_f32_16x16x32_bf16 v[132:135], a[124:127], a[12:15], v[132:135]\n" \
"v_exp_f32_e32 v159, v159\n" \
"v_mfma_f32_16x16x32_bf16 v[32:35], a[160:163], v[196:199], v[32:35]\n" \
"ds_read_b128 a[64:67], %[kb0] offset:16384\n" \
"v_mfma_f32_16x16x32_bf16 v[32:35], a[128:131], v[188:191], v[32:35]\n" \
"v_exp_f32_e64 v128, v128\n" \
"v_mfma_f32_16x16x32_bf16 v[36:39], a[164:167], v[196:199], v[36:39]\n" \
"v_cvt_pk_bf16_f32 v222, v156, v157\n" \
"v_cvt_pk_bf16_f32 v223, v158, v159\n" \
"v_mfma_f32_16x16x32_bf16 v[36:39], a[132:135], v[188:191], v[36:39]\n" \
"v_exp_f32_e64 v129, v129\n" \
"v_mfma_f32_16x16x32_bf16 v[40:43], a[168:171], v[196:199], v[40:43]\n" \
"ds_read_b128 a[68:71], v208 offset:16384\n" \
"v_mfma_f32_16x16x32_bf16 v[40:43], a[136:139], v[188:191], v[40:43]\n" \
"v_exp_f32_e64 v130, v130\n" \
"v_mfma_f32_16x16x32_bf16 v[44:47], a[172:175], v[196:199], v[44:47]\n" \
"ds_read_b128 a[72:75], v209 offset:16384\n" \
"v_mfma_f32_16x16x32_bf16 v[44:47], a[140:143], v[188:191], v[44:47]\n" \
"v_exp_f32_e64 v131, v131\n" \
"v_smfmac_f32_16x16x64_bf16 v[176:179], a[192:195], v[184:191], v228\n" \
"v_mfma_f32_16x16x32_bf16 v[136:139], a[96:99], a[16:19], v[164:167]\n" \
"v_exp_f32_e64 v132, v132\n" \
"v_mfma_f32_16x16x32_bf16 v[136:139], a[100:103], a[20:23], v[136:139]\n" \
"ds_read_b128 a[76:79], v210 offset:16384\n" \
"v_mfma_f32_16x16x32_bf16 v[136:139], a[104:107], a[24:27], v[136:139]\n" \
"v_exp_f32_e64 v133, v133\n" \
"v_mfma_f32_16x16x32_bf16 v[136:139], a[108:111], a[28:31], v[136:139]\n" \
"v_cvt_pk_bf16_f32 v192, v128, v129\n" \
"v_cvt_pk_bf16_f32 v193, v130, v131\n" \
"v_mfma_f32_16x16x32_bf16 v[140:143], a[112:115], a[16:19], v[164:167]\n" \
"v_exp_f32_e64 v134, v134\n" \
"v_mfma_f32_16x16x32_bf16 v[140:143], a[116:119], a[20:23], v[140:143]\n" \
"ds_read_b128 a[80:83], %[kb0] offset:20480\n" \
"v_mfma_f32_16x16x32_bf16 v[140:143], a[120:123], a[24:27], v[140:143]\n" \
"buffer_load_dwordx4 v215, %[srdv], s48 offen offset:1024 lds\n" \
"v_mfma_f32_16x16x32_bf16 v[140:143], a[124:127], a[28:31], v[140:143]\n" \
".if . - adaln_body != 20480\n" \
".error \"size model\"\n" \
".endif\n" \
"Lk4_%=:\n" \
"s_cbranch_execz Lk5_%=\n" \
"s_nop 0\n" \
"v_exp_f32_e64 v135, v135\n" \
"v_mfma_f32_16x16x32_bf16 v[48:51], a[176:179], v[196:199], v[48:51]\n" \
"ds_read_b128 a[84:87], v208 offset:20480\n" \
"v_mfma_f32_16x16x32_bf16 v[48:51], a[144:147], v[188:191], v[48:51]\n" \
"v_exp_f32_e64 v136, v136\n" \
"v_mfma_f32_16x16x32_bf16 v[52:55], a[180:183], v[196:199], v[52:55]\n" \
"v_cvt_pk_bf16_f32 v194, v132, v133\n" \
"v_cvt_pk_bf16_f32 v195, v134, v135\n" \
"v_mfma_f32_16x16x32_bf16 v[52:55], a[148:151], v[188:191], v[52:55]\n" \
"v_exp_f32_e64 v137, v137\n" \
"v_mfma_f32_16x16x32_bf16 v[56:59], a[184:187], v[196:199], v[56:59]\n" \
"v_exp_f32_e64 v138, v138\n" \
"v_mfma_f32_16x16x32_bf16 v[56:59], a[152:155], v[188:191], v[56:59]\n" \
"ds_read_b128 a[88:91], v209 offset:20480\n" \
"v_mfma_f32_16x16x32_bf16 v[60:63], a[188:191], v[196:199], v[60:63]\n" \
"v_exp_f32_e64 v139, v139\n" \
"v_mfma_f32_16x16x32_bf16 v[60:63], a[156:159], v[188:191], v[60:63]\n" \
"s_cmp_lg_u32 %[wid], 3\n" \
"s_cbranch_scc1 LtA1_%=\n" \
"global_load_dword v237, v224, s[58:59]\n" \
"s_add_u32 s61, s48, 49152\n" \
"s_mov_b32 exec_hi, 0\n" \
"s_nop 0\n" \
"buffer_load_dword v237, v224, %[srdp], s61 offen\n" \
"s_mov_b32 exec_hi, -1\n" \
"s_nop 0\n" \
"LtA1_%=:\n" \
"v_mfma_f32_16x16x32_bf16 v[144:147], a[96:99], a[32:35], v[168:171]\n" \
"v_exp_f32_e64 v140, v140\n" \
"v_mfma_f32_16x16x32_bf16 v[144:147], a[100:103], a[36:39], v[144:147]\n" \
"ds_read_b128 a[92:95], v210 offset:20480\n" \
"v_mfma_f32_16x16x32_bf16 v[144:147], a[104:107], a[40:43], v[144:147]\n" \
"v_exp_f32_e64 v141, v141\n" \
"v_mfma_f32_16x16x32_bf16 v[144:147], a[108:111], a[44:47], v[144:147]\n" \
"v_cvt_pk_bf16_f32 v196, v136, v137\n" \
"v_cvt_pk_bf16_f32 v197, v138, v139\n" \
"v_mfma_f32_16x16x32_bf16 v[148:151], a[112:115], a[32:35], v[168:171]\n" \
"v_exp_f32_e64 v142, v142\n" \
"v_mfma_f32_16x16x32_bf16 v[148:151], a[116:119], a[36:39], v[148:151]\n" \
"v_mfma_f32_16x16x32_bf16 v[148:151], a[120:123], a[40:43], v[148:151]\n" \
"buffer_load_dwordx4 %[vd0], %[srdv], s48 offen offset:2048 lds\n" \
"v_mfma_f32_16x16x32_bf16 v[148:151], a[124:127], a[44:47], v[148:151]\n" \
"v_exp_f32_e64 v143, v143\n" \
"v_mfma_f32_16x16x32_bf16 v[96:99], a[160:163], v[204:207], v[96:99]\n" \
"ds_read_b64_tr_b16 a[160:161], %[vvb] offset:8192\n" \
"v_mfma_f32_16x16x32_bf16 v[96:99], a[128:131], v[220:223], v[96:99]\n" \
"v_exp_f32_e64 v144, v144\n" \
"v_mfma_f32_16x16x32_bf16 v[100:103], a[164:167], v[204:207], v[100:103]\n" \
"v_cvt_pk_bf16_f32 v198, v140, v141\n" \
"v_cvt_pk_bf16_f32 v199, v142, v143\n" \
"v_mfma_f32_16x16x32_bf16 v[100:103], a[132:135], v[220:223], v[100:103]\n" \
"v_exp_f32_e64 v145, v145\n" \
"v_mfma_f32_16x16x32_bf16 v[104:107], a[168:171], v[204:207], v[104:107]\n" \
"ds_read_b64_tr_b16 a[162:163], %[vvb] offset:12288\n" \
"ds_read_b64_tr_b16 a[164:165], v211 offset:8192\n" \
"v_mfma_f32_16x16x32_bf16 v[104:107], a[136:139], v[220:223], v[104:107]\n" \
"v_exp_f32_e64 v146, v146\n" \
"v_mfma_f32_16x16x32_bf16 v[108:111], a[172:175], v[204:207], v[108:111]\n" \
"ds_read_b64_tr_b16 a[166:167], v211 offset:12288\n" \
"v_mfma_f32_16x16x32_bf16 v[108:111], a[140:143], v[220:223], v[108:111]\n" \
"v_exp_f32_e64 v147, v147\n" \
"v_smfmac_f32_16x16x64_bf16 v[180:183], a[192:195], v[216:223], v228\n" \
"ds_read_b64_tr_b16 a[168:169], %[vvb] offset:8704\n" \
"v_mfma_f32_16x16x32_bf16 v[152:155], a[96:99], a[48:51], v[172:175]\n" \
"v_exp_f32_e64 v148, v148\n" \
"v_mfma_f32_16x16x32_bf16 v[152:155], a[100:103], a[52:55], v[152:155]\n" \
"ds_read_b64_tr_b16 a[170:171], %[vvb] offset:12800\n" \
"v_mfma_f32_16x16x32_bf16 v[152:155], a[104:107], a[56:59], v[152:155]\n" \
"v_exp_f32_e64 v149, v149\n" \
"v_mfma_f32_16x16x32_bf16 v[152:155], a[108:111], a[60:63], v[152:155]\n" \
"v_cvt_pk_bf16_f32 v200, v144, v145\n" \
"v_cvt_pk_bf16_f32 v201, v146, v147\n" \
"v_mfma_f32_16x16x32_bf16 v[156:159], a[112:115], a[48:51], v[172:175]\n" \
"v_exp_f32_e64 v150, v150\n" \
"v_mfma_f32_16x16x32_bf16 v[156:159], a[116:119], a[52:55], v[156:159]\n" \
"ds_read_b64_tr_b16 a[172:173], v211 offset:8704\n" \
"v_mfma_f32_16x16x32_bf16 v[156:159], a[120:123], a[56:59], v[156:159]\n" \
"buffer_load_dwordx4 v215, %[srdv], s48 offen offset:3072 lds\n" \
"v_mfma_f32_16x16x32_bf16 v[156:159], a[124:127], a[60:63], v[156:159]\n" \
"v_exp_f32_e64 v151, v151\n" \
"v_mfma_f32_16x16x32_bf16 v[112:115], a[176:179], v[204:207], v[112:115]\n" \
"ds_read_b64_tr_b16 a[174:175], v211 offset:12800\n" \
"ds_read_b64_tr_b16 a[176:177], %[vvb] offset:9216\n" \
"v_mfma_f32_16x16x32_bf16 v[112:115], a[144:147], v[220:223], v[112:115]\n" \
"v_exp_f32_e64 v152, v152\n" \
"v_mfma_f32_16x16x32_bf16 v[116:119], a[180:183], v[204:207], v[116:119]\n" \
"v_cvt_pk_bf16_f32 v202, v148, v149\n" \
"v_cvt_pk_bf16_f32 v203, v150, v151\n" \
"v_mfma_f32_16x16x32_bf16 v[116:119], a[148:151], v[220:223], v[116:119]\n" \
"v_exp_f32_e64 v153, v153\n" \
"v_mfma_f32_16x16x32_bf16 v[120:123], a[184:187], v[204:207], v[120:123]\n" \
"v_exp_f32_e64 v154, v154\n" \
"v_mfma_f32_16x16x32_bf16 v[120:123], a[152:155], v[220:223], v[120:123]\n" \
"ds_read_b64_tr_b16 a[178:179], %[vvb] offset:13312\n" \
"v_mfma_f32_16x16x32_bf16 v[124:127], a[188:191], v[204:207], v[124:127]\n" \
"v_exp_f32_e64 v155, v155\n" \
"v_mfma_f32_16x16x32_bf16 v[124:127], a[156:159], v[220:223], v[124:127]\n" \
"ds_read_b64_tr_b16 a[180:181], v211 offset:9216\n" \
"s_waitcnt lgkmcnt(13)\n" \
"s_cmp_lg_u32 s60, 4\n" \
"s_cbranch_scc1 LmA_%=\n" \
"s_nop 0\n" \
"v_readlane_b32 s64, %[tlo], 9\n" \
"v_readlane_b32 s65, %[thi], 9\n" \
"s_mov_b32 s66, %[wbytes]\n" \
"v_mov_b32_e32 v229, %[vg0]\n" \
"s_mov_b32 s44, %[m0t]\n" \
"s_mov_b32 s45, 0\n" \
"s_cmp_lg_u32 s52, 0\n" \
"s_cbranch_scc1 LmB_%=\n" \
"s_add_u32 s62, %[sin], 8192\n" \
"s_add_u32 s63, %[sin], 28672\n" \
"s_waitcnt vmcnt(13)\n" \
"s_nop 0\n" \
"ds_read_b64_tr_b16 a[232:233], %[ta0] offset:0\n" \
"ds_read_b64_tr_b16 a[234:235], %[ta0] offset:128\n" \
"ds_read_b64_tr_b16 a[236:237], %[ta0] offset:1024\n" \
"ds_read_b64_tr_b16 a[238:239], %[ta0] offset:1152\n" \
"ds_read_b64_tr_b16 a[240:241], %[ta0] offset:2048\n" \
"ds_read_b64_tr_b16 a[242:243], %[ta0] offset:2176\n" \
"ds_read_b64_tr_b16 a[244:245], %[ta0] offset:3072\n" \
"ds_read_b64_tr_b16 a[246:247], %[ta0] offset:3200\n" \
"ds_read_b64_tr_b16 a[248:249], %[ta0] offset:4096\n" \
"ds_read_b64_tr_b16 a[250:251], %[ta0] offset:4224\n" \
"ds_read_b64_tr_b16 a[252:253], %[ta0] offset:5120\n" \
"ds_read_b64_tr_b16 a[254:255], %[ta0] offset:5248\n" \
"v_add_u32_e32 v234, %[m0t], v235\n" \
"s_waitcnt lgkmcnt(8)\n" \
"v_mfma_f32_16x16x32_bf16 v[230:233], a[232:235], a[200:203], 0\n" \
"v_mfma_f32_16x16x32_bf16 v[230:233], a[236:239], a[204:207], v[230:233]\n" \
"ds_read_b64_tr_b16 a[232:233], %[ta0] offset:6144\n" \
"ds_read_b64_tr_b16 a[234:235], %[ta0] offset:6272\n" \
"ds_read_b64_tr_b16 a[236:237], %[ta0] offset:7168\n" \
"ds_read_b64_tr_b16 a[238:239], %[ta0] offset:7296\n" \
"s_waitcnt lgkmcnt(4)\n" \
"s_nop 0\n" \
"v_mfma_f32_16x16x32_bf16 v[230:233], a[240:243], a[208:211], v[230:233]\n" \
"v_mfma_f32_16x16x32_bf16 v[230:233], a[244:247], a[212:215], v[230:233]\n" \
"v_mfma_f32_16x16x32_bf16 v[230:233], a[248:251], a[216:219], v[230:233]\n" \
"v_mfma_f32_16x16x32_bf16 v[230:233], a[252:255], a[220:223], v[230:233]\n" \
"s_waitcnt lgkmcnt(0)\n" \
"s_nop 0\n" \
"v_mfma_f32_16x16x32_bf16 v[230:233], a[232:235], a[224:227], v[230:233]\n" \
"v_mfma_f32_16x16x32_bf16 v[230:233], a[236:239], a[228:231], v[230:233]\n" \
"s_nop 7\n" \
"s_nop 0\n" \
"ds_write_b128 v234, v[230:233] offset:7168\n" \
"LmB_%=:\n" \
"v_mfma_f32_16x16x32_bf16 v[128:131], a[64:67], a[0:3], v[160:163]\n" \
"v_exp_f32_e64 v156, v156\n" \
"v_mfma_f32_16x16x32_bf16 v[128:131], a[68:71], a[4:7], v[128:131]\n" \
"ds_read_b64_tr_b16 a[182:183], v211 offset:13312\n" \
"s_add_u32 m0, %[m0b], 0\n" \
"v_mfma_f32_16x16x32_bf16 v[128:131], a[72:75], a[8:11], v[128:131]\n" \
"v_exp_f32_e32 v157, v157\n" \
"v_mfma_f32_16x16x32_bf16 v[128:131], a[76:79], a[12:15], v[128:131]\n" \
"v_cvt_pk_bf16_f32 v204, v152, v153\n" \
"v_cvt_pk_bf16_f32 v205, v154, v155\n" \
"v_mfma_f32_16x16x32_bf16 v[132:135], a[80:83], a[0:3], v[160:163]\n" \
"v_exp_f32_e64 v158, v158\n" \
"v_mfma_f32_16x16x32_bf16 v[132:135], a[84:87], a[4:7], v[132:135]\n" \
"ds_read_b64_tr_b16 a[184:185], %[vvb] offset:9728\n" \
"s_waitcnt lgkmcnt(5)\n" \
"v_mfma_f32_16x16x32_bf16 v[132:135], a[88:91], a[8:11], v[132:135]\n" \
"buffer_load_dwordx4 %[kd0], %[srdk], s48 offen offset:0 lds\n" \
"v_mfma_f32_16x16x32_bf16 v[132:135], a[92:95], a[12:15], v[132:135]\n" \
"v_exp_f32_e32 v159, v159\n" \
"v_mfma_f32_16x16x32_bf16 v[64:67], a[128:131], v[216:219], v[64:67]\n" \
"ds_read_b64_tr_b16 a[186:187], %[vvb] offset:13824\n" \
"ds_read_b64_tr_b16 a[188:189], v211 offset:9728\n" \
"v_mfma_f32_16x16x32_bf16 v[64:67], a[160:163], v[200:203], v[64:67]\n" \
"v_exp_f32_e64 v128, v128\n" \
"v_mfma_f32_16x16x32_bf16 v[68:71], a[132:135], v[216:219], v[68:71]\n" \
"v_cvt_pk_bf16_f32 v206, v156, v157\n" \
"v_cvt_pk_bf16_f32 v207, v158, v159\n" \
"v_mfma_f32_16x16x32_bf16 v[68:71], a[164:167], v[200:203], v[68:71]\n" \
"v_exp_f32_e64 v129, v129\n" \
"v_mfma_f32_16x16x32_bf16 v[72:75], a[136:139], v[216:219], v[72:75]\n" \
"ds_read_b64_tr_b16 a[190:191], v211 offset:13824\n" \
"ds_read_b64_tr_b16 a[128:129], %[vvb] offset:16384\n" \
"s_add_u32 s60, s60, 1\n" \
"s_nop 0\n" \
"v_readlane_b32 s58, %[tlo], s60\n" \
"v_mfma_f32_16x16x32_bf16 v[72:75], a[168:171], v[200:203], v[72:75]\n" \
"v_exp_f32_e64 v130, v130\n" \
"v_mfma_f32_16x16x32_bf16 v[76:79], a[140:143], v[216:219], v[76:79]\n" \
"ds_read_b64_tr_b16 a[130:131], %[vvb] offset:20480\n" \
"ds_read_b64_tr_b16 a[132:133], v211 offset:16384\n" \
"v_readlane_b32 s59, %[thi], s60\n" \
"v_mfma_f32_16x16x32_bf16 v[76:79], a[172:175], v[200:203], v[76:79]\n" \
"v_exp_f32_e64 v131, v131\n" \
"v_smfmac_f32_16x16x64_bf16 v[176:179], a[192:195], v[192:199], v228\n" \
"ds_read_b64_tr_b16 a[134:135], v211 offset:20480\n" \
"ds_read_b64_tr_b16 a[136:137], %[vvb] offset:16896\n" \
"s_cmp_eq_u32 s60, 7\n" \
"s_cselect_b32 s61, 56, 1\n" \
"v_mul_u32_u24_e64 v236, s61, v224\n" \
"v_mfma_f32_16x16x32_bf16 v[136:139], a[64:67], a[16:19], v[164:167]\n" \
"v_exp_f32_e64 v132, v132\n" \
"v_mfma_f32_16x16x32_bf16 v[136:139], a[68:71], a[20:23], v[136:139]\n" \
"ds_read_b64_tr_b16 a[138:139], %[vvb] offset:20992\n" \
"ds_read_b64_tr_b16 a[140:141], v211 offset:16896\n" \
"v_mfma_f32_16x16x32_bf16 v[136:139], a[72:75], a[24:27], v[136:139]\n" \
"v_exp_f32_e64 v133, v133\n" \
"v_mfma_f32_16x16x32_bf16 v[136:139], a[76:79], a[28:31], v[136:139]\n" \
"v_cvt_pk_bf16_f32 v184, v128, v129\n" \
"v_cvt_pk_bf16_f32 v185, v130, v131\n" \
"v_mfma_f32_16x16x32_bf16 v[140:143], a[80:83], a[16:19], v[164:167]\n" \
"v_exp_f32_e64 v134, v134\n" \
"v_mfma_f32_16x16x32_bf16 v[140:143], a[84:87], a[20:23], v[140:143]\n" \
"ds_read_b64_tr_b16 a[142:143], v211 offset:20992\n" \
"s_waitcnt lgkmcnt(8)\n" \
"v_mfma_f32_16x16x32_bf16 v[140:143], a[88:91], a[24:27], v[140:143]\n" \
"buffer_load_dwordx4 v212, %[srdk], s48 offen offset:1024 lds\n" \
"v_mfma_f32_16x16x32_bf16 v[140:143], a[92:95], a[28:31], v[140:143]\n" \
"v_exp_f32_e32 v135, v135\n" \
"v_mfma_f32_16x16x32_bf16 v[80:83], a[144:147], v[216:219], v[80:83]\n" \
"ds_read_b64_tr_b16 a[144:145], %[vvb] offset:17408\n" \
"ds_read_b64_tr_b16 a[146:147], %[vvb] offset:21504\n" \
"v_mfma_f32_16x16x32_bf16 v[80:83], a[176:179], v[200:203], v[80:83]\n" \
"v_exp_f32_e64 v136, v136\n" \
"v_mfma_f32_16x16x32_bf16 v[84:87], a[148:151], v[216:219], v[84:87]\n" \
"v_cvt_pk_bf16_f32 v186, v132, v133\n" \
"v_cvt_pk_bf16_f32 v187, v134, v135\n" \
"v_mfma_f32_16x16x32_bf16 v[84:87], a[180:183], v[200:203], v[84:87]\n" \
"v_exp_f32_e64 v137, v137\n" \
"v_mfma_f32_16x16x32_bf16 v[88:91], a[152:155], v[216:219], v[88:91]\n" \
"v_exp_f32_e64 v138, v138\n" \
"v_mfma_f32_16x16x32_bf16 v[88:91], a[184:187], v[200:203], v[88:91]\n" \
"ds_read_b64_tr_b16 a[148:149], v211 offset:17408\n" \
"ds_read_b64_tr_b16 a[150:151], v211 offset:21504\n" \
"v_mfma_f32_16x16x32_bf16 v[92:95], a[156:159], v[216:219], v[92:95]\n" \
"v_exp_f32_e64 v139, v139\n" \
"v_mfma_f32_16x16x32_bf16 v[92:95], a[188:191], v[200:203], v[92:95]\n" \
"ds_read_b64_tr_b16 a[152:153], %[vvb] offset:17920\n" \
"buffer_store_dwordx4 a[196:199], v235, %[srdz], s63 offen offset:0 sc1\n" \
"v_mfma_f32_16x16x32_bf16 v[144:147], a[64:67], a[32:35], v[168:171]\n" \
"v_exp_f32_e64 v140, v140\n" \
"v_mfma_f32_16x16x32_bf16 v[144:147], a[68:71], a[36:39], v[144:147]\n" \
"ds_read_b64_tr_b16 a[154:155], %[vvb] offset:22016\n" \
"ds_read_b64_tr_b16 a[156:157], v211 offset:17920\n" \
"v_mfma_f32_16x16x32_bf16 v[144:147], a[72:75], a[40:43], v[144:147]\n" \
"v_exp_f32_e64 v141, v141\n" \
"v_mfma_f32_16x16x32_bf16 v[144:147], a[76:79], a[44:47], v[144:147]\n" \
"v_cvt_pk_bf16_f32 v188, v136, v137\n" \
"v_cvt_pk_bf16_f32 v189, v138, v139\n" \
"v_mfma_f32_16x16x32_bf16 v[148:151], a[80:83], a[32:35], v[168:171]\n" \
"v_exp_f32_e64 v142, v142\n" \
"v_mfma_f32_16x16x32_bf16 v[148:151], a[84:87], a[36:39], v[148:151]\n" \
"ds_read_b64_tr_b16 a[158:159], v211 offset:22016\n" \
"s_waitcnt lgkmcnt(8)\n" \
"v_mfma_f32_16x16x32_bf16 v[148:151], a[88:91], a[40:43], v[148:151]\n" \
"buffer_load_dwordx4 v213, %[srdk], s48 offen offset:2048 lds\n" \
"v_mfma_f32_16x16x32_bf16 v[148:151], a[92:95], a[44:47], v[148:151]\n" \
"v_exp_f32_e32 v143, v143\n" \
"v_mfma_f32_16x16x32_bf16 v[0:3], a[160:163], v[192:195], v[0:3]\n" \
"ds_read_b128 a[96:99], %[kb0] offset:24576\n" \
"v_mfma_f32_16x16x32_bf16 v[0:3], a[128:131], v[184:187], v[0:3]\n" \
"v_exp_f32_e64 v144, v144\n" \
"v_mfma_f32_16x16x32_bf16 v[4:7], a[164:167], v[192:195], v[4:7]\n" \
"v_cvt_pk_bf16_f32 v190, v140, v141\n" \
"v_cvt_pk_bf16_f32 v191, v142, v143\n" \
"v_mfma_f32_16x16x32_bf16 v[4:7], a[132:135], v[184:187], v[4:7]\n" \
"v_exp_f32_e64 v145, v145\n" \
"v_mfma_f32_16x16x32_bf16 v[8:11], a[168:171], v[192:195], v[8:11]\n" \
"ds_read_b128 a[100:103], v208 offset:24576\n" \
"v_mfma_f32_16x16x32_bf16 v[8:11], a[136:139], v[184:187], v[8:11]\n" \
"v_exp_f32_e64 v146, v146\n" \
"v_mfma_f32_16x16x32_bf16 v[12:15], a[172:175], v[192:195], v[12:15]\n" \
"ds_read_b128 a[104:107], v209 offset:24576\n" \
"v_mfma_f32_16x16x32_bf16 v[12:15], a[140:143], v[184:187], v[12:15]\n" \
"v_exp_f32_e64 v147, v147\n" \
"v_smfmac_f32_16x16x64_bf16 v[180:183], a[192:195], v[200:207], v228\n" \
"ds_read_b128 a[108:111], v210 offset:24576\n" \
"v_mfma_f32_16x16x32_bf16 v[152:155], a[64:67], a[48:51], v[172:175]\n" \
"v_exp_f32_e64 v148, v148\n" \
"v_mfma_f32_16x16x32_bf16 v[152:155], a[68:71], a[52:55], v[152:155]\n" \
"ds_read_b128 a[112:115], %[kb0] offset:28672\n" \
"v_mfma_f32_16x16x32_bf16 v[152:155], a[72:75], a[56:59], v[152:155]\n" \
"v_exp_f32_e64 v149, v149\n" \
"v_mfma_f32_16x16x32_bf16 v[152:155], a[76:79], a[60:63], v[152:155]\n" \
"v_cvt_pk_bf16_f32 v216, v144, v145\n" \
"v_cvt_pk_bf16_f32 v217, v146, v147\n" \
"v_mfma_f32_16x16x32_bf16 v[156:159], a[80:83], a[48:51], v[172:175]\n" \
"v_exp_f32_e64 v150, v150\n" \
"v_mfma_f32_16x16x32_bf16 v[156:159], a[84:87], a[52:55], v[156:159]\n" \
"ds_read_b128 a[116:119], v208 offset:28672\n" \
"s_waitcnt lgkmcnt(6)\n" \
"v_mfma_f32_16x16x32_bf16 v[156:159], a[88:91], a[56:59], v[156:159]\n" \
"buffer_load_dwordx4 v214, %[srdk], s48 offen offset:3072 lds\n" \
"v_mfma_f32_16x16x32_bf16 v[156:159], a[92:95], a[60:63], v[156:159]\n" \
"v_exp_f32_e32 v151, v151\n" \
"v_mfma_f32_16x16x32_bf16 v[16:19], a[176:179], v[192:195], v[16:19]\n" \
"ds_read_b128 a[120:123], v209 offset:28672\n" \
"v_mfma_f32_16x16x32_bf16 v[16:19], a[144:147], v[184:187], v[16:19]\n" \
"v_exp_f32_e64 v152, v152\n" \
"v_mfma_f32_16x16x32_bf16 v[20:23], a[180:183], v[192:195], v[20:23]\n" \
"v_cvt_pk_bf16_f32 v218, v148, v149\n" \
"v_cvt_pk_bf16_f32 v219, v150, v151\n" \
"v_mfma_f32_16x16x32_bf16 v[20:23], a[148:151], v[184:187], v[20:23]\n" \
"v_exp_f32_e64 v153, v153\n" \
"v_mfma_f32_16x16x32_bf16 v[24:27], a[184:187], v[192:195], v[24:27]\n" \
"v_exp_f32_e64 v154, v154\n" \
"v_mfma_f32_16x16x32_bf16 v[24:27], a[152:155], v[184:187], v[24:27]\n" \
"ds_read_b128 a[124:127], v210 offset:28672\n" \
"s_add_u32 s48, s48, 0x4000\n" \
"s_mov_b32 m0, s44\n" \
"s_addk_i32 s44, 0x400\n" \
"buffer_load_dwordx4 v229, s[64:67], s45 offen offset:0 lds\n" \
"s_add_u32 s45, s45, 229376\n" \
"v_mfma_f32_16x16x32_bf16 v[28:31], a[188:191], v[192:195], v[28:31]\n" \
"v_exp_f32_e64 v155, v155\n" \
"v_mfma_f32_16x16x32_bf16 v[28:31], a[156:159], v[184:187], v[28:31]\n" \
"s_waitcnt vmcnt(15) lgkmcnt(2)\n" \
"s_barrier\n" \
"v_mfma_f32_16x16x32_bf16 v[128:131], a[96:99], a[0:3], v[160:163]\n" \
"v_exp_f32_e64 v156, v156\n" \
"v_mfma_f32_16x16x32_bf16 v[128:131], a[100:103], a[4:7], v[128:131]\n" \
"s_add_u32 m0, %[m0b], 65536\n" \
"v_mfma_f32_16x16x32_bf16 v[128:131], a[104:107], a[8:11], v[128:131]\n" \
"v_exp_f32_e64 v157, v157\n" \
"v_mfma_f32_16x16x32_bf16 v[128:131], a[108:111], a[12:15], v[128:131]\n" \
"v_cvt_pk_bf16_f32 v220, v152, v153\n" \
"v_cvt_pk_bf16_f32 v221, v154, v155\n" \
"v_mfma_f32_16x16x32_bf16 v[132:135], a[112:115], a[0:3], v[160:163]\n" \
"v_exp_f32_e64 v158, v158\n" \
"v_mfma_f32_16x16x32_bf16 v[132:135], a[116:119], a[4:7], v[132:135]\n" \
"s_cmp_lg_u32 s60, 5\n" \
"s_cbranch_scc1 Lsjb_%=\n" \
"ds_read_b32 v230, %[rd0]\n" \
"v_readlane_b32 s55, %[thi], 15\n" \
"v_lshrrev_b32_e32 v234, 5, v224\n" \
"s_waitcnt lgkmcnt(0)\n" \
"v_add_f32_dpp v230, v230, v230 row_ror:4 row_mask:0xf bank_mask:0xf\n" \
"s_nop 1\n" \
"v_add_f32_dpp v230, v230, v230 row_ror:8 row_mask:0xf bank_mask:0xf\n" \
"v_cvt_pk_bf16_f32 v230, v230, v230\n" \
"v_lshlrev_b32_e32 v230, 16, v230\n" \
"v_add_f32_e64 v230, s55, v230\n" \
"v_cvt_pk_bf16_f32 v230, v230, v230\n" \
"v_readlane_b32 s55, %[tlo], 15\n" \
"v_lshl_or_b32 v230, v230, 16, 1\n" \
"s_cmp_lg_u32 s52, 0\n" \
"s_cselect_b32 s55, 0x80000000, s55\n" \
"s_mov_b64 exec, 15\n" \
"buffer_store_dword v230, v234, %[srdz], s55 offen sc1\n" \
"s_mov_b64 exec, -1\n" \
"Lsjb_%=:\n" \
"v_mfma_f32_16x16x32_bf16 v[132:135], a[120:123], a[8:11], v[132:135]\n" \
"buffer_load_dwordx4 %[vd0], %[srdv], s48 offen offset:0 lds\n" \
"v_mfma_f32_16x16x32_bf16 v[132:135], a[124:127], a[12:15], v[132:135]\n" \
"v_exp_f32_e32 v159, v159\n" \
"v_mfma_f32_16x16x32_bf16 v[32:35], a[160:163], v[196:199], v[32:35]\n" \
"ds_read_b128 a[64:67], %[kb0] offset:32768\n" \
"v_mfma_f32_16x16x32_bf16 v[32:35], a[128:131], v[188:191], v[32:35]\n" \
"v_exp_f32_e64 v128, v128\n" \
"v_mfma_f32_16x16x32_bf16 v[36:39], a[164:167], v[196:199], v[36:39]\n" \
"v_cvt_pk_bf16_f32 v222, v156, v157\n" \
"v_cvt_pk_bf16_f32 v223, v158, v159\n" \
"v_mfma_f32_16x16x32_bf16 v[36:39], a[132:135], v[188:191], v[36:39]\n" \
"v_exp_f32_e64 v129, v129\n" \
"v_mfma_f32_16x16x32_bf16 v[40:43], a[168:171], v[196:199], v[40:43]\n" \
"ds_read_b128 a[68:71], v208 offset:32768\n" \
"v_mfma_f32_16x16x32_bf16 v[40:43], a[136:139], v[188:191], v[40:43]\n" \
"v_exp_f32_e64 v130, v130\n" \
"v_mfma_f32_16x16x32_bf16 v[44:47], a[172:175], v[196:199], v[44:47]\n" \
"ds_read_b128 a[72:75], v209 offset:32768\n" \
"v_mfma_f32_16x16x32_bf16 v[44:47], a[140:143], v[188:191], v[44:47]\n" \
"v_exp_f32_e64 v131, v131\n" \
"v_smfmac_f32_16x16x64_bf16 v[176:179], a[192:195], v[184:191], v228\n" \
"v_mfma_f32_16x16x32_bf16 v[136:139], a[96:99], a[16:19], v[164:167]\n" \
"v_exp_f32_e64 v132, v132\n" \
"v_mfma_f32_16x16x32_bf16 v[136:139], a[100:103], a[20:23], v[136:139]\n" \
"ds_read_b128 a[76:79], v210 offset:32768\n" \
"v_mfma_f32_16x16x32_bf16 v[136:139], a[104:107], a[24:27], v[136:139]\n" \
"v_exp_f32_e64 v133, v133\n" \
"v_mfma_f32_16x16x32_bf16 v[136:139], a[108:111], a[28:31], v[136:139]\n" \
"v_cvt_pk_bf16_f32 v192, v128, v129\n" \
"v_cvt_pk_bf16_f32 v193, v130, v131\n" \
"v_mfma_f32_16x16x32_bf16 v[140:143], a[112:115], a[16:19], v[164:167]\n" \
"v_exp_f32_e64 v134, v134\n" \
"v_mfma_f32_16x16x32_bf16 v[140:143], a[116:119], a[20:23], v[140:143]\n" \
"ds_read_b128 a[80:83], %[kb0] offset:36864\n" \
"v_mfma_f32_16x16x32_bf16 v[140:143], a[120:123], a[24:27], v[140:143]\n" \
"buffer_load_dwordx4 v215, %[srdv], s48 offen offset:1024 lds\n" \
"v_mfma_f32_16x16x32_bf16 v[140:143], a[124:127], a[28:31], v[140:143]\n" \
"v_exp_f32_e64 v135, v135\n" \
"v_mfma_f32_16x16x32_bf16 v[48:51], a[176:179], v[196:199], v[48:51]\n" \
"ds_read_b128 a[84:87], v208 offset:36864\n" \
"v_mfma_f32_16x16x32_bf16 v[48:51], a[144:147], v[188:191], v[48:51]\n" \
"v_exp_f32_e64 v136, v136\n" \
"v_mfma_f32_16x16x32_bf16 v[52:55], a[180:183], v[196:199], v[52:55]\n" \
"v_cvt_pk_bf16_f32 v194, v132, v133\n" \
"v_cvt_pk_bf16_f32 v195, v134, v135\n" \
"v_mfma_f32_16x16x32_bf16 v[52:55], a[148:151], v[188:191], v[52:55]\n" \
"v_exp_f32_e64 v137, v137\n" \
"v_mfma_f32_16x16x32_bf16 v[56:59], a[184:187], v[196:199], v[56:59]\n" \
"v_exp_f32_e64 v138, v138\n" \
"v_mfma_f32_16x16x32_bf16 v[56:59], a[152:155], v[188:191], v[56:59]\n" \
"ds_read_b128 a[88:91], v209 offset:36864\n" \
"v_mfma_f32_16x16x32_bf16 v[60:63], a[188:191], v[196:199], v[60:63]\n" \
"v_exp_f32_e64 v139, v139\n" \
"v_mfma_f32_16x16x32_bf16 v[60:63], a[156:159], v[188:191], v[60:63]\n" \
"s_cmp_lg_u32 %[wid], 0\n" \
"s_cbranch_scc1 LtB3_%=\n" \
"global_load_dword v237, v236, s[58:59]\n" \
"s_add_u32 s61, s48, 49152\n" \
"s_mov_b32 exec_hi, 0\n" \
"s_nop 0\n" \
"buffer_load_dword v237, v224, %[srdp], s61 offen\n" \
"s_mov_b32 exec_hi, -1\n" \
"s_nop 0\n" \
"LtB3_%=:\n" \
"v_mfma_f32_16x16x32_bf16 v[144:147], a[96:99], a[32:35], v[168:171]\n" \
"v_exp_f32_e64 v140, v140\n" \
"v_mfma_f32_16x16x32_bf16 v[144:147], a[100:103], a[36:39], v[144:147]\n" \
"ds_read_b128 a[92:95], v210 offset:36864\n" \
"v_mfma_f32_16x16x32_bf16 v[144:147], a[104:107], a[40:43], v[144:147]\n" \
"v_exp_f32_e64 v141, v141\n" \
"v_mfma_f32_16x16x32_bf16 v[144:147], a[108:111], a[44:47], v[144:147]\n" \
"v_cvt_pk_bf16_f32 v196, v136, v137\n" \
"v_cvt_pk_bf16_f32 v197, v138, v139\n" \
"v_mfma_f32_16x16x32_bf16 v[148:151], a[112:115], a[32:35], v[168:171]\n" \
"v_exp_f32_e64 v142, v142\n" \
"v_mfma_f32_16x16x32_bf16 v[148:151], a[116:119], a[36:39], v[148:151]\n" \
"v_mfma_f32_16x16x32_bf16 v[148:151], a[120:123], a[40:43], v[148:151]\n" \
"buffer_load_dwordx4 %[vd0], %[srdv], s48 offen offset:2048 lds\n" \
"v_mfma_f32_16x16x32_bf16 v[148:151], a[124:127], a[44:47], v[148:151]\n" \
"v_exp_f32_e64 v143, v143\n" \
"v_mfma_f32_16x16x32_bf16 v[96:99], a[160:163], v[204:207], v[96:99]\n" \
"ds_read_b64_tr_b16 a[160:161], %[vvb] offset:24576\n" \
"v_mfma_f32_16x16x32_bf16 v[96:99], a[128:131], v[220:223], v[96:99]\n" \
"v_exp_f32_e64 v144, v144\n" \
"v_mfma_f32_16x16x32_bf16 v[100:103], a[164:167], v[204:207], v[100:103]\n" \
"v_cvt_pk_bf16_f32 v198, v140, v141\n" \
"v_cvt_pk_bf16_f32 v199, v142, v143\n" \
"v_mfma_f32_16x16x32_bf16 v[100:103], a[132:135], v[220:223], v[100:103]\n" \
"v_exp_f32_e64 v145, v145\n" \
"v_mfma_f32_16x16x32_bf16 v[104:107], a[168:171], v[204:207], v[104:107]\n" \
"ds_read_b64_tr_b16 a[162:163], %[vvb] offset:28672\n" \
"ds_read_b64_tr_b16 a[164:165], v211 offset:24576\n" \
"v_mfma_f32_16x16x32_bf16 v[104:107], a[136:139], v[220:223], v[104:107]\n" \
"v_exp_f32_e64 v146, v146\n" \
"v_mfma_f32_16x16x32_bf16 v[108:111], a[172:175], v[204:207], v[108:111]\n" \
"ds_read_b64_tr_b16 a[166:167], v211 offset:28672\n" \
"v_mfma_f32_16x16x32_bf16 v[108:111], a[140:143], v[220:223], v[108:111]\n" \
"v_exp_f32_e64 v147, v147\n" \
"v_smfmac_f32_16x16x64_bf16 v[180:183], a[192:195], v[216:223], v228\n" \
"ds_read_b64_tr_b16 a[168:169], %[vvb] offset:25088\n" \
"buffer_store_dwordx4 a[196:199], v235, %[srdz], s62 offen offset:0 sc1\n" \
"v_mfma_f32_16x16x32_bf16 v[152:155], a[96:99], a[48:51], v[172:175]\n" \
"v_exp_f32_e64 v148, v148\n" \
"v_mfma_f32_16x16x32_bf16 v[152:155], a[100:103], a[52:55], v[152:155]\n" \
"ds_read_b64_tr_b16 a[170:171], %[vvb] offset:29184\n" \
"v_mfma_f32_16x16x32_bf16 v[152:155], a[104:107], a[56:59], v[152:155]\n" \
"v_exp_f32_e64 v149, v149\n" \
"v_mfma_f32_16x16x32_bf16 v[152:155], a[108:111], a[60:63], v[152:155]\n" \
"v_cvt_pk_bf16_f32 v200, v144, v145\n" \
"v_cvt_pk_bf16_f32 v201, v146, v147\n" \
"v_mfma_f32_16x16x32_bf16 v[156:159], a[112:115], a[48:51], v[172:175]\n" \
"v_exp_f32_e64 v150, v150\n" \
"v_mfma_f32_16x16x32_bf16 v[156:159], a[116:119], a[52:55], v[156:159]\n" \
"ds_read_b64_tr_b16 a[172:173], v211 offset:25088\n" \
"v_mfma_f32_16x16x32_bf16 v[156:159], a[120:123], a[56:59], v[156:159]\n" \
"buffer_load_dwordx4 v215, %[srdv], s48 offen offset:3072 lds\n" \
"v_mfma_f32_16x16x32_bf16 v[156:159], a[124:127], a[60:63], v[156:159]\n" \
"v_exp_f32_e64 v151, v151\n" \
"v_mfma_f32_16x16x32_bf16 v[112:115], a[176:179], v[204:207], v[112:115]\n" \
"ds_read_b64_tr_b16 a[174:175], v211 offset:29184\n" \
"ds_read_b64_tr_b16 a[176:177], %[vvb] offset:25600\n" \
"v_mfma_f32_16x16x32_bf16 v[112:115], a[144:147], v[220:223], v[112:115]\n" \
"v_exp_f32_e64 v152, v152\n" \
"v_mfma_f32_16x16x32_bf16 v[116:119], a[180:183], v[204:207], v[116:119]\n" \
"v_cvt_pk_bf16_f32 v202, v148, v149\n" \
"v_cvt_pk_bf16_f32 v203, v150, v151\n" \
"v_mfma_f32_16x16x32_bf16 v[116:119], a[148:151], v[220:223], v[116:119]\n" \
"v_exp_f32_e64 v153, v153\n" \
"v_mfma_f32_16x16x32_bf16 v[120:123], a[184:187], v[204:207], v[120:123]\n" \
"v_exp_f32_e64 v154, v154\n" \
"v_mfma_f32_16x16x32_bf16 v[120:123], a[152:155], v[220:223], v[120:123]\n" \
"ds_read_b64_tr_b16 a[178:179], %[vvb] offset:29696\n" \
"v_mfma_f32_16x16x32_bf16 v[124:127], a[188:191], v[204:207], v[124:127]\n" \
"v_exp_f32_e64 v155, v155\n" \
"v_mfma_f32_16x16x32_bf16 v[124:127], a[156:159], v[220:223], v[124:127]\n" \
"ds_read_b64_tr_b16 a[180:181], v211 offset:25600\n" \
"s_waitcnt lgkmcnt(13)\n" \
"v_mfma_f32_16x16x32_bf16 v[128:131], a[64:67], a[0:3], v[160:163]\n" \
"v_exp_f32_e32 v156, v156\n" \
"v_mfma_f32_16x16x32_bf16 v[128:131], a[68:71], a[4:7], v[128:131]\n" \
"ds_read_b64_tr_b16 a[182:183], v211 offset:29696\n" \
"s_add_u32 m0, %[m0b], 16384\n" \
"v_mfma_f32_16x16x32_bf16 v[128:131], a[72:75], a[8:11], v[128:131]\n" \
"v_exp_f32_e64 v157, v157\n" \
"v_mfma_f32_16x16x32_bf16 v[128:131], a[76:79], a[12:15], v[128:131]\n" \
"v_cvt_pk_bf16_f32 v204, v152, v153\n" \
"v_cvt_pk_bf16_f32 v205, v154, v155\n" \
"v_mfma_f32_16x16x32_bf16 v[132:135], a[80:83], a[0:3], v[160:163]\n" \
"v_exp_f32_e64 v158, v158\n" \
"v_mfma_f32_16x16x32_bf16 v[132:135], a[84:87], a[4:7], v[132:135]\n" \
"ds_read_b64_tr_b16 a[184:185], %[vvb] offset:26112\n" \
"s_waitcnt lgkmcnt(5)\n" \
"v_mfma_f32_16x16x32_bf16 v[132:135], a[88:91], a[8:11], v[132:135]\n" \
"buffer_load_dwordx4 %[kd0], %[srdk], s48 offen offset:0 lds\n" \
"v_mfma_f32_16x16x32_bf16 v[132:135], a[92:95], a[12:15], v[132:135]\n" \
"v_exp_f32_e32 v159, v159\n" \
"v_mfma_f32_16x16x32_bf16 v[64:67], a[128:131], v[216:219], v[64:67]\n" \
"ds_read_b64_tr_b16 a[186:187], %[vvb] offset:30208\n" \
"ds_read_b64_tr_b16 a[188:189], v211 offset:26112\n" \
"v_mfma_f32_16x16x32_bf16 v[64:67], a[160:163], v[200:203], v[64:67]\n" \
"v_exp_f32_e64 v128, v128\n" \
"v_mfma_f32_16x16x32_bf16 v[68:71], a[132:135], v[216:219], v[68:71]\n" \
"v_cvt_pk_bf16_f32 v206, v156, v157\n" \
"v_cvt_pk_bf16_f32 v207, v158, v159\n" \
"v_mfma_f32_16x16x32_bf16 v[68:71], a[164:167], v[200:203], v[68:71]\n" \
"v_exp_f32_e64 v129, v129\n" \
"v_mfma_f32_16x16x32_bf16 v[72:75], a[136:139], v[216:219], v[72:75]\n" \
"ds_read_b64_tr_b16 a[190:191], v211 offset:30208\n" \
"ds_read_b64_tr_b16 a[128:129], %[vvb] offset:32768\n" \
"v_mfma_f32_16x16x32_bf16 v[72:75], a[168:171], v[200:203], v[72:75]\n" \
"v_exp_f32_e64 v130, v130\n" \
"v_mfma_f32_16x16x32_bf16 v[76:79], a[140:143], v[216:219], v[76:79]\n" \
"ds_read_b64_tr_b16 a[130:131], %[vvb] offset:36864\n" \
"ds_read_b64_tr_b16 a[132:133], v211 offset:32768\n" \
"v_mfma_f32_16x16x32_bf16 v[76:79], a[172:175], v[200:203], v[76:79]\n" \
"v_exp_f32_e64 v131, v131\n" \
"v_smfmac_f32_16x16x64_bf16 v[176:179], a[192:195], v[192:199], v228\n" \
"ds_read_b64_tr_b16 a[134:135], v211 offset:36864\n" \
"ds_read_b64_tr_b16 a[136:137], %[vvb] offset:33280\n" \
"v_mfma_f32_16x16x32_bf16 v[136:139], a[64:67], a[16:19], v[164:167]\n" \
"v_exp_f32_e64 v132, v132\n" \
"v_mfma_f32_16x16x32_bf16 v[136:139], a[68:71], a[20:23], v[136:139]\n" \
"ds_read_b64_tr_b16 a[138:139], %[vvb] offset:37376\n" \
"ds_read_b64_tr_b16 a[140:141], v211 offset:33280\n" \
"v_mfma_f32_16x16x32_bf16 v[136:139], a[72:75], a[24:27], v[136:139]\n" \
"v_exp_f32_e64 v133, v133\n" \
"v_mfma_f32_16x16x32_bf16 v[136:139], a[76:79], a[28:31], v[136:139]\n" \
"v_cvt_pk_bf16_f32 v184, v128, v129\n" \
"v_cvt_pk_bf16_f32 v185, v130, v131\n" \
"v_mfma_f32_16x16x32_bf16 v[140:143], a[80:83], a[16:19], v[164:167]\n" \
"v_exp_f32_e64 v134, v134\n" \
"v_mfma_f32_16x16x32_bf16 v[140:143], a[84:87], a[20:23], v[140:143]\n" \
"ds_read_b64_tr_b16 a[142:143], v211 offset:37376\n" \
"s_waitcnt lgkmcnt(8)\n" \
"v_mfma_f32_16x16x32_bf16 v[140:143], a[88:91], a[24:27], v[140:143]\n" \
"buffer_load_dwordx4 v212, %[srdk], s48 offen offset:1024 lds\n" \
"v_mfma_f32_16x16x32_bf16 v[140:143], a[92:95], a[28:31], v[140:143]\n" \
"v_exp_f32_e32 v135, v135\n" \
"v_mfma_f32_16x16x32_bf16 v[80:83], a[144:147], v[216:219], v[80:83]\n" \
".if . - adaln_body != 24576\n" \
".error \"size model\"\n" \
".endif\n" \
"Lk5_%=:\n" \
"s_cbranch_execz adaln_kr\n" \
"s_nop 0\n" \
"ds_read_b64_tr_b16 a[144:145], %[vvb] offset:33792\n" \
"ds_read_b64_tr_b16 a[146:147], %[vvb] offset:37888\n" \
"v_mfma_f32_16x16x32_bf16 v[80:83], a[176:179], v[200:203], v[80:83]\n" \
"v_exp_f32_e64 v136, v136\n" \
"v_mfma_f32_16x16x32_bf16 v[84:87], a[148:151], v[216:219], v[84:87]\n" \
"v_cvt_pk_bf16_f32 v186, v132, v133\n" \
"v_cvt_pk_bf16_f32 v187, v134, v135\n" \
"v_mfma_f32_16x16x32_bf16 v[84:87], a[180:183], v[200:203], v[84:87]\n" \
"v_exp_f32_e64 v137, v137\n" \
"v_mfma_f32_16x16x32_bf16 v[88:91], a[152:155], v[216:219], v[88:91]\n" \
"v_exp_f32_e64 v138, v138\n" \
"v_mfma_f32_16x16x32_bf16 v[88:91], a[184:187], v[200:203], v[88:91]\n" \
"ds_read_b64_tr_b16 a[148:149], v211 offset:33792\n" \
"ds_read_b64_tr_b16 a[150:151], v211 offset:37888\n" \
"v_mfma_f32_16x16x32_bf16 v[92:95], a[156:159], v[216:219], v[92:95]\n" \
"v_exp_f32_e64 v139, v139\n" \
"v_mfma_f32_16x16x32_bf16 v[92:95], a[188:191], v[200:203], v[92:95]\n" \
"ds_read_b64_tr_b16 a[152:153], %[vvb] offset:34304\n" \
"buffer_store_dwordx4 a[196:199], v235, %[srdz], s63 offen offset:1024 sc1\n" \
"v_mfma_f32_16x16x32_bf16 v[144:147], a[64:67], a[32:35], v[168:171]\n" \
"v_exp_f32_e64 v140, v140\n" \
"v_mfma_f32_16x16x32_bf16 v[144:147], a[68:71], a[36:39], v[144:147]\n" \
"ds_read_b64_tr_b16 a[154:155], %[vvb] offset:38400\n" \
"ds_read_b64_tr_b16 a[156:157], v211 offset:34304\n" \
"v_mfma_f32_16x16x32_bf16 v[144:147], a[72:75], a[40:43], v[144:147]\n" \
"v_exp_f32_e64 v141, v141\n" \
"v_mfma_f32_16x16x32_bf16 v[144:147], a[76:79], a[44:47], v[144:147]\n" \
"v_cvt_pk_bf16_f32 v188, v136, v137\n" \
"v_cvt_pk_bf16_f32 v189, v138, v139\n" \
"v_mfma_f32_16x16x32_bf16 v[148:151], a[80:83], a[32:35], v[168:171]\n" \
"v_exp_f32_e64 v142, v142\n" \
"v_mfma_f32_16x16x32_bf16 v[148:151], a[84:87], a[36:39], v[148:151]\n" \
"ds_read_b64_tr_b16 a[158:159], v211 offset:38400\n" \
"s_waitcnt lgkmcnt(8)\n" \
"v_mfma_f32_16x16x32_bf16 v[148:151], a[88:91], a[40:43], v[148:151]\n" \
"buffer_load_dwordx4 v213, %[srdk], s48 offen offset:2048 lds\n" \
"v_mfma_f32_16x16x32_bf16 v[148:151], a[92:95], a[44:47], v[148:151]\n" \
"v_exp_f32_e32 v143, v143\n" \
"v_mfma_f32_16x16x32_bf16 v[0:3], a[160:163], v[192:195], v[0:3]\n" \
"ds_read_b128 a[96:99], %[kb0] offset:40960\n" \
"v_mfma_f32_16x16x32_bf16 v[0:3], a[128:131], v[184:187], v[0:3]\n" \
"v_exp_f32_e64 v144, v144\n" \
"v_mfma_f32_16x16x32_bf16 v[4:7], a[164:167], v[192:195], v[4:7]\n" \
"v_cvt_pk_bf16_f32 v190, v140, v141\n" \
"v_cvt_pk_bf16_f32 v191, v142, v143\n" \
"v_mfma_f32_16x16x32_bf16 v[4:7], a[132:135], v[184:187], v[4:7]\n" \
"v_exp_f32_e64 v145, v145\n" \
"v_mfma_f32_16x16x32_bf16 v[8:11], a[168:171], v[192:195], v[8:11]\n" \
"ds_read_b128 a[100:103], v208 offset:40960\n" \
"v_mfma_f32_16x16x32_bf16 v[8:11], a[136:139], v[184:187], v[8:11]\n" \
"v_exp_f32_e64 v146, v146\n" \
"v_mfma_f32_16x16x32_bf16 v[12:15], a[172:175], v[192:195], v[12:15]\n" \
"ds_read_b128 a[104:107], v209 offset:40960\n" \
"v_mfma_f32_16x16x32_bf16 v[12:15], a[140:143], v[184:187], v[12:15]\n" \
"v_exp_f32_e64 v147, v147\n" \
"v_smfmac_f32_16x16x64_bf16 v[180:183], a[192:195], v[200:207], v228\n" \
"ds_read_b128 a[108:111], v210 offset:40960\n" \
"v_mfma_f32_16x16x32_bf16 v[152:155], a[64:67], a[48:51], v[172:175]\n" \
"v_exp_f32_e64 v148, v148\n" \
"v_mfma_f32_16x16x32_bf16 v[152:155], a[68:71], a[52:55], v[152:155]\n" \
"ds_read_b128 a[112:115], %[kb0] offset:45056\n" \
"v_mfma_f32_16x16x32_bf16 v[152:155], a[72:75], a[56:59], v[152:155]\n" \
"v_exp_f32_e64 v149, v149\n" \
"v_mfma_f32_16x16x32_bf16 v[152:155], a[76:79], a[60:63], v[152:155]\n" \
"v_cvt_pk_bf16_f32 v216, v144, v145\n" \
"v_cvt_pk_bf16_f32 v217, v146, v147\n" \
"v_mfma_f32_16x16x32_bf16 v[156:159], a[80:83], a[48:51], v[172:175]\n" \
"v_exp_f32_e64 v150, v150\n" \
"v_mfma_f32_16x16x32_bf16 v[156:159], a[84:87], a[52:55], v[156:159]\n" \
"ds_read_b128 a[116:119], v208 offset:45056\n" \
"s_waitcnt lgkmcnt(6)\n" \
"v_mfma_f32_16x16x32_bf16 v[156:159], a[88:91], a[56:59], v[156:159]\n" \
"buffer_load_dwordx4 v214, %[srdk], s48 offen offset:3072 lds\n" \
"v_mfma_f32_16x16x32_bf16 v[156:159], a[92:95], a[60:63], v[156:159]\n" \
"v_exp_f32_e32 v151, v151\n" \
"v_mfma_f32_16x16x32_bf16 v[16:19], a[176:179], v[192:195], v[16:19]\n" \
"ds_read_b128 a[120:123], v209 offset:45056\n" \
"v_mfma_f32_16x16x32_bf16 v[16:19], a[144:147], v[184:187], v[16:19]\n" \
"v_exp_f32_e64 v152, v152\n" \
"v_mfma_f32_16x16x32_bf16 v[20:23], a[180:183], v[192:195], v[20:23]\n" \
"v_cvt_pk_bf16_f32 v218, v148, v149\n" \
"v_cvt_pk_bf16_f32 v219, v150, v151\n" \
"v_mfma_f32_16x16x32_bf16 v[20:23], a[148:151], v[184:187], v[20:23]\n" \
"v_exp_f32_e64 v153, v153\n" \
"v_mfma_f32_16x16x32_bf16 v[24:27], a[184:187], v[192:195], v[24:27]\n" \
"v_exp_f32_e64 v154, v154\n" \
"v_mfma_f32_16x16x32_bf16 v[24:27], a[152:155], v[184:187], v[24:27]\n" \
"ds_read_b128 a[124:127], v210 offset:45056\n" \
"s_add_u32 s48, s48, 0x4000\n" \
"v_mfma_f32_16x16x32_bf16 v[28:31], a[188:191], v[192:195], v[28:31]\n" \
"v_exp_f32_e64 v155, v155\n" \
"v_mfma_f32_16x16x32_bf16 v[28:31], a[156:159], v[184:187], v[28:31]\n" \
"s_waitcnt vmcnt(16) lgkmcnt(2)\n" \
"s_barrier\n" \
"v_mfma_f32_16x16x32_bf16 v[128:131], a[96:99], a[0:3], v[160:163]\n" \
"v_exp_f32_e64 v156, v156\n" \
"v_mfma_f32_16x16x32_bf16 v[128:131], a[100:103], a[4:7], v[128:131]\n" \
"s_add_u32 m0, %[m0b], 81920\n" \
"v_mfma_f32_16x16x32_bf16 v[128:131], a[104:107], a[8:11], v[128:131]\n" \
"v_exp_f32_e64 v157, v157\n" \
"v_mfma_f32_16x16x32_bf16 v[128:131], a[108:111], a[12:15], v[128:131]\n" \
"v_cvt_pk_bf16_f32 v220, v152, v153\n" \
"v_cvt_pk_bf16_f32 v221, v154, v155\n" \
"v_mfma_f32_16x16x32_bf16 v[132:135], a[112:115], a[0:3], v[160:163]\n" \
"v_exp_f32_e64 v158, v158\n" \
"v_mfma_f32_16x16x32_bf16 v[132:135], a[116:119], a[4:7], v[132:135]\n" \
"s_waitcnt lgkmcnt(0)\n" \
"v_mfma_f32_16x16x32_bf16 v[132:135], a[120:123], a[8:11], v[132:135]\n" \
"buffer_load_dwordx4 %[vd0], %[srdv], s48 offen offset:0 lds\n" \
"v_mfma_f32_16x16x32_bf16 v[132:135], a[124:127], a[12:15], v[132:135]\n" \
"v_exp_f32_e32 v159, v159\n" \
"v_mfma_f32_16x16x32_bf16 v[32:35], a[160:163], v[196:199], v[32:35]\n" \
"ds_read_b128 a[64:67], %[kb0] offset:49152\n" \
"v_mfma_f32_16x16x32_bf16 v[32:35], a[128:131], v[188:191], v[32:35]\n" \
"v_exp_f32_e64 v128, v128\n" \
"v_mfma_f32_16x16x32_bf16 v[36:39], a[164:167], v[196:199], v[36:39]\n" \
"v_cvt_pk_bf16_f32 v222, v156, v157\n" \
"v_cvt_pk_bf16_f32 v223, v158, v159\n" \
"v_mfma_f32_16x16x32_bf16 v[36:39], a[132:135], v[188:191], v[36:39]\n" \
"v_exp_f32_e64 v129, v129\n" \
"v_mfma_f32_16x16x32_bf16 v[40:43], a[168:171], v[196:199], v[40:43]\n" \
"ds_read_b128 a[68:71], v208 offset:49152\n" \
"v_mfma_f32_16x16x32_bf16 v[40:43], a[136:139], v[188:191], v[40:43]\n" \
"v_exp_f32_e64 v130, v130\n" \
"v_mfma_f32_16x16x32_bf16 v[44:47], a[172:175], v[196:199], v[44:47]\n" \
"ds_read_b128 a[72:75], v209 offset:49152\n" \
"v_mfma_f32_16x16x32_bf16 v[44:47], a[140:143], v[188:191], v[44:47]\n" \
"v_exp_f32_e64 v131, v131\n" \
"v_smfmac_f32_16x16x64_bf16 v[176:179], a[192:195], v[184:191], v228\n" \
"v_mfma_f32_16x16x32_bf16 v[136:139], a[96:99], a[16:19], v[164:167]\n" \
"v_exp_f32_e64 v132, v132\n" \
"v_mfma_f32_16x16x32_bf16 v[136:139], a[100:103], a[20:23], v[136:139]\n" \
"ds_read_b128 a[76:79], v210 offset:49152\n" \
"v_mfma_f32_16x16x32_bf16 v[136:139], a[104:107], a[24:27], v[136:139]\n" \
"v_exp_f32_e64 v133, v133\n" \
"v_mfma_f32_16x16x32_bf16 v[136:139], a[108:111], a[28:31], v[136:139]\n" \
"v_cvt_pk_bf16_f32 v192, v128, v129\n" \
"v_cvt_pk_bf16_f32 v193, v130, v131\n" \
"v_mfma_f32_16x16x32_bf16 v[140:143], a[112:115], a[16:19], v[164:167]\n" \
"v_exp_f32_e64 v134, v134\n" \
"v_mfma_f32_16x16x32_bf16 v[140:143], a[116:119], a[20:23], v[140:143]\n" \
"ds_read_b128 a[80:83], %[kb0] offset:53248\n" \
"v_mfma_f32_16x16x32_bf16 v[140:143], a[120:123], a[24:27], v[140:143]\n" \
"buffer_load_dwordx4 v215, %[srdv], s48 offen offset:1024 lds\n" \
"v_mfma_f32_16x16x32_bf16 v[140:143], a[124:127], a[28:31], v[140:143]\n" \
"v_exp_f32_e64 v135, v135\n" \
"v_mfma_f32_16x16x32_bf16 v[48:51], a[176:179], v[196:199], v[48:51]\n" \
"ds_read_b128 a[84:87], v208 offset:53248\n" \
"v_mfma_f32_16x16x32_bf16 v[48:51], a[144:147], v[188:191], v[48:51]\n" \
"v_exp_f32_e64 v136, v136\n" \
"v_mfma_f32_16x16x32_bf16 v[52:55], a[180:183], v[196:199], v[52:55]\n" \
"v_cvt_pk_bf16_f32 v194, v132, v133\n" \
"v_cvt_pk_bf16_f32 v195, v134, v135\n" \
"v_mfma_f32_16x16x32_bf16 v[52:55], a[148:151], v[188:191], v[52:55]\n" \
"v_exp_f32_e64 v137, v137\n" \
"v_mfma_f32_16x16x32_bf16 v[56:59], a[184:187], v[196:199], v[56:59]\n" \
"v_exp_f32_e64 v138, v138\n" \
"v_mfma_f32_16x16x32_bf16 v[56:59], a[152:155], v[188:191], v[56:59]\n" \
"ds_read_b128 a[88:91], v209 offset:53248\n" \
"v_mfma_f32_16x16x32_bf16 v[60:63], a[188:191], v[196:199], v[60:63]\n" \
"v_exp_f32_e64 v139, v139\n" \
"v_mfma_f32_16x16x32_bf16 v[60:63], a[156:159], v[188:191], v[60:63]\n" \
"s_cmp_lg_u32 %[wid], 1\n" \
"s_cbranch_scc1 LtB5_%=\n" \
"global_load_dword v237, v236, s[58:59]\n" \
"s_add_u32 s61, s48, 49152\n" \
"s_mov_b32 exec_hi, 0\n" \
"s_nop 0\n" \
"buffer_load_dword v237, v224, %[srdp], s61 offen\n" \
"s_mov_b32 exec_hi, -1\n" \
"s_nop 0\n" \
"LtB5_%=:\n" \
"v_mfma_f32_16x16x32_bf16 v[144:147], a[96:99], a[32:35], v[168:171]\n" \
"v_exp_f32_e64 v140, v140\n" \
"v_mfma_f32_16x16x32_bf16 v[144:147], a[100:103], a[36:39], v[144:147]\n" \
"ds_read_b128 a[92:95], v210 offset:53248\n" \
"v_mfma_f32_16x16x32_bf16 v[144:147], a[104:107], a[40:43], v[144:147]\n" \
"v_exp_f32_e64 v141, v141\n" \
"v_mfma_f32_16x16x32_bf16 v[144:147], a[108:111], a[44:47], v[144:147]\n" \
"v_cvt_pk_bf16_f32 v196, v136, v137\n" \
"v_cvt_pk_bf16_f32 v197, v138, v139\n" \
"v_mfma_f32_16x16x32_bf16 v[148:151], a[112:115], a[32:35], v[168:171]\n" \
"v_exp_f32_e64 v142, v142\n" \
"v_mfma_f32_16x16x32_bf16 v[148:151], a[116:119], a[36:39], v[148:151]\n" \
"v_mfma_f32_16x16x32_bf16 v[148:151], a[120:123], a[40:43], v[148:151]\n" \
"buffer_load_dwordx4 %[vd0], %[srdv], s48 offen offset:2048 lds\n" \
"v_mfma_f32_16x16x32_bf16 v[148:151], a[124:127], a[44:47], v[148:151]\n" \
"v_exp_f32_e64 v143, v143\n" \
"v_mfma_f32_16x16x32_bf16 v[96:99], a[160:163], v[204:207], v[96:99]\n" \
"ds_read_b64_tr_b16 a[160:161], %[vvb] offset:40960\n" \
"v_mfma_f32_16x16x32_bf16 v[96:99], a[128:131], v[220:223], v[96:99]\n" \
"v_exp_f32_e64 v144, v144\n" \
"v_mfma_f32_16x16x32_bf16 v[100:103], a[164:167], v[204:207], v[100:103]\n" \
"v_cvt_pk_bf16_f32 v198, v140, v141\n" \
"v_cvt_pk_bf16_f32 v199, v142, v143\n" \
"v_mfma_f32_16x16x32_bf16 v[100:103], a[132:135], v[220:223], v[100:103]\n" \
"v_exp_f32_e64 v145, v145\n" \
"v_mfma_f32_16x16x32_bf16 v[104:107], a[168:171], v[204:207], v[104:107]\n" \
"ds_read_b64_tr_b16 a[162:163], %[vvb] offset:45056\n" \
"ds_read_b64_tr_b16 a[164:165], v211 offset:40960\n" \
"v_mfma_f32_16x16x32_bf16 v[104:107], a[136:139], v[220:223], v[104:107]\n" \
"v_exp_f32_e64 v146, v146\n" \
"v_mfma_f32_16x16x32_bf16 v[108:111], a[172:175], v[204:207], v[108:111]\n" \
"ds_read_b64_tr_b16 a[166:167], v211 offset:45056\n" \
"v_mfma_f32_16x16x32_bf16 v[108:111], a[140:143], v[220:223], v[108:111]\n" \
"v_exp_f32_e64 v147, v147\n" \
"v_smfmac_f32_16x16x64_bf16 v[180:183], a[192:195], v[216:223], v228\n" \
"ds_read_b64_tr_b16 a[168:169], %[vvb] offset:41472\n" \
"v_mfma_f32_16x16x32_bf16 v[152:155], a[96:99], a[48:51], v[172:175]\n" \
"v_exp_f32_e64 v148, v148\n" \
"v_mfma_f32_16x16x32_bf16 v[152:155], a[100:103], a[52:55], v[152:155]\n" \
"ds_read_b64_tr_b16 a[170:171], %[vvb] offset:45568\n" \
"v_mfma_f32_16x16x32_bf16 v[152:155], a[104:107], a[56:59], v[152:155]\n" \
"v_exp_f32_e64 v149, v149\n" \
"v_mfma_f32_16x16x32_bf16 v[152:155], a[108:111], a[60:63], v[152:155]\n" \
"v_cvt_pk_bf16_f32 v200, v144, v145\n" \
"v_cvt_pk_bf16_f32 v201, v146, v147\n" \
"v_mfma_f32_16x16x32_bf16 v[156:159], a[112:115], a[48:51], v[172:175]\n" \
"v_exp_f32_e64 v150, v150\n" \
"v_mfma_f32_16x16x32_bf16 v[156:159], a[116:119], a[52:55], v[156:159]\n" \
"ds_read_b64_tr_b16 a[172:173], v211 offset:41472\n" \
"v_mfma_f32_16x16x32_bf16 v[156:159], a[120:123], a[56:59], v[156:159]\n" \
"buffer_load_dwordx4 v215, %[srdv], s48 offen offset:3072 lds\n" \
"v_mfma_f32_16x16x32_bf16 v[156:159], a[124:127], a[60:63], v[156:159]\n" \
"v_exp_f32_e64 v151, v151\n" \
"v_mfma_f32_16x16x32_bf16 v[112:115], a[176:179], v[204:207], v[112:115]\n" \
"ds_read_b64_tr_b16 a[174:175], v211 offset:45568\n" \
"ds_read_b64_tr_b16 a[176:177], %[vvb] offset:41984\n" \
"v_mfma_f32_16x16x32_bf16 v[112:115], a[144:147], v[220:223], v[112:115]\n" \
"v_exp_f32_e64 v152, v152\n" \
"v_mfma_f32_16x16x32_bf16 v[116:119], a[180:183], v[204:207], v[116:119]\n" \
"v_cvt_pk_bf16_f32 v202, v148, v149\n" \
"v_cvt_pk_bf16_f32 v203, v150, v151\n" \
"v_mfma_f32_16x16x32_bf16 v[116:119], a[148:151], v[220:223], v[116:119]\n" \
"v_exp_f32_e64 v153, v153\n" \
"v_mfma_f32_16x16x32_bf16 v[120:123], a[184:187], v[204:207], v[120:123]\n" \
"v_exp_f32_e64 v154, v154\n" \
"v_mfma_f32_16x16x32_bf16 v[120:123], a[152:155], v[220:223], v[120:123]\n" \
"ds_read_b64_tr_b16 a[178:179], %[vvb] offset:46080\n" \
"v_mfma_f32_16x16x32_bf16 v[124:127], a[188:191], v[204:207], v[124:127]\n" \
"v_exp_f32_e64 v155, v155\n" \
"v_mfma_f32_16x16x32_bf16 v[124:127], a[156:159], v[220:223], v[124:127]\n" \
"ds_read_b64_tr_b16 a[180:181], v211 offset:41984\n" \
"s_waitcnt lgkmcnt(13)\n" \
"v_mfma_f32_16x16x32_bf16 v[128:131], a[64:67], a[0:3], v[160:163]\n" \
"v_exp_f32_e32 v156, v156\n" \
"v_mfma_f32_16x16x32_bf16 v[128:131], a[68:71], a[4:7], v[128:131]\n" \
"ds_read_b64_tr_b16 a[182:183], v211 offset:46080\n" \
"s_add_u32 m0, %[m0b], 32768\n" \
"v_mfma_f32_16x16x32_bf16 v[128:131], a[72:75], a[8:11], v[128:131]\n" \
"v_exp_f32_e64 v157, v157\n" \
"v_mfma_f32_16x16x32_bf16 v[128:131], a[76:79], a[12:15], v[128:131]\n" \
"v_cvt_pk_bf16_f32 v204, v152, v153\n" \
"v_cvt_pk_bf16_f32 v205, v154, v155\n" \
"v_mfma_f32_16x16x32_bf16 v[132:135], a[80:83], a[0:3], v[160:163]\n" \
"v_exp_f32_e64 v158, v158\n" \
"v_mfma_f32_16x16x32_bf16 v[132:135], a[84:87], a[4:7], v[132:135]\n" \
"ds_read_b64_tr_b16 a[184:185], %[vvb] offset:42496\n" \
"s_waitcnt lgkmcnt(5)\n" \
"v_mfma_f32_16x16x32_bf16 v[132:135], a[88:91], a[8:11], v[132:135]\n" \
"buffer_load_dwordx4 %[kd0], %[srdk], s48 offen offset:0 lds\n" \
"v_mfma_f32_16x16x32_bf16 v[132:135], a[92:95], a[12:15], v[132:135]\n" \
"v_exp_f32_e32 v159, v159\n" \
"v_mfma_f32_16x16x32_bf16 v[64:67], a[128:131], v[216:219], v[64:67]\n" \
"ds_read_b64_tr_b16 a[186:187], %[vvb] offset:46592\n" \
"ds_read_b64_tr_b16 a[188:189], v211 offset:42496\n" \
"v_mfma_f32_16x16x32_bf16 v[64:67], a[160:163], v[200:203], v[64:67]\n" \
"v_exp_f32_e64 v128, v128\n" \
"v_mfma_f32_16x16x32_bf16 v[68:71], a[132:135], v[216:219], v[68:71]\n" \
"v_cvt_pk_bf16_f32 v206, v156, v157\n" \
"v_cvt_pk_bf16_f32 v207, v158, v159\n" \
"v_mfma_f32_16x16x32_bf16 v[68:71], a[164:167], v[200:203], v[68:71]\n" \
"v_exp_f32_e64 v129, v129\n" \
"v_mfma_f32_16x16x32_bf16 v[72:75], a[136:139], v[216:219], v[72:75]\n" \
"ds_read_b64_tr_b16 a[190:191], v211 offset:46592\n" \
"ds_read_b64_tr_b16 a[128:129], %[vvb] offset:49152\n" \
"v_mfma_f32_16x16x32_bf16 v[72:75], a[168:171], v[200:203], v[72:75]\n" \
"v_exp_f32_e64 v130, v130\n" \
"v_mfma_f32_16x16x32_bf16 v[76:79], a[140:143], v[216:219], v[76:79]\n" \
"ds_read_b64_tr_b16 a[130:131], %[vvb] offset:53248\n" \
"ds_read_b64_tr_b16 a[132:133], v211 offset:49152\n" \
"v_mfma_f32_16x16x32_bf16 v[76:79], a[172:175], v[200:203], v[76:79]\n" \
"v_exp_f32_e64 v131, v131\n" \
"v_smfmac_f32_16x16x64_bf16 v[176:179], a[192:195], v[192:199], v228\n" \
"ds_read_b64_tr_b16 a[134:135], v211 offset:53248\n" \
"ds_read_b64_tr_b16 a[136:137], %[vvb] offset:49664\n" \
"buffer_store_dwordx4 a[196:199], v235, %[srdz], s62 offen offset:1024 sc1\n" \
"v_mfma_f32_16x16x32_bf16 v[136:139], a[64:67], a[16:19], v[164:167]\n" \
"v_exp_f32_e64 v132, v132\n" \
"v_mfma_f32_16x16x32_bf16 v[136:139], a[68:71], a[20:23], v[136:139]\n" \
"ds_read_b64_tr_b16 a[138:139], %[vvb] offset:53760\n" \
"ds_read_b64_tr_b16 a[140:141], v211 offset:49664\n" \
"v_mfma_f32_16x16x32_bf16 v[136:139], a[72:75], a[24:27], v[136:139]\n" \
"v_exp_f32_e64 v133, v133\n" \
"v_mfma_f32_16x16x32_bf16 v[136:139], a[76:79], a[28:31], v[136:139]\n" \
"v_cvt_pk_bf16_f32 v184, v128, v129\n" \
"v_cvt_pk_bf16_f32 v185, v130, v131\n" \
"v_mfma_f32_16x16x32_bf16 v[140:143], a[80:83], a[16:19], v[164:167]\n" \
"v_exp_f32_e64 v134, v134\n" \
"v_mfma_f32_16x16x32_bf16 v[140:143], a[84:87], a[20:23], v[140:143]\n" \
"ds_read_b64_tr_b16 a[142:143], v211 offset:53760\n" \
"s_waitcnt lgkmcnt(8)\n" \
"v_mfma_f32_16x16x32_bf16 v[140:143], a[88:91], a[24:27], v[140:143]\n" \
"buffer_load_dwordx4 v212, %[srdk], s48 offen offset:1024 lds\n" \
"v_mfma_f32_16x16x32_bf16 v[140:143], a[92:95], a[28:31], v[140:143]\n" \
"v_exp_f32_e32 v135, v135\n" \
"v_mfma_f32_16x16x32_bf16 v[80:83], a[144:147], v[216:219], v[80:83]\n" \
"ds_read_b64_tr_b16 a[144:145], %[vvb] offset:50176\n" \
"ds_read_b64_tr_b16 a[146:147], %[vvb] offset:54272\n" \
"v_mfma_f32_16x16x32_bf16 v[80:83], a[176:179], v[200:203], v[80:83]\n" \
"v_exp_f32_e64 v136, v136\n" \
"v_mfma_f32_16x16x32_bf16 v[84:87], a[148:151], v[216:219], v[84:87]\n" \
"v_cvt_pk_bf16_f32 v186, v132, v133\n" \
"v_cvt_pk_bf16_f32 v187, v134, v135\n" \
"v_mfma_f32_16x16x32_bf16 v[84:87], a[180:183], v[200:203], v[84:87]\n" \
"v_exp_f32_e64 v137, v137\n" \
"v_mfma_f32_16x16x32_bf16 v[88:91], a[152:155], v[216:219], v[88:91]\n" \
"v_exp_f32_e64 v138, v138\n" \
"v_mfma_f32_16x16x32_bf16 v[88:91], a[184:187], v[200:203], v[88:91]\n" \
"ds_read_b64_tr_b16 a[148:149], v211 offset:50176\n" \
"ds_read_b64_tr_b16 a[150:151], v211 offset:54272\n" \
"v_mfma_f32_16x16x32_bf16 v[92:95], a[156:159], v[216:219], v[92:95]\n" \
"v_exp_f32_e64 v139, v139\n" \
"v_mfma_f32_16x16x32_bf16 v[92:95], a[188:191], v[200:203], v[92:95]\n" \
"ds_read_b64_tr_b16 a[152:153], %[vvb] offset:50688\n" \
"global_load_dword v237, %[vct], %[pct]\n" \
"v_mfma_f32_16x16x32_bf16 v[144:147], a[64:67], a[32:35], v[168:171]\n" \
"v_exp_f32_e64 v140, v140\n" \
"v_mfma_f32_16x16x32_bf16 v[144:147], a[68:71], a[36:39], v[144:147]\n" \
"ds_read_b64_tr_b16 a[154:155], %[vvb] offset:54784\n" \
"ds_read_b64_tr_b16 a[156:157], v211 offset:50688\n" \
"v_mfma_f32_16x16x32_bf16 v[144:147], a[72:75], a[40:43], v[144:147]\n" \
"v_exp_f32_e64 v141, v141\n" \
"v_mfma_f32_16x16x32_bf16 v[144:147], a[76:79], a[44:47], v[144:147]\n" \
"v_cvt_pk_bf16_f32 v188, v136, v137\n" \
"v_cvt_pk_bf16_f32 v189, v138, v139\n" \
"v_mfma_f32_16x16x32_bf16 v[148:151], a[80:83], a[32:35], v[168:171]\n" \
"v_exp_f32_e64 v142, v142\n" \
"v_mfma_f32_16x16x32_bf16 v[148:151], a[84:87], a[36:39], v[148:151]\n" \
"ds_read_b64_tr_b16 a[158:159], v211 offset:54784\n" \
"s_waitcnt lgkmcnt(8)\n" \
"v_mfma_f32_16x16x32_bf16 v[148:151], a[88:91], a[40:43], v[148:151]\n" \
"buffer_load_dwordx4 v213, %[srdk], s48 offen offset:2048 lds\n" \
"v_mfma_f32_16x16x32_bf16 v[148:151], a[92:95], a[44:47], v[148:151]\n" \
"v_exp_f32_e32 v143, v143\n" \
"v_mfma_f32_16x16x32_bf16 v[0:3], a[160:163], v[192:195], v[0:3]\n" \
"ds_read_b128 a[96:99], %[kb0] offset:57344\n" \
"v_mfma_f32_16x16x32_bf16 v[0:3], a[128:131], v[184:187], v[0:3]\n" \
"v_exp_f32_e64 v144, v144\n" \
"v_mfma_f32_16x16x32_bf16 v[4:7], a[164:167], v[192:195], v[4:7]\n" \
"v_cvt_pk_bf16_f32 v190, v140, v141\n" \
"v_cvt_pk_bf16_f32 v191, v142, v143\n" \
"v_mfma_f32_16x16x32_bf16 v[4:7], a[132:135], v[184:187], v[4:7]\n" \
"v_exp_f32_e64 v145, v145\n" \
"v_mfma_f32_16x16x32_bf16 v[8:11], a[168:171], v[192:195], v[8:11]\n" \
"ds_read_b128 a[100:103], v208 offset:57344\n" \
"v_mfma_f32_16x16x32_bf16 v[8:11], a[136:139], v[184:187], v[8:11]\n" \
"v_exp_f32_e64 v146, v146\n" \
"v_mfma_f32_16x16x32_bf16 v[12:15], a[172:175], v[192:195], v[12:15]\n" \
"ds_read_b128 a[104:107], v209 offset:57344\n" \
"v_mfma_f32_16x16x32_bf16 v[12:15], a[140:143], v[184:187], v[12:15]\n" \
"v_exp_f32_e64 v147, v147\n" \
"v_smfmac_f32_16x16x64_bf16 v[180:183], a[192:195], v[200:207], v228\n" \
"ds_read_b128 a[108:111], v210 offset:57344\n" \
"v_mfma_f32_16x16x32_bf16 v[152:155], a[64:67], a[48:51], v[172:175]\n" \
"v_exp_f32_e64 v148, v148\n" \
"v_mfma_f32_16x16x32_bf16 v[152:155], a[68:71], a[52:55], v[152:155]\n" \
"ds_read_b128 a[112:115], %[kb0] offset:61440\n" \
"v_mfma_f32_16x16x32_bf16 v[152:155], a[72:75], a[56:59], v[152:155]\n" \
"v_exp_f32_e64 v149, v149\n" \
"v_mfma_f32_16x16x32_bf16 v[152:155], a[76:79], a[60:63], v[152:155]\n" \
"v_cvt_pk_bf16_f32 v216, v144, v145\n" \
"v_cvt_pk_bf16_f32 v217, v146, v147\n" \
"v_mfma_f32_16x16x32_bf16 v[156:159], a[80:83], a[48:51], v[172:175]\n" \
"v_exp_f32_e64 v150, v150\n" \
"v_mfma_f32_16x16x32_bf16 v[156:159], a[84:87], a[52:55], v[156:159]\n" \
"ds_read_b128 a[116:119], v208 offset:61440\n" \
"s_waitcnt lgkmcnt(6)\n" \
"v_mfma_f32_16x16x32_bf16 v[156:159], a[88:91], a[56:59], v[156:159]\n" \
"buffer_load_dwordx4 v214, %[srdk], s48 offen offset:3072 lds\n" \
"v_mfma_f32_16x16x32_bf16 v[156:159], a[92:95], a[60:63], v[156:159]\n" \
"v_exp_f32_e32 v151, v151\n" \
"v_mfma_f32_16x16x32_bf16 v[16:19], a[176:179], v[192:195], v[16:19]\n" \
"ds_read_b128 a[120:123], v209 offset:61440\n" \
"v_mfma_f32_16x16x32_bf16 v[16:19], a[144:147], v[184:187], v[16:19]\n" \
"v_exp_f32_e64 v152, v152\n" \
"v_mfma_f32_16x16x32_bf16 v[20:23], a[180:183], v[192:195], v[20:23]\n" \
"v_cvt_pk_bf16_f32 v218, v148, v149\n" \
"v_cvt_pk_bf16_f32 v219, v150, v151\n" \
"v_mfma_f32_16x16x32_bf16 v[20:23], a[148:151], v[184:187], v[20:23]\n" \
"v_exp_f32_e64 v153, v153\n" \
"v_mfma_f32_16x16x32_bf16 v[24:27], a[184:187], v[192:195], v[24:27]\n" \
"v_exp_f32_e64 v154, v154\n" \
"v_mfma_f32_16x16x32_bf16 v[24:27], a[152:155], v[184:187], v[24:27]\n" \
"ds_read_b128 a[124:127], v210 offset:61440\n" \
"s_add_u32 s48, s48, 0x4000\n" \
"s_mov_b32 m0, s44\n" \
"s_addk_i32 s44, 0x400\n" \
"buffer_load_dwordx4 v229, s[64:67], s45 offen offset:0 lds\n" \
"s_add_u32 s45, s45, 229376\n" \
"v_mfma_f32_16x16x32_bf16 v[28:31], a[188:191], v[192:195], v[28:31]\n" \
"v_exp_f32_e64 v155, v155\n" \
"v_mfma_f32_16x16x32_bf16 v[28:31], a[156:159], v[184:187], v[28:31]\n" \
"s_waitcnt vmcnt(16) lgkmcnt(2)\n" \
"s_barrier\n" \
"v_mfma_f32_16x16x32_bf16 v[128:131], a[96:99], a[0:3], v[160:163]\n" \
"v_exp_f32_e64 v156, v156\n" \
"v_mfma_f32_16x16x32_bf16 v[128:131], a[100:103], a[4:7], v[128:131]\n" \
"s_add_u32 m0, %[m0b], 98304\n" \
"v_mfma_f32_16x16x32_bf16 v[128:131], a[104:107], a[8:11], v[128:131]\n" \
"v_exp_f32_e64 v157, v157\n" \
"v_mfma_f32_16x16x32_bf16 v[128:131], a[108:111], a[12:15], v[128:131]\n" \
"v_cvt_pk_bf16_f32 v220, v152, v153\n" \
"v_cvt_pk_bf16_f32 v221, v154, v155\n" \
"v_mfma_f32_16x16x32_bf16 v[132:135], a[112:115], a[0:3], v[160:163]\n" \
"v_exp_f32_e64 v158, v158\n" \
"v_mfma_f32_16x16x32_bf16 v[132:135], a[116:119], a[4:7], v[132:135]\n" \
"s_waitcnt lgkmcnt(0)\n" \
"v_mfma_f32_16x16x32_bf16 v[132:135], a[120:123], a[8:11], v[132:135]\n" \
"buffer_load_dwordx4 %[vd0], %[srdv], s48 offen offset:0 lds\n" \
"v_mfma_f32_16x16x32_bf16 v[132:135], a[124:127], a[12:15], v[132:135]\n" \
"v_exp_f32_e32 v159, v159\n" \
"v_mfma_f32_16x16x32_bf16 v[32:35], a[160:163], v[196:199], v[32:35]\n" \
"ds_read_b128 a[64:67], %[kb0] offset:0\n" \
"v_mfma_f32_16x16x32_bf16 v[32:35], a[128:131], v[188:191], v[32:35]\n" \
"v_exp_f32_e64 v128, v128\n" \
"v_mfma_f32_16x16x32_bf16 v[36:39], a[164:167], v[196:199], v[36:39]\n" \
"v_cvt_pk_bf16_f32 v222, v156, v157\n" \
"v_cvt_pk_bf16_f32 v223, v158, v159\n" \
"v_mfma_f32_16x16x32_bf16 v[36:39], a[132:135], v[188:191], v[36:39]\n" \
"v_exp_f32_e64 v129, v129\n" \
"v_mfma_f32_16x16x32_bf16 v[40:43], a[168:171], v[196:199], v[40:43]\n" \
"ds_read_b128 a[68:71], v208 offset:0\n" \
"v_mfma_f32_16x16x32_bf16 v[40:43], a[136:139], v[188:191], v[40:43]\n" \
"v_exp_f32_e64 v130, v130\n" \
"v_mfma_f32_16x16x32_bf16 v[44:47], a[172:175], v[196:199], v[44:47]\n" \
"ds_read_b128 a[72:75], v209 offset:0\n" \
"v_mfma_f32_16x16x32_bf16 v[44:47], a[140:143], v[188:191], v[44:47]\n" \
"v_exp_f32_e64 v131, v131\n" \
"v_smfmac_f32_16x16x64_bf16 v[176:179], a[192:195], v[184:191], v228\n" \
"buffer_store_dwordx4 a[196:199], v235, %[srdz], s63 offen offset:2048 sc1\n" \
"s_add_u32 s63, s63, 3072\n" \
"v_mfma_f32_16x16x32_bf16 v[136:139], a[96:99], a[16:19], v[164:167]\n" \
"v_exp_f32_e64 v132, v132\n" \
"v_mfma_f32_16x16x32_bf16 v[136:139], a[100:103], a[20:23], v[136:139]\n" \
"ds_read_b128 a[76:79], v210 offset:0\n" \
"v_mfma_f32_16x16x32_bf16 v[136:139], a[104:107], a[24:27], v[136:139]\n" \
"v_exp_f32_e64 v133, v133\n" \
"v_mfma_f32_16x16x32_bf16 v[136:139], a[108:111], a[28:31], v[136:139]\n" \
"v_cvt_pk_bf16_f32 v192, v128, v129\n" \
"v_cvt_pk_bf16_f32 v193, v130, v131\n" \
"v_mfma_f32_16x16x32_bf16 v[140:143], a[112:115], a[16:19], v[164:167]\n" \
"v_exp_f32_e64 v134, v134\n" \
"v_mfma_f32_16x16x32_bf16 v[140:143], a[116:119], a[20:23], v[140:143]\n" \
"ds_read_b128 a[80:83], %[kb0] offset:4096\n" \
"v_mfma_f32_16x16x32_bf16 v[140:143], a[120:123], a[24:27], v[140:143]\n" \
"buffer_load_dwordx4 v215, %[srdv], s48 offen offset:1024 lds\n" \
"v_mfma_f32_16x16x32_bf16 v[140:143], a[124:127], a[28:31], v[140:143]\n" \
"v_exp_f32_e64 v135, v135\n" \
"v_mfma_f32_16x16x32_bf16 v[48:51], a[176:179], v[196:199], v[48:51]\n" \
"ds_read_b128 a[84:87], v208 offset:4096\n" \
"v_mfma_f32_16x16x32_bf16 v[48:51], a[144:147], v[188:191], v[48:51]\n" \
"v_exp_f32_e64 v136, v136\n" \
"v_mfma_f32_16x16x32_bf16 v[52:55], a[180:183], v[196:199], v[52:55]\n" \
"v_cvt_pk_bf16_f32 v194, v132, v133\n" \
"v_cvt_pk_bf16_f32 v195, v134, v135\n" \
"v_mfma_f32_16x16x32_bf16 v[52:55], a[148:151], v[188:191], v[52:55]\n" \
"v_exp_f32_e64 v137, v137\n" \
"v_mfma_f32_16x16x32_bf16 v[56:59], a[184:187], v[196:199], v[56:59]\n" \
"v_exp_f32_e64 v138, v138\n" \
"v_mfma_f32_16x16x32_bf16 v[56:59], a[152:155], v[188:191], v[56:59]\n" \
"ds_read_b128 a[88:91], v209 offset:4096\n" \
"v_mfma_f32_16x16x32_bf16 v[60:63], a[188:191], v[196:199], v[60:63]\n" \
"v_exp_f32_e64 v139, v139\n" \
"v_mfma_f32_16x16x32_bf16 v[60:63], a[156:159], v[188:191], v[60:63]\n" \
"s_cmp_lg_u32 %[wid], 2\n" \
"s_cbranch_scc1 LtB7_%=\n" \
"global_load_dword v237, v236, s[58:59]\n" \
"s_add_u32 s61, s48, 49152\n" \
"s_mov_b32 exec_hi, 0\n" \
"s_nop 0\n" \
"buffer_load_dword v237, v224, %[srdp], s61 offen\n" \
"s_mov_b32 exec_hi, -1\n" \
"s_nop 0\n" \
"LtB7_%=:\n" \
"v_mfma_f32_16x16x32_bf16 v[144:147], a[96:99], a[32:35], v[168:171]\n" \
"v_exp_f32_e64 v140, v140\n" \
"v_mfma_f32_16x16x32_bf16 v[144:147], a[100:103], a[36:39], v[144:147]\n" \
"ds_read_b128 a[92:95], v210 offset:4096\n" \
"v_mfma_f32_16x16x32_bf16 v[144:147], a[104:107], a[40:43], v[144:147]\n" \
"v_exp_f32_e64 v141, v141\n" \
"v_mfma_f32_16x16x32_bf16 v[144:147], a[108:111], a[44:47], v[144:147]\n" \
"v_cvt_pk_bf16_f32 v196, v136, v137\n" \
"v_cvt_pk_bf16_f32 v197, v138, v139\n" \
"v_mfma_f32_16x16x32_bf16 v[148:151], a[112:115], a[32:35], v[168:171]\n" \
"v_exp_f32_e64 v142, v142\n" \
"v_mfma_f32_16x16x32_bf16 v[148:151], a[116:119], a[36:39], v[148:151]\n" \
"v_mfma_f32_16x16x32_bf16 v[148:151], a[120:123], a[40:43], v[148:151]\n" \
"buffer_load_dwordx4 %[vd0], %[srdv], s48 offen offset:2048 lds\n" \
"v_mfma_f32_16x16x32_bf16 v[148:151], a[124:127], a[44:47], v[148:151]\n" \
"v_exp_f32_e64 v143, v143\n" \
"v_mfma_f32_16x16x32_bf16 v[96:99], a[160:163], v[204:207], v[96:99]\n" \
"ds_read_b64_tr_b16 a[160:161], %[vvb] offset:57344\n" \
"v_mfma_f32_16x16x32_bf16 v[96:99], a[128:131], v[220:223], v[96:99]\n" \
"v_exp_f32_e64 v144, v144\n" \
"v_mfma_f32_16x16x32_bf16 v[100:103], a[164:167], v[204:207], v[100:103]\n" \
"v_cvt_pk_bf16_f32 v198, v140, v141\n" \
"v_cvt_pk_bf16_f32 v199, v142, v143\n" \
"v_mfma_f32_16x16x32_bf16 v[100:103], a[132:135], v[220:223], v[100:103]\n" \
"v_exp_f32_e64 v145, v145\n" \
"v_mfma_f32_16x16x32_bf16 v[104:107], a[168:171], v[204:207], v[104:107]\n" \
"ds_read_b64_tr_b16 a[162:163], %[vvb] offset:61440\n" \
"ds_read_b64_tr_b16 a[164:165], v211 offset:57344\n" \
"v_mfma_f32_16x16x32_bf16 v[104:107], a[136:139], v[220:223], v[104:107]\n" \
"v_exp_f32_e64 v146, v146\n" \
"v_mfma_f32_16x16x32_bf16 v[108:111], a[172:175], v[204:207], v[108:111]\n" \
"ds_read_b64_tr_b16 a[166:167], v211 offset:61440\n" \
"v_mfma_f32_16x16x32_bf16 v[108:111], a[140:143], v[220:223], v[108:111]\n" \
"v_exp_f32_e64 v147, v147\n" \
"v_smfmac_f32_16x16x64_bf16 v[180:183], a[192:195], v[216:223], v228\n" \
"ds_read_b64_tr_b16 a[168:169], %[vvb] offset:57856\n" \
"v_mfma_f32_16x16x32_bf16 v[152:155], a[96:99], a[48:51], v[172:175]\n" \
"v_exp_f32_e64 v148, v148\n" \
"v_mfma_f32_16x16x32_bf16 v[152:155], a[100:103], a[52:55], v[152:155]\n" \
"ds_read_b64_tr_b16 a[170:171], %[vvb] offset:61952\n" \
"v_mfma_f32_16x16x32_bf16 v[152:155], a[104:107], a[56:59], v[152:155]\n" \
"v_exp_f32_e64 v149, v149\n" \
"v_mfma_f32_16x16x32_bf16 v[152:155], a[108:111], a[60:63], v[152:155]\n" \
"v_cvt_pk_bf16_f32 v200, v144, v145\n" \
"v_cvt_pk_bf16_f32 v201, v146, v147\n" \
"v_mfma_f32_16x16x32_bf16 v[156:159], a[112:115], a[48:51], v[172:175]\n" \
".if . - adaln_body != 28672\n" \
".error \"size model\"\n" \
".endif\n" \
"Lk6_%=:\n" \
"s_cbranch_execz Lk7_%=\n" \
"s_nop 0\n" \
"v_exp_f32_e64 v150, v150\n" \
"v_mfma_f32_16x16x32_bf16 v[156:159], a[116:119], a[52:55], v[156:159]\n" \
"ds_read_b64_tr_b16 a[172:173], v211 offset:57856\n" \
"v_mfma_f32_16x16x32_bf16 v[156:159], a[120:123], a[56:59], v[156:159]\n" \
"buffer_load_dwordx4 v215, %[srdv], s48 offen offset:3072 lds\n" \
"v_mfma_f32_16x16x32_bf16 v[156:159], a[124:127], a[60:63], v[156:159]\n" \
"v_exp_f32_e64 v151, v151\n" \
"v_mfma_f32_16x16x32_bf16 v[112:115], a[176:179], v[204:207], v[112:115]\n" \
"ds_read_b64_tr_b16 a[174:175], v211 offset:61952\n" \
"ds_read_b64_tr_b16 a[176:177], %[vvb] offset:58368\n" \
"v_mfma_f32_16x16x32_bf16 v[112:115], a[144:147], v[220:223], v[112:115]\n" \
"v_exp_f32_e64 v152, v152\n" \
"v_mfma_f32_16x16x32_bf16 v[116:119], a[180:183], v[204:207], v[116:119]\n" \
"v_cvt_pk_bf16_f32 v202, v148, v149\n" \
"v_cvt_pk_bf16_f32 v203, v150, v151\n" \
"v_mfma_f32_16x16x32_bf16 v[116:119], a[148:151], v[220:223], v[116:119]\n" \
"v_exp_f32_e64 v153, v153\n" \
"v_mfma_f32_16x16x32_bf16 v[120:123], a[184:187], v[204:207], v[120:123]\n" \
"v_exp_f32_e64 v154, v154\n" \
"v_mfma_f32_16x16x32_bf16 v[120:123], a[152:155], v[220:223], v[120:123]\n" \
"ds_read_b64_tr_b16 a[178:179], %[vvb] offset:62464\n" \
"v_mfma_f32_16x16x32_bf16 v[124:127], a[188:191], v[204:207], v[124:127]\n" \
"v_exp_f32_e64 v155, v155\n" \
"v_mfma_f32_16x16x32_bf16 v[124:127], a[156:159], v[220:223], v[124:127]\n" \
"ds_read_b64_tr_b16 a[180:181], v211 offset:58368\n" \
"s_waitcnt lgkmcnt(13)\n" \
"v_mfma_f32_16x16x32_bf16 v[128:131], a[64:67], a[0:3], v[160:163]\n" \
"v_exp_f32_e32 v156, v156\n" \
"v_mfma_f32_16x16x32_bf16 v[128:131], a[68:71], a[4:7], v[128:131]\n" \
"ds_read_b64_tr_b16 a[182:183], v211 offset:62464\n" \
"s_add_u32 m0, %[m0b], 49152\n" \
"v_mfma_f32_16x16x32_bf16 v[128:131], a[72:75], a[8:11], v[128:131]\n" \
"v_exp_f32_e64 v157, v157\n" \
"v_mfma_f32_16x16x32_bf16 v[128:131], a[76:79], a[12:15], v[128:131]\n" \
"v_cvt_pk_bf16_f32 v204, v152, v153\n" \
"v_cvt_pk_bf16_f32 v205, v154, v155\n" \
"v_mfma_f32_16x16x32_bf16 v[132:135], a[80:83], a[0:3], v[160:163]\n" \
"v_exp_f32_e64 v158, v158\n" \
"v_mfma_f32_16x16x32_bf16 v[132:135], a[84:87], a[4:7], v[132:135]\n" \
"ds_read_b64_tr_b16 a[184:185], %[vvb] offset:58880\n" \
"s_waitcnt lgkmcnt(5)\n" \
"v_mfma_f32_16x16x32_bf16 v[132:135], a[88:91], a[8:11], v[132:135]\n" \
"buffer_load_dwordx4 %[kd0], %[srdk], s48 offen offset:0 lds\n" \
"v_mfma_f32_16x16x32_bf16 v[132:135], a[92:95], a[12:15], v[132:135]\n" \
"v_exp_f32_e32 v159, v159\n" \
"v_mfma_f32_16x16x32_bf16 v[64:67], a[128:131], v[216:219], v[64:67]\n" \
"ds_read_b64_tr_b16 a[186:187], %[vvb] offset:62976\n" \
"ds_read_b64_tr_b16 a[188:189], v211 offset:58880\n" \
"v_mfma_f32_16x16x32_bf16 v[64:67], a[160:163], v[200:203], v[64:67]\n" \
"v_exp_f32_e64 v128, v128\n" \
"v_mfma_f32_16x16x32_bf16 v[68:71], a[132:135], v[216:219], v[68:71]\n" \
"v_cvt_pk_bf16_f32 v206, v156, v157\n" \
"v_cvt_pk_bf16_f32 v207, v158, v159\n" \
"v_mfma_f32_16x16x32_bf16 v[68:71], a[164:167], v[200:203], v[68:71]\n" \
"v_exp_f32_e64 v129, v129\n" \
"v_mfma_f32_16x16x32_bf16 v[72:75], a[136:139], v[216:219], v[72:75]\n" \
"ds_read_b64_tr_b16 a[190:191], v211 offset:62976\n" \
"ds_read_b64_tr_b16 a[128:129], %[vvb] offset:0\n" \
"v_mfma_f32_16x16x32_bf16 v[72:75], a[168:171], v[200:203], v[72:75]\n" \
"v_exp_f32_e64 v130, v130\n" \
"v_mfma_f32_16x16x32_bf16 v[76:79], a[140:143], v[216:219], v[76:79]\n" \
"ds_read_b64_tr_b16 a[130:131], %[vvb] offset:4096\n" \
"ds_read_b64_tr_b16 a[132:133], v211 offset:0\n" \
"v_mfma_f32_16x16x32_bf16 v[76:79], a[172:175], v[200:203], v[76:79]\n" \
"v_exp_f32_e64 v131, v131\n" \
"v_smfmac_f32_16x16x64_bf16 v[176:179], a[192:195], v[192:199], v228\n" \
"ds_read_b64_tr_b16 a[134:135], v211 offset:4096\n" \
"ds_read_b64_tr_b16 a[136:137], %[vvb] offset:512\n" \
"v_mfma_f32_16x16x32_bf16 v[136:139], a[64:67], a[16:19], v[164:167]\n" \
"v_exp_f32_e64 v132, v132\n" \
"v_mfma_f32_16x16x32_bf16 v[136:139], a[68:71], a[20:23], v[136:139]\n" \
"ds_read_b64_tr_b16 a[138:139], %[vvb] offset:4608\n" \
"ds_read_b64_tr_b16 a[140:141], v211 offset:512\n" \
"v_mfma_f32_16x16x32_bf16 v[136:139], a[72:75], a[24:27], v[136:139]\n" \
"v_exp_f32_e64 v133, v133\n" \
"v_mfma_f32_16x16x32_bf16 v[136:139], a[76:79], a[28:31], v[136:139]\n" \
"v_cvt_pk_bf16_f32 v184, v128, v129\n" \
"v_cvt_pk_bf16_f32 v185, v130, v131\n" \
"v_mfma_f32_16x16x32_bf16 v[140:143], a[80:83], a[16:19], v[164:167]\n" \
"v_exp_f32_e64 v134, v134\n" \
"v_mfma_f32_16x16x32_bf16 v[140:143], a[84:87], a[20:23], v[140:143]\n" \
"ds_read_b64_tr_b16 a[142:143], v211 offset:4608\n" \
"s_waitcnt lgkmcnt(8)\n" \
"v_mfma_f32_16x16x32_bf16 v[140:143], a[88:91], a[24:27], v[140:143]\n" \
"buffer_load_dwordx4 v212, %[srdk], s48 offen offset:1024 lds\n" \
"v_mfma_f32_16x16x32_bf16 v[140:143], a[92:95], a[28:31], v[140:143]\n" \
"v_exp_f32_e32 v135, v135\n" \
"v_mfma_f32_16x16x32_bf16 v[80:83], a[144:147], v[216:219], v[80:83]\n" \
"ds_read_b64_tr_b16 a[144:145], %[vvb] offset:1024\n" \
"ds_read_b64_tr_b16 a[146:147], %[vvb] offset:5120\n" \
"v_mfma_f32_16x16x32_bf16 v[80:83], a[176:179], v[200:203], v[80:83]\n" \
"v_exp_f32_e64 v136, v136\n" \
"v_mfma_f32_16x16x32_bf16 v[84:87], a[148:151], v[216:219], v[84:87]\n" \
"v_cvt_pk_bf16_f32 v186, v132, v133\n" \
"v_cvt_pk_bf16_f32 v187, v134, v135\n" \
"v_mfma_f32_16x16x32_bf16 v[84:87], a[180:183], v[200:203], v[84:87]\n" \
"v_exp_f32_e64 v137, v137\n" \
"v_mfma_f32_16x16x32_bf16 v[88:91], a[152:155], v[216:219], v[88:91]\n" \
"v_exp_f32_e64 v138, v138\n" \
"v_mfma_f32_16x16x32_bf16 v[88:91], a[184:187], v[200:203], v[88:91]\n" \
"ds_read_b64_tr_b16 a[148:149], v211 offset:1024\n" \
"ds_read_b64_tr_b16 a[150:151], v211 offset:5120\n" \
"v_mfma_f32_16x16x32_bf16 v[92:95], a[156:159], v[216:219], v[92:95]\n" \
"v_exp_f32_e64 v139, v139\n" \
"v_mfma_f32_16x16x32_bf16 v[92:95], a[188:191], v[200:203], v[92:95]\n" \
"ds_read_b64_tr_b16 a[152:153], %[vvb] offset:1536\n" \
"buffer_store_dwordx4 a[196:199], v235, %[srdz], s62 offen offset:2048 sc1\n" \
"s_add_u32 s62, s62, 3072\n" \
"v_mfma_f32_16x16x32_bf16 v[144:147], a[64:67], a[32:35], v[168:171]\n" \
"v_exp_f32_e64 v140, v140\n" \
"v_mfma_f32_16x16x32_bf16 v[144:147], a[68:71], a[36:39], v[144:147]\n" \
"ds_read_b64_tr_b16 a[154:155], %[vvb] offset:5632\n" \
"ds_read_b64_tr_b16 a[156:157], v211 offset:1536\n" \
"v_mfma_f32_16x16x32_bf16 v[144:147], a[72:75], a[40:43], v[144:147]\n" \
"v_exp_f32_e64 v141, v141\n" \
"v_mfma_f32_16x16x32_bf16 v[144:147], a[76:79], a[44:47], v[144:147]\n" \
"v_cvt_pk_bf16_f32 v188, v136, v137\n" \
"v_cvt_pk_bf16_f32 v189, v138, v139\n" \
"v_mfma_f32_16x16x32_bf16 v[148:151], a[80:83], a[32:35], v[168:171]\n" \
"v_exp_f32_e64 v142, v142\n" \
"v_mfma_f32_16x16x32_bf16 v[148:151], a[84:87], a[36:39], v[148:151]\n" \
"ds_read_b64_tr_b16 a[158:159], v211 offset:5632\n" \
"s_waitcnt lgkmcnt(8)\n" \
"v_mfma_f32_16x16x32_bf16 v[148:151], a[88:91], a[40:43], v[148:151]\n" \
"buffer_load_dwordx4 v213, %[srdk], s48 offen offset:2048 lds\n" \
"v_mfma_f32_16x16x32_bf16 v[148:151], a[92:95], a[44:47], v[148:151]\n" \
"v_exp_f32_e32 v143, v143\n" \
"v_mfma_f32_16x16x32_bf16 v[0:3], a[160:163], v[192:195], v[0:3]\n" \
"ds_read_b128 a[96:99], %[kb0] offset:8192\n" \
"v_mfma_f32_16x16x32_bf16 v[0:3], a[128:131], v[184:187], v[0:3]\n" \
"v_exp_f32_e64 v144, v144\n" \
"v_mfma_f32_16x16x32_bf16 v[4:7], a[164:167], v[192:195], v[4:7]\n" \
"v_cvt_pk_bf16_f32 v190, v140, v141\n" \
"v_cvt_pk_bf16_f32 v191, v142, v143\n" \
"v_mfma_f32_16x16x32_bf16 v[4:7], a[132:135], v[184:187], v[4:7]\n" \
"v_exp_f32_e64 v145, v145\n" \
"v_mfma_f32_16x16x32_bf16 v[8:11], a[168:171], v[192:195], v[8:11]\n" \
"ds_read_b128 a[100:103], v208 offset:8192\n" \
"v_mfma_f32_16x16x32_bf16 v[8:11], a[136:139], v[184:187], v[8:11]\n" \
"v_exp_f32_e64 v146, v146\n" \
"v_mfma_f32_16x16x32_bf16 v[12:15], a[172:175], v[192:195], v[12:15]\n" \
"ds_read_b128 a[104:107], v209 offset:8192\n" \
"v_mfma_f32_16x16x32_bf16 v[12:15], a[140:143], v[184:187], v[12:15]\n" \
"v_exp_f32_e64 v147, v147\n" \
"v_smfmac_f32_16x16x64_bf16 v[180:183], a[192:195], v[200:207], v228\n" \
"ds_read_b128 a[108:111], v210 offset:8192\n" \
"v_mfma_f32_16x16x32_bf16 v[152:155], a[64:67], a[48:51], v[172:175]\n" \
"v_exp_f32_e64 v148, v148\n" \
"v_mfma_f32_16x16x32_bf16 v[152:155], a[68:71], a[52:55], v[152:155]\n" \
"ds_read_b128 a[112:115], %[kb0] offset:12288\n" \
"v_mfma_f32_16x16x32_bf16 v[152:155], a[72:75], a[56:59], v[152:155]\n" \
"v_exp_f32_e64 v149, v149\n" \
"v_mfma_f32_16x16x32_bf16 v[152:155], a[76:79], a[60:63], v[152:155]\n" \
"v_cvt_pk_bf16_f32 v216, v144, v145\n" \
"v_cvt_pk_bf16_f32 v217, v146, v147\n" \
"v_mfma_f32_16x16x32_bf16 v[156:159], a[80:83], a[48:51], v[172:175]\n" \
"v_exp_f32_e64 v150, v150\n" \
"v_mfma_f32_16x16x32_bf16 v[156:159], a[84:87], a[52:55], v[156:159]\n" \
"ds_read_b128 a[116:119], v208 offset:12288\n" \
"s_waitcnt lgkmcnt(6)\n" \
"v_mfma_f32_16x16x32_bf16 v[156:159], a[88:91], a[56:59], v[156:159]\n" \
"buffer_load_dwordx4 v214, %[srdk], s48 offen offset:3072 lds\n" \
"v_mfma_f32_16x16x32_bf16 v[156:159], a[92:95], a[60:63], v[156:159]\n" \
"v_exp_f32_e32 v151, v151\n" \
"v_mfma_f32_16x16x32_bf16 v[16:19], a[176:179], v[192:195], v[16:19]\n" \
"ds_read_b128 a[120:123], v209 offset:12288\n" \
"v_mfma_f32_16x16x32_bf16 v[16:19], a[144:147], v[184:187], v[16:19]\n" \
"v_exp_f32_e64 v152, v152\n" \
"v_mfma_f32_16x16x32_bf16 v[20:23], a[180:183], v[192:195], v[20:23]\n" \
"v_cvt_pk_bf16_f32 v218, v148, v149\n" \
"v_cvt_pk_bf16_f32 v219, v150, v151\n" \
"v_mfma_f32_16x16x32_bf16 v[20:23], a[148:151], v[184:187], v[20:23]\n" \
"v_exp_f32_e64 v153, v153\n" \
"v_mfma_f32_16x16x32_bf16 v[24:27], a[184:187], v[192:195], v[24:27]\n" \
"v_exp_f32_e64 v154, v154\n" \
"v_mfma_f32_16x16x32_bf16 v[24:27], a[152:155], v[184:187], v[24:27]\n" \
"ds_read_b128 a[124:127], v210 offset:12288\n" \
"s_add_u32 s48, s48, 0x4000\n" \
"v_mfma_f32_16x16x32_bf16 v[28:31], a[188:191], v[192:195], v[28:31]\n" \
"v_exp_f32_e64 v155, v155\n" \
"v_mfma_f32_16x16x32_bf16 v[28:31], a[156:159], v[184:187], v[28:31]\n" \
"s_waitcnt vmcnt(17) lgkmcnt(2)\n" \
"s_barrier\n" \
"v_mfma_f32_16x16x32_bf16 v[128:131], a[96:99], a[0:3], v[160:163]\n" \
"v_exp_f32_e64 v156, v156\n" \
"v_mfma_f32_16x16x32_bf16 v[128:131], a[100:103], a[4:7], v[128:131]\n" \
"s_add_u32 m0, %[m0b], 114688\n" \
"v_mfma_f32_16x16x32_bf16 v[128:131], a[104:107], a[8:11], v[128:131]\n" \
"v_exp_f32_e64 v157, v157\n" \
"v_mfma_f32_16x16x32_bf16 v[128:131], a[108:111], a[12:15], v[128:131]\n" \
"v_cvt_pk_bf16_f32 v220, v152, v153\n" \
"v_cvt_pk_bf16_f32 v221, v154, v155\n" \
"v_mfma_f32_16x16x32_bf16 v[132:135], a[112:115], a[0:3], v[160:163]\n" \
"v_exp_f32_e64 v158, v158\n" \
"v_mfma_f32_16x16x32_bf16 v[132:135], a[116:119], a[4:7], v[132:135]\n" \
"s_waitcnt lgkmcnt(0)\n" \
"v_mfma_f32_16x16x32_bf16 v[132:135], a[120:123], a[8:11], v[132:135]\n" \
"buffer_load_dwordx4 %[vd0], %[srdv], s48 offen offset:0 lds\n" \
"v_mfma_f32_16x16x32_bf16 v[132:135], a[124:127], a[12:15], v[132:135]\n" \
"v_exp_f32_e32 v159, v159\n" \
"v_mfma_f32_16x16x32_bf16 v[32:35], a[160:163], v[196:199], v[32:35]\n" \
"ds_read_b128 a[64:67], %[kb0] offset:16384\n" \
"v_mfma_f32_16x16x32_bf16 v[32:35], a[128:131], v[188:191], v[32:35]\n" \
"v_exp_f32_e64 v128, v128\n" \
"v_mfma_f32_16x16x32_bf16 v[36:39], a[164:167], v[196:199], v[36:39]\n" \
"v_cvt_pk_bf16_f32 v222, v156, v157\n" \
"v_cvt_pk_bf16_f32 v223, v158, v159\n" \
"v_mfma_f32_16x16x32_bf16 v[36:39], a[132:135], v[188:191], v[36:39]\n" \
"v_exp_f32_e64 v129, v129\n" \
"v_mfma_f32_16x16x32_bf16 v[40:43], a[168:171], v[196:199], v[40:43]\n" \
"ds_read_b128 a[68:71], v208 offset:16384\n" \
"v_mfma_f32_16x16x32_bf16 v[40:43], a[136:139], v[188:191], v[40:43]\n" \
"v_exp_f32_e64 v130, v130\n" \
"v_mfma_f32_16x16x32_bf16 v[44:47], a[172:175], v[196:199], v[44:47]\n" \
"ds_read_b128 a[72:75], v209 offset:16384\n" \
"v_mfma_f32_16x16x32_bf16 v[44:47], a[140:143], v[188:191], v[44:47]\n" \
"v_exp_f32_e64 v131, v131\n" \
"v_smfmac_f32_16x16x64_bf16 v[176:179], a[192:195], v[184:191], v228\n" \
"v_mfma_f32_16x16x32_bf16 v[136:139], a[96:99], a[16:19], v[164:167]\n" \
"v_exp_f32_e64 v132, v132\n" \
"v_mfma_f32_16x16x32_bf16 v[136:139], a[100:103], a[20:23], v[136:139]\n" \
"ds_read_b128 a[76:79], v210 offset:16384\n" \
"v_mfma_f32_16x16x32_bf16 v[136:139], a[104:107], a[24:27], v[136:139]\n" \
"v_exp_f32_e64 v133, v133\n" \
"v_mfma_f32_16x16x32_bf16 v[136:139], a[108:111], a[28:31], v[136:139]\n" \
"v_cvt_pk_bf16_f32 v192, v128, v129\n" \
"v_cvt_pk_bf16_f32 v193, v130, v131\n" \
"v_mfma_f32_16x16x32_bf16 v[140:143], a[112:115], a[16:19], v[164:167]\n" \
"v_exp_f32_e64 v134, v134\n" \
"v_mfma_f32_16x16x32_bf16 v[140:143], a[116:119], a[20:23], v[140:143]\n" \
"ds_read_b128 a[80:83], %[kb0] offset:20480\n" \
"v_mfma_f32_16x16x32_bf16 v[140:143], a[120:123], a[24:27], v[140:143]\n" \
"buffer_load_dwordx4 v215, %[srdv], s48 offen offset:1024 lds\n" \
"v_mfma_f32_16x16x32_bf16 v[140:143], a[124:127], a[28:31], v[140:143]\n" \
"v_exp_f32_e64 v135, v135\n" \
"v_mfma_f32_16x16x32_bf16 v[48:51], a[176:179], v[196:199], v[48:51]\n" \
"ds_read_b128 a[84:87], v208 offset:20480\n" \
"v_mfma_f32_16x16x32_bf16 v[48:51], a[144:147], v[188:191], v[48:51]\n" \
"v_exp_f32_e64 v136, v136\n" \
"v_mfma_f32_16x16x32_bf16 v[52:55], a[180:183], v[196:199], v[52:55]\n" \
"v_cvt_pk_bf16_f32 v194, v132, v133\n" \
"v_cvt_pk_bf16_f32 v195, v134, v135\n" \
"v_mfma_f32_16x16x32_bf16 v[52:55], a[148:151], v[188:191], v[52:55]\n" \
"v_exp_f32_e64 v137, v137\n" \
"v_mfma_f32_16x16x32_bf16 v[56:59], a[184:187], v[196:199], v[56:59]\n" \
"v_exp_f32_e64 v138, v138\n" \
"v_mfma_f32_16x16x32_bf16 v[56:59], a[152:155], v[188:191], v[56:59]\n" \
"ds_read_b128 a[88:91], v209 offset:20480\n" \
"v_mfma_f32_16x16x32_bf16 v[60:63], a[188:191], v[196:199], v[60:63]\n" \
"v_exp_f32_e64 v139, v139\n" \
"v_mfma_f32_16x16x32_bf16 v[60:63], a[156:159], v[188:191], v[60:63]\n" \
"s_cmp_lg_u32 %[wid], 3\n" \
"s_cbranch_scc1 LtB1_%=\n" \
"global_load_dword v237, v236, s[58:59]\n" \
"s_add_u32 s61, s48, 49152\n" \
"s_mov_b32 exec_hi, 0\n" \
"s_nop 0\n" \
"buffer_load_dword v237, v224, %[srdp], s61 offen\n" \
"s_mov_b32 exec_hi, -1\n" \
"s_nop 0\n" \
"LtB1_%=:\n" \
"v_mfma_f32_16x16x32_bf16 v[144:147], a[96:99], a[32:35], v[168:171]\n" \
"v_exp_f32_e64 v140, v140\n" \
"v_mfma_f32_16x16x32_bf16 v[144:147], a[100:103], a[36:39], v[144:147]\n" \
"ds_read_b128 a[92:95], v210 offset:20480\n" \
"v_mfma_f32_16x16x32_bf16 v[144:147], a[104:107], a[40:43], v[144:147]\n" \
"v_exp_f32_e64 v141, v141\n" \
"v_mfma_f32_16x16x32_bf16 v[144:147], a[108:111], a[44:47], v[144:147]\n" \
"v_cvt_pk_bf16_f32 v196, v136, v137\n" \
"v_cvt_pk_bf16_f32 v197, v138, v139\n" \
"v_mfma_f32_16x16x32_bf16 v[148:151], a[112:115], a[32:35], v[168:171]\n" \
"v_exp_f32_e64 v142, v142\n" \
"v_mfma_f32_16x16x32_bf16 v[148:151], a[116:119], a[36:39], v[148:151]\n" \
"v_mfma_f32_16x16x32_bf16 v[148:151], a[120:123], a[40:43], v[148:151]\n" \
"buffer_load_dwordx4 %[vd0], %[srdv], s48 offen offset:2048 lds\n" \
"v_mfma_f32_16x16x32_bf16 v[148:151], a[124:127], a[44:47], v[148:151]\n" \
"v_exp_f32_e64 v143, v143\n" \
"v_mfma_f32_16x16x32_bf16 v[96:99], a[160:163], v[204:207], v[96:99]\n" \
"ds_read_b64_tr_b16 a[160:161], %[vvb] offset:8192\n" \
"v_mfma_f32_16x16x32_bf16 v[96:99], a[128:131], v[220:223], v[96:99]\n" \
"v_exp_f32_e64 v144, v144\n" \
"v_mfma_f32_16x16x32_bf16 v[100:103], a[164:167], v[204:207], v[100:103]\n" \
"v_cvt_pk_bf16_f32 v198, v140, v141\n" \
"v_cvt_pk_bf16_f32 v199, v142, v143\n" \
"v_mfma_f32_16x16x32_bf16 v[100:103], a[132:135], v[220:223], v[100:103]\n" \
"v_exp_f32_e64 v145, v145\n" \
"v_mfma_f32_16x16x32_bf16 v[104:107], a[168:171], v[204:207], v[104:107]\n" \
"ds_read_b64_tr_b16 a[162:163], %[vvb] offset:12288\n" \
"ds_read_b64_tr_b16 a[164:165], v211 offset:8192\n" \
"v_mfma_f32_16x16x32_bf16 v[104:107], a[136:139], v[220:223], v[104:107]\n" \
"v_exp_f32_e64 v146, v146\n" \
"v_mfma_f32_16x16x32_bf16 v[108:111], a[172:175], v[204:207], v[108:111]\n" \
"ds_read_b64_tr_b16 a[166:167], v211 offset:12288\n" \
"v_mfma_f32_16x16x32_bf16 v[108:111], a[140:143], v[220:223], v[108:111]\n" \
"v_exp_f32_e64 v147, v147\n" \
"v_smfmac_f32_16x16x64_bf16 v[180:183], a[192:195], v[216:223], v228\n" \
"ds_read_b64_tr_b16 a[168:169], %[vvb] offset:8704\n" \
"v_mfma_f32_16x16x32_bf16 v[152:155], a[96:99], a[48:51], v[172:175]\n" \
"v_exp_f32_e64 v148, v148\n" \
"v_mfma_f32_16x16x32_bf16 v[152:155], a[100:103], a[52:55], v[152:155]\n" \
"ds_read_b64_tr_b16 a[170:171], %[vvb] offset:12800\n" \
"v_mfma_f32_16x16x32_bf16 v[152:155], a[104:107], a[56:59], v[152:155]\n" \
"v_exp_f32_e64 v149, v149\n" \
"v_mfma_f32_16x16x32_bf16 v[152:155], a[108:111], a[60:63], v[152:155]\n" \
"v_cvt_pk_bf16_f32 v200, v144, v145\n" \
"v_cvt_pk_bf16_f32 v201, v146, v147\n" \
"v_mfma_f32_16x16x32_bf16 v[156:159], a[112:115], a[48:51], v[172:175]\n" \
"v_exp_f32_e64 v150, v150\n" \
"v_mfma_f32_16x16x32_bf16 v[156:159], a[116:119], a[52:55], v[156:159]\n" \
"ds_read_b64_tr_b16 a[172:173], v211 offset:8704\n" \
"v_mfma_f32_16x16x32_bf16 v[156:159], a[120:123], a[56:59], v[156:159]\n" \
"buffer_load_dwordx4 v215, %[srdv], s48 offen offset:3072 lds\n" \
"v_mfma_f32_16x16x32_bf16 v[156:159], a[124:127], a[60:63], v[156:159]\n" \
"v_exp_f32_e64 v151, v151\n" \
"v_mfma_f32_16x16x32_bf16 v[112:115], a[176:179], v[204:207], v[112:115]\n" \
"ds_read_b64_tr_b16 a[174:175], v211 offset:12800\n" \
"ds_read_b64_tr_b16 a[176:177], %[vvb] offset:9216\n" \
"v_mfma_f32_16x16x32_bf16 v[112:115], a[144:147], v[220:223], v[112:115]\n" \
"v_exp_f32_e64 v152, v152\n" \
"v_mfma_f32_16x16x32_bf16 v[116:119], a[180:183], v[204:207], v[116:119]\n" \
"v_cvt_pk_bf16_f32 v202, v148, v149\n" \
"v_cvt_pk_bf16_f32 v203, v150, v151\n" \
"v_mfma_f32_16x16x32_bf16 v[116:119], a[148:151], v[220:223], v[116:119]\n" \
"v_exp_f32_e64 v153, v153\n" \
"v_mfma_f32_16x16x32_bf16 v[120:123], a[184:187], v[204:207], v[120:123]\n" \
"v_exp_f32_e64 v154, v154\n" \
"v_mfma_f32_16x16x32_bf16 v[120:123], a[152:155], v[220:223], v[120:123]\n" \
"ds_read_b64_tr_b16 a[178:179], %[vvb] offset:13312\n" \
"v_mfma_f32_16x16x32_bf16 v[124:127], a[188:191], v[204:207], v[124:127]\n" \
"v_exp_f32_e64 v155, v155\n" \
"v_mfma_f32_16x16x32_bf16 v[124:127], a[156:159], v[220:223], v[124:127]\n" \
"ds_read_b64_tr_b16 a[180:181], v211 offset:9216\n" \
"s_waitcnt lgkmcnt(13)\n" \
"s_cmp_lg_u32 s60, 7\n" \
"s_cbranch_scc1 LmB_%=\n" \
"s_nop 0\n" \
"v_readlane_b32 s48, %[tlo], 11\n" \
"v_readlane_b32 s58, %[tlo], 12\n" \
"v_lshlrev_b32_e64 v227, 2, v224\n" \
"v_and_b32_e32 v227, 0x1800, v227\n" \
"v_bfe_u32 v234, v224, 11, 1\n" \
"v_lshl_or_b32 v227, v234, 15, v227\n" \
"v_lshrrev_b32_e64 v234, 12, v224\n" \
"v_lshl_or_b32 v227, v234, 13, v227\n" \
"v_and_or_b32 v227, v235, 48, v227\n" \
"v_readlane_b32 s51, %[tlo], 31\n" \
"s_lshl_b32 s62, %[wid], 10\n" \
"s_add_u32 s62, s62, 262144\n" \
"s_add_u32 s55, s62, 8192\n" \
"v_readlane_b32 s68, %[tlo], 20\n" \
"v_readlane_b32 s69, %[thi], 20\n" \
"s_lshl_b32 s63, %[wid], 9\n" \
"v_lshrrev_b32_e64 v230, 1, v235\n" \
"buffer_load_dwordx4 a[200:203], v235, %[srdz], s62 offen sc1\n" \
"v_add_u32_e64 v230, s63, v230\n" \
"buffer_load_dwordx4 a[204:207], v235, %[srdz], s55 offen sc1\n" \
"s_nop 1\n" \
"global_load_dwordx2 a[208:209], v230, s[68:69]\n" \
"v_mfma_f32_16x16x32_bf16 v[128:131], a[64:67], a[0:3], v[160:163]\n" \
"v_exp_f32_e32 v156, v156\n" \
"v_mfma_f32_16x16x32_bf16 v[128:131], a[68:71], a[4:7], v[128:131]\n" \
"ds_read_b64_tr_b16 a[182:183], v211 offset:13312\n" \
"s_add_u32 m0, %[m0b], 0\n" \
"s_mov_b32 s49, s48\n" \
"v_mfma_f32_16x16x32_bf16 v[128:131], a[72:75], a[8:11], v[128:131]\n" \
"v_exp_f32_e64 v157, v157\n" \
"v_mfma_f32_16x16x32_bf16 v[128:131], a[76:79], a[12:15], v[128:131]\n" \
"v_cvt_pk_bf16_f32 v204, v152, v153\n" \
"v_cvt_pk_bf16_f32 v205, v154, v155\n" \
"v_mfma_f32_16x16x32_bf16 v[132:135], a[80:83], a[0:3], v[160:163]\n" \
"v_exp_f32_e64 v158, v158\n" \
"v_mfma_f32_16x16x32_bf16 v[132:135], a[84:87], a[4:7], v[132:135]\n" \
"ds_read_b64_tr_b16 a[184:185], %[vvb] offset:9728\n" \
"s_lshl_b32 s49, s49, 18\n" \
"s_or_b32 s49, s49, s58\n" \
"s_waitcnt lgkmcnt(5)\n" \
"v_mfma_f32_16x16x32_bf16 v[132:135], a[88:91], a[8:11], v[132:135]\n" \
"buffer_load_dwordx4 %[wo0], %[srdt], s49 offen offset:0 lds\n" \
"v_mfma_f32_16x16x32_bf16 v[132:135], a[92:95], a[12:15], v[132:135]\n" \
"v_exp_f32_e32 v159, v159\n" \
"v_mfma_f32_16x16x32_bf16 v[64:67], a[128:131], v[216:219], v[64:67]\n" \
"ds_read_b64_tr_b16 a[186:187], %[vvb] offset:13824\n" \
"ds_read_b64_tr_b16 a[188:189], v211 offset:9728\n" \
"v_readlane_b32 s60, %[thi], 11\n" \
"buffer_load_dwordx4 a[212:215], v227, %[srdt], s51 offen offset:0\n" \
"v_mfma_f32_16x16x32_bf16 v[64:67], a[160:163], v[200:203], v[64:67]\n" \
"v_exp_f32_e64 v128, v128\n" \
"v_mfma_f32_16x16x32_bf16 v[68:71], a[132:135], v[216:219], v[68:71]\n" \
"v_cvt_pk_bf16_f32 v206, v156, v157\n" \
"v_cvt_pk_bf16_f32 v207, v158, v159\n" \
"v_mfma_f32_16x16x32_bf16 v[68:71], a[164:167], v[200:203], v[68:71]\n" \
"v_exp_f32_e64 v129, v129\n" \
"v_mfma_f32_16x16x32_bf16 v[72:75], a[136:139], v[216:219], v[72:75]\n" \
"ds_read_b64_tr_b16 a[190:191], v211 offset:13824\n" \
"ds_read_b64_tr_b16 a[128:129], %[vvb] offset:16384\n" \
"v_readlane_b32 s59, %[thi], 12\n" \
"v_mfma_f32_16x16x32_bf16 v[72:75], a[168:171], v[200:203], v[72:75]\n" \
"v_exp_f32_e64 v130, v130\n" \
"v_mfma_f32_16x16x32_bf16 v[76:79], a[140:143], v[216:219], v[76:79]\n" \
"ds_read_b64_tr_b16 a[130:131], %[vvb] offset:20480\n" \
"ds_read_b64_tr_b16 a[132:133], v211 offset:16384\n" \
"v_readlane_b32 s53, %[tlo], 13\n" \
"buffer_load_dwordx4 a[216:219], v227, %[srdt], s51 offen offset:64\n" \
"v_mfma_f32_16x16x32_bf16 v[76:79], a[172:175], v[200:203], v[76:79]\n" \
"v_exp_f32_e64 v131, v131\n" \
"v_smfmac_f32_16x16x64_bf16 v[176:179], a[192:195], v[192:199], v228\n" \
"ds_read_b64_tr_b16 a[134:135], v211 offset:20480\n" \
"ds_read_b64_tr_b16 a[136:137], %[vvb] offset:16896\n" \
"v_readlane_b32 s50, %[thi], 13\n" \
"v_mfma_f32_16x16x32_bf16 v[136:139], a[64:67], a[16:19], v[164:167]\n" \
"v_exp_f32_e64 v132, v132\n" \
"v_mfma_f32_16x16x32_bf16 v[136:139], a[68:71], a[20:23], v[136:139]\n" \
"ds_read_b64_tr_b16 a[138:139], %[vvb] offset:20992\n" \
"ds_read_b64_tr_b16 a[140:141], v211 offset:16896\n" \
"v_readlane_b32 s46, %[tlo], 14\n" \
"v_add_u32_e32 v212, 0x3c00, %[wo0]\n" \
"v_mfma_f32_16x16x32_bf16 v[136:139], a[72:75], a[24:27], v[136:139]\n" \
"v_exp_f32_e64 v133, v133\n" \
"v_mfma_f32_16x16x32_bf16 v[136:139], a[76:79], a[28:31], v[136:139]\n" \
"v_cvt_pk_bf16_f32 v184, v128, v129\n" \
"v_cvt_pk_bf16_f32 v185, v130, v131\n" \
"v_mfma_f32_16x16x32_bf16 v[140:143], a[80:83], a[16:19], v[164:167]\n" \
"v_exp_f32_e64 v134, v134\n" \
"v_mfma_f32_16x16x32_bf16 v[140:143], a[84:87], a[20:23], v[140:143]\n" \
"ds_read_b64_tr_b16 a[142:143], v211 offset:20992\n" \
"v_readlane_b32 s47, %[thi], 14\n" \
"v_add_u32_e32 v213, 0x7800, %[wo0]\n" \
"buffer_load_dwordx4 a[220:223], v227, %[srdt], s51 offen offset:128\n" \
"s_waitcnt lgkmcnt(8)\n" \
"v_mfma_f32_16x16x32_bf16 v[140:143], a[88:91], a[24:27], v[140:143]\n" \
"buffer_load_dwordx4 v212, %[srdt], s49 offen offset:1024 lds\n" \
"v_mfma_f32_16x16x32_bf16 v[140:143], a[92:95], a[28:31], v[140:143]\n" \
"v_exp_f32_e32 v135, v135\n" \
"v_mfma_f32_16x16x32_bf16 v[80:83], a[144:147], v[216:219], v[80:83]\n" \
"ds_read_b64_tr_b16 a[144:145], %[vvb] offset:17408\n" \
"ds_read_b64_tr_b16 a[146:147], %[vvb] offset:21504\n" \
"v_mfma_f32_16x16x32_bf16 v[80:83], a[176:179], v[200:203], v[80:83]\n" \
"v_exp_f32_e64 v136, v136\n" \
"v_mfma_f32_16x16x32_bf16 v[84:87], a[148:151], v[216:219], v[84:87]\n" \
"v_cvt_pk_bf16_f32 v186, v132, v133\n" \
"v_cvt_pk_bf16_f32 v187, v134, v135\n" \
"v_mfma_f32_16x16x32_bf16 v[84:87], a[180:183], v[200:203], v[84:87]\n" \
"v_exp_f32_e64 v137, v137\n" \
"v_mfma_f32_16x16x32_bf16 v[88:91], a[152:155], v[216:219], v[88:91]\n" \
"v_exp_f32_e64 v138, v138\n" \
"v_mfma_f32_16x16x32_bf16 v[88:91], a[184:187], v[200:203], v[88:91]\n" \
"ds_read_b64_tr_b16 a[148:149], v211 offset:17408\n" \
"buffer_load_dwordx4 a[224:227], v227, %[srdt], s51 offen offset:192\n" \
"ds_read_b64_tr_b16 a[150:151], v211 offset:21504\n" \
"v_mfma_f32_16x16x32_bf16 v[92:95], a[156:159], v[216:219], v[92:95]\n" \
"v_exp_f32_e64 v139, v139\n" \
"v_mfma_f32_16x16x32_bf16 v[92:95], a[188:191], v[200:203], v[92:95]\n" \
"ds_read_b64_tr_b16 a[152:153], %[vvb] offset:17920\n" \
"v_mfma_f32_16x16x32_bf16 v[144:147], a[64:67], a[32:35], v[168:171]\n" \
"v_exp_f32_e64 v140, v140\n" \
"v_mfma_f32_16x16x32_bf16 v[144:147], a[68:71], a[36:39], v[144:147]\n" \
"ds_read_b64_tr_b16 a[154:155], %[vvb] offset:22016\n" \
"ds_read_b64_tr_b16 a[156:157], v211 offset:17920\n" \
"buffer_load_dwordx4 a[228:231], v227, %[srdt], s51 offen offset:256\n" \
"v_mfma_f32_16x16x32_bf16 v[144:147], a[72:75], a[40:43], v[144:147]\n" \
"v_exp_f32_e64 v141, v141\n" \
"v_mfma_f32_16x16x32_bf16 v[144:147], a[76:79], a[44:47], v[144:147]\n" \
"v_cvt_pk_bf16_f32 v188, v136, v137\n" \
"v_cvt_pk_bf16_f32 v189, v138, v139\n" \
"v_mfma_f32_16x16x32_bf16 v[148:151], a[80:83], a[32:35], v[168:171]\n" \
"v_exp_f32_e64 v142, v142\n" \
"v_mfma_f32_16x16x32_bf16 v[148:151], a[84:87], a[36:39], v[148:151]\n" \
"ds_read_b64_tr_b16 a[158:159], v211 offset:22016\n" \
"s_waitcnt lgkmcnt(8)\n" \
"v_mfma_f32_16x16x32_bf16 v[148:151], a[88:91], a[40:43], v[148:151]\n" \
"buffer_load_dwordx4 v213, %[srdt], s49 offen offset:2048 lds\n" \
"v_mfma_f32_16x16x32_bf16 v[148:151], a[92:95], a[44:47], v[148:151]\n" \
"v_exp_f32_e32 v143, v143\n" \
"v_mfma_f32_16x16x32_bf16 v[0:3], a[160:163], v[192:195], v[0:3]\n" \
"ds_read_b128 a[96:99], %[kb0] offset:24576\n" \
"buffer_load_dwordx4 a[232:235], v227, %[srdt], s51 offen offset:320\n" \
"s_waitcnt vmcnt(9)\n" \
"v_accvgpr_read_b32 v230, a208\n" \
"v_accvgpr_read_b32 v234, a204\n" \
"v_mfma_f32_16x16x32_bf16 v[0:3], a[128:131], v[184:187], v[0:3]\n" \
"v_exp_f32_e32 v144, v144\n" \
"v_accvgpr_read_b32 v225, a205\n" \
"v_accvgpr_read_b32 v226, a200\n" \
"v_mfma_f32_16x16x32_bf16 v[4:7], a[164:167], v[192:195], v[4:7]\n" \
"v_cvt_pk_bf16_f32 v190, v140, v141\n" \
"v_cvt_pk_bf16_f32 v191, v142, v143\n" \
"v_accvgpr_read_b32 v231, a201\n" \
"v_bitop3_b32 v229, v234, v225, -1 bitop3:0x80\n" \
"v_mfma_f32_16x16x32_bf16 v[4:7], a[132:135], v[184:187], v[4:7]\n" \
"v_exp_f32_e64 v145, v145\n" \
"v_bitop3_b32 v229, v226, v231, v229 bitop3:0x80\n" \
"v_lshlrev_b32_e64 v236, 16, v230\n" \
"v_mfma_f32_16x16x32_bf16 v[8:11], a[168:171], v[192:195], v[8:11]\n" \
"ds_read_b128 a[100:103], v208 offset:24576\n" \
"v_and_b32_e32 v230, 0xffff0000, v230\n" \
"v_mul_f32_e64 v234, v234, v236\n" \
"v_mfma_f32_16x16x32_bf16 v[8:11], a[136:139], v[184:187], v[8:11]\n" \
"v_exp_f32_e32 v146, v146\n" \
"v_mul_f32_e32 v225, v225, v230\n" \
"v_mul_f32_e64 v226, v226, v234\n" \
"v_mfma_f32_16x16x32_bf16 v[12:15], a[172:175], v[192:195], v[12:15]\n" \
"ds_read_b128 a[104:107], v209 offset:24576\n" \
"v_add_u32_e32 v214, 0xb400, %[wo0]\n" \
"buffer_load_dwordx4 a[236:239], v227, %[srdt], s51 offen offset:384\n" \
"v_mul_f32_e64 v231, v231, v225\n" \
"v_cvt_pk_bf16_f32 v236, v234, v225\n" \
"v_mfma_f32_16x16x32_bf16 v[12:15], a[140:143], v[184:187], v[12:15]\n" \
"v_exp_f32_e64 v147, v147\n" \
"v_cvt_pk_bf16_f32 v232, v226, v231\n" \
"v_smfmac_f32_16x16x64_bf16 v[180:183], a[192:195], v[200:207], v228\n" \
"ds_read_b128 a[108:111], v210 offset:24576\n" \
"v_accvgpr_read_b32 v230, a209\n" \
"v_accvgpr_read_b32 v234, a206\n" \
"v_mfma_f32_16x16x32_bf16 v[152:155], a[64:67], a[48:51], v[172:175]\n" \
"v_exp_f32_e64 v148, v148\n" \
"v_accvgpr_read_b32 v225, a207\n" \
"v_accvgpr_read_b32 v226, a202\n" \
"v_mfma_f32_16x16x32_bf16 v[152:155], a[68:71], a[52:55], v[152:155]\n" \
"ds_read_b128 a[112:115], %[kb0] offset:28672\n" \
"v_accvgpr_read_b32 v231, a203\n" \
"v_bitop3_b32 v229, v234, v225, v229 bitop3:0x80\n" \
"v_mfma_f32_16x16x32_bf16 v[152:155], a[72:75], a[56:59], v[152:155]\n" \
"v_exp_f32_e64 v149, v149\n" \
"v_bitop3_b32 v229, v226, v231, v229 bitop3:0x80\n" \
"v_lshlrev_b32_e64 v237, 16, v230\n" \
"v_mfma_f32_16x16x32_bf16 v[152:155], a[76:79], a[60:63], v[152:155]\n" \
"v_cvt_pk_bf16_f32 v216, v144, v145\n" \
"v_cvt_pk_bf16_f32 v217, v146, v147\n" \
"v_and_b32_e32 v230, 0xffff0000, v230\n" \
"v_mul_f32_e64 v234, v234, v237\n" \
"v_mfma_f32_16x16x32_bf16 v[156:159], a[80:83], a[48:51], v[172:175]\n" \
"v_exp_f32_e32 v150, v150\n" \
"v_mul_f32_e32 v225, v225, v230\n" \
"v_mul_f32_e64 v226, v226, v234\n" \
".if . - adaln_body != 32768\n" \
".error \"size model\"\n" \
".endif\n" \
"Lk7_%=:\n" \
"s_cbranch_execz Lk8_%=\n" \
"s_nop 0\n" \
"v_mfma_f32_16x16x32_bf16 v[156:159], a[84:87], a[52:55], v[156:159]\n" \
"ds_read_b128 a[116:119], v208 offset:28672\n" \
"buffer_load_dwordx4 a[196:199], v227, %[srdt], s51 offen offset:448\n" \
"v_mul_f32_e64 v231, v231, v225\n" \
"v_cvt_pk_bf16_f32 v237, v234, v225\n" \
"s_waitcnt lgkmcnt(6)\n" \
"v_mfma_f32_16x16x32_bf16 v[156:159], a[88:91], a[56:59], v[156:159]\n" \
"buffer_load_dwordx4 v214, %[srdt], s49 offen offset:3072 lds\n" \
"v_cvt_pk_bf16_f32 v233, v226, v231\n" \
"v_mfma_f32_16x16x32_bf16 v[156:159], a[92:95], a[60:63], v[156:159]\n" \
"v_exp_f32_e32 v151, v151\n" \
"v_lshrrev_b32_e64 v230, 1, v235\n" \
"s_add_u32 s62, %[m0t], 6144\n" \
"v_mfma_f32_16x16x32_bf16 v[16:19], a[176:179], v[192:195], v[16:19]\n" \
"ds_read_b128 a[120:123], v209 offset:28672\n" \
"v_add_u32_e32 v230, s62, v230\n" \
"v_and_b32_e32 v229, 1, v229\n" \
"v_mfma_f32_16x16x32_bf16 v[16:19], a[144:147], v[184:187], v[16:19]\n" \
"v_exp_f32_e64 v152, v152\n" \
"v_cmp_eq_u32_e64 s[68:69], 0, v229\n" \
"ds_write_b64 v230, v[232:233] offset:0\n" \
"v_mfma_f32_16x16x32_bf16 v[20:23], a[180:183], v[192:195], v[20:23]\n" \
"v_cvt_pk_bf16_f32 v218, v148, v149\n" \
"v_cvt_pk_bf16_f32 v219, v150, v151\n" \
"v_and_b32_e32 v230, 0x170, v235\n" \
"v_lshrrev_b32_e64 v234, 7, v224\n" \
"v_mfma_f32_16x16x32_bf16 v[20:23], a[148:151], v[184:187], v[20:23]\n" \
"v_exp_f32_e64 v153, v153\n" \
"v_and_or_b32 v230, v234, 8, v230\n" \
"v_lshrrev_b32_e64 v234, 12, v224\n" \
"v_mfma_f32_16x16x32_bf16 v[24:27], a[184:187], v[192:195], v[24:27]\n" \
"v_exp_f32_e64 v154, v154\n" \
"v_lshl_or_b32 v230, v234, 7, v230\n" \
"v_add_u32_e64 v230, s62, v230\n" \
"v_mfma_f32_16x16x32_bf16 v[24:27], a[152:155], v[184:187], v[24:27]\n" \
"ds_read_b128 a[124:127], v210 offset:28672\n" \
"ds_write_b64 v230, v[236:237] offset:512\n" \
"v_mfma_f32_16x16x32_bf16 v[28:31], a[188:191], v[192:195], v[28:31]\n" \
"v_exp_f32_e32 v155, v155\n" \
"s_cmp_lg_u64 s[68:69], 0\n" \
"s_cbranch_scc1 Lus_%=\n" \
"Lub_%=:\n" \
"v_lshrrev_b32_e32 v234, 1, v235\n" \
"v_mfma_f32_16x16x32_bf16 v[28:31], a[156:159], v[184:187], v[28:31]\n" \
"s_waitcnt vmcnt(15) lgkmcnt(4)\n" \
"s_barrier\n" \
"v_mfma_f32_16x16x32_bf16 v[128:131], a[96:99], a[0:3], v[160:163]\n" \
"v_exp_f32_e64 v156, v156\n" \
"v_readlane_b32 s64, %[tlo], 8\n" \
"v_readlane_b32 s65, %[thi], 8\n" \
"v_add_u32_e32 v234, s62, v234\n" \
"v_mov_b64_e32 v[232:233], 0\n" \
"v_mfma_f32_16x16x32_bf16 v[128:131], a[100:103], a[4:7], v[128:131]\n" \
"v_mbcnt_lo_u32_b32 v231, -1, 0\n" \
"ds_write_b64 v234, v[232:233] offset:1024\n" \
"s_mov_b32 s68, 0xf0000f\n" \
"v_mfma_f32_16x16x32_bf16 v[128:131], a[104:107], a[8:11], v[128:131]\n" \
"v_exp_f32_e64 v157, v157\n" \
"s_mov_b32 s69, 0xf0000f00\n" \
"v_mov_b32_e32 v234, 0x400\n" \
"v_mfma_f32_16x16x32_bf16 v[128:131], a[108:111], a[12:15], v[128:131]\n" \
"v_cvt_pk_bf16_f32 v220, v152, v153\n" \
"v_cvt_pk_bf16_f32 v221, v154, v155\n" \
"v_cndmask_b32_e64 v234, v234, 0, s[68:69]\n" \
"v_and_or_b32 v230, v235, 48, v234\n" \
"v_mfma_f32_16x16x32_bf16 v[132:135], a[112:115], a[0:3], v[160:163]\n" \
"v_exp_f32_e32 v158, v158\n" \
"v_add_u32_e32 v230, s62, v230\n" \
"v_mfma_f32_16x16x32_bf16 v[132:135], a[116:119], a[4:7], v[132:135]\n" \
"v_mbcnt_hi_u32_b32 v231, -1, v231\n" \
"s_waitcnt lgkmcnt(2)\n" \
"v_mfma_f32_16x16x32_bf16 v[132:135], a[120:123], a[8:11], v[132:135]\n" \
"s_add_u32 m0, %[m0b], 65536\n" \
"s_mov_b32 s61, 0\n" \
"buffer_load_dwordx4 %[vg0], s[64:67], s61 offen offset:0 lds\n" \
"v_mfma_f32_16x16x32_bf16 v[132:135], a[124:127], a[12:15], v[132:135]\n" \
"v_exp_f32_e64 v159, v159\n" \
"v_mfma_f32_16x16x32_bf16 v[32:35], a[160:163], v[196:199], v[32:35]\n" \
"ds_read_b128 a[64:67], %[kb0] offset:32768\n" \
"v_and_b32_e64 v226, 31, v231\n" \
"v_mfma_f32_16x16x32_bf16 v[32:35], a[128:131], v[188:191], v[32:35]\n" \
"v_exp_f32_e64 v128, v128\n" \
"v_mfma_f32_16x16x32_bf16 v[36:39], a[164:167], v[196:199], v[36:39]\n" \
"v_cvt_pk_bf16_f32 v222, v156, v157\n" \
"v_cvt_pk_bf16_f32 v223, v158, v159\n" \
"v_mfma_f32_16x16x32_bf16 v[36:39], a[132:135], v[188:191], v[36:39]\n" \
"v_exp_f32_e64 v129, v129\n" \
"v_mfma_f32_16x16x32_bf16 v[40:43], a[168:171], v[196:199], v[40:43]\n" \
"ds_read_b128 a[68:71], v208 offset:32768\n" \
"v_lshlrev_b32_e64 v226, 2, v226\n" \
"v_mfma_f32_16x16x32_bf16 v[40:43], a[136:139], v[188:191], v[40:43]\n" \
"v_exp_f32_e64 v130, v130\n" \
"v_mfma_f32_16x16x32_bf16 v[44:47], a[172:175], v[196:199], v[44:47]\n" \
"ds_read_b128 a[72:75], v209 offset:32768\n" \
"v_cmp_gt_u32_e32 vcc, 1, v231\n" \
"v_mfma_f32_16x16x32_bf16 v[44:47], a[140:143], v[188:191], v[44:47]\n" \
"v_exp_f32_e32 v131, v131\n" \
"ds_read_b128 a[200:203], v230 offset:0\n" \
"v_smfmac_f32_16x16x64_bf16 v[176:179], a[192:195], v[184:191], v228\n" \
"v_mov_b32_e32 v225, 0x80000000\n" \
"v_mfma_f32_16x16x32_bf16 v[136:139], a[96:99], a[16:19], v[164:167]\n" \
"v_exp_f32_e64 v132, v132\n" \
"v_mfma_f32_16x16x32_bf16 v[136:139], a[100:103], a[20:23], v[136:139]\n" \
"ds_read_b128 a[76:79], v210 offset:32768\n" \
"v_mfma_f32_16x16x32_bf16 v[136:139], a[104:107], a[24:27], v[136:139]\n" \
"v_exp_f32_e64 v133, v133\n" \
"v_mfma_f32_16x16x32_bf16 v[136:139], a[108:111], a[28:31], v[136:139]\n" \
"v_cvt_pk_bf16_f32 v192, v128, v129\n" \
"v_cvt_pk_bf16_f32 v193, v130, v131\n" \
"ds_read_b128 a[204:207], v230 offset:64\n" \
"v_mfma_f32_16x16x32_bf16 v[140:143], a[112:115], a[16:19], v[164:167]\n" \
"v_exp_f32_e64 v134, v134\n" \
"v_mfma_f32_16x16x32_bf16 v[140:143], a[116:119], a[20:23], v[140:143]\n" \
"ds_read_b128 a[80:83], %[kb0] offset:36864\n" \
"v_cndmask_b32_e32 v225, v225, v224, vcc\n" \
"v_mfma_f32_16x16x32_bf16 v[140:143], a[120:123], a[24:27], v[140:143]\n" \
"s_add_u32 m0, %[m0b], 66560\n" \
"s_mov_b32 s61, 229376\n" \
"buffer_load_dwordx4 %[vg0], s[64:67], s61 offen offset:0 lds\n" \
"v_mfma_f32_16x16x32_bf16 v[140:143], a[124:127], a[28:31], v[140:143]\n" \
"v_exp_f32_e32 v135, v135\n" \
"s_waitcnt vmcnt(12) lgkmcnt(3)\n" \
"v_mfma_f32_16x16x32_bf16 a[208:211], a[212:215], a[200:203], 0\n" \
"ds_read_b128 a[200:203], v230 offset:128\n" \
"v_mfma_f32_16x16x32_bf16 v[48:51], a[176:179], v[196:199], v[48:51]\n" \
"ds_read_b128 a[84:87], v208 offset:36864\n" \
"v_mfma_f32_16x16x32_bf16 v[48:51], a[144:147], v[188:191], v[48:51]\n" \
"v_exp_f32_e32 v136, v136\n" \
"v_mfma_f32_16x16x32_bf16 v[52:55], a[180:183], v[196:199], v[52:55]\n" \
"v_cvt_pk_bf16_f32 v194, v132, v133\n" \
"v_cvt_pk_bf16_f32 v195, v134, v135\n" \
"v_mfma_f32_16x16x32_bf16 v[52:55], a[148:151], v[188:191], v[52:55]\n" \
"v_exp_f32_e64 v137, v137\n" \
"v_mfma_f32_16x16x32_bf16 v[56:59], a[184:187], v[196:199], v[56:59]\n" \
"v_exp_f32_e64 v138, v138\n" \
"v_mfma_f32_16x16x32_bf16 v[56:59], a[152:155], v[188:191], v[56:59]\n" \
"ds_read_b128 a[88:91], v209 offset:36864\n" \
"s_waitcnt vmcnt(11) lgkmcnt(4)\n" \
"v_mfma_f32_16x16x32_bf16 a[208:211], a[216:219], a[204:207], a[208:211]\n" \
"ds_read_b128 a[204:207], v230 offset:192\n" \
"v_mfma_f32_16x16x32_bf16 v[60:63], a[188:191], v[196:199], v[60:63]\n" \
"v_exp_f32_e32 v139, v139\n" \
"v_mfma_f32_16x16x32_bf16 v[60:63], a[156:159], v[188:191], v[60:63]\n" \
"s_waitcnt vmcnt(10) lgkmcnt(3)\n" \
"v_mfma_f32_16x16x32_bf16 a[208:211], a[220:223], a[200:203], a[208:211]\n" \
"ds_read_b128 a[200:203], v230 offset:256\n" \
"v_mfma_f32_16x16x32_bf16 v[144:147], a[96:99], a[32:35], v[168:171]\n" \
"v_exp_f32_e32 v140, v140\n" \
"v_mfma_f32_16x16x32_bf16 v[144:147], a[100:103], a[36:39], v[144:147]\n" \
"ds_read_b128 a[92:95], v210 offset:36864\n" \
"s_sub_u32 s61, s48, 0\n" \
"s_cmp_lt_u32 s61, 4\n" \
"s_cselect_b64 vcc, -1, 0\n" \
"v_mfma_f32_16x16x32_bf16 v[144:147], a[104:107], a[40:43], v[144:147]\n" \
"v_exp_f32_e32 v141, v141\n" \
"v_mfma_f32_16x16x32_bf16 v[144:147], a[108:111], a[44:47], v[144:147]\n" \
"v_cvt_pk_bf16_f32 v196, v136, v137\n" \
"v_cvt_pk_bf16_f32 v197, v138, v139\n" \
"v_mfma_f32_16x16x32_bf16 v[148:151], a[112:115], a[32:35], v[168:171]\n" \
"v_exp_f32_e64 v142, v142\n" \
"v_mfma_f32_16x16x32_bf16 v[148:151], a[116:119], a[36:39], v[148:151]\n" \
"v_readlane_b32 s55, %[tlo], 16\n" \
"s_waitcnt vmcnt(8) lgkmcnt(2)\n" \
"v_mfma_f32_16x16x32_bf16 a[208:211], a[224:227], a[204:207], a[208:211]\n" \
"ds_read_b128 a[204:207], v230 offset:320\n" \
"v_mfma_f32_16x16x32_bf16 v[148:151], a[120:123], a[40:43], v[148:151]\n" \
"s_add_u32 m0, %[m0b], 67584\n" \
"s_mov_b32 s61, 458752\n" \
"buffer_load_dwordx4 %[vg0], s[64:67], s61 offen offset:0 lds\n" \
"v_mfma_f32_16x16x32_bf16 v[148:151], a[124:127], a[44:47], v[148:151]\n" \
"v_exp_f32_e32 v143, v143\n" \
"v_mfma_f32_16x16x32_bf16 v[96:99], a[160:163], v[204:207], v[96:99]\n" \
"ds_read_b64_tr_b16 a[160:161], %[vvb] offset:24576\n" \
"v_cmp_gt_u32_e64 s[56:57], 32, v231\n" \
"v_mfma_f32_16x16x32_bf16 v[96:99], a[128:131], v[220:223], v[96:99]\n" \
"v_exp_f32_e64 v144, v144\n" \
"v_mfma_f32_16x16x32_bf16 v[100:103], a[164:167], v[204:207], v[100:103]\n" \
"v_cvt_pk_bf16_f32 v198, v140, v141\n" \
"v_cvt_pk_bf16_f32 v199, v142, v143\n" \
"s_waitcnt vmcnt(8) lgkmcnt(3)\n" \
"v_mfma_f32_16x16x32_bf16 a[208:211], a[228:231], a[200:203], a[208:211]\n" \
"ds_read_b128 a[200:203], v230 offset:384\n" \
"v_mfma_f32_16x16x32_bf16 v[100:103], a[132:135], v[220:223], v[100:103]\n" \
"v_exp_f32_e32 v145, v145\n" \
"v_mfma_f32_16x16x32_bf16 v[104:107], a[168:171], v[204:207], v[104:107]\n" \
"ds_read_b64_tr_b16 a[162:163], %[vvb] offset:28672\n" \
"ds_read_b64_tr_b16 a[164:165], v211 offset:24576\n" \
"v_lshlrev_b32_e64 v232, 2, v224\n" \
"v_mfma_f32_16x16x32_bf16 v[104:107], a[136:139], v[220:223], v[104:107]\n" \
"v_exp_f32_e64 v146, v146\n" \
"v_mfma_f32_16x16x32_bf16 v[108:111], a[172:175], v[204:207], v[108:111]\n" \
"ds_read_b64_tr_b16 a[166:167], v211 offset:28672\n" \
"v_mov_b32_e32 v233, 0x80000000\n" \
"v_mfma_f32_16x16x32_bf16 v[108:111], a[140:143], v[220:223], v[108:111]\n" \
"v_exp_f32_e64 v147, v147\n" \
"v_smfmac_f32_16x16x64_bf16 v[180:183], a[192:195], v[216:223], v228\n" \
"ds_read_b64_tr_b16 a[168:169], %[vvb] offset:25088\n" \
"s_waitcnt vmcnt(6) lgkmcnt(6)\n" \
"v_mfma_f32_16x16x32_bf16 a[208:211], a[232:235], a[204:207], a[208:211]\n" \
"ds_read_b128 a[204:207], v230 offset:448\n" \
"v_mfma_f32_16x16x32_bf16 v[152:155], a[96:99], a[48:51], v[172:175]\n" \
"v_exp_f32_e32 v148, v148\n" \
"v_mfma_f32_16x16x32_bf16 v[152:155], a[100:103], a[52:55], v[152:155]\n" \
"ds_read_b64_tr_b16 a[170:171], %[vvb] offset:29184\n" \
"v_cndmask_b32_e64 v232, v233, v232, s[56:57]\n" \
"v_mfma_f32_16x16x32_bf16 v[152:155], a[104:107], a[56:59], v[152:155]\n" \
"v_exp_f32_e64 v149, v149\n" \
"v_mfma_f32_16x16x32_bf16 v[152:155], a[108:111], a[60:63], v[152:155]\n" \
"v_cvt_pk_bf16_f32 v200, v144, v145\n" \
"v_cvt_pk_bf16_f32 v201, v146, v147\n" \
"v_mfma_f32_16x16x32_bf16 v[156:159], a[112:115], a[48:51], v[172:175]\n" \
"v_exp_f32_e64 v150, v150\n" \
"v_mfma_f32_16x16x32_bf16 v[156:159], a[116:119], a[52:55], v[156:159]\n" \
"ds_read_b64_tr_b16 a[172:173], v211 offset:25088\n" \
"v_mov_b32_e32 v230, 1\n" \
"s_waitcnt vmcnt(5) lgkmcnt(7)\n" \
"v_mfma_f32_16x16x32_bf16 a[208:211], a[236:239], a[200:203], a[208:211]\n" \
"v_mfma_f32_16x16x32_bf16 v[156:159], a[120:123], a[56:59], v[156:159]\n" \
"s_add_u32 m0, %[m0b], 68608\n" \
"s_mov_b32 s61, 688128\n" \
"buffer_load_dwordx4 %[vg0], s[64:67], s61 offen offset:0 lds\n" \
"v_mfma_f32_16x16x32_bf16 v[156:159], a[124:127], a[60:63], v[156:159]\n" \
"v_exp_f32_e64 v151, v151\n" \
"v_mfma_f32_16x16x32_bf16 v[112:115], a[176:179], v[204:207], v[112:115]\n" \
"ds_read_b64_tr_b16 a[174:175], v211 offset:29184\n" \
"ds_read_b64_tr_b16 a[176:177], %[vvb] offset:25600\n" \
"v_mfma_f32_16x16x32_bf16 v[112:115], a[144:147], v[220:223], v[112:115]\n" \
"v_exp_f32_e64 v152, v152\n" \
"v_mfma_f32_16x16x32_bf16 v[116:119], a[180:183], v[204:207], v[116:119]\n" \
"v_cvt_pk_bf16_f32 v202, v148, v149\n" \
"v_cvt_pk_bf16_f32 v203, v150, v151\n" \
"s_waitcnt vmcnt(5) lgkmcnt(4)\n" \
"v_mfma_f32_16x16x32_bf16 a[208:211], a[196:199], a[204:207], a[208:211]\n" \
"v_mfma_f32_16x16x32_bf16 v[116:119], a[148:151], v[220:223], v[116:119]\n" \
"v_exp_f32_e32 v153, v153\n" \
"v_mfma_f32_16x16x32_bf16 v[120:123], a[184:187], v[204:207], v[120:123]\n" \
"v_exp_f32_e64 v154, v154\n" \
"v_mfma_f32_16x16x32_bf16 v[120:123], a[152:155], v[220:223], v[120:123]\n" \
"ds_read_b64_tr_b16 a[178:179], %[vvb] offset:29696\n" \
"buffer_store_dword v230, v232, %[srdz], s55 offen sc1\n" \
"v_mfma_f32_16x16x32_bf16 v[124:127], a[188:191], v[204:207], v[124:127]\n" \
"v_exp_f32_e64 v155, v155\n" \
"v_mfma_f32_16x16x32_bf16 v[124:127], a[156:159], v[220:223], v[124:127]\n" \
"ds_read_b64_tr_b16 a[180:181], v211 offset:25600\n" \
"s_waitcnt lgkmcnt(14)\n" \
"v_mfma_f32_16x16x32_bf16 v[128:131], a[64:67], a[0:3], v[160:163]\n" \
"v_exp_f32_e32 v156, v156\n" \
"v_mfma_f32_16x16x32_bf16 v[128:131], a[68:71], a[4:7], v[128:131]\n" \
"ds_read_b64_tr_b16 a[182:183], v211 offset:29696\n" \
"s_add_u32 m0, %[m0b], 16384\n" \
"s_mov_b32 s49, s48\n" \
"s_mov_b32 s68, 0xaaaaaaaa\n" \
"s_mov_b32 s69, 0xaaaaaaaa\n" \
"v_mfma_f32_16x16x32_bf16 v[128:131], a[72:75], a[8:11], v[128:131]\n" \
"v_exp_f32_e32 v157, v157\n" \
"v_mfma_f32_16x16x32_bf16 v[128:131], a[76:79], a[12:15], v[128:131]\n" \
"v_cvt_pk_bf16_f32 v204, v152, v153\n" \
"v_cvt_pk_bf16_f32 v205, v154, v155\n" \
"v_accvgpr_read_b32 v232, a208\n" \
"v_accvgpr_read_b32 v233, a209\n" \
"v_mfma_f32_16x16x32_bf16 v[132:135], a[80:83], a[0:3], v[160:163]\n" \
"v_exp_f32_e64 v158, v158\n" \
"v_mfma_f32_16x16x32_bf16 v[132:135], a[84:87], a[4:7], v[132:135]\n" \
"ds_read_b64_tr_b16 a[184:185], %[vvb] offset:26112\n" \
"s_lshl_b32 s49, s49, 18\n" \
"s_or_b32 s49, s49, s59\n" \
"v_accvgpr_read_b32 v234, a210\n" \
"v_accvgpr_read_b32 v236, a211\n" \
"s_waitcnt lgkmcnt(5)\n" \
"v_mfma_f32_16x16x32_bf16 v[132:135], a[88:91], a[8:11], v[132:135]\n" \
"buffer_load_dwordx4 %[wo0], %[srdt], s49 offen offset:0 lds\n" \
"v_mfma_f32_16x16x32_bf16 v[132:135], a[92:95], a[12:15], v[132:135]\n" \
"v_exp_f32_e32 v159, v159\n" \
"v_mfma_f32_16x16x32_bf16 v[64:67], a[128:131], v[216:219], v[64:67]\n" \
"ds_read_b64_tr_b16 a[186:187], %[vvb] offset:30208\n" \
"ds_read_b64_tr_b16 a[188:189], v211 offset:26112\n" \
"v_xor_b32_e32 v229, 0x80, %[vx0]\n" \
"v_cndmask_b32_e64 v232, v232, v233, s[68:69]\n" \
"v_cndmask_b32_e64 v234, v234, v236, s[68:69]\n" \
"v_mfma_f32_16x16x32_bf16 v[64:67], a[160:163], v[200:203], v[64:67]\n" \
"v_exp_f32_e64 v128, v128\n" \
"v_mfma_f32_16x16x32_bf16 v[68:71], a[132:135], v[216:219], v[68:71]\n" \
"v_cvt_pk_bf16_f32 v206, v156, v157\n" \
"v_cvt_pk_bf16_f32 v207, v158, v159\n" \
"s_mov_b32 s68, 0xcccccccc\n" \
"s_mov_b32 s69, 0xcccccccc\n" \
"v_mfma_f32_16x16x32_bf16 v[68:71], a[164:167], v[200:203], v[68:71]\n" \
"v_exp_f32_e64 v129, v129\n" \
"v_mfma_f32_16x16x32_bf16 v[72:75], a[136:139], v[216:219], v[72:75]\n" \
"ds_read_b64_tr_b16 a[190:191], v211 offset:30208\n" \
"ds_read_b64_tr_b16 a[128:129], %[vvb] offset:32768\n" \
"v_cndmask_b32_e64 v232, v232, v234, s[68:69]\n" \
"v_and_b32_e32 v230, 0xc0, v235\n" \
"v_mfma_f32_16x16x32_bf16 v[72:75], a[168:171], v[200:203], v[72:75]\n" \
"v_exp_f32_e64 v130, v130\n" \
"v_mfma_f32_16x16x32_bf16 v[76:79], a[140:143], v[216:219], v[76:79]\n" \
"ds_read_b64_tr_b16 a[130:131], %[vvb] offset:36864\n" \
"ds_read_b64_tr_b16 a[132:133], v211 offset:32768\n" \
"v_lshrrev_b32_e64 v233, 7, v224\n" \
"s_mul_i32 s62, %[wid], 8188\n" \
"v_mfma_f32_16x16x32_bf16 v[76:79], a[172:175], v[200:203], v[76:79]\n" \
"v_exp_f32_e64 v131, v131\n" \
"v_smfmac_f32_16x16x64_bf16 v[176:179], a[192:195], v[192:199], v228\n" \
"ds_read_b64_tr_b16 a[134:135], v211 offset:36864\n" \
"ds_read_b64_tr_b16 a[136:137], %[vvb] offset:33280\n" \
"global_load_dwordx2 a[240:241], %[vx0], %[px] offset:0\n" \
"v_mfma_f32_16x16x32_bf16 v[136:139], a[64:67], a[16:19], v[164:167]\n" \
"v_exp_f32_e64 v132, v132\n" \
"v_mfma_f32_16x16x32_bf16 v[136:139], a[68:71], a[20:23], v[136:139]\n" \
"ds_read_b64_tr_b16 a[138:139], %[vvb] offset:37376\n" \
"ds_read_b64_tr_b16 a[140:141], v211 offset:33280\n" \
"v_add_f32_dpp v232, v232, v232 quad_perm:[1,0,3,2] row_mask:0xf bank_mask:0xf\n" \
"v_and_or_b32 v230, v233, 48, v230\n" \
"v_mfma_f32_16x16x32_bf16 v[136:139], a[72:75], a[24:27], v[136:139]\n" \
"v_exp_f32_e64 v133, v133\n" \
"v_mfma_f32_16x16x32_bf16 v[136:139], a[76:79], a[28:31], v[136:139]\n" \
"v_cvt_pk_bf16_f32 v184, v128, v129\n" \
"v_cvt_pk_bf16_f32 v185, v130, v131\n" \
"s_sub_u32 s62, %[m0t], s62\n" \
"v_add_u32_e32 v230, s62, v230\n" \
"v_mfma_f32_16x16x32_bf16 v[140:143], a[80:83], a[16:19], v[164:167]\n" \
"v_exp_f32_e64 v134, v134\n" \
"v_mfma_f32_16x16x32_bf16 v[140:143], a[84:87], a[20:23], v[140:143]\n" \
"ds_read_b64_tr_b16 a[142:143], v211 offset:37376\n" \
"v_add_f32_dpp v232, v232, v232 quad_perm:[2,3,0,1] row_mask:0xf bank_mask:0xf\n" \
"s_waitcnt lgkmcnt(8)\n" \
"v_mfma_f32_16x16x32_bf16 v[140:143], a[88:91], a[24:27], v[140:143]\n" \
"buffer_load_dwordx4 v212, %[srdt], s49 offen offset:1024 lds\n" \
"v_mfma_f32_16x16x32_bf16 v[140:143], a[92:95], a[28:31], v[140:143]\n" \
"v_exp_f32_e32 v135, v135\n" \
"v_mfma_f32_16x16x32_bf16 v[80:83], a[144:147], v[216:219], v[80:83]\n" \
"ds_read_b64_tr_b16 a[144:145], %[vvb] offset:33792\n" \
"ds_read_b64_tr_b16 a[146:147], %[vvb] offset:37888\n" \
"ds_write_b32 v230, v232 offset:7680\n" \
"v_mfma_f32_16x16x32_bf16 v[80:83], a[176:179], v[200:203], v[80:83]\n" \
"v_exp_f32_e64 v136, v136\n" \
"v_mfma_f32_16x16x32_bf16 v[84:87], a[148:151], v[216:219], v[84:87]\n" \
"v_cvt_pk_bf16_f32 v186, v132, v133\n" \
"v_cvt_pk_bf16_f32 v187, v134, v135\n" \
"v_mfma_f32_16x16x32_bf16 v[84:87], a[180:183], v[200:203], v[84:87]\n" \
"v_exp_f32_e64 v137, v137\n" \
"v_mfma_f32_16x16x32_bf16 v[88:91], a[152:155], v[216:219], v[88:91]\n" \
"v_exp_f32_e64 v138, v138\n" \
"v_mfma_f32_16x16x32_bf16 v[88:91], a[184:187], v[200:203], v[88:91]\n" \
"ds_read_b64_tr_b16 a[148:149], v211 offset:33792\n" \
"ds_read_b64_tr_b16 a[150:151], v211 offset:37888\n" \
"v_mfma_f32_16x16x32_bf16 v[92:95], a[156:159], v[216:219], v[92:95]\n" \
"v_exp_f32_e64 v139, v139\n" \
"v_mfma_f32_16x16x32_bf16 v[92:95], a[188:191], v[200:203], v[92:95]\n" \
"ds_read_b64_tr_b16 a[152:153], %[vvb] offset:34304\n" \
"global_load_dwordx2 a[244:245], %[vx0], %[px] offset:1024\n" \
"v_mfma_f32_16x16x32_bf16 v[144:147], a[64:67], a[32:35], v[168:171]\n" \
"v_exp_f32_e64 v140, v140\n" \
"v_mfma_f32_16x16x32_bf16 v[144:147], a[68:71], a[36:39], v[144:147]\n" \
"ds_read_b64_tr_b16 a[154:155], %[vvb] offset:38400\n" \
"ds_read_b64_tr_b16 a[156:157], v211 offset:34304\n" \
"v_mfma_f32_16x16x32_bf16 v[144:147], a[72:75], a[40:43], v[144:147]\n" \
"v_exp_f32_e64 v141, v141\n" \
"v_mfma_f32_16x16x32_bf16 v[144:147], a[76:79], a[44:47], v[144:147]\n" \
"v_cvt_pk_bf16_f32 v188, v136, v137\n" \
"v_cvt_pk_bf16_f32 v189, v138, v139\n" \
"v_mfma_f32_16x16x32_bf16 v[148:151], a[80:83], a[32:35], v[168:171]\n" \
"v_exp_f32_e64 v142, v142\n" \
"v_mfma_f32_16x16x32_bf16 v[148:151], a[84:87], a[36:39], v[148:151]\n" \
"ds_read_b64_tr_b16 a[158:159], v211 offset:38400\n" \
"s_waitcnt lgkmcnt(9)\n" \
"v_mfma_f32_16x16x32_bf16 v[148:151], a[88:91], a[40:43], v[148:151]\n" \
"buffer_load_dwordx4 v213, %[srdt], s49 offen offset:2048 lds\n" \
"v_mfma_f32_16x16x32_bf16 v[148:151], a[92:95], a[44:47], v[148:151]\n" \
"v_exp_f32_e32 v143, v143\n" \
"v_mfma_f32_16x16x32_bf16 v[0:3], a[160:163], v[192:195], v[0:3]\n" \
"ds_read_b128 a[96:99], %[kb0] offset:40960\n" \
"v_mfma_f32_16x16x32_bf16 v[0:3], a[128:131], v[184:187], v[0:3]\n" \
"v_exp_f32_e64 v144, v144\n" \
"v_mfma_f32_16x16x32_bf16 v[4:7], a[164:167], v[192:195], v[4:7]\n" \
"v_cvt_pk_bf16_f32 v190, v140, v141\n" \
"v_cvt_pk_bf16_f32 v191, v142, v143\n" \
"v_mfma_f32_16x16x32_bf16 v[4:7], a[132:135], v[184:187], v[4:7]\n" \
"v_exp_f32_e64 v145, v145\n" \
"v_mfma_f32_16x16x32_bf16 v[8:11], a[168:171], v[192:195], v[8:11]\n" \
"ds_read_b128 a[100:103], v208 offset:40960\n" \
"v_mfma_f32_16x16x32_bf16 v[8:11], a[136:139], v[184:187], v[8:11]\n" \
"v_exp_f32_e64 v146, v146\n" \
"v_mfma_f32_16x16x32_bf16 v[12:15], a[172:175], v[192:195], v[12:15]\n" \
"ds_read_b128 a[104:107], v209 offset:40960\n" \
"v_mfma_f32_16x16x32_bf16 v[12:15], a[140:143], v[184:187], v[12:15]\n" \
"v_exp_f32_e64 v147, v147\n" \
"v_smfmac_f32_16x16x64_bf16 v[180:183], a[192:195], v[200:207], v228\n" \
"ds_read_b128 a[108:111], v210 offset:40960\n" \
"global_load_dwordx2 a[248:249], v229, %[px] offset:0\n" \
"v_mfma_f32_16x16x32_bf16 v[152:155], a[64:67], a[48:51], v[172:175]\n" \
"v_exp_f32_e64 v148, v148\n" \
"v_mfma_f32_16x16x32_bf16 v[152:155], a[68:71], a[52:55], v[152:155]\n" \
"ds_read_b128 a[112:115], %[kb0] offset:45056\n" \
"v_mfma_f32_16x16x32_bf16 v[152:155], a[72:75], a[56:59], v[152:155]\n" \
"v_exp_f32_e64 v149, v149\n" \
"v_mfma_f32_16x16x32_bf16 v[152:155], a[76:79], a[60:63], v[152:155]\n" \
"v_cvt_pk_bf16_f32 v216, v144, v145\n" \
"v_cvt_pk_bf16_f32 v217, v146, v147\n" \
"v_mfma_f32_16x16x32_bf16 v[156:159], a[80:83], a[48:51], v[172:175]\n" \
"v_exp_f32_e64 v150, v150\n" \
"v_mfma_f32_16x16x32_bf16 v[156:159], a[84:87], a[52:55], v[156:159]\n" \
"ds_read_b128 a[116:119], v208 offset:45056\n" \
"s_waitcnt lgkmcnt(6)\n" \
"v_mfma_f32_16x16x32_bf16 v[156:159], a[88:91], a[56:59], v[156:159]\n" \
"buffer_load_dwordx4 v214, %[srdt], s49 offen offset:3072 lds\n" \
"v_mfma_f32_16x16x32_bf16 v[156:159], a[92:95], a[60:63], v[156:159]\n" \
"v_exp_f32_e32 v151, v151\n" \
"v_mfma_f32_16x16x32_bf16 v[16:19], a[176:179], v[192:195], v[16:19]\n" \
"ds_read_b128 a[120:123], v209 offset:45056\n" \
"v_mfma_f32_16x16x32_bf16 v[16:19], a[144:147], v[184:187], v[16:19]\n" \
"v_exp_f32_e64 v152, v152\n" \
"v_mfma_f32_16x16x32_bf16 v[20:23], a[180:183], v[192:195], v[20:23]\n" \
"v_cvt_pk_bf16_f32 v218, v148, v149\n" \
"v_cvt_pk_bf16_f32 v219, v150, v151\n" \
"v_mfma_f32_16x16x32_bf16 v[20:23], a[148:151], v[184:187], v[20:23]\n" \
"v_exp_f32_e64 v153, v153\n" \
"v_mfma_f32_16x16x32_bf16 v[24:27], a[184:187], v[192:195], v[24:27]\n" \
"v_exp_f32_e64 v154, v154\n" \
"v_mfma_f32_16x16x32_bf16 v[24:27], a[152:155], v[184:187], v[24:27]\n" \
"ds_read_b128 a[124:127], v210 offset:45056\n" \
"global_load_dwordx2 a[252:253], v229, %[px] offset:1024\n" \
"v_mfma_f32_16x16x32_bf16 v[28:31], a[188:191], v[192:195], v[28:31]\n" \
"v_exp_f32_e64 v155, v155\n" \
"v_mfma_f32_16x16x32_bf16 v[28:31], a[156:159], v[184:187], v[28:31]\n" \
"s_waitcnt vmcnt(63) lgkmcnt(2)\n" \
"s_barrier\n" \
"v_mfma_f32_16x16x32_bf16 v[128:131], a[96:99], a[0:3], v[160:163]\n" \
"v_exp_f32_e64 v156, v156\n" \
"s_lshl_b32 s62, %[wid], 13\n" \
"s_sub_u32 s62, %[m0t], s62\n" \
"v_mfma_f32_16x16x32_bf16 v[128:131], a[100:103], a[4:7], v[128:131]\n" \
"v_lshrrev_b32_e64 v227, 5, v231\n" \
"v_and_b32_e32 v230, 0xf0, v235\n" \
"v_add_u32_e64 v230, s62, v230\n" \
"v_mfma_f32_16x16x32_bf16 v[128:131], a[104:107], a[8:11], v[128:131]\n" \
"v_exp_f32_e64 v157, v157\n" \
"v_mfma_f32_16x16x32_bf16 v[128:131], a[108:111], a[12:15], v[128:131]\n" \
"v_cvt_pk_bf16_f32 v220, v152, v153\n" \
"v_cvt_pk_bf16_f32 v221, v154, v155\n" \
"ds_read_b128 v[232:235], v230 offset:7680\n" \
"v_readlane_b32 s51, %[thi], 31\n" \
"v_mfma_f32_16x16x32_bf16 v[132:135], a[112:115], a[0:3], v[160:163]\n" \
"v_exp_f32_e64 v158, v158\n" \
"v_mfma_f32_16x16x32_bf16 v[132:135], a[116:119], a[4:7], v[132:135]\n" \
"v_lshlrev_b32_e32 v227, 10, v227\n" \
"s_waitcnt lgkmcnt(1)\n" \
"v_mfma_f32_16x16x32_bf16 v[132:135], a[120:123], a[8:11], v[132:135]\n" \
"s_add_u32 m0, %[m0b], 81920\n" \
"s_mov_b32 s61, 917504\n" \
"buffer_load_dwordx4 %[vg0], s[64:67], s61 offen offset:0 lds\n" \
"v_mfma_f32_16x16x32_bf16 v[132:135], a[124:127], a[12:15], v[132:135]\n" \
"v_exp_f32_e64 v159, v159\n" \
"v_mfma_f32_16x16x32_bf16 v[32:35], a[160:163], v[196:199], v[32:35]\n" \
"ds_read_b128 a[64:67], %[kb0] offset:49152\n" \
"v_add_u32_e32 v227, 0x10000, v227\n" \
"v_mfma_f32_16x16x32_bf16 v[32:35], a[128:131], v[188:191], v[32:35]\n" \
"v_exp_f32_e64 v128, v128\n" \
"v_mfma_f32_16x16x32_bf16 v[36:39], a[164:167], v[196:199], v[36:39]\n" \
"v_cvt_pk_bf16_f32 v222, v156, v157\n" \
"v_cvt_pk_bf16_f32 v223, v158, v159\n" \
"s_waitcnt lgkmcnt(1)\n" \
"v_add_f32_e32 v232, v232, v233\n" \
"v_add_f32_e64 v234, v234, v235\n" \
"v_mfma_f32_16x16x32_bf16 v[36:39], a[132:135], v[188:191], v[36:39]\n" \
"v_exp_f32_e64 v129, v129\n" \
"v_mfma_f32_16x16x32_bf16 v[40:43], a[168:171], v[196:199], v[40:43]\n" \
"ds_read_b128 a[68:71], v208 offset:49152\n" \
"v_sub_u32_e32 v215, %[vvb], v227\n" \
"v_add_f32_e32 v232, v232, v234\n" \
"v_lshrrev_b32_e64 v230, 6, v224\n" \
"v_mfma_f32_16x16x32_bf16 v[40:43], a[136:139], v[188:191], v[40:43]\n" \
"v_exp_f32_e64 v130, v130\n" \
"v_mfma_f32_16x16x32_bf16 v[44:47], a[172:175], v[196:199], v[44:47]\n" \
"ds_read_b128 a[72:75], v209 offset:49152\n" \
"v_xor_b32_e64 v227, 32, v215\n" \
"v_mov_b32_dpp v233, v232 quad_perm:[1,0,3,2] row_mask:0xf bank_mask:0xf\n" \
"v_mfma_f32_16x16x32_bf16 v[44:47], a[140:143], v[188:191], v[44:47]\n" \
"v_exp_f32_e64 v131, v131\n" \
"v_smfmac_f32_16x16x64_bf16 v[176:179], a[192:195], v[184:191], v228\n" \
"global_load_dwordx2 a[242:243], %[vx0], %[px] offset:64\n" \
"v_mfma_f32_16x16x32_bf16 v[136:139], a[96:99], a[16:19], v[164:167]\n" \
"v_exp_f32_e64 v132, v132\n" \
"v_mfma_f32_16x16x32_bf16 v[136:139], a[100:103], a[20:23], v[136:139]\n" \
"ds_read_b128 a[76:79], v210 offset:49152\n" \
"v_cvt_pk_bf16_f32 v232, v232, v233\n" \
"v_mfma_f32_16x16x32_bf16 v[136:139], a[104:107], a[24:27], v[136:139]\n" \
"v_exp_f32_e64 v133, v133\n" \
"v_mfma_f32_16x16x32_bf16 v[136:139], a[108:111], a[28:31], v[136:139]\n" \
"v_cvt_pk_bf16_f32 v192, v128, v129\n" \
"v_cvt_pk_bf16_f32 v193, v130, v131\n" \
"s_cmp_eq_u32 %[wid], 0\n" \
"s_cselect_b32 s51, s51, 0x80000000\n" \
"s_mov_b64 exec, 0x5555\n" \
"buffer_store_dword v232, v230, %[srdz], s51 offen sc1\n" \
"s_mov_b64 exec, -1\n" \
"v_mfma_f32_16x16x32_bf16 v[140:143], a[112:115], a[16:19], v[164:167]\n" \
"v_exp_f32_e64 v134, v134\n" \
"v_mfma_f32_16x16x32_bf16 v[140:143], a[116:119], a[20:23], v[140:143]\n" \
"ds_read_b128 a[80:83], %[kb0] offset:53248\n" \
"v_mfma_f32_16x16x32_bf16 v[140:143], a[120:123], a[24:27], v[140:143]\n" \
"s_add_u32 m0, %[m0b], 82944\n" \
"s_mov_b32 s61, 1146880\n" \
"buffer_load_dwordx4 %[vg0], s[64:67], s61 offen offset:0 lds\n" \
"v_mfma_f32_16x16x32_bf16 v[140:143], a[124:127], a[28:31], v[140:143]\n" \
"v_exp_f32_e64 v135, v135\n" \
"v_mfma_f32_16x16x32_bf16 v[48:51], a[176:179], v[196:199], v[48:51]\n" \
"ds_read_b128 a[84:87], v208 offset:53248\n" \
"v_mfma_f32_16x16x32_bf16 v[48:51], a[144:147], v[188:191], v[48:51]\n" \
"v_exp_f32_e64 v136, v136\n" \
"v_mfma_f32_16x16x32_bf16 v[52:55], a[180:183], v[196:199], v[52:55]\n" \
"v_cvt_pk_bf16_f32 v194, v132, v133\n" \
"v_cvt_pk_bf16_f32 v195, v134, v135\n" \
"v_mfma_f32_16x16x32_bf16 v[52:55], a[148:151], v[188:191], v[52:55]\n" \
"v_exp_f32_e64 v137, v137\n" \
"v_mfma_f32_16x16x32_bf16 v[56:59], a[184:187], v[196:199], v[56:59]\n" \
"v_exp_f32_e64 v138, v138\n" \
"v_mfma_f32_16x16x32_bf16 v[56:59], a[152:155], v[188:191], v[56:59]\n" \
"ds_read_b128 a[88:91], v209 offset:53248\n" \
"v_mfma_f32_16x16x32_bf16 v[60:63], a[188:191], v[196:199], v[60:63]\n" \
"v_exp_f32_e64 v139, v139\n" \
"v_mfma_f32_16x16x32_bf16 v[60:63], a[156:159], v[188:191], v[60:63]\n" \
"global_load_dwordx2 a[246:247], %[vx0], %[px] offset:1088\n" \
"v_mfma_f32_16x16x32_bf16 v[144:147], a[96:99], a[32:35], v[168:171]\n" \
"v_exp_f32_e64 v140, v140\n" \
"v_mfma_f32_16x16x32_bf16 v[144:147], a[100:103], a[36:39], v[144:147]\n" \
"ds_read_b128 a[92:95], v210 offset:53248\n" \
"v_lshrrev_b32_e64 v236, 3, v224\n" \
"v_and_b32_e32 v230, 0xb0, v236\n" \
"v_mfma_f32_16x16x32_bf16 v[144:147], a[104:107], a[40:43], v[144:147]\n" \
"v_exp_f32_e64 v141, v141\n" \
"v_mfma_f32_16x16x32_bf16 v[144:147], a[108:111], a[44:47], v[144:147]\n" \
"v_cvt_pk_bf16_f32 v196, v136, v137\n" \
"v_cvt_pk_bf16_f32 v197, v138, v139\n" \
"v_lshrrev_b32_e64 v236, 2, v236\n" \
"v_and_or_b32 v230, v236, 64, v230\n" \
"v_mfma_f32_16x16x32_bf16 v[148:151], a[112:115], a[32:35], v[168:171]\n" \
"v_exp_f32_e64 v142, v142\n" \
"v_mfma_f32_16x16x32_bf16 v[148:151], a[116:119], a[36:39], v[148:151]\n" \
"v_lshrrev_b32_e64 v236, 12, v224\n" \
"v_lshl_or_b32 v230, v236, 13, v230\n" \
"v_mfma_f32_16x16x32_bf16 v[148:151], a[120:123], a[40:43], v[148:151]\n" \
"s_add_u32 m0, %[m0b], 83968\n" \
"s_mov_b32 s61, 1376256\n" \
"buffer_load_dwordx4 %[vg0], s[64:67], s61 offen offset:0 lds\n" \
"v_mfma_f32_16x16x32_bf16 v[148:151], a[124:127], a[44:47], v[148:151]\n" \
".if . - adaln_body != 36864\n" \
".error \"size model\"\n" \
".endif\n" \
"Lk8_%=:\n" \
"s_cbranch_execz Lk9_%=\n" \
"s_nop 0\n" \
"v_exp_f32_e64 v143, v143\n" \
"v_readlane_b32 s51, %[thi], 26\n" \
"v_add_u32_e64 v230, s62, v230\n" \
"v_mfma_f32_16x16x32_bf16 v[96:99], a[160:163], v[204:207], v[96:99]\n" \
"ds_read_b64_tr_b16 a[160:161], %[vvb] offset:40960\n" \
"v_mfma_f32_16x16x32_bf16 v[96:99], a[128:131], v[220:223], v[96:99]\n" \
"v_exp_f32_e64 v144, v144\n" \
"v_mfma_f32_16x16x32_bf16 v[100:103], a[164:167], v[204:207], v[100:103]\n" \
"v_cvt_pk_bf16_f32 v198, v140, v141\n" \
"v_cvt_pk_bf16_f32 v199, v142, v143\n" \
"v_add_u32_e64 v230, s51, v230\n" \
"v_mfma_f32_16x16x32_bf16 v[100:103], a[132:135], v[220:223], v[100:103]\n" \
"v_exp_f32_e64 v145, v145\n" \
"v_mfma_f32_16x16x32_bf16 v[104:107], a[168:171], v[204:207], v[104:107]\n" \
"ds_read_b64_tr_b16 a[162:163], %[vvb] offset:45056\n" \
"ds_read_b64_tr_b16 a[164:165], v211 offset:40960\n" \
"v_xor_b32_e32 v236, 0x100, v230\n" \
"v_mfma_f32_16x16x32_bf16 v[104:107], a[136:139], v[220:223], v[104:107]\n" \
"v_exp_f32_e64 v146, v146\n" \
"v_mfma_f32_16x16x32_bf16 v[108:111], a[172:175], v[204:207], v[108:111]\n" \
"ds_read_b64_tr_b16 a[166:167], v211 offset:45056\n" \
"ds_read_b128 a[212:215], v230 offset:6656\n" \
"v_mfma_f32_16x16x32_bf16 v[108:111], a[140:143], v[220:223], v[108:111]\n" \
"v_exp_f32_e64 v147, v147\n" \
"v_smfmac_f32_16x16x64_bf16 v[180:183], a[192:195], v[216:223], v228\n" \
"ds_read_b64_tr_b16 a[168:169], %[vvb] offset:41472\n" \
"global_load_dwordx2 a[250:251], v229, %[px] offset:64\n" \
"ds_read_b128 a[216:219], v230 offset:23040\n" \
"v_mfma_f32_16x16x32_bf16 v[152:155], a[96:99], a[48:51], v[172:175]\n" \
"v_exp_f32_e64 v148, v148\n" \
"v_mfma_f32_16x16x32_bf16 v[152:155], a[100:103], a[52:55], v[152:155]\n" \
"ds_read_b64_tr_b16 a[170:171], %[vvb] offset:45568\n" \
"ds_read_b128 a[220:223], v236 offset:6656\n" \
"v_mfma_f32_16x16x32_bf16 v[152:155], a[104:107], a[56:59], v[152:155]\n" \
"v_exp_f32_e64 v149, v149\n" \
"v_mfma_f32_16x16x32_bf16 v[152:155], a[108:111], a[60:63], v[152:155]\n" \
"v_cvt_pk_bf16_f32 v200, v144, v145\n" \
"v_cvt_pk_bf16_f32 v201, v146, v147\n" \
"ds_read_b128 a[224:227], v236 offset:23040\n" \
"v_mfma_f32_16x16x32_bf16 v[156:159], a[112:115], a[48:51], v[172:175]\n" \
"v_exp_f32_e64 v150, v150\n" \
"v_mfma_f32_16x16x32_bf16 v[156:159], a[116:119], a[52:55], v[156:159]\n" \
"ds_read_b64_tr_b16 a[172:173], v211 offset:41472\n" \
"v_mfma_f32_16x16x32_bf16 v[156:159], a[120:123], a[56:59], v[156:159]\n" \
"s_add_u32 m0, %[m0b], 84992\n" \
"s_mov_b32 s61, 1605632\n" \
"buffer_load_dwordx4 %[vg0], s[64:67], s61 offen offset:0 lds\n" \
"v_mfma_f32_16x16x32_bf16 v[156:159], a[124:127], a[60:63], v[156:159]\n" \
"v_exp_f32_e64 v151, v151\n" \
"v_mfma_f32_16x16x32_bf16 v[112:115], a[176:179], v[204:207], v[112:115]\n" \
"ds_read_b64_tr_b16 a[174:175], v211 offset:45568\n" \
"ds_read_b64_tr_b16 a[176:177], %[vvb] offset:41984\n" \
"v_mfma_f32_16x16x32_bf16 v[112:115], a[144:147], v[220:223], v[112:115]\n" \
"v_exp_f32_e64 v152, v152\n" \
"v_mfma_f32_16x16x32_bf16 v[116:119], a[180:183], v[204:207], v[116:119]\n" \
"v_cvt_pk_bf16_f32 v202, v148, v149\n" \
"v_cvt_pk_bf16_f32 v203, v150, v151\n" \
"v_mfma_f32_16x16x32_bf16 v[116:119], a[148:151], v[220:223], v[116:119]\n" \
"v_exp_f32_e64 v153, v153\n" \
"v_mfma_f32_16x16x32_bf16 v[120:123], a[184:187], v[204:207], v[120:123]\n" \
"v_exp_f32_e64 v154, v154\n" \
"v_mfma_f32_16x16x32_bf16 v[120:123], a[152:155], v[220:223], v[120:123]\n" \
"ds_read_b64_tr_b16 a[178:179], %[vvb] offset:46080\n" \
"global_load_dwordx2 a[254:255], v229, %[px] offset:1088\n" \
"v_mfma_f32_16x16x32_bf16 v[124:127], a[188:191], v[204:207], v[124:127]\n" \
"v_exp_f32_e64 v155, v155\n" \
"v_mfma_f32_16x16x32_bf16 v[124:127], a[156:159], v[220:223], v[124:127]\n" \
"ds_read_b64_tr_b16 a[180:181], v211 offset:41984\n" \
"s_waitcnt lgkmcnt(14)\n" \
"v_mfma_f32_16x16x32_bf16 v[128:131], a[64:67], a[0:3], v[160:163]\n" \
"v_exp_f32_e32 v156, v156\n" \
"v_mfma_f32_16x16x32_bf16 v[128:131], a[68:71], a[4:7], v[128:131]\n" \
"ds_read_b64_tr_b16 a[182:183], v211 offset:46080\n" \
"s_add_u32 m0, %[m0b], 32768\n" \
"s_cmp_gt_u32 s48, 0\n" \
"v_mfma_f32_16x16x32_bf16 v[128:131], a[72:75], a[8:11], v[128:131]\n" \
"v_exp_f32_e32 v157, v157\n" \
"v_mfma_f32_16x16x32_bf16 v[128:131], a[76:79], a[12:15], v[128:131]\n" \
"v_cvt_pk_bf16_f32 v204, v152, v153\n" \
"v_cvt_pk_bf16_f32 v205, v154, v155\n" \
"v_mfma_f32_16x16x32_bf16 v[132:135], a[80:83], a[0:3], v[160:163]\n" \
"v_exp_f32_e64 v158, v158\n" \
"v_mfma_f32_16x16x32_bf16 v[132:135], a[84:87], a[4:7], v[132:135]\n" \
"ds_read_b64_tr_b16 a[184:185], %[vvb] offset:42496\n" \
"s_cselect_b32 s49, 0, 1\n" \
"s_lshl_b32 s49, s49, 18\n" \
"s_or_b32 s49, s49, s58\n" \
"s_waitcnt lgkmcnt(5)\n" \
"v_mfma_f32_16x16x32_bf16 v[132:135], a[88:91], a[8:11], v[132:135]\n" \
"buffer_load_dwordx4 %[wo0], %[srdt], s49 offen offset:0 lds\n" \
"v_mfma_f32_16x16x32_bf16 v[132:135], a[92:95], a[12:15], v[132:135]\n" \
"v_exp_f32_e64 v159, v159\n" \
"v_mfma_f32_16x16x32_bf16 v[64:67], a[128:131], v[216:219], v[64:67]\n" \
"ds_read_b64_tr_b16 a[186:187], %[vvb] offset:46592\n" \
"ds_read_b64_tr_b16 a[188:189], v211 offset:42496\n" \
"v_mfma_f32_16x16x32_bf16 v[64:67], a[160:163], v[200:203], v[64:67]\n" \
"v_exp_f32_e64 v128, v128\n" \
"v_mfma_f32_16x16x32_bf16 v[68:71], a[132:135], v[216:219], v[68:71]\n" \
"v_cvt_pk_bf16_f32 v206, v156, v157\n" \
"v_cvt_pk_bf16_f32 v207, v158, v159\n" \
"v_mfma_f32_16x16x32_bf16 v[68:71], a[164:167], v[200:203], v[68:71]\n" \
"v_exp_f32_e64 v129, v129\n" \
"v_mfma_f32_16x16x32_bf16 v[72:75], a[136:139], v[216:219], v[72:75]\n" \
"ds_read_b64_tr_b16 a[190:191], v211 offset:46592\n" \
"ds_read_b64_tr_b16 a[128:129], %[vvb] offset:49152\n" \
"v_mfma_f32_16x16x32_bf16 v[72:75], a[168:171], v[200:203], v[72:75]\n" \
"v_exp_f32_e64 v130, v130\n" \
"v_mfma_f32_16x16x32_bf16 v[76:79], a[140:143], v[216:219], v[76:79]\n" \
"ds_read_b64_tr_b16 a[130:131], %[vvb] offset:53248\n" \
"ds_read_b64_tr_b16 a[132:133], v211 offset:49152\n" \
"v_mfma_f32_16x16x32_bf16 v[76:79], a[172:175], v[200:203], v[76:79]\n" \
"v_exp_f32_e64 v131, v131\n" \
"v_smfmac_f32_16x16x64_bf16 v[176:179], a[192:195], v[192:199], v228\n" \
"ds_read_b64_tr_b16 a[134:135], v211 offset:53248\n" \
"ds_read_b64_tr_b16 a[136:137], %[vvb] offset:49664\n" \
"v_mfma_f32_16x16x32_bf16 v[136:139], a[64:67], a[16:19], v[164:167]\n" \
"v_exp_f32_e64 v132, v132\n" \
"v_mfma_f32_16x16x32_bf16 v[136:139], a[68:71], a[20:23], v[136:139]\n" \
"ds_read_b64_tr_b16 a[138:139], %[vvb] offset:53760\n" \
"ds_read_b64_tr_b16 a[140:141], v211 offset:49664\n" \
"v_mfma_f32_16x16x32_bf16 v[136:139], a[72:75], a[24:27], v[136:139]\n" \
"v_exp_f32_e64 v133, v133\n" \
"v_mfma_f32_16x16x32_bf16 v[136:139], a[76:79], a[28:31], v[136:139]\n" \
"v_cvt_pk_bf16_f32 v184, v128, v129\n" \
"v_cvt_pk_bf16_f32 v185, v130, v131\n" \
"v_mfma_f32_16x16x32_bf16 v[140:143], a[80:83], a[16:19], v[164:167]\n" \
"v_exp_f32_e64 v134, v134\n" \
"v_mfma_f32_16x16x32_bf16 v[140:143], a[84:87], a[20:23], v[140:143]\n" \
"ds_read_b64_tr_b16 a[142:143], v211 offset:53760\n" \
"s_waitcnt lgkmcnt(8)\n" \
"v_mfma_f32_16x16x32_bf16 v[140:143], a[88:91], a[24:27], v[140:143]\n" \
"buffer_load_dwordx4 v212, %[srdt], s49 offen offset:1024 lds\n" \
"v_mfma_f32_16x16x32_bf16 v[140:143], a[92:95], a[28:31], v[140:143]\n" \
"v_exp_f32_e32 v135, v135\n" \
"v_mfma_f32_16x16x32_bf16 v[80:83], a[144:147], v[216:219], v[80:83]\n" \
"ds_read_b64_tr_b16 a[144:145], %[vvb] offset:50176\n" \
"ds_read_b64_tr_b16 a[146:147], %[vvb] offset:54272\n" \
"v_mfma_f32_16x16x32_bf16 v[80:83], a[176:179], v[200:203], v[80:83]\n" \
"v_exp_f32_e64 v136, v136\n" \
"v_mfma_f32_16x16x32_bf16 v[84:87], a[148:151], v[216:219], v[84:87]\n" \
"v_cvt_pk_bf16_f32 v186, v132, v133\n" \
"v_cvt_pk_bf16_f32 v187, v134, v135\n" \
"v_mfma_f32_16x16x32_bf16 v[84:87], a[180:183], v[200:203], v[84:87]\n" \
"v_exp_f32_e64 v137, v137\n" \
"v_mfma_f32_16x16x32_bf16 v[88:91], a[152:155], v[216:219], v[88:91]\n" \
"v_exp_f32_e64 v138, v138\n" \
"v_mfma_f32_16x16x32_bf16 v[88:91], a[184:187], v[200:203], v[88:91]\n" \
"ds_read_b64_tr_b16 a[148:149], v211 offset:50176\n" \
"ds_read_b64_tr_b16 a[150:151], v211 offset:54272\n" \
"v_readlane_b32 s64, %[tlo], 9\n" \
"v_mfma_f32_16x16x32_bf16 v[92:95], a[156:159], v[216:219], v[92:95]\n" \
"v_exp_f32_e64 v139, v139\n" \
"v_mfma_f32_16x16x32_bf16 v[92:95], a[188:191], v[200:203], v[92:95]\n" \
"ds_read_b64_tr_b16 a[152:153], %[vvb] offset:50688\n" \
"v_readlane_b32 s65, %[thi], 9\n" \
"v_mfma_f32_16x16x32_bf16 v[144:147], a[64:67], a[32:35], v[168:171]\n" \
"v_exp_f32_e64 v140, v140\n" \
"v_mfma_f32_16x16x32_bf16 v[144:147], a[68:71], a[36:39], v[144:147]\n" \
"ds_read_b64_tr_b16 a[154:155], %[vvb] offset:54784\n" \
"ds_read_b64_tr_b16 a[156:157], v211 offset:50688\n" \
"v_mfma_f32_16x16x32_bf16 v[144:147], a[72:75], a[40:43], v[144:147]\n" \
"v_exp_f32_e64 v141, v141\n" \
"v_mfma_f32_16x16x32_bf16 v[144:147], a[76:79], a[44:47], v[144:147]\n" \
"v_cvt_pk_bf16_f32 v188, v136, v137\n" \
"v_cvt_pk_bf16_f32 v189, v138, v139\n" \
"v_mfma_f32_16x16x32_bf16 v[148:151], a[80:83], a[32:35], v[168:171]\n" \
"v_exp_f32_e64 v142, v142\n" \
"v_mfma_f32_16x16x32_bf16 v[148:151], a[84:87], a[36:39], v[148:151]\n" \
"ds_read_b64_tr_b16 a[158:159], v211 offset:54784\n" \
"s_waitcnt lgkmcnt(12)\n" \
"v_mfma_f32_16x16x32_bf16 v[148:151], a[88:91], a[40:43], v[148:151]\n" \
"buffer_load_dwordx4 v213, %[srdt], s49 offen offset:2048 lds\n" \
"v_mfma_f32_16x16x32_bf16 v[148:151], a[92:95], a[44:47], v[148:151]\n" \
"v_exp_f32_e32 v143, v143\n" \
"v_mfma_f32_16x16x32_bf16 v[0:3], a[160:163], v[192:195], v[0:3]\n" \
"ds_read_b128 a[96:99], %[kb0] offset:57344\n" \
"v_mfma_f32_16x16x32_bf16 v[0:3], a[128:131], v[184:187], v[0:3]\n" \
"v_exp_f32_e64 v144, v144\n" \
"v_mfma_f32_16x16x32_bf16 v[4:7], a[164:167], v[192:195], v[4:7]\n" \
"v_cvt_pk_bf16_f32 v190, v140, v141\n" \
"v_cvt_pk_bf16_f32 v191, v142, v143\n" \
"v_mfma_f32_16x16x32_bf16 v[4:7], a[132:135], v[184:187], v[4:7]\n" \
"v_exp_f32_e64 v145, v145\n" \
"v_mfma_f32_16x16x32_bf16 v[8:11], a[168:171], v[192:195], v[8:11]\n" \
"ds_read_b128 a[100:103], v208 offset:57344\n" \
"s_waitcnt vmcnt(3) lgkmcnt(10)\n" \
"v_mfma_f32_16x16x32_bf16 v[232:235], a[240:243], a[212:215], 0\n" \
"v_mfma_f32_16x16x32_bf16 v[8:11], a[136:139], v[184:187], v[8:11]\n" \
"v_exp_f32_e32 v146, v146\n" \
"v_mfma_f32_16x16x32_bf16 v[12:15], a[172:175], v[192:195], v[12:15]\n" \
"ds_read_b128 a[104:107], v209 offset:57344\n" \
"v_mfma_f32_16x16x32_bf16 v[232:235], a[244:247], a[216:219], v[232:235]\n" \
"v_mfma_f32_16x16x32_bf16 v[12:15], a[140:143], v[184:187], v[12:15]\n" \
"v_exp_f32_e64 v147, v147\n" \
"v_smfmac_f32_16x16x64_bf16 v[180:183], a[192:195], v[200:207], v228\n" \
"ds_read_b128 a[108:111], v210 offset:57344\n" \
"buffer_load_dword v229, v226, %[srdz], s50 offen sc1\n" \
"v_mfma_f32_16x16x32_bf16 v[232:235], a[248:251], a[220:223], v[232:235]\n" \
"v_mfma_f32_16x16x32_bf16 v[152:155], a[64:67], a[48:51], v[172:175]\n" \
"v_exp_f32_e64 v148, v148\n" \
"v_mfma_f32_16x16x32_bf16 v[152:155], a[68:71], a[52:55], v[152:155]\n" \
"ds_read_b128 a[112:115], %[kb0] offset:61440\n" \
"v_mfma_f32_16x16x32_bf16 v[232:235], a[252:255], a[224:227], v[232:235]\n" \
"v_mfma_f32_16x16x32_bf16 v[152:155], a[72:75], a[56:59], v[152:155]\n" \
"v_exp_f32_e64 v149, v149\n" \
"v_mfma_f32_16x16x32_bf16 v[152:155], a[76:79], a[60:63], v[152:155]\n" \
"v_cvt_pk_bf16_f32 v216, v144, v145\n" \
"v_cvt_pk_bf16_f32 v217, v146, v147\n" \
"v_mfma_f32_16x16x32_bf16 v[156:159], a[80:83], a[48:51], v[172:175]\n" \
"v_exp_f32_e64 v150, v150\n" \
"v_mfma_f32_16x16x32_bf16 v[156:159], a[84:87], a[52:55], v[156:159]\n" \
"ds_read_b128 a[116:119], v208 offset:61440\n" \
"s_mov_b32 s68, 0xaaaaaaaa\n" \
"s_mov_b32 s69, 0xaaaaaaaa\n" \
"s_waitcnt lgkmcnt(6)\n" \
"v_mfma_f32_16x16x32_bf16 v[156:159], a[88:91], a[56:59], v[156:159]\n" \
"buffer_load_dwordx4 v214, %[srdt], s49 offen offset:3072 lds\n" \
"v_mfma_f32_16x16x32_bf16 v[156:159], a[92:95], a[60:63], v[156:159]\n" \
"v_exp_f32_e32 v151, v151\n" \
"v_cndmask_b32_e64 v232, v232, v233, s[68:69]\n" \
"v_cndmask_b32_e64 v234, v234, v235, s[68:69]\n" \
"v_mfma_f32_16x16x32_bf16 v[16:19], a[176:179], v[192:195], v[16:19]\n" \
"ds_read_b128 a[120:123], v209 offset:61440\n" \
"v_mfma_f32_16x16x32_bf16 v[16:19], a[144:147], v[184:187], v[16:19]\n" \
"v_exp_f32_e64 v152, v152\n" \
"s_mov_b32 s68, 0xcccccccc\n" \
"s_mov_b32 s69, 0xcccccccc\n" \
"v_mfma_f32_16x16x32_bf16 v[20:23], a[180:183], v[192:195], v[20:23]\n" \
"v_cvt_pk_bf16_f32 v218, v148, v149\n" \
"v_cvt_pk_bf16_f32 v219, v150, v151\n" \
"v_mfma_f32_16x16x32_bf16 v[20:23], a[148:151], v[184:187], v[20:23]\n" \
"v_exp_f32_e64 v153, v153\n" \
"v_cndmask_b32_e64 v232, v232, v234, s[68:69]\n" \
"v_mfma_f32_16x16x32_bf16 v[24:27], a[184:187], v[192:195], v[24:27]\n" \
"v_exp_f32_e64 v154, v154\n" \
"s_mov_b32 s68, 0xf0000f\n" \
"s_mov_b32 s69, 0xf0000f00\n" \
"v_mfma_f32_16x16x32_bf16 v[24:27], a[152:155], v[184:187], v[24:27]\n" \
"ds_read_b128 a[124:127], v210 offset:61440\n" \
"v_mfma_f32_16x16x32_bf16 v[28:31], a[188:191], v[192:195], v[28:31]\n" \
"v_exp_f32_e64 v155, v155\n" \
"v_cndmask_b32_e64 %[rd0], 0, v232, s[68:69]\n" \
"v_mfma_f32_16x16x32_bf16 v[28:31], a[156:159], v[184:187], v[28:31]\n" \
"s_waitcnt vmcnt(27) lgkmcnt(2)\n" \
"s_barrier\n" \
"v_mfma_f32_16x16x32_bf16 v[128:131], a[96:99], a[0:3], v[160:163]\n" \
"v_exp_f32_e64 v156, v156\n" \
"s_mov_b32 m0, s44\n" \
"s_addk_i32 s44, 0x400\n" \
"buffer_load_dwordx4 %[vg0], s[64:67], s45 offen offset:0 lds\n" \
"s_add_u32 s45, s45, 229376\n" \
"v_mfma_f32_16x16x32_bf16 v[128:131], a[100:103], a[4:7], v[128:131]\n" \
"s_add_u32 m0, %[m0b], 98304\n" \
"s_cmp_gt_u32 s48, 0\n" \
"v_mfma_f32_16x16x32_bf16 v[128:131], a[104:107], a[8:11], v[128:131]\n" \
"v_exp_f32_e32 v157, v157\n" \
"v_mfma_f32_16x16x32_bf16 v[128:131], a[108:111], a[12:15], v[128:131]\n" \
"v_cvt_pk_bf16_f32 v220, v152, v153\n" \
"v_cvt_pk_bf16_f32 v221, v154, v155\n" \
"v_mfma_f32_16x16x32_bf16 v[132:135], a[112:115], a[0:3], v[160:163]\n" \
"v_exp_f32_e64 v158, v158\n" \
"v_mfma_f32_16x16x32_bf16 v[132:135], a[116:119], a[4:7], v[132:135]\n" \
"s_cselect_b32 s49, 0, 1\n" \
"s_lshl_b32 s49, s49, 18\n" \
"s_or_b32 s49, s49, s59\n" \
"s_waitcnt lgkmcnt(0)\n" \
"v_mfma_f32_16x16x32_bf16 v[132:135], a[120:123], a[8:11], v[132:135]\n" \
"buffer_load_dwordx4 %[wo0], %[srdt], s49 offen offset:0 lds\n" \
"v_mfma_f32_16x16x32_bf16 v[132:135], a[124:127], a[12:15], v[132:135]\n" \
"v_exp_f32_e64 v159, v159\n" \
"v_mfma_f32_16x16x32_bf16 v[32:35], a[160:163], v[196:199], v[32:35]\n" \
"ds_read_b64_tr_b16 a[196:197], v215 offset:0\n" \
"v_readlane_b32 s62, %[tlo], 32\n" \
"v_lshrrev_b32_e64 v237, 7, v224\n" \
"v_mfma_f32_16x16x32_bf16 v[32:35], a[128:131], v[188:191], v[32:35]\n" \
"v_exp_f32_e64 v128, v128\n" \
"v_mfma_f32_16x16x32_bf16 v[36:39], a[164:167], v[196:199], v[36:39]\n" \
"v_cvt_pk_bf16_f32 v222, v156, v157\n" \
"v_cvt_pk_bf16_f32 v223, v158, v159\n" \
"v_mfma_f32_16x16x32_bf16 v[36:39], a[132:135], v[188:191], v[36:39]\n" \
"v_exp_f32_e64 v129, v129\n" \
"v_mfma_f32_16x16x32_bf16 v[40:43], a[168:171], v[196:199], v[40:43]\n" \
"ds_read_b64_tr_b16 a[198:199], v215 offset:2048\n" \
"v_and_b32_e64 v237, 48, v237\n" \
"v_mfma_f32_16x16x32_bf16 v[40:43], a[136:139], v[188:191], v[40:43]\n" \
"v_exp_f32_e64 v130, v130\n" \
"v_mfma_f32_16x16x32_bf16 v[44:47], a[172:175], v[196:199], v[44:47]\n" \
"ds_read_b64_tr_b16 a[200:201], v215 offset:4096\n" \
"v_mfma_f32_16x16x32_bf16 v[44:47], a[140:143], v[188:191], v[44:47]\n" \
"v_exp_f32_e64 v131, v131\n" \
"v_smfmac_f32_16x16x64_bf16 v[176:179], a[192:195], v[184:191], v228\n" \
"v_add_u32_e64 v237, s62, v237\n" \
"v_mfma_f32_16x16x32_bf16 v[136:139], a[96:99], a[16:19], v[164:167]\n" \
"v_exp_f32_e64 v132, v132\n" \
"v_mfma_f32_16x16x32_bf16 v[136:139], a[100:103], a[20:23], v[136:139]\n" \
"ds_read_b64_tr_b16 a[202:203], v215 offset:6144\n" \
"v_mfma_f32_16x16x32_bf16 v[136:139], a[104:107], a[24:27], v[136:139]\n" \
"v_exp_f32_e64 v133, v133\n" \
"v_mfma_f32_16x16x32_bf16 v[136:139], a[108:111], a[28:31], v[136:139]\n" \
"v_cvt_pk_bf16_f32 v192, v128, v129\n" \
"v_cvt_pk_bf16_f32 v193, v130, v131\n" \
"v_mfma_f32_16x16x32_bf16 v[140:143], a[112:115], a[16:19], v[164:167]\n" \
"v_exp_f32_e64 v134, v134\n" \
"v_mfma_f32_16x16x32_bf16 v[140:143], a[116:119], a[20:23], v[140:143]\n" \
"ds_read_b64_tr_b16 a[204:205], v215 offset:8192\n" \
"v_mfma_f32_16x16x32_bf16 v[140:143], a[120:123], a[24:27], v[140:143]\n" \
"buffer_load_dwordx4 v212, %[srdt], s49 offen offset:1024 lds\n" \
"v_mfma_f32_16x16x32_bf16 v[140:143], a[124:127], a[28:31], v[140:143]\n" \
"v_exp_f32_e64 v135, v135\n" \
"v_mfma_f32_16x16x32_bf16 v[48:51], a[176:179], v[196:199], v[48:51]\n" \
"ds_read_b64_tr_b16 a[206:207], v215 offset:10240\n" \
"v_add_u32_e32 v208, 0x1000, %[oq0]\n" \
"v_mfma_f32_16x16x32_bf16 v[48:51], a[144:147], v[188:191], v[48:51]\n" \
"v_exp_f32_e64 v136, v136\n" \
"v_mfma_f32_16x16x32_bf16 v[52:55], a[180:183], v[196:199], v[52:55]\n" \
"v_cvt_pk_bf16_f32 v194, v132, v133\n" \
"v_cvt_pk_bf16_f32 v195, v134, v135\n" \
"v_mfma_f32_16x16x32_bf16 v[52:55], a[148:151], v[188:191], v[52:55]\n" \
"v_exp_f32_e64 v137, v137\n" \
"v_mfma_f32_16x16x32_bf16 v[56:59], a[184:187], v[196:199], v[56:59]\n" \
"v_exp_f32_e64 v138, v138\n" \
"v_mfma_f32_16x16x32_bf16 v[56:59], a[152:155], v[188:191], v[56:59]\n" \
"ds_read_b64_tr_b16 a[208:209], v215 offset:12288\n" \
"v_add_u32_e32 v209, 0x2000, %[oq0]\n" \
"v_mfma_f32_16x16x32_bf16 v[60:63], a[188:191], v[196:199], v[60:63]\n" \
"v_exp_f32_e64 v139, v139\n" \
"v_mfma_f32_16x16x32_bf16 v[60:63], a[156:159], v[188:191], v[60:63]\n" \
"v_add_u32_e32 v210, 0x3000, %[oq0]\n" \
"v_mfma_f32_16x16x32_bf16 v[144:147], a[96:99], a[32:35], v[168:171]\n" \
"v_exp_f32_e64 v140, v140\n" \
"v_mfma_f32_16x16x32_bf16 v[144:147], a[100:103], a[36:39], v[144:147]\n" \
"ds_read_b64_tr_b16 a[210:211], v215 offset:14336\n" \
"v_mfma_f32_16x16x32_bf16 v[144:147], a[104:107], a[40:43], v[144:147]\n" \
"v_exp_f32_e64 v141, v141\n" \
"v_mfma_f32_16x16x32_bf16 v[144:147], a[108:111], a[44:47], v[144:147]\n" \
"v_cvt_pk_bf16_f32 v196, v136, v137\n" \
"v_cvt_pk_bf16_f32 v197, v138, v139\n" \
"v_mfma_f32_16x16x32_bf16 v[148:151], a[112:115], a[32:35], v[168:171]\n" \
"v_exp_f32_e64 v142, v142\n" \
"v_mfma_f32_16x16x32_bf16 v[148:151], a[116:119], a[36:39], v[148:151]\n" \
"v_mfma_f32_16x16x32_bf16 v[148:151], a[120:123], a[40:43], v[148:151]\n" \
"buffer_load_dwordx4 v213, %[srdt], s49 offen offset:2048 lds\n" \
"v_mfma_f32_16x16x32_bf16 v[148:151], a[124:127], a[44:47], v[148:151]\n" \
"v_exp_f32_e64 v143, v143\n" \
"v_mfma_f32_16x16x32_bf16 v[96:99], a[160:163], v[204:207], v[96:99]\n" \
"ds_read_b64_tr_b16 a[160:161], %[vvb] offset:57344\n" \
"v_mfma_f32_16x16x32_bf16 v[96:99], a[128:131], v[220:223], v[96:99]\n" \
"v_exp_f32_e64 v144, v144\n" \
"v_mfma_f32_16x16x32_bf16 v[100:103], a[164:167], v[204:207], v[100:103]\n" \
"v_cvt_pk_bf16_f32 v198, v140, v141\n" \
"v_cvt_pk_bf16_f32 v199, v142, v143\n" \
"v_mfma_f32_16x16x32_bf16 v[100:103], a[132:135], v[220:223], v[100:103]\n" \
"v_exp_f32_e64 v145, v145\n" \
"v_mfma_f32_16x16x32_bf16 v[104:107], a[168:171], v[204:207], v[104:107]\n" \
"ds_read_b64_tr_b16 a[162:163], %[vvb] offset:61440\n" \
"ds_read_b64_tr_b16 a[164:165], v211 offset:57344\n" \
"v_mfma_f32_16x16x32_bf16 v[104:107], a[136:139], v[220:223], v[104:107]\n" \
"v_exp_f32_e64 v146, v146\n" \
"v_mfma_f32_16x16x32_bf16 v[108:111], a[172:175], v[204:207], v[108:111]\n" \
"ds_read_b64_tr_b16 a[166:167], v211 offset:61440\n" \
"v_mfma_f32_16x16x32_bf16 v[108:111], a[140:143], v[220:223], v[108:111]\n" \
"v_exp_f32_e64 v147, v147\n" \
"v_smfmac_f32_16x16x64_bf16 v[180:183], a[192:195], v[216:223], v228\n" \
"ds_read_b64_tr_b16 a[168:169], %[vvb] offset:57856\n" \
"v_mfma_f32_16x16x32_bf16 v[152:155], a[96:99], a[48:51], v[172:175]\n" \
"v_exp_f32_e64 v148, v148\n" \
"v_mfma_f32_16x16x32_bf16 v[152:155], a[100:103], a[52:55], v[152:155]\n" \
"ds_read_b64_tr_b16 a[170:171], %[vvb] offset:61952\n" \
"v_mfma_f32_16x16x32_bf16 v[152:155], a[104:107], a[56:59], v[152:155]\n" \
"v_exp_f32_e64 v149, v149\n" \
"v_mfma_f32_16x16x32_bf16 v[152:155], a[108:111], a[60:63], v[152:155]\n" \
"v_cvt_pk_bf16_f32 v200, v144, v145\n" \
"v_cvt_pk_bf16_f32 v201, v146, v147\n" \
"v_mfma_f32_16x16x32_bf16 v[156:159], a[112:115], a[48:51], v[172:175]\n" \
"v_exp_f32_e64 v150, v150\n" \
"v_mfma_f32_16x16x32_bf16 v[156:159], a[116:119], a[52:55], v[156:159]\n" \
"ds_read_b64_tr_b16 a[172:173], v211 offset:57856\n" \
"v_mfma_f32_16x16x32_bf16 v[156:159], a[120:123], a[56:59], v[156:159]\n" \
"buffer_load_dwordx4 v214, %[srdt], s49 offen offset:3072 lds\n" \
"v_mfma_f32_16x16x32_bf16 v[156:159], a[124:127], a[60:63], v[156:159]\n" \
"v_exp_f32_e64 v151, v151\n" \
"v_mfma_f32_16x16x32_bf16 v[112:115], a[176:179], v[204:207], v[112:115]\n" \
"ds_read_b64_tr_b16 a[174:175], v211 offset:61952\n" \
"ds_read_b64_tr_b16 a[176:177], %[vvb] offset:58368\n" \
"v_mfma_f32_16x16x32_bf16 v[112:115], a[144:147], v[220:223], v[112:115]\n" \
"v_exp_f32_e64 v152, v152\n" \
"s_mov_b32 m0, s44\n" \
"s_addk_i32 s44, 0x400\n" \
"buffer_load_dwordx4 %[vg0], s[64:67], s45 offen offset:0 lds\n" \
"s_add_u32 s45, s45, 229376\n" \
"v_mfma_f32_16x16x32_bf16 v[116:119], a[180:183], v[204:207], v[116:119]\n" \
"v_cvt_pk_bf16_f32 v202, v148, v149\n" \
"v_cvt_pk_bf16_f32 v203, v150, v151\n" \
"v_mfma_f32_16x16x32_bf16 v[116:119], a[148:151], v[220:223], v[116:119]\n" \
"v_exp_f32_e64 v153, v153\n" \
"v_mfma_f32_16x16x32_bf16 v[120:123], a[184:187], v[204:207], v[120:123]\n" \
"v_exp_f32_e64 v154, v154\n" \
"v_mfma_f32_16x16x32_bf16 v[120:123], a[152:155], v[220:223], v[120:123]\n" \
"ds_read_b64_tr_b16 a[178:179], %[vvb] offset:62464\n" \
"s_waitcnt vmcnt(7)\n" \
"v_cmp_eq_u32_e64 s[56:57], 0, v229\n" \
"s_cmp_lg_u64 s[56:57], 0\n" \
"s_cbranch_scc1 Lspk_%=\n" \
"Lbkk_%=:\n" \
"v_mfma_f32_16x16x32_bf16 v[124:127], a[188:191], v[204:207], v[124:127]\n" \
"v_exp_f32_e32 v155, v155\n" \
"v_mfma_f32_16x16x32_bf16 v[124:127], a[156:159], v[220:223], v[124:127]\n" \
"ds_read_b64_tr_b16 a[180:181], v211 offset:58368\n" \
"ds_read_b64_tr_b16 a[182:183], v211 offset:62464\n" \
"ds_read_b64_tr_b16 a[184:185], %[vvb] offset:58880\n" \
"ds_read_b64_tr_b16 a[186:187], %[vvb] offset:62976\n" \
"ds_read_b64_tr_b16 a[188:189], v211 offset:58880\n" \
"ds_read_b64_tr_b16 a[190:191], v211 offset:62976\n" \
"s_waitcnt lgkmcnt(8)\n" \
"s_nop 0\n" \
"v_smfmac_f32_16x16x64_bf16 v[176:179], a[192:195], v[192:199], v228\n" \
"v_exp_f32_e64 v156, v156\n" \
"ds_read_b64_tr_b16 a[212:213], v227 offset:0\n" \
"v_mfma_f32_16x16x32_bf16 v[0:3], a[160:163], v[192:195], v[0:3]\n" \
"v_exp_f32_e64 v157, v157\n" \
"ds_read_b64_tr_b16 a[214:215], v227 offset:2048\n" \
"v_mfma_f32_16x16x32_bf16 v[4:7], a[164:167], v[192:195], v[4:7]\n" \
"v_cvt_pk_bf16_f32 v204, v152, v153\n" \
"ds_read_b64_tr_b16 a[216:217], v227 offset:4096\n" \
"v_mfma_f32_16x16x32_bf16 v[8:11], a[168:171], v[192:195], v[8:11]\n" \
"v_cvt_pk_bf16_f32 v205, v154, v155\n" \
"ds_read_b64_tr_b16 a[218:219], v227 offset:6144\n" \
"v_mfma_f32_16x16x32_bf16 v[12:15], a[172:175], v[192:195], v[12:15]\n" \
"v_exp_f32_e64 v158, v158\n" \
"ds_read_b64_tr_b16 a[220:221], v227 offset:8192\n" \
"s_waitcnt lgkmcnt(5)\n" \
"s_nop 0\n" \
"v_mfma_f32_16x16x32_bf16 v[16:19], a[176:179], v[192:195], v[16:19]\n" \
"v_exp_f32_e64 v159, v159\n" \
"ds_read_b64_tr_b16 a[222:223], v227 offset:10240\n" \
"v_mfma_f32_16x16x32_bf16 v[20:23], a[180:183], v[192:195], v[20:23]\n" \
"v_cvt_pk_bf16_f32 v206, v156, v157\n" \
"ds_read_b64_tr_b16 a[224:225], v227 offset:12288\n" \
"v_mfma_f32_16x16x32_bf16 v[24:27], a[184:187], v[192:195], v[24:27]\n" \
"v_cvt_pk_bf16_f32 v207, v158, v159\n" \
"ds_read_b64_tr_b16 a[226:227], v227 offset:14336\n" \
"v_mfma_f32_16x16x32_bf16 v[28:31], a[188:191], v[192:195], v[28:31]\n" \
"v_add_f32_e64 v176, v176, v177\n" \
"v_smfmac_f32_16x16x64_bf16 v[180:183], a[192:195], v[200:207], v228\n" \
"v_add_f32_e32 v178, v178, v179\n" \
"v_add_f32_e32 v231, v176, v178\n" \
"v_rcp_f32_e32 v176, v176\n" \
"v_rcp_f32_e32 v178, v178\n" \
"v_mfma_f32_16x16x32_bf16 v[32:35], a[160:163], v[196:199], v[32:35]\n" \
"v_pk_mul_f32 v[0:1], v[0:1], v[176:177] op_sel_hi:[1,0]\n" \
"v_pk_mul_f32 v[2:3], v[2:3], v[176:177] op_sel_hi:[1,0]\n" \
"v_pk_mul_f32 v[4:5], v[4:5], v[176:177] op_sel_hi:[1,0]\n" \
"v_pk_mul_f32 v[6:7], v[6:7], v[176:177] op_sel_hi:[1,0]\n" \
"v_cvt_pk_bf16_f32 v0, v0, v1\n" \
"v_cvt_pk_bf16_f32 v1, v2, v3\n" \
"v_cvt_pk_bf16_f32 v2, v4, v5\n" \
"v_mfma_f32_16x16x32_bf16 v[36:39], a[164:167], v[196:199], v[36:39]\n" \
"v_cvt_pk_bf16_f32 v3, v6, v7\n" \
"s_mov_b32 s55, 0x4e800000\n" \
"v_mov_b32_e32 v236, %[flag]\n" \
"v_add_f32_e32 v180, v180, v181\n" \
"v_add_f32_e32 v182, v182, v183\n" \
"v_add_f32_e32 v231, v231, v180\n" \
"v_add_f32_e64 v231, v231, v182\n" \
"v_mfma_f32_16x16x32_bf16 v[40:43], a[168:171], v[196:199], v[40:43]\n" \
"v_rcp_f32_e64 v180, v180\n" \
"v_cmp_nle_f32_e64 s[56:57], v231, s55\n" \
"v_rcp_f32_e32 v182, v182\n" \
"s_nop 3\n" \
"s_cmp_lg_u64 s[56:57], 0\n" \
"s_cselect_b32 s55, 1, 0\n" \
"v_mov_b32_e64 v230, s55\n" \
"v_mfma_f32_16x16x32_bf16 v[44:47], a[172:175], v[196:199], v[44:47]\n" \
"s_mov_b32 s69, s55\n" \
"s_andn2_b32 s55, s55, s52\n" \
"s_cselect_b64 vcc, 0, vcc\n" \
"s_nop 0\n" \
"ds_write_b32 v236, v230\n" \
"v_and_b32_e32 v236, -16, v236\n" \
"s_cbranch_vccz Lg37_%=\n" \
"buffer_store_dwordx4 v[0:3], %[oq0], %[srdz], s60 offen offset:0 sc1\n" \
"Lg37_%=:\n" \
"v_pk_mul_f32 v[8:9], v[8:9], v[176:177] op_sel_hi:[1,0]\n" \
"v_mfma_f32_16x16x32_bf16 v[48:51], a[176:179], v[196:199], v[48:51]\n" \
"v_pk_mul_f32 v[10:11], v[10:11], v[176:177] op_sel_hi:[1,0]\n" \
"v_pk_mul_f32 v[12:13], v[12:13], v[176:177] op_sel_hi:[1,0]\n" \
"v_pk_mul_f32 v[14:15], v[14:15], v[176:177] op_sel_hi:[1,0]\n" \
"v_cvt_pk_bf16_f32 v8, v8, v9\n" \
"v_cvt_pk_bf16_f32 v9, v10, v11\n" \
"v_cvt_pk_bf16_f32 v10, v12, v13\n" \
"v_cvt_pk_bf16_f32 v11, v14, v15\n" \
"v_mfma_f32_16x16x32_bf16 v[52:55], a[180:183], v[196:199], v[52:55]\n" \
"s_cbranch_vccz Lg38_%=\n" \
"s_nop 0\n" \
"buffer_store_dwordx4 v[8:11], %[oq0], %[srdz], s60 offen offset:1024 sc1\n" \
"Lg38_%=:\n" \
"v_pk_mul_f32 v[16:17], v[16:17], v[176:177] op_sel_hi:[1,0]\n" \
"v_pk_mul_f32 v[18:19], v[18:19], v[176:177] op_sel_hi:[1,0]\n" \
"v_pk_mul_f32 v[20:21], v[20:21], v[176:177] op_sel_hi:[1,0]\n" \
"v_pk_mul_f32 v[22:23], v[22:23], v[176:177] op_sel_hi:[1,0]\n" \
"v_cvt_pk_bf16_f32 v16, v16, v17\n" \
"v_cvt_pk_bf16_f32 v17, v18, v19\n" \
"v_mfma_f32_16x16x32_bf16 v[56:59], a[184:187], v[196:199], v[56:59]\n" \
"v_cvt_pk_bf16_f32 v18, v20, v21\n" \
"v_cvt_pk_bf16_f32 v19, v22, v23\n" \
"s_cbranch_vccz Lg39_%=\n" \
"s_nop 0\n" \
"buffer_store_dwordx4 v[16:19], %[oq0], %[srdz], s60 offen offset:2048 sc1\n" \
"Lg39_%=:\n" \
"v_pk_mul_f32 v[24:25], v[24:25], v[176:177] op_sel_hi:[1,0]\n" \
"v_pk_mul_f32 v[26:27], v[26:27], v[176:177] op_sel_hi:[1,0]\n" \
"v_pk_mul_f32 v[28:29], v[28:29], v[176:177] op_sel_hi:[1,0]\n" \
"v_pk_mul_f32 v[30:31], v[30:31], v[176:177] op_sel_hi:[1,0]\n" \
"v_mfma_f32_16x16x32_bf16 v[60:63], a[188:191], v[196:199], v[60:63]\n" \
"v_cvt_pk_bf16_f32 v24, v24, v25\n" \
"v_cvt_pk_bf16_f32 v25, v26, v27\n" \
"v_cvt_pk_bf16_f32 v26, v28, v29\n" \
"v_cvt_pk_bf16_f32 v27, v30, v31\n" \
"s_cbranch_vccz Lg40_%=\n" \
"s_nop 0\n" \
"buffer_store_dwordx4 v[24:27], %[oq0], %[srdz], s60 offen offset:3072 sc1\n" \
"Lg40_%=:\n" \
"v_mfma_f32_16x16x32_bf16 v[64:67], a[128:131], v[216:219], v[64:67]\n" \
"v_pk_mul_f32 v[32:33], v[32:33], v[178:179] op_sel_hi:[1,0]\n" \
"v_pk_mul_f32 v[34:35], v[34:35], v[178:179] op_sel_hi:[1,0]\n" \
"v_pk_mul_f32 v[36:37], v[36:37], v[178:179] op_sel_hi:[1,0]\n" \
"v_mfma_f32_16x16x32_bf16 v[64:67], a[160:163], v[200:203], v[64:67]\n" \
"v_pk_mul_f32 v[38:39], v[38:39], v[178:179] op_sel_hi:[1,0]\n" \
"v_cvt_pk_bf16_f32 v32, v32, v33\n" \
"v_cvt_pk_bf16_f32 v33, v34, v35\n" \
"v_mfma_f32_16x16x32_bf16 v[68:71], a[132:135], v[216:219], v[68:71]\n" \
"v_cvt_pk_bf16_f32 v34, v36, v37\n" \
"v_cvt_pk_bf16_f32 v35, v38, v39\n" \
"s_cbranch_vccz Lg41_%=\n" \
"s_nop 0\n" \
"buffer_store_dwordx4 v[32:35], v208, %[srdz], s60 offen offset:0 sc1\n" \
"Lg41_%=:\n" \
"v_mfma_f32_16x16x32_bf16 v[68:71], a[164:167], v[200:203], v[68:71]\n" \
"v_pk_mul_f32 v[40:41], v[40:41], v[178:179] op_sel_hi:[1,0]\n" \
"v_pk_mul_f32 v[42:43], v[42:43], v[178:179] op_sel_hi:[1,0]\n" \
"v_pk_mul_f32 v[44:45], v[44:45], v[178:179] op_sel_hi:[1,0]\n" \
"v_mfma_f32_16x16x32_bf16 v[72:75], a[136:139], v[216:219], v[72:75]\n" \
"v_pk_mul_f32 v[46:47], v[46:47], v[178:179] op_sel_hi:[1,0]\n" \
"v_cvt_pk_bf16_f32 v40, v40, v41\n" \
"v_cvt_pk_bf16_f32 v41, v42, v43\n" \
"v_mfma_f32_16x16x32_bf16 v[72:75], a[168:171], v[200:203], v[72:75]\n" \
"v_cvt_pk_bf16_f32 v42, v44, v45\n" \
"v_cvt_pk_bf16_f32 v43, v46, v47\n" \
".if . - adaln_body != 40960\n" \
".error \"size model\"\n" \
".endif\n" \
"Lk9_%=:\n" \
"s_cbranch_execz Lk10_%=\n" \
"s_nop 0\n" \
"s_cbranch_vccz Lg42_%=\n" \
"s_nop 0\n" \
"buffer_store_dwordx4 v[40:43], v208, %[srdz], s60 offen offset:1024 sc1\n" \
"Lg42_%=:\n" \
"v_mfma_f32_16x16x32_bf16 v[76:79], a[140:143], v[216:219], v[76:79]\n" \
"v_pk_mul_f32 v[48:49], v[48:49], v[178:179] op_sel_hi:[1,0]\n" \
"v_pk_mul_f32 v[50:51], v[50:51], v[178:179] op_sel_hi:[1,0]\n" \
"v_pk_mul_f32 v[52:53], v[52:53], v[178:179] op_sel_hi:[1,0]\n" \
"v_mfma_f32_16x16x32_bf16 v[76:79], a[172:175], v[200:203], v[76:79]\n" \
"v_pk_mul_f32 v[54:55], v[54:55], v[178:179] op_sel_hi:[1,0]\n" \
"v_cvt_pk_bf16_f32 v48, v48, v49\n" \
"v_cvt_pk_bf16_f32 v49, v50, v51\n" \
"v_mfma_f32_16x16x32_bf16 v[80:83], a[144:147], v[216:219], v[80:83]\n" \
"v_cvt_pk_bf16_f32 v50, v52, v53\n" \
"v_cvt_pk_bf16_f32 v51, v54, v55\n" \
"s_cbranch_vccz Lg43_%=\n" \
"s_nop 0\n" \
"buffer_store_dwordx4 v[48:51], v208, %[srdz], s60 offen offset:2048 sc1\n" \
"Lg43_%=:\n" \
"v_mfma_f32_16x16x32_bf16 v[80:83], a[176:179], v[200:203], v[80:83]\n" \
"v_pk_mul_f32 v[56:57], v[56:57], v[178:179] op_sel_hi:[1,0]\n" \
"v_pk_mul_f32 v[58:59], v[58:59], v[178:179] op_sel_hi:[1,0]\n" \
"v_pk_mul_f32 v[60:61], v[60:61], v[178:179] op_sel_hi:[1,0]\n" \
"v_mfma_f32_16x16x32_bf16 v[84:87], a[148:151], v[216:219], v[84:87]\n" \
"v_pk_mul_f32 v[62:63], v[62:63], v[178:179] op_sel_hi:[1,0]\n" \
"v_cvt_pk_bf16_f32 v56, v56, v57\n" \
"v_cvt_pk_bf16_f32 v57, v58, v59\n" \
"v_mfma_f32_16x16x32_bf16 v[84:87], a[180:183], v[200:203], v[84:87]\n" \
"v_cvt_pk_bf16_f32 v58, v60, v61\n" \
"v_cvt_pk_bf16_f32 v59, v62, v63\n" \
"s_cbranch_vccz Lg44_%=\n" \
"s_nop 0\n" \
"buffer_store_dwordx4 v[56:59], v208, %[srdz], s60 offen offset:3072 sc1\n" \
"Lg44_%=:\n" \
"v_mfma_f32_16x16x32_bf16 v[88:91], a[152:155], v[216:219], v[88:91]\n" \
"v_mfma_f32_16x16x32_bf16 v[88:91], a[184:187], v[200:203], v[88:91]\n" \
"v_mfma_f32_16x16x32_bf16 v[92:95], a[156:159], v[216:219], v[92:95]\n" \
"v_mfma_f32_16x16x32_bf16 v[92:95], a[188:191], v[200:203], v[92:95]\n" \
"s_waitcnt vmcnt(14) lgkmcnt(0)\n" \
"s_bitcmp1_b32 0xf, s48\n" \
"s_cbranch_scc1 Lcw1_%=\n" \
"s_waitcnt vmcnt(6)\n" \
"Lcw1_%=:\n" \
"s_barrier\n" \
"s_nop 0\n" \
"ds_read_b128 v[232:235], v236\n" \
"v_mfma_f32_16x16x32_bf16 v[96:99], a[160:163], v[204:207], v[96:99]\n" \
"v_pk_mul_f32 v[64:65], v[64:65], v[180:181] op_sel_hi:[1,0]\n" \
"v_pk_mul_f32 v[66:67], v[66:67], v[180:181] op_sel_hi:[1,0]\n" \
"v_pk_mul_f32 v[68:69], v[68:69], v[180:181] op_sel_hi:[1,0]\n" \
"v_pk_mul_f32 v[70:71], v[70:71], v[180:181] op_sel_hi:[1,0]\n" \
"v_cvt_pk_bf16_f32 v64, v64, v65\n" \
"v_mfma_f32_16x16x32_bf16 v[100:103], a[164:167], v[204:207], v[100:103]\n" \
"v_cvt_pk_bf16_f32 v65, v66, v67\n" \
"v_cvt_pk_bf16_f32 v66, v68, v69\n" \
"v_cvt_pk_bf16_f32 v67, v70, v71\n" \
"s_cbranch_vccz Lg45_%=\n" \
"s_nop 0\n" \
"buffer_store_dwordx4 v[64:67], v209, %[srdz], s60 offen offset:0 sc1\n" \
"Lg45_%=:\n" \
"v_pk_mul_f32 v[72:73], v[72:73], v[180:181] op_sel_hi:[1,0]\n" \
"v_mfma_f32_16x16x32_bf16 v[104:107], a[168:171], v[204:207], v[104:107]\n" \
"v_pk_mul_f32 v[74:75], v[74:75], v[180:181] op_sel_hi:[1,0]\n" \
"v_pk_mul_f32 v[76:77], v[76:77], v[180:181] op_sel_hi:[1,0]\n" \
"v_pk_mul_f32 v[78:79], v[78:79], v[180:181] op_sel_hi:[1,0]\n" \
"v_cvt_pk_bf16_f32 v72, v72, v73\n" \
"v_cvt_pk_bf16_f32 v73, v74, v75\n" \
"v_mfma_f32_16x16x32_bf16 v[108:111], a[172:175], v[204:207], v[108:111]\n" \
"v_cvt_pk_bf16_f32 v74, v76, v77\n" \
"v_cvt_pk_bf16_f32 v75, v78, v79\n" \
"s_cbranch_vccz Lg46_%=\n" \
"s_nop 0\n" \
"buffer_store_dwordx4 v[72:75], v209, %[srdz], s60 offen offset:1024 sc1\n" \
"Lg46_%=:\n" \
"v_pk_mul_f32 v[80:81], v[80:81], v[180:181] op_sel_hi:[1,0]\n" \
"v_pk_mul_f32 v[82:83], v[82:83], v[180:181] op_sel_hi:[1,0]\n" \
"v_mfma_f32_16x16x32_bf16 v[112:115], a[176:179], v[204:207], v[112:115]\n" \
"v_pk_mul_f32 v[84:85], v[84:85], v[180:181] op_sel_hi:[1,0]\n" \
"v_pk_mul_f32 v[86:87], v[86:87], v[180:181] op_sel_hi:[1,0]\n" \
"v_cvt_pk_bf16_f32 v80, v80, v81\n" \
"v_cvt_pk_bf16_f32 v81, v82, v83\n" \
"v_cvt_pk_bf16_f32 v82, v84, v85\n" \
"v_mfma_f32_16x16x32_bf16 v[116:119], a[180:183], v[204:207], v[116:119]\n" \
"v_cvt_pk_bf16_f32 v83, v86, v87\n" \
"s_cbranch_vccz Lg47_%=\n" \
"s_nop 0\n" \
"buffer_store_dwordx4 v[80:83], v209, %[srdz], s60 offen offset:2048 sc1\n" \
"Lg47_%=:\n" \
"v_pk_mul_f32 v[88:89], v[88:89], v[180:181] op_sel_hi:[1,0]\n" \
"v_pk_mul_f32 v[90:91], v[90:91], v[180:181] op_sel_hi:[1,0]\n" \
"v_pk_mul_f32 v[92:93], v[92:93], v[180:181] op_sel_hi:[1,0]\n" \
"v_mfma_f32_16x16x32_bf16 v[120:123], a[184:187], v[204:207], v[120:123]\n" \
"v_pk_mul_f32 v[94:95], v[94:95], v[180:181] op_sel_hi:[1,0]\n" \
"v_cvt_pk_bf16_f32 v88, v88, v89\n" \
"v_cvt_pk_bf16_f32 v89, v90, v91\n" \
"v_cvt_pk_bf16_f32 v90, v92, v93\n" \
"v_cvt_pk_bf16_f32 v91, v94, v95\n" \
"v_mfma_f32_16x16x32_bf16 v[124:127], a[188:191], v[204:207], v[124:127]\n" \
"s_cbranch_vccz Lg48_%=\n" \
"s_nop 0\n" \
"buffer_store_dwordx4 v[88:91], v209, %[srdz], s60 offen offset:3072 sc1\n" \
"Lg48_%=:\n" \
"s_waitcnt lgkmcnt(0)\n" \
"s_nop 0\n" \
"v_or3_b32 v232, v232, v233, v234\n" \
"v_or_b32_e32 v232, v232, v235\n" \
"s_nop 0\n" \
"v_readfirstlane_b32 s55, v232\n" \
"s_nop 3\n" \
"s_andn2_b32 s55, s55, s52\n" \
"s_cbranch_scc1 Lslow_%=\n" \
"v_mfma_f32_16x16x32_bf16 v[192:195], a[196:199], v[0:3], 0\n" \
"v_mfma_f32_16x16x32_bf16 v[192:195], a[200:203], v[8:11], v[192:195]\n" \
"v_pk_mul_f32 v[96:97], v[96:97], v[182:183] op_sel_hi:[1,0]\n" \
"v_pk_mul_f32 v[98:99], v[98:99], v[182:183] op_sel_hi:[1,0]\n" \
"v_mfma_f32_16x16x32_bf16 v[192:195], a[204:207], v[16:19], v[192:195]\n" \
"v_pk_mul_f32 v[100:101], v[100:101], v[182:183] op_sel_hi:[1,0]\n" \
"v_pk_mul_f32 v[102:103], v[102:103], v[182:183] op_sel_hi:[1,0]\n" \
"s_add_u32 m0, %[m0b], 114688\n" \
"s_cmp_gt_u32 s48, 1\n" \
"s_cselect_b32 s49, 1, 2\n" \
"s_lshl_b32 s49, s49, 18\n" \
"s_or_b32 s49, s49, s58\n" \
"buffer_load_dwordx4 %[wo0], %[srdt], s49 offen offset:0 lds\n" \
"v_mfma_f32_16x16x32_bf16 v[192:195], a[208:211], v[24:27], v[192:195]\n" \
"v_cvt_pk_bf16_f32 v96, v96, v97\n" \
"v_cvt_pk_bf16_f32 v97, v98, v99\n" \
"v_mfma_f32_16x16x32_bf16 v[144:147], a[212:215], v[0:3], 0\n" \
"v_cvt_pk_bf16_f32 v98, v100, v101\n" \
"v_mfma_f32_16x16x32_bf16 v[144:147], a[216:219], v[8:11], v[144:147]\n" \
"v_cvt_pk_bf16_f32 v99, v102, v103\n" \
"s_cbranch_vccz Lg1_%=\n" \
"s_nop 0\n" \
"buffer_store_dwordx4 v[96:99], v210, %[srdz], s60 offen offset:0 sc1\n" \
"Lg1_%=:\n" \
"v_mfma_f32_16x16x32_bf16 v[144:147], a[220:223], v[16:19], v[144:147]\n" \
"ds_read_b64_tr_b16 a[228:229], v215 offset:512\n" \
"v_pk_mul_f32 v[104:105], v[104:105], v[182:183] op_sel_hi:[1,0]\n" \
"v_pk_mul_f32 v[106:107], v[106:107], v[182:183] op_sel_hi:[1,0]\n" \
"v_mfma_f32_16x16x32_bf16 v[144:147], a[224:227], v[24:27], v[144:147]\n" \
"ds_read_b64_tr_b16 a[230:231], v215 offset:2560\n" \
"v_pk_mul_f32 v[108:109], v[108:109], v[182:183] op_sel_hi:[1,0]\n" \
"v_mfma_f32_16x16x32_bf16 v[196:199], a[196:199], v[32:35], 0\n" \
"ds_read_b64_tr_b16 a[232:233], v215 offset:4608\n" \
"v_pk_mul_f32 v[110:111], v[110:111], v[182:183] op_sel_hi:[1,0]\n" \
"v_cvt_pk_bf16_f32 v104, v104, v105\n" \
"v_mfma_f32_16x16x32_bf16 v[196:199], a[200:203], v[40:43], v[196:199]\n" \
"ds_read_b64_tr_b16 a[234:235], v215 offset:6656\n" \
"v_cvt_pk_bf16_f32 v105, v106, v107\n" \
"v_cvt_pk_bf16_f32 v106, v108, v109\n" \
"v_mfma_f32_16x16x32_bf16 v[196:199], a[204:207], v[48:51], v[196:199]\n" \
"v_cvt_pk_bf16_f32 v107, v110, v111\n" \
"s_cbranch_vccz Lg2_%=\n" \
"s_nop 0\n" \
"buffer_store_dwordx4 v[104:107], v210, %[srdz], s60 offen offset:1024 sc1\n" \
"Lg2_%=:\n" \
"v_mfma_f32_16x16x32_bf16 v[196:199], a[208:211], v[56:59], v[196:199]\n" \
"v_pk_mul_f32 v[112:113], v[112:113], v[182:183] op_sel_hi:[1,0]\n" \
"v_mfma_f32_16x16x32_bf16 v[148:151], a[212:215], v[32:35], 0\n" \
"v_pk_mul_f32 v[114:115], v[114:115], v[182:183] op_sel_hi:[1,0]\n" \
"v_pk_mul_f32 v[116:117], v[116:117], v[182:183] op_sel_hi:[1,0]\n" \
"buffer_load_dwordx4 v212, %[srdt], s49 offen offset:1024 lds\n" \
"v_mfma_f32_16x16x32_bf16 v[148:151], a[216:219], v[40:43], v[148:151]\n" \
"v_pk_mul_f32 v[118:119], v[118:119], v[182:183] op_sel_hi:[1,0]\n" \
"v_cvt_pk_bf16_f32 v112, v112, v113\n" \
"v_mfma_f32_16x16x32_bf16 v[148:151], a[220:223], v[48:51], v[148:151]\n" \
"ds_read_b64_tr_b16 a[236:237], v215 offset:8704\n" \
"v_cvt_pk_bf16_f32 v113, v114, v115\n" \
"v_mfma_f32_16x16x32_bf16 v[148:151], a[224:227], v[56:59], v[148:151]\n" \
"v_cvt_pk_bf16_f32 v114, v116, v117\n" \
"v_cvt_pk_bf16_f32 v115, v118, v119\n" \
"v_mfma_f32_16x16x32_bf16 v[200:203], a[196:199], v[64:67], 0\n" \
"ds_read_b64_tr_b16 a[238:239], v215 offset:10752\n" \
"s_cbranch_vccz Lg3_%=\n" \
"s_nop 0\n" \
"buffer_store_dwordx4 v[112:115], v210, %[srdz], s60 offen offset:2048 sc1\n" \
"Lg3_%=:\n" \
"v_pk_mul_f32 v[120:121], v[120:121], v[182:183] op_sel_hi:[1,0]\n" \
"v_mfma_f32_16x16x32_bf16 v[200:203], a[200:203], v[72:75], v[200:203]\n" \
"v_pk_mul_f32 v[122:123], v[122:123], v[182:183] op_sel_hi:[1,0]\n" \
"v_pk_mul_f32 v[124:125], v[124:125], v[182:183] op_sel_hi:[1,0]\n" \
"v_mfma_f32_16x16x32_bf16 v[200:203], a[204:207], v[80:83], v[200:203]\n" \
"v_pk_mul_f32 v[126:127], v[126:127], v[182:183] op_sel_hi:[1,0]\n" \
"v_mfma_f32_16x16x32_bf16 v[200:203], a[208:211], v[88:91], v[200:203]\n" \
"v_cvt_pk_bf16_f32 v120, v120, v121\n" \
"v_cvt_pk_bf16_f32 v121, v122, v123\n" \
"v_mfma_f32_16x16x32_bf16 v[152:155], a[212:215], v[64:67], 0\n" \
"v_cvt_pk_bf16_f32 v122, v124, v125\n" \
"v_cvt_pk_bf16_f32 v123, v126, v127\n" \
"v_mfma_f32_16x16x32_bf16 v[152:155], a[216:219], v[72:75], v[152:155]\n" \
"s_cbranch_vccz Lg4_%=\n" \
"s_nop 0\n" \
"buffer_store_dwordx4 v[120:123], v210, %[srdz], s60 offen offset:3072 sc1\n" \
"Lg4_%=:\n" \
"v_mfma_f32_16x16x32_bf16 v[152:155], a[220:223], v[80:83], v[152:155]\n" \
"v_add_u32_e32 v220, 0x10000, v215\n" \
"v_mfma_f32_16x16x32_bf16 v[152:155], a[224:227], v[88:91], v[152:155]\n" \
"v_readlane_b32 s62, %[tlo], 19\n" \
"buffer_load_dwordx4 v213, %[srdt], s49 offen offset:2048 lds\n" \
"v_mfma_f32_16x16x32_bf16 v[204:207], a[196:199], v[96:99], 0\n" \
"v_add_u32_e32 v221, 0x10000, v227\n" \
"v_mfma_f32_16x16x32_bf16 v[204:207], a[200:203], v[104:107], v[204:207]\n" \
"ds_read_b64_tr_b16 a[196:197], v215 offset:12800\n" \
"v_mfma_f32_16x16x32_bf16 v[204:207], a[204:207], v[112:115], v[204:207]\n" \
"ds_read_b64_tr_b16 a[198:199], v215 offset:14848\n" \
"v_mfma_f32_16x16x32_bf16 v[204:207], a[208:211], v[120:123], v[204:207]\n" \
"ds_read_b64_tr_b16 a[200:201], v227 offset:512\n" \
"v_mfma_f32_16x16x32_bf16 v[156:159], a[212:215], v[96:99], 0\n" \
"ds_read_b64_tr_b16 a[202:203], v227 offset:2560\n" \
"v_mfma_f32_16x16x32_bf16 v[156:159], a[216:219], v[104:107], v[156:159]\n" \
"ds_read_b64_tr_b16 a[204:205], v227 offset:4608\n" \
"v_mfma_f32_16x16x32_bf16 v[156:159], a[220:223], v[112:115], v[156:159]\n" \
"ds_read_b64_tr_b16 a[206:207], v227 offset:6656\n" \
"v_mfma_f32_16x16x32_bf16 v[156:159], a[224:227], v[120:123], v[156:159]\n" \
"s_waitcnt vmcnt(63) lgkmcnt(4)\n" \
"s_nop 0\n" \
"v_mfma_f32_16x16x32_bf16 v[160:163], a[228:231], v[0:3], 0\n" \
"ds_read_b64_tr_b16 a[208:209], v227 offset:8704\n" \
"v_mfma_f32_16x16x32_bf16 v[160:163], a[232:235], v[8:11], v[160:163]\n" \
"ds_read_b64_tr_b16 a[210:211], v227 offset:10752\n" \
"s_mov_b32 s40, 606208\n" \
"buffer_load_dwordx4 a[176:179], v237, %[srdz], s40 offen sc1\n" \
"v_mfma_f32_16x16x32_bf16 v[164:167], a[228:231], v[32:35], 0\n" \
"ds_read_b64_tr_b16 a[212:213], v227 offset:12800\n" \
"v_mfma_f32_16x16x32_bf16 v[164:167], a[232:235], v[40:43], v[164:167]\n" \
"ds_read_b64_tr_b16 a[214:215], v227 offset:14848\n" \
"buffer_load_dwordx4 v214, %[srdt], s49 offen offset:3072 lds\n" \
"v_mfma_f32_16x16x32_bf16 v[168:171], a[228:231], v[64:67], 0\n" \
"v_mfma_f32_16x16x32_bf16 v[168:171], a[232:235], v[72:75], v[168:171]\n" \
"v_mfma_f32_16x16x32_bf16 v[172:175], a[228:231], v[96:99], 0\n" \
"ds_read_b64_tr_b16 a[216:217], v215 offset:16384\n" \
"v_mfma_f32_16x16x32_bf16 v[172:175], a[232:235], v[104:107], v[172:175]\n" \
"ds_read_b64_tr_b16 a[218:219], v215 offset:18432\n" \
"v_mfma_f32_16x16x32_bf16 v[160:163], a[236:239], v[16:19], v[160:163]\n" \
"ds_read_b64_tr_b16 a[220:221], v215 offset:20480\n" \
"v_mfma_f32_16x16x32_bf16 v[160:163], a[196:199], v[24:27], v[160:163]\n" \
"ds_read_b64_tr_b16 a[222:223], v215 offset:22528\n" \
"s_mov_b32 s40, 610304\n" \
"buffer_load_dwordx4 a[180:183], v237, %[srdz], s40 offen sc1\n" \
"v_mfma_f32_16x16x32_bf16 v[164:167], a[236:239], v[48:51], v[164:167]\n" \
"v_mfma_f32_16x16x32_bf16 v[164:167], a[196:199], v[56:59], v[164:167]\n" \
"v_mfma_f32_16x16x32_bf16 v[168:171], a[236:239], v[80:83], v[168:171]\n" \
"s_waitcnt vmcnt(63) lgkmcnt(0)\n" \
"s_barrier\n" \
"v_mfma_f32_16x16x32_bf16 v[168:171], a[196:199], v[88:91], v[168:171]\n" \
"v_mfma_f32_16x16x32_bf16 v[172:175], a[236:239], v[112:115], v[172:175]\n" \
"ds_read_b64_tr_b16 a[224:225], v215 offset:24576\n" \
"v_mfma_f32_16x16x32_bf16 v[172:175], a[196:199], v[120:123], v[172:175]\n" \
"s_waitcnt vmcnt(63) lgkmcnt(5)\n" \
"s_nop 0\n" \
"v_mfma_f32_16x16x32_bf16 v[176:179], a[200:203], v[0:3], 0\n" \
"ds_read_b64_tr_b16 a[226:227], v215 offset:26624\n" \
"v_mfma_f32_16x16x32_bf16 v[176:179], a[204:207], v[8:11], v[176:179]\n" \
"ds_read_b64_tr_b16 a[228:229], v215 offset:28672\n" \
"s_mov_b32 s40, 614400\n" \
"buffer_load_dwordx4 a[184:187], v237, %[srdz], s40 offen sc1\n" \
"v_mfma_f32_16x16x32_bf16 v[180:183], a[200:203], v[32:35], 0\n" \
"ds_read_b64_tr_b16 a[230:231], v215 offset:30720\n" \
"v_mfma_f32_16x16x32_bf16 v[180:183], a[204:207], v[40:43], v[180:183]\n" \
"s_add_u32 m0, %[m0b], 49152\n" \
"s_cmp_gt_u32 s48, 1\n" \
"s_cselect_b32 s49, 1, 2\n" \
"s_lshl_b32 s49, s49, 18\n" \
"s_or_b32 s49, s49, s59\n" \
"buffer_load_dwordx4 %[wo0], %[srdt], s49 offen offset:0 lds\n" \
"v_mfma_f32_16x16x32_bf16 v[184:187], a[200:203], v[64:67], 0\n" \
"v_mfma_f32_16x16x32_bf16 v[184:187], a[204:207], v[72:75], v[184:187]\n" \
"buffer_load_dwordx4 v212, %[srdt], s49 offen offset:1024 lds\n" \
"v_mfma_f32_16x16x32_bf16 v[188:191], a[200:203], v[96:99], 0\n" \
"ds_read_b64_tr_b16 a[232:233], v227 offset:16384\n" \
"v_mfma_f32_16x16x32_bf16 v[188:191], a[204:207], v[104:107], v[188:191]\n" \
"ds_read_b64_tr_b16 a[234:235], v227 offset:18432\n" \
"v_mfma_f32_16x16x32_bf16 v[176:179], a[208:211], v[16:19], v[176:179]\n" \
"ds_read_b64_tr_b16 a[236:237], v227 offset:20480\n" \
"v_mfma_f32_16x16x32_bf16 v[176:179], a[212:215], v[24:27], v[176:179]\n" \
"ds_read_b64_tr_b16 a[238:239], v227 offset:22528\n" \
"s_mov_b32 s40, 618496\n" \
"buffer_load_dwordx4 a[188:191], v237, %[srdz], s40 offen sc1\n" \
"v_mfma_f32_16x16x32_bf16 v[180:183], a[208:211], v[48:51], v[180:183]\n" \
"v_mfma_f32_16x16x32_bf16 v[180:183], a[212:215], v[56:59], v[180:183]\n" \
"v_mfma_f32_16x16x32_bf16 v[184:187], a[208:211], v[80:83], v[184:187]\n" \
"v_mfma_f32_16x16x32_bf16 v[184:187], a[212:215], v[88:91], v[184:187]\n" \
"buffer_load_dwordx4 v213, %[srdt], s49 offen offset:2048 lds\n" \
"v_mfma_f32_16x16x32_bf16 v[188:191], a[208:211], v[112:115], v[188:191]\n" \
"ds_read_b64_tr_b16 a[196:197], v227 offset:24576\n" \
"v_mfma_f32_16x16x32_bf16 v[188:191], a[212:215], v[120:123], v[188:191]\n" \
"s_waitcnt vmcnt(63) lgkmcnt(5)\n" \
"s_nop 0\n" \
"v_mfma_f32_16x16x32_bf16 v[4:7], a[216:219], v[0:3], 0\n" \
"ds_read_b64_tr_b16 a[198:199], v227 offset:26624\n" \
"v_mfma_f32_16x16x32_bf16 v[4:7], a[220:223], v[8:11], v[4:7]\n" \
"ds_read_b64_tr_b16 a[200:201], v227 offset:28672\n" \
"v_mfma_f32_16x16x32_bf16 v[36:39], a[216:219], v[32:35], 0\n" \
"ds_read_b64_tr_b16 a[202:203], v227 offset:30720\n" \
"v_mfma_f32_16x16x32_bf16 v[36:39], a[220:223], v[40:43], v[36:39]\n" \
"v_mfma_f32_16x16x32_bf16 v[68:71], a[216:219], v[64:67], 0\n" \
"buffer_load_dwordx4 v214, %[srdt], s49 offen offset:3072 lds\n" \
"v_mfma_f32_16x16x32_bf16 v[68:71], a[220:223], v[72:75], v[68:71]\n" \
"v_mfma_f32_16x16x32_bf16 v[100:103], a[216:219], v[96:99], 0\n" \
"ds_read_b64_tr_b16 a[204:205], v215 offset:16896\n" \
"v_mfma_f32_16x16x32_bf16 v[100:103], a[220:223], v[104:107], v[100:103]\n" \
"ds_read_b64_tr_b16 a[206:207], v215 offset:18944\n" \
"v_mfma_f32_16x16x32_bf16 v[4:7], a[224:227], v[16:19], v[4:7]\n" \
"ds_read_b64_tr_b16 a[208:209], v215 offset:20992\n" \
"v_mfma_f32_16x16x32_bf16 v[4:7], a[228:231], v[24:27], v[4:7]\n" \
"ds_read_b64_tr_b16 a[210:211], v215 offset:23040\n" \
"v_mfma_f32_16x16x32_bf16 v[36:39], a[224:227], v[48:51], v[36:39]\n" \
"v_mfma_f32_16x16x32_bf16 v[36:39], a[228:231], v[56:59], v[36:39]\n" \
"v_mfma_f32_16x16x32_bf16 v[68:71], a[224:227], v[80:83], v[68:71]\n" \
"v_mfma_f32_16x16x32_bf16 v[68:71], a[228:231], v[88:91], v[68:71]\n" \
"v_mfma_f32_16x16x32_bf16 v[100:103], a[224:227], v[112:115], v[100:103]\n" \
"ds_read_b64_tr_b16 a[212:213], v215 offset:25088\n" \
"v_mfma_f32_16x16x32_bf16 v[100:103], a[228:231], v[120:123], v[100:103]\n" \
"s_waitcnt vmcnt(63) lgkmcnt(5)\n" \
"s_nop 0\n" \
"v_mfma_f32_16x16x32_bf16 v[12:15], a[232:235], v[0:3], 0\n" \
"ds_read_b64_tr_b16 a[214:215], v215 offset:27136\n" \
"s_cmp_gt_u32 s48, 0\n" \
"s_cselect_b32 s50, 0, 1\n" \
"s_mul_i32 s50, s50, 152064\n" \
"buffer_load_dwordx4 a[64:67], %[oq0], %[srdz], s50 offen offset:0 sc1\n" \
"v_mfma_f32_16x16x32_bf16 v[12:15], a[236:239], v[8:11], v[12:15]\n" \
"ds_read_b64_tr_b16 a[216:217], v215 offset:29184\n" \
"v_mfma_f32_16x16x32_bf16 v[44:47], a[232:235], v[32:35], 0\n" \
"ds_read_b64_tr_b16 a[218:219], v215 offset:31232\n" \
"buffer_load_dwordx4 a[68:71], %[oq0], %[srdz], s50 offen offset:1024 sc1\n" \
"v_mfma_f32_16x16x32_bf16 v[44:47], a[236:239], v[40:43], v[44:47]\n" \
"v_mfma_f32_16x16x32_bf16 v[76:79], a[232:235], v[64:67], 0\n" \
"buffer_load_dwordx4 a[80:83], v208, %[srdz], s50 offen offset:0 sc1\n" \
"v_mfma_f32_16x16x32_bf16 v[76:79], a[236:239], v[72:75], v[76:79]\n" \
"v_mfma_f32_16x16x32_bf16 v[108:111], a[232:235], v[96:99], 0\n" \
"ds_read_b64_tr_b16 a[220:221], v227 offset:16896\n" \
"buffer_load_dwordx4 a[84:87], v208, %[srdz], s50 offen offset:1024 sc1\n" \
"v_mfma_f32_16x16x32_bf16 v[108:111], a[236:239], v[104:107], v[108:111]\n" \
"ds_read_b64_tr_b16 a[222:223], v227 offset:18944\n" \
"v_mfma_f32_16x16x32_bf16 v[12:15], a[196:199], v[16:19], v[12:15]\n" \
"ds_read_b64_tr_b16 a[224:225], v227 offset:25088\n" \
"buffer_load_dwordx4 a[96:99], v209, %[srdz], s50 offen offset:0 sc1\n" \
"v_mfma_f32_16x16x32_bf16 v[12:15], a[200:203], v[24:27], v[12:15]\n" \
"ds_read_b64_tr_b16 a[226:227], v227 offset:27136\n" \
"v_mfma_f32_16x16x32_bf16 v[44:47], a[196:199], v[48:51], v[44:47]\n" \
"ds_read_b64_tr_b16 a[228:229], v227 offset:20992\n" \
"buffer_load_dwordx4 a[100:103], v209, %[srdz], s50 offen offset:1024 sc1\n" \
"v_mfma_f32_16x16x32_bf16 v[44:47], a[200:203], v[56:59], v[44:47]\n" \
"ds_read_b64_tr_b16 a[230:231], v227 offset:23040\n" \
"v_mfma_f32_16x16x32_bf16 v[76:79], a[196:199], v[80:83], v[76:79]\n" \
"ds_read_b64_tr_b16 a[232:233], v227 offset:29184\n" \
"buffer_load_dwordx4 a[112:115], v210, %[srdz], s50 offen offset:0 sc1\n" \
"v_mfma_f32_16x16x32_bf16 v[76:79], a[200:203], v[88:91], v[76:79]\n" \
"ds_read_b64_tr_b16 a[234:235], v227 offset:31232\n" \
"v_mfma_f32_16x16x32_bf16 v[108:111], a[196:199], v[112:115], v[108:111]\n" \
"ds_read_b64_tr_b16 a[236:237], v215 offset:32768\n" \
"buffer_load_dwordx4 a[116:119], v210, %[srdz], s50 offen offset:1024 sc1\n" \
"v_mfma_f32_16x16x32_bf16 v[108:111], a[200:203], v[120:123], v[108:111]\n" \
"s_waitcnt vmcnt(63) lgkmcnt(13)\n" \
"s_nop 0\n" \
"v_mfma_f32_16x16x32_bf16 v[20:23], a[204:207], v[0:3], 0\n" \
"ds_read_b64_tr_b16 a[238:239], v215 offset:34816\n" \
"buffer_load_dwordx4 a[72:75], %[oq0], %[srdz], s50 offen offset:2048 sc1\n" \
"v_mfma_f32_16x16x32_bf16 v[20:23], a[208:211], v[8:11], v[20:23]\n" \
"ds_read_b64_tr_b16 a[196:197], v215 offset:36864\n" \
"v_mfma_f32_16x16x32_bf16 v[52:55], a[204:207], v[32:35], 0\n" \
"ds_read_b64_tr_b16 a[198:199], v215 offset:38912\n" \
"buffer_load_dwordx4 a[76:79], %[oq0], %[srdz], s50 offen offset:3072 sc1\n" \
"v_mfma_f32_16x16x32_bf16 v[52:55], a[208:211], v[40:43], v[52:55]\n" \
"v_mfma_f32_16x16x32_bf16 v[84:87], a[204:207], v[64:67], 0\n" \
"s_waitcnt vmcnt(63) lgkmcnt(4)\n" \
"s_barrier\n" \
"v_mfma_f32_16x16x32_bf16 v[84:87], a[208:211], v[72:75], v[84:87]\n" \
"buffer_load_dwordx4 a[88:91], v208, %[srdz], s50 offen offset:2048 sc1\n" \
"v_mfma_f32_16x16x32_bf16 v[116:119], a[204:207], v[96:99], 0\n" \
"ds_read_b64_tr_b16 a[200:201], v215 offset:40960\n" \
"v_mfma_f32_16x16x32_bf16 v[116:119], a[208:211], v[104:107], v[116:119]\n" \
"ds_read_b64_tr_b16 a[202:203], v215 offset:43008\n" \
"buffer_load_dwordx4 a[92:95], v208, %[srdz], s50 offen offset:3072 sc1\n" \
"v_mfma_f32_16x16x32_bf16 v[20:23], a[212:215], v[16:19], v[20:23]\n" \
"ds_read_b64_tr_b16 a[204:205], v215 offset:45056\n" \
"v_mfma_f32_16x16x32_bf16 v[20:23], a[216:219], v[24:27], v[20:23]\n" \
"ds_read_b64_tr_b16 a[206:207], v215 offset:47104\n" \
"buffer_load_dwordx4 a[104:107], v209, %[srdz], s50 offen offset:2048 sc1\n" \
"s_waitcnt vmcnt(15)\n" \
"s_nop 0\n" \
"v_mfma_f32_16x16x32_bf16 v[52:55], a[212:215], v[48:51], v[52:55]\n" \
"v_mfma_f32_16x16x32_bf16 v[216:219], a[176:179], v[0:3], 0\n" \
"v_mfma_f32_16x16x32_bf16 v[216:219], a[180:183], v[8:11], v[216:219]\n" \
"v_mfma_f32_16x16x32_bf16 v[216:219], a[184:187], v[16:19], v[216:219]\n" \
"v_mfma_f32_16x16x32_bf16 v[216:219], a[188:191], v[24:27], v[216:219]\n" \
"v_mfma_f32_16x16x32_bf16 v[52:55], a[216:219], v[56:59], v[52:55]\n" \
"buffer_load_dwordx4 a[108:111], v209, %[srdz], s50 offen offset:3072 sc1\n" \
"v_mfma_f32_16x16x32_bf16 v[84:87], a[212:215], v[80:83], v[84:87]\n" \
"v_mfma_f32_16x16x32_bf16 v[84:87], a[216:219], v[88:91], v[84:87]\n" \
"buffer_load_dwordx4 a[120:123], v210, %[srdz], s50 offen offset:2048 sc1\n" \
"v_mfma_f32_16x16x32_bf16 v[116:119], a[212:215], v[112:115], v[116:119]\n" \
"v_mfma_f32_16x16x32_bf16 v[232:235], a[176:179], v[32:35], 0\n" \
"v_mfma_f32_16x16x32_bf16 v[232:235], a[180:183], v[40:43], v[232:235]\n" \
"v_mfma_f32_16x16x32_bf16 v[232:235], a[184:187], v[48:51], v[232:235]\n" \
"v_mfma_f32_16x16x32_bf16 v[232:235], a[188:191], v[56:59], v[232:235]\n" \
"v_mfma_f32_16x16x32_bf16 v[116:119], a[216:219], v[120:123], v[116:119]\n" \
"v_mov_b32_dpp v231, v216 quad_perm:[0,1,2,3] row_mask:0x1 bank_mask:0xf\n" \
"s_waitcnt vmcnt(63) lgkmcnt(10)\n" \
"s_nop 0\n" \
"v_mfma_f32_16x16x32_bf16 v[28:31], a[220:223], v[0:3], 0\n" \
"buffer_load_dwordx4 a[124:127], v210, %[srdz], s50 offen offset:3072 sc1\n" \
"v_mfma_f32_16x16x32_bf16 v[28:31], a[228:231], v[8:11], v[28:31]\n" \
"s_sub_u32 s61, s48, 4\n" \
"s_cmp_lt_u32 s61, 2\n" \
"s_cselect_b64 vcc, -1, 0\n" \
"s_nop 0\n" \
"v_mfma_f32_16x16x32_bf16 v[60:63], a[220:223], v[32:35], 0\n" \
"s_cbranch_vccz Lg5_%=\n" \
"s_nop 0\n" \
"buffer_store_dwordx4 v[0:3], %[oq0], %[srdz], s60 offen offset:0 sc1\n" \
"Lg5_%=:\n" \
"v_mfma_f32_16x16x32_bf16 v[216:219], a[176:179], v[64:67], 0\n" \
"v_mfma_f32_16x16x32_bf16 v[216:219], a[180:183], v[72:75], v[216:219]\n" \
"v_mfma_f32_16x16x32_bf16 v[216:219], a[184:187], v[80:83], v[216:219]\n" \
"v_mfma_f32_16x16x32_bf16 v[216:219], a[188:191], v[88:91], v[216:219]\n" \
"v_mfma_f32_16x16x32_bf16 v[60:63], a[228:231], v[40:43], v[60:63]\n" \
"v_mov_b32_dpp v231, v232 quad_perm:[0,1,2,3] row_mask:0x2 bank_mask:0xf\n" \
"v_mfma_f32_16x16x32_bf16 v[92:95], a[220:223], v[64:67], 0\n" \
"s_cmp_gt_u32 s48, 1\n" \
"s_cselect_b32 s50, 1, 2\n" \
"s_mul_i32 s50, s50, 152064\n" \
"buffer_load_dwordx4 a[0:3], %[oq0], %[srdz], s50 offen offset:0 sc1\n" \
"v_mfma_f32_16x16x32_bf16 v[92:95], a[228:231], v[72:75], v[92:95]\n" \
"v_mfma_f32_16x16x32_bf16 v[124:127], a[220:223], v[96:99], 0\n" \
"ds_read_b64_tr_b16 a[208:209], v227 offset:32768\n" \
"v_mfma_f32_16x16x32_bf16 v[232:235], a[176:179], v[96:99], 0\n" \
"v_mfma_f32_16x16x32_bf16 v[232:235], a[180:183], v[104:107], v[232:235]\n" \
"v_mfma_f32_16x16x32_bf16 v[232:235], a[184:187], v[112:115], v[232:235]\n" \
"v_mfma_f32_16x16x32_bf16 v[232:235], a[188:191], v[120:123], v[232:235]\n" \
"v_mfma_f32_16x16x32_bf16 v[124:127], a[228:231], v[104:107], v[124:127]\n" \
"ds_read_b64_tr_b16 a[210:211], v227 offset:34816\n" \
"v_mov_b32_dpp v231, v216 quad_perm:[0,1,2,3] row_mask:0x4 bank_mask:0xf\n" \
"s_waitcnt vmcnt(15) lgkmcnt(6)\n" \
"s_nop 0\n" \
"v_mfma_f32_16x16x32_bf16 v[128:131], a[236:239], a[64:67], v[192:195]\n" \
"ds_read_b64_tr_b16 a[212:213], v227 offset:36864\n" \
"v_mfma_f32_16x16x32_bf16 v[128:131], a[196:199], a[68:71], v[128:131]\n" \
"ds_read_b64_tr_b16 a[214:215], v227 offset:38912\n" \
"s_waitcnt vmcnt(13) lgkmcnt(15)\n" \
"s_nop 0\n" \
"v_mfma_f32_16x16x32_bf16 v[132:135], a[236:239], a[80:83], v[196:199]\n" \
"s_cbranch_vccz Lg6_%=\n" \
"s_nop 0\n" \
"buffer_store_dwordx4 v[8:11], %[oq0], %[srdz], s60 offen offset:1024 sc1\n" \
"Lg6_%=:\n" \
"v_mfma_f32_16x16x32_bf16 v[132:135], a[196:199], a[84:87], v[132:135]\n" \
"s_waitcnt vmcnt(13) lgkmcnt(15)\n" \
"s_bitcmp1_b32 0x30, s48\n" \
"s_cbranch_scc1 Lcw2_%=\n" \
"s_waitcnt vmcnt(11)\n" \
"Lcw2_%=:\n" \
"v_mfma_f32_16x16x32_bf16 v[136:139], a[236:239], a[96:99], v[200:203]\n" \
"v_mov_b32_dpp v231, v232 quad_perm:[0,1,2,3] row_mask:0x8 bank_mask:0xf\n" \
"buffer_load_dwordx4 a[4:7], %[oq0], %[srdz], s50 offen offset:1024 sc1\n" \
"v_mfma_f32_16x16x32_bf16 v[136:139], a[196:199], a[100:103], v[136:139]\n" \
"s_waitcnt vmcnt(12) lgkmcnt(15)\n" \
"s_bitcmp1_b32 0x30, s48\n" \
"s_cbranch_scc1 Lcw3_%=\n" \
"s_waitcnt vmcnt(10)\n" \
"Lcw3_%=:\n" \
"v_mfma_f32_16x16x32_bf16 v[140:143], a[236:239], a[112:115], v[204:207]\n" \
"ds_read_b64_tr_b16 a[216:217], v227 offset:40960\n" \
"v_cmp_u_f32_e64 s[42:43], v231, v231\n" \
"v_readlane_b32 s41, %[tlo], 33\n" \
"v_mfma_f32_16x16x32_bf16 v[140:143], a[196:199], a[116:119], v[140:143]\n" \
"s_waitcnt vmcnt(10) lgkmcnt(5)\n" \
"s_bitcmp1_b32 0x30, s48\n" \
"s_cbranch_scc1 Lcw4_%=\n" \
"s_waitcnt vmcnt(8)\n" \
"Lcw4_%=:\n" \
"v_mfma_f32_16x16x32_bf16 v[128:131], a[200:203], a[72:75], v[128:131]\n" \
"ds_read_b64_tr_b16 a[218:219], v227 offset:43008\n" \
"buffer_load_dwordx4 a[8:11], %[oq0], %[srdz], s50 offen offset:2048 sc1\n" \
"v_mfma_f32_16x16x32_bf16 v[128:131], a[204:207], a[76:79], v[128:131]\n" \
"ds_read_b64_tr_b16 a[220:221], v227 offset:45056\n" \
"v_lshrrev_b32_e64 v222, 5, v224\n" \
"s_waitcnt vmcnt(9) lgkmcnt(15)\n" \
"s_bitcmp1_b32 0x30, s48\n" \
"s_cbranch_scc1 Lcw5_%=\n" \
"s_waitcnt vmcnt(7)\n" \
"Lcw5_%=:\n" \
"v_mfma_f32_16x16x32_bf16 v[132:135], a[200:203], a[88:91], v[132:135]\n" \
"ds_read_b64_tr_b16 a[222:223], v227 offset:47104\n" \
"v_mfma_f32_16x16x32_bf16 v[132:135], a[204:207], a[92:95], v[132:135]\n" \
"s_cbranch_vccz Lg7_%=\n" \
"s_nop 0\n" \
"buffer_store_dwordx4 v[16:19], %[oq0], %[srdz], s60 offen offset:2048 sc1\n" \
"Lg7_%=:\n" \
"s_waitcnt vmcnt(6) lgkmcnt(14)\n" \
"s_bitcmp1_b32 0x30, s48\n" \
"s_cbranch_scc1 Lcw6_%=\n" \
"s_waitcnt vmcnt(3)\n" \
"Lcw6_%=:\n" \
"v_mfma_f32_16x16x32_bf16 v[136:139], a[200:203], a[104:107], v[136:139]\n" \
"s_cmp_lg_u64 s[42:43], 0\n" \
"s_cbranch_scc1 Ls0_%=\n" \
"Ls0b_%=:\n" \
"v_mfma_f32_16x16x32_bf16 v[136:139], a[204:207], a[108:111], v[136:139]\n" \
"v_or_b32_e64 v231, 1, v231\n" \
"buffer_load_dwordx4 a[12:15], %[oq0], %[srdz], s50 offen offset:3072 sc1\n" \
"v_mfma_f32_16x16x32_bf16 v[140:143], a[200:203], a[120:123], v[140:143]\n" \
"ds_read_b64_tr_b16 a[228:229], v215 offset:33280\n" \
"v_mfma_f32_16x16x32_bf16 v[140:143], a[204:207], a[124:127], v[140:143]\n" \
"ds_read_b64_tr_b16 a[230:231], v215 offset:35328\n" \
"v_mfma_f32_16x16x32_bf16 v[28:31], a[224:227], v[16:19], v[28:31]\n" \
"buffer_store_dword v231, v222, %[srdz], s41 offen sc1\n" \
"v_mfma_f32_16x16x32_bf16 v[28:31], a[232:235], v[24:27], v[28:31]\n" \
"v_mfma_f32_16x16x32_bf16 v[60:63], a[224:227], v[48:51], v[60:63]\n" \
"v_mfma_f32_16x16x32_bf16 v[60:63], a[232:235], v[56:59], v[60:63]\n" \
"s_cbranch_vccz Lg8_%=\n" \
"s_nop 0\n" \
"buffer_store_dwordx4 v[24:27], %[oq0], %[srdz], s60 offen offset:3072 sc1\n" \
"Lg8_%=:\n" \
"v_mfma_f32_16x16x32_bf16 v[92:95], a[224:227], v[80:83], v[92:95]\n" \
"v_mfma_f32_16x16x32_bf16 v[92:95], a[232:235], v[88:91], v[92:95]\n" \
"v_cmp_u_f32_e64 s[44:45], v128, v132\n" \
"buffer_load_dwordx4 a[16:19], v208, %[srdz], s50 offen offset:0 sc1\n" \
"v_mfma_f32_16x16x32_bf16 v[124:127], a[224:227], v[112:115], v[124:127]\n" \
"v_mfma_f32_16x16x32_bf16 v[124:127], a[232:235], v[120:123], v[124:127]\n" \
"v_cmp_u_f32_e64 s[56:57], v136, v140\n" \
"s_or_b64 s[56:57], s[56:57], s[44:45]\n" \
"s_cbranch_scc1 Lsp1_%=\n" \
"Lbk1_%=:\n" \
"s_waitcnt vmcnt(63) lgkmcnt(2)\n" \
"s_nop 0\n" \
"v_mfma_f32_16x16x32_bf16 v[144:147], a[208:211], a[64:67], v[144:147]\n" \
"ds_read_b64_tr_b16 a[224:225], v215 offset:37376\n" \
"v_mfma_f32_16x16x32_bf16 v[144:147], a[212:215], a[68:71], v[144:147]\n" \
"ds_read_b64_tr_b16 a[226:227], v215 offset:39424\n" \
"buffer_load_dwordx4 a[20:23], v208, %[srdz], s50 offen offset:1024 sc1\n" \
"v_mfma_f32_16x16x32_bf16 v[148:151], a[208:211], a[80:83], v[148:151]\n" \
"ds_read_b64_tr_b16 a[196:197], v215 offset:41472\n" \
"v_mfma_f32_16x16x32_bf16 v[148:151], a[212:215], a[84:87], v[148:151]\n" \
"ds_read_b64_tr_b16 a[198:199], v215 offset:43520\n" \
"v_mfma_f32_16x16x32_bf16 v[152:155], a[208:211], a[96:99], v[152:155]\n" \
"ds_read_b64_tr_b16 a[200:201], v215 offset:45568\n" \
"s_cbranch_vccz Lg9_%=\n" \
"s_nop 0\n" \
"buffer_store_dwordx4 v[32:35], v208, %[srdz], s60 offen offset:0 sc1\n" \
"Lg9_%=:\n" \
"v_mfma_f32_16x16x32_bf16 v[152:155], a[212:215], a[100:103], v[152:155]\n" \
"ds_read_b64_tr_b16 a[202:203], v215 offset:47616\n" \
"v_mfma_f32_16x16x32_bf16 v[156:159], a[208:211], a[112:115], v[156:159]\n" \
"ds_read_b64_tr_b16 a[204:205], v227 offset:33280\n" \
"buffer_load_dwordx4 a[24:27], v208, %[srdz], s50 offen offset:2048 sc1\n" \
"v_mfma_f32_16x16x32_bf16 v[156:159], a[212:215], a[116:119], v[156:159]\n" \
"ds_read_b64_tr_b16 a[206:207], v227 offset:35328\n" \
"v_mfma_f32_16x16x32_bf16 v[144:147], a[216:219], a[72:75], v[144:147]\n" \
"ds_read_b64_tr_b16 a[232:233], v227 offset:37376\n" \
"v_mfma_f32_16x16x32_bf16 v[144:147], a[220:223], a[76:79], v[144:147]\n" \
"ds_read_b64_tr_b16 a[234:235], v227 offset:39424\n" \
"v_mfma_f32_16x16x32_bf16 v[148:151], a[216:219], a[88:91], v[148:151]\n" \
"buffer_load_dwordx4 a[28:31], v208, %[srdz], s50 offen offset:3072 sc1\n" \
"v_mfma_f32_16x16x32_bf16 v[148:151], a[220:223], a[92:95], v[148:151]\n" \
"v_mfma_f32_16x16x32_bf16 v[152:155], a[216:219], a[104:107], v[152:155]\n" \
"s_cbranch_vccz Lg10_%=\n" \
"s_nop 0\n" \
"buffer_store_dwordx4 v[40:43], v208, %[srdz], s60 offen offset:1024 sc1\n" \
"Lg10_%=:\n" \
"v_mfma_f32_16x16x32_bf16 v[152:155], a[220:223], a[108:111], v[152:155]\n" \
"v_mfma_f32_16x16x32_bf16 v[156:159], a[216:219], a[120:123], v[156:159]\n" \
"ds_read_b64_tr_b16 a[236:237], v227 offset:41472\n" \
"buffer_load_dwordx4 a[32:35], v209, %[srdz], s50 offen offset:0 sc1\n" \
"v_mfma_f32_16x16x32_bf16 v[156:159], a[220:223], a[124:127], v[156:159]\n" \
"s_waitcnt vmcnt(63) lgkmcnt(5)\n" \
"s_nop 0\n" \
"v_mfma_f32_16x16x32_bf16 v[160:163], a[228:231], a[64:67], v[160:163]\n" \
"ds_read_b64_tr_b16 a[238:239], v227 offset:43520\n" \
"v_mfma_f32_16x16x32_bf16 v[160:163], a[224:227], a[68:71], v[160:163]\n" \
"ds_read_b64_tr_b16 a[208:209], v227 offset:45568\n" \
"v_mfma_f32_16x16x32_bf16 v[164:167], a[228:231], a[80:83], v[164:167]\n" \
"ds_read_b64_tr_b16 a[210:211], v227 offset:47616\n" \
"buffer_load_dwordx4 a[36:39], v209, %[srdz], s50 offen offset:1024 sc1\n" \
"v_mfma_f32_16x16x32_bf16 v[164:167], a[224:227], a[84:87], v[164:167]\n" \
"v_mfma_f32_16x16x32_bf16 v[168:171], a[228:231], a[96:99], v[168:171]\n" \
"v_mfma_f32_16x16x32_bf16 v[168:171], a[224:227], a[100:103], v[168:171]\n" \
"s_cbranch_vccz Lg11_%=\n" \
"s_nop 0\n" \
"buffer_store_dwordx4 v[48:51], v208, %[srdz], s60 offen offset:2048 sc1\n" \
"Lg11_%=:\n" \
"v_mfma_f32_16x16x32_bf16 v[172:175], a[228:231], a[112:115], v[172:175]\n" \
"v_mfma_f32_16x16x32_bf16 v[172:175], a[224:227], a[116:119], v[172:175]\n" \
"buffer_load_dwordx4 a[40:43], v209, %[srdz], s50 offen offset:2048 sc1\n" \
"v_mfma_f32_16x16x32_bf16 v[160:163], a[196:199], a[72:75], v[160:163]\n" \
"v_mfma_f32_16x16x32_bf16 v[160:163], a[200:203], a[76:79], v[160:163]\n" \
"buffer_load_dwordx4 a[44:47], v209, %[srdz], s50 offen offset:3072 sc1\n" \
"v_mfma_f32_16x16x32_bf16 v[164:167], a[196:199], a[88:91], v[164:167]\n" \
"v_mfma_f32_16x16x32_bf16 v[164:167], a[200:203], a[92:95], v[164:167]\n" \
"buffer_load_dwordx4 a[48:51], v210, %[srdz], s50 offen offset:0 sc1\n" \
"v_mfma_f32_16x16x32_bf16 v[168:171], a[196:199], a[104:107], v[168:171]\n" \
"s_waitcnt vmcnt(59) lgkmcnt(0)\n" \
"s_bitcmp1_b32 0xf, s48\n" \
"s_cbranch_scc1 Lcw7_%=\n" \
"s_waitcnt vmcnt(50)\n" \
"s_bitcmp1_b32 0x30, s48\n" \
"s_cbranch_scc1 Lcw7_%=\n" \
".if . - adaln_body != 45056\n" \
".error \"size model\"\n" \
".endif\n" \
"Lk10_%=:\n" \
"s_cbranch_execz Lk11_%=\n" \
"s_nop 0\n" \
"s_waitcnt vmcnt(43)\n" \
"Lcw7_%=:\n" \
"s_barrier\n" \
"v_mfma_f32_16x16x32_bf16 v[168:171], a[200:203], a[108:111], v[168:171]\n" \
"ds_read_b64_tr_b16 a[212:213], v220 offset:32768\n" \
"s_cbranch_vccz Lg12_%=\n" \
"s_nop 0\n" \
"buffer_store_dwordx4 v[56:59], v208, %[srdz], s60 offen offset:3072 sc1\n" \
"Lg12_%=:\n" \
"v_mfma_f32_16x16x32_bf16 v[172:175], a[196:199], a[120:123], v[172:175]\n" \
"ds_read_b64_tr_b16 a[214:215], v220 offset:34816\n" \
"v_mfma_f32_16x16x32_bf16 v[172:175], a[200:203], a[124:127], v[172:175]\n" \
"s_waitcnt vmcnt(63) lgkmcnt(2)\n" \
"s_nop 0\n" \
"v_mfma_f32_16x16x32_bf16 v[176:179], a[204:207], a[64:67], v[176:179]\n" \
"ds_read_b64_tr_b16 a[216:217], v220 offset:36864\n" \
"buffer_load_dwordx4 a[52:55], v210, %[srdz], s50 offen offset:1024 sc1\n" \
"v_mfma_f32_16x16x32_bf16 v[176:179], a[232:235], a[68:71], v[176:179]\n" \
"ds_read_b64_tr_b16 a[218:219], v220 offset:38912\n" \
"v_mfma_f32_16x16x32_bf16 v[180:183], a[204:207], a[80:83], v[180:183]\n" \
"ds_read_b64_tr_b16 a[220:221], v220 offset:40960\n" \
"v_mfma_f32_16x16x32_bf16 v[180:183], a[232:235], a[84:87], v[180:183]\n" \
"ds_read_b64_tr_b16 a[222:223], v220 offset:43008\n" \
"v_mfma_f32_16x16x32_bf16 v[184:187], a[204:207], a[96:99], v[184:187]\n" \
"ds_read_b64_tr_b16 a[228:229], v220 offset:45056\n" \
"buffer_load_dwordx4 a[56:59], v210, %[srdz], s50 offen offset:2048 sc1\n" \
"v_mfma_f32_16x16x32_bf16 v[184:187], a[232:235], a[100:103], v[184:187]\n" \
"ds_read_b64_tr_b16 a[230:231], v220 offset:47104\n" \
"v_mfma_f32_16x16x32_bf16 v[188:191], a[204:207], a[112:115], v[188:191]\n" \
"ds_read_b64_tr_b16 a[224:225], v221 offset:32768\n" \
"s_cbranch_vccz Lg13_%=\n" \
"s_nop 0\n" \
"buffer_store_dwordx4 v[64:67], v209, %[srdz], s60 offen offset:0 sc1\n" \
"Lg13_%=:\n" \
"v_mfma_f32_16x16x32_bf16 v[188:191], a[232:235], a[116:119], v[188:191]\n" \
"ds_read_b64_tr_b16 a[226:227], v221 offset:34816\n" \
"v_mfma_f32_16x16x32_bf16 v[176:179], a[236:239], a[72:75], v[176:179]\n" \
"ds_read_b64_tr_b16 a[196:197], v221 offset:36864\n" \
"buffer_load_dwordx4 a[60:63], v210, %[srdz], s50 offen offset:3072 sc1\n" \
"v_mfma_f32_16x16x32_bf16 v[176:179], a[208:211], a[76:79], v[176:179]\n" \
"ds_read_b64_tr_b16 a[198:199], v221 offset:38912\n" \
"v_mfma_f32_16x16x32_bf16 v[180:183], a[236:239], a[88:91], v[180:183]\n" \
"s_cmp_gt_u32 s48, 2\n" \
"s_cselect_b32 s50, 2, 3\n" \
"s_mul_i32 s50, s50, 152064\n" \
"buffer_load_dwordx4 a[128:131], %[oq0], %[srdz], s50 offen offset:0 sc1\n" \
"v_mfma_f32_16x16x32_bf16 v[180:183], a[208:211], a[92:95], v[180:183]\n" \
"v_mfma_f32_16x16x32_bf16 v[184:187], a[236:239], a[104:107], v[184:187]\n" \
"v_mfma_f32_16x16x32_bf16 v[184:187], a[208:211], a[108:111], v[184:187]\n" \
"v_mfma_f32_16x16x32_bf16 v[188:191], a[236:239], a[120:123], v[188:191]\n" \
"ds_read_b64_tr_b16 a[200:201], v221 offset:40960\n" \
"s_cbranch_vccz Lg14_%=\n" \
"s_nop 0\n" \
"buffer_store_dwordx4 v[72:75], v209, %[srdz], s60 offen offset:1024 sc1\n" \
"Lg14_%=:\n" \
"v_mfma_f32_16x16x32_bf16 v[188:191], a[208:211], a[124:127], v[188:191]\n" \
"s_waitcnt vmcnt(63) lgkmcnt(5)\n" \
"s_nop 0\n" \
"v_mfma_f32_16x16x32_bf16 v[4:7], a[212:215], a[64:67], v[4:7]\n" \
"ds_read_b64_tr_b16 a[202:203], v221 offset:43008\n" \
"v_mfma_f32_16x16x32_bf16 v[4:7], a[216:219], a[68:71], v[4:7]\n" \
"ds_read_b64_tr_b16 a[204:205], v221 offset:45056\n" \
"s_add_u32 m0, %[m0b], 0\n" \
"s_cmp_gt_u32 s48, 2\n" \
"s_cselect_b32 s49, 2, 3\n" \
"s_lshl_b32 s49, s49, 18\n" \
"s_or_b32 s49, s49, s58\n" \
"s_nop 0\n" \
"buffer_load_dwordx4 %[wo0], %[srdt], s49 offen offset:0 lds\n" \
"v_mfma_f32_16x16x32_bf16 v[36:39], a[212:215], a[80:83], v[36:39]\n" \
"ds_read_b64_tr_b16 a[206:207], v221 offset:47104\n" \
"v_mfma_f32_16x16x32_bf16 v[36:39], a[216:219], a[84:87], v[36:39]\n" \
"buffer_load_dwordx4 a[132:135], %[oq0], %[srdz], s50 offen offset:1024 sc1\n" \
"v_mfma_f32_16x16x32_bf16 v[68:71], a[212:215], a[96:99], v[68:71]\n" \
"v_mfma_f32_16x16x32_bf16 v[68:71], a[216:219], a[100:103], v[68:71]\n" \
"v_mfma_f32_16x16x32_bf16 v[100:103], a[212:215], a[112:115], v[100:103]\n" \
"ds_read_b64_tr_b16 a[232:233], v220 offset:33280\n" \
"v_mfma_f32_16x16x32_bf16 v[100:103], a[216:219], a[116:119], v[100:103]\n" \
"ds_read_b64_tr_b16 a[234:235], v220 offset:35328\n" \
"s_cbranch_vccz Lg15_%=\n" \
"s_nop 0\n" \
"buffer_store_dwordx4 v[80:83], v209, %[srdz], s60 offen offset:2048 sc1\n" \
"Lg15_%=:\n" \
"v_mfma_f32_16x16x32_bf16 v[4:7], a[220:223], a[72:75], v[4:7]\n" \
"ds_read_b64_tr_b16 a[236:237], v220 offset:37376\n" \
"s_lshl_b32 s40, %[wid], 12\n" \
"s_nop 0\n" \
"s_mov_b32 s43, 0xffff0000\n" \
"v_mfma_f32_16x16x32_bf16 v[4:7], a[228:231], a[76:79], v[4:7]\n" \
"ds_read_b64_tr_b16 a[238:239], v220 offset:39424\n" \
"buffer_load_dwordx4 a[136:139], %[oq0], %[srdz], s50 offen offset:2048 sc1\n" \
"v_mfma_f32_16x16x32_bf16 v[36:39], a[220:223], a[88:91], v[36:39]\n" \
"v_lshrrev_b32_e32 v222, 7, v224\n" \
"s_sub_u32 s40, %[m0b], s40\n" \
"v_mfma_f32_16x16x32_bf16 v[36:39], a[228:231], a[92:95], v[36:39]\n" \
"v_readlane_b32 s41, %[tlo], 26\n" \
"v_mfma_f32_16x16x32_bf16 v[68:71], a[220:223], a[104:107], v[68:71]\n" \
"v_readlane_b32 s42, %[thi], 26\n" \
"v_mfma_f32_16x16x32_bf16 v[68:71], a[228:231], a[108:111], v[68:71]\n" \
"buffer_load_dwordx4 a[140:143], %[oq0], %[srdz], s50 offen offset:3072 sc1\n" \
"v_mfma_f32_16x16x32_bf16 v[100:103], a[220:223], a[120:123], v[100:103]\n" \
"ds_read_b64_tr_b16 a[208:209], v220 offset:41472\n" \
"v_lshrrev_b32_e64 v217, 3, v224\n" \
"v_mfma_f32_16x16x32_bf16 v[100:103], a[228:231], a[124:127], v[100:103]\n" \
"s_waitcnt vmcnt(63) lgkmcnt(5)\n" \
"s_nop 0\n" \
"v_mfma_f32_16x16x32_bf16 v[12:15], a[224:227], a[64:67], v[12:15]\n" \
"ds_read_b64_tr_b16 a[210:211], v220 offset:43520\n" \
"s_cbranch_vccz Lg16_%=\n" \
"s_nop 0\n" \
"buffer_store_dwordx4 v[88:91], v209, %[srdz], s60 offen offset:3072 sc1\n" \
"Lg16_%=:\n" \
"v_mfma_f32_16x16x32_bf16 v[12:15], a[196:199], a[68:71], v[12:15]\n" \
"ds_read_b64_tr_b16 a[212:213], v220 offset:45568\n" \
"v_lshrrev_b32_e64 v219, 5, v222\n" \
"v_mfma_f32_16x16x32_bf16 v[44:47], a[224:227], a[80:83], v[44:47]\n" \
"ds_read_b64_tr_b16 a[214:215], v220 offset:47616\n" \
"buffer_load_dwordx4 v212, %[srdt], s49 offen offset:1024 lds\n" \
"v_mfma_f32_16x16x32_bf16 v[44:47], a[196:199], a[84:87], v[44:47]\n" \
"v_bfe_u32 v235, v222, 2, 1\n" \
"v_mfma_f32_16x16x32_bf16 v[76:79], a[224:227], a[96:99], v[76:79]\n" \
"v_lshl_or_b32 v219, v219, 1, v235\n" \
"v_mfma_f32_16x16x32_bf16 v[76:79], a[196:199], a[100:103], v[76:79]\n" \
"buffer_load_dwordx4 a[144:147], v208, %[srdz], s50 offen offset:0 sc1\n" \
"v_mfma_f32_16x16x32_bf16 v[108:111], a[224:227], a[112:115], v[108:111]\n" \
"ds_read_b64_tr_b16 a[216:217], v221 offset:33280\n" \
"v_add_u32_e64 v235, s41, v235\n" \
"v_mfma_f32_16x16x32_bf16 v[108:111], a[196:199], a[116:119], v[108:111]\n" \
"ds_read_b64_tr_b16 a[218:219], v221 offset:35328\n" \
"v_sub_u32_e32 %[ta0], 0x7ff, v235\n" \
"v_mfma_f32_16x16x32_bf16 v[12:15], a[200:203], a[72:75], v[12:15]\n" \
"ds_read_b64_tr_b16 a[220:221], v221 offset:41472\n" \
"s_cbranch_vccz Lg17_%=\n" \
"s_nop 0\n" \
"buffer_store_dwordx4 v[96:99], v210, %[srdz], s60 offen offset:0 sc1\n" \
"Lg17_%=:\n" \
"v_mfma_f32_16x16x32_bf16 v[12:15], a[204:207], a[76:79], v[12:15]\n" \
"ds_read_b64_tr_b16 a[222:223], v221 offset:43520\n" \
"v_and_b32_e64 v234, 16, v222\n" \
"v_mfma_f32_16x16x32_bf16 v[44:47], a[200:203], a[88:91], v[44:47]\n" \
"ds_read_b64_tr_b16 a[228:229], v221 offset:37376\n" \
"v_and_b32_e64 v233, 1, v222\n" \
"v_mfma_f32_16x16x32_bf16 v[44:47], a[204:207], a[92:95], v[44:47]\n" \
"ds_read_b64_tr_b16 a[230:231], v221 offset:39424\n" \
"v_cmp_eq_u32_e64 s[68:69], 1, v233\n" \
"v_mfma_f32_16x16x32_bf16 v[76:79], a[200:203], a[104:107], v[76:79]\n" \
"ds_read_b64_tr_b16 a[224:225], v221 offset:45568\n" \
"s_lshl_b32 s52, %[wid], 2\n" \
"s_nop 0\n" \
"v_mfma_f32_16x16x32_bf16 v[76:79], a[204:207], a[108:111], v[76:79]\n" \
"ds_read_b64_tr_b16 a[226:227], v221 offset:47616\n" \
"v_cndmask_b32_e64 v234, v234, 32, s[68:69]\n" \
"v_mfma_f32_16x16x32_bf16 v[108:111], a[200:203], a[120:123], v[108:111]\n" \
"buffer_load_dwordx4 a[148:151], v208, %[srdz], s50 offen offset:1024 sc1\n" \
"v_mfma_f32_16x16x32_bf16 v[108:111], a[204:207], a[124:127], v[108:111]\n" \
"s_waitcnt vmcnt(63) lgkmcnt(12)\n" \
"s_nop 0\n" \
"v_mfma_f32_16x16x32_bf16 v[20:23], a[232:235], a[64:67], v[20:23]\n" \
"s_cbranch_vccz Lg18_%=\n" \
"s_nop 0\n" \
"buffer_store_dwordx4 v[104:107], v210, %[srdz], s60 offen offset:1024 sc1\n" \
"Lg18_%=:\n" \
"v_mfma_f32_16x16x32_bf16 v[20:23], a[236:239], a[68:71], v[20:23]\n" \
"s_add_u32 s52, s52, s40\n" \
"s_nop 0\n" \
"v_mfma_f32_16x16x32_bf16 v[52:55], a[232:235], a[80:83], v[52:55]\n" \
"buffer_load_dwordx4 v213, %[srdt], s49 offen offset:2048 lds\n" \
"v_mfma_f32_16x16x32_bf16 v[52:55], a[236:239], a[84:87], v[52:55]\n" \
"v_mad_u32_u24 v228, v219, 48, v234\n" \
"s_add_u32 s63, s52, 29184\n" \
"v_mfma_f32_16x16x32_bf16 v[84:87], a[232:235], a[96:99], v[84:87]\n" \
"s_waitcnt vmcnt(63) lgkmcnt(0)\n" \
"s_bitcmp1_b32 0x30, s48\n" \
"s_cbranch_scc1 Lcw8_%=\n" \
"s_waitcnt vmcnt(49)\n" \
"Lcw8_%=:\n" \
"s_barrier\n" \
"s_nop 0\n" \
"v_mfma_f32_16x16x32_bf16 v[84:87], a[236:239], a[100:103], v[84:87]\n" \
"ds_read_b64_tr_b16 a[196:197], v220 offset:49152\n" \
"v_add_u32_e64 v228, s63, v228\n" \
"v_mfma_f32_16x16x32_bf16 v[116:119], a[232:235], a[112:115], v[116:119]\n" \
"ds_read_b64_tr_b16 a[198:199], v220 offset:51200\n" \
"buffer_load_dwordx4 a[152:155], v208, %[srdz], s50 offen offset:2048 sc1\n" \
"v_mfma_f32_16x16x32_bf16 v[116:119], a[236:239], a[116:119], v[116:119]\n" \
"ds_read_b64_tr_b16 a[200:201], v220 offset:53248\n" \
"v_and_b32_e64 v216, 15, v222\n" \
"v_mfma_f32_16x16x32_bf16 v[20:23], a[208:211], a[72:75], v[20:23]\n" \
"ds_read_b64_tr_b16 a[202:203], v220 offset:55296\n" \
"v_lshrrev_b32_e64 v218, 4, v222\n" \
"v_mfma_f32_16x16x32_bf16 v[20:23], a[212:215], a[76:79], v[20:23]\n" \
"ds_read_b64_tr_b16 a[204:205], v220 offset:57344\n" \
"s_cbranch_vccz Lg19_%=\n" \
"s_nop 0\n" \
"buffer_store_dwordx4 v[112:115], v210, %[srdz], s60 offen offset:2048 sc1\n" \
"Lg19_%=:\n" \
"v_mfma_f32_16x16x32_bf16 v[52:55], a[208:211], a[88:91], v[52:55]\n" \
"ds_read_b64_tr_b16 a[206:207], v220 offset:59392\n" \
"v_lshrrev_b32_e64 v230, 3, v216\n" \
"v_mfma_f32_16x16x32_bf16 v[52:55], a[212:215], a[92:95], v[52:55]\n" \
"ds_read_b64_tr_b16 a[232:233], v220 offset:61440\n" \
"v_lshl_or_b32 v230, v230, 1, v233\n" \
"v_mfma_f32_16x16x32_bf16 v[84:87], a[208:211], a[104:107], v[84:87]\n" \
"ds_read_b64_tr_b16 a[234:235], v220 offset:63488\n" \
"s_add_u32 s63, s40, 29184\n" \
"v_mfma_f32_16x16x32_bf16 v[84:87], a[212:215], a[108:111], v[84:87]\n" \
"buffer_load_dwordx4 a[156:159], v208, %[srdz], s50 offen offset:3072 sc1\n" \
"v_mfma_f32_16x16x32_bf16 v[116:119], a[208:211], a[120:123], v[116:119]\n" \
"v_mad_u32_u24 %[vg0], v230, 48, s63\n" \
"s_add_u32 s63, s52, 25600\n" \
"v_mfma_f32_16x16x32_bf16 v[116:119], a[212:215], a[124:127], v[116:119]\n" \
"s_waitcnt vmcnt(20) lgkmcnt(4)\n" \
"s_bitcmp1_b32 0x30, s48\n" \
"s_cbranch_scc1 Lcw9_%=\n" \
"s_waitcnt vmcnt(13)\n" \
"Lcw9_%=:\n" \
"v_mfma_f32_16x16x32_bf16 v[28:31], a[216:219], a[64:67], v[28:31]\n" \
"v_lshlrev_b32_e64 v235, 6, v218\n" \
"v_mfma_f32_16x16x32_bf16 v[28:31], a[228:231], a[68:71], v[28:31]\n" \
"s_cbranch_vccz Lg20_%=\n" \
"s_nop 0\n" \
"buffer_store_dwordx4 v[120:123], v210, %[srdz], s60 offen offset:3072 sc1\n" \
"Lg20_%=:\n" \
"v_mfma_f32_16x16x32_bf16 v[60:63], a[216:219], a[80:83], v[60:63]\n" \
"v_add_u32_e32 v234, s63, v235\n" \
"s_movk_i32 s52, 272\n" \
"v_mfma_f32_16x16x32_bf16 v[60:63], a[228:231], a[84:87], v[60:63]\n" \
"buffer_load_dwordx4 v214, %[srdt], s49 offen offset:3072 lds\n" \
"v_mfma_f32_16x16x32_bf16 v[92:95], a[216:219], a[96:99], v[92:95]\n" \
"v_mad_u32_u24 %[kd0], v216, s52, v234\n" \
"v_mfma_f32_16x16x32_bf16 v[92:95], a[228:231], a[100:103], v[92:95]\n" \
"buffer_load_dwordx4 a[160:163], v209, %[srdz], s50 offen offset:0 sc1\n" \
"v_mfma_f32_16x16x32_bf16 v[124:127], a[216:219], a[112:115], v[124:127]\n" \
"ds_read_b64_tr_b16 a[236:237], v221 offset:49152\n" \
"v_bfe_u32 v234, v222, 1, 2\n" \
"v_mfma_f32_16x16x32_bf16 v[124:127], a[228:231], a[116:119], v[124:127]\n" \
"ds_read_b64_tr_b16 a[238:239], v221 offset:51200\n" \
"v_lshl_add_u32 %[vx0], v234, 4, v235\n" \
"v_mfma_f32_16x16x32_bf16 v[192:195], a[196:199], a[0:3], v[128:131]\n" \
"ds_read_b64_tr_b16 a[208:209], v221 offset:53248\n" \
"s_add_u32 s63, s40, 25600\n" \
"v_mfma_f32_16x16x32_bf16 v[192:195], a[200:203], a[4:7], v[192:195]\n" \
"ds_read_b64_tr_b16 a[210:211], v221 offset:55296\n" \
"v_add_u32_e64 %[vx0], s63, %[vx0]\n" \
"v_mfma_f32_16x16x32_bf16 v[196:199], a[196:199], a[16:19], v[132:135]\n" \
"buffer_load_dwordx4 a[164:167], v209, %[srdz], s50 offen offset:1024 sc1\n" \
"v_mfma_f32_16x16x32_bf16 v[196:199], a[200:203], a[20:23], v[196:199]\n" \
"s_movk_i32 s52, 2112\n" \
"s_nop 0\n" \
"v_mfma_f32_16x16x32_bf16 v[200:203], a[196:199], a[32:35], v[136:139]\n" \
"v_mul_u32_u24_e64 v211, s52, v216\n" \
"v_mfma_f32_16x16x32_bf16 v[200:203], a[200:203], a[36:39], v[200:203]\n" \
"v_add_u32_e32 v211, 16384, v211\n" \
"v_mfma_f32_16x16x32_bf16 v[204:207], a[196:199], a[48:51], v[140:143]\n" \
"ds_read_b64_tr_b16 a[212:213], v221 offset:57344\n" \
"s_sub_u32 s61, s48, 6\n" \
"s_cmp_lt_u32 s61, 2\n" \
"s_cselect_b64 vcc, -1, 0\n" \
"s_nop 0\n" \
"v_mov_b32_e32 v235, 2048\n" \
"v_mfma_f32_16x16x32_bf16 v[204:207], a[200:203], a[52:55], v[204:207]\n" \
"s_waitcnt vmcnt(21) lgkmcnt(5)\n" \
"s_bitcmp1_b32 0x30, s48\n" \
"s_cbranch_scc1 Lcw10_%=\n" \
"s_waitcnt vmcnt(14)\n" \
"Lcw10_%=:\n" \
"v_mfma_f32_16x16x32_bf16 v[192:195], a[204:207], a[8:11], v[192:195]\n" \
"ds_read_b64_tr_b16 a[214:215], v221 offset:59392\n" \
"s_cbranch_vccz Lg21_%=\n" \
"s_nop 0\n" \
"buffer_store_dwordx4 v[0:3], %[oq0], %[srdz], s60 offen offset:0 sc1\n" \
"Lg21_%=:\n" \
"v_mfma_f32_16x16x32_bf16 v[192:195], a[232:235], a[12:15], v[192:195]\n" \
"ds_read_b64_tr_b16 a[216:217], v221 offset:61440\n" \
"v_cmp_eq_u32_e64 s[68:69], 4, v216\n" \
"v_mfma_f32_16x16x32_bf16 v[196:199], a[204:207], a[24:27], v[196:199]\n" \
"ds_read_b64_tr_b16 a[218:219], v221 offset:63488\n" \
"s_lshl_b32 s52, %[wid], 9\n" \
"s_nop 0\n" \
"v_mfma_f32_16x16x32_bf16 v[196:199], a[232:235], a[28:31], v[196:199]\n" \
"buffer_load_dwordx4 a[168:171], v209, %[srdz], s50 offen offset:2048 sc1\n" \
"v_mfma_f32_16x16x32_bf16 v[200:203], a[204:207], a[40:43], v[200:203]\n" \
"v_cndmask_b32_e64 v235, 0, v235, s[68:69]\n" \
"v_mfma_f32_16x16x32_bf16 v[200:203], a[232:235], a[44:47], v[200:203]\n" \
"v_cmp_gt_u32_e64 s[68:69], 4, v216\n" \
"s_add_u32 s52, s52, s40\n" \
"s_nop 0\n" \
"v_mfma_f32_16x16x32_bf16 v[204:207], a[204:207], a[56:59], v[204:207]\n" \
"ds_read_b64_tr_b16 a[228:229], v220 offset:49664\n" \
"v_cndmask_b32_e64 v211, v235, v211, s[68:69]\n" \
"v_mfma_f32_16x16x32_bf16 v[204:207], a[232:235], a[60:63], v[204:207]\n" \
"ds_read_b64_tr_b16 a[230:231], v220 offset:51712\n" \
"v_lshlrev_b32_e64 v235, 4, v218\n" \
"v_mfma_f32_16x16x32_bf16 v[28:31], a[220:223], a[72:75], v[28:31]\n" \
"s_cbranch_vccz Lg22_%=\n" \
"s_nop 0\n" \
"buffer_store_dwordx4 v[8:11], %[oq0], %[srdz], s60 offen offset:1024 sc1\n" \
"Lg22_%=:\n" \
"v_mfma_f32_16x16x32_bf16 v[28:31], a[224:227], a[76:79], v[28:31]\n" \
"v_add3_u32 v211, v211, v235, s52\n" \
"v_mfma_f32_16x16x32_bf16 v[60:63], a[220:223], a[88:91], v[60:63]\n" \
"buffer_load_dwordx4 a[172:175], v209, %[srdz], s50 offen offset:3072 sc1\n" \
"v_mfma_f32_16x16x32_bf16 v[60:63], a[224:227], a[92:95], v[60:63]\n" \
"v_and_b32_e32 v216, 0x2b0, v217\n" \
"v_mfma_f32_16x16x32_bf16 v[92:95], a[220:223], a[104:107], v[92:95]\n" \
"v_lshrrev_b32_e64 v218, 2, v217\n" \
"v_mfma_f32_16x16x32_bf16 v[92:95], a[224:227], a[108:111], v[92:95]\n" \
"v_cmp_u_f32_e64 s[44:45], v192, v196\n" \
"v_and_or_b32 v216, v218, 64, v216\n" \
"s_add_u32 s52, s40, s42\n" \
"s_nop 0\n" \
"v_mfma_f32_16x16x32_bf16 v[124:127], a[220:223], a[120:123], v[124:127]\n" \
"s_add_u32 m0, %[m0b], 16384\n" \
"s_cmp_gt_u32 s48, 2\n" \
"s_cselect_b32 s49, 2, 3\n" \
"s_lshl_b32 s49, s49, 18\n" \
"s_or_b32 s49, s49, s59\n" \
"buffer_load_dwordx4 %[wo0], %[srdt], s49 offen offset:0 lds\n" \
"v_mfma_f32_16x16x32_bf16 v[124:127], a[224:227], a[124:127], v[124:127]\n" \
"v_cmp_u_f32_e64 s[56:57], v200, v204\n" \
"s_or_b64 s[56:57], s[56:57], s[44:45]\n" \
"s_cbranch_scc1 Lsp2_%=\n" \
"Lbk2_%=:\n" \
"s_waitcnt vmcnt(63) lgkmcnt(2)\n" \
"s_nop 0\n" \
"v_mfma_f32_16x16x32_bf16 v[144:147], a[236:239], a[0:3], v[144:147]\n" \
"ds_read_b64_tr_b16 a[220:221], v220 offset:53760\n" \
"v_add_u32_e64 %[kb0], s52, v216\n" \
"v_mfma_f32_16x16x32_bf16 v[144:147], a[208:211], a[4:7], v[144:147]\n" \
"ds_read_b64_tr_b16 a[222:223], v220 offset:55808\n" \
"s_cbranch_vccz Lg23_%=\n" \
"s_nop 0\n" \
"buffer_store_dwordx4 v[16:19], %[oq0], %[srdz], s60 offen offset:2048 sc1\n" \
"Lg23_%=:\n" \
"v_mfma_f32_16x16x32_bf16 v[148:151], a[236:239], a[16:19], v[148:151]\n" \
"ds_read_b64_tr_b16 a[196:197], v220 offset:57856\n" \
"v_xor_b32_e32 %[vvb], 0x100, %[kb0]\n" \
"v_mfma_f32_16x16x32_bf16 v[148:151], a[208:211], a[20:23], v[148:151]\n" \
"ds_read_b64_tr_b16 a[198:199], v220 offset:59904\n" \
"buffer_load_dwordx4 a[176:179], v210, %[srdz], s50 offen offset:0 sc1\n" \
"v_mfma_f32_16x16x32_bf16 v[152:155], a[236:239], a[32:35], v[152:155]\n" \
"ds_read_b64_tr_b16 a[200:201], v220 offset:61952\n" \
"v_and_b32_e32 v235, 0x200, v217\n" \
"v_mfma_f32_16x16x32_bf16 v[152:155], a[208:211], a[36:39], v[152:155]\n" \
"ds_read_b64_tr_b16 a[202:203], v220 offset:64000\n" \
"v_and_b32_e64 v234, 8, v222\n" \
"v_mfma_f32_16x16x32_bf16 v[156:159], a[236:239], a[48:51], v[156:159]\n" \
"ds_read_b64_tr_b16 a[204:205], v221 offset:49664\n" \
"v_lshl_or_b32 v235, v234, 5, v235\n" \
"v_mfma_f32_16x16x32_bf16 v[156:159], a[208:211], a[52:55], v[156:159]\n" \
"ds_read_b64_tr_b16 a[206:207], v221 offset:51712\n" \
"v_and_b32_e64 v234, 16, v222\n" \
"v_mfma_f32_16x16x32_bf16 v[144:147], a[212:215], a[8:11], v[144:147]\n" \
"ds_read_b64_tr_b16 a[224:225], v221 offset:53760\n" \
"v_lshl_or_b32 v235, v234, 1, v235\n" \
"v_mfma_f32_16x16x32_bf16 v[144:147], a[216:219], a[12:15], v[144:147]\n" \
"ds_read_b64_tr_b16 a[226:227], v221 offset:55808\n" \
"v_lshl_or_b32 v235, v233, 6, v235\n" \
"v_mfma_f32_16x16x32_bf16 v[148:151], a[212:215], a[24:27], v[148:151]\n" \
"s_cbranch_vccz Lg24_%=\n" \
"s_nop 0\n" \
"buffer_store_dwordx4 v[24:27], %[oq0], %[srdz], s60 offen offset:3072 sc1\n" \
"Lg24_%=:\n" \
"v_mfma_f32_16x16x32_bf16 v[148:151], a[216:219], a[28:31], v[148:151]\n" \
"v_and_b32_e64 v216, 2, v222\n" \
"v_mfma_f32_16x16x32_bf16 v[152:155], a[212:215], a[40:43], v[152:155]\n" \
"buffer_load_dwordx4 a[180:183], v210, %[srdz], s50 offen offset:1024 sc1\n" \
"v_mfma_f32_16x16x32_bf16 v[152:155], a[216:219], a[44:47], v[152:155]\n" \
"v_lshl_or_b32 v235, v216, 3, v235\n" \
"s_lshr_b32 s52, s42, 1\n" \
"s_nop 0\n" \
"v_mfma_f32_16x16x32_bf16 v[156:159], a[212:215], a[56:59], v[156:159]\n" \
"ds_read_b64_tr_b16 a[232:233], v221 offset:57856\n" \
"v_sub_u32_e32 v234, 0x7ff, %[ta0]\n" \
"v_mfma_f32_16x16x32_bf16 v[156:159], a[216:219], a[60:63], v[156:159]\n" \
"s_waitcnt vmcnt(63) lgkmcnt(5)\n" \
"s_nop 0\n" \
"v_mfma_f32_16x16x32_bf16 v[160:163], a[228:231], a[0:3], v[160:163]\n" \
"ds_read_b64_tr_b16 a[234:235], v221 offset:59904\n" \
"buffer_load_dwordx4 v212, %[srdt], s49 offen offset:1024 lds\n" \
"v_mfma_f32_16x16x32_bf16 v[160:163], a[220:223], a[4:7], v[160:163]\n" \
"ds_read_b64_tr_b16 a[236:237], v221 offset:61952\n" \
"v_lshlrev_b32_e64 v234, 11, v234\n" \
"v_mfma_f32_16x16x32_bf16 v[164:167], a[228:231], a[16:19], v[164:167]\n" \
"ds_read_b64_tr_b16 a[238:239], v221 offset:64000\n" \
"v_add3_u32 %[vd0], v234, v235, s52\n" \
"v_mfma_f32_16x16x32_bf16 v[164:167], a[220:223], a[20:23], v[164:167]\n" \
"s_cbranch_vccz Lg25_%=\n" \
"s_nop 0\n" \
"buffer_store_dwordx4 v[32:35], v208, %[srdz], s60 offen offset:0 sc1\n" \
"Lg25_%=:\n" \
"v_mfma_f32_16x16x32_bf16 v[168:171], a[228:231], a[32:35], v[168:171]\n" \
"v_mfma_f32_16x16x32_bf16 v[168:171], a[220:223], a[36:39], v[168:171]\n" \
"buffer_load_dwordx4 a[184:187], v210, %[srdz], s50 offen offset:2048 sc1\n" \
"v_mfma_f32_16x16x32_bf16 v[172:175], a[228:231], a[48:51], v[172:175]\n" \
"v_mfma_f32_16x16x32_bf16 v[172:175], a[220:223], a[52:55], v[172:175]\n" \
"buffer_load_dwordx4 a[188:191], v210, %[srdz], s50 offen offset:3072 sc1\n" \
"v_mfma_f32_16x16x32_bf16 v[160:163], a[196:199], a[8:11], v[160:163]\n" \
"v_mfma_f32_16x16x32_bf16 v[160:163], a[200:203], a[12:15], v[160:163]\n" \
"v_mfma_f32_16x16x32_bf16 v[164:167], a[196:199], a[24:27], v[164:167]\n" \
"v_mfma_f32_16x16x32_bf16 v[164:167], a[200:203], a[28:31], v[164:167]\n" \
"v_mfma_f32_16x16x32_bf16 v[168:171], a[196:199], a[40:43], v[168:171]\n" \
"s_waitcnt vmcnt(63) lgkmcnt(0)\n" \
"s_bitcmp1_b32 0x30, s48\n" \
"s_cbranch_scc1 Lcw11_%=\n" \
"s_waitcnt vmcnt(60)\n" \
"s_bitcmp1_b32 0xc0, s48\n" \
"s_cbranch_scc1 Lcw11_%=\n" \
"s_waitcnt vmcnt(55)\n" \
"Lcw11_%=:\n" \
"s_barrier\n" \
"s_nop 0\n" \
"v_mfma_f32_16x16x32_bf16 v[168:171], a[200:203], a[44:47], v[168:171]\n" \
"ds_read_b64_tr_b16 a[208:209], v215 offset:49152\n" \
"s_cbranch_vccz Lg26_%=\n" \
"s_nop 0\n" \
"buffer_store_dwordx4 v[40:43], v208, %[srdz], s60 offen offset:1024 sc1\n" \
"Lg26_%=:\n" \
"v_mfma_f32_16x16x32_bf16 v[172:175], a[196:199], a[56:59], v[172:175]\n" \
"ds_read_b64_tr_b16 a[210:211], v215 offset:51200\n" \
"v_mfma_f32_16x16x32_bf16 v[172:175], a[200:203], a[60:63], v[172:175]\n" \
"s_waitcnt vmcnt(63) lgkmcnt(2)\n" \
"s_nop 0\n" \
"v_mfma_f32_16x16x32_bf16 v[176:179], a[204:207], a[0:3], v[176:179]\n" \
"ds_read_b64_tr_b16 a[212:213], v215 offset:53248\n" \
"global_load_dword v237, %[vct], %[pct]\n" \
"v_mfma_f32_16x16x32_bf16 v[176:179], a[224:227], a[4:7], v[176:179]\n" \
"ds_read_b64_tr_b16 a[214:215], v215 offset:55296\n" \
"v_mfma_f32_16x16x32_bf16 v[180:183], a[204:207], a[16:19], v[180:183]\n" \
"ds_read_b64_tr_b16 a[216:217], v215 offset:57344\n" \
"buffer_load_dwordx4 v213, %[srdt], s49 offen offset:2048 lds\n" \
"v_mfma_f32_16x16x32_bf16 v[180:183], a[224:227], a[20:23], v[180:183]\n" \
"ds_read_b64_tr_b16 a[218:219], v215 offset:59392\n" \
"v_mfma_f32_16x16x32_bf16 v[184:187], a[204:207], a[32:35], v[184:187]\n" \
"ds_read_b64_tr_b16 a[228:229], v215 offset:61440\n" \
"v_mfma_f32_16x16x32_bf16 v[184:187], a[224:227], a[36:39], v[184:187]\n" \
"ds_read_b64_tr_b16 a[230:231], v215 offset:63488\n" \
"s_cbranch_vccz Lg27_%=\n" \
"s_nop 0\n" \
"buffer_store_dwordx4 v[48:51], v208, %[srdz], s60 offen offset:2048 sc1\n" \
"Lg27_%=:\n" \
"v_mfma_f32_16x16x32_bf16 v[188:191], a[204:207], a[48:51], v[188:191]\n" \
"ds_read_b64_tr_b16 a[220:221], v227 offset:49152\n" \
"v_mfma_f32_16x16x32_bf16 v[188:191], a[224:227], a[52:55], v[188:191]\n" \
"ds_read_b64_tr_b16 a[222:223], v227 offset:51200\n" \
"v_mfma_f32_16x16x32_bf16 v[176:179], a[232:235], a[8:11], v[176:179]\n" \
"ds_read_b64_tr_b16 a[196:197], v227 offset:53248\n" \
"v_mfma_f32_16x16x32_bf16 v[176:179], a[236:239], a[12:15], v[176:179]\n" \
"ds_read_b64_tr_b16 a[198:199], v227 offset:55296\n" \
"s_cmp_gt_u32 s48, 3\n" \
"s_cselect_b32 s50, 3, 4\n" \
"s_mul_i32 s50, s50, 152064\n" \
"buffer_load_dwordx4 a[64:67], %[oq0], %[srdz], s50 offen offset:0 sc1\n" \
"v_mfma_f32_16x16x32_bf16 v[180:183], a[232:235], a[24:27], v[180:183]\n" \
"v_mfma_f32_16x16x32_bf16 v[180:183], a[236:239], a[28:31], v[180:183]\n" \
"buffer_load_dwordx4 v214, %[srdt], s49 offen offset:3072 lds\n" \
"v_mfma_f32_16x16x32_bf16 v[184:187], a[232:235], a[40:43], v[184:187]\n" \
"v_mfma_f32_16x16x32_bf16 v[184:187], a[236:239], a[44:47], v[184:187]\n" \
"v_mfma_f32_16x16x32_bf16 v[188:191], a[232:235], a[56:59], v[188:191]\n" \
"ds_read_b64_tr_b16 a[200:201], v227 offset:57344\n" \
"s_cbranch_vccz Lg28_%=\n" \
"s_nop 0\n" \
"buffer_store_dwordx4 v[56:59], v208, %[srdz], s60 offen offset:3072 sc1\n" \
"Lg28_%=:\n" \
"v_mfma_f32_16x16x32_bf16 v[188:191], a[236:239], a[60:63], v[188:191]\n" \
"s_waitcnt vmcnt(63) lgkmcnt(5)\n" \
"s_nop 0\n" \
"v_mfma_f32_16x16x32_bf16 v[4:7], a[208:211], a[0:3], v[4:7]\n" \
"ds_read_b64_tr_b16 a[202:203], v227 offset:59392\n" \
"buffer_load_dwordx4 a[68:71], %[oq0], %[srdz], s50 offen offset:1024 sc1\n" \
"v_mfma_f32_16x16x32_bf16 v[4:7], a[212:215], a[4:7], v[4:7]\n" \
"ds_read_b64_tr_b16 a[204:205], v227 offset:61440\n" \
"v_mfma_f32_16x16x32_bf16 v[36:39], a[208:211], a[16:19], v[36:39]\n" \
"ds_read_b64_tr_b16 a[206:207], v227 offset:63488\n" \
"v_mfma_f32_16x16x32_bf16 v[36:39], a[212:215], a[20:23], v[36:39]\n" \
"v_mfma_f32_16x16x32_bf16 v[68:71], a[208:211], a[32:35], v[68:71]\n" \
"buffer_load_dwordx4 a[72:75], %[oq0], %[srdz], s50 offen offset:2048 sc1\n" \
"v_mfma_f32_16x16x32_bf16 v[68:71], a[212:215], a[36:39], v[68:71]\n" \
"v_mfma_f32_16x16x32_bf16 v[100:103], a[208:211], a[48:51], v[100:103]\n" \
"ds_read_b64_tr_b16 a[224:225], v215 offset:49664\n" \
"v_mfma_f32_16x16x32_bf16 v[100:103], a[212:215], a[52:55], v[100:103]\n" \
"ds_read_b64_tr_b16 a[226:227], v215 offset:51712\n" \
"s_cbranch_vccz Lg29_%=\n" \
"s_nop 0\n" \
"buffer_store_dwordx4 v[64:67], v209, %[srdz], s60 offen offset:0 sc1\n" \
"Lg29_%=:\n" \
"v_mfma_f32_16x16x32_bf16 v[4:7], a[216:219], a[8:11], v[4:7]\n" \
"ds_read_b64_tr_b16 a[232:233], v215 offset:53760\n" \
"v_mfma_f32_16x16x32_bf16 v[4:7], a[228:231], a[12:15], v[4:7]\n" \
"ds_read_b64_tr_b16 a[234:235], v215 offset:55808\n" \
"v_mfma_f32_16x16x32_bf16 v[36:39], a[216:219], a[24:27], v[36:39]\n" \
"v_mfma_f32_16x16x32_bf16 v[36:39], a[228:231], a[28:31], v[36:39]\n" \
"buffer_load_dwordx4 a[76:79], %[oq0], %[srdz], s50 offen offset:3072 sc1\n" \
"v_mfma_f32_16x16x32_bf16 v[68:71], a[216:219], a[40:43], v[68:71]\n" \
"v_mfma_f32_16x16x32_bf16 v[68:71], a[228:231], a[44:47], v[68:71]\n" \
"v_mfma_f32_16x16x32_bf16 v[100:103], a[216:219], a[56:59], v[100:103]\n" \
"ds_read_b64_tr_b16 a[236:237], v215 offset:57856\n" \
"v_mfma_f32_16x16x32_bf16 v[100:103], a[228:231], a[60:63], v[100:103]\n" \
"s_waitcnt vmcnt(63) lgkmcnt(5)\n" \
"s_nop 0\n" \
"v_mfma_f32_16x16x32_bf16 v[12:15], a[220:223], a[0:3], v[12:15]\n" \
"ds_read_b64_tr_b16 a[238:239], v215 offset:59904\n" \
"s_cbranch_vccz Lg30_%=\n" \
"s_nop 0\n" \
"buffer_store_dwordx4 v[72:75], v209, %[srdz], s60 offen offset:1024 sc1\n" \
"Lg30_%=:\n" \
"v_mfma_f32_16x16x32_bf16 v[12:15], a[196:199], a[4:7], v[12:15]\n" \
"ds_read_b64_tr_b16 a[208:209], v215 offset:61952\n" \
"v_mfma_f32_16x16x32_bf16 v[44:47], a[220:223], a[16:19], v[44:47]\n" \
"ds_read_b64_tr_b16 a[210:211], v215 offset:64000\n" \
"v_mfma_f32_16x16x32_bf16 v[44:47], a[196:199], a[20:23], v[44:47]\n" \
"buffer_load_dwordx4 a[80:83], v208, %[srdz], s50 offen offset:0 sc1\n" \
"v_mfma_f32_16x16x32_bf16 v[76:79], a[220:223], a[32:35], v[76:79]\n" \
"v_mfma_f32_16x16x32_bf16 v[76:79], a[196:199], a[36:39], v[76:79]\n" \
"s_add_u32 m0, %[m0b], 32768\n" \
"s_cmp_gt_u32 s48, 3\n" \
"s_cselect_b32 s49, 3, 4\n" \
"s_lshl_b32 s49, s49, 18\n" \
"s_or_b32 s49, s49, s58\n" \
"buffer_load_dwordx4 %[wo0], %[srdt], s49 offen offset:0 lds\n" \
"v_mfma_f32_16x16x32_bf16 v[108:111], a[220:223], a[48:51], v[108:111]\n" \
"ds_read_b64_tr_b16 a[212:213], v227 offset:49664\n" \
"v_mfma_f32_16x16x32_bf16 v[108:111], a[196:199], a[52:55], v[108:111]\n" \
"ds_read_b64_tr_b16 a[214:215], v227 offset:51712\n" \
"v_mfma_f32_16x16x32_bf16 v[12:15], a[200:203], a[8:11], v[12:15]\n" \
"ds_read_b64_tr_b16 a[216:217], v227 offset:57856\n" \
"v_mfma_f32_16x16x32_bf16 v[12:15], a[204:207], a[12:15], v[12:15]\n" \
"ds_read_b64_tr_b16 a[218:219], v227 offset:59904\n" \
"s_cbranch_vccz Lg31_%=\n" \
"s_nop 0\n" \
"buffer_store_dwordx4 v[80:83], v209, %[srdz], s60 offen offset:2048 sc1\n" \
"Lg31_%=:\n" \
"v_mfma_f32_16x16x32_bf16 v[44:47], a[200:203], a[24:27], v[44:47]\n" \
"ds_read_b64_tr_b16 a[228:229], v227 offset:53760\n" \
"v_mfma_f32_16x16x32_bf16 v[44:47], a[204:207], a[28:31], v[44:47]\n" \
"ds_read_b64_tr_b16 a[230:231], v227 offset:55808\n" \
"v_mfma_f32_16x16x32_bf16 v[76:79], a[200:203], a[40:43], v[76:79]\n" \
"ds_read_b64_tr_b16 a[220:221], v227 offset:61952\n" \
"buffer_load_dwordx4 a[84:87], v208, %[srdz], s50 offen offset:1024 sc1\n" \
"v_mfma_f32_16x16x32_bf16 v[76:79], a[204:207], a[44:47], v[76:79]\n" \
"ds_read_b64_tr_b16 a[222:223], v227 offset:64000\n" \
"v_mfma_f32_16x16x32_bf16 v[108:111], a[200:203], a[56:59], v[108:111]\n" \
"v_mfma_f32_16x16x32_bf16 v[108:111], a[204:207], a[60:63], v[108:111]\n" \
"s_waitcnt vmcnt(63) lgkmcnt(12)\n" \
"s_nop 0\n" \
"v_mfma_f32_16x16x32_bf16 v[20:23], a[224:227], a[0:3], v[20:23]\n" \
"v_mfma_f32_16x16x32_bf16 v[20:23], a[232:235], a[4:7], v[20:23]\n" \
"v_mfma_f32_16x16x32_bf16 v[52:55], a[224:227], a[16:19], v[52:55]\n" \
"s_cbranch_vccz Lg32_%=\n" \
"s_nop 0\n" \
"buffer_store_dwordx4 v[88:91], v209, %[srdz], s60 offen offset:3072 sc1\n" \
"Lg32_%=:\n" \
"v_mfma_f32_16x16x32_bf16 v[52:55], a[232:235], a[20:23], v[52:55]\n" \
"v_mfma_f32_16x16x32_bf16 v[84:87], a[224:227], a[32:35], v[84:87]\n" \
"s_waitcnt vmcnt(32) lgkmcnt(0)\n" \
"s_nop 0\n" \
"s_bitcmp1_b32 0xc0, s48\n" \
"s_cbranch_scc1 Lcw12_%=\n" \
"s_waitcnt vmcnt(20)\n" \
"Lcw12_%=:\n" \
"s_barrier\n" \
"s_nop 0\n" \
"v_mfma_f32_16x16x32_bf16 v[84:87], a[232:235], a[36:39], v[84:87]\n" \
"ds_read_b64_tr_b16 a[196:197], v215 offset:0\n" \
"buffer_load_dwordx4 a[88:91], v208, %[srdz], s50 offen offset:2048 sc1\n" \
"v_mfma_f32_16x16x32_bf16 v[116:119], a[224:227], a[48:51], v[116:119]\n" \
"ds_read_b64_tr_b16 a[198:199], v215 offset:2048\n" \
"v_mfma_f32_16x16x32_bf16 v[116:119], a[232:235], a[52:55], v[116:119]\n" \
"ds_read_b64_tr_b16 a[200:201], v215 offset:4096\n" \
"buffer_load_dwordx4 v212, %[srdt], s49 offen offset:1024 lds\n" \
"v_mfma_f32_16x16x32_bf16 v[20:23], a[236:239], a[8:11], v[20:23]\n" \
"ds_read_b64_tr_b16 a[202:203], v215 offset:6144\n" \
"v_mfma_f32_16x16x32_bf16 v[20:23], a[208:211], a[12:15], v[20:23]\n" \
"ds_read_b64_tr_b16 a[204:205], v215 offset:8192\n" \
"v_mfma_f32_16x16x32_bf16 v[52:55], a[236:239], a[24:27], v[52:55]\n" \
"ds_read_b64_tr_b16 a[206:207], v215 offset:10240\n" \
"v_mfma_f32_16x16x32_bf16 v[52:55], a[208:211], a[28:31], v[52:55]\n" \
"ds_read_b64_tr_b16 a[224:225], v215 offset:12288\n" \
"s_cbranch_vccz Lg33_%=\n" \
"s_nop 0\n" \
"buffer_store_dwordx4 v[96:99], v210, %[srdz], s60 offen offset:0 sc1\n" \
"Lg33_%=:\n" \
"v_mfma_f32_16x16x32_bf16 v[84:87], a[236:239], a[40:43], v[84:87]\n" \
"ds_read_b64_tr_b16 a[226:227], v215 offset:14336\n" \
"v_mfma_f32_16x16x32_bf16 v[84:87], a[208:211], a[44:47], v[84:87]\n" \
"buffer_load_dwordx4 a[92:95], v208, %[srdz], s50 offen offset:3072 sc1\n" \
"v_mfma_f32_16x16x32_bf16 v[116:119], a[236:239], a[56:59], v[116:119]\n" \
"v_mfma_f32_16x16x32_bf16 v[116:119], a[208:211], a[60:63], v[116:119]\n" \
"s_waitcnt vmcnt(25) lgkmcnt(4)\n" \
"s_nop 0\n" \
"s_bitcmp1_b32 0xc0, s48\n" \
"s_cbranch_scc1 Lcw13_%=\n" \
"s_waitcnt vmcnt(16)\n" \
"Lcw13_%=:\n" \
"v_mfma_f32_16x16x32_bf16 v[28:31], a[212:215], a[0:3], v[28:31]\n" \
"buffer_load_dwordx4 a[96:99], v209, %[srdz], s50 offen offset:0 sc1\n" \
"v_mfma_f32_16x16x32_bf16 v[28:31], a[228:231], a[4:7], v[28:31]\n" \
"v_mfma_f32_16x16x32_bf16 v[60:63], a[212:215], a[16:19], v[60:63]\n" \
"buffer_load_dwordx4 v213, %[srdt], s49 offen offset:2048 lds\n" \
"v_mfma_f32_16x16x32_bf16 v[60:63], a[228:231], a[20:23], v[60:63]\n" \
"v_mfma_f32_16x16x32_bf16 v[92:95], a[212:215], a[32:35], v[92:95]\n" \
"s_cbranch_vccz Lg34_%=\n" \
"s_nop 0\n" \
"buffer_store_dwordx4 v[104:107], v210, %[srdz], s60 offen offset:1024 sc1\n" \
"Lg34_%=:\n" \
"v_mfma_f32_16x16x32_bf16 v[92:95], a[228:231], a[36:39], v[92:95]\n" \
"v_mfma_f32_16x16x32_bf16 v[124:127], a[212:215], a[48:51], v[124:127]\n" \
"ds_read_b64_tr_b16 a[232:233], v227 offset:0\n" \
"v_mfma_f32_16x16x32_bf16 v[124:127], a[228:231], a[52:55], v[124:127]\n" \
"ds_read_b64_tr_b16 a[234:235], v227 offset:2048\n" \
"v_mfma_f32_16x16x32_bf16 v[128:131], a[196:199], a[128:131], v[192:195]\n" \
"ds_read_b64_tr_b16 a[236:237], v227 offset:4096\n" \
"v_mfma_f32_16x16x32_bf16 v[128:131], a[200:203], a[132:135], v[128:131]\n" \
"ds_read_b64_tr_b16 a[238:239], v227 offset:6144\n" \
".if . - adaln_body != 49152\n" \
".error \"size model\"\n" \
".endif\n" \
"Lk11_%=:\n" \
"s_cbranch_execz adaln_kr\n" \
"s_nop 0\n" \
"buffer_load_dwordx4 a[100:103], v209, %[srdz], s50 offen offset:1024 sc1\n" \
"v_mfma_f32_16x16x32_bf16 v[132:135], a[196:199], a[144:147], v[196:199]\n" \
"v_mfma_f32_16x16x32_bf16 v[132:135], a[200:203], a[148:151], v[132:135]\n" \
"v_mfma_f32_16x16x32_bf16 v[136:139], a[196:199], a[160:163], v[200:203]\n" \
"v_mfma_f32_16x16x32_bf16 v[136:139], a[200:203], a[164:167], v[136:139]\n" \
"s_cbranch_vccz Lg35_%=\n" \
"s_nop 0\n" \
"buffer_store_dwordx4 v[112:115], v210, %[srdz], s60 offen offset:2048 sc1\n" \
"Lg35_%=:\n" \
"v_mfma_f32_16x16x32_bf16 v[140:143], a[196:199], a[176:179], v[204:207]\n" \
"ds_read_b64_tr_b16 a[208:209], v227 offset:8192\n" \
"v_mfma_f32_16x16x32_bf16 v[140:143], a[200:203], a[180:183], v[140:143]\n" \
"s_waitcnt vmcnt(26) lgkmcnt(5)\n" \
"s_nop 0\n" \
"s_bitcmp1_b32 0xc0, s48\n" \
"s_cbranch_scc1 Lcw14_%=\n" \
"s_waitcnt vmcnt(16)\n" \
"Lcw14_%=:\n" \
"v_mfma_f32_16x16x32_bf16 v[128:131], a[204:207], a[136:139], v[128:131]\n" \
"ds_read_b64_tr_b16 a[210:211], v227 offset:10240\n" \
"buffer_load_dwordx4 a[104:107], v209, %[srdz], s50 offen offset:2048 sc1\n" \
"v_mfma_f32_16x16x32_bf16 v[128:131], a[224:227], a[140:143], v[128:131]\n" \
"ds_read_b64_tr_b16 a[212:213], v227 offset:12288\n" \
"v_mfma_f32_16x16x32_bf16 v[132:135], a[204:207], a[152:155], v[132:135]\n" \
"ds_read_b64_tr_b16 a[214:215], v227 offset:14336\n" \
"v_mfma_f32_16x16x32_bf16 v[132:135], a[224:227], a[156:159], v[132:135]\n" \
"buffer_load_dwordx4 v214, %[srdt], s49 offen offset:3072 lds\n" \
"v_mfma_f32_16x16x32_bf16 v[136:139], a[204:207], a[168:171], v[136:139]\n" \
"v_mfma_f32_16x16x32_bf16 v[136:139], a[224:227], a[172:175], v[136:139]\n" \
"v_mfma_f32_16x16x32_bf16 v[140:143], a[204:207], a[184:187], v[140:143]\n" \
"ds_read_b64_tr_b16 a[228:229], v215 offset:512\n" \
"s_cbranch_vccz Lg36_%=\n" \
"s_nop 0\n" \
"buffer_store_dwordx4 v[120:123], v210, %[srdz], s60 offen offset:3072 sc1\n" \
"Lg36_%=:\n" \
"v_mfma_f32_16x16x32_bf16 v[140:143], a[224:227], a[188:191], v[140:143]\n" \
"ds_read_b64_tr_b16 a[230:231], v215 offset:2560\n" \
"v_mfma_f32_16x16x32_bf16 v[28:31], a[216:219], a[8:11], v[28:31]\n" \
"buffer_load_dwordx4 a[108:111], v209, %[srdz], s50 offen offset:3072 sc1\n" \
"v_mfma_f32_16x16x32_bf16 v[28:31], a[220:223], a[12:15], v[28:31]\n" \
"v_mfma_f32_16x16x32_bf16 v[60:63], a[216:219], a[24:27], v[60:63]\n" \
"v_mfma_f32_16x16x32_bf16 v[60:63], a[220:223], a[28:31], v[60:63]\n" \
"v_mfma_f32_16x16x32_bf16 v[92:95], a[216:219], a[40:43], v[92:95]\n" \
"buffer_load_dwordx4 a[112:115], v210, %[srdz], s50 offen offset:0 sc1\n" \
"v_mfma_f32_16x16x32_bf16 v[92:95], a[220:223], a[44:47], v[92:95]\n" \
"v_cmp_u_f32_e64 s[44:45], v128, v132\n" \
"v_mfma_f32_16x16x32_bf16 v[124:127], a[216:219], a[56:59], v[124:127]\n" \
"v_mfma_f32_16x16x32_bf16 v[124:127], a[220:223], a[60:63], v[124:127]\n" \
"v_cmp_u_f32_e64 s[56:57], v136, v140\n" \
"s_or_b64 s[56:57], s[56:57], s[44:45]\n" \
"s_cbranch_scc1 Lsp3_%=\n" \
"Lbk3_%=:\n" \
"s_waitcnt vmcnt(63) lgkmcnt(2)\n" \
"s_nop 0\n" \
"v_mfma_f32_16x16x32_bf16 v[144:147], a[232:235], a[128:131], v[144:147]\n" \
"ds_read_b64_tr_b16 a[216:217], v215 offset:4608\n" \
"v_mfma_f32_16x16x32_bf16 v[144:147], a[236:239], a[132:135], v[144:147]\n" \
"ds_read_b64_tr_b16 a[218:219], v215 offset:6656\n" \
"v_mfma_f32_16x16x32_bf16 v[148:151], a[232:235], a[144:147], v[148:151]\n" \
"ds_read_b64_tr_b16 a[196:197], v215 offset:8704\n" \
"v_mfma_f32_16x16x32_bf16 v[148:151], a[236:239], a[148:151], v[148:151]\n" \
"ds_read_b64_tr_b16 a[198:199], v215 offset:10752\n" \
"v_mfma_f32_16x16x32_bf16 v[152:155], a[232:235], a[160:163], v[152:155]\n" \
"ds_read_b64_tr_b16 a[200:201], v215 offset:12800\n" \
"v_mfma_f32_16x16x32_bf16 v[152:155], a[236:239], a[164:167], v[152:155]\n" \
"ds_read_b64_tr_b16 a[202:203], v215 offset:14848\n" \
"v_mfma_f32_16x16x32_bf16 v[156:159], a[232:235], a[176:179], v[156:159]\n" \
"ds_read_b64_tr_b16 a[204:205], v227 offset:512\n" \
"buffer_load_dwordx4 a[116:119], v210, %[srdz], s50 offen offset:1024 sc1\n" \
"v_mfma_f32_16x16x32_bf16 v[156:159], a[236:239], a[180:183], v[156:159]\n" \
"ds_read_b64_tr_b16 a[206:207], v227 offset:2560\n" \
"v_mfma_f32_16x16x32_bf16 v[144:147], a[208:211], a[136:139], v[144:147]\n" \
"ds_read_b64_tr_b16 a[220:221], v227 offset:4608\n" \
"v_mfma_f32_16x16x32_bf16 v[144:147], a[212:215], a[140:143], v[144:147]\n" \
"ds_read_b64_tr_b16 a[222:223], v227 offset:6656\n" \
"v_mfma_f32_16x16x32_bf16 v[148:151], a[208:211], a[152:155], v[148:151]\n" \
"s_add_u32 m0, %[m0b], 98304\n" \
"s_cmp_gt_u32 s48, 4\n" \
"s_cselect_b32 s49, 4, 5\n" \
"s_lshl_b32 s49, s49, 18\n" \
"s_or_b32 s49, s49, s58\n" \
"buffer_load_dwordx4 %[wo0], %[srdt], s49 offen offset:0 lds\n" \
"v_mfma_f32_16x16x32_bf16 v[148:151], a[212:215], a[156:159], v[148:151]\n" \
"v_mfma_f32_16x16x32_bf16 v[152:155], a[208:211], a[168:171], v[152:155]\n" \
"buffer_load_dwordx4 a[120:123], v210, %[srdz], s50 offen offset:2048 sc1\n" \
"v_mfma_f32_16x16x32_bf16 v[152:155], a[212:215], a[172:175], v[152:155]\n" \
"v_mfma_f32_16x16x32_bf16 v[156:159], a[208:211], a[184:187], v[156:159]\n" \
"ds_read_b64_tr_b16 a[224:225], v227 offset:8704\n" \
"v_mfma_f32_16x16x32_bf16 v[156:159], a[212:215], a[188:191], v[156:159]\n" \
"s_waitcnt vmcnt(63) lgkmcnt(5)\n" \
"s_nop 0\n" \
"v_mfma_f32_16x16x32_bf16 v[160:163], a[228:231], a[128:131], v[160:163]\n" \
"ds_read_b64_tr_b16 a[226:227], v227 offset:10752\n" \
"v_mfma_f32_16x16x32_bf16 v[160:163], a[216:219], a[132:135], v[160:163]\n" \
"ds_read_b64_tr_b16 a[232:233], v227 offset:12800\n" \
"v_mfma_f32_16x16x32_bf16 v[164:167], a[228:231], a[144:147], v[164:167]\n" \
"ds_read_b64_tr_b16 a[234:235], v227 offset:14848\n" \
"v_mfma_f32_16x16x32_bf16 v[164:167], a[216:219], a[148:151], v[164:167]\n" \
"buffer_load_dwordx4 a[124:127], v210, %[srdz], s50 offen offset:3072 sc1\n" \
"v_mfma_f32_16x16x32_bf16 v[168:171], a[228:231], a[160:163], v[168:171]\n" \
"v_mfma_f32_16x16x32_bf16 v[168:171], a[216:219], a[164:167], v[168:171]\n" \
"v_mfma_f32_16x16x32_bf16 v[172:175], a[228:231], a[176:179], v[172:175]\n" \
"v_mfma_f32_16x16x32_bf16 v[172:175], a[216:219], a[180:183], v[172:175]\n" \
"v_mfma_f32_16x16x32_bf16 v[160:163], a[196:199], a[136:139], v[160:163]\n" \
"v_mfma_f32_16x16x32_bf16 v[160:163], a[200:203], a[140:143], v[160:163]\n" \
"buffer_load_dwordx4 v212, %[srdt], s49 offen offset:1024 lds\n" \
"v_mfma_f32_16x16x32_bf16 v[164:167], a[196:199], a[152:155], v[164:167]\n" \
"v_mfma_f32_16x16x32_bf16 v[164:167], a[200:203], a[156:159], v[164:167]\n" \
"v_mfma_f32_16x16x32_bf16 v[168:171], a[196:199], a[168:171], v[168:171]\n" \
"s_waitcnt vmcnt(30) lgkmcnt(0)\n" \
"s_nop 0\n" \
"s_bitcmp1_b32 0xc0, s48\n" \
"s_cbranch_scc1 Lcw15_%=\n" \
"s_waitcnt vmcnt(21)\n" \
"Lcw15_%=:\n" \
"s_barrier\n" \
"s_nop 0\n" \
"v_mfma_f32_16x16x32_bf16 v[168:171], a[200:203], a[172:175], v[168:171]\n" \
"ds_read_b64_tr_b16 a[236:237], v215 offset:16384\n" \
"v_mfma_f32_16x16x32_bf16 v[172:175], a[196:199], a[184:187], v[172:175]\n" \
"ds_read_b64_tr_b16 a[238:239], v215 offset:18432\n" \
"v_mfma_f32_16x16x32_bf16 v[172:175], a[200:203], a[188:191], v[172:175]\n" \
"s_waitcnt vmcnt(63) lgkmcnt(2)\n" \
"s_nop 0\n" \
"v_mfma_f32_16x16x32_bf16 v[176:179], a[204:207], a[128:131], v[176:179]\n" \
"ds_read_b64_tr_b16 a[208:209], v215 offset:20480\n" \
"s_cmp_gt_u32 s48, 4\n" \
"s_cselect_b32 s50, 4, 5\n" \
"s_mul_i32 s50, s50, 152064\n" \
"buffer_load_dwordx4 v[0:3], %[oq0], %[srdz], s50 offen offset:0 sc1\n" \
"v_mfma_f32_16x16x32_bf16 v[176:179], a[220:223], a[132:135], v[176:179]\n" \
"ds_read_b64_tr_b16 a[210:211], v215 offset:22528\n" \
"v_mfma_f32_16x16x32_bf16 v[180:183], a[204:207], a[144:147], v[180:183]\n" \
"ds_read_b64_tr_b16 a[212:213], v215 offset:24576\n" \
"v_mfma_f32_16x16x32_bf16 v[180:183], a[220:223], a[148:151], v[180:183]\n" \
"ds_read_b64_tr_b16 a[214:215], v215 offset:26624\n" \
"v_mfma_f32_16x16x32_bf16 v[184:187], a[204:207], a[160:163], v[184:187]\n" \
"ds_read_b64_tr_b16 a[228:229], v215 offset:28672\n" \
"buffer_load_dwordx4 v[8:11], %[oq0], %[srdz], s50 offen offset:1024 sc1\n" \
"v_mfma_f32_16x16x32_bf16 v[184:187], a[220:223], a[164:167], v[184:187]\n" \
"ds_read_b64_tr_b16 a[230:231], v215 offset:30720\n" \
"v_mfma_f32_16x16x32_bf16 v[188:191], a[204:207], a[176:179], v[188:191]\n" \
"ds_read_b64_tr_b16 a[216:217], v227 offset:16384\n" \
"v_mfma_f32_16x16x32_bf16 v[188:191], a[220:223], a[180:183], v[188:191]\n" \
"ds_read_b64_tr_b16 a[218:219], v227 offset:18432\n" \
"v_mfma_f32_16x16x32_bf16 v[176:179], a[224:227], a[136:139], v[176:179]\n" \
"ds_read_b64_tr_b16 a[196:197], v227 offset:20480\n" \
"buffer_load_dwordx4 v[16:19], %[oq0], %[srdz], s50 offen offset:2048 sc1\n" \
"v_mfma_f32_16x16x32_bf16 v[176:179], a[232:235], a[140:143], v[176:179]\n" \
"ds_read_b64_tr_b16 a[198:199], v227 offset:22528\n" \
"v_mfma_f32_16x16x32_bf16 v[180:183], a[224:227], a[152:155], v[180:183]\n" \
"buffer_load_dwordx4 v213, %[srdt], s49 offen offset:2048 lds\n" \
"v_mfma_f32_16x16x32_bf16 v[180:183], a[232:235], a[156:159], v[180:183]\n" \
"v_mfma_f32_16x16x32_bf16 v[184:187], a[224:227], a[168:171], v[184:187]\n" \
"buffer_load_dwordx4 v[24:27], %[oq0], %[srdz], s50 offen offset:3072 sc1\n" \
"v_mfma_f32_16x16x32_bf16 v[184:187], a[232:235], a[172:175], v[184:187]\n" \
"v_mfma_f32_16x16x32_bf16 v[188:191], a[224:227], a[184:187], v[188:191]\n" \
"ds_read_b64_tr_b16 a[200:201], v227 offset:24576\n" \
"v_mfma_f32_16x16x32_bf16 v[188:191], a[232:235], a[188:191], v[188:191]\n" \
"s_waitcnt vmcnt(63) lgkmcnt(5)\n" \
"s_nop 0\n" \
"v_mfma_f32_16x16x32_bf16 v[4:7], a[236:239], a[128:131], v[4:7]\n" \
"ds_read_b64_tr_b16 a[202:203], v227 offset:26624\n" \
"buffer_load_dwordx4 v[32:35], v208, %[srdz], s50 offen offset:0 sc1\n" \
"v_mfma_f32_16x16x32_bf16 v[4:7], a[208:211], a[132:135], v[4:7]\n" \
"ds_read_b64_tr_b16 a[204:205], v227 offset:28672\n" \
"v_mfma_f32_16x16x32_bf16 v[36:39], a[236:239], a[144:147], v[36:39]\n" \
"ds_read_b64_tr_b16 a[206:207], v227 offset:30720\n" \
"v_mfma_f32_16x16x32_bf16 v[36:39], a[208:211], a[148:151], v[36:39]\n" \
"buffer_load_dwordx4 v[40:43], v208, %[srdz], s50 offen offset:1024 sc1\n" \
"v_mfma_f32_16x16x32_bf16 v[68:71], a[236:239], a[160:163], v[68:71]\n" \
"v_mfma_f32_16x16x32_bf16 v[68:71], a[208:211], a[164:167], v[68:71]\n" \
"buffer_load_dwordx4 v[48:51], v208, %[srdz], s50 offen offset:2048 sc1\n" \
"v_mfma_f32_16x16x32_bf16 v[100:103], a[236:239], a[176:179], v[100:103]\n" \
"ds_read_b64_tr_b16 a[220:221], v215 offset:16896\n" \
"v_mfma_f32_16x16x32_bf16 v[100:103], a[208:211], a[180:183], v[100:103]\n" \
"ds_read_b64_tr_b16 a[222:223], v215 offset:18944\n" \
"v_mfma_f32_16x16x32_bf16 v[4:7], a[212:215], a[136:139], v[4:7]\n" \
"ds_read_b64_tr_b16 a[224:225], v215 offset:20992\n" \
"v_mfma_f32_16x16x32_bf16 v[4:7], a[228:231], a[140:143], v[4:7]\n" \
"ds_read_b64_tr_b16 a[226:227], v215 offset:23040\n" \
"v_mfma_f32_16x16x32_bf16 v[36:39], a[212:215], a[152:155], v[36:39]\n" \
"buffer_load_dwordx4 v214, %[srdt], s49 offen offset:3072 lds\n" \
"v_mfma_f32_16x16x32_bf16 v[36:39], a[228:231], a[156:159], v[36:39]\n" \
"v_mfma_f32_16x16x32_bf16 v[68:71], a[212:215], a[168:171], v[68:71]\n" \
"buffer_load_dwordx4 v[56:59], v208, %[srdz], s50 offen offset:3072 sc1\n" \
"v_mfma_f32_16x16x32_bf16 v[68:71], a[228:231], a[172:175], v[68:71]\n" \
"v_mfma_f32_16x16x32_bf16 v[100:103], a[212:215], a[184:187], v[100:103]\n" \
"ds_read_b64_tr_b16 a[232:233], v215 offset:25088\n" \
"v_mfma_f32_16x16x32_bf16 v[100:103], a[228:231], a[188:191], v[100:103]\n" \
"s_waitcnt vmcnt(63) lgkmcnt(5)\n" \
"s_nop 0\n" \
"v_mfma_f32_16x16x32_bf16 v[12:15], a[216:219], a[128:131], v[12:15]\n" \
"ds_read_b64_tr_b16 a[234:235], v215 offset:27136\n" \
"buffer_load_dwordx4 v[64:67], v209, %[srdz], s50 offen offset:0 sc1\n" \
"v_mfma_f32_16x16x32_bf16 v[12:15], a[196:199], a[132:135], v[12:15]\n" \
"ds_read_b64_tr_b16 a[236:237], v215 offset:29184\n" \
"v_mfma_f32_16x16x32_bf16 v[44:47], a[216:219], a[144:147], v[44:47]\n" \
"ds_read_b64_tr_b16 a[238:239], v215 offset:31232\n" \
"v_mfma_f32_16x16x32_bf16 v[44:47], a[196:199], a[148:151], v[44:47]\n" \
"buffer_load_dwordx4 v[72:75], v209, %[srdz], s50 offen offset:1024 sc1\n" \
"v_mfma_f32_16x16x32_bf16 v[76:79], a[216:219], a[160:163], v[76:79]\n" \
"v_mfma_f32_16x16x32_bf16 v[76:79], a[196:199], a[164:167], v[76:79]\n" \
"buffer_load_dwordx4 v[80:83], v209, %[srdz], s50 offen offset:2048 sc1\n" \
"v_mfma_f32_16x16x32_bf16 v[108:111], a[216:219], a[176:179], v[108:111]\n" \
"ds_read_b64_tr_b16 a[208:209], v227 offset:16896\n" \
"v_mfma_f32_16x16x32_bf16 v[108:111], a[196:199], a[180:183], v[108:111]\n" \
"ds_read_b64_tr_b16 a[210:211], v227 offset:18944\n" \
"v_mfma_f32_16x16x32_bf16 v[12:15], a[200:203], a[136:139], v[12:15]\n" \
"ds_read_b64_tr_b16 a[212:213], v227 offset:25088\n" \
"v_mfma_f32_16x16x32_bf16 v[12:15], a[204:207], a[140:143], v[12:15]\n" \
"ds_read_b64_tr_b16 a[214:215], v227 offset:27136\n" \
"v_mfma_f32_16x16x32_bf16 v[44:47], a[200:203], a[152:155], v[44:47]\n" \
"ds_read_b64_tr_b16 a[228:229], v227 offset:20992\n" \
"v_mfma_f32_16x16x32_bf16 v[44:47], a[204:207], a[156:159], v[44:47]\n" \
"ds_read_b64_tr_b16 a[230:231], v227 offset:23040\n" \
"buffer_load_dwordx4 v[88:91], v209, %[srdz], s50 offen offset:3072 sc1\n" \
"v_mfma_f32_16x16x32_bf16 v[76:79], a[200:203], a[168:171], v[76:79]\n" \
"ds_read_b64_tr_b16 a[216:217], v227 offset:29184\n" \
"v_mfma_f32_16x16x32_bf16 v[76:79], a[204:207], a[172:175], v[76:79]\n" \
"ds_read_b64_tr_b16 a[218:219], v227 offset:31232\n" \
"v_mfma_f32_16x16x32_bf16 v[108:111], a[200:203], a[184:187], v[108:111]\n" \
"buffer_load_dwordx4 v[96:99], v210, %[srdz], s50 offen offset:0 sc1\n" \
"v_mfma_f32_16x16x32_bf16 v[108:111], a[204:207], a[188:191], v[108:111]\n" \
"s_waitcnt vmcnt(63) lgkmcnt(12)\n" \
"s_nop 0\n" \
"v_mfma_f32_16x16x32_bf16 v[20:23], a[220:223], a[128:131], v[20:23]\n" \
"s_add_u32 m0, %[m0b], 114688\n" \
"s_cmp_gt_u32 s48, 5\n" \
"s_cselect_b32 s49, 5, 6\n" \
"s_lshl_b32 s49, s49, 18\n" \
"s_or_b32 s49, s49, s58\n" \
"buffer_load_dwordx4 %[wo0], %[srdt], s49 offen offset:0 lds\n" \
"v_mfma_f32_16x16x32_bf16 v[20:23], a[224:227], a[132:135], v[20:23]\n" \
"v_mfma_f32_16x16x32_bf16 v[52:55], a[220:223], a[144:147], v[52:55]\n" \
"buffer_load_dwordx4 v[104:107], v210, %[srdz], s50 offen offset:1024 sc1\n" \
"v_mfma_f32_16x16x32_bf16 v[52:55], a[224:227], a[148:151], v[52:55]\n" \
"v_mfma_f32_16x16x32_bf16 v[84:87], a[220:223], a[160:163], v[84:87]\n" \
"s_waitcnt vmcnt(24) lgkmcnt(0)\n" \
"s_barrier\n" \
"v_mfma_f32_16x16x32_bf16 v[84:87], a[224:227], a[164:167], v[84:87]\n" \
"ds_read_b64_tr_b16 a[196:197], v215 offset:32768\n" \
"v_mfma_f32_16x16x32_bf16 v[116:119], a[220:223], a[176:179], v[116:119]\n" \
"ds_read_b64_tr_b16 a[198:199], v215 offset:34816\n" \
"v_mfma_f32_16x16x32_bf16 v[116:119], a[224:227], a[180:183], v[116:119]\n" \
"ds_read_b64_tr_b16 a[200:201], v215 offset:36864\n" \
"buffer_load_dwordx4 v[112:115], v210, %[srdz], s50 offen offset:2048 sc1\n" \
"v_mfma_f32_16x16x32_bf16 v[20:23], a[232:235], a[136:139], v[20:23]\n" \
"ds_read_b64_tr_b16 a[202:203], v215 offset:38912\n" \
"v_mfma_f32_16x16x32_bf16 v[20:23], a[236:239], a[140:143], v[20:23]\n" \
"ds_read_b64_tr_b16 a[204:205], v215 offset:40960\n" \
"v_mfma_f32_16x16x32_bf16 v[52:55], a[232:235], a[152:155], v[52:55]\n" \
"ds_read_b64_tr_b16 a[206:207], v215 offset:43008\n" \
"v_mfma_f32_16x16x32_bf16 v[52:55], a[236:239], a[156:159], v[52:55]\n" \
"ds_read_b64_tr_b16 a[220:221], v215 offset:45056\n" \
"buffer_load_dwordx4 v[120:123], v210, %[srdz], s50 offen offset:3072 sc1\n" \
"v_mfma_f32_16x16x32_bf16 v[84:87], a[232:235], a[168:171], v[84:87]\n" \
"ds_read_b64_tr_b16 a[222:223], v215 offset:47104\n" \
"v_readlane_b32 s54, %[thi], 16\n" \
"v_mfma_f32_16x16x32_bf16 v[84:87], a[236:239], a[172:175], v[84:87]\n" \
"v_mfma_f32_16x16x32_bf16 v[116:119], a[232:235], a[184:187], v[116:119]\n" \
"v_mfma_f32_16x16x32_bf16 v[116:119], a[236:239], a[188:191], v[116:119]\n" \
"s_waitcnt vmcnt(23) lgkmcnt(4)\n" \
"s_nop 0\n" \
"v_mfma_f32_16x16x32_bf16 v[28:31], a[208:211], a[128:131], v[28:31]\n" \
"buffer_load_dwordx4 v212, %[srdt], s49 offen offset:1024 lds\n" \
"v_mfma_f32_16x16x32_bf16 v[28:31], a[228:231], a[132:135], v[28:31]\n" \
"v_mfma_f32_16x16x32_bf16 v[60:63], a[208:211], a[144:147], v[60:63]\n" \
"s_cmp_gt_u32 s48, 5\n" \
"s_cselect_b32 s50, 5, 6\n" \
"s_mul_i32 s50, s50, 152064\n" \
"buffer_load_dwordx4 a[0:3], %[oq0], %[srdz], s50 offen offset:0 sc1\n" \
"v_mfma_f32_16x16x32_bf16 v[60:63], a[228:231], a[148:151], v[60:63]\n" \
"v_mfma_f32_16x16x32_bf16 v[92:95], a[208:211], a[160:163], v[92:95]\n" \
"buffer_load_dword v232, v226, %[srdz], s54 offen sc1\n" \
"v_mfma_f32_16x16x32_bf16 v[92:95], a[228:231], a[164:167], v[92:95]\n" \
"v_mfma_f32_16x16x32_bf16 v[124:127], a[208:211], a[176:179], v[124:127]\n" \
"ds_read_b64_tr_b16 a[224:225], v227 offset:32768\n" \
"buffer_load_dwordx4 a[4:7], %[oq0], %[srdz], s50 offen offset:1024 sc1\n" \
"v_mfma_f32_16x16x32_bf16 v[124:127], a[228:231], a[180:183], v[124:127]\n" \
"ds_read_b64_tr_b16 a[226:227], v227 offset:34816\n" \
"v_mfma_f32_16x16x32_bf16 v[192:195], a[196:199], a[64:67], v[128:131]\n" \
"ds_read_b64_tr_b16 a[232:233], v227 offset:36864\n" \
"v_mfma_f32_16x16x32_bf16 v[192:195], a[200:203], a[68:71], v[192:195]\n" \
"ds_read_b64_tr_b16 a[234:235], v227 offset:38912\n" \
"v_mfma_f32_16x16x32_bf16 v[196:199], a[196:199], a[80:83], v[132:135]\n" \
"buffer_load_dwordx4 a[8:11], %[oq0], %[srdz], s50 offen offset:2048 sc1\n" \
"v_mfma_f32_16x16x32_bf16 v[196:199], a[200:203], a[84:87], v[196:199]\n" \
"v_mfma_f32_16x16x32_bf16 v[200:203], a[196:199], a[96:99], v[136:139]\n" \
"buffer_load_dwordx4 a[12:15], %[oq0], %[srdz], s50 offen offset:3072 sc1\n" \
"v_mfma_f32_16x16x32_bf16 v[200:203], a[200:203], a[100:103], v[200:203]\n" \
"v_mfma_f32_16x16x32_bf16 v[204:207], a[196:199], a[112:115], v[140:143]\n" \
"ds_read_b64_tr_b16 a[236:237], v227 offset:40960\n" \
"v_mfma_f32_16x16x32_bf16 v[204:207], a[200:203], a[116:119], v[204:207]\n" \
"s_waitcnt vmcnt(26) lgkmcnt(5)\n" \
"s_nop 0\n" \
"v_mfma_f32_16x16x32_bf16 v[192:195], a[204:207], a[72:75], v[192:195]\n" \
"ds_read_b64_tr_b16 a[238:239], v227 offset:43008\n" \
"v_mfma_f32_16x16x32_bf16 v[192:195], a[220:223], a[76:79], v[192:195]\n" \
"ds_read_b64_tr_b16 a[208:209], v227 offset:45056\n" \
"buffer_load_dwordx4 v213, %[srdt], s49 offen offset:2048 lds\n" \
"v_mfma_f32_16x16x32_bf16 v[196:199], a[204:207], a[88:91], v[196:199]\n" \
"ds_read_b64_tr_b16 a[210:211], v227 offset:47104\n" \
"v_mfma_f32_16x16x32_bf16 v[196:199], a[220:223], a[92:95], v[196:199]\n" \
"buffer_load_dwordx4 a[16:19], v208, %[srdz], s50 offen offset:0 sc1\n" \
"v_mfma_f32_16x16x32_bf16 v[200:203], a[204:207], a[104:107], v[200:203]\n" \
"v_mfma_f32_16x16x32_bf16 v[200:203], a[220:223], a[108:111], v[200:203]\n" \
"buffer_load_dwordx4 a[20:23], v208, %[srdz], s50 offen offset:1024 sc1\n" \
"v_mfma_f32_16x16x32_bf16 v[204:207], a[204:207], a[120:123], v[204:207]\n" \
"ds_read_b64_tr_b16 a[228:229], v215 offset:33280\n" \
"v_mfma_f32_16x16x32_bf16 v[204:207], a[220:223], a[124:127], v[204:207]\n" \
"ds_read_b64_tr_b16 a[230:231], v215 offset:35328\n" \
"v_mfma_f32_16x16x32_bf16 v[28:31], a[212:215], a[136:139], v[28:31]\n" \
"buffer_load_dwordx4 a[24:27], v208, %[srdz], s50 offen offset:2048 sc1\n" \
"v_mfma_f32_16x16x32_bf16 v[28:31], a[216:219], a[140:143], v[28:31]\n" \
"v_mfma_f32_16x16x32_bf16 v[60:63], a[212:215], a[152:155], v[60:63]\n" \
"buffer_load_dwordx4 a[28:31], v208, %[srdz], s50 offen offset:3072 sc1\n" \
"v_mfma_f32_16x16x32_bf16 v[60:63], a[216:219], a[156:159], v[60:63]\n" \
"v_mfma_f32_16x16x32_bf16 v[92:95], a[212:215], a[168:171], v[92:95]\n" \
"v_mfma_f32_16x16x32_bf16 v[92:95], a[216:219], a[172:175], v[92:95]\n" \
"v_cmp_u_f32_e64 s[44:45], v192, v196\n" \
"buffer_load_dwordx4 v214, %[srdt], s49 offen offset:3072 lds\n" \
"v_mfma_f32_16x16x32_bf16 v[124:127], a[212:215], a[184:187], v[124:127]\n" \
"v_mfma_f32_16x16x32_bf16 v[124:127], a[216:219], a[188:191], v[124:127]\n" \
"v_cmp_u_f32_e64 s[56:57], v200, v204\n" \
"s_or_b64 s[56:57], s[56:57], s[44:45]\n" \
"s_cbranch_scc1 Lsp4_%=\n" \
"Lbk4_%=:\n" \
"s_waitcnt vmcnt(63) lgkmcnt(2)\n" \
"s_nop 0\n" \
"v_mfma_f32_16x16x32_bf16 v[144:147], a[224:227], a[64:67], v[144:147]\n" \
"ds_read_b64_tr_b16 a[212:213], v215 offset:37376\n" \
"v_mfma_f32_16x16x32_bf16 v[144:147], a[232:235], a[68:71], v[144:147]\n" \
"ds_read_b64_tr_b16 a[214:215], v215 offset:39424\n" \
"buffer_load_dwordx4 a[32:35], v209, %[srdz], s50 offen offset:0 sc1\n" \
"v_mfma_f32_16x16x32_bf16 v[148:151], a[224:227], a[80:83], v[148:151]\n" \
"ds_read_b64_tr_b16 a[196:197], v215 offset:41472\n" \
"v_mfma_f32_16x16x32_bf16 v[148:151], a[232:235], a[84:87], v[148:151]\n" \
"ds_read_b64_tr_b16 a[198:199], v215 offset:43520\n" \
"v_mfma_f32_16x16x32_bf16 v[152:155], a[224:227], a[96:99], v[152:155]\n" \
"ds_read_b64_tr_b16 a[200:201], v215 offset:45568\n" \
"v_mfma_f32_16x16x32_bf16 v[152:155], a[232:235], a[100:103], v[152:155]\n" \
"ds_read_b64_tr_b16 a[202:203], v215 offset:47616\n" \
"buffer_load_dwordx4 a[36:39], v209, %[srdz], s50 offen offset:1024 sc1\n" \
"v_mfma_f32_16x16x32_bf16 v[156:159], a[224:227], a[112:115], v[156:159]\n" \
"ds_read_b64_tr_b16 a[204:205], v227 offset:33280\n" \
"v_mfma_f32_16x16x32_bf16 v[156:159], a[232:235], a[116:119], v[156:159]\n" \
"ds_read_b64_tr_b16 a[206:207], v227 offset:35328\n" \
"v_mfma_f32_16x16x32_bf16 v[144:147], a[236:239], a[72:75], v[144:147]\n" \
"ds_read_b64_tr_b16 a[216:217], v227 offset:41472\n" \
"v_mfma_f32_16x16x32_bf16 v[144:147], a[208:211], a[76:79], v[144:147]\n" \
"ds_read_b64_tr_b16 a[218:219], v227 offset:43520\n" \
"buffer_load_dwordx4 a[40:43], v209, %[srdz], s50 offen offset:2048 sc1\n" \
"v_mfma_f32_16x16x32_bf16 v[148:151], a[236:239], a[88:91], v[148:151]\n" \
"ds_read_b64_tr_b16 a[220:221], v227 offset:37376\n" \
"v_mfma_f32_16x16x32_bf16 v[148:151], a[208:211], a[92:95], v[148:151]\n" \
"ds_read_b64_tr_b16 a[222:223], v227 offset:39424\n" \
"v_mfma_f32_16x16x32_bf16 v[152:155], a[236:239], a[104:107], v[152:155]\n" \
"ds_read_b64_tr_b16 a[224:225], v227 offset:45568\n" \
"v_mfma_f32_16x16x32_bf16 v[152:155], a[208:211], a[108:111], v[152:155]\n" \
"ds_read_b64_tr_b16 a[226:227], v227 offset:47616\n" \
"buffer_load_dwordx4 a[44:47], v209, %[srdz], s50 offen offset:3072 sc1\n" \
"v_mfma_f32_16x16x32_bf16 v[156:159], a[236:239], a[120:123], v[156:159]\n" \
"v_mfma_f32_16x16x32_bf16 v[156:159], a[208:211], a[124:127], v[156:159]\n" \
"s_waitcnt vmcnt(63) lgkmcnt(12)\n" \
"s_nop 0\n" \
"v_mfma_f32_16x16x32_bf16 v[160:163], a[228:231], a[64:67], v[160:163]\n" \
"buffer_load_dwordx4 a[48:51], v210, %[srdz], s50 offen offset:0 sc1\n" \
"v_mfma_f32_16x16x32_bf16 v[160:163], a[212:215], a[68:71], v[160:163]\n" \
"v_mfma_f32_16x16x32_bf16 v[164:167], a[228:231], a[80:83], v[164:167]\n" \
"buffer_load_dwordx4 a[52:55], v210, %[srdz], s50 offen offset:1024 sc1\n" \
"v_mfma_f32_16x16x32_bf16 v[164:167], a[212:215], a[84:87], v[164:167]\n" \
"v_mfma_f32_16x16x32_bf16 v[168:171], a[228:231], a[96:99], v[168:171]\n" \
"s_waitcnt vmcnt(28) lgkmcnt(0)\n" \
"s_barrier\n" \
"v_mfma_f32_16x16x32_bf16 v[168:171], a[212:215], a[100:103], v[168:171]\n" \
"ds_read_b64_tr_b16 a[232:233], v220 offset:32768\n" \
"v_mfma_f32_16x16x32_bf16 v[172:175], a[228:231], a[112:115], v[172:175]\n" \
"ds_read_b64_tr_b16 a[234:235], v220 offset:34816\n" \
"v_mfma_f32_16x16x32_bf16 v[172:175], a[212:215], a[116:119], v[172:175]\n" \
"ds_read_b64_tr_b16 a[236:237], v220 offset:36864\n" \
"v_mfma_f32_16x16x32_bf16 v[160:163], a[196:199], a[72:75], v[160:163]\n" \
"ds_read_b64_tr_b16 a[238:239], v220 offset:38912\n" \
"v_mfma_f32_16x16x32_bf16 v[160:163], a[200:203], a[76:79], v[160:163]\n" \
"ds_read_b64_tr_b16 a[208:209], v220 offset:40960\n" \
"s_add_u32 m0, %[m0b], 49152\n" \
"s_cmp_gt_u32 s48, 6\n" \
"s_cselect_b32 s49, 6, 7\n" \
"s_lshl_b32 s49, s49, 18\n" \
"s_or_b32 s49, s49, s58\n" \
"buffer_load_dwordx4 %[wo0], %[srdt], s49 offen offset:0 lds\n" \
"v_mfma_f32_16x16x32_bf16 v[164:167], a[196:199], a[88:91], v[164:167]\n" \
"ds_read_b64_tr_b16 a[210:211], v220 offset:43008\n" \
"v_mfma_f32_16x16x32_bf16 v[164:167], a[200:203], a[92:95], v[164:167]\n" \
"ds_read_b64_tr_b16 a[228:229], v220 offset:45056\n" \
"buffer_load_dwordx4 a[56:59], v210, %[srdz], s50 offen offset:2048 sc1\n" \
"v_mfma_f32_16x16x32_bf16 v[168:171], a[196:199], a[104:107], v[168:171]\n" \
"ds_read_b64_tr_b16 a[230:231], v220 offset:47104\n" \
"v_mfma_f32_16x16x32_bf16 v[168:171], a[200:203], a[108:111], v[168:171]\n" \
"buffer_load_dwordx4 a[60:63], v210, %[srdz], s50 offen offset:3072 sc1\n" \
"v_mfma_f32_16x16x32_bf16 v[172:175], a[196:199], a[120:123], v[172:175]\n" \
"v_mfma_f32_16x16x32_bf16 v[172:175], a[200:203], a[124:127], v[172:175]\n" \
"s_waitcnt vmcnt(23) lgkmcnt(4)\n" \
"s_nop 0\n" \
"v_mfma_f32_16x16x32_bf16 v[176:179], a[204:207], a[64:67], v[176:179]\n" \
"v_mfma_f32_16x16x32_bf16 v[176:179], a[220:223], a[68:71], v[176:179]\n" \
"s_cmp_gt_u32 s48, 6\n" \
"s_cselect_b32 s50, 6, 7\n" \
"s_mul_i32 s50, s50, 152064\n" \
"buffer_load_dwordx4 a[128:131], %[oq0], %[srdz], s50 offen offset:0 sc1\n" \
"v_mfma_f32_16x16x32_bf16 v[180:183], a[204:207], a[80:83], v[180:183]\n" \
"v_mfma_f32_16x16x32_bf16 v[180:183], a[220:223], a[84:87], v[180:183]\n" \
"v_mfma_f32_16x16x32_bf16 v[184:187], a[204:207], a[96:99], v[184:187]\n" \
"v_mfma_f32_16x16x32_bf16 v[184:187], a[220:223], a[100:103], v[184:187]\n" \
"buffer_load_dwordx4 v212, %[srdt], s49 offen offset:1024 lds\n" \
"v_mfma_f32_16x16x32_bf16 v[188:191], a[204:207], a[112:115], v[188:191]\n" \
"ds_read_b64_tr_b16 a[212:213], v221 offset:32768\n" \
"v_mfma_f32_16x16x32_bf16 v[188:191], a[220:223], a[116:119], v[188:191]\n" \
"ds_read_b64_tr_b16 a[214:215], v221 offset:34816\n" \
"v_mfma_f32_16x16x32_bf16 v[128:131], a[232:235], v[0:3], v[192:195]\n" \
"ds_read_b64_tr_b16 a[196:197], v221 offset:36864\n" \
"buffer_load_dwordx4 a[132:135], %[oq0], %[srdz], s50 offen offset:1024 sc1\n" \
"v_mfma_f32_16x16x32_bf16 v[128:131], a[236:239], v[8:11], v[128:131]\n" \
"ds_read_b64_tr_b16 a[198:199], v221 offset:38912\n" \
"v_mfma_f32_16x16x32_bf16 v[132:135], a[232:235], v[32:35], v[196:199]\n" \
"buffer_load_dwordx4 a[136:139], %[oq0], %[srdz], s50 offen offset:2048 sc1\n" \
"v_mfma_f32_16x16x32_bf16 v[132:135], a[236:239], v[40:43], v[132:135]\n" \
"v_mfma_f32_16x16x32_bf16 v[136:139], a[232:235], v[64:67], v[200:203]\n" \
"v_mfma_f32_16x16x32_bf16 v[136:139], a[236:239], v[72:75], v[136:139]\n" \
"buffer_load_dwordx4 a[140:143], %[oq0], %[srdz], s50 offen offset:3072 sc1\n" \
"v_mfma_f32_16x16x32_bf16 v[140:143], a[232:235], v[96:99], v[204:207]\n" \
"ds_read_b64_tr_b16 a[200:201], v221 offset:40960\n" \
"v_mfma_f32_16x16x32_bf16 v[140:143], a[236:239], v[104:107], v[140:143]\n" \
"s_waitcnt vmcnt(26) lgkmcnt(5)\n" \
"s_nop 0\n" \
"v_mfma_f32_16x16x32_bf16 v[128:131], a[208:211], v[16:19], v[128:131]\n" \
"ds_read_b64_tr_b16 a[202:203], v221 offset:43008\n" \
"v_mfma_f32_16x16x32_bf16 v[128:131], a[228:231], v[24:27], v[128:131]\n" \
"ds_read_b64_tr_b16 a[204:205], v221 offset:45056\n" \
"v_mfma_f32_16x16x32_bf16 v[132:135], a[208:211], v[48:51], v[132:135]\n" \
"ds_read_b64_tr_b16 a[206:207], v221 offset:47104\n" \
"v_mfma_f32_16x16x32_bf16 v[132:135], a[228:231], v[56:59], v[132:135]\n" \
"buffer_load_dwordx4 a[144:147], v208, %[srdz], s50 offen offset:0 sc1\n" \
"v_mfma_f32_16x16x32_bf16 v[136:139], a[208:211], v[80:83], v[136:139]\n" \
"v_mfma_f32_16x16x32_bf16 v[136:139], a[228:231], v[88:91], v[136:139]\n" \
"buffer_load_dwordx4 v213, %[srdt], s49 offen offset:2048 lds\n" \
"v_mfma_f32_16x16x32_bf16 v[140:143], a[208:211], v[112:115], v[140:143]\n" \
"ds_read_b64_tr_b16 a[220:221], v220 offset:33280\n" \
"v_mfma_f32_16x16x32_bf16 v[140:143], a[228:231], v[120:123], v[140:143]\n" \
"ds_read_b64_tr_b16 a[222:223], v220 offset:35328\n" \
"v_mfma_f32_16x16x32_bf16 v[176:179], a[216:219], a[72:75], v[176:179]\n" \
"buffer_load_dwordx4 a[148:151], v208, %[srdz], s50 offen offset:1024 sc1\n" \
"v_mfma_f32_16x16x32_bf16 v[176:179], a[224:227], a[76:79], v[176:179]\n" \
"v_mfma_f32_16x16x32_bf16 v[180:183], a[216:219], a[88:91], v[180:183]\n" \
"buffer_load_dwordx4 a[152:155], v208, %[srdz], s50 offen offset:2048 sc1\n" \
"v_mfma_f32_16x16x32_bf16 v[180:183], a[224:227], a[92:95], v[180:183]\n" \
"v_mfma_f32_16x16x32_bf16 v[184:187], a[216:219], a[104:107], v[184:187]\n" \
"v_mfma_f32_16x16x32_bf16 v[184:187], a[224:227], a[108:111], v[184:187]\n" \
"v_cmp_u_f32_e64 s[44:45], v128, v132\n" \
"buffer_load_dwordx4 a[156:159], v208, %[srdz], s50 offen offset:3072 sc1\n" \
"v_mfma_f32_16x16x32_bf16 v[188:191], a[216:219], a[120:123], v[188:191]\n" \
"v_mfma_f32_16x16x32_bf16 v[188:191], a[224:227], a[124:127], v[188:191]\n" \
"v_cmp_u_f32_e64 s[56:57], v136, v140\n" \
"s_or_b64 s[56:57], s[56:57], s[44:45]\n" \
"s_cbranch_scc1 Lsp5_%=\n" \
"Lbk5_%=:\n" \
"s_waitcnt vmcnt(63) lgkmcnt(2)\n" \
"s_nop 0\n" \
"v_mfma_f32_16x16x32_bf16 v[144:147], a[212:215], v[0:3], v[144:147]\n" \
"ds_read_b64_tr_b16 a[216:217], v220 offset:37376\n" \
"v_mfma_f32_16x16x32_bf16 v[144:147], a[196:199], v[8:11], v[144:147]\n" \
"ds_read_b64_tr_b16 a[218:219], v220 offset:39424\n" \
"v_mfma_f32_16x16x32_bf16 v[148:151], a[212:215], v[32:35], v[148:151]\n" \
"ds_read_b64_tr_b16 a[208:209], v220 offset:41472\n" \
"v_mfma_f32_16x16x32_bf16 v[148:151], a[196:199], v[40:43], v[148:151]\n" \
"ds_read_b64_tr_b16 a[210:211], v220 offset:43520\n" \
"v_mfma_f32_16x16x32_bf16 v[152:155], a[212:215], v[64:67], v[152:155]\n" \
"ds_read_b64_tr_b16 a[224:225], v220 offset:45568\n" \
"buffer_load_dwordx4 a[160:163], v209, %[srdz], s50 offen offset:0 sc1\n" \
"v_mfma_f32_16x16x32_bf16 v[152:155], a[196:199], v[72:75], v[152:155]\n" \
"ds_read_b64_tr_b16 a[226:227], v220 offset:47616\n" \
"v_mfma_f32_16x16x32_bf16 v[156:159], a[212:215], v[96:99], v[156:159]\n" \
"ds_read_b64_tr_b16 a[228:229], v221 offset:33280\n" \
"buffer_load_dwordx4 v214, %[srdt], s49 offen offset:3072 lds\n" \
"v_mfma_f32_16x16x32_bf16 v[156:159], a[196:199], v[104:107], v[156:159]\n" \
"ds_read_b64_tr_b16 a[230:231], v221 offset:35328\n" \
"v_mfma_f32_16x16x32_bf16 v[144:147], a[200:203], v[16:19], v[144:147]\n" \
"ds_read_b64_tr_b16 a[232:233], v221 offset:41472\n" \
"v_readlane_b32 s64, %[tlo], 10\n" \
"v_readlane_b32 s65, %[thi], 10\n" \
"buffer_load_dwordx4 a[164:167], v209, %[srdz], s50 offen offset:1024 sc1\n" \
"v_mfma_f32_16x16x32_bf16 v[144:147], a[204:207], v[24:27], v[144:147]\n" \
"ds_read_b64_tr_b16 a[234:235], v221 offset:43520\n" \
"v_mfma_f32_16x16x32_bf16 v[148:151], a[200:203], v[48:51], v[148:151]\n" \
"ds_read_b64_tr_b16 a[236:237], v221 offset:37376\n" \
"v_mfma_f32_16x16x32_bf16 v[148:151], a[204:207], v[56:59], v[148:151]\n" \
"ds_read_b64_tr_b16 a[238:239], v221 offset:39424\n" \
"v_mfma_f32_16x16x32_bf16 v[152:155], a[200:203], v[80:83], v[152:155]\n" \
"ds_read_b64_tr_b16 a[212:213], v221 offset:45568\n" \
"buffer_load_dwordx4 a[168:171], v209, %[srdz], s50 offen offset:2048 sc1\n" \
"v_mfma_f32_16x16x32_bf16 v[152:155], a[204:207], v[88:91], v[152:155]\n" \
"ds_read_b64_tr_b16 a[214:215], v221 offset:47616\n" \
"v_mfma_f32_16x16x32_bf16 v[156:159], a[200:203], v[112:115], v[156:159]\n" \
"buffer_load_dwordx4 a[172:175], v209, %[srdz], s50 offen offset:3072 sc1\n" \
"v_mfma_f32_16x16x32_bf16 v[156:159], a[204:207], v[120:123], v[156:159]\n" \
"s_waitcnt vmcnt(63) lgkmcnt(12)\n" \
"s_nop 0\n" \
"v_mfma_f32_16x16x32_bf16 v[160:163], a[220:223], v[0:3], v[160:163]\n" \
"v_mfma_f32_16x16x32_bf16 v[160:163], a[216:219], v[8:11], v[160:163]\n" \
"buffer_load_dwordx4 a[176:179], v210, %[srdz], s50 offen offset:0 sc1\n" \
"v_mfma_f32_16x16x32_bf16 v[164:167], a[220:223], v[32:35], v[164:167]\n" \
"v_readlane_b32 s63, %[thi], 33\n" \
"v_lshlrev_b32_e64 v234, 3, v224\n" \
"v_mfma_f32_16x16x32_bf16 v[164:167], a[216:219], v[40:43], v[164:167]\n" \
"v_mfma_f32_16x16x32_bf16 v[168:171], a[220:223], v[64:67], v[168:171]\n" \
"v_and_b32_e32 v234, 0xc00, v234\n" \
"v_lshlrev_b32_e64 v235, 2, v224\n" \
"s_waitcnt vmcnt(25) lgkmcnt(0)\n" \
"s_barrier\n" \
"v_mfma_f32_16x16x32_bf16 v[168:171], a[216:219], v[72:75], v[168:171]\n" \
"ds_read_b64_tr_b16 a[196:197], v220 offset:49152\n" \
"v_mfma_f32_16x16x32_bf16 v[172:175], a[220:223], v[96:99], v[172:175]\n" \
"ds_read_b64_tr_b16 a[198:199], v220 offset:51200\n" \
"v_and_b32_e32 v235, 0x7000, v235\n" \
"v_bfe_u32 v230, v224, 9, 1\n" \
"v_mfma_f32_16x16x32_bf16 v[172:175], a[216:219], v[104:107], v[172:175]\n" \
"ds_read_b64_tr_b16 a[200:201], v220 offset:53248\n" \
"v_mfma_f32_16x16x32_bf16 v[160:163], a[208:211], v[16:19], v[160:163]\n" \
"ds_read_b64_tr_b16 a[202:203], v220 offset:55296\n" \
"v_lshl_or_b32 v235, v230, 2, v235\n" \
"v_readlane_b32 s52, %[thi], 35\n" \
"buffer_load_dwordx4 a[180:183], v210, %[srdz], s50 offen offset:1024 sc1\n" \
"v_mfma_f32_16x16x32_bf16 v[160:163], a[224:227], v[24:27], v[160:163]\n" \
"ds_read_b64_tr_b16 a[204:205], v220 offset:57344\n" \
"v_or_b32_e64 v234, v234, v235\n" \
"v_mfma_f32_16x16x32_bf16 v[164:167], a[208:211], v[48:51], v[164:167]\n" \
"ds_read_b64_tr_b16 a[206:207], v220 offset:59392\n" \
"v_mfma_f32_16x16x32_bf16 v[164:167], a[224:227], v[56:59], v[164:167]\n" \
"ds_read_b64_tr_b16 a[220:221], v220 offset:61440\n" \
"v_mfma_f32_16x16x32_bf16 v[168:171], a[208:211], v[80:83], v[168:171]\n" \
"ds_read_b64_tr_b16 a[222:223], v220 offset:63488\n" \
"v_mfma_f32_16x16x32_bf16 v[168:171], a[224:227], v[88:91], v[168:171]\n" \
"buffer_load_dword v235, v234, %[srdz], s63 offen sc1\n" \
"v_mfma_f32_16x16x32_bf16 v[172:175], a[208:211], v[112:115], v[172:175]\n" \
"v_mfma_f32_16x16x32_bf16 v[172:175], a[224:227], v[120:123], v[172:175]\n" \
"s_waitcnt vmcnt(63) lgkmcnt(4)\n" \
"s_nop 0\n" \
"v_mfma_f32_16x16x32_bf16 v[176:179], a[228:231], v[0:3], v[176:179]\n" \
"buffer_load_dwordx4 a[184:187], v210, %[srdz], s50 offen offset:2048 sc1\n" \
"v_mfma_f32_16x16x32_bf16 v[176:179], a[236:239], v[8:11], v[176:179]\n" \
"v_mfma_f32_16x16x32_bf16 v[180:183], a[228:231], v[32:35], v[180:183]\n" \
"buffer_load_dwordx4 a[188:191], v210, %[srdz], s50 offen offset:3072 sc1\n" \
"v_mfma_f32_16x16x32_bf16 v[180:183], a[236:239], v[40:43], v[180:183]\n" \
"v_mfma_f32_16x16x32_bf16 v[184:187], a[228:231], v[64:67], v[184:187]\n" \
"v_mfma_f32_16x16x32_bf16 v[184:187], a[236:239], v[72:75], v[184:187]\n" \
".if . - adaln_body != 53248\n" \
".error \"size model\"\n" \
".endif\n" \
"Lk12_%=:\n" \
"s_cbranch_execz Lk13_%=\n" \
"s_nop 0\n" \
"v_mfma_f32_16x16x32_bf16 v[188:191], a[228:231], v[96:99], v[188:191]\n" \
"ds_read_b64_tr_b16 a[216:217], v221 offset:49152\n" \
"s_waitcnt vmcnt(23)\n" \
"v_cmp_eq_u32_e32 vcc, 0, v232\n" \
"s_cbranch_vccnz Lspp_%=\n" \
"Lbkp_%=:\n" \
"s_nop 0\n" \
"v_mfma_f32_16x16x32_bf16 v[188:191], a[236:239], v[104:107], v[188:191]\n" \
"ds_read_b64_tr_b16 a[218:219], v221 offset:51200\n" \
"v_mfma_f32_16x16x32_bf16 v[192:195], a[196:199], a[0:3], v[128:131]\n" \
"ds_read_b64_tr_b16 a[208:209], v221 offset:53248\n" \
"v_mfma_f32_16x16x32_bf16 v[192:195], a[200:203], a[4:7], v[192:195]\n" \
"ds_read_b64_tr_b16 a[210:211], v221 offset:55296\n" \
"s_add_u32 m0, %[m0b], 0\n" \
"s_cmp_gt_u32 s48, 3\n" \
"s_cselect_b32 s49, 3, 4\n" \
"s_lshl_b32 s49, s49, 18\n" \
"s_or_b32 s49, s49, s59\n" \
"s_nop 0\n" \
"buffer_load_dwordx4 %[wo0], %[srdt], s49 offen offset:0 lds\n" \
"v_mfma_f32_16x16x32_bf16 v[196:199], a[196:199], a[16:19], v[132:135]\n" \
"v_mfma_f32_16x16x32_bf16 v[196:199], a[200:203], a[20:23], v[196:199]\n" \
"v_mfma_f32_16x16x32_bf16 v[200:203], a[196:199], a[32:35], v[136:139]\n" \
"v_mfma_f32_16x16x32_bf16 v[200:203], a[200:203], a[36:39], v[200:203]\n" \
"v_mfma_f32_16x16x32_bf16 v[204:207], a[196:199], a[48:51], v[140:143]\n" \
"ds_read_b64_tr_b16 a[224:225], v221 offset:57344\n" \
"v_mfma_f32_16x16x32_bf16 v[204:207], a[200:203], a[52:55], v[204:207]\n" \
"s_waitcnt vmcnt(21) lgkmcnt(5)\n" \
"s_nop 0\n" \
"v_mfma_f32_16x16x32_bf16 v[192:195], a[204:207], a[8:11], v[192:195]\n" \
"ds_read_b64_tr_b16 a[226:227], v221 offset:59392\n" \
"v_mfma_f32_16x16x32_bf16 v[192:195], a[220:223], a[12:15], v[192:195]\n" \
"ds_read_b64_tr_b16 a[228:229], v221 offset:61440\n" \
"v_mfma_f32_16x16x32_bf16 v[196:199], a[204:207], a[24:27], v[196:199]\n" \
"ds_read_b64_tr_b16 a[230:231], v221 offset:63488\n" \
"v_mfma_f32_16x16x32_bf16 v[196:199], a[220:223], a[28:31], v[196:199]\n" \
"buffer_load_dwordx4 v212, %[srdt], s49 offen offset:1024 lds\n" \
"v_mfma_f32_16x16x32_bf16 v[200:203], a[204:207], a[40:43], v[200:203]\n" \
"v_mfma_f32_16x16x32_bf16 v[200:203], a[220:223], a[44:47], v[200:203]\n" \
"v_mfma_f32_16x16x32_bf16 v[204:207], a[204:207], a[56:59], v[204:207]\n" \
"ds_read_b64_tr_b16 a[236:237], v220 offset:49664\n" \
"v_mfma_f32_16x16x32_bf16 v[204:207], a[220:223], a[60:63], v[204:207]\n" \
"ds_read_b64_tr_b16 a[238:239], v220 offset:51712\n" \
"v_mfma_f32_16x16x32_bf16 v[176:179], a[232:235], v[16:19], v[176:179]\n" \
"v_mfma_f32_16x16x32_bf16 v[176:179], a[212:215], v[24:27], v[176:179]\n" \
"v_mfma_f32_16x16x32_bf16 v[180:183], a[232:235], v[48:51], v[180:183]\n" \
"buffer_load_dwordx4 v213, %[srdt], s49 offen offset:2048 lds\n" \
"v_mfma_f32_16x16x32_bf16 v[180:183], a[212:215], v[56:59], v[180:183]\n" \
"v_mfma_f32_16x16x32_bf16 v[184:187], a[232:235], v[80:83], v[184:187]\n" \
"s_add_u32 m0, %[m0b], 98304\n" \
"s_mov_b32 s61, 0\n" \
"s_nop 0\n" \
"buffer_load_dwordx4 %[vdn], s[64:67], s61 offen offset:0 lds\n" \
"v_mfma_f32_16x16x32_bf16 v[184:187], a[212:215], v[88:91], v[184:187]\n" \
"v_cmp_u_f32_e64 s[44:45], v192, v196\n" \
"v_mfma_f32_16x16x32_bf16 v[188:191], a[232:235], v[112:115], v[188:191]\n" \
"v_mfma_f32_16x16x32_bf16 v[188:191], a[212:215], v[120:123], v[188:191]\n" \
"v_cmp_u_f32_e64 s[56:57], v200, v204\n" \
"s_or_b64 s[56:57], s[56:57], s[44:45]\n" \
"s_cbranch_scc1 Lsp6_%=\n" \
"Lbk6_%=:\n" \
"s_waitcnt vmcnt(63) lgkmcnt(2)\n" \
"s_nop 0\n" \
"v_mfma_f32_16x16x32_bf16 v[144:147], a[216:219], a[0:3], v[144:147]\n" \
"ds_read_b64_tr_b16 a[232:233], v220 offset:53760\n" \
"v_mfma_f32_16x16x32_bf16 v[144:147], a[208:211], a[4:7], v[144:147]\n" \
"ds_read_b64_tr_b16 a[234:235], v220 offset:55808\n" \
"v_mfma_f32_16x16x32_bf16 v[148:151], a[216:219], a[16:19], v[148:151]\n" \
"ds_read_b64_tr_b16 a[196:197], v220 offset:57856\n" \
"v_mfma_f32_16x16x32_bf16 v[148:151], a[208:211], a[20:23], v[148:151]\n" \
"ds_read_b64_tr_b16 a[198:199], v220 offset:59904\n" \
"v_mfma_f32_16x16x32_bf16 v[152:155], a[216:219], a[32:35], v[152:155]\n" \
"ds_read_b64_tr_b16 a[200:201], v220 offset:61952\n" \
"v_mfma_f32_16x16x32_bf16 v[152:155], a[208:211], a[36:39], v[152:155]\n" \
"ds_read_b64_tr_b16 a[202:203], v220 offset:64000\n" \
"v_mfma_f32_16x16x32_bf16 v[156:159], a[216:219], a[48:51], v[156:159]\n" \
"ds_read_b64_tr_b16 a[204:205], v221 offset:49664\n" \
"v_mfma_f32_16x16x32_bf16 v[156:159], a[208:211], a[52:55], v[156:159]\n" \
"ds_read_b64_tr_b16 a[206:207], v221 offset:51712\n" \
"s_add_u32 m0, %[m0b], 0\n" \
"s_nop 0\n" \
"buffer_load_dwordx4 v214, %[srdt], s49 offen offset:3072 lds\n" \
"v_mfma_f32_16x16x32_bf16 v[144:147], a[224:227], a[8:11], v[144:147]\n" \
"ds_read_b64_tr_b16 a[212:213], v221 offset:57856\n" \
"v_mfma_f32_16x16x32_bf16 v[144:147], a[228:231], a[12:15], v[144:147]\n" \
"ds_read_b64_tr_b16 a[214:215], v221 offset:59904\n" \
"v_mfma_f32_16x16x32_bf16 v[148:151], a[224:227], a[24:27], v[148:151]\n" \
"ds_read_b64_tr_b16 a[220:221], v221 offset:53760\n" \
"v_mfma_f32_16x16x32_bf16 v[148:151], a[228:231], a[28:31], v[148:151]\n" \
"ds_read_b64_tr_b16 a[222:223], v221 offset:55808\n" \
"v_mfma_f32_16x16x32_bf16 v[152:155], a[224:227], a[40:43], v[152:155]\n" \
"ds_read_b64_tr_b16 a[216:217], v221 offset:61952\n" \
"v_mfma_f32_16x16x32_bf16 v[152:155], a[228:231], a[44:47], v[152:155]\n" \
"ds_read_b64_tr_b16 a[218:219], v221 offset:64000\n" \
"v_mfma_f32_16x16x32_bf16 v[156:159], a[224:227], a[56:59], v[156:159]\n" \
"v_xor_b32_e64 v218, 64, %[vdn]\n" \
"s_add_u32 m0, %[m0b], 99328\n" \
"s_mov_b32 s61, 4096\n" \
"buffer_load_dwordx4 v218, s[64:67], s61 offen offset:0 lds\n" \
"v_mfma_f32_16x16x32_bf16 v[156:159], a[228:231], a[60:63], v[156:159]\n" \
"s_waitcnt vmcnt(63) lgkmcnt(12)\n" \
"s_nop 0\n" \
"v_mfma_f32_16x16x32_bf16 v[160:163], a[236:239], a[0:3], v[160:163]\n" \
"v_mfma_f32_16x16x32_bf16 v[160:163], a[232:235], a[4:7], v[160:163]\n" \
"v_mfma_f32_16x16x32_bf16 v[164:167], a[236:239], a[16:19], v[164:167]\n" \
"v_mfma_f32_16x16x32_bf16 v[164:167], a[232:235], a[20:23], v[164:167]\n" \
"s_add_u32 m0, %[m0b], 16384\n" \
"s_cmp_gt_u32 s48, 4\n" \
"s_cselect_b32 s49, 4, 5\n" \
"s_lshl_b32 s49, s49, 18\n" \
"s_or_b32 s49, s49, s59\n" \
"buffer_load_dwordx4 %[wo0], %[srdt], s49 offen offset:0 lds\n" \
"v_mfma_f32_16x16x32_bf16 v[168:171], a[236:239], a[32:35], v[168:171]\n" \
"s_waitcnt vmcnt(15) lgkmcnt(0)\n" \
"s_barrier\n" \
"v_mfma_f32_16x16x32_bf16 v[168:171], a[232:235], a[36:39], v[168:171]\n" \
"ds_read_b64_tr_b16 a[208:209], v215 offset:49152\n" \
"v_mfma_f32_16x16x32_bf16 v[172:175], a[236:239], a[48:51], v[172:175]\n" \
"ds_read_b64_tr_b16 a[210:211], v215 offset:51200\n" \
"v_mfma_f32_16x16x32_bf16 v[172:175], a[232:235], a[52:55], v[172:175]\n" \
"ds_read_b64_tr_b16 a[224:225], v215 offset:53248\n" \
"v_mfma_f32_16x16x32_bf16 v[160:163], a[196:199], a[8:11], v[160:163]\n" \
"ds_read_b64_tr_b16 a[226:227], v215 offset:55296\n" \
"v_mfma_f32_16x16x32_bf16 v[160:163], a[200:203], a[12:15], v[160:163]\n" \
"ds_read_b64_tr_b16 a[228:229], v215 offset:57344\n" \
"v_mfma_f32_16x16x32_bf16 v[164:167], a[196:199], a[24:27], v[164:167]\n" \
"ds_read_b64_tr_b16 a[230:231], v215 offset:59392\n" \
"v_mfma_f32_16x16x32_bf16 v[164:167], a[200:203], a[28:31], v[164:167]\n" \
"ds_read_b64_tr_b16 a[236:237], v215 offset:61440\n" \
"v_mfma_f32_16x16x32_bf16 v[168:171], a[196:199], a[40:43], v[168:171]\n" \
"ds_read_b64_tr_b16 a[238:239], v215 offset:63488\n" \
"v_mfma_f32_16x16x32_bf16 v[168:171], a[200:203], a[44:47], v[168:171]\n" \
"buffer_load_dwordx4 v212, %[srdt], s49 offen offset:1024 lds\n" \
"v_mfma_f32_16x16x32_bf16 v[172:175], a[196:199], a[56:59], v[172:175]\n" \
"v_mfma_f32_16x16x32_bf16 v[172:175], a[200:203], a[60:63], v[172:175]\n" \
"s_waitcnt vmcnt(63) lgkmcnt(4)\n" \
"s_nop 0\n" \
"v_mfma_f32_16x16x32_bf16 v[176:179], a[204:207], a[0:3], v[176:179]\n" \
"v_xor_b32_e32 v218, 0x80, %[vdn]\n" \
"s_add_u32 m0, %[m0b], 100352\n" \
"s_mov_b32 s61, 8192\n" \
"buffer_load_dwordx4 v218, s[64:67], s61 offen offset:0 lds\n" \
"v_mfma_f32_16x16x32_bf16 v[176:179], a[220:223], a[4:7], v[176:179]\n" \
"v_mfma_f32_16x16x32_bf16 v[180:183], a[204:207], a[16:19], v[180:183]\n" \
"s_waitcnt vmcnt(11)\n" \
"v_and_b32_e32 v230, 1, v235\n" \
"v_mfma_f32_16x16x32_bf16 v[180:183], a[220:223], a[20:23], v[180:183]\n" \
"v_cmp_eq_u32_e64 s[68:69], 0, v230\n" \
"v_mfma_f32_16x16x32_bf16 v[184:187], a[204:207], a[32:35], v[184:187]\n" \
"s_cmp_lg_u64 s[68:69], 0\n" \
"s_cbranch_scc1 Ls1_%=\n" \
"Ls1b_%=:\n" \
"v_add_f32_e64 v235, %[rd0], v235\n" \
"v_mfma_f32_16x16x32_bf16 v[184:187], a[220:223], a[36:39], v[184:187]\n" \
"v_bfe_u32 v234, v224, 9, 1\n" \
"v_mfma_f32_16x16x32_bf16 v[188:191], a[204:207], a[48:51], v[188:191]\n" \
"ds_read_b64_tr_b16 a[232:233], v227 offset:49152\n" \
"v_add_f32_dpp v235, v235, v235 quad_perm:[1,0,3,2] row_mask:0xf bank_mask:0xf\n" \
"v_add_u32_e64 v230, s52, v234\n" \
"v_mfma_f32_16x16x32_bf16 v[188:191], a[220:223], a[52:55], v[188:191]\n" \
"ds_read_b64_tr_b16 a[234:235], v227 offset:51200\n" \
"s_mul_i32 s63, %[wid], 4088\n" \
"v_mfma_f32_16x16x32_bf16 v[128:131], a[208:211], a[128:131], v[192:195]\n" \
"ds_read_b64_tr_b16 a[196:197], v227 offset:53248\n" \
"v_mfma_f32_16x16x32_bf16 v[128:131], a[224:227], a[132:135], v[128:131]\n" \
"ds_read_b64_tr_b16 a[198:199], v227 offset:55296\n" \
"v_add_f32_dpp v235, v235, v235 quad_perm:[2,3,0,1] row_mask:0xf bank_mask:0xf\n" \
"s_sub_u32 s63, %[m0b], s63\n" \
"s_nop 0\n" \
"v_mfma_f32_16x16x32_bf16 v[132:135], a[208:211], a[144:147], v[196:199]\n" \
"v_lshl_add_u32 v234, v234, 2, s63\n" \
"s_add_u32 m0, %[m0b], 16384\n" \
"s_nop 0\n" \
"s_nop 0\n" \
"buffer_load_dwordx4 v213, %[srdt], s49 offen offset:2048 lds\n" \
"v_mfma_f32_16x16x32_bf16 v[132:135], a[224:227], a[148:151], v[132:135]\n" \
"v_add_f32_dpp v235, v235, v235 row_ror:8 row_mask:0xf bank_mask:0xf\n" \
"v_sub_u32_e32 v230, 0x7ff, v230\n" \
"v_mfma_f32_16x16x32_bf16 v[136:139], a[208:211], a[160:163], v[200:203]\n" \
"v_mov_b32_e64 v216, v235\n" \
"v_mfma_f32_16x16x32_bf16 v[136:139], a[224:227], a[164:167], v[136:139]\n" \
"v_add_u32_e32 v234, 0xcc04, v234\n" \
"v_mfma_f32_16x16x32_bf16 v[140:143], a[208:211], a[176:179], v[204:207]\n" \
"ds_read_b64_tr_b16 a[200:201], v227 offset:57344\n" \
"v_permlane16_swap_b32_e32 v235, v216\n" \
"s_nop 0\n" \
"v_mfma_f32_16x16x32_bf16 v[140:143], a[224:227], a[180:183], v[140:143]\n" \
"s_waitcnt vmcnt(10) lgkmcnt(5)\n" \
"s_nop 0\n" \
"v_mfma_f32_16x16x32_bf16 v[128:131], a[228:231], a[136:139], v[128:131]\n" \
"ds_read_b64_tr_b16 a[202:203], v227 offset:59392\n" \
"v_add_f32_e64 v235, v235, v216\n" \
"v_mfma_f32_16x16x32_bf16 v[128:131], a[236:239], a[140:143], v[128:131]\n" \
"ds_read_b64_tr_b16 a[204:205], v227 offset:61440\n" \
"v_mov_b32_e64 v216, v235\n" \
"v_xor_b32_e32 v218, 0xc0, %[vdn]\n" \
"s_add_u32 m0, %[m0b], 101376\n" \
"s_mov_b32 s61, 12288\n" \
"buffer_load_dwordx4 v218, s[64:67], s61 offen offset:0 lds\n" \
"v_mfma_f32_16x16x32_bf16 v[132:135], a[228:231], a[152:155], v[132:135]\n" \
"ds_read_b64_tr_b16 a[206:207], v227 offset:63488\n" \
"s_nop 0\n" \
"s_nop 0\n" \
"v_mfma_f32_16x16x32_bf16 v[132:135], a[236:239], a[156:159], v[132:135]\n" \
"v_permlane32_swap_b32_e32 v235, v216\n" \
"s_nop 0\n" \
"s_add_u32 m0, %[m0b], 16384\n" \
"s_nop 0\n" \
"s_nop 0\n" \
"buffer_load_dwordx4 v214, %[srdt], s49 offen offset:3072 lds\n" \
"v_mfma_f32_16x16x32_bf16 v[136:139], a[228:231], a[168:171], v[136:139]\n" \
"v_add_f32_e64 v235, v235, v216\n" \
"v_mfma_f32_16x16x32_bf16 v[136:139], a[236:239], a[172:175], v[136:139]\n" \
"v_ashrrev_i32_e64 v216, 31, v235\n" \
"v_mfma_f32_16x16x32_bf16 v[140:143], a[228:231], a[184:187], v[140:143]\n" \
"ds_read_b64_tr_b16 a[220:221], v215 offset:49664\n" \
"v_mfma_f32_16x16x32_bf16 v[140:143], a[236:239], a[188:191], v[140:143]\n" \
"ds_read_b64_tr_b16 a[222:223], v215 offset:51712\n" \
"v_or_b32_e32 v216, 0x80000000, v216\n" \
"v_mfma_f32_16x16x32_bf16 v[176:179], a[212:215], a[8:11], v[176:179]\n" \
"v_xor_b32_e64 v235, v235, v216\n" \
"v_mfma_f32_16x16x32_bf16 v[176:179], a[216:219], a[12:15], v[176:179]\n" \
"v_and_b32_e32 v235, 0xfffff800, v235\n" \
"v_mfma_f32_16x16x32_bf16 v[180:183], a[212:215], a[24:27], v[180:183]\n" \
"v_max_u32_e32 v235, 0x800, v235\n" \
"v_mfma_f32_16x16x32_bf16 v[180:183], a[216:219], a[28:31], v[180:183]\n" \
"v_or_b32_e64 v235, v235, v230\n" \
"v_mfma_f32_16x16x32_bf16 v[184:187], a[212:215], a[40:43], v[184:187]\n" \
"ds_write_b32 v234, v235 offset:65532\n" \
"v_mfma_f32_16x16x32_bf16 v[184:187], a[216:219], a[44:47], v[184:187]\n" \
"v_cmp_u_f32_e64 s[44:45], v128, v132\n" \
"v_mfma_f32_16x16x32_bf16 v[188:191], a[212:215], a[56:59], v[188:191]\n" \
"v_mfma_f32_16x16x32_bf16 v[188:191], a[216:219], a[60:63], v[188:191]\n" \
"v_cmp_u_f32_e64 s[56:57], v136, v140\n" \
"s_or_b64 s[56:57], s[56:57], s[44:45]\n" \
"s_cbranch_scc1 Lsp7_%=\n" \
"Lbk7_%=:\n" \
"s_waitcnt vmcnt(63) lgkmcnt(3)\n" \
"s_nop 0\n" \
"v_mfma_f32_16x16x32_bf16 v[144:147], a[232:235], a[128:131], v[144:147]\n" \
"ds_read_b64_tr_b16 a[212:213], v215 offset:53760\n" \
"v_mfma_f32_16x16x32_bf16 v[144:147], a[196:199], a[132:135], v[144:147]\n" \
"ds_read_b64_tr_b16 a[214:215], v215 offset:55808\n" \
"v_mfma_f32_16x16x32_bf16 v[148:151], a[232:235], a[144:147], v[148:151]\n" \
"ds_read_b64_tr_b16 a[208:209], v215 offset:57856\n" \
"v_mfma_f32_16x16x32_bf16 v[148:151], a[196:199], a[148:151], v[148:151]\n" \
"ds_read_b64_tr_b16 a[210:211], v215 offset:59904\n" \
"s_lshl_b32 s61, %[wid], 10\n" \
"v_lshrrev_b32_e32 v231, 3, v224\n" \
"v_add_u32_e64 v231, s61, v231\n" \
"s_add_u32 m0, %[m0b], 32768\n" \
"s_cmp_gt_u32 s48, 5\n" \
"s_cselect_b32 s49, 5, 6\n" \
"s_lshl_b32 s49, s49, 18\n" \
"s_or_b32 s49, s49, s59\n" \
"buffer_load_dwordx4 %[wo0], %[srdt], s49 offen offset:0 lds\n" \
"v_mfma_f32_16x16x32_bf16 v[152:155], a[232:235], a[160:163], v[152:155]\n" \
"ds_read_b64_tr_b16 a[216:217], v215 offset:61952\n" \
"v_readlane_b32 s44, %[tlo], 20\n" \
"v_readlane_b32 s45, %[thi], 20\n" \
"v_lshrrev_b32_e64 v236, 1, v231\n" \
"v_mfma_f32_16x16x32_bf16 v[152:155], a[196:199], a[164:167], v[152:155]\n" \
"ds_read_b64_tr_b16 a[218:219], v215 offset:64000\n" \
"v_mfma_f32_16x16x32_bf16 v[156:159], a[232:235], a[176:179], v[156:159]\n" \
"ds_read_b64_tr_b16 a[224:225], v227 offset:49664\n" \
"v_mfma_f32_16x16x32_bf16 v[156:159], a[196:199], a[180:183], v[156:159]\n" \
"ds_read_b64_tr_b16 a[226:227], v227 offset:51712\n" \
"v_mfma_f32_16x16x32_bf16 v[144:147], a[200:203], a[136:139], v[144:147]\n" \
"ds_read_b64_tr_b16 a[228:229], v227 offset:53760\n" \
"v_mfma_f32_16x16x32_bf16 v[144:147], a[204:207], a[140:143], v[144:147]\n" \
"ds_read_b64_tr_b16 a[230:231], v227 offset:55808\n" \
"v_mfma_f32_16x16x32_bf16 v[148:151], a[200:203], a[152:155], v[148:151]\n" \
"s_mov_b32 s61, 262144\n" \
"buffer_load_dwordx4 v[192:195], v231, %[srdz], s61 offen offset:0 sc1\n" \
"v_mfma_f32_16x16x32_bf16 v[148:151], a[204:207], a[156:159], v[148:151]\n" \
"v_mfma_f32_16x16x32_bf16 v[152:155], a[200:203], a[168:171], v[152:155]\n" \
"buffer_load_dwordx4 v212, %[srdt], s49 offen offset:1024 lds\n" \
"v_mfma_f32_16x16x32_bf16 v[152:155], a[204:207], a[172:175], v[152:155]\n" \
"v_mfma_f32_16x16x32_bf16 v[156:159], a[200:203], a[184:187], v[156:159]\n" \
"ds_read_b64_tr_b16 a[236:237], v227 offset:57856\n" \
"v_mfma_f32_16x16x32_bf16 v[156:159], a[204:207], a[188:191], v[156:159]\n" \
"s_waitcnt vmcnt(63) lgkmcnt(5)\n" \
"s_nop 0\n" \
"v_mfma_f32_16x16x32_bf16 v[160:163], a[220:223], a[128:131], v[160:163]\n" \
"ds_read_b64_tr_b16 a[238:239], v227 offset:59904\n" \
"v_mfma_f32_16x16x32_bf16 v[160:163], a[212:215], a[132:135], v[160:163]\n" \
"ds_read_b64_tr_b16 a[232:233], v227 offset:61952\n" \
"v_mfma_f32_16x16x32_bf16 v[164:167], a[220:223], a[144:147], v[164:167]\n" \
"ds_read_b64_tr_b16 a[234:235], v227 offset:64000\n" \
"s_mov_b32 s61, 266240\n" \
"buffer_load_dwordx4 v[196:199], v231, %[srdz], s61 offen offset:0 sc1\n" \
"v_mfma_f32_16x16x32_bf16 v[164:167], a[212:215], a[148:151], v[164:167]\n" \
"v_mfma_f32_16x16x32_bf16 v[168:171], a[220:223], a[160:163], v[168:171]\n" \
"v_cvt_pk_bf16_f32 v128, v128, v129\n" \
"buffer_load_dwordx4 v213, %[srdt], s49 offen offset:2048 lds\n" \
"v_mfma_f32_16x16x32_bf16 v[168:171], a[212:215], a[164:167], v[168:171]\n" \
"v_cvt_pk_bf16_f32 v129, v130, v131\n" \
"v_mfma_f32_16x16x32_bf16 v[172:175], a[220:223], a[176:179], v[172:175]\n" \
"v_cvt_pk_bf16_f32 v130, v144, v145\n" \
"v_mfma_f32_16x16x32_bf16 v[172:175], a[212:215], a[180:183], v[172:175]\n" \
"v_cvt_pk_bf16_f32 v131, v146, v147\n" \
"v_mfma_f32_16x16x32_bf16 v[160:163], a[208:211], a[136:139], v[160:163]\n" \
"s_add_u32 s55, s46, 0\n" \
"s_nop 0\n" \
"buffer_store_dwordx4 v[128:131], %[vxs], %[srdz], s55 offen offset:0 sc1\n" \
"v_lshrrev_b32_e32 v230, 5, v224\n" \
"s_lshl_b32 s63, %[wid], 12\n" \
"v_mfma_f32_16x16x32_bf16 v[160:163], a[216:219], a[140:143], v[160:163]\n" \
"v_and_b32_e32 v230, 28, v230\n" \
"s_sub_u32 s63, %[m0b], s63\n" \
"v_mfma_f32_16x16x32_bf16 v[164:167], a[208:211], a[152:155], v[164:167]\n" \
"v_cvt_pk_bf16_f32 v132, v132, v133\n" \
"s_mov_b32 s61, 270336\n" \
"buffer_load_dwordx4 v[200:203], v231, %[srdz], s61 offen offset:0 sc1\n" \
"s_add_u32 s63, s63, 117760\n" \
"v_add_u32_e64 v230, s63, v230\n" \
"v_mfma_f32_16x16x32_bf16 v[164:167], a[216:219], a[156:159], v[164:167]\n" \
"v_cvt_pk_bf16_f32 v133, v134, v135\n" \
"v_mfma_f32_16x16x32_bf16 v[168:171], a[208:211], a[168:171], v[168:171]\n" \
"s_waitcnt vmcnt(14) lgkmcnt(0)\n" \
"s_barrier\n" \
"v_mfma_f32_16x16x32_bf16 v[168:171], a[216:219], a[172:175], v[168:171]\n" \
"ds_read_b64_tr_b16 a[196:197], v215 offset:0\n" \
"v_cvt_pk_bf16_f32 v134, v148, v149\n" \
"ds_read_b32 v235, v230 offset:0\n" \
"v_readlane_b32 s63, %[thi], 34\n" \
"v_mfma_f32_16x16x32_bf16 v[172:175], a[208:211], a[184:187], v[172:175]\n" \
"ds_read_b64_tr_b16 a[198:199], v215 offset:2048\n" \
"v_cvt_pk_bf16_f32 v135, v150, v151\n" \
"v_mfma_f32_16x16x32_bf16 v[172:175], a[216:219], a[188:191], v[172:175]\n" \
"s_waitcnt vmcnt(63) lgkmcnt(7)\n" \
"s_nop 0\n" \
"v_mfma_f32_16x16x32_bf16 v[176:179], a[224:227], a[128:131], v[176:179]\n" \
"ds_read_b64_tr_b16 a[200:201], v215 offset:4096\n" \
"v_cvt_pk_bf16_f32 v136, v136, v137\n" \
"v_mfma_f32_16x16x32_bf16 v[176:179], a[228:231], a[132:135], v[176:179]\n" \
"ds_read_b64_tr_b16 a[202:203], v215 offset:6144\n" \
"v_cvt_pk_bf16_f32 v137, v138, v139\n" \
"v_mfma_f32_16x16x32_bf16 v[180:183], a[224:227], a[144:147], v[180:183]\n" \
"ds_read_b64_tr_b16 a[204:205], v215 offset:8192\n" \
"s_add_u32 s55, s46, 304128\n" \
"buffer_store_dwordx4 v[132:135], %[vxs], %[srdz], s55 offen offset:0 sc1\n" \
"v_mfma_f32_16x16x32_bf16 v[180:183], a[228:231], a[148:151], v[180:183]\n" \
"ds_read_b64_tr_b16 a[206:207], v215 offset:10240\n" \
"v_cvt_pk_bf16_f32 v138, v152, v153\n" \
"v_mfma_f32_16x16x32_bf16 v[184:187], a[224:227], a[160:163], v[184:187]\n" \
"ds_read_b64_tr_b16 a[220:221], v215 offset:12288\n" \
"v_cvt_pk_bf16_f32 v139, v154, v155\n" \
"s_mov_b32 s61, 274432\n" \
"buffer_load_dwordx4 v[204:207], v231, %[srdz], s61 offen offset:0 sc1\n" \
"s_waitcnt lgkmcnt(6)\n" \
"s_nop 0\n" \
"v_min_u32_dpp v234, v235, v235 quad_perm:[1,0,3,2] row_mask:0xf bank_mask:0xf\n" \
"v_max_u32_dpp v235, v235, v235 quad_perm:[1,0,3,2] row_mask:0xf bank_mask:0xf\n" \
"v_mfma_f32_16x16x32_bf16 v[184:187], a[228:231], a[164:167], v[184:187]\n" \
"ds_read_b64_tr_b16 a[222:223], v215 offset:14336\n" \
"v_mfma_f32_16x16x32_bf16 v[188:191], a[224:227], a[176:179], v[188:191]\n" \
"ds_read_b64_tr_b16 a[212:213], v227 offset:0\n" \
"v_cvt_pk_bf16_f32 v140, v140, v141\n" \
"v_min_u32_dpp v216, v235, v235 quad_perm:[2,3,0,1] row_mask:0xf bank_mask:0xf\n" \
"v_max_u32_dpp v230, v234, v234 quad_perm:[2,3,0,1] row_mask:0xf bank_mask:0xf\n" \
"buffer_load_dwordx4 v214, %[srdt], s49 offen offset:3072 lds\n" \
"v_mfma_f32_16x16x32_bf16 v[188:191], a[228:231], a[180:183], v[188:191]\n" \
"ds_read_b64_tr_b16 a[214:215], v227 offset:2048\n" \
"v_cvt_pk_bf16_f32 v141, v142, v143\n" \
"v_max_u32_dpp v235, v235, v235 quad_perm:[2,3,0,1] row_mask:0xf bank_mask:0xf\n" \
"v_max_u32_e64 v234, v216, v230\n" \
"v_mfma_f32_16x16x32_bf16 v[176:179], a[236:239], a[136:139], v[176:179]\n" \
"ds_read_b64_tr_b16 a[208:209], v227 offset:4096\n" \
"s_add_u32 s55, s46, 608256\n" \
"buffer_store_dwordx4 v[136:139], %[vxs], %[srdz], s55 offen offset:0 sc1\n" \
"v_mfma_f32_16x16x32_bf16 v[176:179], a[232:235], a[140:143], v[176:179]\n" \
"ds_read_b64_tr_b16 a[210:211], v227 offset:6144\n" \
"v_cvt_pk_bf16_f32 v142, v156, v157\n" \
"v_min_u32_dpp v216, v235, v235 row_half_mirror row_mask:0xf bank_mask:0xf\n" \
"v_max_u32_dpp v230, v234, v234 row_half_mirror row_mask:0xf bank_mask:0xf\n" \
"v_mfma_f32_16x16x32_bf16 v[180:183], a[236:239], a[152:155], v[180:183]\n" \
"v_cvt_pk_bf16_f32 v143, v158, v159\n" \
"v_max_u32_dpp v235, v235, v235 row_half_mirror row_mask:0xf bank_mask:0xf\n" \
"v_max_u32_e64 v234, v216, v230\n" \
"v_mfma_f32_16x16x32_bf16 v[180:183], a[232:235], a[156:159], v[180:183]\n" \
"s_add_u32 s55, s46, 912384\n" \
"buffer_store_dwordx4 v[140:143], %[vxs], %[srdz], s55 offen offset:0 sc1\n" \
"v_lshlrev_b32_e64 v230, 4, v224\n" \
"v_mfma_f32_16x16x32_bf16 v[184:187], a[236:239], a[168:171], v[184:187]\n" \
"v_mfma_f32_16x16x32_bf16 v[184:187], a[232:235], a[172:175], v[184:187]\n" \
"global_load_dwordx2 v[232:233], v236, s[44:45]\n" \
"v_mfma_f32_16x16x32_bf16 v[188:191], a[236:239], a[184:187], v[188:191]\n" \
"ds_read_b64_tr_b16 a[216:217], v227 offset:8192\n" \
"v_mfma_f32_16x16x32_bf16 v[188:191], a[232:235], a[188:191], v[188:191]\n" \
"s_waitcnt vmcnt(63) lgkmcnt(5)\n" \
"s_nop 0\n" \
"v_mfma_f32_16x16x32_bf16 v[4:7], a[196:199], a[64:67], v[4:7]\n" \
"ds_read_b64_tr_b16 a[218:219], v227 offset:10240\n" \
"s_mov_b64 exec, 3\n" \
"s_nop 0\n" \
"buffer_atomic_or_x2 v[234:235], v230, %[srdz], s63 offen sc1\n" \
"s_mov_b64 exec, -1\n" \
"s_nop 0\n" \
"v_mfma_f32_16x16x32_bf16 v[4:7], a[200:203], a[68:71], v[4:7]\n" \
"ds_read_b64_tr_b16 a[224:225], v227 offset:12288\n" \
"v_mfma_f32_16x16x32_bf16 v[36:39], a[196:199], a[80:83], v[36:39]\n" \
"ds_read_b64_tr_b16 a[226:227], v227 offset:14336\n" \
"v_mfma_f32_16x16x32_bf16 v[36:39], a[200:203], a[84:87], v[36:39]\n" \
"v_mfma_f32_16x16x32_bf16 v[68:71], a[196:199], a[96:99], v[68:71]\n" \
"v_cvt_pk_bf16_f32 v160, v160, v161\n" \
"v_mfma_f32_16x16x32_bf16 v[68:71], a[200:203], a[100:103], v[68:71]\n" \
"v_cvt_pk_bf16_f32 v161, v162, v163\n" \
"v_mfma_f32_16x16x32_bf16 v[100:103], a[196:199], a[112:115], v[100:103]\n" \
"ds_read_b64_tr_b16 a[228:229], v215 offset:512\n" \
"v_cvt_pk_bf16_f32 v162, v176, v177\n" \
"v_mfma_f32_16x16x32_bf16 v[100:103], a[200:203], a[116:119], v[100:103]\n" \
"ds_read_b64_tr_b16 a[230:231], v215 offset:2560\n" \
"v_cvt_pk_bf16_f32 v163, v178, v179\n" \
"v_mfma_f32_16x16x32_bf16 v[4:7], a[204:207], a[72:75], v[4:7]\n" \
"ds_read_b64_tr_b16 a[236:237], v215 offset:4608\n" \
"v_mfma_f32_16x16x32_bf16 v[4:7], a[220:223], a[76:79], v[4:7]\n" \
"ds_read_b64_tr_b16 a[238:239], v215 offset:6656\n" \
"v_mfma_f32_16x16x32_bf16 v[36:39], a[204:207], a[88:91], v[36:39]\n" \
"s_add_u32 s55, s46, 4096\n" \
"buffer_store_dwordx4 v[160:163], %[vxs], %[srdz], s55 offen offset:0 sc1\n" \
"v_mfma_f32_16x16x32_bf16 v[36:39], a[220:223], a[92:95], v[36:39]\n" \
"v_cvt_pk_bf16_f32 v164, v164, v165\n" \
"v_cvt_pk_bf16_f32 v165, v166, v167\n" \
"v_mfma_f32_16x16x32_bf16 v[68:71], a[204:207], a[104:107], v[68:71]\n" \
"v_cvt_pk_bf16_f32 v166, v180, v181\n" \
"s_add_u32 m0, %[m0b], 49152\n" \
"s_cmp_gt_u32 s48, 6\n" \
"s_cselect_b32 s49, 6, 7\n" \
"s_lshl_b32 s49, s49, 18\n" \
"s_or_b32 s49, s49, s59\n" \
"buffer_load_dwordx4 %[wo0], %[srdt], s49 offen offset:0 lds\n" \
"v_mfma_f32_16x16x32_bf16 v[68:71], a[220:223], a[108:111], v[68:71]\n" \
"v_cvt_pk_bf16_f32 v167, v182, v183\n" \
"v_mfma_f32_16x16x32_bf16 v[100:103], a[204:207], a[120:123], v[100:103]\n" \
"ds_read_b64_tr_b16 a[232:233], v215 offset:8704\n" \
"v_mfma_f32_16x16x32_bf16 v[100:103], a[220:223], a[124:127], v[100:103]\n" \
"s_waitcnt vmcnt(63) lgkmcnt(5)\n" \
"s_nop 0\n" \
"v_mfma_f32_16x16x32_bf16 v[12:15], a[212:215], a[64:67], v[12:15]\n" \
"ds_read_b64_tr_b16 a[234:235], v215 offset:10752\n" \
"v_cvt_pk_bf16_f32 v168, v168, v169\n" \
"v_mfma_f32_16x16x32_bf16 v[12:15], a[208:211], a[68:71], v[12:15]\n" \
"ds_read_b64_tr_b16 a[196:197], v215 offset:12800\n" \
"s_add_u32 s55, s46, 308224\n" \
"buffer_store_dwordx4 v[164:167], %[vxs], %[srdz], s55 offen offset:0 sc1\n" \
"v_mfma_f32_16x16x32_bf16 v[44:47], a[212:215], a[80:83], v[44:47]\n" \
"ds_read_b64_tr_b16 a[198:199], v215 offset:14848\n" \
"v_cvt_pk_bf16_f32 v169, v170, v171\n" \
"v_mfma_f32_16x16x32_bf16 v[44:47], a[208:211], a[84:87], v[44:47]\n" \
"v_cvt_pk_bf16_f32 v170, v184, v185\n" \
"v_cvt_pk_bf16_f32 v171, v186, v187\n" \
"v_mfma_f32_16x16x32_bf16 v[76:79], a[212:215], a[96:99], v[76:79]\n" \
"s_add_u32 s55, s46, 612352\n" \
"buffer_store_dwordx4 v[168:171], %[vxs], %[srdz], s55 offen offset:0 sc1\n" \
"v_mfma_f32_16x16x32_bf16 v[76:79], a[208:211], a[100:103], v[76:79]\n" \
"v_mfma_f32_16x16x32_bf16 v[108:111], a[212:215], a[112:115], v[108:111]\n" \
"ds_read_b64_tr_b16 a[200:201], v227 offset:512\n" \
"v_cvt_pk_bf16_f32 v172, v172, v173\n" \
"buffer_load_dwordx4 v212, %[srdt], s49 offen offset:1024 lds\n" \
"v_mfma_f32_16x16x32_bf16 v[108:111], a[208:211], a[116:119], v[108:111]\n" \
"ds_read_b64_tr_b16 a[202:203], v227 offset:2560\n" \
"v_cvt_pk_bf16_f32 v173, v174, v175\n" \
"v_mfma_f32_16x16x32_bf16 v[12:15], a[216:219], a[72:75], v[12:15]\n" \
"ds_read_b64_tr_b16 a[204:205], v227 offset:4608\n" \
"v_cvt_pk_bf16_f32 v174, v188, v189\n" \
"v_mfma_f32_16x16x32_bf16 v[12:15], a[224:227], a[76:79], v[12:15]\n" \
"ds_read_b64_tr_b16 a[206:207], v227 offset:6656\n" \
"v_cvt_pk_bf16_f32 v175, v190, v191\n" \
"v_mfma_f32_16x16x32_bf16 v[44:47], a[216:219], a[88:91], v[44:47]\n" \
"s_add_u32 s55, s46, 916480\n" \
"buffer_store_dwordx4 v[172:175], %[vxs], %[srdz], s55 offen offset:0 sc1\n" \
"v_mfma_f32_16x16x32_bf16 v[44:47], a[224:227], a[92:95], v[44:47]\n" \
"v_mfma_f32_16x16x32_bf16 v[76:79], a[216:219], a[104:107], v[76:79]\n" \
"buffer_load_dwordx4 v213, %[srdt], s49 offen offset:2048 lds\n" \
"v_mfma_f32_16x16x32_bf16 v[76:79], a[224:227], a[108:111], v[76:79]\n" \
"v_mfma_f32_16x16x32_bf16 v[108:111], a[216:219], a[120:123], v[108:111]\n" \
"ds_read_b64_tr_b16 a[220:221], v227 offset:8704\n" \
"v_mfma_f32_16x16x32_bf16 v[108:111], a[224:227], a[124:127], v[108:111]\n" \
"s_waitcnt vmcnt(63) lgkmcnt(5)\n" \
"s_nop 0\n" \
"v_mfma_f32_16x16x32_bf16 v[20:23], a[228:231], a[64:67], v[20:23]\n" \
"ds_read_b64_tr_b16 a[222:223], v227 offset:10752\n" \
"v_mfma_f32_16x16x32_bf16 v[20:23], a[236:239], a[68:71], v[20:23]\n" \
"ds_read_b64_tr_b16 a[212:213], v227 offset:12800\n" \
"v_mfma_f32_16x16x32_bf16 v[52:55], a[228:231], a[80:83], v[52:55]\n" \
"ds_read_b64_tr_b16 a[214:215], v227 offset:14848\n" \
"v_mfma_f32_16x16x32_bf16 v[52:55], a[236:239], a[84:87], v[52:55]\n" \
"global_load_dword v237, %[vct], %[pct]\n" \
"v_mfma_f32_16x16x32_bf16 v[84:87], a[228:231], a[96:99], v[84:87]\n" \
"v_mfma_f32_16x16x32_bf16 v[84:87], a[236:239], a[100:103], v[84:87]\n" \
"v_xor_b32_e32 v218, 0x80, %[vdn]\n" \
"s_add_u32 m0, %[m0b], 114688\n" \
"s_mov_b32 s61, 16384\n" \
"buffer_load_dwordx4 v218, s[64:67], s61 offen offset:0 lds\n" \
"v_mfma_f32_16x16x32_bf16 v[116:119], a[228:231], a[112:115], v[116:119]\n" \
"v_mfma_f32_16x16x32_bf16 v[116:119], a[236:239], a[116:119], v[116:119]\n" \
"s_add_u32 m0, %[m0b], 49152\n" \
"s_nop 0\n" \
"s_nop 0\n" \
"buffer_load_dwordx4 v214, %[srdt], s49 offen offset:3072 lds\n" \
"v_mfma_f32_16x16x32_bf16 v[20:23], a[232:235], a[72:75], v[20:23]\n" \
"v_mfma_f32_16x16x32_bf16 v[20:23], a[196:199], a[76:79], v[20:23]\n" \
"v_mfma_f32_16x16x32_bf16 v[52:55], a[232:235], a[88:91], v[52:55]\n" \
"v_mfma_f32_16x16x32_bf16 v[52:55], a[196:199], a[92:95], v[52:55]\n" \
"v_mfma_f32_16x16x32_bf16 v[84:87], a[232:235], a[104:107], v[84:87]\n" \
"s_waitcnt vmcnt(24) lgkmcnt(0)\n" \
"s_barrier\n" \
"v_mfma_f32_16x16x32_bf16 v[84:87], a[196:199], a[108:111], v[84:87]\n" \
"ds_read_b64_tr_b16 a[208:209], v215 offset:16384\n" \
"v_mfma_f32_16x16x32_bf16 v[116:119], a[232:235], a[120:123], v[116:119]\n" \
"ds_read_b64_tr_b16 a[210:211], v215 offset:18432\n" \
"v_mfma_f32_16x16x32_bf16 v[116:119], a[196:199], a[124:127], v[116:119]\n" \
"s_waitcnt vmcnt(63) lgkmcnt(2)\n" \
"s_nop 0\n" \
"v_mfma_f32_16x16x32_bf16 v[28:31], a[200:203], a[64:67], v[28:31]\n" \
"ds_read_b64_tr_b16 a[216:217], v215 offset:20480\n" \
"v_mfma_f32_16x16x32_bf16 v[28:31], a[204:207], a[68:71], v[28:31]\n" \
"ds_read_b64_tr_b16 a[218:219], v215 offset:22528\n" \
"v_mfma_f32_16x16x32_bf16 v[60:63], a[200:203], a[80:83], v[60:63]\n" \
"ds_read_b64_tr_b16 a[224:225], v215 offset:24576\n" \
"v_mfma_f32_16x16x32_bf16 v[60:63], a[204:207], a[84:87], v[60:63]\n" \
"ds_read_b64_tr_b16 a[226:227], v215 offset:26624\n" \
"v_mfma_f32_16x16x32_bf16 v[92:95], a[200:203], a[96:99], v[92:95]\n" \
"ds_read_b64_tr_b16 a[228:229], v215 offset:28672\n" \
"v_xor_b32_e32 v218, 0xc0, %[vdn]\n" \
"s_add_u32 m0, %[m0b], 115712\n" \
"s_mov_b32 s61, 20480\n" \
"buffer_load_dwordx4 v218, s[64:67], s61 offen offset:0 lds\n" \
"v_mfma_f32_16x16x32_bf16 v[92:95], a[204:207], a[100:103], v[92:95]\n" \
"ds_read_b64_tr_b16 a[230:231], v215 offset:30720\n" \
"v_mfma_f32_16x16x32_bf16 v[124:127], a[200:203], a[112:115], v[124:127]\n" \
"ds_read_b64_tr_b16 a[236:237], v227 offset:16384\n" \
"v_mfma_f32_16x16x32_bf16 v[124:127], a[204:207], a[116:119], v[124:127]\n" \
"ds_read_b64_tr_b16 a[238:239], v227 offset:18432\n" \
"v_mfma_f32_16x16x32_bf16 v[28:31], a[220:223], a[72:75], v[28:31]\n" \
"ds_read_b64_tr_b16 a[232:233], v227 offset:20480\n" \
"v_mfma_f32_16x16x32_bf16 v[28:31], a[212:215], a[76:79], v[28:31]\n" \
"ds_read_b64_tr_b16 a[234:235], v227 offset:22528\n" \
"v_mfma_f32_16x16x32_bf16 v[60:63], a[220:223], a[88:91], v[60:63]\n" \
"v_mfma_f32_16x16x32_bf16 v[60:63], a[212:215], a[92:95], v[60:63]\n" \
"v_mfma_f32_16x16x32_bf16 v[92:95], a[220:223], a[104:107], v[92:95]\n" \
"v_mfma_f32_16x16x32_bf16 v[92:95], a[212:215], a[108:111], v[92:95]\n" \
"v_mfma_f32_16x16x32_bf16 v[124:127], a[220:223], a[120:123], v[124:127]\n" \
"ds_read_b64_tr_b16 a[196:197], v227 offset:24576\n" \
"v_mfma_f32_16x16x32_bf16 v[124:127], a[212:215], a[124:127], v[124:127]\n" \
"s_waitcnt vmcnt(63) lgkmcnt(5)\n" \
"s_nop 0\n" \
"v_mfma_f32_16x16x32_bf16 v[4:7], a[208:211], v[0:3], v[4:7]\n" \
"ds_read_b64_tr_b16 a[198:199], v227 offset:26624\n" \
"v_mfma_f32_16x16x32_bf16 v[4:7], a[216:219], v[8:11], v[4:7]\n" \
"ds_read_b64_tr_b16 a[200:201], v227 offset:28672\n" \
"v_mfma_f32_16x16x32_bf16 v[36:39], a[208:211], v[32:35], v[36:39]\n" \
"ds_read_b64_tr_b16 a[202:203], v227 offset:30720\n" \
"v_mfma_f32_16x16x32_bf16 v[36:39], a[216:219], v[40:43], v[36:39]\n" \
"s_waitcnt vmcnt(12)\n" \
"s_nop 0\n" \
"v_and_or_b32 v236, v192, v193, 0\n" \
"v_bitop3_b32 v236, v236, v194, v195 bitop3:0x80\n" \
"s_add_u32 m0, %[m0b], 116736\n" \
"s_mov_b32 s61, 24576\n" \
"buffer_load_dwordx4 %[vdn], s[64:67], s61 offen offset:0 lds\n" \
"v_mfma_f32_16x16x32_bf16 v[68:71], a[208:211], v[64:67], v[68:71]\n" \
".if . - adaln_body != 57344\n" \
".error \"size model\"\n" \
".endif\n" \
"Lk13_%=:\n" \
"s_cbranch_execz Lk14_%=\n" \
"s_nop 0\n" \
"v_bitop3_b32 v236, v236, v196, v197 bitop3:0x80\n" \
"v_mfma_f32_16x16x32_bf16 v[68:71], a[216:219], v[72:75], v[68:71]\n" \
"v_bitop3_b32 v236, v236, v198, v199 bitop3:0x80\n" \
"v_mfma_f32_16x16x32_bf16 v[100:103], a[208:211], v[96:99], v[100:103]\n" \
"v_bitop3_b32 v236, v236, v200, v201 bitop3:0x80\n" \
"v_mfma_f32_16x16x32_bf16 v[100:103], a[216:219], v[104:107], v[100:103]\n" \
"v_bitop3_b32 v236, v236, v202, v203 bitop3:0x80\n" \
"v_mfma_f32_16x16x32_bf16 v[4:7], a[224:227], v[16:19], v[4:7]\n" \
"v_bitop3_b32 v236, v236, v204, v205 bitop3:0x80\n" \
"v_mfma_f32_16x16x32_bf16 v[4:7], a[228:231], v[24:27], v[4:7]\n" \
"v_bitop3_b32 v236, v236, v206, v207 bitop3:0x80\n" \
"v_mfma_f32_16x16x32_bf16 v[36:39], a[224:227], v[48:51], v[36:39]\n" \
"s_mov_b32 s54, 0x07060302\n" \
"v_and_b32_e64 v236, 1, v236\n" \
"v_mfma_f32_16x16x32_bf16 v[36:39], a[228:231], v[56:59], v[36:39]\n" \
"v_and_b32_e32 v231, 0x170, v231\n" \
"v_mfma_f32_16x16x32_bf16 v[68:71], a[224:227], v[80:83], v[68:71]\n" \
"s_waitcnt vmcnt(16) lgkmcnt(15)\n" \
"s_barrier\n" \
"v_mfma_f32_16x16x32_bf16 v[68:71], a[228:231], v[88:91], v[68:71]\n" \
"ds_read_b64_tr_b16 a[204:205], v215 offset:32768\n" \
"v_cmp_eq_u32_e64 s[56:57], 0, v236\n" \
"v_mfma_f32_16x16x32_bf16 v[100:103], a[224:227], v[112:115], v[100:103]\n" \
"ds_read_b64_tr_b16 a[206:207], v215 offset:34816\n" \
"v_lshrrev_b32_e64 v236, 7, v224\n" \
"v_mfma_f32_16x16x32_bf16 v[100:103], a[228:231], v[120:123], v[100:103]\n" \
"s_waitcnt vmcnt(13) lgkmcnt(2)\n" \
"s_nop 0\n" \
"v_mfma_f32_16x16x32_bf16 v[12:15], a[236:239], v[0:3], v[12:15]\n" \
"ds_read_b64_tr_b16 a[220:221], v215 offset:36864\n" \
"v_and_or_b32 v231, v236, 8, v231\n" \
"v_mfma_f32_16x16x32_bf16 v[12:15], a[232:235], v[8:11], v[12:15]\n" \
"ds_read_b64_tr_b16 a[222:223], v215 offset:38912\n" \
"v_lshrrev_b32_e64 v236, 12, v224\n" \
"v_mfma_f32_16x16x32_bf16 v[44:47], a[236:239], v[32:35], v[44:47]\n" \
"ds_read_b64_tr_b16 a[212:213], v215 offset:40960\n" \
"v_lshl_or_b32 v231, v236, 7, v231\n" \
"v_mfma_f32_16x16x32_bf16 v[44:47], a[232:235], v[40:43], v[44:47]\n" \
"ds_read_b64_tr_b16 a[214:215], v215 offset:43008\n" \
"s_mul_i32 s61, %[wid], 3584\n" \
"s_sub_u32 s61, %[m0b], s61\n" \
"v_add_u32_e32 v231, s61, v231\n" \
"v_mfma_f32_16x16x32_bf16 v[76:79], a[236:239], v[64:67], v[76:79]\n" \
"ds_read_b64_tr_b16 a[208:209], v215 offset:45056\n" \
"s_cmp_lg_u64 s[56:57], 0\n" \
"s_cbranch_scc1 Lpars_%=\n" \
"Lparb_%=:\n" \
"v_mov_b32_e64 v234, 0\n" \
"v_mfma_f32_16x16x32_bf16 v[76:79], a[232:235], v[72:75], v[76:79]\n" \
"ds_read_b64_tr_b16 a[210:211], v215 offset:47104\n" \
"v_lshlrev_b32_e64 v184, 16, v232\n" \
"v_mfma_f32_16x16x32_bf16 v[108:111], a[236:239], v[96:99], v[108:111]\n" \
"ds_read_b64_tr_b16 a[216:217], v227 offset:32768\n" \
"v_and_b32_e32 v185, 0xffff0000, v232\n" \
"v_mfma_f32_16x16x32_bf16 v[108:111], a[232:235], v[104:107], v[108:111]\n" \
"ds_read_b64_tr_b16 a[218:219], v227 offset:34816\n" \
"v_lshlrev_b32_e32 v186, 16, v233\n" \
"v_xor_b32_e32 v218, 64, %[vdn]\n" \
"s_add_u32 m0, %[m0b], 117760\n" \
"s_mov_b32 s61, 28672\n" \
"buffer_load_dwordx4 v218, s[64:67], s61 offen offset:0 lds\n" \
"v_mfma_f32_16x16x32_bf16 v[12:15], a[196:199], v[16:19], v[12:15]\n" \
"ds_read_b64_tr_b16 a[224:225], v227 offset:36864\n" \
"v_and_b32_e32 v187, 0xffff0000, v233\n" \
"v_mfma_f32_16x16x32_bf16 v[12:15], a[200:203], v[24:27], v[12:15]\n" \
"ds_read_b64_tr_b16 a[226:227], v227 offset:38912\n" \
"v_mul_f32_e64 v188, v200, v184\n" \
"v_mfma_f32_16x16x32_bf16 v[44:47], a[196:199], v[48:51], v[44:47]\n" \
"v_mul_f32_e32 v189, v201, v185\n" \
"v_mul_f32_e32 v190, v202, v186\n" \
"v_mfma_f32_16x16x32_bf16 v[44:47], a[200:203], v[56:59], v[44:47]\n" \
"v_mul_f32_e32 v191, v203, v187\n" \
"v_mul_f32_e32 v235, v196, v184\n" \
"v_mfma_f32_16x16x32_bf16 v[76:79], a[196:199], v[80:83], v[76:79]\n" \
"v_fmac_f32_e32 v235, v197, v185\n" \
"s_nop 0\n" \
"v_mfma_f32_16x16x32_bf16 v[76:79], a[200:203], v[88:91], v[76:79]\n" \
"v_fmac_f32_e32 v235, v198, v186\n" \
"s_nop 0\n" \
"v_mfma_f32_16x16x32_bf16 v[108:111], a[196:199], v[112:115], v[108:111]\n" \
"ds_read_b64_tr_b16 a[228:229], v227 offset:40960\n" \
"v_fmac_f32_e32 v235, v199, v187\n" \
"s_nop 0\n" \
"v_mfma_f32_16x16x32_bf16 v[108:111], a[200:203], v[120:123], v[108:111]\n" \
"s_waitcnt vmcnt(63) lgkmcnt(5)\n" \
"s_nop 0\n" \
"v_mfma_f32_16x16x32_bf16 v[4:7], a[204:207], a[0:3], v[4:7]\n" \
"ds_read_b64_tr_b16 a[230:231], v227 offset:43008\n" \
"v_add_f32_e64 v184, v188, v189\n" \
"v_mfma_f32_16x16x32_bf16 v[4:7], a[220:223], a[4:7], v[4:7]\n" \
"ds_read_b64_tr_b16 a[236:237], v227 offset:45056\n" \
"v_add_f32_e64 v185, v190, v191\n" \
"v_mfma_f32_16x16x32_bf16 v[36:39], a[204:207], a[16:19], v[36:39]\n" \
"ds_read_b64_tr_b16 a[238:239], v227 offset:47104\n" \
"v_add_f32_e64 v184, v184, v185\n" \
"v_mfma_f32_16x16x32_bf16 v[36:39], a[220:223], a[20:23], v[36:39]\n" \
"s_waitcnt vmcnt(7)\n" \
"v_mov_b32_e32 v230, 1\n" \
"buffer_store_dword v230, v225, %[srdz], s62 offen offset:64 sc1\n" \
"v_mfma_f32_16x16x32_bf16 v[68:71], a[204:207], a[32:35], v[68:71]\n" \
"v_lshrrev_b32_e32 v236, 12, v224\n" \
"v_permlane32_swap_b32_e32 v184, v235\n" \
"v_mfma_f32_16x16x32_bf16 v[68:71], a[220:223], a[36:39], v[68:71]\n" \
"v_mov_b32_e64 v230, 1\n" \
"buffer_atomic_add v230, v225, %[srdz], s47 offen sc1\n" \
"v_mfma_f32_16x16x32_bf16 v[100:103], a[204:207], a[48:51], v[100:103]\n" \
"v_add_f32_e64 v184, v184, v235\n" \
"s_mul_i32 s61, %[wid], 4092\n" \
"v_mfma_f32_16x16x32_bf16 v[100:103], a[220:223], a[52:55], v[100:103]\n" \
"v_add_f32_dpp v184, v184, v184 quad_perm:[1,0,3,2] row_mask:0xf bank_mask:0xf\n" \
"s_sub_u32 s61, %[m0b], s61\n" \
"s_nop 0\n" \
"v_mfma_f32_16x16x32_bf16 v[4:7], a[212:215], a[8:11], v[4:7]\n" \
"v_add_f32_dpp v184, v184, v184 quad_perm:[2,3,0,1] row_mask:0xf bank_mask:0xf\n" \
"v_mfma_f32_16x16x32_bf16 v[4:7], a[208:211], a[12:15], v[4:7]\n" \
"v_lshl_add_u32 v236, v236, 4, s61\n" \
"v_mfma_f32_16x16x32_bf16 v[36:39], a[212:215], a[24:27], v[36:39]\n" \
"v_add_f32_dpp v184, v184, v184 row_ror:4 row_mask:0xf bank_mask:0xf\n" \
"v_mfma_f32_16x16x32_bf16 v[36:39], a[208:211], a[28:31], v[36:39]\n" \
"s_nop 0\n" \
"s_nop 0\n" \
"v_mfma_f32_16x16x32_bf16 v[68:71], a[212:215], a[40:43], v[68:71]\n" \
"s_waitcnt vmcnt(5) lgkmcnt(15)\n" \
"s_barrier\n" \
"v_mfma_f32_16x16x32_bf16 v[68:71], a[208:211], a[44:47], v[68:71]\n" \
"ds_read_b64_tr_b16 a[232:233], v215 offset:49152\n" \
"v_add_f32_dpp v184, v184, v184 row_ror:8 row_mask:0xf bank_mask:0xf\n" \
"v_mfma_f32_16x16x32_bf16 v[100:103], a[212:215], a[56:59], v[100:103]\n" \
"ds_read_b64_tr_b16 a[234:235], v215 offset:51200\n" \
"v_mov_b32_e64 v185, v184\n" \
"v_mfma_f32_16x16x32_bf16 v[100:103], a[208:211], a[60:63], v[100:103]\n" \
"s_waitcnt vmcnt(63) lgkmcnt(2)\n" \
"s_nop 0\n" \
"v_mfma_f32_16x16x32_bf16 v[12:15], a[216:219], a[0:3], v[12:15]\n" \
"ds_read_b64_tr_b16 a[196:197], v215 offset:53248\n" \
"s_nop 0\n" \
"s_nop 0\n" \
"v_mfma_f32_16x16x32_bf16 v[12:15], a[224:227], a[4:7], v[12:15]\n" \
"ds_read_b64_tr_b16 a[198:199], v215 offset:55296\n" \
"v_permlane16_swap_b32_e32 v184, v185\n" \
"s_nop 0\n" \
"v_mfma_f32_16x16x32_bf16 v[44:47], a[216:219], a[16:19], v[44:47]\n" \
"ds_read_b64_tr_b16 a[200:201], v215 offset:57344\n" \
"v_add_f32_e64 v184, v184, v185\n" \
"v_mfma_f32_16x16x32_bf16 v[44:47], a[224:227], a[20:23], v[44:47]\n" \
"ds_read_b64_tr_b16 a[202:203], v215 offset:59392\n" \
"v_bfe_u32 v186, v231, 3, 1\n" \
"v_mfma_f32_16x16x32_bf16 v[76:79], a[216:219], a[32:35], v[76:79]\n" \
"ds_read_b64_tr_b16 a[204:205], v215 offset:61440\n" \
"s_movk_i32 s61, 2040\n" \
"s_nop 0\n" \
"v_mfma_f32_16x16x32_bf16 v[76:79], a[224:227], a[36:39], v[76:79]\n" \
"ds_read_b64_tr_b16 a[206:207], v215 offset:63488\n" \
"v_mad_u32_u24 v186, v186, s61, v231\n" \
"v_mfma_f32_16x16x32_bf16 v[108:111], a[216:219], a[48:51], v[108:111]\n" \
"ds_read_b64_tr_b16 a[220:221], v227 offset:49152\n" \
"v_perm_b32 v196, v197, v196, s54\n" \
"v_mfma_f32_16x16x32_bf16 v[108:111], a[224:227], a[52:55], v[108:111]\n" \
"ds_read_b64_tr_b16 a[222:223], v227 offset:51200\n" \
"v_perm_b32 v197, v199, v198, s54\n" \
"v_mfma_f32_16x16x32_bf16 v[12:15], a[228:231], a[8:11], v[12:15]\n" \
"ds_read_b64_tr_b16 a[212:213], v227 offset:53248\n" \
"v_perm_b32 v200, v201, v200, s54\n" \
"v_mfma_f32_16x16x32_bf16 v[12:15], a[236:239], a[12:15], v[12:15]\n" \
"ds_read_b64_tr_b16 a[214:215], v227 offset:55296\n" \
"v_perm_b32 v201, v203, v202, s54\n" \
"v_mfma_f32_16x16x32_bf16 v[44:47], a[228:231], a[24:27], v[44:47]\n" \
"v_perm_b32 v204, v205, v204, s54\n" \
"v_perm_b32 v205, v207, v206, s54\n" \
"v_mfma_f32_16x16x32_bf16 v[44:47], a[236:239], a[28:31], v[44:47]\n" \
"ds_write_b32 v236, v184 offset:14368\n" \
"v_mfma_f32_16x16x32_bf16 v[76:79], a[228:231], a[40:43], v[76:79]\n" \
"ds_write_b32 v236, v234 offset:14336\n" \
"v_mfma_f32_16x16x32_bf16 v[76:79], a[236:239], a[44:47], v[76:79]\n" \
"ds_write_b128 v186, v[192:195] offset:6144\n" \
"v_mfma_f32_16x16x32_bf16 v[108:111], a[228:231], a[56:59], v[108:111]\n" \
"ds_read_b64_tr_b16 a[208:209], v227 offset:57344\n" \
"ds_write_b128 v186, v[188:191] offset:10240\n" \
"v_mfma_f32_16x16x32_bf16 v[108:111], a[236:239], a[60:63], v[108:111]\n" \
"s_waitcnt vmcnt(63) lgkmcnt(9)\n" \
"s_nop 0\n" \
"v_mfma_f32_16x16x32_bf16 v[4:7], a[232:235], a[128:131], v[4:7]\n" \
"ds_read_b64_tr_b16 a[210:211], v227 offset:59392\n" \
"ds_write_b64 v231, v[196:197] offset:0\n" \
"v_mfma_f32_16x16x32_bf16 v[4:7], a[196:199], a[132:135], v[4:7]\n" \
"ds_read_b64_tr_b16 a[216:217], v227 offset:61440\n" \
"ds_write_b64 v231, v[200:201] offset:2048\n" \
"v_mfma_f32_16x16x32_bf16 v[36:39], a[232:235], a[144:147], v[36:39]\n" \
"ds_read_b64_tr_b16 a[218:219], v227 offset:63488\n" \
"ds_write_b64 v231, v[204:205] offset:4096\n" \
"v_mfma_f32_16x16x32_bf16 v[36:39], a[196:199], a[148:151], v[36:39]\n" \
"v_mfma_f32_16x16x32_bf16 v[68:71], a[232:235], a[160:163], v[68:71]\n" \
"v_mfma_f32_16x16x32_bf16 v[68:71], a[196:199], a[164:167], v[68:71]\n" \
"v_mfma_f32_16x16x32_bf16 v[100:103], a[232:235], a[176:179], v[100:103]\n" \
"ds_read_b64_tr_b16 a[224:225], v215 offset:16896\n" \
"v_mfma_f32_16x16x32_bf16 v[100:103], a[196:199], a[180:183], v[100:103]\n" \
"ds_read_b64_tr_b16 a[226:227], v215 offset:18944\n" \
"v_mfma_f32_16x16x32_bf16 v[4:7], a[200:203], a[136:139], v[4:7]\n" \
"ds_read_b64_tr_b16 a[228:229], v215 offset:20992\n" \
"v_mfma_f32_16x16x32_bf16 v[4:7], a[204:207], a[140:143], v[4:7]\n" \
"ds_read_b64_tr_b16 a[230:231], v215 offset:23040\n" \
"v_mfma_f32_16x16x32_bf16 v[36:39], a[200:203], a[152:155], v[36:39]\n" \
"s_waitcnt vmcnt(63) lgkmcnt(4)\n" \
"s_barrier\n" \
"v_mfma_f32_16x16x32_bf16 v[36:39], a[204:207], a[156:159], v[36:39]\n" \
"v_mov_b32_e64 v192, s40\n" \
"v_mfma_f32_16x16x32_bf16 v[68:71], a[200:203], a[168:171], v[68:71]\n" \
"ds_read_b128 v[196:199], v192 offset:14368\n" \
"v_mfma_f32_16x16x32_bf16 v[68:71], a[204:207], a[172:175], v[68:71]\n" \
"ds_read_b128 v[128:131], %[kb0] offset:6144\n" \
"v_mfma_f32_16x16x32_bf16 v[100:103], a[200:203], a[184:187], v[100:103]\n" \
"ds_read_b64_tr_b16 a[236:237], v215 offset:25088\n" \
"ds_read_b128 v[132:135], %[kb0] offset:8192\n" \
"v_mfma_f32_16x16x32_bf16 v[100:103], a[204:207], a[188:191], v[100:103]\n" \
"s_waitcnt vmcnt(63) lgkmcnt(9)\n" \
"s_nop 0\n" \
"v_mfma_f32_16x16x32_bf16 v[12:15], a[220:223], a[128:131], v[12:15]\n" \
"ds_read_b64_tr_b16 a[238:239], v215 offset:27136\n" \
"ds_read_b128 v[136:139], %[kb0] offset:7168\n" \
"v_mfma_f32_16x16x32_bf16 v[12:15], a[212:215], a[132:135], v[12:15]\n" \
"ds_read_b64_tr_b16 a[232:233], v215 offset:29184\n" \
"ds_read_b128 v[140:143], %[kb0] offset:9216\n" \
"v_mfma_f32_16x16x32_bf16 v[44:47], a[220:223], a[144:147], v[44:47]\n" \
"ds_read_b64_tr_b16 a[234:235], v215 offset:31232\n" \
"ds_read_b128 v[144:147], %[vvb] offset:6144\n" \
"v_mfma_f32_16x16x32_bf16 v[44:47], a[212:215], a[148:151], v[44:47]\n" \
"ds_read_b128 v[148:151], %[vvb] offset:8192\n" \
"v_mfma_f32_16x16x32_bf16 v[76:79], a[220:223], a[160:163], v[76:79]\n" \
"ds_read_b128 v[152:155], %[vvb] offset:7168\n" \
"v_mfma_f32_16x16x32_bf16 v[76:79], a[212:215], a[164:167], v[76:79]\n" \
"ds_read_b128 v[156:159], %[vvb] offset:9216\n" \
"v_mfma_f32_16x16x32_bf16 v[108:111], a[220:223], a[176:179], v[108:111]\n" \
"ds_read_b64_tr_b16 a[196:197], v227 offset:16896\n" \
"ds_read_b128 v[160:163], %[kb0] offset:10240\n" \
"v_mfma_f32_16x16x32_bf16 v[108:111], a[212:215], a[180:183], v[108:111]\n" \
"ds_read_b64_tr_b16 a[198:199], v227 offset:18944\n" \
"ds_read_b128 v[164:167], %[kb0] offset:12288\n" \
"v_mfma_f32_16x16x32_bf16 v[12:15], a[208:211], a[136:139], v[12:15]\n" \
"ds_read_b64_tr_b16 a[200:201], v227 offset:20992\n" \
"ds_read_b128 v[168:171], %[kb0] offset:11264\n" \
"v_mfma_f32_16x16x32_bf16 v[12:15], a[216:219], a[140:143], v[12:15]\n" \
"ds_read_b64_tr_b16 a[202:203], v227 offset:23040\n" \
"ds_read_b128 v[172:175], %[kb0] offset:13312\n" \
"v_mfma_f32_16x16x32_bf16 v[44:47], a[208:211], a[152:155], v[44:47]\n" \
"ds_read_b128 v[176:179], %[vvb] offset:10240\n" \
"v_mfma_f32_16x16x32_bf16 v[44:47], a[216:219], a[156:159], v[44:47]\n" \
"ds_read_b128 v[180:183], %[vvb] offset:12288\n" \
"v_mfma_f32_16x16x32_bf16 v[76:79], a[208:211], a[168:171], v[76:79]\n" \
"v_mov_b32_e64 v230, 64\n" \
"buffer_atomic_add v230, v225, %[srdz], s47 offen sc1\n" \
"v_mfma_f32_16x16x32_bf16 v[76:79], a[216:219], a[172:175], v[76:79]\n" \
"ds_read_b128 v[184:187], %[vvb] offset:11264\n" \
"v_mfma_f32_16x16x32_bf16 v[108:111], a[208:211], a[184:187], v[108:111]\n" \
"ds_read_b64_tr_b16 a[204:205], v227 offset:25088\n" \
"ds_read_b128 v[188:191], %[vvb] offset:13312\n" \
"v_mfma_f32_16x16x32_bf16 v[108:111], a[216:219], a[188:191], v[108:111]\n" \
"s_waitcnt vmcnt(63) lgkmcnt(14)\n" \
"s_nop 0\n" \
"v_mfma_f32_16x16x32_bf16 v[20:23], a[224:227], v[0:3], v[20:23]\n" \
"ds_read_b64_tr_b16 a[206:207], v227 offset:27136\n" \
"s_waitcnt lgkmcnt(14)\n" \
"v_add_f32_e32 v223, v196, v197\n" \
"v_mfma_f32_16x16x32_bf16 v[20:23], a[228:231], v[8:11], v[20:23]\n" \
"ds_read_b64_tr_b16 a[220:221], v227 offset:29184\n" \
"v_add_f32_e64 v198, v198, v199\n" \
"v_mfma_f32_16x16x32_bf16 v[52:55], a[224:227], v[32:35], v[52:55]\n" \
"ds_read_b64_tr_b16 a[222:223], v227 offset:31232\n" \
"v_add_f32_e64 v223, v223, v198\n" \
"v_mfma_f32_16x16x32_bf16 v[52:55], a[228:231], v[40:43], v[52:55]\n" \
"ds_read_b128 v[192:195], %[kb0] offset:2048\n" \
"v_mfma_f32_16x16x32_bf16 v[84:87], a[224:227], v[64:67], v[84:87]\n" \
"v_cvt_pk_bf16_f32 v4, v4, v5\n" \
"ds_read_b128 v[196:199], %[kb0] offset:3072\n" \
"v_mfma_f32_16x16x32_bf16 v[84:87], a[228:231], v[72:75], v[84:87]\n" \
"v_cvt_pk_bf16_f32 v5, v6, v7\n" \
"ds_read_b128 v[200:203], %[vvb] offset:2048\n" \
"v_mfma_f32_16x16x32_bf16 v[116:119], a[224:227], v[96:99], v[116:119]\n" \
"ds_read_b64_tr_b16 a[212:213], v215 offset:33280\n" \
"v_cvt_pk_bf16_f32 v6, v12, v13\n" \
"v_mfma_f32_16x16x32_bf16 v[116:119], a[228:231], v[104:107], v[116:119]\n" \
"ds_read_b64_tr_b16 a[214:215], v215 offset:35328\n" \
"v_cvt_pk_bf16_f32 v7, v14, v15\n" \
"v_mfma_f32_16x16x32_bf16 v[20:23], a[236:239], v[16:19], v[20:23]\n" \
"ds_read_b64_tr_b16 a[208:209], v215 offset:37376\n" \
"ds_read_b128 v[204:207], %[vvb] offset:3072\n" \
"v_mfma_f32_16x16x32_bf16 v[20:23], a[232:235], v[24:27], v[20:23]\n" \
"ds_read_b64_tr_b16 a[210:211], v215 offset:39424\n" \
"v_mfma_f32_16x16x32_bf16 v[52:55], a[236:239], v[48:51], v[52:55]\n" \
"s_add_u32 s55, s46, 8192\n" \
"buffer_store_dwordx4 v[4:7], %[vxs], %[srdz], s55 offen offset:0 sc1\n" \
"v_mfma_f32_16x16x32_bf16 v[52:55], a[232:235], v[56:59], v[52:55]\n" \
"v_cvt_pk_bf16_f32 v36, v36, v37\n" \
"v_cvt_pk_bf16_f32 v37, v38, v39\n" \
"v_mfma_f32_16x16x32_bf16 v[84:87], a[236:239], v[80:83], v[84:87]\n" \
"v_cvt_pk_bf16_f32 v38, v44, v45\n" \
"v_mfma_f32_16x16x32_bf16 v[84:87], a[232:235], v[88:91], v[84:87]\n" \
"v_cvt_pk_bf16_f32 v39, v46, v47\n" \
"v_mfma_f32_16x16x32_bf16 v[116:119], a[236:239], v[112:115], v[116:119]\n" \
"ds_read_b64_tr_b16 a[216:217], v215 offset:41472\n" \
"v_mfma_f32_16x16x32_bf16 v[116:119], a[232:235], v[120:123], v[116:119]\n" \
"s_waitcnt vmcnt(63) lgkmcnt(9)\n" \
"s_nop 0\n" \
"v_mfma_f32_16x16x32_bf16 v[28:31], a[196:199], v[0:3], v[28:31]\n" \
"ds_read_b64_tr_b16 a[218:219], v215 offset:43520\n" \
"v_cvt_pk_bf16_f32 v68, v68, v69\n" \
"v_mfma_f32_16x16x32_bf16 v[28:31], a[200:203], v[8:11], v[28:31]\n" \
"ds_read_b64_tr_b16 a[224:225], v215 offset:45568\n" \
"s_add_u32 s55, s46, 312320\n" \
"buffer_store_dwordx4 v[36:39], %[vxs], %[srdz], s55 offen offset:0 sc1\n" \
"v_mfma_f32_16x16x32_bf16 v[60:63], a[196:199], v[32:35], v[60:63]\n" \
"ds_read_b64_tr_b16 a[226:227], v215 offset:47616\n" \
"v_cvt_pk_bf16_f32 v69, v70, v71\n" \
"v_mfma_f32_16x16x32_bf16 v[60:63], a[200:203], v[40:43], v[60:63]\n" \
"v_cvt_pk_bf16_f32 v70, v76, v77\n" \
"v_cvt_pk_bf16_f32 v71, v78, v79\n" \
"v_mfma_f32_16x16x32_bf16 v[92:95], a[196:199], v[64:67], v[92:95]\n" \
"s_add_u32 s55, s46, 616448\n" \
"buffer_store_dwordx4 v[68:71], %[vxs], %[srdz], s55 offen offset:0 sc1\n" \
"v_mfma_f32_16x16x32_bf16 v[92:95], a[200:203], v[72:75], v[92:95]\n" \
"v_accvgpr_read_b32 v1, a240\n" \
"v_mfma_f32_16x16x32_bf16 v[124:127], a[196:199], v[96:99], v[124:127]\n" \
"ds_read_b64_tr_b16 a[228:229], v227 offset:33280\n" \
"v_cvt_pk_bf16_f32 v100, v100, v101\n" \
"v_mfma_f32_16x16x32_bf16 v[124:127], a[200:203], v[104:107], v[124:127]\n" \
"ds_read_b64_tr_b16 a[230:231], v227 offset:35328\n" \
"v_cvt_pk_bf16_f32 v101, v102, v103\n" \
"v_mfma_f32_16x16x32_bf16 v[28:31], a[204:207], v[16:19], v[28:31]\n" \
"ds_read_b64_tr_b16 a[236:237], v227 offset:37376\n" \
"v_cvt_pk_bf16_f32 v102, v108, v109\n" \
"v_readlane_b32 s68, %[thi], 18\n" \
"v_lshlrev_b32_e64 v226, 7, v226\n" \
"v_mfma_f32_16x16x32_bf16 v[28:31], a[220:223], v[24:27], v[28:31]\n" \
"ds_read_b64_tr_b16 a[238:239], v227 offset:39424\n" \
"v_cvt_pk_bf16_f32 v103, v110, v111\n" \
"v_mfma_f32_16x16x32_bf16 v[60:63], a[204:207], v[48:51], v[60:63]\n" \
"s_add_u32 s55, s46, 920576\n" \
"buffer_store_dwordx4 v[100:103], %[vxs], %[srdz], s55 offen offset:0 sc1\n" \
"v_mfma_f32_16x16x32_bf16 v[60:63], a[220:223], v[56:59], v[60:63]\n" \
"v_lshlrev_b32_e64 v0, 16, v1\n" \
"v_mfma_f32_16x16x32_bf16 v[92:95], a[204:207], v[80:83], v[92:95]\n" \
"v_and_b32_e64 v1, s43, v1\n" \
"v_mfma_f32_16x16x32_bf16 v[92:95], a[220:223], v[88:91], v[92:95]\n" \
"v_accvgpr_read_b32 v3, a241\n" \
"v_mfma_f32_16x16x32_bf16 v[124:127], a[204:207], v[112:115], v[124:127]\n" \
"ds_read_b64_tr_b16 a[232:233], v227 offset:41472\n" \
"v_lshlrev_b32_e64 v2, 16, v3\n" \
"v_mfma_f32_16x16x32_bf16 v[124:127], a[220:223], v[120:123], v[124:127]\n" \
"s_waitcnt vmcnt(63) lgkmcnt(5)\n" \
"s_nop 0\n" \
"v_mfma_f32_16x16x32_bf16 v[20:23], a[212:215], a[0:3], v[20:23]\n" \
"ds_read_b64_tr_b16 a[234:235], v227 offset:43520\n" \
"v_and_b32_e64 v3, s43, v3\n" \
"v_mfma_f32_16x16x32_bf16 v[20:23], a[208:211], a[4:7], v[20:23]\n" \
"ds_read_b64_tr_b16 a[196:197], v227 offset:45568\n" \
"v_accvgpr_read_b32 v9, a244\n" \
"v_mfma_f32_16x16x32_bf16 v[52:55], a[212:215], a[16:19], v[52:55]\n" \
"ds_read_b64_tr_b16 a[198:199], v227 offset:47616\n" \
"v_lshlrev_b32_e64 v8, 16, v9\n" \
"v_mfma_f32_16x16x32_bf16 v[52:55], a[208:211], a[20:23], v[52:55]\n" \
"buffer_load_dword v216, v226, %[srdz], s68 offen offset:64 sc1\n" \
"v_mfma_f32_16x16x32_bf16 v[84:87], a[212:215], a[32:35], v[84:87]\n" \
"v_mov_b32_e32 v226, 0\n" \
"v_lshrrev_b32_e32 v217, 3, v224\n" \
"s_add_u32 s68, %[sin], 4096\n" \
"v_and_b32_e64 v9, s43, v9\n" \
"v_mfma_f32_16x16x32_bf16 v[84:87], a[208:211], a[36:39], v[84:87]\n" \
"v_accvgpr_read_b32 v11, a245\n" \
"v_mfma_f32_16x16x32_bf16 v[116:119], a[212:215], a[48:51], v[116:119]\n" \
"ds_read_b64_tr_b16 a[200:201], v215 offset:49664\n" \
"v_lshlrev_b32_e64 v10, 16, v11\n" \
"v_mfma_f32_16x16x32_bf16 v[116:119], a[208:211], a[52:55], v[116:119]\n" \
"ds_read_b64_tr_b16 a[202:203], v215 offset:51712\n" \
"v_and_b32_e64 v11, s43, v11\n" \
"v_mfma_f32_16x16x32_bf16 v[20:23], a[216:219], a[8:11], v[20:23]\n" \
"ds_read_b64_tr_b16 a[204:205], v215 offset:53760\n" \
"v_accvgpr_read_b32 v33, a248\n" \
"v_mfma_f32_16x16x32_bf16 v[20:23], a[224:227], a[12:15], v[20:23]\n" \
"ds_read_b64_tr_b16 a[206:207], v215 offset:55808\n" \
"v_lshlrev_b32_e64 v32, 16, v33\n" \
"v_mfma_f32_16x16x32_bf16 v[52:55], a[216:219], a[24:27], v[52:55]\n" \
"ds_read_b64_tr_b16 a[220:221], v215 offset:57856\n" \
"v_and_b32_e64 v33, s43, v33\n" \
"v_mfma_f32_16x16x32_bf16 v[52:55], a[224:227], a[28:31], v[52:55]\n" \
"ds_read_b64_tr_b16 a[222:223], v215 offset:59904\n" \
"v_accvgpr_read_b32 v35, a249\n" \
"v_mfma_f32_16x16x32_bf16 v[84:87], a[216:219], a[40:43], v[84:87]\n" \
"ds_read_b64_tr_b16 a[212:213], v215 offset:61952\n" \
"v_lshlrev_b32_e64 v34, 16, v35\n" \
"v_mfma_f32_16x16x32_bf16 v[84:87], a[224:227], a[44:47], v[84:87]\n" \
"ds_read_b64_tr_b16 a[214:215], v215 offset:64000\n" \
"v_and_b32_e64 v35, s43, v35\n" \
"v_mfma_f32_16x16x32_bf16 v[116:119], a[216:219], a[56:59], v[116:119]\n" \
"ds_read_b64_tr_b16 a[208:209], v227 offset:49664\n" \
"v_accvgpr_read_b32 v41, a252\n" \
"v_mfma_f32_16x16x32_bf16 v[116:119], a[224:227], a[60:63], v[116:119]\n" \
"s_waitcnt vmcnt(63) lgkmcnt(9)\n" \
"s_nop 0\n" \
"v_mfma_f32_16x16x32_bf16 v[28:31], a[228:231], a[0:3], v[28:31]\n" \
"ds_read_b64_tr_b16 a[210:211], v227 offset:51712\n" \
"v_lshlrev_b32_e64 v40, 16, v41\n" \
"v_mfma_f32_16x16x32_bf16 v[28:31], a[236:239], a[4:7], v[28:31]\n" \
"ds_read_b64_tr_b16 a[216:217], v227 offset:53760\n" \
"v_and_b32_e64 v41, s43, v41\n" \
"v_mfma_f32_16x16x32_bf16 v[60:63], a[228:231], a[16:19], v[60:63]\n" \
"ds_read_b64_tr_b16 a[218:219], v227 offset:55808\n" \
"v_accvgpr_read_b32 v43, a253\n" \
"v_mfma_f32_16x16x32_bf16 v[60:63], a[236:239], a[20:23], v[60:63]\n" \
"ds_read_b64_tr_b16 a[224:225], v227 offset:57856\n" \
"v_lshlrev_b32_e64 v42, 16, v43\n" \
"v_mfma_f32_16x16x32_bf16 v[92:95], a[228:231], a[32:35], v[92:95]\n" \
"ds_read_b64_tr_b16 a[226:227], v227 offset:59904\n" \
"v_and_b32_e64 v43, s43, v43\n" \
"v_mfma_f32_16x16x32_bf16 v[92:95], a[236:239], a[36:39], v[92:95]\n" \
"v_accvgpr_read_b32 v17, a242\n" \
"v_mfma_f32_16x16x32_bf16 v[124:127], a[228:231], a[48:51], v[124:127]\n" \
"v_lshlrev_b32_e64 v16, 16, v17\n" \
"v_mfma_f32_16x16x32_bf16 v[124:127], a[236:239], a[52:55], v[124:127]\n" \
"ds_read_b64_tr_b16 a[228:229], v227 offset:61952\n" \
"v_and_b32_e64 v17, s43, v17\n" \
"v_mfma_f32_16x16x32_bf16 v[28:31], a[232:235], a[8:11], v[28:31]\n" \
"ds_read_b64_tr_b16 a[230:231], v227 offset:64000\n" \
"v_accvgpr_read_b32 v19, a243\n" \
"v_mfma_f32_16x16x32_bf16 v[28:31], a[196:199], a[12:15], v[28:31]\n" \
"v_lshlrev_b32_e64 v18, 16, v19\n" \
"v_mfma_f32_16x16x32_bf16 v[60:63], a[232:235], a[24:27], v[60:63]\n" \
"v_and_b32_e64 v19, s43, v19\n" \
"v_mfma_f32_16x16x32_bf16 v[60:63], a[196:199], a[28:31], v[60:63]\n" \
"v_accvgpr_read_b32 v25, a246\n" \
"v_mfma_f32_16x16x32_bf16 v[92:95], a[232:235], a[40:43], v[92:95]\n" \
"v_lshlrev_b32_e64 v24, 16, v25\n" \
"v_mfma_f32_16x16x32_bf16 v[92:95], a[196:199], a[44:47], v[92:95]\n" \
"v_and_b32_e64 v25, s43, v25\n" \
"v_mfma_f32_16x16x32_bf16 v[124:127], a[232:235], a[56:59], v[124:127]\n" \
"v_accvgpr_read_b32 v27, a247\n" \
"v_mfma_f32_16x16x32_bf16 v[124:127], a[196:199], a[60:63], v[124:127]\n" \
"s_waitcnt vmcnt(63) lgkmcnt(0)\n" \
"s_nop 0\n" \
"v_mfma_f32_16x16x32_bf16 v[20:23], a[200:203], a[128:131], v[20:23]\n" \
"v_lshlrev_b32_e64 v26, 16, v27\n" \
"v_mfma_f32_16x16x32_bf16 v[20:23], a[204:207], a[132:135], v[20:23]\n" \
"v_and_b32_e64 v27, s43, v27\n" \
"v_mfma_f32_16x16x32_bf16 v[20:23], a[220:223], a[136:139], v[20:23]\n" \
"v_accvgpr_read_b32 v49, a250\n" \
"v_mfma_f32_16x16x32_bf16 v[20:23], a[212:215], a[140:143], v[20:23]\n" \
"v_lshlrev_b32_e64 v48, 16, v49\n" \
"v_mfma_f32_16x16x32_bf16 v[28:31], a[208:211], a[128:131], v[28:31]\n" \
"v_and_b32_e64 v49, s43, v49\n" \
"v_mfma_f32_16x16x32_bf16 v[28:31], a[216:219], a[132:135], v[28:31]\n" \
"v_accvgpr_read_b32 v51, a251\n" \
"v_mfma_f32_16x16x32_bf16 v[28:31], a[224:227], a[136:139], v[28:31]\n" \
"v_lshlrev_b32_e64 v50, 16, v51\n" \
"v_mfma_f32_16x16x32_bf16 v[28:31], a[228:231], a[140:143], v[28:31]\n" \
"v_and_b32_e64 v51, s43, v51\n" \
"v_mfma_f32_16x16x32_bf16 v[52:55], a[200:203], a[144:147], v[52:55]\n" \
"v_accvgpr_read_b32 v57, a254\n" \
"v_mfma_f32_16x16x32_bf16 v[52:55], a[204:207], a[148:151], v[52:55]\n" \
"v_lshlrev_b32_e64 v56, 16, v57\n" \
"v_mfma_f32_16x16x32_bf16 v[52:55], a[220:223], a[152:155], v[52:55]\n" \
"v_and_b32_e64 v57, s43, v57\n" \
"v_mfma_f32_16x16x32_bf16 v[52:55], a[212:215], a[156:159], v[52:55]\n" \
"v_cvt_pk_bf16_f32 v20, v20, v21\n" \
"v_cvt_pk_bf16_f32 v21, v22, v23\n" \
"v_accvgpr_read_b32 v59, a255\n" \
"v_mfma_f32_16x16x32_bf16 v[60:63], a[208:211], a[144:147], v[60:63]\n" \
"v_lshlrev_b32_e64 v58, 16, v59\n" \
"v_mfma_f32_16x16x32_bf16 v[60:63], a[216:219], a[148:151], v[60:63]\n" \
"v_cvt_pk_bf16_f32 v22, v28, v29\n" \
"v_cvt_pk_bf16_f32 v23, v30, v31\n" \
"v_and_b32_e64 v59, s43, v59\n" \
"v_mfma_f32_16x16x32_bf16 v[60:63], a[224:227], a[152:155], v[60:63]\n" \
"s_add_u32 s55, s46, 12288\n" \
"buffer_store_dwordx4 v[20:23], %[vxs], %[srdz], s55 offen offset:0 sc1\n" \
"v_mfma_f32_16x16x32_bf16 v[60:63], a[228:231], a[156:159], v[60:63]\n" \
"v_mfma_f32_16x16x32_bf16 v[84:87], a[200:203], a[160:163], v[84:87]\n" \
"v_mfma_f32_16x16x32_bf16 v[84:87], a[204:207], a[164:167], v[84:87]\n" \
"v_mfma_f32_16x16x32_bf16 v[84:87], a[220:223], a[168:171], v[84:87]\n" \
"v_mfma_f32_16x16x32_bf16 v[84:87], a[212:215], a[172:175], v[84:87]\n" \
"v_cvt_pk_bf16_f32 v52, v52, v53\n" \
"v_cvt_pk_bf16_f32 v53, v54, v55\n" \
"v_mfma_f32_16x16x32_bf16 v[92:95], a[208:211], a[160:163], v[92:95]\n" \
"v_mfma_f32_16x16x32_bf16 v[92:95], a[216:219], a[164:167], v[92:95]\n" \
"v_cvt_pk_bf16_f32 v54, v60, v61\n" \
"v_cvt_pk_bf16_f32 v55, v62, v63\n" \
"v_mfma_f32_16x16x32_bf16 v[92:95], a[224:227], a[168:171], v[92:95]\n" \
"s_add_u32 s55, s46, 316416\n" \
"buffer_store_dwordx4 v[52:55], %[vxs], %[srdz], s55 offen offset:0 sc1\n" \
"v_mfma_f32_16x16x32_bf16 v[92:95], a[228:231], a[172:175], v[92:95]\n" \
"v_mfma_f32_16x16x32_bf16 v[116:119], a[200:203], a[176:179], v[116:119]\n" \
"v_mfma_f32_16x16x32_bf16 v[116:119], a[204:207], a[180:183], v[116:119]\n" \
"v_mfma_f32_16x16x32_bf16 v[116:119], a[220:223], a[184:187], v[116:119]\n" \
"v_mfma_f32_16x16x32_bf16 v[116:119], a[212:215], a[188:191], v[116:119]\n" \
"v_cvt_pk_bf16_f32 v84, v84, v85\n" \
"v_cvt_pk_bf16_f32 v85, v86, v87\n" \
"v_mfma_f32_16x16x32_bf16 v[124:127], a[208:211], a[176:179], v[124:127]\n" \
"v_mfma_f32_16x16x32_bf16 v[124:127], a[216:219], a[180:183], v[124:127]\n" \
"v_cvt_pk_bf16_f32 v86, v92, v93\n" \
"v_cvt_pk_bf16_f32 v87, v94, v95\n" \
"v_mfma_f32_16x16x32_bf16 v[124:127], a[224:227], a[184:187], v[124:127]\n" \
"s_add_u32 s55, s46, 620544\n" \
"buffer_store_dwordx4 v[84:87], %[vxs], %[srdz], s55 offen offset:0 sc1\n" \
"v_mfma_f32_16x16x32_bf16 v[124:127], a[228:231], a[188:191], v[124:127]\n" \
"v_cvt_pk_bf16_f32 v116, v116, v117\n" \
"v_cvt_pk_bf16_f32 v117, v118, v119\n" \
"s_waitcnt lgkmcnt(0)\n" \
"s_nop 4\n" \
"v_cvt_pk_bf16_f32 v118, v124, v125\n" \
"v_cvt_pk_bf16_f32 v119, v126, v127\n" \
"s_add_u32 s55, s46, 924672\n" \
"buffer_store_dwordx4 v[116:119], %[vxs], %[srdz], s55 offen offset:0 sc1\n" \
"buffer_load_dword v229, v226, %[srdz], s53 offen sc1\n" \
"v_readlane_b32 s62, %[tlo], 35\n" \
"v_lshrrev_b32_e32 v72, 2, v224\n" \
"s_add_u32 s54, %[sin], 8192\n" \
"s_add_u32 s55, %[sin], 12288\n" \
"s_movk_i32 s56, 0x1000\n" \
"s_movk_i32 s57, 0x2000\n" \
"s_movk_i32 s58, 0x3000\n" \
"buffer_load_dwordx4 v[64:67], v72, %[srdz], s62 offen offset:0 sc1\n" \
"buffer_load_dwordx4 v[68:71], v72, %[srdz], s62 offen offset:16 sc1\n" \
"v_mov_b32_e32 v226, v211\n" \
"v_bfe_u32 v221, v222, 3, 1\n" \
"v_and_b32_e32 v208, 1, v222\n" \
"v_lshl_or_b32 v221, v221, 1, v208\n" \
"s_movk_i32 s63, 272\n" \
"v_mad_u32_u24 v221, v221, s63, %[vx0]\n" \
"v_readlane_b32 s64, %[tlo], 25\n" \
"v_readlane_b32 s65, %[thi], 25\n" \
"s_mov_b32 s66, 0x400000\n" \
"s_mov_b32 s67, 0x20000\n" \
"v_readlane_b32 s48, %[tlo], 27\n" \
"v_readlane_b32 s49, %[thi], 27\n" \
"v_readlane_b32 s69, %[tlo], 21\n" \
"v_accvgpr_write_b32 a128, -1\n" \
"v_lshrrev_b32_e32 v214, 4, v224\n" \
"s_add_u32 s62, %[m0b], 65536\n" \
"v_add_u32_e32 v215, %[m0t], v214\n" \
"v_add_u32_e32 v214, s62, v214\n" \
"ds_read_b64_tr_b16 a[64:65], v214 offset:0\n" \
"v_and_b32_e32 v4, 1, v222\n" \
"ds_read_b64_tr_b16 a[66:67], v214 offset:1024\n" \
"v_bfe_u32 v5, v222, 1, 3\n" \
"ds_read_b64_tr_b16 a[96:97], v215 offset:0\n" \
"v_lshlrev_b32_e32 v4, 4, v4\n" \
"ds_read_b64_tr_b16 a[98:99], v215 offset:1024\n" \
"v_mov_b32_e32 v6, 0x3f80\n" \
"ds_read_b64_tr_b16 a[68:69], v214 offset:512\n" \
"v_lshlrev_b32_e64 v5, v5, 1\n" \
"ds_read_b64_tr_b16 a[70:71], v214 offset:1536\n" \
"v_lshlrev_b32_e32 v4, v4, v6\n" \
"ds_read_b64_tr_b16 a[100:101], v215 offset:512\n" \
"v_bfe_i32 v230, v5, 0, 1\n" \
"ds_read_b64_tr_b16 a[102:103], v215 offset:1536\n" \
"v_bfe_i32 v231, v5, 1, 1\n" \
"ds_read_b64_tr_b16 a[72:73], v214 offset:16384\n" \
"v_bfe_i32 v232, v5, 2, 1\n" \
"ds_read_b64_tr_b16 a[74:75], v214 offset:17408\n" \
"v_bfe_i32 v233, v5, 3, 1\n" \
"ds_read_b64_tr_b16 a[104:105], v215 offset:4096\n" \
"v_bfe_i32 v234, v5, 4, 1\n" \
"ds_read_b64_tr_b16 a[106:107], v215 offset:5120\n" \
"v_bfe_i32 v235, v5, 5, 1\n" \
"ds_read_b64_tr_b16 a[76:77], v214 offset:16896\n" \
"v_bfe_i32 v236, v5, 6, 1\n" \
"ds_read_b64_tr_b16 a[78:79], v214 offset:17920\n" \
"v_bfe_i32 v237, v5, 7, 1\n" \
"ds_read_b64_tr_b16 a[108:109], v215 offset:4608\n" \
"v_and_b32_e32 v230, v4, v230\n" \
".if . - adaln_body != 61576\n" \
".error \"size model\"\n" \
".endif\n" \
"Lk14_%=:\n" \
"s_cbranch_execz Lk15_%=\n" \
"s_nop 0\n" \
"ds_read_b64_tr_b16 a[110:111], v215 offset:5632\n" \
"v_and_b32_e32 v231, v4, v231\n" \
"ds_read_b64_tr_b16 a[80:81], v214 offset:2048\n" \
"v_and_b32_e32 v232, v4, v232\n" \
"ds_read_b64_tr_b16 a[82:83], v214 offset:3072\n" \
"v_and_b32_e32 v233, v4, v233\n" \
"ds_read_b64_tr_b16 a[112:113], v215 offset:2048\n" \
"v_and_b32_e32 v234, v4, v234\n" \
"ds_read_b64_tr_b16 a[114:115], v215 offset:3072\n" \
"v_and_b32_e32 v235, v4, v235\n" \
"ds_read_b64_tr_b16 a[84:85], v214 offset:2560\n" \
"v_and_b32_e32 v236, v4, v236\n" \
"ds_read_b64_tr_b16 a[86:87], v214 offset:3584\n" \
"v_and_b32_e32 v237, v4, v237\n" \
"ds_read_b64_tr_b16 a[116:117], v215 offset:2560\n" \
"ds_read_b64_tr_b16 a[118:119], v215 offset:3584\n" \
"ds_read_b64_tr_b16 a[88:89], v214 offset:18432\n" \
"ds_read_b64_tr_b16 a[90:91], v214 offset:19456\n" \
"ds_read_b64_tr_b16 a[120:121], v215 offset:6144\n" \
"ds_read_b64_tr_b16 a[122:123], v215 offset:7168\n" \
"ds_read_b64_tr_b16 a[92:93], v214 offset:18944\n" \
"ds_read_b64_tr_b16 a[94:95], v214 offset:19968\n" \
"ds_read_b64_tr_b16 a[124:125], v215 offset:6656\n" \
"ds_read_b64_tr_b16 a[126:127], v215 offset:7680\n" \
"v_lshrrev_b32_e32 v208, 1, v217\n" \
"v_and_b32_e32 v210, 24, v208\n" \
"v_bfe_u32 v215, v222, 4, 1\n" \
"v_lshl_or_b32 v210, v215, 14, v210\n" \
"v_lshl_or_b32 v210, v215, 7, v210\n" \
"v_bfe_u32 v215, v222, 2, 2\n" \
"v_lshl_or_b32 v210, v215, 9, v210\n" \
"v_lshrrev_b32_e32 v211, 5, v222\n" \
"v_xor_b32_e32 v215, v215, v211\n" \
"v_lshl_or_b32 v210, v215, 5, v210\n" \
"s_add_u32 s62, %[m0b], 98304\n" \
"v_add_u32_e32 v210, s62, v210\n" \
"v_xor_b32_e32 v211, 64, v210\n" \
"v_xor_b32_e32 v213, 0x80, v210\n" \
"v_xor_b32_e32 v214, 0xc0, v210\n" \
"v_readlane_b32 s62, %[thi], 28\n" \
"v_and_b32_e32 v100, 7, v222\n" \
"v_lshrrev_b32_e32 v103, 1, v222\n" \
"v_and_b32_e32 v101, 1, v222\n" \
"v_lshlrev_b32_e32 v103, 13, v103\n" \
"v_lshl_or_b32 v103, v101, 4, v103\n" \
"v_add_u32_e32 v100, s62, v100\n" \
"v_accvgpr_write_b32 a34, v103\n" \
"v_lshrrev_b32_e32 v101, 8, v100\n" \
"v_lshlrev_b32_e32 v101, 7, v101\n" \
"v_bfe_u32 v103, v100, 6, 2\n" \
"v_lshl_or_b32 v101, v103, 5, v101\n" \
"v_bfe_u32 v103, v100, 1, 1\n" \
"v_lshl_or_b32 v101, v103, 4, v101\n" \
"v_bfe_u32 v103, v100, 2, 4\n" \
"v_or_b32_e32 v101, v103, v101\n" \
"v_lshrrev_b32_e32 v102, 8, v101\n" \
"v_lshlrev_b32_e32 v102, 9, v102\n" \
"v_accvgpr_write_b32 a32, v101\n" \
"v_bfe_u32 v103, v101, 6, 1\n" \
"v_lshl_or_b32 v102, v103, 8, v102\n" \
"v_bfe_u32 v103, v101, 7, 1\n" \
"v_lshl_or_b32 v102, v103, 7, v102\n" \
"v_bfe_u32 v103, v101, 4, 1\n" \
"v_lshl_or_b32 v102, v103, 6, v102\n" \
"v_bfe_u32 v103, v101, 2, 2\n" \
"v_lshl_or_b32 v102, v103, 4, v102\n" \
"v_bfe_u32 v103, v101, 5, 1\n" \
"v_lshl_or_b32 v102, v103, 3, v102\n" \
"v_bfe_u32 v103, v101, 0, 2\n" \
"v_lshl_or_b32 v102, v103, 1, v102\n" \
"v_accvgpr_write_b32 a33, v102\n" \
"s_mov_b32 s51, 0x10000\n" \
"s_mov_b32 s50, 0\n" \
"s_waitcnt vmcnt(7)\n" \
"v_cmp_eq_u32_e32 vcc, 0, v216\n" \
"s_mov_b32 %[got], 0\n" \
"s_cbranch_vccnz TLf1_%=\n" \
"s_mov_b32 %[got], 1\n" \
"buffer_load_dwordx4 a[196:199], v217, %[srdz], %[sin] offen offset:0 sc1\n" \
"buffer_load_dwordx4 a[200:203], v217, %[srdz], %[sin] offen offset:1024 sc1\n" \
"buffer_load_dwordx4 a[204:207], v217, %[srdz], %[sin] offen offset:2048 sc1\n" \
"buffer_load_dwordx4 a[208:211], v217, %[srdz], %[sin] offen offset:3072 sc1\n" \
"buffer_load_dwordx4 a[212:215], v217, %[srdz], s68 offen offset:0 sc1\n" \
"buffer_load_dwordx4 a[216:219], v217, %[srdz], s68 offen offset:1024 sc1\n" \
"buffer_load_dwordx4 a[220:223], v217, %[srdz], s68 offen offset:2048 sc1\n" \
"buffer_load_dwordx4 a[224:227], v217, %[srdz], s68 offen offset:3072 sc1\n" \
"buffer_load_dwordx4 a[164:167], v217, %[srdz], s54 offen offset:0 sc1\n" \
"buffer_load_dwordx4 a[168:171], v217, %[srdz], s54 offen offset:1024 sc1\n" \
"buffer_load_dwordx4 a[172:175], v217, %[srdz], s54 offen offset:2048 sc1\n" \
"buffer_load_dwordx4 a[176:179], v217, %[srdz], s54 offen offset:3072 sc1\n" \
"TLf1_%=:\n" \
"ds_read_b64_tr_b16 a[0:1], v210 offset:0\n" \
"ds_read_b64_tr_b16 a[2:3], v213 offset:2048\n" \
"ds_read_b64_tr_b16 a[4:5], v211 offset:0\n" \
"ds_read_b64_tr_b16 a[6:7], v214 offset:2048\n" \
"ds_read_b64_tr_b16 a[8:9], v213 offset:0\n" \
"ds_read_b64_tr_b16 a[10:11], v210 offset:2048\n" \
"ds_read_b64_tr_b16 a[12:13], v214 offset:0\n" \
"ds_read_b64_tr_b16 a[14:15], v211 offset:2048\n" \
"ds_read_b64_tr_b16 a[16:17], v210 offset:256\n" \
"ds_read_b64_tr_b16 a[18:19], v213 offset:2304\n" \
"ds_read_b64_tr_b16 a[20:21], v211 offset:256\n" \
"ds_read_b64_tr_b16 a[22:23], v214 offset:2304\n" \
"ds_read_b64_tr_b16 a[24:25], v213 offset:256\n" \
"ds_read_b64_tr_b16 a[26:27], v210 offset:2304\n" \
"ds_read_b64_tr_b16 a[28:29], v214 offset:256\n" \
"ds_read_b64_tr_b16 a[30:31], v211 offset:2304\n" \
"s_waitcnt vmcnt(12)\n" \
"s_cmp_lg_u32 %[got], 0\n" \
"s_cbranch_scc1 TLrp4_%=\n" \
"s_waitcnt vmcnt(0)\n" \
"v_and_b32_e32 v208, 32, v229\n" \
"v_cmp_eq_u32_e32 vcc, 0, v208\n" \
"s_cbranch_vccz TLrl2_%=\n" \
"v_mov_b32_e32 v216, s40\n" \
"TLrq2_%=:\n" \
"buffer_load_dword v225, off, %[srdz], s53 offset:0 sc1\n" \
"s_mov_b32 s52, 10\n" \
"TLrs2_%=:\n" \
"ds_read_b32 v229, v216 offset:14336\n" \
"s_waitcnt lgkmcnt(0)\n" \
"v_and_b32_e32 v208, 32, v229\n" \
"v_cmp_eq_u32_e32 vcc, 0, v208\n" \
"s_cbranch_vccz TLrd2_%=\n" \
"s_sub_u32 s52, s52, 1\n" \
"s_cbranch_scc0 TLrs2_%=\n" \
"s_sub_u32 s51, s51, 1\n" \
"s_cselect_b32 s51, 0, s51\n" \
"s_waitcnt vmcnt(0)\n" \
"v_and_b32_e32 v208, 32, v225\n" \
"v_cmp_eq_u32_e32 vcc, 0, v208\n" \
"s_cbranch_scc1 TLrl2_%=\n" \
"s_cbranch_vccnz TLrq2_%=\n" \
"v_mov_b32_e32 v229, v225\n" \
"s_mov_b64 exec, 1\n" \
"ds_max_u32 v216, v225 offset:14336\n" \
"s_mov_b64 exec, -1\n" \
"TLrd2_%=:\n" \
"s_sleep 0\n" \
"TLrl2_%=:\n" \
"buffer_load_dwordx4 a[196:199], v217, %[srdz], %[sin] offen offset:0 sc1\n" \
"buffer_load_dwordx4 a[200:203], v217, %[srdz], %[sin] offen offset:1024 sc1\n" \
"buffer_load_dwordx4 a[204:207], v217, %[srdz], %[sin] offen offset:2048 sc1\n" \
"buffer_load_dwordx4 a[208:211], v217, %[srdz], %[sin] offen offset:3072 sc1\n" \
"buffer_load_dwordx4 a[212:215], v217, %[srdz], s68 offen offset:0 sc1\n" \
"buffer_load_dwordx4 a[216:219], v217, %[srdz], s68 offen offset:1024 sc1\n" \
"buffer_load_dwordx4 a[220:223], v217, %[srdz], s68 offen offset:2048 sc1\n" \
"buffer_load_dwordx4 a[224:227], v217, %[srdz], s68 offen offset:3072 sc1\n" \
"v_and_b32_e32 v44, 0x800, v229\n" \
"v_cmp_eq_u32_e32 vcc, 0, v44\n" \
"s_cbranch_vccnz TLrn_%=\n" \
"buffer_load_dwordx4 a[164:167], v217, %[srdz], s54 offen offset:0 sc1\n" \
"buffer_load_dwordx4 a[168:171], v217, %[srdz], s54 offen offset:1024 sc1\n" \
"buffer_load_dwordx4 a[172:175], v217, %[srdz], s54 offen offset:2048 sc1\n" \
"buffer_load_dwordx4 a[176:179], v217, %[srdz], s54 offen offset:3072 sc1\n" \
"s_branch TLrp5_%=\n" \
"TLrp4_%=:\n" \
"v_and_b32_e32 v44, 0x800, v229\n" \
"v_cmp_eq_u32_e32 vcc, 0, v44\n" \
"s_cbranch_vccnz TLrn_%=\n" \
"TLrp5_%=:\n" \
"s_mov_b32 s50, 1\n" \
"buffer_load_dwordx4 a[180:183], v217, %[srdz], s55 offen offset:0 sc1\n" \
"buffer_load_dwordx4 a[184:187], v217, %[srdz], s55 offen offset:1024 sc1\n" \
"buffer_load_dwordx4 a[188:191], v217, %[srdz], s55 offen offset:2048 sc1\n" \
"buffer_load_dwordx4 a[192:195], v217, %[srdz], s55 offen offset:3072 sc1\n" \
"TLrn_%=:\n" \
"v_min3_u32 v208, v64, v65, v66\n" \
"v_min3_u32 v209, v67, v68, v69\n" \
"v_min3_u32 v208, v208, v70, v71\n" \
"v_min_u32_e32 v208, v208, v209\n" \
"v_cmp_eq_u32_e32 vcc, 0, v208\n" \
"s_cbranch_vccnz TLpk_%=\n" \
"TLpkb_%=:\n" \
"v_min_u32_e32 v208, v65, v67\n" \
"v_min_u32_e32 v209, v69, v71\n" \
"v_max_u32_e32 v65, v65, v67\n" \
"v_max_u32_e32 v69, v69, v71\n" \
"v_max3_u32 v64, v208, v64, v66\n" \
"v_max3_u32 v68, v209, v68, v70\n" \
"v_min_u32_e32 v208, v65, v69\n" \
"v_max_u32_e32 v65, v65, v69\n" \
"v_max3_u32 v64, v208, v64, v68\n" \
"v_mov_b64_e32 v[68:69], v[64:65]\n" \
"v_min_u32_dpp v208, v65, v65 quad_perm:[1,0,3,2] row_mask:0xf bank_mask:0xf\n" \
"v_max_u32_dpp v209, v64, v64 quad_perm:[1,0,3,2] row_mask:0xf bank_mask:0xf\n" \
"v_max_u32_dpp v65, v65, v65 quad_perm:[1,0,3,2] row_mask:0xf bank_mask:0xf\n" \
"v_max_u32_e32 v64, v208, v209\n" \
"s_nop 0\n" \
"v_min_u32_dpp v208, v65, v65 quad_perm:[2,3,0,1] row_mask:0xf bank_mask:0xf\n" \
"v_max_u32_dpp v209, v64, v64 quad_perm:[2,3,0,1] row_mask:0xf bank_mask:0xf\n" \
"v_max_u32_dpp v65, v65, v65 quad_perm:[2,3,0,1] row_mask:0xf bank_mask:0xf\n" \
"v_max_u32_e32 v64, v208, v209\n" \
"s_nop 0\n" \
"v_min_u32_dpp v208, v65, v65 row_half_mirror row_mask:0xf bank_mask:0xf\n" \
"v_max_u32_dpp v209, v64, v64 row_half_mirror row_mask:0xf bank_mask:0xf\n" \
"v_max_u32_dpp v65, v65, v65 row_half_mirror row_mask:0xf bank_mask:0xf\n" \
"v_max_u32_e32 v64, v208, v209\n" \
"s_nop 0\n" \
"v_min_u32_dpp v208, v65, v65 row_mirror row_mask:0xf bank_mask:0xf\n" \
"v_max_u32_dpp v209, v64, v64 row_mirror row_mask:0xf bank_mask:0xf\n" \
"v_max_u32_dpp v65, v65, v65 row_mirror row_mask:0xf bank_mask:0xf\n" \
"v_max_u32_e32 v64, v208, v209\n" \
"v_mov_b32_e32 v67, v65\n" \
"v_mov_b32_e32 v66, v64\n" \
"s_nop 0\n" \
"v_permlane16_swap_b32_e32 v65, v67\n" \
"v_permlane16_swap_b32_e32 v64, v66\n" \
"v_min_u32_e32 v208, v65, v67\n" \
"v_max_u32_e32 v65, v65, v67\n" \
"v_max3_u32 v64, v208, v64, v66\n" \
"v_mov_b32_e32 v67, v65\n" \
"v_mov_b32_e32 v66, v64\n" \
"s_nop 0\n" \
"v_permlane32_swap_b32_e32 v65, v67\n" \
"v_permlane32_swap_b32_e32 v64, v66\n" \
"v_min_u32_e32 v208, v65, v67\n" \
"v_max_u32_e32 v65, v65, v67\n" \
"v_max3_u32 v64, v208, v64, v66\n" \
"v_readfirstlane_b32 s44, v65\n" \
"v_readfirstlane_b32 s45, v64\n" \
"v_cmp_ge_u32_e32 vcc, v68, v64\n" \
"s_nop 1\n" \
"v_cndmask_b32_e64 v68, v68, 0, vcc\n" \
"v_cmp_ge_u32_e32 vcc, v69, v64\n" \
"s_nop 1\n" \
"v_cndmask_b32_e32 v69, v69, v68, vcc\n" \
"v_cndmask_b32_e64 v68, v68, 0, vcc\n" \
"s_nop 0\n" \
"v_min_u32_dpp v208, v69, v69 quad_perm:[1,0,3,2] row_mask:0xf bank_mask:0xf\n" \
"v_max_u32_dpp v209, v68, v68 quad_perm:[1,0,3,2] row_mask:0xf bank_mask:0xf\n" \
"v_max_u32_dpp v69, v69, v69 quad_perm:[1,0,3,2] row_mask:0xf bank_mask:0xf\n" \
"v_max_u32_e32 v68, v208, v209\n" \
"s_nop 0\n" \
"v_min_u32_dpp v208, v69, v69 quad_perm:[2,3,0,1] row_mask:0xf bank_mask:0xf\n" \
"v_max_u32_dpp v209, v68, v68 quad_perm:[2,3,0,1] row_mask:0xf bank_mask:0xf\n" \
"v_max_u32_dpp v69, v69, v69 quad_perm:[2,3,0,1] row_mask:0xf bank_mask:0xf\n" \
"v_max_u32_e32 v68, v208, v209\n" \
"s_nop 0\n" \
"v_min_u32_dpp v208, v69, v69 row_half_mirror row_mask:0xf bank_mask:0xf\n" \
"v_max_u32_dpp v209, v68, v68 row_half_mirror row_mask:0xf bank_mask:0xf\n" \
"v_max_u32_dpp v69, v69, v69 row_half_mirror row_mask:0xf bank_mask:0xf\n" \
"v_max_u32_e32 v68, v208, v209\n" \
"s_nop 0\n" \
"v_min_u32_dpp v208, v69, v69 row_mirror row_mask:0xf bank_mask:0xf\n" \
"v_max_u32_dpp v209, v68, v68 row_mirror row_mask:0xf bank_mask:0xf\n" \
"v_max_u32_dpp v69, v69, v69 row_mirror row_mask:0xf bank_mask:0xf\n" \
"v_max_u32_e32 v68, v208, v209\n" \
"v_mov_b32_e32 v67, v69\n" \
"v_mov_b32_e32 v66, v68\n" \
"s_nop 0\n" \
"v_permlane16_swap_b32_e32 v69, v67\n" \
"v_permlane16_swap_b32_e32 v68, v66\n" \
"v_min_u32_e32 v208, v69, v67\n" \
"v_max_u32_e32 v69, v69, v67\n" \
"v_max3_u32 v68, v208, v68, v66\n" \
"v_mov_b32_e32 v67, v69\n" \
"v_mov_b32_e32 v66, v68\n" \
"s_nop 0\n" \
"v_permlane32_swap_b32_e32 v69, v67\n" \
"v_permlane32_swap_b32_e32 v68, v66\n" \
"v_min_u32_e32 v208, v69, v67\n" \
"v_max_u32_e32 v69, v69, v67\n" \
"v_max3_u32 v68, v208, v68, v66\n" \
"v_readfirstlane_b32 s46, v69\n" \
"v_readfirstlane_b32 s47, v68\n" \
"s_andn2_b32 s44, 0x7ff, s44\n" \
"s_andn2_b32 s45, 0x7ff, s45\n" \
"s_andn2_b32 s46, 0x7ff, s46\n" \
"s_andn2_b32 s47, 0x7ff, s47\n" \
"v_and_b32_e32 v213, 4, v222\n" \
"v_mov_b32_e32 v208, s44\n" \
"v_cmp_ne_u32_e32 vcc, 0, v213\n" \
"v_mov_b32_e32 v214, s45\n" \
"v_mov_b32_e32 v209, s46\n" \
"v_mov_b32_e32 v215, s47\n" \
"v_cndmask_b32_e32 v208, v208, v214, vcc\n" \
"v_cndmask_b32_e32 v209, v209, v215, vcc\n" \
"v_cmp_lt_u32_e32 vcc, 31, v222\n" \
"v_and_b32_e32 v211, 0xf0, %[kb0]\n" \
"s_lshl_b32 s62, %[wid], 9\n" \
"v_cndmask_b32_e32 v210, v208, v209, vcc\n" \
"v_lshrrev_b32_e32 %[vdn], 6, v210\n" \
"v_lshlrev_b32_e32 %[vdn], 9, %[vdn]\n" \
"v_lshlrev_b32_e32 v213, 11, v210\n" \
"v_lshrrev_b32_e32 v214, 1, v210\n" \
"v_and_b32_e32 v214, 0x80, v214\n" \
"v_lshrrev_b32_e32 v215, 1, v211\n" \
"v_and_b32_e32 v215, 56, v215\n" \
"v_add3_u32 v213, v213, v214, v215\n" \
"v_and_b32_e32 v215, 0x80, v211\n" \
"v_lshlrev_b32_e32 v215, 1, v215\n" \
"v_add3_u32 v80, v213, v215, s62\n" \
"v_xor_b32_e32 v81, 0x80, v80\n" \
"s_mov_b32 s63, 278528\n" \
"buffer_load_dword %[vxs], %[vdn], %[srdz], s63 offen offset:0 sc1\n" \
"global_load_dwordx2 v[88:89], v80, %[px] offset:0\n" \
"global_load_dwordx2 v[90:91], v80, %[px] offset:64\n" \
"global_load_dwordx2 v[92:93], v81, %[px] offset:0\n" \
"global_load_dwordx2 v[94:95], v81, %[px] offset:64\n" \
"v_and_b32_e32 v213, 0x100, v210\n" \
"s_add_u32 s63, s62, s40\n" \
"v_add3_u32 v213, v213, v211, s63\n" \
"v_xor_b32_e32 v214, 0x100, v213\n" \
"ds_read_b128 v[104:107], v213 offset:2048\n" \
"ds_read_b128 v[108:111], v214 offset:2048\n" \
"ds_read_b128 v[64:67], v213 offset:6144\n" \
"ds_read_b128 v[68:71], v213 offset:8192\n" \
"ds_read_b128 v[72:75], v214 offset:6144\n" \
"ds_read_b128 v[76:79], v214 offset:8192\n" \
"ds_read_b128 v[112:115], v213 offset:10240\n" \
"ds_read_b128 v[116:119], v213 offset:12288\n" \
"ds_read_b128 v[120:123], v214 offset:10240\n" \
"ds_read_b128 v[124:127], v214 offset:12288\n" \
"s_movk_i32 s63, 2112\n" \
"v_mad_u32_u24 %[oq0], v219, s63, v213\n" \
"v_mad_u32_u24 %[wo0], v219, s63, v214\n" \
"s_lshl_b32 s62, %[wid], 10\n" \
"s_add_u32 s62, s62, 7340032\n" \
"v_and_b32_e32 v215, 0x3b0, v217\n" \
"v_add_u32_e32 v215, s62, v215\n" \
"s_mov_b32 s63, 38016\n" \
"v_lshrrev_b32_e32 %[vct], 1, v208\n" \
"v_and_b32_e32 v208, 1, v208\n" \
"v_mad_u32_u24 %[vct], %[vct], s63, v215\n" \
"v_lshl_add_u32 %[vct], v208, 6, %[vct]\n" \
"v_lshrrev_b32_e32 %[rd0], 1, v209\n" \
"v_and_b32_e32 v209, 1, v209\n" \
"v_mad_u32_u24 %[rd0], %[rd0], s63, v215\n" \
"v_lshl_add_u32 %[rd0], v209, 6, %[rd0]\n" \
"s_waitcnt vmcnt(13) lgkmcnt(0)\n" \
"s_cmp_lg_u32 s50, 0\n" \
"s_cbranch_scc1 TLrw_%=\n" \
"s_waitcnt vmcnt(5)\n" \
"TLrw_%=:\n" \
"v_mfma_f32_16x16x32_bf16 v[208:211], v[230:233], a[196:199], 0\n" \
"v_mfma_f32_16x16x32_bf16 v[212:215], v[230:233], a[204:207], 0\n" \
"v_mfma_f32_16x16x32_bf16 v[208:211], v[234:237], a[200:203], v[208:211]\n" \
"v_mfma_f32_16x16x32_bf16 v[212:215], v[234:237], a[208:211], v[212:215]\n" \
"v_lshlrev_b32_e32 v96, 16, v104\n" \
"v_and_b32_e32 v97, s43, v104\n" \
"v_lshlrev_b32_e32 v98, 16, v105\n" \
"v_and_b32_e32 v99, s43, v105\n" \
"v_lshlrev_b32_e32 v100, 16, v106\n" \
"v_and_b32_e32 v101, s43, v106\n" \
"v_lshlrev_b32_e32 v102, 16, v107\n" \
"v_and_b32_e32 v103, s43, v107\n" \
"v_pk_fma_f32 v[0:1], v[208:209], v[128:129], v[0:1]\n" \
"v_pk_fma_f32 v[2:3], v[210:211], v[130:131], v[2:3]\n" \
"v_pk_fma_f32 v[8:9], v[212:213], v[136:137], v[8:9]\n" \
"v_pk_fma_f32 v[10:11], v[214:215], v[138:139], v[10:11]\n" \
"v_pk_mul_f32 v[6:7], v[0:1], v[160:161]\n" \
"v_pk_mul_f32 v[12:13], v[0:1], v[0:1]\n" \
"v_pk_add_f32 v[4:5], v[0:1], v[2:3]\n" \
"v_pk_fma_f32 v[6:7], v[2:3], v[162:163], v[6:7]\n" \
"v_pk_fma_f32 v[12:13], v[2:3], v[2:3], v[12:13]\n" \
"v_pk_add_f32 v[4:5], v[4:5], v[8:9]\n" \
"v_pk_fma_f32 v[6:7], v[8:9], v[168:169], v[6:7]\n" \
"v_pk_fma_f32 v[12:13], v[8:9], v[8:9], v[12:13]\n" \
"v_pk_add_f32 v[4:5], v[4:5], v[10:11]\n" \
"v_pk_fma_f32 v[6:7], v[10:11], v[170:171], v[6:7]\n" \
"v_pk_fma_f32 v[12:13], v[10:11], v[10:11], v[12:13]\n" \
"v_mfma_f32_16x16x32_bf16 v[208:211], v[230:233], a[212:215], 0\n" \
"v_mfma_f32_16x16x32_bf16 v[212:215], v[230:233], a[220:223], 0\n" \
"v_mfma_f32_16x16x32_bf16 v[208:211], v[234:237], a[216:219], v[208:211]\n" \
"v_mfma_f32_16x16x32_bf16 v[212:215], v[234:237], a[224:227], v[212:215]\n" \
"v_lshlrev_b32_e32 v104, 16, v108\n" \
"v_and_b32_e32 v105, s43, v108\n" \
"v_lshlrev_b32_e32 v106, 16, v109\n" \
"v_and_b32_e32 v107, s43, v109\n" \
"v_lshlrev_b32_e32 v108, 16, v110\n" \
"v_and_b32_e32 v109, s43, v110\n" \
"v_lshlrev_b32_e32 v110, 16, v111\n" \
"v_and_b32_e32 v111, s43, v111\n" \
"v_pk_fma_f32 v[16:17], v[208:209], v[132:133], v[16:17]\n" \
"v_pk_fma_f32 v[18:19], v[210:211], v[134:135], v[18:19]\n" \
"v_pk_fma_f32 v[24:25], v[212:213], v[140:141], v[24:25]\n" \
"v_pk_fma_f32 v[26:27], v[214:215], v[142:143], v[26:27]\n" \
"v_pk_add_f32 v[4:5], v[4:5], v[16:17]\n" \
"v_pk_fma_f32 v[6:7], v[16:17], v[164:165], v[6:7]\n" \
"v_pk_fma_f32 v[12:13], v[16:17], v[16:17], v[12:13]\n" \
"v_pk_add_f32 v[4:5], v[4:5], v[18:19]\n" \
"v_pk_fma_f32 v[6:7], v[18:19], v[166:167], v[6:7]\n" \
"v_pk_fma_f32 v[12:13], v[18:19], v[18:19], v[12:13]\n" \
"v_pk_add_f32 v[4:5], v[4:5], v[24:25]\n" \
"v_pk_fma_f32 v[6:7], v[24:25], v[172:173], v[6:7]\n" \
"v_pk_fma_f32 v[12:13], v[24:25], v[24:25], v[12:13]\n" \
"v_pk_add_f32 v[4:5], v[4:5], v[26:27]\n" \
"v_pk_fma_f32 v[6:7], v[26:27], v[174:175], v[6:7]\n" \
"v_pk_fma_f32 v[12:13], v[26:27], v[26:27], v[12:13]\n" \
"s_cmp_lg_u32 s50, 0\n" \
"s_cbranch_scc1 TLre_%=\n" \
"v_and_b32_e32 v208, 0x800, v229\n" \
"v_cmp_eq_u32_e32 vcc, 0, v208\n" \
"s_cbranch_vccz TLrl4_%=\n" \
"v_mov_b32_e32 v216, s40\n" \
"TLrq4_%=:\n" \
"buffer_load_dword v225, off, %[srdz], s53 offset:0 sc1\n" \
"s_mov_b32 s52, 10\n" \
"TLrs4_%=:\n" \
"ds_read_b32 v229, v216 offset:14336\n" \
"s_waitcnt lgkmcnt(0)\n" \
"v_and_b32_e32 v208, 0x800, v229\n" \
"v_cmp_eq_u32_e32 vcc, 0, v208\n" \
"s_cbranch_vccz TLrd4_%=\n" \
"s_sub_u32 s52, s52, 1\n" \
"s_cbranch_scc0 TLrs4_%=\n" \
"s_sub_u32 s51, s51, 1\n" \
"s_cselect_b32 s51, 0, s51\n" \
"s_waitcnt vmcnt(0)\n" \
"v_and_b32_e32 v208, 0x800, v225\n" \
"v_cmp_eq_u32_e32 vcc, 0, v208\n" \
"s_cbranch_scc1 TLrl4_%=\n" \
"s_cbranch_vccnz TLrq4_%=\n" \
"v_mov_b32_e32 v229, v225\n" \
"s_mov_b64 exec, 1\n" \
"ds_max_u32 v216, v225 offset:14336\n" \
"s_mov_b64 exec, -1\n" \
"TLrd4_%=:\n" \
"s_sleep 4\n" \
"TLrl4_%=:\n" \
"s_mov_b32 s63, 278528\n" \
"s_nop 0\n" \
"buffer_load_dword %[vxs], %[vdn], %[srdz], s63 offen offset:0 sc1\n" \
"buffer_load_dwordx4 a[164:167], v217, %[srdz], s54 offen offset:0 sc1\n" \
"buffer_load_dwordx4 a[168:171], v217, %[srdz], s54 offen offset:1024 sc1\n" \
"buffer_load_dwordx4 a[172:175], v217, %[srdz], s54 offen offset:2048 sc1\n" \
"buffer_load_dwordx4 a[176:179], v217, %[srdz], s54 offen offset:3072 sc1\n" \
"buffer_load_dwordx4 a[180:183], v217, %[srdz], s55 offen offset:0 sc1\n" \
"buffer_load_dwordx4 a[184:187], v217, %[srdz], s55 offen offset:1024 sc1\n" \
"buffer_load_dwordx4 a[188:191], v217, %[srdz], s55 offen offset:2048 sc1\n" \
"buffer_load_dwordx4 a[192:195], v217, %[srdz], s55 offen offset:3072 sc1\n" \
"s_waitcnt vmcnt(4)\n" \
"s_branch TLrf_%=\n" \
"TLre_%=:\n" \
"s_waitcnt vmcnt(9)\n" \
"TLrf_%=:\n" \
"v_mfma_f32_16x16x32_bf16 v[208:211], v[230:233], a[164:167], 0\n" \
"v_mfma_f32_16x16x32_bf16 v[212:215], v[230:233], a[172:175], 0\n" \
"v_mfma_f32_16x16x32_bf16 v[208:211], v[234:237], a[168:171], v[208:211]\n" \
"v_mfma_f32_16x16x32_bf16 v[212:215], v[234:237], a[176:179], v[212:215]\n" \
"s_lshl_b32 s62, %[wid], 3\n" \
"v_and_b32_e32 v216, 4, v222\n" \
"s_add_u32 s62, s62, s40\n" \
"v_lshlrev_b32_e32 v229, 4, v224\n" \
"v_add_u32_e32 v216, s62, v216\n" \
"s_movk_i32 s52, 0x1000\n" \
"s_nop 0\n" \
"v_pk_fma_f32 v[32:33], v[208:209], v[144:145], v[32:33]\n" \
"v_pk_fma_f32 v[34:35], v[210:211], v[146:147], v[34:35]\n" \
"v_pk_fma_f32 v[40:41], v[212:213], v[152:153], v[40:41]\n" \
"v_pk_fma_f32 v[42:43], v[214:215], v[154:155], v[42:43]\n" \
"v_pk_mul_f32 v[36:37], v[32:33], v[176:177]\n" \
"v_pk_mul_f32 v[38:39], v[32:33], v[32:33]\n" \
"v_pk_add_f32 v[14:15], v[32:33], v[34:35]\n" \
"v_pk_fma_f32 v[36:37], v[34:35], v[178:179], v[36:37]\n" \
"v_pk_fma_f32 v[38:39], v[34:35], v[34:35], v[38:39]\n" \
"v_pk_add_f32 v[14:15], v[14:15], v[40:41]\n" \
"v_pk_fma_f32 v[36:37], v[40:41], v[184:185], v[36:37]\n" \
"v_pk_fma_f32 v[38:39], v[40:41], v[40:41], v[38:39]\n" \
"v_pk_add_f32 v[14:15], v[14:15], v[42:43]\n" \
"v_pk_fma_f32 v[36:37], v[42:43], v[186:187], v[36:37]\n" \
"v_pk_fma_f32 v[38:39], v[42:43], v[42:43], v[38:39]\n" \
"s_waitcnt vmcnt(0)\n" \
"v_and_b32_e32 v225, 7, v222\n" \
"v_lshl_add_u32 v225, v225, 2, s40\n" \
"v_and_b32_e32 v44, 0x820, %[vxs]\n" \
"v_cmp_ne_u32_e32 vcc, 0x820, v44\n" \
"s_mov_b32 s59, 0\n" \
"s_cbranch_vccnz TLcn_%=\n" \
"s_mov_b32 s59, 1\n" \
"buffer_load_dwordx4 a[128:131], %[vct], %[srdz], 0 offen offset:0 sc1\n" \
"buffer_load_dwordx4 a[132:135], %[rd0], %[srdz], 0 offen offset:0 sc1\n" \
"buffer_load_dwordx4 a[136:139], %[vct], %[srdz], s56 offen offset:0 sc1\n" \
"buffer_load_dwordx4 a[140:143], %[rd0], %[srdz], s56 offen offset:0 sc1\n" \
"buffer_load_dwordx4 a[144:147], %[vct], %[srdz], s57 offen offset:0 sc1\n" \
"buffer_load_dwordx4 a[148:151], %[rd0], %[srdz], s57 offen offset:0 sc1\n" \
"buffer_load_dwordx4 a[152:155], %[vct], %[srdz], s58 offen offset:0 sc1\n" \
"buffer_load_dwordx4 a[156:159], %[rd0], %[srdz], s58 offen offset:0 sc1\n" \
"TLcn_%=:\n" \
"v_mfma_f32_16x16x32_bf16 v[208:211], v[230:233], a[180:183], 0\n" \
"v_mfma_f32_16x16x32_bf16 v[212:215], v[230:233], a[188:191], 0\n" \
"v_mfma_f32_16x16x32_bf16 v[208:211], v[234:237], a[184:187], v[208:211]\n" \
"v_mfma_f32_16x16x32_bf16 v[212:215], v[234:237], a[192:195], v[212:215]\n" \
"v_lshlrev_b32_e32 v80, 16, v88\n" \
"v_and_b32_e32 v81, s43, v88\n" \
"v_lshlrev_b32_e32 v82, 16, v89\n" \
"v_and_b32_e32 v83, s43, v89\n" \
"v_lshlrev_b32_e32 v84, 16, v90\n" \
"v_and_b32_e32 v85, s43, v90\n" \
"v_lshlrev_b32_e32 v86, 16, v91\n" \
"v_pk_fma_f32 v[48:49], v[208:209], v[148:149], v[48:49]\n" \
"v_pk_fma_f32 v[50:51], v[210:211], v[150:151], v[50:51]\n" \
"v_pk_fma_f32 v[56:57], v[212:213], v[156:157], v[56:57]\n" \
"v_pk_fma_f32 v[58:59], v[214:215], v[158:159], v[58:59]\n" \
"v_pk_add_f32 v[14:15], v[14:15], v[48:49]\n" \
"v_pk_fma_f32 v[36:37], v[48:49], v[180:181], v[36:37]\n" \
"v_pk_fma_f32 v[38:39], v[48:49], v[48:49], v[38:39]\n" \
"v_pk_add_f32 v[14:15], v[14:15], v[50:51]\n" \
"v_pk_fma_f32 v[36:37], v[50:51], v[182:183], v[36:37]\n" \
"v_pk_fma_f32 v[38:39], v[50:51], v[50:51], v[38:39]\n" \
"v_pk_add_f32 v[14:15], v[14:15], v[56:57]\n" \
"v_pk_fma_f32 v[36:37], v[56:57], v[188:189], v[36:37]\n" \
"v_pk_fma_f32 v[38:39], v[56:57], v[56:57], v[38:39]\n" \
"v_pk_add_f32 v[14:15], v[14:15], v[58:59]\n" \
"v_pk_fma_f32 v[36:37], v[58:59], v[190:191], v[36:37]\n" \
"v_pk_fma_f32 v[38:39], v[58:59], v[58:59], v[38:39]\n" \
"v_pk_add_f32 v[4:5], v[4:5], v[14:15]\n" \
"v_pk_add_f32 v[6:7], v[6:7], v[36:37]\n" \
"v_pk_add_f32 v[12:13], v[12:13], v[38:39]\n" \
"v_add_f32_e32 v4, v4, v5\n" \
"v_add_f32_e32 v6, v6, v7\n" \
"v_add_f32_e32 v12, v12, v13\n" \
"v_and_b32_e32 v87, s43, v91\n" \
"v_mov_b32_e32 v208, v12\n" \
"v_permlane32_swap_b32_e32 v4, v6\n" \
"v_lshlrev_b32_e32 v88, 16, v92\n" \
"v_permlane32_swap_b32_e32 v12, v208\n" \
"v_add_f32_e32 v4, v4, v6\n" \
"v_add_f32_e32 v12, v12, v208\n" \
"v_and_b32_e32 v89, s43, v92\n" \
"v_lshlrev_b32_e32 v90, 16, v93\n" \
"v_permlane16_swap_b32_e32 v4, v12\n" \
"v_add_f32_e32 v4, v4, v12\n" \
"v_and_b32_e32 v91, s43, v93\n" \
"v_lshlrev_b32_e32 v92, 16, v94\n" \
"v_add_f32_dpp v4, v4, v4 quad_perm:[1,0,3,2] row_mask:0xf bank_mask:0xf\n" \
"v_and_b32_e32 v93, s43, v94\n" \
"v_lshlrev_b32_e32 v94, 16, v95\n" \
"v_add_f32_dpp v4, v4, v4 quad_perm:[2,3,0,1] row_mask:0xf bank_mask:0xf\n" \
"v_and_b32_e32 v95, s43, v95\n" \
"s_nop 0\n" \
"v_add_f32_dpp v4, v4, v4 row_ror:8 row_mask:0xf bank_mask:0xf\n" \
"v_mov_b32_e32 v12, v4\n" \
"s_nop 0\n" \
"s_nop 0\n" \
"v_permlane16_swap_b32_e32 v4, v12\n" \
"v_mov_b32_e32 v6, v4\n" \
"v_mul_f32_e32 v12, 0x3a800000, v12\n" \
"s_nop 0\n" \
"v_permlane32_swap_b32_e32 v4, v6\n" \
"v_mul_f32_e32 v218, 0x3a800000, v4\n" \
"v_mul_f32_e32 v208, v218, v218\n" \
"v_sub_f32_e32 v12, v12, v208\n" \
"v_cmp_nle_f32_e32 vcc, v208, v12\n" \
"v_add_f32_e32 v12, 0x3727c5ac, v12\n" \
"v_rsq_f32_e32 v220, v12\n" \
"v_fma_f32 v210, -v218, v223, v6\n" \
"s_mov_b64 exec, 17\n" \
"v_mul_f32_e32 v210, v210, v220\n" \
"v_ashrrev_i32_e32 v208, 31, v210\n" \
"v_or_b32_e32 v208, 0x80000000, v208\n" \
"v_xor_b32_e32 v210, v210, v208\n" \
"v_and_b32_e32 v210, 0xfffff800, v210\n" \
"v_max_u32_e32 v210, 0x800, v210\n" \
"v_or_b32_e32 v210, v210, %[ta0]\n" \
"ds_write_b32 v216, v210 offset:14400\n" \
"s_mov_b64 exec, -1\n" \
"s_cbranch_vccnz TLrare_%=\n" \
"TLrok_%=:\n" \
"s_movk_i32 s52, 0x1000\n" \
"s_waitcnt lgkmcnt(0)\n" \
"s_barrier\n" \
"ds_read_b32 v209, v225 offset:14400\n" \
"v_mov_b32_e32 v227, 1\n" \
"v_mov_b32_e32 v191, 0\n" \
"v_mov_b32_e32 v190, 0\n" \
"v_mov_b32_e32 v225, -1\n" \
"s_mov_b32 s41, 1\n" \
"s_cmp_lg_u32 %[wbytes], 0\n" \
"s_cselect_b32 s42, -8, 0\n" \
"s_waitcnt lgkmcnt(0)\n" \
"v_min_u32_dpp v208, v209, v209 quad_perm:[1,0,3,2] row_mask:0xf bank_mask:0xf\n" \
"v_max_u32_dpp v209, v209, v209 quad_perm:[1,0,3,2] row_mask:0xf bank_mask:0xf\n" \
"s_nop 1\n" \
"v_min_u32_dpp v210, v209, v209 quad_perm:[2,3,0,1] row_mask:0xf bank_mask:0xf\n" \
"v_max_u32_dpp v211, v208, v208 quad_perm:[2,3,0,1] row_mask:0xf bank_mask:0xf\n" \
"v_max_u32_dpp v209, v209, v209 quad_perm:[2,3,0,1] row_mask:0xf bank_mask:0xf\n" \
"v_max_u32_e32 v208, v210, v211\n" \
"s_nop 0\n" \
"v_min_u32_dpp v210, v209, v209 row_half_mirror row_mask:0xf bank_mask:0xf\n" \
"v_max_u32_dpp v211, v208, v208 row_half_mirror row_mask:0xf bank_mask:0xf\n" \
"v_max_u32_dpp v209, v209, v209 row_half_mirror row_mask:0xf bank_mask:0xf\n" \
"v_max_u32_e32 v208, v210, v211\n" \
"s_mov_b64 exec, 3\n" \
"buffer_atomic_or_x2 v[208:209], v229, %[srdz], s48 offen sc1\n" \
"s_mov_b64 exec, -1\n" \
"s_waitcnt vmcnt(1)\n" \
"TLcs_%=:\n" \
"v_mfma_f32_16x16x32_bf16 v[128:131], v[230:233], a[128:131], 0\n" \
"v_mfma_f32_16x16x32_bf16 v[132:135], v[230:233], a[136:139], 0\n" \
"v_mfma_f32_16x16x32_bf16 v[128:131], v[234:237], a[132:135], v[128:131]\n" \
"v_mfma_f32_16x16x32_bf16 v[132:135], v[234:237], a[140:143], v[132:135]\n" \
"v_mfma_f32_16x16x32_bf16 v[136:139], v[230:233], a[144:147], 0\n" \
"v_mfma_f32_16x16x32_bf16 v[140:143], v[230:233], a[152:155], 0\n" \
"v_mfma_f32_16x16x32_bf16 v[136:139], v[234:237], a[148:151], v[136:139]\n" \
"v_mfma_f32_16x16x32_bf16 v[140:143], v[234:237], a[156:159], v[140:143]\n" \
"s_nop 2\n" \
"v_pk_fma_f32 v[144:145], v[128:129], v[64:65], v[80:81]\n" \
"v_pk_fma_f32 v[146:147], v[130:131], v[66:67], v[82:83]\n" \
"v_pk_fma_f32 v[148:149], v[132:133], v[68:69], v[84:85]\n" \
"v_pk_fma_f32 v[150:151], v[134:135], v[70:71], v[86:87]\n" \
"v_pk_mul_f32 v[160:161], v[144:145], v[96:97]\n" \
"v_pk_mul_f32 v[162:163], v[146:147], v[98:99]\n" \
"v_pk_mul_f32 v[164:165], v[148:149], v[100:101]\n" \
"v_pk_mul_f32 v[166:167], v[150:151], v[102:103]\n" \
"v_cvt_pk_bf16_f32 v176, v160, v161\n" \
"v_cvt_pk_bf16_f32 v177, v162, v163\n" \
"v_cvt_pk_bf16_f32 v178, v164, v165\n" \
"v_cvt_pk_bf16_f32 v179, v166, v167\n" \
"ds_write_b128 %[oq0], v[176:179] offset:16384\n" \
"v_pk_fma_f32 v[152:153], v[136:137], v[72:73], v[88:89]\n" \
"v_pk_fma_f32 v[154:155], v[138:139], v[74:75], v[90:91]\n" \
"v_pk_fma_f32 v[156:157], v[140:141], v[76:77], v[92:93]\n" \
"v_pk_fma_f32 v[158:159], v[142:143], v[78:79], v[94:95]\n" \
"v_pk_mul_f32 v[168:169], v[152:153], v[104:105]\n" \
"v_pk_mul_f32 v[170:171], v[154:155], v[106:107]\n" \
"v_pk_mul_f32 v[172:173], v[156:157], v[108:109]\n" \
"v_pk_mul_f32 v[174:175], v[158:159], v[110:111]\n" \
"v_cvt_pk_bf16_f32 v180, v168, v169\n" \
"v_cvt_pk_bf16_f32 v181, v170, v171\n" \
"v_cvt_pk_bf16_f32 v182, v172, v173\n" \
"v_cvt_pk_bf16_f32 v183, v174, v175\n" \
"ds_write_b128 %[wo0], v[180:183] offset:16384\n" \
"ds_read_b128 v[4:7], v226 offset:0\n" \
"ds_read_b128 v[12:15], v226 offset:64\n" \
"ds_read_b128 v[20:23], v226 offset:128\n" \
"ds_read_b128 v[28:31], v226 offset:192\n" \
"ds_read_b128 v[36:39], v226 offset:256\n" \
"ds_read_b128 v[44:47], v226 offset:320\n" \
"ds_read_b128 v[52:55], v226 offset:384\n" \
"ds_read_b128 v[60:63], v226 offset:448\n" \
"v_cmp_u_f32_e32 vcc, v128, v132\n" \
"v_cmp_u_f32_e64 s[62:63], v136, v140\n" \
"s_or_b64 vcc, vcc, s[62:63]\n" \
"s_cbranch_scc1 TLcx_%=\n" \
"TLco_%=:\n" \
"v_pk_add_f32 v[184:185], v[144:145], v[146:147]\n" \
"v_pk_mul_f32 v[186:187], v[144:145], v[144:145]\n" \
"v_pk_mul_f32 v[188:189], v[144:145], v[112:113]\n" \
"v_pk_fma_f32 v[186:187], v[146:147], v[146:147], v[186:187]\n" \
"v_pk_fma_f32 v[188:189], v[146:147], v[114:115], v[188:189]\n" \
"v_pk_add_f32 v[184:185], v[184:185], v[148:149]\n" \
"v_pk_fma_f32 v[186:187], v[148:149], v[148:149], v[186:187]\n" \
"v_pk_fma_f32 v[188:189], v[148:149], v[116:117], v[188:189]\n" \
"v_pk_add_f32 v[184:185], v[184:185], v[150:151]\n" \
"v_pk_fma_f32 v[186:187], v[150:151], v[150:151], v[186:187]\n" \
"v_pk_fma_f32 v[188:189], v[150:151], v[118:119], v[188:189]\n" \
"v_pk_add_f32 v[184:185], v[184:185], v[152:153]\n" \
"v_pk_fma_f32 v[186:187], v[152:153], v[152:153], v[186:187]\n" \
"v_pk_fma_f32 v[188:189], v[152:153], v[120:121], v[188:189]\n" \
"v_pk_add_f32 v[184:185], v[184:185], v[154:155]\n" \
"v_pk_fma_f32 v[186:187], v[154:155], v[154:155], v[186:187]\n" \
"v_pk_fma_f32 v[188:189], v[154:155], v[122:123], v[188:189]\n" \
"v_pk_add_f32 v[184:185], v[184:185], v[156:157]\n" \
"v_pk_fma_f32 v[186:187], v[156:157], v[156:157], v[186:187]\n" \
"v_pk_fma_f32 v[188:189], v[156:157], v[124:125], v[188:189]\n" \
"v_pk_add_f32 v[184:185], v[184:185], v[158:159]\n" \
"v_pk_fma_f32 v[186:187], v[158:159], v[158:159], v[186:187]\n" \
"v_pk_fma_f32 v[188:189], v[158:159], v[126:127], v[188:189]\n" \
"v_add_f32_e32 v184, v184, v185\n" \
".if . - adaln_body != 65616\n" \
".error \"size model\"\n" \
".endif\n" \
"Lk15_%=:\n" \
"s_cbranch_execz Lk16_%=\n" \
"s_nop 0\n" \
"v_add_f32_e32 v186, v186, v187\n" \
"v_add_f32_e32 v188, v188, v189\n" \
"s_waitcnt lgkmcnt(7)\n" \
"v_mfma_f32_16x16x32_bf16 v[208:211], a[64:67], v[4:7], 0\n" \
"v_mfma_f32_16x16x32_bf16 v[212:215], a[96:99], v[4:7], 0\n" \
"v_permlane16_swap_b32_e32 v184, v186\n" \
"v_mov_b32_e32 v189, v188\n" \
"s_waitcnt lgkmcnt(6)\n" \
"v_mfma_f32_16x16x32_bf16 v[208:211], a[68:71], v[12:15], v[208:211]\n" \
"v_mfma_f32_16x16x32_bf16 v[212:215], a[100:103], v[12:15], v[212:215]\n" \
"v_add_f32_e32 v184, v184, v186\n" \
"v_permlane16_swap_b32_e32 v188, v189\n" \
"s_waitcnt lgkmcnt(5)\n" \
"v_mfma_f32_16x16x32_bf16 v[208:211], a[72:75], v[20:23], v[208:211]\n" \
"v_mfma_f32_16x16x32_bf16 v[212:215], a[104:107], v[20:23], v[212:215]\n" \
"v_add_f32_e32 v188, v188, v189\n" \
"v_add_u32_e32 v176, 1632, %[kd0]\n" \
"s_waitcnt lgkmcnt(4)\n" \
"v_mfma_f32_16x16x32_bf16 v[208:211], a[76:79], v[28:31], v[208:211]\n" \
"v_mfma_f32_16x16x32_bf16 v[212:215], a[108:111], v[28:31], v[212:215]\n" \
"v_add_f32_dpp v184, v184, v184 quad_perm:[1,0,3,2] row_mask:0xf bank_mask:0xf\n" \
"v_add_f32_dpp v188, v188, v188 quad_perm:[1,0,3,2] row_mask:0xf bank_mask:0xf\n" \
"s_waitcnt lgkmcnt(3)\n" \
"v_mfma_f32_16x16x32_bf16 v[208:211], a[80:83], v[36:39], v[208:211]\n" \
"v_mfma_f32_16x16x32_bf16 v[212:215], a[112:115], v[36:39], v[212:215]\n" \
"v_add_f32_dpp v184, v184, v184 quad_perm:[2,3,0,1] row_mask:0xf bank_mask:0xf\n" \
"v_add_f32_dpp v188, v188, v188 quad_perm:[2,3,0,1] row_mask:0xf bank_mask:0xf\n" \
"s_waitcnt lgkmcnt(2)\n" \
"v_mfma_f32_16x16x32_bf16 v[208:211], a[84:87], v[44:47], v[208:211]\n" \
"v_mfma_f32_16x16x32_bf16 v[212:215], a[116:119], v[44:47], v[212:215]\n" \
"v_add_f32_dpp v184, v184, v184 row_ror:8 row_mask:0xf bank_mask:0xf\n" \
"v_add_f32_dpp v188, v188, v188 row_ror:8 row_mask:0xf bank_mask:0xf\n" \
"s_waitcnt lgkmcnt(1)\n" \
"v_mfma_f32_16x16x32_bf16 v[208:211], a[88:91], v[52:55], v[208:211]\n" \
"v_mfma_f32_16x16x32_bf16 v[212:215], a[120:123], v[52:55], v[212:215]\n" \
"s_mov_b32 vcc_lo, 0xaaaaaaaa\n" \
"s_mov_b32 vcc_hi, 0xaaaaaaaa\n" \
"v_cndmask_b32_e32 v184, v184, v188, vcc\n" \
"s_waitcnt lgkmcnt(0)\n" \
"v_mfma_f32_16x16x32_bf16 v[208:211], a[92:95], v[60:63], v[208:211]\n" \
"v_mfma_f32_16x16x32_bf16 v[212:215], a[124:127], v[60:63], v[212:215]\n" \
"s_mov_b32 exec_lo, 0x330033\n" \
"s_mov_b32 exec_hi, 0x330033\n" \
"ds_write_b32 v228, v184 offset:0\n" \
"s_mov_b32 exec_lo, 0x3f003f\n" \
"s_mov_b32 exec_hi, 0x3f003f\n" \
"s_nop 1\n" \
"ds_write2_b32 %[kd0], v208, v209 offset0:0 offset1:4\n" \
"ds_write2_b32 %[kd0], v210, v211 offset0:8 offset1:12\n" \
"ds_write2_b32 v176, v212, v213 offset0:0 offset1:4\n" \
"ds_write2_b32 v176, v214, v215 offset0:8 offset1:12\n" \
"s_mov_b64 exec, -1\n" \
"s_waitcnt lgkmcnt(0)\n" \
"s_barrier\n" \
"ds_read_b128 v[4:7], %[vg0] offset:0\n" \
"ds_read_b128 v[12:15], %[vg0] offset:16\n" \
"ds_read_b128 v[20:23], %[vg0] offset:32\n" \
"ds_read_b128 v[28:31], v221 offset:0\n" \
"ds_read_b128 v[36:39], %[vx0] offset:1088\n" \
"ds_read_b128 v[44:47], %[vx0] offset:1360\n" \
"ds_read_b128 v[52:55], v221 offset:1632\n" \
"ds_read_b128 v[60:63], %[vx0] offset:2720\n" \
"ds_read_b128 v[128:131], %[vx0] offset:2992\n" \
"v_bfe_u32 v208, v222, 3, 1\n" \
"v_and_b32_e32 v209, 1, v222\n" \
"v_lshl_or_b32 v208, v208, 1, v209\n" \
"s_waitcnt lgkmcnt(6)\n" \
"v_pk_add_f32 v[4:5], v[4:5], v[6:7]\n" \
"v_pk_add_f32 v[12:13], v[12:13], v[14:15]\n" \
"v_pk_add_f32 v[20:21], v[20:21], v[22:23]\n" \
"v_add_f32_e32 v4, v4, v5\n" \
"v_add_f32_e32 v12, v12, v13\n" \
"v_add_f32_e32 v20, v20, v21\n" \
"v_mul_f32_e32 v4, 0x3a800000, v4\n" \
"v_mul_f32_e32 v12, 0x3a800000, v12\n" \
"v_mul_f32_e32 v209, v4, v4\n" \
"v_sub_f32_e32 v12, v12, v209\n" \
"s_waitcnt lgkmcnt(3)\n" \
"v_cmp_nle_f32_e64 s[50:51], v209, v12\n" \
"v_add_f32_e32 v12, 0x3727c5ac, v12\n" \
"v_pk_add_f32 v[28:29], v[28:29], v[30:31]\n" \
"v_rsq_f32_e32 v132, v12\n" \
"v_pk_add_f32 v[36:37], v[36:37], v[38:39]\n" \
"v_fma_f32 v20, -v4, v223, v20\n" \
"v_pk_add_f32 v[44:45], v[44:45], v[46:47]\n" \
"v_add_f32_e32 v28, v28, v29\n" \
"v_add_f32_e32 v36, v36, v37\n" \
"v_add_f32_e32 v44, v44, v45\n" \
"v_mul_f32_e32 v20, v20, v132\n" \
"s_waitcnt lgkmcnt(0)\n" \
"v_pk_add_f32 v[52:53], v[52:53], v[54:55]\n" \
"v_ashrrev_i32_e32 v209, 31, v20\n" \
"v_pk_add_f32 v[60:61], v[60:61], v[62:63]\n" \
"v_or_b32_e32 v209, 0x80000000, v209\n" \
"v_pk_add_f32 v[128:129], v[128:129], v[130:131]\n" \
"v_xor_b32_e32 v210, v20, v209\n" \
"v_add_f32_e32 v52, v52, v53\n" \
"v_and_b32_e32 v210, 0xfffff800, v210\n" \
"v_add_f32_e32 v60, v60, v61\n" \
"v_max_u32_e32 v210, 0x800, v210\n" \
"v_add_f32_e32 v128, v128, v129\n" \
"v_and_or_b32 v210, v210, v225, v208\n" \
"v_fma_f32 v28, -v4, v36, v28\n" \
"v_fma_f32 v52, -v4, v60, v52\n" \
"v_fma_f32 v28, v28, v132, v44\n" \
"v_min_u32_dpp v211, v210, v210 quad_perm:[1,0,3,2] row_mask:0xf bank_mask:0xf\n" \
"v_fma_f32 v52, v52, v132, v128\n" \
"v_max_u32_dpp v210, v210, v210 quad_perm:[1,0,3,2] row_mask:0xf bank_mask:0xf\n" \
"v_cvt_pk_bf16_f32 v28, 0, v28\n" \
"v_cvt_pk_bf16_f32 v52, 0, v52\n" \
"v_min_u32_dpp v212, v210, v210 row_ror:8 row_mask:0xf bank_mask:0xf\n" \
"v_mul_f32_e32 v214, 0xbfb8aa3b, v28\n" \
"v_max_u32_dpp v213, v211, v211 row_ror:8 row_mask:0xf bank_mask:0xf\n" \
"v_exp_f32_e32 v214, v214\n" \
"v_max_u32_dpp v210, v210, v210 row_ror:8 row_mask:0xf bank_mask:0xf\n" \
"v_add_f32_e32 v214, 1.0, v214\n" \
"v_max_u32_e32 v211, v212, v213\n" \
"v_rcp_f32_e32 v214, v214\n" \
"s_nop 0\n" \
"v_mul_f32_e32 v28, v28, v214\n" \
"v_cvt_pk_bf16_f32 v28, 0, v28\n" \
"v_mul_f32_e32 v28, v28, v52\n" \
"v_cvt_pk_bf16_f32 v28, 0, v28\n" \
"v_cndmask_b32_e32 v133, v210, v211, vcc\n" \
"v_readfirstlane_b32 s60, v210\n" \
"v_readfirstlane_b32 s61, v211\n" \
"v_xor_b32_e32 v212, v133, v208\n" \
"v_and_b32_e32 v213, 1, v212\n" \
"v_and_b32_e32 v212, 2, v212\n" \
"v_cmp_ne_u32_e64 s[54:55], 0, v213\n" \
"v_cmp_ne_u32_e64 s[56:57], 0, v212\n" \
"s_mov_b32 s62, 0x07060302\n" \
"v_mov_b32_dpp v213, v28 quad_perm:[1,0,3,2] row_mask:0xf bank_mask:0xf\n" \
"v_cndmask_b32_e64 v28, v28, v213, s[54:55]\n" \
"s_nop 1\n" \
"v_mov_b32_dpp v214, v28 row_ror:8 row_mask:0xf bank_mask:0xf\n" \
"v_cndmask_b32_e64 v28, v28, v214, s[56:57]\n" \
"s_mov_b32 vcc_lo, 0xcccccccc\n" \
"s_mov_b32 vcc_hi, 0xcccccccc\n" \
"v_mov_b32_dpp v213, v28 quad_perm:[2,3,0,1] row_mask:0xf bank_mask:0xf\n" \
"s_mov_b32 s58, 0x30003\n" \
"s_mov_b32 s59, 0xc000c\n" \
"v_perm_b32 v214, v213, v28, s62\n" \
"v_perm_b32 v215, v28, v213, s62\n" \
"v_cndmask_b32_e32 v214, v214, v215, vcc\n" \
"s_mov_b32 vcc_lo, 0xf0f0f0f0\n" \
"s_mov_b32 vcc_hi, 0xf0f0f0f0\n" \
"v_mov_b32_dpp v213, v214 row_ror:4 row_mask:0xf bank_mask:0xf\n" \
"v_cndmask_b32_e32 v164, v214, v213, vcc\n" \
"v_cndmask_b32_e32 v165, v213, v214, vcc\n" \
"v_mov_b32_e32 v166, v164\n" \
"v_mov_b32_e32 v167, v165\n" \
"s_nop 0\n" \
"v_permlane32_swap_b32_e32 v164, v166\n" \
"v_permlane32_swap_b32_e32 v165, v167\n" \
"s_nop 0\n" \
"v_permlane16_swap_b32_e32 v164, v166\n" \
"v_permlane16_swap_b32_e32 v165, v167\n" \
"v_cndmask_b32_e64 v176, 0, v164, s[58:59]\n" \
"v_cndmask_b32_e64 v177, 0, v165, s[58:59]\n" \
"v_cndmask_b32_e64 v178, 0, v166, s[58:59]\n" \
"v_cndmask_b32_e64 v179, 0, v167, s[58:59]\n" \
"s_lshl_b64 s[58:59], s[58:59], 4\n" \
"v_cndmask_b32_e64 v180, 0, v164, s[58:59]\n" \
"v_cndmask_b32_e64 v181, 0, v165, s[58:59]\n" \
"v_cndmask_b32_e64 v182, 0, v166, s[58:59]\n" \
"v_cndmask_b32_e64 v183, 0, v167, s[58:59]\n" \
"s_lshl_b64 s[58:59], s[58:59], 4\n" \
"v_mfma_f32_16x16x32_bf16 v[168:171], v[176:179], a[0:3], 0\n" \
"v_mfma_f32_16x16x32_bf16 v[172:175], v[176:179], a[16:19], 0\n" \
"v_cndmask_b32_e64 v184, 0, v164, s[58:59]\n" \
"v_cndmask_b32_e64 v185, 0, v165, s[58:59]\n" \
"v_cndmask_b32_e64 v186, 0, v166, s[58:59]\n" \
"v_cndmask_b32_e64 v187, 0, v167, s[58:59]\n" \
"s_lshl_b64 s[58:59], s[58:59], 4\n" \
"v_mfma_f32_16x16x32_bf16 v[168:171], v[180:183], a[4:7], v[168:171]\n" \
"v_mfma_f32_16x16x32_bf16 v[172:175], v[180:183], a[20:23], v[172:175]\n" \
"v_cndmask_b32_e64 v136, 0, v164, s[58:59]\n" \
"v_cndmask_b32_e64 v137, 0, v165, s[58:59]\n" \
"v_cndmask_b32_e64 v138, 0, v166, s[58:59]\n" \
"v_cndmask_b32_e64 v139, 0, v167, s[58:59]\n" \
"v_mfma_f32_16x16x32_bf16 v[168:171], v[184:187], a[8:11], v[168:171]\n" \
"v_mfma_f32_16x16x32_bf16 v[172:175], v[184:187], a[24:27], v[172:175]\n" \
"v_mfma_f32_16x16x32_bf16 v[168:171], v[136:139], a[12:15], v[168:171]\n" \
"v_mfma_f32_16x16x32_bf16 v[172:175], v[136:139], a[28:31], v[172:175]\n" \
"ds_read_b128 v[64:67], %[kb0] offset:0\n" \
"ds_read_b128 v[68:71], %[kb0] offset:1024\n" \
"ds_read_b128 v[72:75], %[vvb] offset:0\n" \
"ds_read_b128 v[76:79], %[vvb] offset:1024\n" \
"ds_read_b128 v[80:83], %[kb0] offset:4096\n" \
"ds_read_b128 v[84:87], %[kb0] offset:5120\n" \
"ds_read_b128 v[88:91], %[vvb] offset:4096\n" \
"ds_read_b128 v[92:95], %[vvb] offset:5120\n" \
"v_and_or_b32 v168, v168, s42, v227\n" \
"v_and_or_b32 v169, v169, s42, v227\n" \
"v_and_or_b32 v170, v170, s42, v227\n" \
"v_and_or_b32 v171, v171, s42, v227\n" \
"buffer_store_dwordx4 v[168:171], v217, %[srdz], s69 offen offset:0 sc1\n" \
"v_and_or_b32 v172, v172, s42, v227\n" \
"v_and_or_b32 v173, v173, s42, v227\n" \
"v_and_or_b32 v174, v174, s42, v227\n" \
"v_and_or_b32 v175, v175, s42, v227\n" \
"buffer_store_dwordx4 v[172:175], v217, %[srdz], s69 offen offset:1024 sc1\n" \
"s_movk_i32 s62, -128\n" \
"s_sub_u32 s63, 65536, s40\n" \
"v_mul_i32_i24_e64 v208, v219, s62\n" \
"v_add_u32_e32 v208, s63, v208\n" \
"v_lshl_add_u32 v209, %[oq0], 1, v208\n" \
"v_lshl_add_u32 v210, %[wo0], 1, v208\n" \
"ds_write_b128 v209, v[144:147] offset:0\n" \
"ds_write_b128 v209, v[148:151] offset:16\n" \
"ds_write_b128 v210, v[152:155] offset:0\n" \
"ds_write_b128 v210, v[156:159] offset:16\n" \
"v_mov_b32_dpp v211, v4 quad_perm:[1,0,3,2] row_mask:0xf bank_mask:0xf\n" \
"v_mov_b32_dpp v213, v132 quad_perm:[1,0,3,2] row_mask:0xf bank_mask:0xf\n" \
"v_mov_b32_dpp v215, v20 quad_perm:[1,0,3,2] row_mask:0xf bank_mask:0xf\n" \
"v_cndmask_b32_e64 v4, v4, v211, s[54:55]\n" \
"v_cndmask_b32_e64 v132, v132, v213, s[54:55]\n" \
"v_cndmask_b32_e64 v20, v20, v215, s[54:55]\n" \
"v_mov_b32_dpp v212, v4 row_ror:8 row_mask:0xf bank_mask:0xf\n" \
"v_mov_b32_dpp v214, v132 row_ror:8 row_mask:0xf bank_mask:0xf\n" \
"v_mov_b32_dpp v208, v20 row_ror:8 row_mask:0xf bank_mask:0xf\n" \
"v_cndmask_b32_e64 v4, v4, v212, s[56:57]\n" \
"v_cndmask_b32_e64 v132, v132, v214, s[56:57]\n" \
"v_cndmask_b32_e64 v20, v20, v208, s[56:57]\n" \
"v_mov_b32_e32 v126, v132\n" \
"v_and_b32_e32 v125, 3, v133\n" \
"s_bitcmp1_b32 s60, 0\n" \
"s_cselect_b32 s56, s45, s44\n" \
"s_cselect_b32 s57, s47, s46\n" \
"s_bitcmp1_b32 s60, 1\n" \
"s_cselect_b32 s60, s57, s56\n" \
"s_bitcmp1_b32 s61, 0\n" \
"s_cselect_b32 s56, s45, s44\n" \
"s_cselect_b32 s57, s47, s46\n" \
"s_bitcmp1_b32 s61, 1\n" \
"s_cselect_b32 s61, s57, s56\n" \
"s_cmp_lg_u64 s[50:51], 0\n" \
"s_cselect_b32 s53, 1, 0\n" \
"s_mov_b32 s51, 0x10000\n" \
"s_cmp_lg_u32 s41, 1\n" \
"s_cbranch_scc1 TLls_%=\n" \
"v_mul_f32_e64 v208, -v218, v220\n" \
"v_pk_fma_f32 v[128:129], v[0:1], v[220:221], v[208:209] op_sel_hi:[1,0,0]\n" \
"v_pk_fma_f32 v[130:131], v[2:3], v[220:221], v[208:209] op_sel_hi:[1,0,0]\n" \
"v_pk_fma_f32 v[132:133], v[8:9], v[220:221], v[208:209] op_sel_hi:[1,0,0]\n" \
"v_pk_fma_f32 v[134:135], v[10:11], v[220:221], v[208:209] op_sel_hi:[1,0,0]\n" \
"v_pk_fma_f32 v[136:137], v[16:17], v[220:221], v[208:209] op_sel_hi:[1,0,0]\n" \
"v_pk_fma_f32 v[138:139], v[18:19], v[220:221], v[208:209] op_sel_hi:[1,0,0]\n" \
"v_pk_fma_f32 v[140:141], v[24:25], v[220:221], v[208:209] op_sel_hi:[1,0,0]\n" \
"v_pk_fma_f32 v[142:143], v[26:27], v[220:221], v[208:209] op_sel_hi:[1,0,0]\n" \
"v_pk_fma_f32 v[144:145], v[32:33], v[220:221], v[208:209] op_sel_hi:[1,0,0]\n" \
"v_pk_fma_f32 v[146:147], v[34:35], v[220:221], v[208:209] op_sel_hi:[1,0,0]\n" \
"v_pk_fma_f32 v[148:149], v[40:41], v[220:221], v[208:209] op_sel_hi:[1,0,0]\n" \
"v_pk_fma_f32 v[150:151], v[42:43], v[220:221], v[208:209] op_sel_hi:[1,0,0]\n" \
"v_pk_fma_f32 v[152:153], v[48:49], v[220:221], v[208:209] op_sel_hi:[1,0,0]\n" \
"v_pk_fma_f32 v[154:155], v[50:51], v[220:221], v[208:209] op_sel_hi:[1,0,0]\n" \
"v_pk_fma_f32 v[156:157], v[56:57], v[220:221], v[208:209] op_sel_hi:[1,0,0]\n" \
"v_pk_fma_f32 v[158:159], v[58:59], v[220:221], v[208:209] op_sel_hi:[1,0,0]\n" \
"v_sub_u32_e32 v216, 0x7ff, %[ta0]\n" \
"v_cmp_eq_u32_e64 s[56:57], s60, v216\n" \
"v_cmp_eq_u32_e64 s[58:59], s61, v216\n" \
"s_cmp_eq_u32 %[wid], 3\n" \
"s_cselect_b32 s49, s49, 0x80000000\n" \
"v_lshrrev_b32_e32 v216, 2, v224\n" \
"s_waitcnt lgkmcnt(0)\n" \
"v_lshlrev_b32_e32 v160, 16, v192\n" \
"v_and_b32_e32 v161, s43, v192\n" \
"v_lshlrev_b32_e32 v176, 16, v64\n" \
"v_and_b32_e32 v177, s43, v64\n" \
"v_lshlrev_b32_e32 v162, 16, v193\n" \
"v_and_b32_e32 v163, s43, v193\n" \
"v_lshlrev_b32_e32 v178, 16, v65\n" \
"v_and_b32_e32 v179, s43, v65\n" \
"v_lshlrev_b32_e32 v164, 16, v196\n" \
"v_and_b32_e32 v165, s43, v196\n" \
"v_lshlrev_b32_e32 v180, 16, v68\n" \
"v_and_b32_e32 v181, s43, v68\n" \
"v_lshlrev_b32_e32 v166, 16, v197\n" \
"v_and_b32_e32 v167, s43, v197\n" \
"v_lshlrev_b32_e32 v182, 16, v69\n" \
"v_and_b32_e32 v183, s43, v69\n" \
"v_pk_fma_f32 v[128:129], v[128:129], v[160:161], v[176:177]\n" \
"v_pk_fma_f32 v[130:131], v[130:131], v[162:163], v[178:179]\n" \
"v_pk_fma_f32 v[132:133], v[132:133], v[164:165], v[180:181]\n" \
"v_pk_fma_f32 v[134:135], v[134:135], v[166:167], v[182:183]\n" \
"v_lshlrev_b32_e32 v168, 16, v80\n" \
"v_and_b32_e32 v169, s43, v80\n" \
"v_lshlrev_b32_e32 v170, 16, v81\n" \
"v_and_b32_e32 v171, s43, v81\n" \
"v_lshlrev_b32_e32 v172, 16, v84\n" \
"v_and_b32_e32 v173, s43, v84\n" \
"v_lshlrev_b32_e32 v174, 16, v85\n" \
"v_and_b32_e32 v175, s43, v85\n" \
"v_pk_fma_f32 v[128:129], v[128:129], v[168:169], v[0:1]\n" \
"v_pk_fma_f32 v[130:131], v[130:131], v[170:171], v[2:3]\n" \
"v_pk_fma_f32 v[132:133], v[132:133], v[172:173], v[8:9]\n" \
"v_pk_fma_f32 v[134:135], v[134:135], v[174:175], v[10:11]\n" \
"v_cvt_pk_bf16_f32 v128, v128, v129\n" \
"v_cvt_pk_bf16_f32 v130, v130, v131\n" \
"v_cvt_pk_bf16_f32 v132, v132, v133\n" \
"v_cvt_pk_bf16_f32 v134, v134, v135\n" \
"v_lshlrev_b32_e32 v160, 16, v194\n" \
"v_and_b32_e32 v161, s43, v194\n" \
"v_lshlrev_b32_e32 v176, 16, v66\n" \
"v_and_b32_e32 v177, s43, v66\n" \
"v_lshlrev_b32_e32 v162, 16, v195\n" \
"v_and_b32_e32 v163, s43, v195\n" \
"v_lshlrev_b32_e32 v178, 16, v67\n" \
"v_and_b32_e32 v179, s43, v67\n" \
"v_lshlrev_b32_e32 v164, 16, v198\n" \
"v_and_b32_e32 v165, s43, v198\n" \
"v_lshlrev_b32_e32 v180, 16, v70\n" \
"v_and_b32_e32 v181, s43, v70\n" \
"v_lshlrev_b32_e32 v166, 16, v199\n" \
"v_and_b32_e32 v167, s43, v199\n" \
"v_lshlrev_b32_e32 v182, 16, v71\n" \
"v_and_b32_e32 v183, s43, v71\n" \
"v_pk_fma_f32 v[136:137], v[136:137], v[160:161], v[176:177]\n" \
"v_pk_fma_f32 v[138:139], v[138:139], v[162:163], v[178:179]\n" \
"v_pk_fma_f32 v[140:141], v[140:141], v[164:165], v[180:181]\n" \
"v_pk_fma_f32 v[142:143], v[142:143], v[166:167], v[182:183]\n" \
"v_lshlrev_b32_e32 v168, 16, v82\n" \
"v_and_b32_e32 v169, s43, v82\n" \
"v_lshlrev_b32_e32 v170, 16, v83\n" \
"v_and_b32_e32 v171, s43, v83\n" \
"v_lshlrev_b32_e32 v172, 16, v86\n" \
"v_and_b32_e32 v173, s43, v86\n" \
"v_lshlrev_b32_e32 v174, 16, v87\n" \
"v_and_b32_e32 v175, s43, v87\n" \
"v_pk_fma_f32 v[136:137], v[136:137], v[168:169], v[16:17]\n" \
"v_pk_fma_f32 v[138:139], v[138:139], v[170:171], v[18:19]\n" \
"v_pk_fma_f32 v[140:141], v[140:141], v[172:173], v[24:25]\n" \
"v_pk_fma_f32 v[142:143], v[142:143], v[174:175], v[26:27]\n" \
"v_cvt_pk_bf16_f32 v136, v136, v137\n" \
"v_cvt_pk_bf16_f32 v138, v138, v139\n" \
"v_cvt_pk_bf16_f32 v140, v140, v141\n" \
"v_cvt_pk_bf16_f32 v142, v142, v143\n" \
"buffer_load_dwordx4 v[112:115], v216, %[srdz], s49 offen offset:0 sc1\n" \
"buffer_load_dwordx4 v[116:119], v216, %[srdz], s49 offen offset:16 sc1\n" \
"v_lshlrev_b32_e32 v160, 16, v200\n" \
"v_and_b32_e32 v161, s43, v200\n" \
"v_lshlrev_b32_e32 v176, 16, v72\n" \
"v_and_b32_e32 v177, s43, v72\n" \
"v_lshlrev_b32_e32 v162, 16, v201\n" \
"v_and_b32_e32 v163, s43, v201\n" \
"v_lshlrev_b32_e32 v178, 16, v73\n" \
"v_and_b32_e32 v179, s43, v73\n" \
"v_lshlrev_b32_e32 v164, 16, v204\n" \
"v_and_b32_e32 v165, s43, v204\n" \
"v_lshlrev_b32_e32 v180, 16, v76\n" \
"v_and_b32_e32 v181, s43, v76\n" \
"v_lshlrev_b32_e32 v166, 16, v205\n" \
"v_and_b32_e32 v167, s43, v205\n" \
"v_lshlrev_b32_e32 v182, 16, v77\n" \
"v_and_b32_e32 v183, s43, v77\n" \
"v_pk_fma_f32 v[144:145], v[144:145], v[160:161], v[176:177]\n" \
"v_pk_fma_f32 v[146:147], v[146:147], v[162:163], v[178:179]\n" \
"v_pk_fma_f32 v[148:149], v[148:149], v[164:165], v[180:181]\n" \
"v_pk_fma_f32 v[150:151], v[150:151], v[166:167], v[182:183]\n" \
"v_lshlrev_b32_e32 v168, 16, v88\n" \
"v_and_b32_e32 v169, s43, v88\n" \
"v_lshlrev_b32_e32 v170, 16, v89\n" \
"v_and_b32_e32 v171, s43, v89\n" \
"v_lshlrev_b32_e32 v172, 16, v92\n" \
"v_and_b32_e32 v173, s43, v92\n" \
"v_lshlrev_b32_e32 v174, 16, v93\n" \
"v_and_b32_e32 v175, s43, v93\n" \
"v_pk_fma_f32 v[144:145], v[144:145], v[168:169], v[32:33]\n" \
"v_pk_fma_f32 v[146:147], v[146:147], v[170:171], v[34:35]\n" \
"v_pk_fma_f32 v[148:149], v[148:149], v[172:173], v[40:41]\n" \
"v_pk_fma_f32 v[150:151], v[150:151], v[174:175], v[42:43]\n" \
"v_cvt_pk_bf16_f32 v144, v144, v145\n" \
"v_cvt_pk_bf16_f32 v146, v146, v147\n" \
"v_cvt_pk_bf16_f32 v148, v148, v149\n" \
"v_cvt_pk_bf16_f32 v150, v150, v151\n" \
"v_lshlrev_b32_e32 v160, 16, v202\n" \
"v_and_b32_e32 v161, s43, v202\n" \
"v_lshlrev_b32_e32 v176, 16, v74\n" \
"v_and_b32_e32 v177, s43, v74\n" \
"v_lshlrev_b32_e32 v162, 16, v203\n" \
"v_and_b32_e32 v163, s43, v203\n" \
"v_lshlrev_b32_e32 v178, 16, v75\n" \
"v_and_b32_e32 v179, s43, v75\n" \
"v_lshlrev_b32_e32 v164, 16, v206\n" \
"v_and_b32_e32 v165, s43, v206\n" \
"v_lshlrev_b32_e32 v180, 16, v78\n" \
"v_and_b32_e32 v181, s43, v78\n" \
"v_lshlrev_b32_e32 v166, 16, v207\n" \
"v_and_b32_e32 v167, s43, v207\n" \
"v_lshlrev_b32_e32 v182, 16, v79\n" \
"v_and_b32_e32 v183, s43, v79\n" \
"v_pk_fma_f32 v[152:153], v[152:153], v[160:161], v[176:177]\n" \
"v_pk_fma_f32 v[154:155], v[154:155], v[162:163], v[178:179]\n" \
"v_pk_fma_f32 v[156:157], v[156:157], v[164:165], v[180:181]\n" \
"v_pk_fma_f32 v[158:159], v[158:159], v[166:167], v[182:183]\n" \
"v_lshlrev_b32_e32 v168, 16, v90\n" \
"v_and_b32_e32 v169, s43, v90\n" \
"v_lshlrev_b32_e32 v170, 16, v91\n" \
"v_and_b32_e32 v171, s43, v91\n" \
"v_lshlrev_b32_e32 v172, 16, v94\n" \
"v_and_b32_e32 v173, s43, v94\n" \
"v_lshlrev_b32_e32 v174, 16, v95\n" \
"v_and_b32_e32 v175, s43, v95\n" \
"v_pk_fma_f32 v[152:153], v[152:153], v[168:169], v[48:49]\n" \
"v_pk_fma_f32 v[154:155], v[154:155], v[170:171], v[50:51]\n" \
"v_pk_fma_f32 v[156:157], v[156:157], v[172:173], v[56:57]\n" \
"v_pk_fma_f32 v[158:159], v[158:159], v[174:175], v[58:59]\n" \
"v_cvt_pk_bf16_f32 v152, v152, v153\n" \
"v_cvt_pk_bf16_f32 v154, v154, v155\n" \
"v_cvt_pk_bf16_f32 v156, v156, v157\n" \
"v_cvt_pk_bf16_f32 v158, v158, v159\n" \
"s_or_b64 %[lat], s[56:57], s[58:59]\n" \
"s_mov_b32 vcc_lo, 0x55555555\n" \
"s_mov_b32 vcc_hi, 0x55555555\n" \
"v_bfrev_b32_e32 v208, 1\n" \
"v_cndmask_b32_e64 v208, %[vd0], v208, %[lat]\n" \
"v_cndmask_b32_dpp v96, v136, v128, vcc quad_perm:[1,0,3,2] row_mask:0xf bank_mask:0xf\n" \
"v_cndmask_b32_dpp v97, v138, v130, vcc quad_perm:[1,0,3,2] row_mask:0xf bank_mask:0xf\n" \
"v_cndmask_b32_dpp v100, v140, v132, vcc quad_perm:[1,0,3,2] row_mask:0xf bank_mask:0xf\n" \
"v_cndmask_b32_dpp v101, v142, v134, vcc quad_perm:[1,0,3,2] row_mask:0xf bank_mask:0xf\n" \
"v_cndmask_b32_dpp v104, v152, v144, vcc quad_perm:[1,0,3,2] row_mask:0xf bank_mask:0xf\n" \
"v_cndmask_b32_dpp v105, v154, v146, vcc quad_perm:[1,0,3,2] row_mask:0xf bank_mask:0xf\n" \
"v_cndmask_b32_dpp v108, v156, v148, vcc quad_perm:[1,0,3,2] row_mask:0xf bank_mask:0xf\n" \
"v_cndmask_b32_dpp v109, v158, v150, vcc quad_perm:[1,0,3,2] row_mask:0xf bank_mask:0xf\n" \
"s_not_b64 vcc, vcc\n" \
"v_xor_b32_e32 v209, 0x80, v208\n" \
"v_cndmask_b32_dpp v98, v128, v136, vcc quad_perm:[1,0,3,2] row_mask:0xf bank_mask:0xf\n" \
"v_cndmask_b32_dpp v99, v130, v138, vcc quad_perm:[1,0,3,2] row_mask:0xf bank_mask:0xf\n" \
"v_cndmask_b32_dpp v102, v132, v140, vcc quad_perm:[1,0,3,2] row_mask:0xf bank_mask:0xf\n" \
"v_cndmask_b32_dpp v103, v134, v142, vcc quad_perm:[1,0,3,2] row_mask:0xf bank_mask:0xf\n" \
"v_cndmask_b32_dpp v106, v144, v152, vcc quad_perm:[1,0,3,2] row_mask:0xf bank_mask:0xf\n" \
"v_cndmask_b32_dpp v107, v146, v154, vcc quad_perm:[1,0,3,2] row_mask:0xf bank_mask:0xf\n" \
"v_cndmask_b32_dpp v110, v148, v156, vcc quad_perm:[1,0,3,2] row_mask:0xf bank_mask:0xf\n" \
"v_cndmask_b32_dpp v111, v150, v158, vcc quad_perm:[1,0,3,2] row_mask:0xf bank_mask:0xf\n" \
"buffer_store_dwordx4 v[96:99], v208, s[64:67], 0 offen offset:0 sc1\n" \
"buffer_store_dwordx4 v[100:103], v208, s[64:67], 0 offen offset:1024 sc1\n" \
"buffer_store_dwordx4 v[104:107], v209, s[64:67], 0 offen offset:0 sc1\n" \
"buffer_store_dwordx4 v[108:111], v209, s[64:67], 0 offen offset:1024 sc1\n" \
"TLls_%=:\n" \
"v_readlane_b32 s63, %[tlo], 28\n" \
"v_accvgpr_read_b32 v124, a32\n" \
"v_accvgpr_read_b32 v214, a33\n" \
"v_accvgpr_read_b32 v73, a34\n" \
"s_add_u32 s62, s40, 65536\n" \
"v_lshl_add_u32 v125, v125, 11, v214\n" \
"v_mov_b32_e32 v72, s40\n" \
"v_lshl_add_u32 v125, v125, 1, s62\n" \
"v_add_u32_e32 v214, s40, v214\n" \
"v_and_b32_e32 v74, 1, v222\n" \
"s_add_u32 s58, s63, 0x100000\n" \
"s_lshl_b32 s59, s41, 2\n" \
"s_sub_u32 s59, s59, %[wid]\n" \
"s_cmp_eq_u32 s59, 1\n" \
"s_cselect_b32 s59, 1, 0\n" \
"s_cbranch_scc0 TLkw_%=\n" \
"s_waitcnt vmcnt(4)\n" \
"TLk2_%=:\n" \
"v_min3_u32 v64, v112, v113, v114\n" \
"v_min3_u32 v65, v115, v116, v117\n" \
"v_min3_u32 v64, v64, v118, v119\n" \
"v_min_u32_e32 v64, v64, v65\n" \
"v_cmp_eq_u32_e32 vcc, 0, v64\n" \
"s_cbranch_vccnz TLk1_%=\n" \
"TLkw_%=:\n" \
"s_waitcnt lgkmcnt(0)\n" \
"s_barrier\n" \
"buffer_load_dwordx4 v[128:131], v73, %[srdz], s63 offen offset:0 sc1\n" \
"buffer_load_dwordx4 v[132:135], v73, %[srdz], s58 offen offset:0 sc1\n" \
"ds_read_b32 v120, v125 offset:0\n" \
"ds_read_u16 v121, v214 offset:2048\n" \
"ds_read_u16 v212, v214 offset:0\n" \
"ds_read_u16 v123, v214 offset:4096\n" \
"ds_read_b128 v[68:71], v72 offset:14384\n" \
"s_cmp_eq_u32 s59, 0\n" \
"s_cbranch_scc0 TLg1_%=\n" \
"s_waitcnt lgkmcnt(0)\n" \
"v_lshlrev_b32_e32 v121, 16, v121\n" \
"v_lshlrev_b32_e32 v212, 16, v212\n" \
"v_lshlrev_b32_e32 v123, 16, v123\n" \
"v_sub_f32_e32 v213, v120, v4\n" \
"v_mul_f32_e32 v213, v213, v126\n" \
"v_fma_f32 v127, v213, v121, v212\n" \
"v_add_f32_e32 v120, v120, v191\n" \
"v_add_f32_e32 v64, v68, v69\n" \
"v_add_f32_e32 v65, v70, v71\n" \
"v_add_f32_e32 v122, v64, v65\n" \
"v_add_f32_e32 v122, v122, v20\n" \
"v_cvt_pk_bf16_f32 v122, 0, v122\n" \
"v_mul_f32_e32 v122, 0xbfb8aa3b, v122\n" \
"v_exp_f32_e32 v122, v122\n" \
"s_nop 0\n" \
"v_add_f32_e32 v122, 1.0, v122\n" \
"v_rcp_f32_e32 v122, v122\n" \
"s_nop 0\n" \
"v_cvt_pk_bf16_f32 v122, 0, v122\n" \
"TLg2_%=:\n" \
"s_lshl_b32 s62, %[wid], 5\n" \
"v_lshrrev_b32_e32 v66, 2, v222\n" \
"s_add_u32 s62, s62, s40\n" \
"v_and_b32_e32 v66, 12, v66\n" \
"v_lshl_or_b32 v66, v74, 4, v66\n" \
"v_add_u32_e32 v66, s62, v66\n" \
"TLg3_%=:\n" \
"s_waitcnt vmcnt(0)\n" \
"v_bitop3_b32 v65, v132, v133, v134 bitop3:0x80\n" \
"v_bitop3_b32 v67, v128, v129, v130 bitop3:0x80\n" \
"v_bitop3_b32 v65, v65, v135, v131 bitop3:0x80\n" \
"v_pk_add_f32 v[128:129], v[128:129], v[132:133]\n" \
"v_bitop3_b32 v65, v65, v67, s41 bitop3:0x80\n" \
"v_pk_add_f32 v[130:131], v[130:131], v[134:135]\n" \
"v_cmp_eq_u32_e32 vcc, 0, v65\n" \
"s_nop 0\n" \
"v_permlane32_swap_b32_e32 v128, v130\n" \
"v_permlane32_swap_b32_e32 v129, v131\n" \
"v_add_f32_e32 v128, v128, v130\n" \
"v_add_f32_e32 v129, v129, v131\n" \
"s_cbranch_vccnz TLga_%=\n" \
"TLgb_%=:\n" \
"s_nop 0\n" \
"v_permlane16_swap_b32_e32 v128, v129\n" \
"v_add_f32_e32 v128, v128, v129\n" \
"s_nop 1\n" \
"v_add_f32_dpp v128, v128, v128 row_ror:8 row_mask:0xf bank_mask:0xf\n" \
"s_nop 1\n" \
"v_add_f32_dpp v128, v128, v128 row_ror:4 row_mask:0xf bank_mask:0xf\n" \
"s_nop 1\n" \
"v_add_f32_dpp v128, v128, v128 quad_perm:[2,3,0,1] row_mask:0xf bank_mask:0xf\n" \
"ds_write_b32 v66, v128 offset:29696\n" \
"v_and_b32_e32 v65, 7, v222\n" \
"v_mov_b32_e32 v74, s60\n" \
"v_lshl_add_u32 v65, v65, 2, s40\n" \
"v_mov_b32_e32 v75, s61\n" \
"s_mov_b32 vcc_lo, 0xaaaaaaaa\n" \
"s_min_u32 s56, s60, s61\n" \
"v_cndmask_b32_e32 v74, v74, v75, vcc\n" \
"s_max_u32 s57, s60, s61\n" \
"v_lshl_or_b32 v74, v74, 10, v124\n" \
"v_lshlrev_b32_e32 v74, 1, v74\n" \
"s_waitcnt lgkmcnt(0)\n" \
"s_barrier\n" \
"ds_read_b64 v[66:67], v72 offset:14432\n" \
"ds_read_b32 v68, v65 offset:29696\n" \
"ds_read_b32 v69, v65 offset:29728\n" \
"ds_read_b32 v70, v65 offset:29760\n" \
"ds_read_b32 v71, v65 offset:29792\n" \
"s_waitcnt lgkmcnt(4)\n" \
"v_readfirstlane_b32 %[k2], v66\n" \
"v_readfirstlane_b32 %[k1], v67\n" \
"s_waitcnt lgkmcnt(0)\n" \
"v_add_f32_e32 v68, v68, v69\n" \
"v_add_f32_e32 v70, v70, v71\n" \
"s_andn2_b32 %[k1], 0x7ff, %[k1]\n" \
"s_andn2_b32 %[k2], 0x7ff, %[k2]\n" \
"v_add_f32_e32 v68, v68, v70\n" \
"s_min_u32 s62, %[k1], %[k2]\n" \
"s_max_u32 s63, %[k1], %[k2]\n" \
"v_fma_f32 v68, v68, v122, v127\n" \
"s_cmp_lg_u64 s[62:63], s[56:57]\n" \
"s_cselect_b32 %[got], 2, 0\n" \
"v_fma_f32 v68, v68, v123, v120\n" \
"s_or_b32 %[got], %[got], s53\n" \
"s_cbranch_scc1 TLagain_%=\n" \
"TLfin_%=:\n" \
"s_lshr_b32 %[got], s41, 1\n" \
"s_cmp_lg_u32 %[wid], 0\n" \
"s_cselect_b32 s58, 0x80000000, 0\n" \
"v_cvt_pk_bf16_f32 v68, v68, v68\n" \
"s_mov_b64 exec, 0xff\n" \
"buffer_store_short v68, v74, s[64:67], s58 offen offset:0 sc1\n" \
"s_mov_b64 exec, -1\n" \
"s_branch Lend_%=\n" \
"TLpk_%=:\n" \
"v_readlane_b32 s62, %[tlo], 35\n" \
"v_lshrrev_b32_e32 v72, 2, v224\n" \
"s_mov_b32 s51, 0x10000\n" \
"s_nop 4\n" \
"TLpk1_%=:\n" \
"buffer_load_dwordx4 v[64:67], v72, %[srdz], s62 offen offset:0 sc1\n" \
"buffer_load_dwordx4 v[68:71], v72, %[srdz], s62 offen offset:16 sc1\n" \
"s_waitcnt vmcnt(0)\n" \
"v_min3_u32 v208, v64, v65, v66\n" \
"v_min3_u32 v209, v67, v68, v69\n" \
"v_min3_u32 v208, v208, v70, v71\n" \
"v_min_u32_e32 v208, v208, v209\n" \
"v_cmp_eq_u32_e32 vcc, 0, v208\n" \
"s_sub_u32 s51, s51, 1\n" \
"s_cbranch_scc1 TLpkb_%=\n" \
"s_cbranch_vccz TLpkb_%=\n" \
"s_sleep 8\n" \
"s_branch TLpk1_%=\n" \
"TLrare_%=:\n" \
"v_cmp_u_f32_e32 vcc, v14, v15\n" \
"s_cbranch_vccz TLvar_%=\n" \
"s_sub_u32 s52, s52, 1\n" \
"s_cbranch_scc1 TLvar_%=\n" \
"buffer_load_dwordx4 a[164:167], v217, %[srdz], s54 offen offset:0 sc1\n" \
"buffer_load_dwordx4 a[168:171], v217, %[srdz], s54 offen offset:1024 sc1\n" \
"buffer_load_dwordx4 a[172:175], v217, %[srdz], s54 offen offset:2048 sc1\n" \
"buffer_load_dwordx4 a[176:179], v217, %[srdz], s54 offen offset:3072 sc1\n" \
"buffer_load_dwordx4 a[180:183], v217, %[srdz], s55 offen offset:0 sc1\n" \
"buffer_load_dwordx4 a[184:187], v217, %[srdz], s55 offen offset:1024 sc1\n" \
"buffer_load_dwordx4 a[188:191], v217, %[srdz], s55 offen offset:2048 sc1\n" \
"buffer_load_dwordx4 a[192:195], v217, %[srdz], s55 offen offset:3072 sc1\n" \
"v_accvgpr_read_b32 v33, a248\n" \
"v_lshlrev_b32_e32 v32, 16, v33\n" \
"v_and_b32_e32 v33, s43, v33\n" \
"v_accvgpr_read_b32 v35, a249\n" \
"v_lshlrev_b32_e32 v34, 16, v35\n" \
"v_and_b32_e32 v35, s43, v35\n" \
"v_accvgpr_read_b32 v41, a252\n" \
"v_lshlrev_b32_e32 v40, 16, v41\n" \
"v_and_b32_e32 v41, s43, v41\n" \
"v_accvgpr_read_b32 v43, a253\n" \
"v_lshlrev_b32_e32 v42, 16, v43\n" \
"v_and_b32_e32 v43, s43, v43\n" \
"v_accvgpr_read_b32 v49, a250\n" \
"v_lshlrev_b32_e32 v48, 16, v49\n" \
"v_and_b32_e32 v49, s43, v49\n" \
"v_accvgpr_read_b32 v51, a251\n" \
"v_lshlrev_b32_e32 v50, 16, v51\n" \
"v_and_b32_e32 v51, s43, v51\n" \
"v_accvgpr_read_b32 v57, a254\n" \
"v_lshlrev_b32_e32 v56, 16, v57\n" \
"v_and_b32_e32 v57, s43, v57\n" \
"v_accvgpr_read_b32 v59, a255\n" \
"v_lshlrev_b32_e32 v58, 16, v59\n" \
"v_and_b32_e32 v59, s43, v59\n" \
"v_pk_mul_f32 v[6:7], v[0:1], v[160:161]\n" \
"v_pk_mul_f32 v[12:13], v[0:1], v[0:1]\n" \
"v_pk_add_f32 v[4:5], v[0:1], v[2:3]\n" \
"v_pk_fma_f32 v[6:7], v[2:3], v[162:163], v[6:7]\n" \
"v_pk_fma_f32 v[12:13], v[2:3], v[2:3], v[12:13]\n" \
"v_pk_add_f32 v[4:5], v[4:5], v[8:9]\n" \
"v_pk_fma_f32 v[6:7], v[8:9], v[168:169], v[6:7]\n" \
"v_pk_fma_f32 v[12:13], v[8:9], v[8:9], v[12:13]\n" \
"v_pk_add_f32 v[4:5], v[4:5], v[10:11]\n" \
"v_pk_fma_f32 v[6:7], v[10:11], v[170:171], v[6:7]\n" \
"v_pk_fma_f32 v[12:13], v[10:11], v[10:11], v[12:13]\n" \
"v_pk_add_f32 v[4:5], v[4:5], v[16:17]\n" \
"v_pk_fma_f32 v[6:7], v[16:17], v[164:165], v[6:7]\n" \
"v_pk_fma_f32 v[12:13], v[16:17], v[16:17], v[12:13]\n" \
"v_pk_add_f32 v[4:5], v[4:5], v[18:19]\n" \
"v_pk_fma_f32 v[6:7], v[18:19], v[166:167], v[6:7]\n" \
"v_pk_fma_f32 v[12:13], v[18:19], v[18:19], v[12:13]\n" \
"v_pk_add_f32 v[4:5], v[4:5], v[24:25]\n" \
"v_pk_fma_f32 v[6:7], v[24:25], v[172:173], v[6:7]\n" \
"v_pk_fma_f32 v[12:13], v[24:25], v[24:25], v[12:13]\n" \
"v_pk_add_f32 v[4:5], v[4:5], v[26:27]\n" \
"v_pk_fma_f32 v[6:7], v[26:27], v[174:175], v[6:7]\n" \
"v_pk_fma_f32 v[12:13], v[26:27], v[26:27], v[12:13]\n" \
"s_waitcnt vmcnt(0)\n" \
"s_nop 1\n" \
"v_mfma_f32_16x16x32_bf16 v[208:211], v[230:233], a[164:167], 0\n" \
"v_mfma_f32_16x16x32_bf16 v[212:215], v[230:233], a[172:175], 0\n" \
"v_mfma_f32_16x16x32_bf16 v[208:211], v[234:237], a[168:171], v[208:211]\n" \
"v_mfma_f32_16x16x32_bf16 v[212:215], v[234:237], a[176:179], v[212:215]\n" \
"s_nop 6\n" \
"v_pk_fma_f32 v[32:33], v[208:209], v[144:145], v[32:33]\n" \
"v_pk_fma_f32 v[34:35], v[210:211], v[146:147], v[34:35]\n" \
"v_pk_fma_f32 v[40:41], v[212:213], v[152:153], v[40:41]\n" \
"v_pk_fma_f32 v[42:43], v[214:215], v[154:155], v[42:43]\n" \
"v_pk_mul_f32 v[36:37], v[32:33], v[176:177]\n" \
"v_pk_mul_f32 v[38:39], v[32:33], v[32:33]\n" \
"v_pk_add_f32 v[14:15], v[32:33], v[34:35]\n" \
"v_pk_fma_f32 v[36:37], v[34:35], v[178:179], v[36:37]\n" \
"v_pk_fma_f32 v[38:39], v[34:35], v[34:35], v[38:39]\n" \
"v_pk_add_f32 v[14:15], v[14:15], v[40:41]\n" \
"v_pk_fma_f32 v[36:37], v[40:41], v[184:185], v[36:37]\n" \
"v_pk_fma_f32 v[38:39], v[40:41], v[40:41], v[38:39]\n" \
"v_pk_add_f32 v[14:15], v[14:15], v[42:43]\n" \
"v_pk_fma_f32 v[36:37], v[42:43], v[186:187], v[36:37]\n" \
"v_pk_fma_f32 v[38:39], v[42:43], v[42:43], v[38:39]\n" \
"s_nop 7\n" \
".if . - adaln_body != 69744\n" \
".error \"size model\"\n" \
".endif\n" \
"Lk16_%=:\n" \
"s_cbranch_execz adaln_kr\n" \
"s_nop 0\n" \
"v_mfma_f32_16x16x32_bf16 v[208:211], v[230:233], a[180:183], 0\n" \
"v_mfma_f32_16x16x32_bf16 v[212:215], v[230:233], a[188:191], 0\n" \
"v_mfma_f32_16x16x32_bf16 v[208:211], v[234:237], a[184:187], v[208:211]\n" \
"v_mfma_f32_16x16x32_bf16 v[212:215], v[234:237], a[192:195], v[212:215]\n" \
"s_nop 6\n" \
"v_pk_fma_f32 v[48:49], v[208:209], v[148:149], v[48:49]\n" \
"v_pk_fma_f32 v[50:51], v[210:211], v[150:151], v[50:51]\n" \
"v_pk_fma_f32 v[56:57], v[212:213], v[156:157], v[56:57]\n" \
"v_pk_fma_f32 v[58:59], v[214:215], v[158:159], v[58:59]\n" \
"v_pk_add_f32 v[14:15], v[14:15], v[48:49]\n" \
"v_pk_fma_f32 v[36:37], v[48:49], v[180:181], v[36:37]\n" \
"v_pk_fma_f32 v[38:39], v[48:49], v[48:49], v[38:39]\n" \
"v_pk_add_f32 v[14:15], v[14:15], v[50:51]\n" \
"v_pk_fma_f32 v[36:37], v[50:51], v[182:183], v[36:37]\n" \
"v_pk_fma_f32 v[38:39], v[50:51], v[50:51], v[38:39]\n" \
"v_pk_add_f32 v[14:15], v[14:15], v[56:57]\n" \
"v_pk_fma_f32 v[36:37], v[56:57], v[188:189], v[36:37]\n" \
"v_pk_fma_f32 v[38:39], v[56:57], v[56:57], v[38:39]\n" \
"v_pk_add_f32 v[14:15], v[14:15], v[58:59]\n" \
"v_pk_fma_f32 v[36:37], v[58:59], v[190:191], v[36:37]\n" \
"v_pk_fma_f32 v[38:39], v[58:59], v[58:59], v[38:39]\n" \
"v_pk_add_f32 v[4:5], v[4:5], v[14:15]\n" \
"v_pk_add_f32 v[6:7], v[6:7], v[36:37]\n" \
"v_pk_add_f32 v[12:13], v[12:13], v[38:39]\n" \
"v_add_f32_e32 v4, v4, v5\n" \
"v_add_f32_e32 v6, v6, v7\n" \
"v_add_f32_e32 v12, v12, v13\n" \
"v_mov_b32_e32 v208, v12\n" \
"v_permlane32_swap_b32_e32 v4, v6\n" \
"s_nop 0\n" \
"v_permlane32_swap_b32_e32 v12, v208\n" \
"v_add_f32_e32 v4, v4, v6\n" \
"v_add_f32_e32 v12, v12, v208\n" \
"s_nop 0\n" \
"s_nop 0\n" \
"v_permlane16_swap_b32_e32 v4, v12\n" \
"v_add_f32_e32 v4, v4, v12\n" \
"s_nop 1\n" \
"v_add_f32_dpp v4, v4, v4 quad_perm:[1,0,3,2] row_mask:0xf bank_mask:0xf\n" \
"s_nop 1\n" \
"v_add_f32_dpp v4, v4, v4 quad_perm:[2,3,0,1] row_mask:0xf bank_mask:0xf\n" \
"s_nop 1\n" \
"v_add_f32_dpp v4, v4, v4 row_ror:8 row_mask:0xf bank_mask:0xf\n" \
"v_mov_b32_e32 v12, v4\n" \
"s_nop 0\n" \
"s_nop 0\n" \
"v_permlane16_swap_b32_e32 v4, v12\n" \
"v_mov_b32_e32 v6, v4\n" \
"v_mul_f32_e32 v12, 0x3a800000, v12\n" \
"s_nop 0\n" \
"v_permlane32_swap_b32_e32 v4, v6\n" \
"v_mul_f32_e32 v218, 0x3a800000, v4\n" \
"v_mul_f32_e32 v208, v218, v218\n" \
"v_sub_f32_e32 v12, v12, v208\n" \
"v_cmp_nle_f32_e32 vcc, v208, v12\n" \
"v_add_f32_e32 v12, 0x3727c5ac, v12\n" \
"v_rsq_f32_e32 v220, v12\n" \
"v_fma_f32 v210, -v218, v223, v6\n" \
"s_mov_b64 exec, 17\n" \
"v_mul_f32_e32 v210, v210, v220\n" \
"v_ashrrev_i32_e32 v208, 31, v210\n" \
"v_or_b32_e32 v208, 0x80000000, v208\n" \
"v_xor_b32_e32 v210, v210, v208\n" \
"v_and_b32_e32 v210, 0xfffff800, v210\n" \
"v_max_u32_e32 v210, 0x800, v210\n" \
"v_or_b32_e32 v210, v210, %[ta0]\n" \
"ds_write_b32 v216, v210 offset:14400\n" \
"s_mov_b64 exec, -1\n" \
"s_cbranch_vccnz TLrare_%=\n" \
"s_branch TLrok_%=\n" \
"TLvar_%=:\n" \
"v_sub_f32_e32 v210, 0, v218\n" \
"v_pk_add_f32 v[212:213], v[0:1], v[210:211] op_sel_hi:[1,0]\n" \
"v_pk_mul_f32 v[214:215], v[212:213], v[212:213]\n" \
"v_pk_add_f32 v[212:213], v[2:3], v[210:211] op_sel_hi:[1,0]\n" \
"v_pk_fma_f32 v[214:215], v[212:213], v[212:213], v[214:215]\n" \
"v_pk_add_f32 v[212:213], v[8:9], v[210:211] op_sel_hi:[1,0]\n" \
"v_pk_fma_f32 v[214:215], v[212:213], v[212:213], v[214:215]\n" \
"v_pk_add_f32 v[212:213], v[10:11], v[210:211] op_sel_hi:[1,0]\n" \
"v_pk_fma_f32 v[214:215], v[212:213], v[212:213], v[214:215]\n" \
"v_pk_add_f32 v[212:213], v[16:17], v[210:211] op_sel_hi:[1,0]\n" \
"v_pk_fma_f32 v[214:215], v[212:213], v[212:213], v[214:215]\n" \
"v_pk_add_f32 v[212:213], v[18:19], v[210:211] op_sel_hi:[1,0]\n" \
"v_pk_fma_f32 v[214:215], v[212:213], v[212:213], v[214:215]\n" \
"v_pk_add_f32 v[212:213], v[24:25], v[210:211] op_sel_hi:[1,0]\n" \
"v_pk_fma_f32 v[214:215], v[212:213], v[212:213], v[214:215]\n" \
"v_pk_add_f32 v[212:213], v[26:27], v[210:211] op_sel_hi:[1,0]\n" \
"v_pk_fma_f32 v[214:215], v[212:213], v[212:213], v[214:215]\n" \
"v_pk_add_f32 v[212:213], v[32:33], v[210:211] op_sel_hi:[1,0]\n" \
"v_pk_fma_f32 v[214:215], v[212:213], v[212:213], v[214:215]\n" \
"v_pk_add_f32 v[212:213], v[34:35], v[210:211] op_sel_hi:[1,0]\n" \
"v_pk_fma_f32 v[214:215], v[212:213], v[212:213], v[214:215]\n" \
"v_pk_add_f32 v[212:213], v[40:41], v[210:211] op_sel_hi:[1,0]\n" \
"v_pk_fma_f32 v[214:215], v[212:213], v[212:213], v[214:215]\n" \
"v_pk_add_f32 v[212:213], v[42:43], v[210:211] op_sel_hi:[1,0]\n" \
"v_pk_fma_f32 v[214:215], v[212:213], v[212:213], v[214:215]\n" \
"v_pk_add_f32 v[212:213], v[48:49], v[210:211] op_sel_hi:[1,0]\n" \
"v_pk_fma_f32 v[214:215], v[212:213], v[212:213], v[214:215]\n" \
"v_pk_add_f32 v[212:213], v[50:51], v[210:211] op_sel_hi:[1,0]\n" \
"v_pk_fma_f32 v[214:215], v[212:213], v[212:213], v[214:215]\n" \
"v_pk_add_f32 v[212:213], v[56:57], v[210:211] op_sel_hi:[1,0]\n" \
"v_pk_fma_f32 v[214:215], v[212:213], v[212:213], v[214:215]\n" \
"v_pk_add_f32 v[212:213], v[58:59], v[210:211] op_sel_hi:[1,0]\n" \
"v_pk_fma_f32 v[214:215], v[212:213], v[212:213], v[214:215]\n" \
"v_add_f32_e32 v12, v214, v215\n" \
"s_nop 1\n" \
"v_add_f32_dpp v12, v12, v12 quad_perm:[1,0,3,2] row_mask:0xf bank_mask:0xf\n" \
"s_nop 1\n" \
"v_add_f32_dpp v12, v12, v12 quad_perm:[2,3,0,1] row_mask:0xf bank_mask:0xf\n" \
"s_nop 1\n" \
"v_add_f32_dpp v12, v12, v12 row_ror:8 row_mask:0xf bank_mask:0xf\n" \
"v_mov_b32_e32 v210, v12\n" \
"s_nop 0\n" \
"s_nop 0\n" \
"v_permlane16_swap_b32_e32 v12, v210\n" \
"v_add_f32_e32 v12, v12, v210\n" \
"v_mov_b32_e32 v210, v12\n" \
"s_nop 0\n" \
"s_nop 0\n" \
"v_permlane32_swap_b32_e32 v12, v210\n" \
"v_add_f32_e32 v12, v12, v210\n" \
"v_mul_f32_e32 v12, 0x3a800000, v12\n" \
"v_add_f32_e32 v12, 0x3727c5ac, v12\n" \
"v_rsq_f32_e32 v220, v12\n" \
"v_fma_f32 v210, -v218, v223, v6\n" \
"s_mov_b64 exec, 17\n" \
"v_mul_f32_e32 v210, v210, v220\n" \
"v_ashrrev_i32_e32 v208, 31, v210\n" \
"v_or_b32_e32 v208, 0x80000000, v208\n" \
"v_xor_b32_e32 v210, v210, v208\n" \
"v_and_b32_e32 v210, 0xfffff800, v210\n" \
"v_max_u32_e32 v210, 0x800, v210\n" \
"v_or_b32_e32 v210, v210, %[ta0]\n" \
"ds_write_b32 v216, v210 offset:14400\n" \
"s_mov_b64 exec, -1\n" \
"s_branch TLrok_%=\n" \
"TLcw_%=:\n" \
"s_waitcnt lgkmcnt(0)\n" \
"s_mov_b32 s63, 278528\n" \
"s_mov_b32 s51, 0x10000\n" \
"TLcw1_%=:\n" \
"buffer_load_dword %[vxs], %[vdn], %[srdz], s63 offen offset:0 sc1\n" \
"s_waitcnt vmcnt(0)\n" \
"v_and_b32_e32 v44, 0x820, %[vxs]\n" \
"v_cmp_ne_u32_e32 vcc, 0x820, v44\n" \
"s_sub_u32 s51, s51, 1\n" \
"s_cbranch_scc1 TLcw2_%=\n" \
"s_nop 3\n" \
"s_cbranch_vccz TLcw2_%=\n" \
"s_sleep 8\n" \
"s_branch TLcw1_%=\n" \
"TLcw2_%=:\n" \
"s_sleep 4\n" \
"s_mov_b32 s59, 1\n" \
"buffer_load_dwordx4 a[128:131], %[vct], %[srdz], 0 offen offset:0 sc1\n" \
"buffer_load_dwordx4 a[132:135], %[rd0], %[srdz], 0 offen offset:0 sc1\n" \
"buffer_load_dwordx4 a[136:139], %[vct], %[srdz], s56 offen offset:0 sc1\n" \
"buffer_load_dwordx4 a[140:143], %[rd0], %[srdz], s56 offen offset:0 sc1\n" \
"buffer_load_dwordx4 a[144:147], %[vct], %[srdz], s57 offen offset:0 sc1\n" \
"buffer_load_dwordx4 a[148:151], %[rd0], %[srdz], s57 offen offset:0 sc1\n" \
"buffer_load_dwordx4 a[152:155], %[vct], %[srdz], s58 offen offset:0 sc1\n" \
"buffer_load_dwordx4 a[156:159], %[rd0], %[srdz], s58 offen offset:0 sc1\n" \
"s_waitcnt vmcnt(0)\n" \
"s_branch TLcs_%=\n" \
"TLcx_%=:\n" \
"s_cmp_eq_u32 s59, 0\n" \
"s_cbranch_scc1 TLcw_%=\n" \
"s_sub_u32 s52, s52, 1\n" \
"s_cbranch_scc1 TLco_%=\n" \
"buffer_load_dwordx4 a[128:131], %[vct], %[srdz], 0 offen offset:0 sc1\n" \
"buffer_load_dwordx4 a[132:135], %[rd0], %[srdz], 0 offen offset:0 sc1\n" \
"buffer_load_dwordx4 a[136:139], %[vct], %[srdz], s56 offen offset:0 sc1\n" \
"buffer_load_dwordx4 a[140:143], %[rd0], %[srdz], s56 offen offset:0 sc1\n" \
"buffer_load_dwordx4 a[144:147], %[vct], %[srdz], s57 offen offset:0 sc1\n" \
"buffer_load_dwordx4 a[148:151], %[rd0], %[srdz], s57 offen offset:0 sc1\n" \
"buffer_load_dwordx4 a[152:155], %[vct], %[srdz], s58 offen offset:0 sc1\n" \
"buffer_load_dwordx4 a[156:159], %[rd0], %[srdz], s58 offen offset:0 sc1\n" \
"s_waitcnt vmcnt(0) lgkmcnt(0)\n" \
"s_branch TLcs_%=\n" \
"TLk1_%=:\n" \
"s_sub_u32 s51, s51, 1\n" \
"s_cbranch_scc1 TLkw_%=\n" \
"s_sleep 3\n" \
"buffer_load_dwordx4 v[112:115], v216, %[srdz], s49 offen offset:0 sc1\n" \
"buffer_load_dwordx4 v[116:119], v216, %[srdz], s49 offen offset:16 sc1\n" \
"s_waitcnt vmcnt(0)\n" \
"s_branch TLk2_%=\n" \
"TLg1_%=:\n" \
"s_waitcnt lgkmcnt(0)\n" \
"v_min_u32_e32 v64, v113, v115\n" \
"v_min_u32_e32 v65, v117, v119\n" \
"v_max_u32_e32 v113, v113, v115\n" \
"v_max_u32_e32 v117, v117, v119\n" \
"v_max3_u32 v112, v64, v112, v114\n" \
"v_max3_u32 v116, v65, v116, v118\n" \
"v_min_u32_e32 v64, v113, v117\n" \
"v_max_u32_e32 v113, v113, v117\n" \
"v_max3_u32 v112, v64, v112, v116\n" \
"s_nop 0\n" \
"v_min_u32_dpp v64, v113, v113 quad_perm:[1,0,3,2] row_mask:0xf bank_mask:0xf\n" \
"v_max_u32_dpp v65, v112, v112 quad_perm:[1,0,3,2] row_mask:0xf bank_mask:0xf\n" \
"v_max_u32_dpp v113, v113, v113 quad_perm:[1,0,3,2] row_mask:0xf bank_mask:0xf\n" \
"v_max_u32_e32 v112, v64, v65\n" \
"s_nop 0\n" \
"v_min_u32_dpp v64, v113, v113 quad_perm:[2,3,0,1] row_mask:0xf bank_mask:0xf\n" \
"v_max_u32_dpp v65, v112, v112 quad_perm:[2,3,0,1] row_mask:0xf bank_mask:0xf\n" \
"v_max_u32_dpp v113, v113, v113 quad_perm:[2,3,0,1] row_mask:0xf bank_mask:0xf\n" \
"v_max_u32_e32 v112, v64, v65\n" \
"s_nop 0\n" \
"v_min_u32_dpp v64, v113, v113 row_half_mirror row_mask:0xf bank_mask:0xf\n" \
"v_max_u32_dpp v65, v112, v112 row_half_mirror row_mask:0xf bank_mask:0xf\n" \
"v_max_u32_dpp v113, v113, v113 row_half_mirror row_mask:0xf bank_mask:0xf\n" \
"v_max_u32_e32 v112, v64, v65\n" \
"s_nop 0\n" \
"v_min_u32_dpp v64, v113, v113 row_mirror row_mask:0xf bank_mask:0xf\n" \
"v_max_u32_dpp v65, v112, v112 row_mirror row_mask:0xf bank_mask:0xf\n" \
"v_max_u32_dpp v113, v113, v113 row_mirror row_mask:0xf bank_mask:0xf\n" \
"v_max_u32_e32 v112, v64, v65\n" \
"v_mov_b32_e32 v115, v113\n" \
"v_mov_b32_e32 v114, v112\n" \
"s_nop 0\n" \
"v_permlane16_swap_b32_e32 v113, v115\n" \
"v_permlane16_swap_b32_e32 v112, v114\n" \
"v_min_u32_e32 v64, v113, v115\n" \
"v_max_u32_e32 v113, v113, v115\n" \
"v_max3_u32 v112, v64, v112, v114\n" \
"v_mov_b32_e32 v115, v113\n" \
"v_mov_b32_e32 v114, v112\n" \
"s_nop 0\n" \
"v_permlane32_swap_b32_e32 v113, v115\n" \
"v_permlane32_swap_b32_e32 v112, v114\n" \
"v_min_u32_e32 v64, v113, v115\n" \
"v_max_u32_e32 v113, v113, v115\n" \
"v_max3_u32 v112, v64, v112, v114\n" \
"s_mov_b64 exec, 1\n" \
"ds_write_b64 v72, v[112:113] offset:14432\n" \
"s_mov_b64 exec, -1\n" \
"s_branch TLg2_%=\n" \
"TLga_%=:\n" \
"s_sub_u32 s51, s51, 1\n" \
"s_cbranch_scc1 TLgb_%=\n" \
"buffer_load_dwordx4 v[128:131], v73, %[srdz], s63 offen offset:0 sc1\n" \
"buffer_load_dwordx4 v[132:135], v73, %[srdz], s58 offen offset:0 sc1\n" \
"s_branch TLg3_%=\n" \
"TLagain_%=:\n" \
"s_cmp_eq_u32 s41, 4\n" \
"s_cbranch_scc1 TLfin_%=\n" \
"s_lshl_b32 s41, s41, 1\n" \
"v_add_f32_e32 v191, v191, v4\n" \
"v_lshl_or_b32 v227, v227, 1, 1\n" \
"v_readlane_b32 s62, v191, 0\n" \
"v_readlane_b32 s63, v191, 1\n" \
"s_bitcmp1_b32 %[got], 1\n" \
"s_cselect_b32 s44, %[k1], s60\n" \
"s_cselect_b32 s45, %[k2], s61\n" \
"s_cselect_b32 s62, 0, s62\n" \
"s_cselect_b32 s63, 0, s63\n" \
"s_mov_b32 s46, s44\n" \
"s_mov_b32 s47, s45\n" \
"v_mov_b32_e32 v208, s62\n" \
"v_mov_b32_e32 v209, s63\n" \
"s_mov_b32 vcc_lo, 0xaaaaaaaa\n" \
"s_mov_b32 vcc_hi, 0xaaaaaaaa\n" \
"v_cndmask_b32_e32 v191, v208, v209, vcc\n" \
"s_mov_b32 vcc_lo, 0xf0f0f0f0\n" \
"s_mov_b32 vcc_hi, 0xf0f0f0f0\n" \
"v_cndmask_b32_e32 v190, v208, v209, vcc\n" \
"s_mov_b32 vcc_lo, 0xff00ff\n" \
"s_mov_b32 vcc_hi, 0xff00ff\n" \
"v_cndmask_b32_e64 v225, 0, -1, vcc\n" \
"v_sub_u32_e32 v216, 0x7ff, %[ta0]\n" \
"v_cmp_ne_u32_e64 s[56:57], s44, v216\n" \
"v_cmp_ne_u32_e64 s[58:59], s45, v216\n" \
"s_and_b64 s[56:57], s[56:57], s[58:59]\n" \
"s_and_b64 s[56:57], s[56:57], %[lat]\n" \
"s_mov_b64 %[lat], 0\n" \
"v_bfrev_b32_e32 v208, 1\n" \
"v_cndmask_b32_e64 v208, v208, %[vd0], s[56:57]\n" \
"v_xor_b32_e32 v209, 0x80, v208\n" \
"buffer_store_dwordx4 v[96:99], v208, s[64:67], 0 offen offset:0 sc1\n" \
"buffer_store_dwordx4 v[100:103], v208, s[64:67], 0 offen offset:1024 sc1\n" \
"buffer_store_dwordx4 v[104:107], v209, s[64:67], 0 offen offset:0 sc1\n" \
"buffer_store_dwordx4 v[108:111], v209, s[64:67], 0 offen offset:1024 sc1\n" \
"s_movk_i32 s56, 0x1000\n" \
"s_movk_i32 s57, 0x2000\n" \
"s_movk_i32 s58, 0x3000\n" \
"s_movk_i32 s52, 0x1000\n" \
"s_mov_b32 s59, 1\n" \
"s_nop 1\n" \
"v_and_b32_e32 v213, 4, v222\n" \
"v_mov_b32_e32 v208, s44\n" \
"v_cmp_ne_u32_e32 vcc, 0, v213\n" \
"v_mov_b32_e32 v214, s45\n" \
"v_mov_b32_e32 v209, s46\n" \
"v_mov_b32_e32 v215, s47\n" \
"v_cndmask_b32_e32 v208, v208, v214, vcc\n" \
"v_cndmask_b32_e32 v209, v209, v215, vcc\n" \
"v_cmp_lt_u32_e32 vcc, 31, v222\n" \
"v_and_b32_e32 v211, 0xf0, %[kb0]\n" \
"s_lshl_b32 s62, %[wid], 9\n" \
"v_cndmask_b32_e32 v210, v208, v209, vcc\n" \
"v_lshrrev_b32_e32 %[vdn], 6, v210\n" \
"v_lshlrev_b32_e32 %[vdn], 9, %[vdn]\n" \
"v_lshlrev_b32_e32 v213, 11, v210\n" \
"v_lshrrev_b32_e32 v214, 1, v210\n" \
"v_and_b32_e32 v214, 0x80, v214\n" \
"v_lshrrev_b32_e32 v215, 1, v211\n" \
"v_and_b32_e32 v215, 56, v215\n" \
"v_add3_u32 v213, v213, v214, v215\n" \
"v_and_b32_e32 v215, 0x80, v211\n" \
"v_lshlrev_b32_e32 v215, 1, v215\n" \
"v_add3_u32 v80, v213, v215, s62\n" \
"v_xor_b32_e32 v81, 0x80, v80\n" \
"s_mov_b32 s63, 278528\n" \
"buffer_load_dword %[vxs], %[vdn], %[srdz], s63 offen offset:0 sc1\n" \
"global_load_dwordx2 v[88:89], v80, %[px] offset:0\n" \
"global_load_dwordx2 v[90:91], v80, %[px] offset:64\n" \
"global_load_dwordx2 v[92:93], v81, %[px] offset:0\n" \
"global_load_dwordx2 v[94:95], v81, %[px] offset:64\n" \
"v_and_b32_e32 v213, 0x100, v210\n" \
"s_add_u32 s63, s62, s40\n" \
"v_add3_u32 v213, v213, v211, s63\n" \
"v_xor_b32_e32 v214, 0x100, v213\n" \
"ds_read_b128 v[104:107], v213 offset:2048\n" \
"ds_read_b128 v[108:111], v214 offset:2048\n" \
"ds_read_b128 v[64:67], v213 offset:6144\n" \
"ds_read_b128 v[68:71], v213 offset:8192\n" \
"ds_read_b128 v[72:75], v214 offset:6144\n" \
"ds_read_b128 v[76:79], v214 offset:8192\n" \
"ds_read_b128 v[112:115], v213 offset:10240\n" \
"ds_read_b128 v[116:119], v213 offset:12288\n" \
"ds_read_b128 v[120:123], v214 offset:10240\n" \
"ds_read_b128 v[124:127], v214 offset:12288\n" \
"s_movk_i32 s63, 2112\n" \
"v_mad_u32_u24 %[oq0], v219, s63, v213\n" \
"v_mad_u32_u24 %[wo0], v219, s63, v214\n" \
"s_lshl_b32 s62, %[wid], 10\n" \
"s_add_u32 s62, s62, 7340032\n" \
"v_and_b32_e32 v215, 0x3b0, v217\n" \
"v_add_u32_e32 v215, s62, v215\n" \
"s_mov_b32 s63, 38016\n" \
"v_lshrrev_b32_e32 %[vct], 1, v208\n" \
"v_and_b32_e32 v208, 1, v208\n" \
"v_mad_u32_u24 %[vct], %[vct], s63, v215\n" \
"v_lshl_add_u32 %[vct], v208, 6, %[vct]\n" \
"v_lshrrev_b32_e32 %[rd0], 1, v209\n" \
"v_and_b32_e32 v209, 1, v209\n" \
"v_mad_u32_u24 %[rd0], %[rd0], s63, v215\n" \
"v_lshl_add_u32 %[rd0], v209, 6, %[rd0]\n" \
"buffer_load_dwordx4 a[128:131], %[vct], %[srdz], 0 offen offset:0 sc1\n" \
"buffer_load_dwordx4 a[132:135], %[rd0], %[srdz], 0 offen offset:0 sc1\n" \
"buffer_load_dwordx4 a[136:139], %[vct], %[srdz], s56 offen offset:0 sc1\n" \
"buffer_load_dwordx4 a[140:143], %[rd0], %[srdz], s56 offen offset:0 sc1\n" \
"buffer_load_dwordx4 a[144:147], %[vct], %[srdz], s57 offen offset:0 sc1\n" \
"buffer_load_dwordx4 a[148:151], %[rd0], %[srdz], s57 offen offset:0 sc1\n" \
"buffer_load_dwordx4 a[152:155], %[vct], %[srdz], s58 offen offset:0 sc1\n" \
"buffer_load_dwordx4 a[156:159], %[rd0], %[srdz], s58 offen offset:0 sc1\n" \
"s_waitcnt vmcnt(0) lgkmcnt(0)\n" \
"v_lshlrev_b32_e32 v96, 16, v104\n" \
"v_and_b32_e32 v97, s43, v104\n" \
"v_lshlrev_b32_e32 v98, 16, v105\n" \
"v_and_b32_e32 v99, s43, v105\n" \
"v_lshlrev_b32_e32 v100, 16, v106\n" \
"v_and_b32_e32 v101, s43, v106\n" \
"v_lshlrev_b32_e32 v102, 16, v107\n" \
"v_and_b32_e32 v103, s43, v107\n" \
"v_lshlrev_b32_e32 v104, 16, v108\n" \
"v_and_b32_e32 v105, s43, v108\n" \
"v_lshlrev_b32_e32 v106, 16, v109\n" \
"v_and_b32_e32 v107, s43, v109\n" \
"v_lshlrev_b32_e32 v108, 16, v110\n" \
"v_and_b32_e32 v109, s43, v110\n" \
"v_lshlrev_b32_e32 v110, 16, v111\n" \
"v_and_b32_e32 v111, s43, v111\n" \
"v_lshlrev_b32_e32 v80, 16, v88\n" \
"v_and_b32_e32 v81, s43, v88\n" \
"v_lshlrev_b32_e32 v82, 16, v89\n" \
"v_and_b32_e32 v83, s43, v89\n" \
"v_lshlrev_b32_e32 v84, 16, v90\n" \
"v_and_b32_e32 v85, s43, v90\n" \
"v_lshlrev_b32_e32 v86, 16, v91\n" \
"v_and_b32_e32 v87, s43, v91\n" \
"v_lshlrev_b32_e32 v88, 16, v92\n" \
"v_and_b32_e32 v89, s43, v92\n" \
"v_lshlrev_b32_e32 v90, 16, v93\n" \
"v_and_b32_e32 v91, s43, v93\n" \
"v_lshlrev_b32_e32 v92, 16, v94\n" \
"v_and_b32_e32 v93, s43, v94\n" \
"v_lshlrev_b32_e32 v94, 16, v95\n" \
"v_and_b32_e32 v95, s43, v95\n" \
"v_sub_f32_e32 v80, v80, v190\n" \
"v_sub_f32_e32 v81, v81, v190\n" \
"v_sub_f32_e32 v82, v82, v190\n" \
"v_sub_f32_e32 v83, v83, v190\n" \
"v_sub_f32_e32 v84, v84, v190\n" \
"v_sub_f32_e32 v85, v85, v190\n" \
"v_sub_f32_e32 v86, v86, v190\n" \
"v_sub_f32_e32 v87, v87, v190\n" \
"v_sub_f32_e32 v88, v88, v190\n" \
"v_sub_f32_e32 v89, v89, v190\n" \
"v_sub_f32_e32 v90, v90, v190\n" \
"v_sub_f32_e32 v91, v91, v190\n" \
"v_sub_f32_e32 v92, v92, v190\n" \
"v_sub_f32_e32 v93, v93, v190\n" \
"v_sub_f32_e32 v94, v94, v190\n" \
"v_sub_f32_e32 v95, v95, v190\n" \
"s_branch TLcs_%=\n" \
"Lsp1_%=:\n" \
"s_movk_i32 s51, 0x1000\n" \
"s_cmp_gt_u32 s48, 0\n" \
"s_cselect_b32 s61, 0, 1\n" \
"s_nop 0\n" \
"s_mul_i32 s61, s61, 152064\n" \
"Lsq1_%=:\n" \
"buffer_load_dwordx4 a[64:67], %[oq0], %[srdz], s61 offen offset:0 sc1\n" \
"buffer_load_dwordx4 a[68:71], %[oq0], %[srdz], s61 offen offset:1024 sc1\n" \
"buffer_load_dwordx4 a[72:75], %[oq0], %[srdz], s61 offen offset:2048 sc1\n" \
"buffer_load_dwordx4 a[76:79], %[oq0], %[srdz], s61 offen offset:3072 sc1\n" \
"buffer_load_dwordx4 a[80:83], v208, %[srdz], s61 offen offset:0 sc1\n" \
"buffer_load_dwordx4 a[84:87], v208, %[srdz], s61 offen offset:1024 sc1\n" \
"buffer_load_dwordx4 a[88:91], v208, %[srdz], s61 offen offset:2048 sc1\n" \
"buffer_load_dwordx4 a[92:95], v208, %[srdz], s61 offen offset:3072 sc1\n" \
"buffer_load_dwordx4 a[96:99], v209, %[srdz], s61 offen offset:0 sc1\n" \
"buffer_load_dwordx4 a[100:103], v209, %[srdz], s61 offen offset:1024 sc1\n" \
"buffer_load_dwordx4 a[104:107], v209, %[srdz], s61 offen offset:2048 sc1\n" \
"buffer_load_dwordx4 a[108:111], v209, %[srdz], s61 offen offset:3072 sc1\n" \
"buffer_load_dwordx4 a[112:115], v210, %[srdz], s61 offen offset:0 sc1\n" \
"buffer_load_dwordx4 a[116:119], v210, %[srdz], s61 offen offset:1024 sc1\n" \
"buffer_load_dwordx4 a[120:123], v210, %[srdz], s61 offen offset:2048 sc1\n" \
"buffer_load_dwordx4 a[124:127], v210, %[srdz], s61 offen offset:3072 sc1\n" \
"s_waitcnt vmcnt(0)\n" \
"s_nop 0\n" \
"v_mfma_f32_16x16x32_bf16 v[128:131], a[236:239], a[64:67], v[192:195]\n" \
"v_mfma_f32_16x16x32_bf16 v[128:131], a[196:199], a[68:71], v[128:131]\n" \
"v_mfma_f32_16x16x32_bf16 v[128:131], a[200:203], a[72:75], v[128:131]\n" \
"v_mfma_f32_16x16x32_bf16 v[128:131], a[204:207], a[76:79], v[128:131]\n" \
"v_mfma_f32_16x16x32_bf16 v[132:135], a[236:239], a[80:83], v[196:199]\n" \
"v_mfma_f32_16x16x32_bf16 v[132:135], a[196:199], a[84:87], v[132:135]\n" \
"v_mfma_f32_16x16x32_bf16 v[132:135], a[200:203], a[88:91], v[132:135]\n" \
"v_mfma_f32_16x16x32_bf16 v[132:135], a[204:207], a[92:95], v[132:135]\n" \
"v_mfma_f32_16x16x32_bf16 v[136:139], a[236:239], a[96:99], v[200:203]\n" \
"v_mfma_f32_16x16x32_bf16 v[136:139], a[196:199], a[100:103], v[136:139]\n" \
"v_mfma_f32_16x16x32_bf16 v[136:139], a[200:203], a[104:107], v[136:139]\n" \
"v_mfma_f32_16x16x32_bf16 v[136:139], a[204:207], a[108:111], v[136:139]\n" \
"v_mfma_f32_16x16x32_bf16 v[140:143], a[236:239], a[112:115], v[204:207]\n" \
"v_mfma_f32_16x16x32_bf16 v[140:143], a[196:199], a[116:119], v[140:143]\n" \
"v_mfma_f32_16x16x32_bf16 v[140:143], a[200:203], a[120:123], v[140:143]\n" \
"v_mfma_f32_16x16x32_bf16 v[140:143], a[204:207], a[124:127], v[140:143]\n" \
"s_nop 7\n" \
"s_nop 0\n" \
"v_cmp_u_f32_e64 s[44:45], v128, v132\n" \
"v_cmp_u_f32_e64 s[56:57], v136, v140\n" \
"s_or_b64 s[56:57], s[56:57], s[44:45]\n" \
"s_cbranch_scc0 Lbk1_%=\n" \
"s_sub_u32 s51, s51, 1\n" \
"s_cbranch_scc0 Lsq1_%=\n" \
"s_branch Lbk1_%=\n" \
"Lsp2_%=:\n" \
"s_movk_i32 s51, 0x1000\n" \
"s_cmp_gt_u32 s48, 1\n" \
"s_cselect_b32 s61, 1, 2\n" \
"s_mul_i32 s61, s61, 152064\n" \
"Lsq2_%=:\n" \
"buffer_load_dwordx4 a[0:3], %[oq0], %[srdz], s61 offen offset:0 sc1\n" \
"buffer_load_dwordx4 a[4:7], %[oq0], %[srdz], s61 offen offset:1024 sc1\n" \
"buffer_load_dwordx4 a[8:11], %[oq0], %[srdz], s61 offen offset:2048 sc1\n" \
"buffer_load_dwordx4 a[12:15], %[oq0], %[srdz], s61 offen offset:3072 sc1\n" \
"buffer_load_dwordx4 a[16:19], v208, %[srdz], s61 offen offset:0 sc1\n" \
"buffer_load_dwordx4 a[20:23], v208, %[srdz], s61 offen offset:1024 sc1\n" \
"buffer_load_dwordx4 a[24:27], v208, %[srdz], s61 offen offset:2048 sc1\n" \
"buffer_load_dwordx4 a[28:31], v208, %[srdz], s61 offen offset:3072 sc1\n" \
"buffer_load_dwordx4 a[32:35], v209, %[srdz], s61 offen offset:0 sc1\n" \
"buffer_load_dwordx4 a[36:39], v209, %[srdz], s61 offen offset:1024 sc1\n" \
"buffer_load_dwordx4 a[40:43], v209, %[srdz], s61 offen offset:2048 sc1\n" \
"buffer_load_dwordx4 a[44:47], v209, %[srdz], s61 offen offset:3072 sc1\n" \
"buffer_load_dwordx4 a[48:51], v210, %[srdz], s61 offen offset:0 sc1\n" \
"buffer_load_dwordx4 a[52:55], v210, %[srdz], s61 offen offset:1024 sc1\n" \
"buffer_load_dwordx4 a[56:59], v210, %[srdz], s61 offen offset:2048 sc1\n" \
"buffer_load_dwordx4 a[60:63], v210, %[srdz], s61 offen offset:3072 sc1\n" \
"s_waitcnt vmcnt(0)\n" \
"s_nop 0\n" \
"v_mfma_f32_16x16x32_bf16 v[192:195], a[196:199], a[0:3], v[128:131]\n" \
"v_mfma_f32_16x16x32_bf16 v[192:195], a[200:203], a[4:7], v[192:195]\n" \
"v_mfma_f32_16x16x32_bf16 v[192:195], a[204:207], a[8:11], v[192:195]\n" \
"v_mfma_f32_16x16x32_bf16 v[192:195], a[232:235], a[12:15], v[192:195]\n" \
"v_mfma_f32_16x16x32_bf16 v[196:199], a[196:199], a[16:19], v[132:135]\n" \
"v_mfma_f32_16x16x32_bf16 v[196:199], a[200:203], a[20:23], v[196:199]\n" \
"v_mfma_f32_16x16x32_bf16 v[196:199], a[204:207], a[24:27], v[196:199]\n" \
"v_mfma_f32_16x16x32_bf16 v[196:199], a[232:235], a[28:31], v[196:199]\n" \
"v_mfma_f32_16x16x32_bf16 v[200:203], a[196:199], a[32:35], v[136:139]\n" \
"v_mfma_f32_16x16x32_bf16 v[200:203], a[200:203], a[36:39], v[200:203]\n" \
"v_mfma_f32_16x16x32_bf16 v[200:203], a[204:207], a[40:43], v[200:203]\n" \
"v_mfma_f32_16x16x32_bf16 v[200:203], a[232:235], a[44:47], v[200:203]\n" \
"v_mfma_f32_16x16x32_bf16 v[204:207], a[196:199], a[48:51], v[140:143]\n" \
"v_mfma_f32_16x16x32_bf16 v[204:207], a[200:203], a[52:55], v[204:207]\n" \
"v_mfma_f32_16x16x32_bf16 v[204:207], a[204:207], a[56:59], v[204:207]\n" \
"v_mfma_f32_16x16x32_bf16 v[204:207], a[232:235], a[60:63], v[204:207]\n" \
"s_nop 7\n" \
"s_nop 0\n" \
"v_cmp_u_f32_e64 s[44:45], v192, v196\n" \
"v_cmp_u_f32_e64 s[56:57], v200, v204\n" \
"s_or_b64 s[56:57], s[56:57], s[44:45]\n" \
"s_cbranch_scc0 Lbk2_%=\n" \
"s_sub_u32 s51, s51, 1\n" \
"s_cbranch_scc0 Lsq2_%=\n" \
"s_branch Lbk2_%=\n" \
"Lsp3_%=:\n" \
"s_movk_i32 s51, 0x1000\n" \
"s_cmp_gt_u32 s48, 2\n" \
"s_cselect_b32 s61, 2, 3\n" \
"s_mul_i32 s61, s61, 152064\n" \
"Lsq3_%=:\n" \
"buffer_load_dwordx4 a[128:131], %[oq0], %[srdz], s61 offen offset:0 sc1\n" \
"buffer_load_dwordx4 a[132:135], %[oq0], %[srdz], s61 offen offset:1024 sc1\n" \
"buffer_load_dwordx4 a[136:139], %[oq0], %[srdz], s61 offen offset:2048 sc1\n" \
"buffer_load_dwordx4 a[140:143], %[oq0], %[srdz], s61 offen offset:3072 sc1\n" \
"buffer_load_dwordx4 a[144:147], v208, %[srdz], s61 offen offset:0 sc1\n" \
"buffer_load_dwordx4 a[148:151], v208, %[srdz], s61 offen offset:1024 sc1\n" \
"buffer_load_dwordx4 a[152:155], v208, %[srdz], s61 offen offset:2048 sc1\n" \
"buffer_load_dwordx4 a[156:159], v208, %[srdz], s61 offen offset:3072 sc1\n" \
"buffer_load_dwordx4 a[160:163], v209, %[srdz], s61 offen offset:0 sc1\n" \
"buffer_load_dwordx4 a[164:167], v209, %[srdz], s61 offen offset:1024 sc1\n" \
"buffer_load_dwordx4 a[168:171], v209, %[srdz], s61 offen offset:2048 sc1\n" \
"buffer_load_dwordx4 a[172:175], v209, %[srdz], s61 offen offset:3072 sc1\n" \
"buffer_load_dwordx4 a[176:179], v210, %[srdz], s61 offen offset:0 sc1\n" \
"buffer_load_dwordx4 a[180:183], v210, %[srdz], s61 offen offset:1024 sc1\n" \
"buffer_load_dwordx4 a[184:187], v210, %[srdz], s61 offen offset:2048 sc1\n" \
"buffer_load_dwordx4 a[188:191], v210, %[srdz], s61 offen offset:3072 sc1\n" \
"s_waitcnt vmcnt(0)\n" \
"s_nop 0\n" \
"v_mfma_f32_16x16x32_bf16 v[128:131], a[196:199], a[128:131], v[192:195]\n" \
"v_mfma_f32_16x16x32_bf16 v[128:131], a[200:203], a[132:135], v[128:131]\n" \
"v_mfma_f32_16x16x32_bf16 v[128:131], a[204:207], a[136:139], v[128:131]\n" \
"v_mfma_f32_16x16x32_bf16 v[128:131], a[224:227], a[140:143], v[128:131]\n" \
"v_mfma_f32_16x16x32_bf16 v[132:135], a[196:199], a[144:147], v[196:199]\n" \
"v_mfma_f32_16x16x32_bf16 v[132:135], a[200:203], a[148:151], v[132:135]\n" \
"v_mfma_f32_16x16x32_bf16 v[132:135], a[204:207], a[152:155], v[132:135]\n" \
"v_mfma_f32_16x16x32_bf16 v[132:135], a[224:227], a[156:159], v[132:135]\n" \
"v_mfma_f32_16x16x32_bf16 v[136:139], a[196:199], a[160:163], v[200:203]\n" \
"v_mfma_f32_16x16x32_bf16 v[136:139], a[200:203], a[164:167], v[136:139]\n" \
"v_mfma_f32_16x16x32_bf16 v[136:139], a[204:207], a[168:171], v[136:139]\n" \
"v_mfma_f32_16x16x32_bf16 v[136:139], a[224:227], a[172:175], v[136:139]\n" \
"v_mfma_f32_16x16x32_bf16 v[140:143], a[196:199], a[176:179], v[204:207]\n" \
"v_mfma_f32_16x16x32_bf16 v[140:143], a[200:203], a[180:183], v[140:143]\n" \
"v_mfma_f32_16x16x32_bf16 v[140:143], a[204:207], a[184:187], v[140:143]\n" \
"v_mfma_f32_16x16x32_bf16 v[140:143], a[224:227], a[188:191], v[140:143]\n" \
"s_nop 7\n" \
"s_nop 0\n" \
"v_cmp_u_f32_e64 s[44:45], v128, v132\n" \
"v_cmp_u_f32_e64 s[56:57], v136, v140\n" \
"s_or_b64 s[56:57], s[56:57], s[44:45]\n" \
"s_cbranch_scc0 Lbk3_%=\n" \
"s_sub_u32 s51, s51, 1\n" \
"s_cbranch_scc0 Lsq3_%=\n" \
"s_branch Lbk3_%=\n" \
"Lsp4_%=:\n" \
"s_movk_i32 s51, 0x1000\n" \
"s_cmp_gt_u32 s48, 3\n" \
"s_cselect_b32 s61, 3, 4\n" \
"s_mul_i32 s61, s61, 152064\n" \
"Lsq4_%=:\n" \
"buffer_load_dwordx4 a[64:67], %[oq0], %[srdz], s61 offen offset:0 sc1\n" \
"buffer_load_dwordx4 a[68:71], %[oq0], %[srdz], s61 offen offset:1024 sc1\n" \
"buffer_load_dwordx4 a[72:75], %[oq0], %[srdz], s61 offen offset:2048 sc1\n" \
"buffer_load_dwordx4 a[76:79], %[oq0], %[srdz], s61 offen offset:3072 sc1\n" \
"buffer_load_dwordx4 a[80:83], v208, %[srdz], s61 offen offset:0 sc1\n" \
"buffer_load_dwordx4 a[84:87], v208, %[srdz], s61 offen offset:1024 sc1\n" \
"buffer_load_dwordx4 a[88:91], v208, %[srdz], s61 offen offset:2048 sc1\n" \
"buffer_load_dwordx4 a[92:95], v208, %[srdz], s61 offen offset:3072 sc1\n" \
"buffer_load_dwordx4 a[96:99], v209, %[srdz], s61 offen offset:0 sc1\n" \
"buffer_load_dwordx4 a[100:103], v209, %[srdz], s61 offen offset:1024 sc1\n" \
"buffer_load_dwordx4 a[104:107], v209, %[srdz], s61 offen offset:2048 sc1\n" \
"buffer_load_dwordx4 a[108:111], v209, %[srdz], s61 offen offset:3072 sc1\n" \
"buffer_load_dwordx4 a[112:115], v210, %[srdz], s61 offen offset:0 sc1\n" \
"buffer_load_dwordx4 a[116:119], v210, %[srdz], s61 offen offset:1024 sc1\n" \
"buffer_load_dwordx4 a[120:123], v210, %[srdz], s61 offen offset:2048 sc1\n" \
"buffer_load_dwordx4 a[124:127], v210, %[srdz], s61 offen offset:3072 sc1\n" \
"s_waitcnt vmcnt(0)\n" \
"s_nop 0\n" \
"v_mfma_f32_16x16x32_bf16 v[192:195], a[196:199], a[64:67], v[128:131]\n" \
"v_mfma_f32_16x16x32_bf16 v[192:195], a[200:203], a[68:71], v[192:195]\n" \
"v_mfma_f32_16x16x32_bf16 v[192:195], a[204:207], a[72:75], v[192:195]\n" \
"v_mfma_f32_16x16x32_bf16 v[192:195], a[220:223], a[76:79], v[192:195]\n" \
"v_mfma_f32_16x16x32_bf16 v[196:199], a[196:199], a[80:83], v[132:135]\n" \
"v_mfma_f32_16x16x32_bf16 v[196:199], a[200:203], a[84:87], v[196:199]\n" \
"v_mfma_f32_16x16x32_bf16 v[196:199], a[204:207], a[88:91], v[196:199]\n" \
"v_mfma_f32_16x16x32_bf16 v[196:199], a[220:223], a[92:95], v[196:199]\n" \
"v_mfma_f32_16x16x32_bf16 v[200:203], a[196:199], a[96:99], v[136:139]\n" \
"v_mfma_f32_16x16x32_bf16 v[200:203], a[200:203], a[100:103], v[200:203]\n" \
"v_mfma_f32_16x16x32_bf16 v[200:203], a[204:207], a[104:107], v[200:203]\n" \
"v_mfma_f32_16x16x32_bf16 v[200:203], a[220:223], a[108:111], v[200:203]\n" \
"v_mfma_f32_16x16x32_bf16 v[204:207], a[196:199], a[112:115], v[140:143]\n" \
"v_mfma_f32_16x16x32_bf16 v[204:207], a[200:203], a[116:119], v[204:207]\n" \
"v_mfma_f32_16x16x32_bf16 v[204:207], a[204:207], a[120:123], v[204:207]\n" \
"v_mfma_f32_16x16x32_bf16 v[204:207], a[220:223], a[124:127], v[204:207]\n" \
"s_nop 7\n" \
"s_nop 0\n" \
"v_cmp_u_f32_e64 s[44:45], v192, v196\n" \
"v_cmp_u_f32_e64 s[56:57], v200, v204\n" \
"s_or_b64 s[56:57], s[56:57], s[44:45]\n" \
"s_cbranch_scc0 Lbk4_%=\n" \
"s_sub_u32 s51, s51, 1\n" \
"s_cbranch_scc0 Lsq4_%=\n" \
"s_branch Lbk4_%=\n" \
"Lsp5_%=:\n" \
"s_movk_i32 s51, 0x1000\n" \
"s_cmp_gt_u32 s48, 4\n" \
"s_cselect_b32 s61, 4, 5\n" \
"s_mul_i32 s61, s61, 152064\n" \
"Lsq5_%=:\n" \
"buffer_load_dwordx4 v[0:3], %[oq0], %[srdz], s61 offen offset:0 sc1\n" \
"buffer_load_dwordx4 v[8:11], %[oq0], %[srdz], s61 offen offset:1024 sc1\n" \
"buffer_load_dwordx4 v[16:19], %[oq0], %[srdz], s61 offen offset:2048 sc1\n" \
"buffer_load_dwordx4 v[24:27], %[oq0], %[srdz], s61 offen offset:3072 sc1\n" \
"buffer_load_dwordx4 v[32:35], v208, %[srdz], s61 offen offset:0 sc1\n" \
"buffer_load_dwordx4 v[40:43], v208, %[srdz], s61 offen offset:1024 sc1\n" \
"buffer_load_dwordx4 v[48:51], v208, %[srdz], s61 offen offset:2048 sc1\n" \
"buffer_load_dwordx4 v[56:59], v208, %[srdz], s61 offen offset:3072 sc1\n" \
"buffer_load_dwordx4 v[64:67], v209, %[srdz], s61 offen offset:0 sc1\n" \
"buffer_load_dwordx4 v[72:75], v209, %[srdz], s61 offen offset:1024 sc1\n" \
"buffer_load_dwordx4 v[80:83], v209, %[srdz], s61 offen offset:2048 sc1\n" \
"buffer_load_dwordx4 v[88:91], v209, %[srdz], s61 offen offset:3072 sc1\n" \
"buffer_load_dwordx4 v[96:99], v210, %[srdz], s61 offen offset:0 sc1\n" \
"buffer_load_dwordx4 v[104:107], v210, %[srdz], s61 offen offset:1024 sc1\n" \
"buffer_load_dwordx4 v[112:115], v210, %[srdz], s61 offen offset:2048 sc1\n" \
"buffer_load_dwordx4 v[120:123], v210, %[srdz], s61 offen offset:3072 sc1\n" \
"s_waitcnt vmcnt(0)\n" \
"s_nop 0\n" \
"v_mfma_f32_16x16x32_bf16 v[128:131], a[232:235], v[0:3], v[192:195]\n" \
"v_mfma_f32_16x16x32_bf16 v[128:131], a[236:239], v[8:11], v[128:131]\n" \
"v_mfma_f32_16x16x32_bf16 v[128:131], a[208:211], v[16:19], v[128:131]\n" \
"v_mfma_f32_16x16x32_bf16 v[128:131], a[228:231], v[24:27], v[128:131]\n" \
"v_mfma_f32_16x16x32_bf16 v[132:135], a[232:235], v[32:35], v[196:199]\n" \
"v_mfma_f32_16x16x32_bf16 v[132:135], a[236:239], v[40:43], v[132:135]\n" \
"v_mfma_f32_16x16x32_bf16 v[132:135], a[208:211], v[48:51], v[132:135]\n" \
"v_mfma_f32_16x16x32_bf16 v[132:135], a[228:231], v[56:59], v[132:135]\n" \
"v_mfma_f32_16x16x32_bf16 v[136:139], a[232:235], v[64:67], v[200:203]\n" \
"v_mfma_f32_16x16x32_bf16 v[136:139], a[236:239], v[72:75], v[136:139]\n" \
"v_mfma_f32_16x16x32_bf16 v[136:139], a[208:211], v[80:83], v[136:139]\n" \
"v_mfma_f32_16x16x32_bf16 v[136:139], a[228:231], v[88:91], v[136:139]\n" \
"v_mfma_f32_16x16x32_bf16 v[140:143], a[232:235], v[96:99], v[204:207]\n" \
"v_mfma_f32_16x16x32_bf16 v[140:143], a[236:239], v[104:107], v[140:143]\n" \
"v_mfma_f32_16x16x32_bf16 v[140:143], a[208:211], v[112:115], v[140:143]\n" \
"v_mfma_f32_16x16x32_bf16 v[140:143], a[228:231], v[120:123], v[140:143]\n" \
"s_nop 7\n" \
"s_nop 0\n" \
"v_cmp_u_f32_e64 s[44:45], v128, v132\n" \
"v_cmp_u_f32_e64 s[56:57], v136, v140\n" \
"s_or_b64 s[56:57], s[56:57], s[44:45]\n" \
"s_cbranch_scc0 Lbk5_%=\n" \
"s_sub_u32 s51, s51, 1\n" \
"s_cbranch_scc0 Lsq5_%=\n" \
"s_branch Lbk5_%=\n" \
"Lsp6_%=:\n" \
"s_movk_i32 s51, 0x1000\n" \
"s_cmp_gt_u32 s48, 5\n" \
"s_cselect_b32 s61, 5, 6\n" \
"s_mul_i32 s61, s61, 152064\n" \
"Lsq6_%=:\n" \
"buffer_load_dwordx4 a[0:3], %[oq0], %[srdz], s61 offen offset:0 sc1\n" \
"buffer_load_dwordx4 a[4:7], %[oq0], %[srdz], s61 offen offset:1024 sc1\n" \
"buffer_load_dwordx4 a[8:11], %[oq0], %[srdz], s61 offen offset:2048 sc1\n" \
"buffer_load_dwordx4 a[12:15], %[oq0], %[srdz], s61 offen offset:3072 sc1\n" \
"buffer_load_dwordx4 a[16:19], v208, %[srdz], s61 offen offset:0 sc1\n" \
"buffer_load_dwordx4 a[20:23], v208, %[srdz], s61 offen offset:1024 sc1\n" \
"buffer_load_dwordx4 a[24:27], v208, %[srdz], s61 offen offset:2048 sc1\n" \
"buffer_load_dwordx4 a[28:31], v208, %[srdz], s61 offen offset:3072 sc1\n" \
"buffer_load_dwordx4 a[32:35], v209, %[srdz], s61 offen offset:0 sc1\n" \
"buffer_load_dwordx4 a[36:39], v209, %[srdz], s61 offen offset:1024 sc1\n" \
"buffer_load_dwordx4 a[40:43], v209, %[srdz], s61 offen offset:2048 sc1\n" \
"buffer_load_dwordx4 a[44:47], v209, %[srdz], s61 offen offset:3072 sc1\n" \
"buffer_load_dwordx4 a[48:51], v210, %[srdz], s61 offen offset:0 sc1\n" \
"buffer_load_dwordx4 a[52:55], v210, %[srdz], s61 offen offset:1024 sc1\n" \
"buffer_load_dwordx4 a[56:59], v210, %[srdz], s61 offen offset:2048 sc1\n" \
"buffer_load_dwordx4 a[60:63], v210, %[srdz], s61 offen offset:3072 sc1\n" \
"s_waitcnt vmcnt(0)\n" \
"s_nop 0\n" \
"v_mfma_f32_16x16x32_bf16 v[192:195], a[196:199], a[0:3], v[128:131]\n" \
"v_mfma_f32_16x16x32_bf16 v[192:195], a[200:203], a[4:7], v[192:195]\n" \
"v_mfma_f32_16x16x32_bf16 v[192:195], a[204:207], a[8:11], v[192:195]\n" \
"v_mfma_f32_16x16x32_bf16 v[192:195], a[220:223], a[12:15], v[192:195]\n" \
"v_mfma_f32_16x16x32_bf16 v[196:199], a[196:199], a[16:19], v[132:135]\n" \
"v_mfma_f32_16x16x32_bf16 v[196:199], a[200:203], a[20:23], v[196:199]\n" \
"v_mfma_f32_16x16x32_bf16 v[196:199], a[204:207], a[24:27], v[196:199]\n" \
"v_mfma_f32_16x16x32_bf16 v[196:199], a[220:223], a[28:31], v[196:199]\n" \
"v_mfma_f32_16x16x32_bf16 v[200:203], a[196:199], a[32:35], v[136:139]\n" \
"v_mfma_f32_16x16x32_bf16 v[200:203], a[200:203], a[36:39], v[200:203]\n" \
"v_mfma_f32_16x16x32_bf16 v[200:203], a[204:207], a[40:43], v[200:203]\n" \
"v_mfma_f32_16x16x32_bf16 v[200:203], a[220:223], a[44:47], v[200:203]\n" \
"v_mfma_f32_16x16x32_bf16 v[204:207], a[196:199], a[48:51], v[140:143]\n" \
"v_mfma_f32_16x16x32_bf16 v[204:207], a[200:203], a[52:55], v[204:207]\n" \
"v_mfma_f32_16x16x32_bf16 v[204:207], a[204:207], a[56:59], v[204:207]\n" \
"v_mfma_f32_16x16x32_bf16 v[204:207], a[220:223], a[60:63], v[204:207]\n" \
"s_nop 7\n" \
"s_nop 0\n" \
"v_cmp_u_f32_e64 s[44:45], v192, v196\n" \
"v_cmp_u_f32_e64 s[56:57], v200, v204\n" \
"s_or_b64 s[56:57], s[56:57], s[44:45]\n" \
"s_cbranch_scc0 Lbk6_%=\n" \
"s_sub_u32 s51, s51, 1\n" \
"s_cbranch_scc0 Lsq6_%=\n" \
"s_branch Lbk6_%=\n" \
"Lsp7_%=:\n" \
"s_movk_i32 s51, 0x1000\n" \
"s_cmp_gt_u32 s48, 6\n" \
"s_cselect_b32 s61, 6, 7\n" \
"s_mul_i32 s61, s61, 152064\n" \
"Lsq7_%=:\n" \
"buffer_load_dwordx4 a[128:131], %[oq0], %[srdz], s61 offen offset:0 sc1\n" \
"buffer_load_dwordx4 a[132:135], %[oq0], %[srdz], s61 offen offset:1024 sc1\n" \
"buffer_load_dwordx4 a[136:139], %[oq0], %[srdz], s61 offen offset:2048 sc1\n" \
"buffer_load_dwordx4 a[140:143], %[oq0], %[srdz], s61 offen offset:3072 sc1\n" \
"buffer_load_dwordx4 a[144:147], v208, %[srdz], s61 offen offset:0 sc1\n" \
"buffer_load_dwordx4 a[148:151], v208, %[srdz], s61 offen offset:1024 sc1\n" \
"buffer_load_dwordx4 a[152:155], v208, %[srdz], s61 offen offset:2048 sc1\n" \
"buffer_load_dwordx4 a[156:159], v208, %[srdz], s61 offen offset:3072 sc1\n" \
"buffer_load_dwordx4 a[160:163], v209, %[srdz], s61 offen offset:0 sc1\n" \
"buffer_load_dwordx4 a[164:167], v209, %[srdz], s61 offen offset:1024 sc1\n" \
"buffer_load_dwordx4 a[168:171], v209, %[srdz], s61 offen offset:2048 sc1\n" \
"buffer_load_dwordx4 a[172:175], v209, %[srdz], s61 offen offset:3072 sc1\n" \
"buffer_load_dwordx4 a[176:179], v210, %[srdz], s61 offen offset:0 sc1\n" \
"buffer_load_dwordx4 a[180:183], v210, %[srdz], s61 offen offset:1024 sc1\n" \
"buffer_load_dwordx4 a[184:187], v210, %[srdz], s61 offen offset:2048 sc1\n" \
"buffer_load_dwordx4 a[188:191], v210, %[srdz], s61 offen offset:3072 sc1\n" \
"s_waitcnt vmcnt(0)\n" \
"s_nop 0\n" \
"v_mfma_f32_16x16x32_bf16 v[128:131], a[208:211], a[128:131], v[192:195]\n" \
"v_mfma_f32_16x16x32_bf16 v[128:131], a[224:227], a[132:135], v[128:131]\n" \
"v_mfma_f32_16x16x32_bf16 v[128:131], a[228:231], a[136:139], v[128:131]\n" \
"v_mfma_f32_16x16x32_bf16 v[128:131], a[236:239], a[140:143], v[128:131]\n" \
"v_mfma_f32_16x16x32_bf16 v[132:135], a[208:211], a[144:147], v[196:199]\n" \
"v_mfma_f32_16x16x32_bf16 v[132:135], a[224:227], a[148:151], v[132:135]\n" \
"v_mfma_f32_16x16x32_bf16 v[132:135], a[228:231], a[152:155], v[132:135]\n" \
"v_mfma_f32_16x16x32_bf16 v[132:135], a[236:239], a[156:159], v[132:135]\n" \
"v_mfma_f32_16x16x32_bf16 v[136:139], a[208:211], a[160:163], v[200:203]\n" \
"v_mfma_f32_16x16x32_bf16 v[136:139], a[224:227], a[164:167], v[136:139]\n" \
"v_mfma_f32_16x16x32_bf16 v[136:139], a[228:231], a[168:171], v[136:139]\n" \
"v_mfma_f32_16x16x32_bf16 v[136:139], a[236:239], a[172:175], v[136:139]\n" \
"v_mfma_f32_16x16x32_bf16 v[140:143], a[208:211], a[176:179], v[204:207]\n" \
"v_mfma_f32_16x16x32_bf16 v[140:143], a[224:227], a[180:183], v[140:143]\n" \
"v_mfma_f32_16x16x32_bf16 v[140:143], a[228:231], a[184:187], v[140:143]\n" \
"v_mfma_f32_16x16x32_bf16 v[140:143], a[236:239], a[188:191], v[140:143]\n" \
"s_nop 7\n" \
"s_nop 0\n" \
"v_cmp_u_f32_e64 s[44:45], v128, v132\n" \
"v_cmp_u_f32_e64 s[56:57], v136, v140\n" \
"s_or_b64 s[56:57], s[56:57], s[44:45]\n" \
"s_cbranch_scc0 Lbk7_%=\n" \
"s_sub_u32 s51, s51, 1\n" \
"s_cbranch_scc0 Lsq7_%=\n" \
"s_branch Lbk7_%=\n" \
"Lspk_%=:\n" \
"s_nop 0\n" \
"s_mov_b32 s51, 0x10000\n" \
"Lsqk_%=:\n" \
"buffer_load_dword v229, v226, %[srdz], s50 offen sc1\n" \
"s_waitcnt vmcnt(0)\n" \
"s_nop 0\n" \
"v_cmp_eq_u32_e64 s[56:57], 0, v229\n" \
"s_cmp_lg_u64 s[56:57], 0\n" \
"s_cbranch_scc0 Lbkk_%=\n" \
"s_sub_u32 s51, s51, 1\n" \
"s_cbranch_scc0 Lsqk_%=\n" \
"s_branch Lbkk_%=\n" \
"Lspp_%=:\n" \
"s_nop 0\n" \
"s_mov_b32 s51, 0x10000\n" \
"Lsqp_%=:\n" \
"buffer_load_dword v232, v226, %[srdz], s54 offen sc1\n" \
"s_waitcnt vmcnt(0)\n" \
"v_cmp_eq_u32_e32 vcc, 0, v232\n" \
"s_cbranch_vccz Lbkp_%=\n" \
"s_sub_u32 s51, s51, 1\n" \
"s_cbranch_scc0 Lsqp_%=\n" \
"s_branch Lbkp_%=\n" \
"Lpars_%=:\n" \
"s_mov_b32 s51, 0x10000\n" \
"s_lshl_b32 s61, %[wid], 10\n" \
"v_lshrrev_b32_e32 v235, 3, v224\n" \
"v_add_u32_e64 v235, s61, v235\n" \
"Lpar1_%=:\n" \
"s_mov_b32 s61, 262144\n" \
"s_nop 0\n" \
"s_nop 0\n" \
"buffer_load_dwordx4 v[192:195], v235, %[srdz], s61 offen offset:0 sc1\n" \
"s_mov_b32 s61, 266240\n" \
"s_nop 0\n" \
"s_nop 0\n" \
"buffer_load_dwordx4 v[196:199], v235, %[srdz], s61 offen offset:0 sc1\n" \
"s_mov_b32 s61, 270336\n" \
"s_nop 0\n" \
"s_nop 0\n" \
"buffer_load_dwordx4 v[200:203], v235, %[srdz], s61 offen offset:0 sc1\n" \
"s_mov_b32 s61, 274432\n" \
"s_nop 0\n" \
"s_nop 0\n" \
"buffer_load_dwordx4 v[204:207], v235, %[srdz], s61 offen offset:0 sc1\n" \
"s_waitcnt vmcnt(0)\n" \
"s_nop 0\n" \
"v_and_or_b32 v236, v192, v193, 0\n" \
"v_bitop3_b32 v236, v236, v194, v195 bitop3:0x80\n" \
"v_bitop3_b32 v236, v236, v196, v197 bitop3:0x80\n" \
"v_bitop3_b32 v236, v236, v198, v199 bitop3:0x80\n" \
"v_bitop3_b32 v236, v236, v200, v201 bitop3:0x80\n" \
"v_bitop3_b32 v236, v236, v202, v203 bitop3:0x80\n" \
"v_bitop3_b32 v236, v236, v204, v205 bitop3:0x80\n" \
"v_bitop3_b32 v236, v236, v206, v207 bitop3:0x80\n" \
"v_and_b32_e64 v236, 1, v236\n" \
"v_cmp_eq_u32_e64 s[56:57], 0, v236\n" \
"s_cmp_lg_u64 s[56:57], 0\n" \
"s_cbranch_scc0 Lparb_%=\n" \
"s_sub_u32 s51, s51, 1\n" \
"s_cbranch_scc0 Lpar1_%=\n" \
"s_branch Lparb_%=\n" \
"Ls0_%=:\n" \
"s_movk_i32 s51, 0x1000\n" \
"s_waitcnt vmcnt(0)\n" \
"Ls01_%=:\n" \
"s_nop 0\n" \
"s_mov_b32 s40, 606208\n" \
"buffer_load_dwordx4 a[176:179], v237, %[srdz], s40 offen sc1\n" \
"s_mov_b32 s40, 610304\n" \
"buffer_load_dwordx4 a[180:183], v237, %[srdz], s40 offen sc1\n" \
"s_mov_b32 s40, 614400\n" \
"buffer_load_dwordx4 a[184:187], v237, %[srdz], s40 offen sc1\n" \
"s_mov_b32 s40, 618496\n" \
"buffer_load_dwordx4 a[188:191], v237, %[srdz], s40 offen sc1\n" \
"s_waitcnt vmcnt(0)\n" \
"s_nop 0\n" \
"v_mfma_f32_16x16x32_bf16 v[216:219], a[176:179], v[0:3], 0\n" \
"v_mfma_f32_16x16x32_bf16 v[216:219], a[180:183], v[8:11], v[216:219]\n" \
"v_mfma_f32_16x16x32_bf16 v[216:219], a[184:187], v[16:19], v[216:219]\n" \
"v_mfma_f32_16x16x32_bf16 v[216:219], a[188:191], v[24:27], v[216:219]\n" \
"s_nop 7\n" \
"s_nop 0\n" \
"v_mov_b32_dpp v231, v216 quad_perm:[0,1,2,3] row_mask:0x1 bank_mask:0xf\n" \
"v_mfma_f32_16x16x32_bf16 v[232:235], a[176:179], v[32:35], 0\n" \
"v_mfma_f32_16x16x32_bf16 v[232:235], a[180:183], v[40:43], v[232:235]\n" \
"v_mfma_f32_16x16x32_bf16 v[232:235], a[184:187], v[48:51], v[232:235]\n" \
"v_mfma_f32_16x16x32_bf16 v[232:235], a[188:191], v[56:59], v[232:235]\n" \
"s_nop 7\n" \
"s_nop 0\n" \
"v_mov_b32_dpp v231, v232 quad_perm:[0,1,2,3] row_mask:0x2 bank_mask:0xf\n" \
"v_mfma_f32_16x16x32_bf16 v[216:219], a[176:179], v[64:67], 0\n" \
"v_mfma_f32_16x16x32_bf16 v[216:219], a[180:183], v[72:75], v[216:219]\n" \
"v_mfma_f32_16x16x32_bf16 v[216:219], a[184:187], v[80:83], v[216:219]\n" \
"v_mfma_f32_16x16x32_bf16 v[216:219], a[188:191], v[88:91], v[216:219]\n" \
"s_nop 7\n" \
"s_nop 0\n" \
"v_mov_b32_dpp v231, v216 quad_perm:[0,1,2,3] row_mask:0x4 bank_mask:0xf\n" \
"v_mfma_f32_16x16x32_bf16 v[232:235], a[176:179], v[96:99], 0\n" \
"v_mfma_f32_16x16x32_bf16 v[232:235], a[180:183], v[104:107], v[232:235]\n" \
"v_mfma_f32_16x16x32_bf16 v[232:235], a[184:187], v[112:115], v[232:235]\n" \
"v_mfma_f32_16x16x32_bf16 v[232:235], a[188:191], v[120:123], v[232:235]\n" \
"s_nop 7\n" \
"s_nop 0\n" \
"v_mov_b32_dpp v231, v232 quad_perm:[0,1,2,3] row_mask:0x8 bank_mask:0xf\n" \
"v_cmp_u_f32_e64 s[42:43], v231, v231\n" \
"s_cmp_lg_u64 s[42:43], 0\n" \
"s_cbranch_scc0 Ls0b_%=\n" \
"s_sub_u32 s51, s51, 1\n" \
"s_cbranch_scc0 Ls01_%=\n" \
"s_branch Ls0b_%=\n" \
"Ls1_%=:\n" \
"s_nop 0\n" \
"s_mov_b32 s51, 0x10000\n" \
"Ls11_%=:\n" \
"buffer_load_dword v235, v234, %[srdz], s63 offen sc1\n" \
"s_waitcnt vmcnt(0)\n" \
"v_and_b32_e32 v230, 1, v235\n" \
"v_cmp_eq_u32_e64 s[68:69], 0, v230\n" \
"s_cmp_lg_u64 s[68:69], 0\n" \
"s_cbranch_scc0 Ls1b_%=\n" \
"s_sub_u32 s51, s51, 1\n" \
"s_cbranch_scc0 Ls11_%=\n" \
"s_branch Ls1b_%=\n" \
"Lus_%=:\n" \
"s_nop 0\n" \
"s_mov_b32 s51, 0x10000\n" \
"Lus1_%=:\n" \
"s_lshl_b32 s62, %[wid], 10\n" \
"s_nop 0\n" \
"s_add_u32 s62, s62, 262144\n" \
"s_add_u32 s55, s62, 8192\n" \
"v_readlane_b32 s68, %[tlo], 20\n" \
"v_readlane_b32 s69, %[thi], 20\n" \
"s_lshl_b32 s63, %[wid], 9\n" \
"v_lshrrev_b32_e32 v230, 1, v235\n" \
"buffer_load_dwordx4 a[200:203], v235, %[srdz], s62 offen sc1\n" \
"v_add_u32_e64 v230, s63, v230\n" \
"buffer_load_dwordx4 a[204:207], v235, %[srdz], s55 offen sc1\n" \
"s_nop 1\n" \
"s_nop 0\n" \
"global_load_dwordx2 a[208:209], v230, s[68:69]\n" \
"s_waitcnt vmcnt(0)\n" \
"s_nop 0\n" \
"v_accvgpr_read_b32 v230, a208\n" \
"v_accvgpr_read_b32 v234, a204\n" \
"v_accvgpr_read_b32 v225, a205\n" \
"v_accvgpr_read_b32 v226, a200\n" \
"v_accvgpr_read_b32 v231, a201\n" \
"v_bitop3_b32 v229, v234, v225, -1 bitop3:0x80\n" \
"v_bitop3_b32 v229, v226, v231, v229 bitop3:0x80\n" \
"v_lshlrev_b32_e64 v236, 16, v230\n" \
"v_and_b32_e32 v230, 0xffff0000, v230\n" \
"v_mul_f32_e32 v234, v234, v236\n" \
"v_mul_f32_e32 v225, v225, v230\n" \
"v_mul_f32_e32 v226, v226, v234\n" \
"v_mul_f32_e32 v231, v231, v225\n" \
"v_cvt_pk_bf16_f32 v236, v234, v225\n" \
"v_cvt_pk_bf16_f32 v232, v226, v231\n" \
"v_accvgpr_read_b32 v230, a209\n" \
"v_accvgpr_read_b32 v234, a206\n" \
"v_accvgpr_read_b32 v225, a207\n" \
"v_accvgpr_read_b32 v226, a202\n" \
"v_accvgpr_read_b32 v231, a203\n" \
"v_bitop3_b32 v229, v234, v225, v229 bitop3:0x80\n" \
"v_bitop3_b32 v229, v226, v231, v229 bitop3:0x80\n" \
"v_lshlrev_b32_e64 v237, 16, v230\n" \
"v_and_b32_e32 v230, 0xffff0000, v230\n" \
"v_mul_f32_e32 v234, v234, v237\n" \
"v_mul_f32_e32 v225, v225, v230\n" \
"v_mul_f32_e32 v226, v226, v234\n" \
"v_mul_f32_e32 v231, v231, v225\n" \
"v_cvt_pk_bf16_f32 v237, v234, v225\n" \
"v_cvt_pk_bf16_f32 v233, v226, v231\n" \
"v_lshrrev_b32_e64 v230, 1, v235\n" \
"s_add_u32 s62, %[m0t], 6144\n" \
"v_add_u32_e32 v230, s62, v230\n" \
"v_and_b32_e32 v229, 1, v229\n" \
"v_cmp_eq_u32_e64 s[68:69], 0, v229\n" \
"ds_write_b64 v230, v[232:233] offset:0\n" \
"v_and_b32_e32 v230, 0x170, v235\n" \
"v_lshrrev_b32_e64 v234, 7, v224\n" \
"v_and_or_b32 v230, v234, 8, v230\n" \
"v_lshrrev_b32_e64 v234, 12, v224\n" \
"v_lshl_or_b32 v230, v234, 7, v230\n" \
"v_add_u32_e64 v230, s62, v230\n" \
"ds_write_b64 v230, v[236:237] offset:512\n" \
"s_cmp_lg_u64 s[68:69], 0\n" \
"s_cbranch_scc0 Lub_%=\n" \
"s_sub_u32 s51, s51, 1\n" \
"s_cbranch_scc0 Lus1_%=\n" \
"s_branch Lub_%=\n" \
"s_nop 0\n" \
"Lwalk_%=:\n" \
"s_mov_b64 exec, 0\n" \
"s_cmp_eq_u32 %[wid], 1\n" \
"s_cbranch_scc1 Lk0_%=\n" \
"s_cmp_eq_u32 %[wid], 2\n" \
"s_cbranch_scc1 Lk6_%=\n" \
"s_branch Lk12_%=\n" \
"adaln_kr:\n" \
"s_mov_b64 exec, -1\n" \
"s_branch Lgo_%=\n" \
"Lslow_%=:\n" \
"s_mov_b32 s52, 1\n" \
"s_mov_b32 s48, 0\n" \
"s_mov_b32 s51, 32\n" \
"s_nop 0\n" \
"s_mov_b32 s62, 0x80000000\n" \
"s_mov_b32 s63, 0x80000000\n" \
"s_waitcnt vmcnt(0)\n" \
"s_nop 0\n" \
"v_readlane_b32 s64, %[tlo], 9\n" \
"v_readlane_b32 s65, %[thi], 9\n" \
"s_mov_b32 s66, %[wbytes]\n" \
"v_mov_b32_e32 v229, %[vg0]\n" \
"s_mov_b32 s44, %[m0t]\n" \
"s_mov_b32 s45, 0\n" \
"v_xor_b32_e64 v208, 64, %[kb0]\n" \
"v_xor_b32_e32 v209, 0x80, %[kb0]\n" \
"v_xor_b32_e32 v210, 0xc0, %[kb0]\n" \
"v_xor_b32_e64 v212, 64, %[kd0]\n" \
"v_xor_b32_e32 v213, 0x80, %[kd0]\n" \
"v_xor_b32_e32 v214, 0xc0, %[kd0]\n" \
"v_xor_b32_e64 v211, 32, %[vvb]\n" \
"v_add_u32_e32 v215, 0xfffffc80, %[vd0]\n" \
"s_mov_b32 s66, 0\n" \
"s_nop 0\n" \
"v_mov_b32_e32 v225, 0xff800000\n" \
"v_mov_b32_e32 v226, 0xff800000\n" \
"v_mov_b32_e32 v227, 0xff800000\n" \
"v_mov_b32_e32 v228, 0xff800000\n" \
"s_waitcnt vmcnt(0) lgkmcnt(0)\n" \
"s_barrier\n" \
"Lsl_%=:\n" \
"s_mov_b32 m0, s40\n" \
"s_nop 0\n" \
"buffer_load_dwordx4 %[kd0], %[srdk], s48 offen offset:0 lds\n" \
"buffer_load_dwordx4 v212, %[srdk], s48 offen offset:1024 lds\n" \
"buffer_load_dwordx4 v213, %[srdk], s48 offen offset:2048 lds\n" \
"buffer_load_dwordx4 v214, %[srdk], s48 offen offset:3072 lds\n" \
"s_waitcnt vmcnt(0)\n" \
"s_barrier\n" \
"ds_read_b128 a[64:67], %[kb0] offset:0\n" \
"ds_read_b128 a[68:71], v208 offset:0\n" \
"ds_read_b128 a[72:75], v209 offset:0\n" \
"ds_read_b128 a[76:79], v210 offset:0\n" \
"ds_read_b128 a[80:83], %[kb0] offset:4096\n" \
"ds_read_b128 a[84:87], v208 offset:4096\n" \
"ds_read_b128 a[88:91], v209 offset:4096\n" \
"ds_read_b128 a[92:95], v210 offset:4096\n" \
"ds_read_b128 a[96:99], %[kb0] offset:8192\n" \
"ds_read_b128 a[100:103], v208 offset:8192\n" \
"ds_read_b128 a[104:107], v209 offset:8192\n" \
"ds_read_b128 a[108:111], v210 offset:8192\n" \
"ds_read_b128 a[112:115], %[kb0] offset:12288\n" \
"ds_read_b128 a[116:119], v208 offset:12288\n" \
"ds_read_b128 a[120:123], v209 offset:12288\n" \
"ds_read_b128 a[124:127], v210 offset:12288\n" \
"s_waitcnt lgkmcnt(0)\n" \
"s_barrier\n" \
"v_mfma_f32_16x16x32_bf16 v[128:131], a[64:67], a[0:3], 0\n" \
"v_mfma_f32_16x16x32_bf16 v[128:131], a[68:71], a[4:7], v[128:131]\n" \
"v_mfma_f32_16x16x32_bf16 v[128:131], a[72:75], a[8:11], v[128:131]\n" \
"v_mfma_f32_16x16x32_bf16 v[128:131], a[76:79], a[12:15], v[128:131]\n" \
"v_mfma_f32_16x16x32_bf16 v[132:135], a[80:83], a[0:3], 0\n" \
"v_mfma_f32_16x16x32_bf16 v[132:135], a[84:87], a[4:7], v[132:135]\n" \
"v_mfma_f32_16x16x32_bf16 v[132:135], a[88:91], a[8:11], v[132:135]\n" \
"v_mfma_f32_16x16x32_bf16 v[132:135], a[92:95], a[12:15], v[132:135]\n" \
"v_mfma_f32_16x16x32_bf16 v[136:139], a[64:67], a[16:19], 0\n" \
"v_mfma_f32_16x16x32_bf16 v[136:139], a[68:71], a[20:23], v[136:139]\n" \
"v_mfma_f32_16x16x32_bf16 v[136:139], a[72:75], a[24:27], v[136:139]\n" \
"v_mfma_f32_16x16x32_bf16 v[136:139], a[76:79], a[28:31], v[136:139]\n" \
"v_mfma_f32_16x16x32_bf16 v[140:143], a[80:83], a[16:19], 0\n" \
"v_mfma_f32_16x16x32_bf16 v[140:143], a[84:87], a[20:23], v[140:143]\n" \
"v_mfma_f32_16x16x32_bf16 v[140:143], a[88:91], a[24:27], v[140:143]\n" \
"v_mfma_f32_16x16x32_bf16 v[140:143], a[92:95], a[28:31], v[140:143]\n" \
"v_mfma_f32_16x16x32_bf16 v[144:147], a[64:67], a[32:35], 0\n" \
"v_mfma_f32_16x16x32_bf16 v[144:147], a[68:71], a[36:39], v[144:147]\n" \
"v_mfma_f32_16x16x32_bf16 v[144:147], a[72:75], a[40:43], v[144:147]\n" \
"v_mfma_f32_16x16x32_bf16 v[144:147], a[76:79], a[44:47], v[144:147]\n" \
"v_mfma_f32_16x16x32_bf16 v[148:151], a[80:83], a[32:35], 0\n" \
"v_mfma_f32_16x16x32_bf16 v[148:151], a[84:87], a[36:39], v[148:151]\n" \
"v_mfma_f32_16x16x32_bf16 v[148:151], a[88:91], a[40:43], v[148:151]\n" \
"v_mfma_f32_16x16x32_bf16 v[148:151], a[92:95], a[44:47], v[148:151]\n" \
"v_mfma_f32_16x16x32_bf16 v[152:155], a[64:67], a[48:51], 0\n" \
"v_mfma_f32_16x16x32_bf16 v[152:155], a[68:71], a[52:55], v[152:155]\n" \
"v_mfma_f32_16x16x32_bf16 v[152:155], a[72:75], a[56:59], v[152:155]\n" \
"v_mfma_f32_16x16x32_bf16 v[152:155], a[76:79], a[60:63], v[152:155]\n" \
"v_mfma_f32_16x16x32_bf16 v[156:159], a[80:83], a[48:51], 0\n" \
"v_mfma_f32_16x16x32_bf16 v[156:159], a[84:87], a[52:55], v[156:159]\n" \
"v_mfma_f32_16x16x32_bf16 v[156:159], a[88:91], a[56:59], v[156:159]\n" \
"v_mfma_f32_16x16x32_bf16 v[156:159], a[92:95], a[60:63], v[156:159]\n" \
"s_nop 7\n" \
"s_nop 0\n" \
"v_max3_f32 v230, v128, v129, v130\n" \
"v_max3_f32 v231, v131, v132, v133\n" \
"v_max3_f32 v230, v230, v134, v135\n" \
"v_max3_f32 v225, v230, v231, v225\n" \
"v_max3_f32 v230, v136, v137, v138\n" \
"v_max3_f32 v231, v139, v140, v141\n" \
"v_max3_f32 v230, v230, v142, v143\n" \
"v_max3_f32 v226, v230, v231, v226\n" \
"v_max3_f32 v230, v144, v145, v146\n" \
"v_max3_f32 v231, v147, v148, v149\n" \
"v_max3_f32 v230, v230, v150, v151\n" \
"v_max3_f32 v227, v230, v231, v227\n" \
"v_max3_f32 v230, v152, v153, v154\n" \
"v_max3_f32 v231, v155, v156, v157\n" \
"v_max3_f32 v230, v230, v158, v159\n" \
"v_max3_f32 v228, v230, v231, v228\n" \
"s_nop 3\n" \
"s_nop 0\n" \
"v_mfma_f32_16x16x32_bf16 v[128:131], a[96:99], a[0:3], 0\n" \
"v_mfma_f32_16x16x32_bf16 v[128:131], a[100:103], a[4:7], v[128:131]\n" \
"v_mfma_f32_16x16x32_bf16 v[128:131], a[104:107], a[8:11], v[128:131]\n" \
"v_mfma_f32_16x16x32_bf16 v[128:131], a[108:111], a[12:15], v[128:131]\n" \
"v_mfma_f32_16x16x32_bf16 v[132:135], a[112:115], a[0:3], 0\n" \
"v_mfma_f32_16x16x32_bf16 v[132:135], a[116:119], a[4:7], v[132:135]\n" \
"v_mfma_f32_16x16x32_bf16 v[132:135], a[120:123], a[8:11], v[132:135]\n" \
"v_mfma_f32_16x16x32_bf16 v[132:135], a[124:127], a[12:15], v[132:135]\n" \
"v_mfma_f32_16x16x32_bf16 v[136:139], a[96:99], a[16:19], 0\n" \
"v_mfma_f32_16x16x32_bf16 v[136:139], a[100:103], a[20:23], v[136:139]\n" \
"v_mfma_f32_16x16x32_bf16 v[136:139], a[104:107], a[24:27], v[136:139]\n" \
"v_mfma_f32_16x16x32_bf16 v[136:139], a[108:111], a[28:31], v[136:139]\n" \
"v_mfma_f32_16x16x32_bf16 v[140:143], a[112:115], a[16:19], 0\n" \
"v_mfma_f32_16x16x32_bf16 v[140:143], a[116:119], a[20:23], v[140:143]\n" \
"v_mfma_f32_16x16x32_bf16 v[140:143], a[120:123], a[24:27], v[140:143]\n" \
"v_mfma_f32_16x16x32_bf16 v[140:143], a[124:127], a[28:31], v[140:143]\n" \
"v_mfma_f32_16x16x32_bf16 v[144:147], a[96:99], a[32:35], 0\n" \
"v_mfma_f32_16x16x32_bf16 v[144:147], a[100:103], a[36:39], v[144:147]\n" \
"v_mfma_f32_16x16x32_bf16 v[144:147], a[104:107], a[40:43], v[144:147]\n" \
"v_mfma_f32_16x16x32_bf16 v[144:147], a[108:111], a[44:47], v[144:147]\n" \
"v_mfma_f32_16x16x32_bf16 v[148:151], a[112:115], a[32:35], 0\n" \
"v_mfma_f32_16x16x32_bf16 v[148:151], a[116:119], a[36:39], v[148:151]\n" \
"v_mfma_f32_16x16x32_bf16 v[148:151], a[120:123], a[40:43], v[148:151]\n" \
"v_mfma_f32_16x16x32_bf16 v[148:151], a[124:127], a[44:47], v[148:151]\n" \
"v_mfma_f32_16x16x32_bf16 v[152:155], a[96:99], a[48:51], 0\n" \
"v_mfma_f32_16x16x32_bf16 v[152:155], a[100:103], a[52:55], v[152:155]\n" \
"v_mfma_f32_16x16x32_bf16 v[152:155], a[104:107], a[56:59], v[152:155]\n" \
"v_mfma_f32_16x16x32_bf16 v[152:155], a[108:111], a[60:63], v[152:155]\n" \
"v_mfma_f32_16x16x32_bf16 v[156:159], a[112:115], a[48:51], 0\n" \
"v_mfma_f32_16x16x32_bf16 v[156:159], a[116:119], a[52:55], v[156:159]\n" \
"v_mfma_f32_16x16x32_bf16 v[156:159], a[120:123], a[56:59], v[156:159]\n" \
"v_mfma_f32_16x16x32_bf16 v[156:159], a[124:127], a[60:63], v[156:159]\n" \
"s_nop 7\n" \
"s_nop 0\n" \
"v_max3_f32 v230, v128, v129, v130\n" \
"v_max3_f32 v231, v131, v132, v133\n" \
"v_max3_f32 v230, v230, v134, v135\n" \
"v_max3_f32 v225, v230, v231, v225\n" \
"v_max3_f32 v230, v136, v137, v138\n" \
"v_max3_f32 v231, v139, v140, v141\n" \
"v_max3_f32 v230, v230, v142, v143\n" \
"v_max3_f32 v226, v230, v231, v226\n" \
"v_max3_f32 v230, v144, v145, v146\n" \
"v_max3_f32 v231, v147, v148, v149\n" \
"v_max3_f32 v230, v230, v150, v151\n" \
"v_max3_f32 v227, v230, v231, v227\n" \
"v_max3_f32 v230, v152, v153, v154\n" \
"v_max3_f32 v231, v155, v156, v157\n" \
"v_max3_f32 v230, v230, v158, v159\n" \
"v_max3_f32 v228, v230, v231, v228\n" \
"s_nop 3\n" \
"s_add_u32 s48, s48, s54\n" \
"s_sub_u32 s51, s51, 1\n" \
"s_cmp_lg_u32 s51, 0\n" \
"s_cbranch_scc1 Lsl_%=\n" \
"s_nop 0\n" \
"v_mov_b32_e32 v232, v225\n" \
"v_mov_b32_e32 v233, v226\n" \
"v_mov_b32_e32 v234, v227\n" \
"v_mov_b32_e32 v235, v228\n" \
"s_nop 0\n" \
"v_permlane16_swap_b32_e32 v225, v232\n" \
"v_permlane16_swap_b32_e32 v226, v233\n" \
"v_permlane16_swap_b32_e32 v227, v234\n" \
"v_permlane16_swap_b32_e32 v228, v235\n" \
"v_max_f32_e32 v225, v225, v232\n" \
"v_max_f32_e32 v226, v226, v233\n" \
"v_max_f32_e32 v227, v227, v234\n" \
"v_max_f32_e32 v228, v228, v235\n" \
"v_mov_b32_e32 v232, v225\n" \
"v_mov_b32_e32 v233, v226\n" \
"v_mov_b32_e32 v234, v227\n" \
"v_mov_b32_e32 v235, v228\n" \
"s_nop 0\n" \
"v_permlane32_swap_b32_e32 v225, v232\n" \
"v_permlane32_swap_b32_e32 v226, v233\n" \
"v_permlane32_swap_b32_e32 v227, v234\n" \
"v_permlane32_swap_b32_e32 v228, v235\n" \
"v_max_f32_e32 v225, v225, v232\n" \
"v_max_f32_e32 v226, v226, v233\n" \
"v_max_f32_e32 v227, v227, v234\n" \
"v_max_f32_e32 v228, v228, v235\n" \
"s_cmp_lg_u32 s69, 0\n" \
"s_cbranch_scc1 Lnewm_%=\n" \
"v_sub_f32_e32 v225, 0, v160\n" \
"v_sub_f32_e32 v226, 0, v164\n" \
"v_sub_f32_e64 v227, 0, v168\n" \
"v_sub_f32_e32 v228, 0, v172\n" \
"Lnewm_%=:\n" \
"s_mov_b32 s48, 0\n" \
"s_mov_b32 m0, s40\n" \
"s_nop 0\n" \
"buffer_load_dwordx4 %[kd0], %[srdk], s48 offen offset:0 lds\n" \
"buffer_load_dwordx4 v212, %[srdk], s48 offen offset:1024 lds\n" \
"buffer_load_dwordx4 v213, %[srdk], s48 offen offset:2048 lds\n" \
"buffer_load_dwordx4 v214, %[srdk], s48 offen offset:3072 lds\n" \
"s_mov_b32 s48, 16384\n" \
"s_mov_b32 m0, s41\n" \
"s_nop 0\n" \
"buffer_load_dwordx4 %[kd0], %[srdk], s48 offen offset:0 lds\n" \
"buffer_load_dwordx4 v212, %[srdk], s48 offen offset:1024 lds\n" \
"buffer_load_dwordx4 v213, %[srdk], s48 offen offset:2048 lds\n" \
"buffer_load_dwordx4 v214, %[srdk], s48 offen offset:3072 lds\n" \
"s_add_u32 m0, %[m0b], 65536\n" \
"s_nop 0\n" \
"s_nop 0\n" \
"buffer_load_dwordx4 %[vd0], %[srdv], s48 offen offset:0 lds\n" \
"buffer_load_dwordx4 v215, %[srdv], s48 offen offset:1024 lds\n" \
"buffer_load_dwordx4 %[vd0], %[srdv], s48 offen offset:2048 lds\n" \
"buffer_load_dwordx4 v215, %[srdv], s48 offen offset:3072 lds\n" \
"s_mov_b32 s48, 32768\n" \
"s_mov_b32 m0, s42\n" \
"s_nop 0\n" \
"buffer_load_dwordx4 %[kd0], %[srdk], s48 offen offset:0 lds\n" \
"buffer_load_dwordx4 v212, %[srdk], s48 offen offset:1024 lds\n" \
"buffer_load_dwordx4 v213, %[srdk], s48 offen offset:2048 lds\n" \
"buffer_load_dwordx4 v214, %[srdk], s48 offen offset:3072 lds\n" \
"s_add_u32 m0, %[m0b], 81920\n" \
"s_nop 0\n" \
"s_nop 0\n" \
"buffer_load_dwordx4 %[vd0], %[srdv], s48 offen offset:0 lds\n" \
"buffer_load_dwordx4 v215, %[srdv], s48 offen offset:1024 lds\n" \
"buffer_load_dwordx4 %[vd0], %[srdv], s48 offen offset:2048 lds\n" \
"buffer_load_dwordx4 v215, %[srdv], s48 offen offset:3072 lds\n" \
"s_mov_b32 s48, 49152\n" \
"s_mov_b32 m0, s43\n" \
"s_nop 0\n" \
"buffer_load_dwordx4 %[kd0], %[srdk], s48 offen offset:0 lds\n" \
"buffer_load_dwordx4 v212, %[srdk], s48 offen offset:1024 lds\n" \
"buffer_load_dwordx4 v213, %[srdk], s48 offen offset:2048 lds\n" \
"buffer_load_dwordx4 v214, %[srdk], s48 offen offset:3072 lds\n" \
"s_add_u32 m0, %[m0b], 98304\n" \
"s_nop 0\n" \
"s_nop 0\n" \
"buffer_load_dwordx4 %[vd0], %[srdv], s48 offen offset:0 lds\n" \
"buffer_load_dwordx4 v215, %[srdv], s48 offen offset:1024 lds\n" \
"buffer_load_dwordx4 %[vd0], %[srdv], s48 offen offset:2048 lds\n" \
"buffer_load_dwordx4 v215, %[srdv], s48 offen offset:3072 lds\n" \
"s_waitcnt vmcnt(0)\n" \
"s_barrier\n" \
"ds_read_b128 a[64:67], %[kb0] offset:0\n" \
"ds_read_b128 a[68:71], v208 offset:0\n" \
"ds_read_b128 a[72:75], v209 offset:0\n" \
"ds_read_b128 a[76:79], v210 offset:0\n" \
"ds_read_b128 a[80:83], %[kb0] offset:4096\n" \
"ds_read_b128 a[84:87], v208 offset:4096\n" \
"ds_read_b128 a[88:91], v209 offset:4096\n" \
"ds_read_b128 a[92:95], v210 offset:4096\n" \
"s_mov_b32 s48, 65536\n" \
"s_mov_b32 s60, 0\n" \
"s_waitcnt lgkmcnt(0)\n" \
"v_mfma_f32_16x16x32_bf16 v[128:131], a[64:67], a[0:3], 0\n" \
"ds_read_b128 a[96:99], %[kb0] offset:8192\n" \
"v_mfma_f32_16x16x32_bf16 v[128:131], a[68:71], a[4:7], v[128:131]\n" \
"ds_read_b128 a[100:103], v208 offset:8192\n" \
"v_mfma_f32_16x16x32_bf16 v[128:131], a[72:75], a[8:11], v[128:131]\n" \
"ds_read_b128 a[104:107], v209 offset:8192\n" \
"v_mfma_f32_16x16x32_bf16 v[128:131], a[76:79], a[12:15], v[128:131]\n" \
"ds_read_b128 a[108:111], v210 offset:8192\n" \
"v_mfma_f32_16x16x32_bf16 v[132:135], a[80:83], a[0:3], 0\n" \
"ds_read_b128 a[112:115], %[kb0] offset:12288\n" \
"v_mfma_f32_16x16x32_bf16 v[132:135], a[84:87], a[4:7], v[132:135]\n" \
"ds_read_b128 a[116:119], v208 offset:12288\n" \
"v_mfma_f32_16x16x32_bf16 v[132:135], a[88:91], a[8:11], v[132:135]\n" \
"ds_read_b128 a[120:123], v209 offset:12288\n" \
"v_mfma_f32_16x16x32_bf16 v[132:135], a[92:95], a[12:15], v[132:135]\n" \
"ds_read_b128 a[124:127], v210 offset:12288\n" \
"v_mfma_f32_16x16x32_bf16 v[136:139], a[64:67], a[16:19], 0\n" \
"ds_read_b64_tr_b16 a[128:129], %[vvb] offset:0\n" \
"v_mfma_f32_16x16x32_bf16 v[136:139], a[68:71], a[20:23], v[136:139]\n" \
"ds_read_b64_tr_b16 a[130:131], %[vvb] offset:4096\n" \
"v_mfma_f32_16x16x32_bf16 v[136:139], a[72:75], a[24:27], v[136:139]\n" \
"ds_read_b64_tr_b16 a[132:133], v211 offset:0\n" \
"v_mfma_f32_16x16x32_bf16 v[136:139], a[76:79], a[28:31], v[136:139]\n" \
"ds_read_b64_tr_b16 a[134:135], v211 offset:4096\n" \
"v_mfma_f32_16x16x32_bf16 v[140:143], a[80:83], a[16:19], 0\n" \
"ds_read_b64_tr_b16 a[136:137], %[vvb] offset:512\n" \
"v_mfma_f32_16x16x32_bf16 v[140:143], a[84:87], a[20:23], v[140:143]\n" \
"ds_read_b64_tr_b16 a[138:139], %[vvb] offset:4608\n" \
"v_mfma_f32_16x16x32_bf16 v[140:143], a[88:91], a[24:27], v[140:143]\n" \
"ds_read_b64_tr_b16 a[140:141], v211 offset:512\n" \
"v_mfma_f32_16x16x32_bf16 v[140:143], a[92:95], a[28:31], v[140:143]\n" \
"ds_read_b64_tr_b16 a[142:143], v211 offset:4608\n" \
"v_mfma_f32_16x16x32_bf16 v[144:147], a[64:67], a[32:35], 0\n" \
"ds_read_b64_tr_b16 a[144:145], %[vvb] offset:1024\n" \
"v_mfma_f32_16x16x32_bf16 v[144:147], a[68:71], a[36:39], v[144:147]\n" \
"ds_read_b64_tr_b16 a[146:147], %[vvb] offset:5120\n" \
"v_mfma_f32_16x16x32_bf16 v[144:147], a[72:75], a[40:43], v[144:147]\n" \
"ds_read_b64_tr_b16 a[148:149], v211 offset:1024\n" \
"v_mfma_f32_16x16x32_bf16 v[144:147], a[76:79], a[44:47], v[144:147]\n" \
"ds_read_b64_tr_b16 a[150:151], v211 offset:5120\n" \
"v_mfma_f32_16x16x32_bf16 v[148:151], a[80:83], a[32:35], 0\n" \
"ds_read_b64_tr_b16 a[152:153], %[vvb] offset:1536\n" \
"v_mfma_f32_16x16x32_bf16 v[148:151], a[84:87], a[36:39], v[148:151]\n" \
"ds_read_b64_tr_b16 a[154:155], %[vvb] offset:5632\n" \
"v_mfma_f32_16x16x32_bf16 v[148:151], a[88:91], a[40:43], v[148:151]\n" \
"ds_read_b64_tr_b16 a[156:157], v211 offset:1536\n" \
"v_mfma_f32_16x16x32_bf16 v[148:151], a[92:95], a[44:47], v[148:151]\n" \
"ds_read_b64_tr_b16 a[158:159], v211 offset:5632\n" \
"v_mfma_f32_16x16x32_bf16 v[152:155], a[64:67], a[48:51], 0\n" \
"v_mfma_f32_16x16x32_bf16 v[152:155], a[68:71], a[52:55], v[152:155]\n" \
"v_mfma_f32_16x16x32_bf16 v[152:155], a[72:75], a[56:59], v[152:155]\n" \
"v_mfma_f32_16x16x32_bf16 v[152:155], a[76:79], a[60:63], v[152:155]\n" \
"v_mfma_f32_16x16x32_bf16 v[156:159], a[80:83], a[48:51], 0\n" \
"v_mfma_f32_16x16x32_bf16 v[156:159], a[84:87], a[52:55], v[156:159]\n" \
"v_mfma_f32_16x16x32_bf16 v[156:159], a[88:91], a[56:59], v[156:159]\n" \
"v_mfma_f32_16x16x32_bf16 v[156:159], a[92:95], a[60:63], v[156:159]\n" \
"s_branch Lhavem_%=\n" \
"s_nop 0\n" \
"Lend_%=:\n" \
"s_waitcnt lgkmcnt(0)\n" \
"s_nop 0\n" \
""

#define SRD_WORD3 0x00020000
DI u32x4 ld16_sc1(const void* p) { u32x4 v; asm volatile("global_load_dwordx4 %0, %1, off sc1" : "=v"(v) : "v"(p) : "memory"); return v; }
DI unsigned ord_key(float r, unsigned row) {
  const unsigned b = __float_as_uint(r), o = (b & 0x80000000u) ? ~b : (b | 0x80000000u);
  return max(o & 0xfffff800u, 0x800u) | (2047u - row);
}
DI void top2_merge(unsigned& a1, unsigned& a2, unsigned b1, unsigned b2) {
  const unsigned n1 = max(a1, b1), n2 = max(min(a1, b1), max(a2, b2)); a1 = n1; a2 = n2;
}
template <int CTRL> DI void top2_dpp(unsigned& k1, unsigned& k2) {
  const unsigned p1 = __builtin_amdgcn_update_dpp(0u, k1, CTRL, 0xf, 0xf, true), p2 = __builtin_amdgcn_update_dpp(0u, k2, CTRL, 0xf, 0xf, true); top2_merge(k1, k2, p1, p2);
}
DI float row_sum(float v) {
  v = dpp_add<DPP_XOR1>(v); v = dpp_add<DPP_XOR2>(v); v = dpp_add<DPP_ROR8>(v);
  v = swap16_add(v, v); return swap32_add(v, v);
}
DI unsigned h2_pair(float x0, float x1, float m, float rden, unsigned c1w, unsigned sbw) { return ln_pair(x0, x1, m, rden, lo16(c1w), hi16(c1w), lo16(sbw), hi16(sbw)); }
#define PAGE(n, nx) asm volatile("s_branch adaln_pe" #n "\n.p2align 12\nadaln_pg" #n ":\n\ts_cbranch_execz " nx "\nadaln_pe" #n ":")
constexpr int MOE_WGS = DFF / 16;
constexpr unsigned RETRY_CAP = 4096;
#define ASLEEP 2
#define KLOOK 4
#define YSPLIT 8
#define FSLEEP 14
#define KSLEEP 3
#define YEARLY 8
#define FREST 0
#define KL0 1u
#define KLS 2u
#define KST 2u
#define KCHK 8
#define KPAUSE 2
#define KEY_SEL b

#define WBYTES (moe ? DM * DFF * 2 : 0)
namespace fa {
constexpr unsigned LDS_BYTES = 163840;
constexpr float QSCALE = 0.12751743082f;
struct Args { const u16 *x, *wmod, *wq, *kc, *vc, *wo, *wg, *wu, *wd, *c, *wr; char* ws; u16* out;
};
__global__ __launch_bounds__(256, 1) void fattn_k(const Args p) {
  extern __shared__ __attribute__((aligned(16))) char lds[];
  const unsigned wid = __builtin_amdgcn_readfirstlane(threadIdx.x >> 6);
  const unsigned b = blockIdx.x, idx = b >> 3, kg = b & 3u, m = idx & 7u, hd = 8u * kg + m, y = (b & 4u) + (idx >> 3);
  const unsigned ldsb = (unsigned)(size_t)lds;
  const unsigned m0b = ldsb + 4096u * wid, m0t = ldsb + 131072u + 8192u * wid;
  const __amdgpu_buffer_rsrc_t srdz = __builtin_amdgcn_make_buffer_rsrc((void*)p.ws, (short)0, (int)WS_END, SRD_WORD3);
  const bool moe = b < MOE_WGS;
  const unsigned sl = 4u * ((b & 7u) * 7u + (idx >> 2)) + (idx & 3u), yp2 = (y & 1u) * 2u;
  const unsigned sin = (unsigned)WS_XIN + ((y * 32u + hd) * 4u + wid) * BOX;
  u32x2 xq[8]; unsigned fl, fl2, got, got2; unsigned long long tag;
  unsigned k1, k2; unsigned long long lat;
  const unsigned sxf = (unsigned)WS_XFL + (y * 4u + wid) * 512u, sxl = (unsigned)WS_XFL + (y * 4u + kg) * 512u;
#define FQ(i)
#define TQ(i)
  {
    const unsigned tid = threadIdx.x, lane = tid & 63u, l16 = lane & 15u, g = lane >> 4, hi = lane >> 5, r8 = (lane >> 2) & 7u;
    unsigned kb0 = ldsb + 256u * l16 + 16u * (g ^ l16);
    unsigned vvb = ldsb + 65536u + 2048u * hi + 288u * (g & 1u) + 64u * ((lane >> 2) & 3u) + 8u * (lane & 3u);
    unsigned vwb = ldsb + 65536u + 2048u * g + 64u * ((lane >> 2) & 3u) + 16u * (lane & 3u);
    unsigned kd0 = 4096u * wid + 256u * g + 16u * (l16 ^ g);
    unsigned vd0 = 4096u * wid + 256u * r8 + 64u * hi + 16u * ((lane & 3u) ^ (2u * (r8 >> 2))) + 1024u;
    unsigned wd0 = 8192u * (16u * wid + r8) + 64u * hi + 16u * (lane & 3u) + 4096u;
    unsigned hq0 = (unsigned)WS_H + (16u * y + 4u * wid) * 32768u + 16u * lane;
    unsigned oq0 = (unsigned)WS_XIN + ((y * 32u + kg * 8u) * 4u + wid) * BOX + 20480u + 16u * lane;
    const unsigned flag = ldsb + 49152u + 4u * wid;
    const __amdgpu_buffer_rsrc_t srdw = __builtin_amdgcn_make_buffer_rsrc((void*)((const char*)p.wq + 256u * hd - 4096), (short)0, (int)(DM * DQ * 2 - 256u * hd + 4096u), SRD_WORD3);
    const __amdgpu_buffer_rsrc_t srdk = __builtin_amdgcn_make_buffer_rsrc((void*)((const char*)p.kc + (size_t)hd * (SEQ * 256)), (short)0, SEQ * 256, SRD_WORD3);
    const __amdgpu_buffer_rsrc_t srdv = __builtin_amdgcn_make_buffer_rsrc((void*)((const char*)p.vc + (size_t)hd * (SEQ * 256) - 17408), (short)0, SEQ * 256 + 17408, SRD_WORD3);
    unsigned long long pct; unsigned clen;
    asm volatile("s_getpc_b64 %0\n\ts_mov_b32 %1, adaln_text_end@rel32@lo+4" : "=s"(pct), "=s"(clen));
    unsigned vct = min(((idx * 4u + wid) * 6u + min(lane, 5u)) * 128u, (clen - 4u) & ~3u);
#define XNT " nt"
#define WNT " nt"
      const unsigned gr = 128u * (b & 7u) + 4u * (tid >> 3), gc = 64u * (b >> 3) + 8u * (tid & 7u), gran0 = b;
      u32x4 w[4], xs[4]; u32x2 ep, cv;
#define XV(i) ({ u32x4 v_; asm volatile("v_accvgpr_read_b32 %0, a[%4]\n\tv_accvgpr_read_b32 %1, a[%4 + 1]\n\tv_accvgpr_read_b32 %2, a[%4 + 2]\n\tv_accvgpr_read_b32 %3, a[%4 + 3]" : "=v"(v_[0]), "=v"(v_[1]), "=v"(v_[2]), "=v"(v_[3]) : "n"(200 + 4 * (i))); v_; })
      asm volatile("global_load_dwordx2 %0, %1, %2" : "=v"(cv) : "v"(gr * 2u), "s"(p.c) : "memory");
      #pragma unroll
      for (int j = 0; j < 4; ++j) asm volatile("global_load_dwordx4 %0, %1, %2" WNT : "=v"(w[j]) : "v"(((gr + j) * NMOD + gc) * 2u), "s"(p.wmod) : "memory");
      #pragma unroll
      for (int q = 0; q < 4; ++q) asm volatile("global_load_dwordx4 %0, %1, %2 offset:%3" XNT : "=v"(xs[q]) : "v"((8u * (y * 32u + hd) + 2u * wid) * 2048u + 16u * lane), "s"(p.x), "n"(1024 * q) : "memory");
      asm volatile("global_load_dwordx2 %0, %1, %2 sc1" : "=v"(ep) : "v"(0u), "s"(p.ws + WS_EP) : "memory");
      {
        const unsigned zo = b * Z_SHARE + tid * 16u - (unsigned)(WS_U - WS_Z);
        u32x4 zv;
        #pragma unroll
        for (int e = 0; e < 4; ++e) { const unsigned h = (zo + 4u * e + 1u) * 2654435761u; zv[e] = zo >= 16384u ? 0u : NORM && (zo & 2048u) ? 0x3f803f80u | ((h ^ (h >> 15)) * 0x2c1b3c6du & 0x80008000u) : ~0u; }
        asm volatile("buffer_store_dwordx4 %0, %1, %2, 0 offen sc1\n\ts_nop 1" :: "v"(zv), "v"(tid < Z_SHARE / 16u ? (unsigned)WS_Z + b * Z_SHARE + tid * 16u : 0x80000000u), "s"(srdz) : "memory");
      }
      FQ(6);
      asm volatile("global_load_dword a255, %0, %1" :: "v"(vct), "s"(pct) : "memory");
#define PRET 0
      asm volatile("s_mov_b64 exec, 0\n\ts_cmp_eq_u32 %0, 1\n\ts_cbranch_scc1 adaln_pg5\n\ts_cmp_eq_u32 %0, 2\n\ts_cbranch_scc1 adaln_pg6\n\ts_cmp_eq_u32 %0, 3\n\ts_cbranch_scc1 adaln_pg7\nadaln_fr:\n\ts_mov_b64 exec, -1" :: "s"(wid) : "scc");
      FQ(7);
      float* red = (float*)(lds + 131072 + 4096);
      auto silu = [](float v) { return rbf(v * __builtin_amdgcn_rcpf(1.f + __builtin_amdgcn_exp2f(v * -1.44269504089f))); };
      asm volatile("s_waitcnt vmcnt(11)" : "+v"(cv) :: "memory");
      unsigned sp[2] = {cvtpk(silu(lo16(cv[0])), silu(hi16(cv[0]))), cvtpk(silu(lo16(cv[1])), silu(hi16(cv[1])))};
      unsigned gsa = lane < 4u ? (unsigned)WS_RST + ((8u * (y * 32u + hd) + 2u * wid) * 2u + lane) * 8u : 0x80000000u;
      unsigned gma = wid ? 0x80000000u : (unsigned)WS_MOD + (gran0 * 64u + lane) * 8u, gra = ldsb + 131072u + 4096u + lane * 4u;
      asm volatile("s_waitcnt vmcnt(7)" : "+v"(sp[0]), "+v"(sp[1]), "+v"(gsa), "+v"(gma), "+v"(gra), "+v"(w[0]), "+v"(w[1]), "+v"(w[2]), "+v"(w[3]) :: "memory");
      FQ(4);
      const unsigned rho = 0;
      unsigned tq0 = (idx * 8u + wid * 2u + ((lane >> 3) & 1u)) * 8192u + (b & 4u) * 256u + (lane & 7u) * 128u + 4096u - 256u * m;
#define WQT 0
#define KVT2 0
#define WQT2 4
#define TNT ""
      asm volatile("buffer_load_dword a255, %0, %1, 0 offen" TNT :: "v"(tq0), "s"(srdw) : "memory");
      {
        float acc[8];
        #pragma unroll
        for (int e = 0; e < 8; ++e) acc[e] = 0.f;
        #pragma unroll
        for (int j = 0; j < 2; ++j) {
          #pragma unroll
          for (int e = 0; e < 8; ++e) acc[e] = __builtin_amdgcn_fdot2_f32_bf16(__builtin_bit_cast(bf16x2, __builtin_amdgcn_perm(w[2 * j + 1][e >> 1], w[2 * j][e >> 1], (e & 1) ? 0x07060302u : 0x05040100u)), __builtin_bit_cast(bf16x2, sp[j]), acc[e], false);
        }
        float t[4], u[2];
        #pragma unroll
        for (int e = 0; e < 4; ++e) t[e] = swap32_add(acc[e], acc[e + 4]);
        #pragma unroll
        for (int e = 0; e < 2; ++e) u[e] = dpp_add<DPP_ROR8>(swap16_add(t[e], t[e + 2]));
        if ((lane & 8u) == 0) *(f32x2*)&red[wid * 64u + 8u * (tid & 7u) + 2u * g] = f32x2{u[0], u[1]};
      }
      unsigned gst, vx0r, vx1r;
      auto rowstats = [&]() __attribute__((always_inline)) {
      float stv4;
      {
        float t4[4] = {0.f, 0.f, 0.f, 0.f};
        #pragma unroll
        for (int e = 0; e < 16; ++e) {
          const unsigned xw = xs[e >> 2][e & 3]; const bf16x2 v = __builtin_bit_cast(bf16x2, xw);
          t4[2 * (e >> 3)] = __builtin_amdgcn_fdot2_f32_bf16(v, __builtin_bit_cast(bf16x2, 0x3f803f80u), t4[2 * (e >> 3)], false);
          t4[2 * (e >> 3) + 1] = __builtin_amdgcn_fdot2_f32_bf16(v, v, t4[2 * (e >> 3) + 1], false);
        }
        float u = swap16_add(swap32_add(t4[0], t4[2]), swap32_add(t4[1], t4[3]));
        u = dpp_add<DPP_XOR1>(u); u = dpp_add<DPP_XOR2>(u); u = dpp_add<DPP_ROR4>(u); u = dpp_add<DPP_ROR8>(u);
        const f32x2 mean = f32x2{__uint_as_float(__builtin_amdgcn_readlane(__float_as_uint(u), 0)), __uint_as_float(__builtin_amdgcn_readlane(__float_as_uint(u), 32))} * (1.f / DM);
        f32x2 v2 = f32x2{__uint_as_float(__builtin_amdgcn_readlane(__float_as_uint(u), 16)), __uint_as_float(__builtin_amdgcn_readlane(__float_as_uint(u), 48))} * (1.f / DM) - mean * mean;
        if (__builtin_expect(!(mean[0] * mean[0] <= v2[0] && mean[1] * mean[1] <= v2[1]), 0)) {
          f32x2 sq = {0.f, 0.f};
          #pragma unroll
          for (int e = 0; e < 8; ++e) {
            const f32x2 d0 = f32x2{lo16(xs[e >> 2][e & 3]), lo16(xs[2 + (e >> 2)][e & 3])} - mean, d1 = f32x2{hi16(xs[e >> 2][e & 3]), hi16(xs[2 + (e >> 2)][e & 3])} - mean;
            sq = __builtin_elementwise_fma(d1, d1, __builtin_elementwise_fma(d0, d0, sq));
          }
          v2 = f32x2{wave_sum(sq[0]), wave_sum(sq[1])} * (1.f / DM);
        }
        asm("s_nop 1\n\tv_readfirstlane_b32 %0, %2\n\tv_readfirstlane_b32 %1, %3" : "=&s"(vx0r), "=&s"(vx1r) : "v"(v2[0] + 1e-5f), "v"(v2[1] + 1e-5f));
        const float rs_ = __builtin_amdgcn_rcpf(rbf(__builtin_amdgcn_sqrtf(rbf(rbf((lane & 2u) ? v2[1] : v2[0]) + 1e-5f))));
        stv4 = rs_ * ((lane & 1u) ? 1.f : -rbf((lane & 2u) ? mean[1] : mean[0]));
      }
      gst = (__float_as_uint(stv4) + 0x80u) & 0xffffff00u; };
#define STP 0
      if (STP == 0) { asm volatile("s_waitcnt vmcnt(4)" : "+v"(xs[0]), "+v"(xs[1]), "+v"(xs[2]), "+v"(xs[3]) :: "memory"); rowstats(); asm volatile("" : "+v"(gst)); }
      asm volatile("s_waitcnt vmcnt(%5) lgkmcnt(0)\n\ts_barrier" : "+v"(xs[0]), "+v"(xs[1]), "+v"(xs[2]), "+v"(xs[3]), "+v"(ep) : "n"(PRET ? 3 : 2) : "memory");

      tag = (((unsigned long long)__builtin_amdgcn_readfirstlane(ep[1]) << 32) | __builtin_amdgcn_readfirstlane(ep[0])) + 1ull;
      const unsigned tl = (unsigned)tag & 0xffu, th = (unsigned)(tag >> 8);
      auto gran = [&](float v) { return ((unsigned long long)th << 32) | (((__float_as_uint(v) + 0x80u) & 0xffffff00u) | tl); };
      {
        float r0, r1, r2, r3;
        if (STP == 0) asm volatile("buffer_store_dwordx2 %0, %1, %2, 0 offen sc1" :: "v"(u32x2{gst | tl, th}), "v"(gsa), "s"(srdz) : "memory");
        asm volatile("ds_read_b32 %0, %4\n\tds_read_b32 %1, %4 offset:256\n\tds_read_b32 %2, %4 offset:512\n\tds_read_b32 %3, %4 offset:768\n\ts_waitcnt lgkmcnt(0)"
                     : "=&v"(r0), "=&v"(r1), "=&v"(r2), "=&v"(r3) : "v"(gra) : "memory");
        asm volatile("buffer_store_dwordx2 %0, %1, %2, 0 offen sc1\n\ts_nop 0" :: "v"(u32x2{((__float_as_uint((r0 + r1) + (r2 + r3)) + 0x80u) & 0xffffff00u) | tl, th}), "v"(gma), "s"(srdz) : "memory");
        if (STP == 1) { rowstats(); asm volatile("buffer_store_dwordx2 %0, %1, %2, 0 offen sc1\n\ts_nop 0" :: "v"(u32x2{gst | tl, th}), "v"(gsa), "s"(srdz) : "memory"); }
      }
      FQ(5);
#define SWQ 0
#define SWQ0 0x400000
      const unsigned rb0 = 16u * y + 4u * (hd >> 3), ch = 16u * (hd & 7u) + (tid >> 4);
      asm volatile("global_load_dwordx2 a[216:217], %0, %1" :: "v"(tid * 8u), "s"(p.c) : "memory");
      #pragma unroll
      for (int i = 0; i < 4; ++i) asm volatile("global_load_dwordx4 a[%2:%2 + 3], %0, %1" :: "v"(((rb0 + i) * 16u + l16) * 2048u + ch * 16u), "s"(p.x), "n"(200 + 4 * i) : "memory");
      PAGE(5, "adaln_fr");
      unsigned lane2 = lane; asm volatile("" : "+v"(lane2));
#define lane lane2
    unsigned wo0 = (32u * wid + r8) * 2048u + m * 256u + 64u * hi + 16u * ((lane & 3u) ^ (2u * (r8 >> 2)));
    const __amdgpu_buffer_rsrc_t srdt = __builtin_amdgcn_make_buffer_rsrc((void*)((const char*)p.wo + (size_t)kg * (DM * DM * 2)), (short)0, DM * DM * 2, SRD_WORD3);
    const __amdgpu_buffer_rsrc_t srdp = __builtin_amdgcn_make_buffer_rsrc((void*)((const char*)(y < 4u ? p.kc : p.vc) + (size_t)hd * (SEQ * 256) + (y & 3u) * 4096u), (short)0, (int)(KVPAR && (b & 1u) ? 0u : (KVPAR ? 0x400000u : 0u) + SEQ * 256 - (y & 3u) * 4096u), SRD_WORD3);
    unsigned vg0 = (256u * wid + (lane >> 1)) * (DFF * 2u) + sl * 32u + 16u * (lane & 1u);
    unsigned vdn = (16u * sl + hi) * 2048u + 512u * wid + 16u * ((lane & 31u) ^ (2u * hi));
    const unsigned mc0 = 2u * DM + 64u * ((idx >> 2) * 8u + (b & 7u)) + 16u * (idx & 3u);
    const unsigned md0 = (256u * wid + (lane >> 1)) * (NMOD * 2u) + mc0 * 2u + 16u * (lane & 1u);
    unsigned sd0 = ldsb + 131072u + 8192u + 512u * wid + 16u * g;
    fl = md0;
    unsigned rd0 = ldsb + 131072u + 7168u + 8192u * ((lane >> 2) & 3u) + 256u * wid + 4u * (lane & 3u);
    unsigned ta0 = m0t + 256u * g + 32u * ((lane >> 2) & 3u) + 8u * (lane & 3u);
    const unsigned c8 = (b * 4u + wid) * 8192u, cd = moe ? sl * 32768u + wid * 8192u : 0u, cw = (idx * 4u + wid) * 16384u;
    const unsigned cm = moe ? ((sl & 3u) * 4u + wid) * 64u * (DFF * 2u) + (sl >> 2) * 128u : 0u;
    unsigned vxs = BOX * (l16 >> 1) + 64u * (l16 & 1u) + 16u * g;
    const unsigned sxs = (unsigned)WS_XIN + (y * 128u + wid * 32u) * BOX + (m >> 1) * 1024u + kg * 256u + (m & 1u) * 128u;
    unsigned vx0 = ((256u * y + 8u * hd + 2u * wid + ((lane >> 2) & 1u)) * DM + 256u * hi + 128u * (l16 >> 3) + 16u * (g & 1u) + 4u * (lane & 3u) + 32u * yp2) * 2u;
    unsigned tlo = 0, thi = 0;
#define tab(ln, lo, hi2) asm("v_writelane_b32 %0, %2, " #ln "\n\tv_writelane_b32 %1, %3, " #ln : "+v"(tlo), "+v"(thi) : "s"(lo), "s"(hi2))
#define tabp(ln, q, off) do { const unsigned long long a_ = (size_t)(q) + (off); tab(ln, (unsigned)a_, (unsigned)(a_ >> 32)); } while (0)
#define TORD 0
#define TAB_X(n)  tabp(n, p.x, ((y * 32u + hd) * 2u + (wid & 1u)) * 8192u)
#define TAB_U(n)  tabp(n, p.wu, moe ? c8 : 0u)
#define TAB_O(n)  tabp(n, p.wo, c8)
#define TAB_D(n)  tabp(n, p.wd, cd)
    TAB_X(1); TAB_U(2); TAB_O(3); TAB_D(4);
    tabp(5, p.wo, kg * 2097152u + cw); tabp(6, p.wo, kg * 2097152u + cw + 8192u); tabp(7, p.wg, cm);
    tabp(17, p.wmod, 0u); tab(18, NP_FILL == 8 ? sin + 20480u : (unsigned)WS_XIN + ((y * 32u + kg * 8u + (m >> 1)) * 4u + wid) * BOX + 20480u + (m & 1u) * 8192u, (unsigned)WS_OFL + y * 16384u + kg * 128u);
    tab(19, (unsigned)WS_OFL + (y * 32u + hd) * 512u + wid * 128u, (unsigned)WS_OFL + (y * 32u + kg * 8u) * 512u + wid * 128u);
    tabp(8, p.wg, 0u); tabp(9, p.wu, 0u); tabp(10, p.wd, 0u);
    tab(11, m, m * 4u * BOX); tab(12, (y & 1u) * 128u, 128u - (y & 1u) * 128u);
    tab(13, sxl, (unsigned)WS_PFL + (y * 32u + hd) * 512u + kg * 128u); tab(14, sxs, sxf);
    tab(15, (unsigned)WS_MODT + (mc0 - 2u * DM + 4u * wid) * 4u, (mc0 >> 10) == 4u ? 0x3f800000u : 0u);
    tab(16, (unsigned)WS_PFL + y * 16384u + (hd * 4u + wid) * 4u, (unsigned)WS_PFL + (y * 32u + hd) * 512u + wid * 128u);
    const char* psig = p.ws + WS_CNT + y * 8192u + hd * 8u;
    const char* pcnt = p.ws + WS_CNT + y * 8192u + hd * 256u;
    tabp(20, p.wr, 0u); tab(21, (unsigned)WS_PART + b * 8192u + wid * 2048u, 0u);
    tabp(25, p.out, 0u); tab(26, 256u * y + 8u * hd + 2u * wid, (y & 1u) * 256u); tab(27, (unsigned)WS_KEYS + b * 8u + wid * 4096u, (unsigned)WS_KEYS + (b & 7u) * 2048u);
    tab(28, (unsigned)WS_PART + ((b & 7u) * 32u + idx) * 32u + wid * 262144u, ((b & 7u) * 32u + idx) * 8u);
    tab(31, (128u * m + 32u * (y >> 1) + 8u * (y & 1u)) * 2048u + 512u * wid, (unsigned)WS_U + (y >> 1) * 4096u + hd * 64u + (y & 1u) * 32u); tab(32, hd * 64u, 0u);
    tab(33, (unsigned)WS_PX + (y * 32u + hd) * 1024u + wid * 256u, (unsigned)WS_PX + y * 32768u + (8u * hd + 2u * wid) * 4u); tab(34, (256u * y + 64u * wid) * 2048u + 64u * hd, (unsigned)WS_PK + b * 8u + wid * 4096u);
    tab(35, (unsigned)WS_PK + (b & 7u) * 2048u, 256u * y + 8u * hd + 2u * wid);
      asm volatile("" : "+v"(tlo), "+v"(thi), "+v"(wo0), "+v"(vg0), "+v"(vdn), "+v"(ta0), "+v"(vxs), "+v"(vx0), "+v"(rd0), "+v"(fl), "+v"(sd0), "+v"(kb0), "+v"(vvb), "+v"(vwb), "+v"(kd0), "+v"(vd0), "+v"(wd0), "+v"(hq0), "+v"(oq0));
#undef lane
      float* par = (float*)(lds + 131072);
      u32x2 mp[8]; u32x4 rs;
      auto look = [&]() __attribute__((always_inline)) {
        #pragma unroll
        for (int k = 0; k < 8; ++k) asm volatile("global_load_dwordx2 %0, %1, %2 offset:%3 sc1" : "=v"(mp[k]) : "v"(((wid >> 1) * 16u + 2u * (hd & 7u) + (wid & 1u)) * 4096u + lane * 8u), "s"(p.ws + WS_MOD), "n"(512 * k) : "memory");
        asm volatile("global_load_dwordx4 %0, %1, %2 sc1" : "=v"(rs) : "v"(((rb0 + wid) * 16u + l16) * 16u), "s"(p.ws + WS_RST) : "memory");
      };
#define LOOKED "+v"(mp[0]), "+v"(mp[1]), "+v"(mp[2]), "+v"(mp[3]), "+v"(mp[4]), "+v"(mp[5]), "+v"(mp[6]), "+v"(mp[7]), "+v"(rs)
      look();
      #pragma unroll
      for (int q = 0; q < 14; ++q)
        asm volatile("s_mov_b32 m0, %0\n\ts_nop 0\n\tbuffer_load_dwordx4 %1, %2, %3 offen lds" :: "s"(m0b + 65536u + 16384u * (q >> 2) + 1024u * (q & 3)), "v"(wd0), "s"(srdw),
                     "s"(0x80000u * ((2u * rho + (q >> 2)) & 15u) + 65536u * ((q >> 1) & 1) + 128u * (q & 1)) : "m0", "memory");
#define KVT 0
#define KV0 0
      #pragma unroll
      for (int j = 0; j < KVT; ++j) asm volatile("buffer_load_dword a255, %0, %1, %2 offen" :: "v"(lane < 32u ? lane * 128u : 0x80000000u), "s"(srdp), "s"((KV0 + wid + 4u * j) * 16384u) : "memory");
      asm volatile("s_waitcnt vmcnt(%0)" :: "n"(27 + KVT) : "memory");
      { const u32x4 cs = XV(4); *(u32x2*)(lds + 131072 + 8192 + tid * 8u) = u32x2{cvtpk(silu(lo16(cs[0])), silu(hi16(cs[0]))), cvtpk(silu(lo16(cs[1])), silu(hi16(cs[1])))}; }
      asm volatile("s_waitcnt vmcnt(%9)" : LOOKED : "n"(14 + KVT) : "memory");
      for (unsigned spins = 0;; ++spins) {
        unsigned bl = (rs[0] ^ tl) | (rs[2] ^ tl), bh = (rs[1] ^ th) | (rs[3] ^ th);
        #pragma unroll
        for (int k = 0; k < 8; ++k) { bl |= mp[k][0] ^ tl; bh |= mp[k][1] ^ th; }
        if (__all(((bl & 0xffu) | bh) == 0u) || spins > SPIN_CAP) break;
        __builtin_amdgcn_s_sleep(ASLEEP);
        look();
        asm volatile("s_waitcnt vmcnt(0)" : LOOKED :: "memory");
      }
      {
        float v = 0.f;
        #pragma unroll
        for (int k = 0; k < 8; ++k) v += __uint_as_float(mp[k][0] & 0xffffff00u);
        v = rbf(v); par[tid] = (tid < 128u ? v : rbf(1.f + v)) * QSCALE;
        if (lane < 16u) *(u32x2*)(par + 256u + 8u * lane + 2u * wid) = u32x2{rs[0] & 0xffffff00u, rs[2] & 0xffffff00u};
      }
      PAGE(6, "adaln_fr");
      asm volatile("s_waitcnt lgkmcnt(0)\n\ts_barrier" ::: "memory");
      asm volatile("buffer_load_dword a255, %0, %1, %2 offen" TNT :: "v"(tq0), "s"(srdw), "s"(0x200000u) : "memory");
      FQ(0);
      {
        const float* pc = par + 8u * (tid >> 4);
        const f32x4 a0 = *(const f32x4*)pc, a1 = *(const f32x4*)(pc + 4), c0 = *(const f32x4*)(pc + 128), c1 = *(const f32x4*)(pc + 132);
        const f32x4 t0 = *(const f32x4*)(par + 256u + 8u * l16), t1 = *(const f32x4*)(par + 260u + 8u * l16);
        #pragma unroll
        for (int i = 0; i < 4; ++i) {
          const float mn = i < 2 ? t0[2 * i] : t1[2 * i - 4], rd = i < 2 ? t0[2 * i + 1] : t1[2 * i - 3];
          u32x4 o = XV(i);
          #pragma unroll
          for (int e = 0; e < 4; ++e) {
            const f32x2 sc = e < 2 ? f32x2{c0[2 * e], c0[2 * e + 1]} : f32x2{c1[2 * e - 4], c1[2 * e - 3]}, sh = e < 2 ? f32x2{a0[2 * e], a0[2 * e + 1]} : f32x2{a1[2 * e - 4], a1[2 * e - 3]};
            o[e] = cvtpk2(__builtin_elementwise_fma(__builtin_elementwise_fma(unpk(o[e]), f32x2{rd, rd}, f32x2{mn, mn}), sc, sh));
          }
          st16_wt(p.ws + WS_H + (rb0 + i) * 32768u + ch * 256u + 16u * l16, o);
        }
      }
      FQ(1);
    asm volatile(FA_BODY
      : FA_OUTS
      : [m0b] "s"(m0b), [flag] "s"(flag), [srdz] "s"(srdz), [srdw] "s"(srdw), [srdk] "s"(srdk), [srdv] "s"(srdv), [m0t] "s"(m0t), [srdt] "s"(srdt), [srdp] "s"(srdp),
        [tag] "s"(tag), [pcnt] "s"(pcnt), [psig] "s"(psig), [wid] "s"(wid), [px] "s"(p.x), [pct] "s"(pct), [sin] "s"(sin), [tlo] "v"(tlo), [thi] "v"(thi), [wbytes] "s"(WBYTES)
      : FA_CLOBBERS);
  }
  if (b == 255u && wid == 0u) __hip_atomic_store((gu64*)(p.ws + WS_EP), tag, RLX_AGENT);
  asm volatile("adaln_text_end:");
}
}

extern "C" void kernel_launch(void* const* d_in, const int* in_sizes, int n_in, void* d_out, int out_size, void* d_ws, size_t ws_size, hipStream_t stream) {
  (void)in_sizes; (void)n_in; (void)out_size;
  if (ws_size < WS_END) return;
  const u16* x    = (const u16*)d_in[0];
  const u16* kc   = (const u16*)d_in[1];
  const u16* vc   = (const u16*)d_in[2];
  const u16* wq   = (const u16*)d_in[3];
  const u16* wo   = (const u16*)d_in[4];
  const u16* wr   = (const u16*)d_in[6];
  const u16* wg   = (const u16*)d_in[7];
  const u16* wu   = (const u16*)d_in[8];
  const u16* wd   = (const u16*)d_in[9];
  const u16* c    = (const u16*)d_in[10];
  const u16* wmod = (const u16*)d_in[11];
  u16* out = (u16*)d_out;
  char* ws = (char*)d_ws;
  fa::Args fr;
  fr.x = x; fr.wmod = wmod; fr.wq = wq; fr.kc = kc; fr.vc = vc; fr.wo = wo; fr.wg = wg; fr.wu = wu; fr.wd = wd; fr.ws = ws; fr.out = out; fr.c = c; fr.wr = wr;
  hipFuncSetAttribute((const void*)fa::fattn_k, hipFuncAttributeMaxDynamicSharedMemorySize, (int)fa::LDS_BYTES);
  hipLaunchKernelGGL(fa::fattn_k, dim3(256), dim3(256), fa::LDS_BYTES, stream, fr);
}
```

```cpp
#include <hip/hip_runtime.h>
#include <hip/hip_bf16.h>
#include <utility>
#include <cstdint>

#pragma clang fp contract(off)
typedef float    f32x16 __attribute__((ext_vector_type(16)));
typedef float    f32x2  __attribute__((ext_vector_type(2)));
typedef float    f32x4  __attribute__((ext_vector_type(4)));
typedef __bf16   bf16x8 __attribute__((ext_vector_type(8)));
typedef __bf16   bf16x4 __attribute__((ext_vector_type(4)));
typedef __bf16   bf16x2 __attribute__((ext_vector_type(2)));
typedef unsigned u32x4  __attribute__((ext_vector_type(4)));
typedef unsigned u32x2  __attribute__((ext_vector_type(2)));
typedef short    s16x4  __attribute__((ext_vector_type(4)));
typedef unsigned short u16;
template <int... Is> using iseq = std::integer_sequence<int, Is...>;
template <int N> using make_iseq = std::make_integer_sequence<int, N>;
#define DI __device__ __forceinline__
#define TAILB
#define PACE2
#define SELB

constexpr int SEQ = 2048, DM = 1024, NHEAD = 32, DHEAD = 128, DQ = NHEAD * DHEAD, DFF = 3584, NMOD = 6 * DM;
constexpr size_t WS_EP    = 0;
constexpr size_t WS_RST   = 32768;
constexpr size_t WS_CNT   = 65536;
constexpr size_t WS_MOD   = 131072;
constexpr size_t WS_Z     = 262144;
constexpr size_t WS_MODT  = WS_Z;
constexpr size_t WS_XFL   = WS_Z + 16384;
#define KEY_REP 8u
constexpr size_t WS_KEYS  = WS_Z + 32768;
constexpr unsigned Z_SHARE = 2560;
static_assert(KEY_REP <= 8 && Z_SHARE * 256 == 655360, "zeroed area");
constexpr size_t WS_MD    = WS_Z + 49152;
constexpr size_t WS_PFL   = WS_Z + 65536;
constexpr size_t WS_OFL   = WS_Z + 196608;
constexpr size_t WS_PK    = WS_Z + 327680;
constexpr size_t WS_U     = WS_Z + 344064;
constexpr size_t WS_PX    = WS_Z + 393216;
constexpr size_t WS_H     = 1048576;
constexpr size_t WS_PART  = WS_H  + (size_t)SEQ * DM * 2;
constexpr size_t WS_XIN   = WS_PART + (size_t)256 * 2 * DM * 4;
#define BOXV 38016
constexpr unsigned BOX    = BOXV;
constexpr size_t WS_END   = WS_XIN + (size_t)BOX * (SEQ / 2);

DI float bf2f(u16 b) { return __uint_as_float((unsigned)b << 16); }
DI float rbf(float x) { return (float)(__bf16)x; }
DI u16   f2bf(float x) { return __builtin_bit_cast(u16, (__bf16)x); }
DI float lo16(unsigned w) { return __uint_as_float(w << 16); }
DI float hi16(unsigned w) { return __uint_as_float(w & 0xffff0000u); }
DI unsigned cvtpk(float lo, float hi) {
  f32x2 v = {lo, hi}; bf16x2 b = __builtin_convertvector(v, bf16x2); return __builtin_bit_cast(unsigned, b);
}
template <int CTRL> DI float dpp_add(float v) { return v + __uint_as_float(__builtin_amdgcn_update_dpp(0u, __float_as_uint(v), CTRL, 0xf, 0xf, true)); }
DI float swap32_add(float a, float b) {
  auto r = __builtin_amdgcn_permlane32_swap(__float_as_uint(a), __float_as_uint(b), false, false); return __uint_as_float(r[0]) + __uint_as_float(r[1]);
}
DI float swap16_add(float a, float b) {
  auto r = __builtin_amdgcn_permlane16_swap(__float_as_uint(a), __float_as_uint(b), false, false); return __uint_as_float(r[0]) + __uint_as_float(r[1]);
}
constexpr int DPP_XOR1 = 0xB1, DPP_XOR2 = 0x4E, DPP_ROR4 = 0x124, DPP_ROR8 = 0x128;
DI float wave_sum(float v) {
  v = dpp_add<DPP_XOR1>(v); v = dpp_add<DPP_XOR2>(v); v = dpp_add<DPP_ROR4>(v); v = dpp_add<DPP_ROR8>(v);
  v = swap16_add(v, v); return swap32_add(v, v);
}

DI void st16_wt(void* p, u32x4 v) { asm volatile("global_store_dwordx4 %0, %1, off sc1\n\ts_nop 1" :: "v"(p), "v"(v) : "memory"); }

DI f32x2 unpk(unsigned w) { return f32x2{lo16(w), hi16(w)}; }
DI unsigned cvtpk2(f32x2 v) { return __builtin_bit_cast(unsigned, __builtin_convertvector(v, bf16x2)); }
DI f32x2 rb2(f32x2 v) { return f32x2{__uint_as_float(cvtpk(0.f, v[0])), __uint_as_float(cvtpk(0.f, v[1]))}; }
DI unsigned ln_pair(float x0, float x1, float m, float rden, float c0, float c1, float s0, float s1) {
  const unsigned d = cvtpk2(f32x2{x0, x1} - m), l = cvtpk2(unpk(d) * rden), t = cvtpk2(unpk(l) * f32x2{c0, c1});
  return cvtpk2(unpk(t) + f32x2{s0, s1});
}
#define GAS __attribute__((address_space(1)))
#define LAS __attribute__((address_space(3)))
typedef GAS unsigned long long gu64;
typedef short v4i16_t __attribute__((ext_vector_type(4)));
DI bf16x4 lds_tr(unsigned a) { return __builtin_bit_cast(bf16x4, __builtin_amdgcn_ds_read_tr16_b64_v4i16((LAS v4i16_t*)a)); }
#define RLX_AGENT __ATOMIC_RELAXED, __HIP_MEMORY_SCOPE_AGENT
constexpr unsigned SPIN_CAP = 1u << 16;

static_assert(BOX == 38016, "box size");
#define NP_FILL 8
#define GOT_WAIT "8"
#define NORM 0
#define UNITP 1
#define INS 0
#define XP2 0
#define FA_CLOBBERS "v0", "v1", "v2", "v3", "v4", "v5", "v6", "v7", "v8", "v9", "v10", "v11", "v12", "v13", "v14", "v15", "v16", "v17", "v18", "v19", "v20", "v21", "v22", "v23", "v24", "v25", "v26", "v27", "v28", "v29", "v30", "v31", "v32", "v33", "v34", "v35", "v36", "v37", "v38", "v39", "v40", "v41", "v42", "v43", "v44", "v45", "v46", "v47", "v48", "v49", "v50", "v51", "v52", "v53", "v54", "v55", "v56", "v57", "v58", "v59", "v60", "v61", "v62", "v63", "v64", "v65", "v66", "v67", "v68", "v69", "v70", "v71", "v72", "v73", "v74", "v75", "v76", "v77", "v78", "v79", "v80", "v81", "v82", "v83", "v84", "v85", "v86", "v87", "v88", "v89", "v90", "v91", "v92", "v93", "v94", "v95", "v96", "v97", "v98", "v99", "v100", "v101", "v102", "v103", "v104", "v105", "v106", "v107", "v108", "v109", "v110", "v111", "v112", "v113", "v114", "v115", "v116", "v117", "v118", "v119", "v120", "v121", "v122", "v123", "v124", "v125", "v126", "v127", "v128", "v129", "v130", "v131", "v132", "v133", "v134", "v135", "v136", "v137", "v138", "v139", "v140", "v141", "v142", "v143", "v144", "v145", "v146", "v147", "v148", "v149", "v150", "v151", "v152", "v153", "v154", "v155", "v156", "v157", "v158", "v159", "v160", "v161", "v162", "v163", "v164", "v165", "v166", "v167", "v168", "v169", "v170", "v171", "v172", "v173", "v174", "v175", "v176", "v177", "v178", "v179", "v180", "v181", "v182", "v183", "v184", "v185", "v186", "v187", "v188", "v189", "v190", "v191", "v192", "v193", "v194", "v195", "v196", "v197", "v198", "v199", "v200", "v201", "v202", "v203", "v204", "v205", "v206", "v207", "v208", "v209", "v210", "v211", "v212", "v213", "v214", "v215", "v216", "v217", "v218", "v219", "v220", "v221", "v222", "v223", "v224", "v230", "v231", "v232", "v233", "v234", "v235", "v237", "a0", "a1", "a2", "a3", "a4", "a5", "a6", "a7", "a8", "a9", "a10", "a11", "a12", "a13", "a14", "a15", "a16", "a17", "a18", "a19", "a20", "a21", "a22", "a23", "a24", "a25", "a26", "a27", "a28", "a29", "a30", "a31", "a32", "a33", "a34", "a35", "a36", "a37", "a38", "a39", "a40", "a41", "a42", "a43", "a44", "a45", "a46", "a47", "a48", "a49", "a50", "a51", "a52", "a53", "a54", "a55", "a56", "a57", "a58", "a59", "a60", "a61", "a62", "a63", "a64", "a65", "a66", "a67", "a68", "a69", "a70", "a71", "a72", "a73", "a74", "a75", "a76", "a77", "a78", "a79", "a80", "a81", "a82", "a83", "a84", "a85", "a86", "a87", "a88", "a89", "a90", "a91", "a92", "a93", "a94", "a95", "a96", "a97", "a98", "a99", "a100", "a101", "a102", "a103", "a104", "a105", "a106", "a107", "a108", "a109", "a110", "a111", "a112", "a113", "a114", "a115", "a116", "a117", "a118", "a119", "a120", "a121", "a122", "a123", "a124", "a125", "a126", "a127", "a128", "a129", "a130", "a131", "a132", "a133", "a134", "a135", "a136", "a137", "a138", "a139", "a140", "a141", "a142", "a143", "a144", "a145", "a146", "a147", "a148", "a149", "a150", "a151", "a152", "a153", "a154", "a155", "a156", "a157", "a158", "a159", "a160", "a161", "a162", "a163", "a164", "a165", "a166", "a167", "a168", "a169", "a170", "a171", "a172", "a173", "a174", "a175", "a176", "a177", "a178", "a179", "a180", "a181", "a182", "a183", "a184", "a185", "a186", "a187", "a188", "a189", "a190", "a191", "a192", "a193", "a194", "a195", "a196", "a197", "a198", "a199", "a200", "a201", "a202", "a203", "a204", "a205", "a206", "a207", "a208", "a209", "a210", "a211", "a212", "a213", "a214", "a215", "a216", "a217", "a218", "a219", "a220", "a221", "a222", "a223", "a224", "a225", "a226", "a227", "a228", "a229", "a230", "a231", "a232", "a233", "a234", "a235", "a236", "a237", "a238", "a239", "a240", "a241", "a242", "a243", "a244", "a245", "a246", "a247", "a248", "a249", "a250", "a251", "a252", "a253", "a254", "a255", "s40", "s41", "s42", "s43", "s44", "s45", "s46", "s47", "s48", "s49", "s50", "s51", "s52", "s53", "s54", "s55", "s56", "s57", "s58", "s59", "s60", "s61", "s62", "s63", "s64", "s65", "s66", "s67", "s68", "s69", "vcc", "m0", "scc", "memory"
#define EARLY2 0
#define SHW 1
#define QROT 0
#define SKV 0
#define KVPAR 0
#define QT2 0
#define QSTAG 0
#define ATAIL 1
static_assert(WS_MODT == 262144 && WS_XFL == 278528 && WS_U == 606208 && WS_XIN == 7340032, "workspace layout");
#define FA_OUTS [got] "=&s"(got), [k1] "=&s"(k1), [k2] "=&s"(k2), [lat] "=&s"(lat), "+{v229}"(fl), "+{v225}"(vwb), "+{v226}"(wd0), "+{v227}"(hq0), "+{v228}"(sd0), "+{v236}"(tq0), [kb0] "+v"(kb0), [vvb] "+v"(vvb), [kd0] "+v"(kd0), [vd0] "+v"(vd0), [oq0] "+v"(oq0), [rd0] "+v"(rd0), [wo0] "+v"(wo0), [vg0] "+v"(vg0), [vx0] "+v"(vx0), [vdn] "+v"(vdn), [ta0] "+v"(ta0), [vxs] "+v"(vxs), [vct] "+v"(vct)
#define FA_BODY \
"s_branch adaln_body\n" \
".p2align 12\n" \
"adaln_pg7:\n" \
"adaln_body:\n" \
"s_cbranch_execz adaln_fr\n" \
"s_nop 0\n" \
"s_mov_b32 s55, 0x80000\n" \
"v_add_u32_e32 v219, 0x8000, v227\n" \
"v_add_u32_e32 v220, 0x10000, v227\n" \
"v_add_u32_e32 v221, 0x18000, v227\n" \
"s_mov_b32 s50, 0\n" \
"s_movk_i32 s53, 0x1000\n" \
"s_mov_b32 s52, 0\n" \
"s_nop 0\n" \
"s_mov_b32 s49, 1572864\n" \
"v_add_u32_e32 v216, 0xfffffc80, v226\n" \
"v_add_u32_e32 v217, 0xf800, v226\n" \
"v_add_u32_e32 v218, 0xf480, v226\n" \
"s_add_u32 s40, %[m0b], 0\n" \
"s_nop 0\n" \
"s_add_u32 s44, %[m0b], 65536\n" \
"s_add_u32 s41, %[m0b], 16384\n" \
"s_add_u32 s45, %[m0b], 81920\n" \
"s_add_u32 s42, %[m0b], 32768\n" \
"s_add_u32 s46, %[m0b], 98304\n" \
"s_add_u32 s43, %[m0b], 49152\n" \
"s_add_u32 s47, %[m0b], 114688\n" \
"v_xor_b32_e64 v208, 64, %[kb0]\n" \
"v_xor_b32_e32 v209, 0x80, %[kb0]\n" \
"v_xor_b32_e32 v210, 0xc0, %[kb0]\n" \
"v_xor_b32_e64 v212, 64, %[kd0]\n" \
"v_xor_b32_e32 v213, 0x80, %[kd0]\n" \
"v_xor_b32_e32 v214, 0xc0, %[kd0]\n" \
"v_xor_b32_e64 v211, 32, %[vvb]\n" \
"v_add_u32_e32 v215, 0xfffffc80, %[vd0]\n" \
"v_readlane_b32 s64, %[tlo], 17\n" \
"v_readlane_b32 s65, %[thi], 17\n" \
"s_mov_b32 s66, 12582912\n" \
"s_mov_b32 s67, 0x20000\n" \
"v_mov_b64_e32 v[0:1], 0\n" \
"v_mov_b64_e32 v[2:3], 0\n" \
"v_mov_b64_e32 v[4:5], 0\n" \
"v_mov_b64_e32 v[6:7], 0\n" \
"v_mov_b64_e32 v[8:9], 0\n" \
"v_mov_b64_e32 v[10:11], 0\n" \
"v_mov_b64_e32 v[12:13], 0\n" \
"v_mov_b64_e32 v[14:15], 0\n" \
"v_mov_b64_e32 v[16:17], 0\n" \
"v_mov_b64_e32 v[18:19], 0\n" \
"v_mov_b64_e32 v[20:21], 0\n" \
"v_mov_b64_e32 v[22:23], 0\n" \
"v_mov_b64_e32 v[24:25], 0\n" \
"v_mov_b64_e32 v[26:27], 0\n" \
"v_mov_b64_e32 v[28:29], 0\n" \
"v_mov_b64_e32 v[30:31], 0\n" \
"v_mov_b64_e32 v[32:33], 0\n" \
"v_mov_b64_e32 v[34:35], 0\n" \
"v_mov_b64_e32 v[36:37], 0\n" \
"v_mov_b64_e32 v[38:39], 0\n" \
"v_mov_b64_e32 v[40:41], 0\n" \
"v_mov_b64_e32 v[42:43], 0\n" \
"v_mov_b64_e32 v[44:45], 0\n" \
"v_mov_b64_e32 v[46:47], 0\n" \
"v_mov_b64_e32 v[48:49], 0\n" \
"v_mov_b64_e32 v[50:51], 0\n" \
"v_mov_b64_e32 v[52:53], 0\n" \
"v_mov_b64_e32 v[54:55], 0\n" \
"v_mov_b64_e32 v[56:57], 0\n" \
"v_mov_b64_e32 v[58:59], 0\n" \
"v_mov_b64_e32 v[60:61], 0\n" \
"v_mov_b64_e32 v[62:63], 0\n" \
"v_mov_b64_e32 v[64:65], 0\n" \
"v_mov_b64_e32 v[66:67], 0\n" \
"v_mov_b64_e32 v[68:69], 0\n" \
"v_mov_b64_e32 v[70:71], 0\n" \
"v_mov_b64_e32 v[72:73], 0\n" \
"v_mov_b64_e32 v[74:75], 0\n" \
"v_mov_b64_e32 v[76:77], 0\n" \
"v_mov_b64_e32 v[78:79], 0\n" \
"v_mov_b64_e32 v[80:81], 0\n" \
"v_mov_b64_e32 v[82:83], 0\n" \
"v_mov_b64_e32 v[84:85], 0\n" \
"v_mov_b64_e32 v[86:87], 0\n" \
"v_mov_b64_e32 v[88:89], 0\n" \
"v_mov_b64_e32 v[90:91], 0\n" \
"v_mov_b64_e32 v[92:93], 0\n" \
"v_mov_b64_e32 v[94:95], 0\n" \
"v_mov_b64_e32 v[96:97], 0\n" \
"v_mov_b64_e32 v[98:99], 0\n" \
"v_mov_b64_e32 v[100:101], 0\n" \
"v_mov_b64_e32 v[102:103], 0\n" \
"v_mov_b64_e32 v[104:105], 0\n" \
"v_mov_b64_e32 v[106:107], 0\n" \
"v_mov_b64_e32 v[108:109], 0\n" \
"v_mov_b64_e32 v[110:111], 0\n" \
"v_mov_b64_e32 v[112:113], 0\n" \
"v_mov_b64_e32 v[114:115], 0\n" \
"v_mov_b64_e32 v[116:117], 0\n" \
"v_mov_b64_e32 v[118:119], 0\n" \
"v_mov_b64_e32 v[120:121], 0\n" \
"v_mov_b64_e32 v[122:123], 0\n" \
"v_mov_b64_e32 v[124:125], 0\n" \
"v_mov_b64_e32 v[126:127], 0\n" \
"s_mov_b32 s51, 0x10000\n" \
"v_mbcnt_lo_u32_b32 v232, -1, 0\n" \
"v_mov_b64_e32 v[234:235], %[tag]\n" \
"v_lshlrev_b32_e32 v233, 3, v232\n" \
"v_lshlrev_b32_e32 v232, 8, v232\n" \
"s_waitcnt vmcnt(0)\n" \
"s_barrier\n" \
"s_cmp_lg_u32 %[wid], 0\n" \
"s_cbranch_scc1 Lwalk_%=\n" \
"s_mov_b32 exec_hi, 0\n" \
"global_store_dwordx2 v232, v[234:235], %[psig] sc1\n" \
"s_mov_b32 exec_hi, -1\n" \
"Lgo_%=:\n" \
"s_nop 0\n" \
"ds_read_b64_tr_b16 v[128:129], v225 offset:0\n" \
"ds_read_b64_tr_b16 v[130:131], v225 offset:256\n" \
"ds_read_b64_tr_b16 v[132:133], v225 offset:8192\n" \
"ds_read_b64_tr_b16 v[134:135], v225 offset:8448\n" \
"ds_read_b64_tr_b16 v[136:137], v225 offset:8\n" \
"ds_read_b64_tr_b16 v[138:139], v225 offset:264\n" \
"ds_read_b64_tr_b16 v[140:141], v225 offset:8200\n" \
"ds_read_b64_tr_b16 v[142:143], v225 offset:8456\n" \
"ds_read_b64_tr_b16 v[144:145], v225 offset:512\n" \
"ds_read_b64_tr_b16 v[146:147], v225 offset:768\n" \
"ds_read_b64_tr_b16 v[148:149], v225 offset:8704\n" \
"ds_read_b64_tr_b16 v[150:151], v225 offset:8960\n" \
"ds_read_b64_tr_b16 v[152:153], v225 offset:520\n" \
"ds_read_b64_tr_b16 v[154:155], v225 offset:776\n" \
"ds_read_b64_tr_b16 v[156:157], v225 offset:8712\n" \
"ds_read_b64_tr_b16 v[158:159], v225 offset:8968\n" \
"s_mov_b32 s60, 4194304\n" \
"v_and_b32_e32 v233, 56, v233\n" \
"s_lshl_b32 s61, %[wid], 6\n" \
"v_add_u32_e64 v233, s61, v233\n" \
"Lpoll_%=:\n" \
"global_load_dwordx2 v[230:231], v233, %[pcnt] sc1\n" \
"s_waitcnt vmcnt(0)\n" \
"v_cmp_ne_u64_e32 vcc, v[230:231], v[234:235]\n" \
"s_sub_u32 s51, s51, 1\n" \
"s_cbranch_scc1 Lpd_%=\n" \
"s_nop 3\n" \
"s_cbranch_vccnz Lpoll_%=\n" \
"Lpd_%=:\n" \
"buffer_load_dwordx4 a[128:131], v227, %[srdz], s50 offen offset:0 sc1\n" \
"buffer_load_dwordx4 a[132:135], v219, %[srdz], s50 offen offset:0 sc1\n" \
"buffer_load_dwordx4 a[136:139], v220, %[srdz], s50 offen offset:0 sc1\n" \
"buffer_load_dwordx4 a[140:143], v221, %[srdz], s50 offen offset:0 sc1\n" \
"buffer_load_dwordx4 a[144:147], v227, %[srdz], s50 offen offset:1024 sc1\n" \
"buffer_load_dwordx4 a[148:151], v219, %[srdz], s50 offen offset:1024 sc1\n" \
"buffer_load_dwordx4 a[152:155], v220, %[srdz], s50 offen offset:1024 sc1\n" \
"buffer_load_dwordx4 a[156:159], v221, %[srdz], s50 offen offset:1024 sc1\n" \
"buffer_load_dwordx4 a[160:163], v227, %[srdz], s50 offen offset:2048 sc1\n" \
"buffer_load_dwordx4 a[164:167], v219, %[srdz], s50 offen offset:2048 sc1\n" \
"buffer_load_dwordx4 a[168:171], v220, %[srdz], s50 offen offset:2048 sc1\n" \
"buffer_load_dwordx4 a[172:175], v221, %[srdz], s50 offen offset:2048 sc1\n" \
"buffer_load_dwordx4 a[176:179], v227, %[srdz], s50 offen offset:3072 sc1\n" \
"buffer_load_dwordx4 a[180:183], v219, %[srdz], s50 offen offset:3072 sc1\n" \
"buffer_load_dwordx4 a[184:187], v220, %[srdz], s50 offen offset:3072 sc1\n" \
"buffer_load_dwordx4 a[188:191], v221, %[srdz], s50 offen offset:3072 sc1\n" \
"s_add_u32 s50, s50, s53\n" \
"s_nop 0\n" \
"buffer_load_dwordx4 a[192:195], v227, %[srdz], s50 offen offset:0 sc1\n" \
"buffer_load_dwordx4 a[196:199], v219, %[srdz], s50 offen offset:0 sc1\n" \
"buffer_load_dwordx4 a[200:203], v220, %[srdz], s50 offen offset:0 sc1\n" \
"buffer_load_dwordx4 a[204:207], v221, %[srdz], s50 offen offset:0 sc1\n" \
"buffer_load_dwordx4 a[208:211], v227, %[srdz], s50 offen offset:1024 sc1\n" \
"buffer_load_dwordx4 a[212:215], v219, %[srdz], s50 offen offset:1024 sc1\n" \
"buffer_load_dwordx4 a[216:219], v220, %[srdz], s50 offen offset:1024 sc1\n" \
"buffer_load_dwordx4 a[220:223], v221, %[srdz], s50 offen offset:1024 sc1\n" \
"s_mov_b32 s51, 3\n" \
"s_waitcnt vmcnt(16) lgkmcnt(0)\n" \
"Lq_%=:\n" \
"v_mfma_f32_16x16x32_bf16 v[0:3], v[128:131], a[128:131], v[0:3]\n" \
"ds_read_b64_tr_b16 v[160:161], v225 offset:1024\n" \
"v_mfma_f32_16x16x32_bf16 v[0:3], v[132:135], a[144:147], v[0:3]\n" \
"ds_read_b64_tr_b16 v[162:163], v225 offset:1280\n" \
"v_mfma_f32_16x16x32_bf16 v[32:35], v[128:131], a[132:135], v[32:35]\n" \
"s_mov_b32 m0, s47\n" \
"s_nop 0\n" \
"buffer_load_dwordx4 v217, %[srdw], s49 offen offset:2048 lds\n" \
"v_mfma_f32_16x16x32_bf16 v[32:35], v[132:135], a[148:151], v[32:35]\n" \
"ds_read_b64_tr_b16 v[164:165], v225 offset:9216\n" \
"v_mfma_f32_16x16x32_bf16 v[64:67], v[128:131], a[136:139], v[64:67]\n" \
"ds_read_b64_tr_b16 v[166:167], v225 offset:9472\n" \
"v_mfma_f32_16x16x32_bf16 v[64:67], v[132:135], a[152:155], v[64:67]\n" \
"v_mfma_f32_16x16x32_bf16 v[96:99], v[128:131], a[140:143], v[96:99]\n" \
"ds_read_b64_tr_b16 v[168:169], v225 offset:1032\n" \
"v_mfma_f32_16x16x32_bf16 v[96:99], v[132:135], a[156:159], v[96:99]\n" \
"ds_read_b64_tr_b16 v[170:171], v225 offset:1288\n" \
"v_mfma_f32_16x16x32_bf16 v[4:7], v[136:139], a[128:131], v[4:7]\n" \
"buffer_load_dwordx4 v218, %[srdw], s49 offen offset:3072 lds\n" \
"v_mfma_f32_16x16x32_bf16 v[4:7], v[140:143], a[144:147], v[4:7]\n" \
"ds_read_b64_tr_b16 v[172:173], v225 offset:9224\n" \
"v_mfma_f32_16x16x32_bf16 v[36:39], v[136:139], a[132:135], v[36:39]\n" \
"ds_read_b64_tr_b16 v[174:175], v225 offset:9480\n" \
"v_mfma_f32_16x16x32_bf16 v[36:39], v[140:143], a[148:151], v[36:39]\n" \
"v_mfma_f32_16x16x32_bf16 v[68:71], v[136:139], a[136:139], v[68:71]\n" \
"ds_read_b64_tr_b16 v[176:177], v225 offset:1536\n" \
"v_mfma_f32_16x16x32_bf16 v[68:71], v[140:143], a[152:155], v[68:71]\n" \
"ds_read_b64_tr_b16 v[178:179], v225 offset:1792\n" \
"v_mfma_f32_16x16x32_bf16 v[100:103], v[136:139], a[140:143], v[100:103]\n" \
"buffer_load_dwordx4 a[224:227], v227, %[srdz], s50 offen offset:2048 sc1\n" \
"v_mfma_f32_16x16x32_bf16 v[100:103], v[140:143], a[156:159], v[100:103]\n" \
"ds_read_b64_tr_b16 v[180:181], v225 offset:9728\n" \
"v_mfma_f32_16x16x32_bf16 v[8:11], v[144:147], a[128:131], v[8:11]\n" \
"ds_read_b64_tr_b16 v[182:183], v225 offset:9984\n" \
"v_mfma_f32_16x16x32_bf16 v[8:11], v[148:151], a[144:147], v[8:11]\n" \
"v_mfma_f32_16x16x32_bf16 v[40:43], v[144:147], a[132:135], v[40:43]\n" \
"ds_read_b64_tr_b16 v[184:185], v225 offset:1544\n" \
"v_mfma_f32_16x16x32_bf16 v[40:43], v[148:151], a[148:151], v[40:43]\n" \
"ds_read_b64_tr_b16 v[186:187], v225 offset:1800\n" \
"v_mfma_f32_16x16x32_bf16 v[72:75], v[144:147], a[136:139], v[72:75]\n" \
"buffer_load_dwordx4 a[228:231], v219, %[srdz], s50 offen offset:2048 sc1\n" \
"v_mfma_f32_16x16x32_bf16 v[72:75], v[148:151], a[152:155], v[72:75]\n" \
"ds_read_b64_tr_b16 v[188:189], v225 offset:9736\n" \
"v_mfma_f32_16x16x32_bf16 v[104:107], v[144:147], a[140:143], v[104:107]\n" \
"ds_read_b64_tr_b16 v[190:191], v225 offset:9992\n" \
"v_mfma_f32_16x16x32_bf16 v[104:107], v[148:151], a[156:159], v[104:107]\n" \
"v_mfma_f32_16x16x32_bf16 v[12:15], v[152:155], a[128:131], v[12:15]\n" \
"buffer_load_dwordx4 a[232:235], v220, %[srdz], s50 offen offset:2048 sc1\n" \
"v_mfma_f32_16x16x32_bf16 v[12:15], v[156:159], a[144:147], v[12:15]\n" \
"v_mfma_f32_16x16x32_bf16 v[44:47], v[152:155], a[132:135], v[44:47]\n" \
"v_mfma_f32_16x16x32_bf16 v[44:47], v[156:159], a[148:151], v[44:47]\n" \
"v_mfma_f32_16x16x32_bf16 v[76:79], v[152:155], a[136:139], v[76:79]\n" \
"buffer_load_dwordx4 a[236:239], v221, %[srdz], s50 offen offset:2048 sc1\n" \
"v_mfma_f32_16x16x32_bf16 v[76:79], v[156:159], a[152:155], v[76:79]\n" \
"v_mfma_f32_16x16x32_bf16 v[108:111], v[152:155], a[140:143], v[108:111]\n" \
"v_mfma_f32_16x16x32_bf16 v[108:111], v[156:159], a[156:159], v[108:111]\n" \
"s_waitcnt vmcnt(29) lgkmcnt(0)\n" \
"s_barrier\n" \
"v_mfma_f32_16x16x32_bf16 v[16:19], v[160:163], a[128:131], v[16:19]\n" \
"ds_read_b64_tr_b16 v[128:129], v225 offset:16384\n" \
"v_mfma_f32_16x16x32_bf16 v[16:19], v[164:167], a[144:147], v[16:19]\n" \
"ds_read_b64_tr_b16 v[130:131], v225 offset:16640\n" \
"v_mfma_f32_16x16x32_bf16 v[48:51], v[160:163], a[132:135], v[48:51]\n" \
"s_add_u32 s49, s49, s55\n" \
"s_mov_b32 m0, s44\n" \
"s_nop 0\n" \
"buffer_load_dwordx4 v226, %[srdw], s49 offen offset:0 lds\n" \
"v_mfma_f32_16x16x32_bf16 v[48:51], v[164:167], a[148:151], v[48:51]\n" \
"ds_read_b64_tr_b16 v[132:133], v225 offset:24576\n" \
"v_mfma_f32_16x16x32_bf16 v[80:83], v[160:163], a[136:139], v[80:83]\n" \
"ds_read_b64_tr_b16 v[134:135], v225 offset:24832\n" \
"v_mfma_f32_16x16x32_bf16 v[80:83], v[164:167], a[152:155], v[80:83]\n" \
"v_mfma_f32_16x16x32_bf16 v[112:115], v[160:163], a[140:143], v[112:115]\n" \
"ds_read_b64_tr_b16 v[136:137], v225 offset:16392\n" \
"v_mfma_f32_16x16x32_bf16 v[112:115], v[164:167], a[156:159], v[112:115]\n" \
"ds_read_b64_tr_b16 v[138:139], v225 offset:16648\n" \
"v_mfma_f32_16x16x32_bf16 v[20:23], v[168:171], a[128:131], v[20:23]\n" \
"buffer_load_dwordx4 v216, %[srdw], s49 offen offset:1024 lds\n" \
"v_mfma_f32_16x16x32_bf16 v[20:23], v[172:175], a[144:147], v[20:23]\n" \
"ds_read_b64_tr_b16 v[140:141], v225 offset:24584\n" \
"v_mfma_f32_16x16x32_bf16 v[52:55], v[168:171], a[132:135], v[52:55]\n" \
"ds_read_b64_tr_b16 v[142:143], v225 offset:24840\n" \
"v_mfma_f32_16x16x32_bf16 v[52:55], v[172:175], a[148:151], v[52:55]\n" \
"v_mfma_f32_16x16x32_bf16 v[84:87], v[168:171], a[136:139], v[84:87]\n" \
"ds_read_b64_tr_b16 v[144:145], v225 offset:16896\n" \
"v_mfma_f32_16x16x32_bf16 v[84:87], v[172:175], a[152:155], v[84:87]\n" \
"ds_read_b64_tr_b16 v[146:147], v225 offset:17152\n" \
"v_mfma_f32_16x16x32_bf16 v[116:119], v[168:171], a[140:143], v[116:119]\n" \
"buffer_load_dwordx4 a[240:243], v227, %[srdz], s50 offen offset:3072 sc1\n" \
"v_mfma_f32_16x16x32_bf16 v[116:119], v[172:175], a[156:159], v[116:119]\n" \
"ds_read_b64_tr_b16 v[148:149], v225 offset:25088\n" \
"v_mfma_f32_16x16x32_bf16 v[24:27], v[176:179], a[128:131], v[24:27]\n" \
"ds_read_b64_tr_b16 v[150:151], v225 offset:25344\n" \
"v_mfma_f32_16x16x32_bf16 v[24:27], v[180:183], a[144:147], v[24:27]\n" \
"v_mfma_f32_16x16x32_bf16 v[56:59], v[176:179], a[132:135], v[56:59]\n" \
"ds_read_b64_tr_b16 v[152:153], v225 offset:16904\n" \
"v_mfma_f32_16x16x32_bf16 v[56:59], v[180:183], a[148:151], v[56:59]\n" \
"ds_read_b64_tr_b16 v[154:155], v225 offset:17160\n" \
"v_mfma_f32_16x16x32_bf16 v[88:91], v[176:179], a[136:139], v[88:91]\n" \
"buffer_load_dwordx4 a[244:247], v219, %[srdz], s50 offen offset:3072 sc1\n" \
"v_mfma_f32_16x16x32_bf16 v[88:91], v[180:183], a[152:155], v[88:91]\n" \
"ds_read_b64_tr_b16 v[156:157], v225 offset:25096\n" \
"v_mfma_f32_16x16x32_bf16 v[120:123], v[176:179], a[140:143], v[120:123]\n" \
"ds_read_b64_tr_b16 v[158:159], v225 offset:25352\n" \
"v_mfma_f32_16x16x32_bf16 v[120:123], v[180:183], a[156:159], v[120:123]\n" \
"v_mfma_f32_16x16x32_bf16 v[28:31], v[184:187], a[128:131], v[28:31]\n" \
"buffer_load_dwordx4 a[248:251], v220, %[srdz], s50 offen offset:3072 sc1\n" \
"v_mfma_f32_16x16x32_bf16 v[28:31], v[188:191], a[144:147], v[28:31]\n" \
"v_mfma_f32_16x16x32_bf16 v[60:63], v[184:187], a[132:135], v[60:63]\n" \
"v_mfma_f32_16x16x32_bf16 v[60:63], v[188:191], a[148:151], v[60:63]\n" \
"v_mfma_f32_16x16x32_bf16 v[92:95], v[184:187], a[136:139], v[92:95]\n" \
"buffer_load_dwordx4 a[252:255], v221, %[srdz], s50 offen offset:3072 sc1\n" \
"v_mfma_f32_16x16x32_bf16 v[92:95], v[188:191], a[152:155], v[92:95]\n" \
"v_mfma_f32_16x16x32_bf16 v[124:127], v[184:187], a[140:143], v[124:127]\n" \
"v_mfma_f32_16x16x32_bf16 v[124:127], v[188:191], a[156:159], v[124:127]\n" \
"s_waitcnt vmcnt(20) lgkmcnt(0)\n" \
"v_mfma_f32_16x16x32_bf16 v[0:3], v[128:131], a[160:163], v[0:3]\n" \
"ds_read_b64_tr_b16 v[160:161], v225 offset:17408\n" \
"v_mfma_f32_16x16x32_bf16 v[0:3], v[132:135], a[176:179], v[0:3]\n" \
"ds_read_b64_tr_b16 v[162:163], v225 offset:17664\n" \
"v_mfma_f32_16x16x32_bf16 v[32:35], v[128:131], a[164:167], v[32:35]\n" \
"s_mov_b32 m0, s44\n" \
"s_nop 0\n" \
"buffer_load_dwordx4 v217, %[srdw], s49 offen offset:2048 lds\n" \
"v_mfma_f32_16x16x32_bf16 v[32:35], v[132:135], a[180:183], v[32:35]\n" \
"ds_read_b64_tr_b16 v[164:165], v225 offset:25600\n" \
"v_mfma_f32_16x16x32_bf16 v[64:67], v[128:131], a[168:171], v[64:67]\n" \
"ds_read_b64_tr_b16 v[166:167], v225 offset:25856\n" \
"v_mfma_f32_16x16x32_bf16 v[64:67], v[132:135], a[184:187], v[64:67]\n" \
"v_mfma_f32_16x16x32_bf16 v[96:99], v[128:131], a[172:175], v[96:99]\n" \
"ds_read_b64_tr_b16 v[168:169], v225 offset:17416\n" \
"v_mfma_f32_16x16x32_bf16 v[96:99], v[132:135], a[188:191], v[96:99]\n" \
"ds_read_b64_tr_b16 v[170:171], v225 offset:17672\n" \
"v_mfma_f32_16x16x32_bf16 v[4:7], v[136:139], a[160:163], v[4:7]\n" \
"buffer_load_dwordx4 v218, %[srdw], s49 offen offset:3072 lds\n" \
"v_mfma_f32_16x16x32_bf16 v[4:7], v[140:143], a[176:179], v[4:7]\n" \
"ds_read_b64_tr_b16 v[172:173], v225 offset:25608\n" \
"v_mfma_f32_16x16x32_bf16 v[36:39], v[136:139], a[164:167], v[36:39]\n" \
"ds_read_b64_tr_b16 v[174:175], v225 offset:25864\n" \
"v_mfma_f32_16x16x32_bf16 v[36:39], v[140:143], a[180:183], v[36:39]\n" \
"v_mfma_f32_16x16x32_bf16 v[68:71], v[136:139], a[168:171], v[68:71]\n" \
"ds_read_b64_tr_b16 v[176:177], v225 offset:17920\n" \
"v_mfma_f32_16x16x32_bf16 v[68:71], v[140:143], a[184:187], v[68:71]\n" \
"ds_read_b64_tr_b16 v[178:179], v225 offset:18176\n" \
"v_mfma_f32_16x16x32_bf16 v[100:103], v[136:139], a[172:175], v[100:103]\n" \
"s_add_u32 s50, s50, s53\n" \
"buffer_load_dwordx4 a[128:131], v227, %[srdz], s50 offen offset:0 sc1\n" \
"v_mfma_f32_16x16x32_bf16 v[100:103], v[140:143], a[188:191], v[100:103]\n" \
"ds_read_b64_tr_b16 v[180:181], v225 offset:26112\n" \
"v_mfma_f32_16x16x32_bf16 v[8:11], v[144:147], a[160:163], v[8:11]\n" \
"ds_read_b64_tr_b16 v[182:183], v225 offset:26368\n" \
"v_mfma_f32_16x16x32_bf16 v[8:11], v[148:151], a[176:179], v[8:11]\n" \
"v_mfma_f32_16x16x32_bf16 v[40:43], v[144:147], a[164:167], v[40:43]\n" \
"ds_read_b64_tr_b16 v[184:185], v225 offset:17928\n" \
"v_mfma_f32_16x16x32_bf16 v[40:43], v[148:151], a[180:183], v[40:43]\n" \
"ds_read_b64_tr_b16 v[186:187], v225 offset:18184\n" \
"v_mfma_f32_16x16x32_bf16 v[72:75], v[144:147], a[168:171], v[72:75]\n" \
"buffer_load_dwordx4 a[132:135], v219, %[srdz], s50 offen offset:0 sc1\n" \
"v_mfma_f32_16x16x32_bf16 v[72:75], v[148:151], a[184:187], v[72:75]\n" \
"ds_read_b64_tr_b16 v[188:189], v225 offset:26120\n" \
"v_mfma_f32_16x16x32_bf16 v[104:107], v[144:147], a[172:175], v[104:107]\n" \
"ds_read_b64_tr_b16 v[190:191], v225 offset:26376\n" \
"v_mfma_f32_16x16x32_bf16 v[104:107], v[148:151], a[188:191], v[104:107]\n" \
"v_mfma_f32_16x16x32_bf16 v[12:15], v[152:155], a[160:163], v[12:15]\n" \
"buffer_load_dwordx4 a[136:139], v220, %[srdz], s50 offen offset:0 sc1\n" \
"v_mfma_f32_16x16x32_bf16 v[12:15], v[156:159], a[176:179], v[12:15]\n" \
"v_mfma_f32_16x16x32_bf16 v[44:47], v[152:155], a[164:167], v[44:47]\n" \
"v_mfma_f32_16x16x32_bf16 v[44:47], v[156:159], a[180:183], v[44:47]\n" \
"v_mfma_f32_16x16x32_bf16 v[76:79], v[152:155], a[168:171], v[76:79]\n" \
"buffer_load_dwordx4 a[140:143], v221, %[srdz], s50 offen offset:0 sc1\n" \
"v_mfma_f32_16x16x32_bf16 v[76:79], v[156:159], a[184:187], v[76:79]\n" \
"v_mfma_f32_16x16x32_bf16 v[108:111], v[152:155], a[172:175], v[108:111]\n" \
"v_mfma_f32_16x16x32_bf16 v[108:111], v[156:159], a[188:191], v[108:111]\n" \
"s_waitcnt vmcnt(29) lgkmcnt(0)\n" \
"s_barrier\n" \
"v_mfma_f32_16x16x32_bf16 v[16:19], v[160:163], a[160:163], v[16:19]\n" \
"ds_read_b64_tr_b16 v[128:129], v225 offset:32768\n" \
"v_mfma_f32_16x16x32_bf16 v[16:19], v[164:167], a[176:179], v[16:19]\n" \
"ds_read_b64_tr_b16 v[130:131], v225 offset:33024\n" \
"v_mfma_f32_16x16x32_bf16 v[48:51], v[160:163], a[164:167], v[48:51]\n" \
"s_add_u32 s49, s49, s55\n" \
"s_mov_b32 m0, s45\n" \
"s_nop 0\n" \
"buffer_load_dwordx4 v226, %[srdw], s49 offen offset:0 lds\n" \
"v_mfma_f32_16x16x32_bf16 v[48:51], v[164:167], a[180:183], v[48:51]\n" \
"ds_read_b64_tr_b16 v[132:133], v225 offset:40960\n" \
"v_mfma_f32_16x16x32_bf16 v[80:83], v[160:163], a[168:171], v[80:83]\n" \
"ds_read_b64_tr_b16 v[134:135], v225 offset:41216\n" \
"v_mfma_f32_16x16x32_bf16 v[80:83], v[164:167], a[184:187], v[80:83]\n" \
"v_mfma_f32_16x16x32_bf16 v[112:115], v[160:163], a[172:175], v[112:115]\n" \
"ds_read_b64_tr_b16 v[136:137], v225 offset:32776\n" \
"v_mfma_f32_16x16x32_bf16 v[112:115], v[164:167], a[188:191], v[112:115]\n" \
"ds_read_b64_tr_b16 v[138:139], v225 offset:33032\n" \
"v_mfma_f32_16x16x32_bf16 v[20:23], v[168:171], a[160:163], v[20:23]\n" \
"buffer_load_dwordx4 v216, %[srdw], s49 offen offset:1024 lds\n" \
"v_mfma_f32_16x16x32_bf16 v[20:23], v[172:175], a[176:179], v[20:23]\n" \
"ds_read_b64_tr_b16 v[140:141], v225 offset:40968\n" \
"v_mfma_f32_16x16x32_bf16 v[52:55], v[168:171], a[164:167], v[52:55]\n" \
"ds_read_b64_tr_b16 v[142:143], v225 offset:41224\n" \
"v_mfma_f32_16x16x32_bf16 v[52:55], v[172:175], a[180:183], v[52:55]\n" \
"v_mfma_f32_16x16x32_bf16 v[84:87], v[168:171], a[168:171], v[84:87]\n" \
"ds_read_b64_tr_b16 v[144:145], v225 offset:33280\n" \
"v_mfma_f32_16x16x32_bf16 v[84:87], v[172:175], a[184:187], v[84:87]\n" \
"ds_read_b64_tr_b16 v[146:147], v225 offset:33536\n" \
"v_mfma_f32_16x16x32_bf16 v[116:119], v[168:171], a[172:175], v[116:119]\n" \
"buffer_load_dwordx4 a[144:147], v227, %[srdz], s50 offen offset:1024 sc1\n" \
"v_mfma_f32_16x16x32_bf16 v[116:119], v[172:175], a[188:191], v[116:119]\n" \
"ds_read_b64_tr_b16 v[148:149], v225 offset:41472\n" \
"v_mfma_f32_16x16x32_bf16 v[24:27], v[176:179], a[160:163], v[24:27]\n" \
"ds_read_b64_tr_b16 v[150:151], v225 offset:41728\n" \
"v_mfma_f32_16x16x32_bf16 v[24:27], v[180:183], a[176:179], v[24:27]\n" \
"v_mfma_f32_16x16x32_bf16 v[56:59], v[176:179], a[164:167], v[56:59]\n" \
"ds_read_b64_tr_b16 v[152:153], v225 offset:33288\n" \
"v_mfma_f32_16x16x32_bf16 v[56:59], v[180:183], a[180:183], v[56:59]\n" \
"ds_read_b64_tr_b16 v[154:155], v225 offset:33544\n" \
"v_mfma_f32_16x16x32_bf16 v[88:91], v[176:179], a[168:171], v[88:91]\n" \
"buffer_load_dwordx4 a[148:151], v219, %[srdz], s50 offen offset:1024 sc1\n" \
"v_mfma_f32_16x16x32_bf16 v[88:91], v[180:183], a[184:187], v[88:91]\n" \
"ds_read_b64_tr_b16 v[156:157], v225 offset:41480\n" \
"v_mfma_f32_16x16x32_bf16 v[120:123], v[176:179], a[172:175], v[120:123]\n" \
"ds_read_b64_tr_b16 v[158:159], v225 offset:41736\n" \
"v_mfma_f32_16x16x32_bf16 v[120:123], v[180:183], a[188:191], v[120:123]\n" \
"v_mfma_f32_16x16x32_bf16 v[28:31], v[184:187], a[160:163], v[28:31]\n" \
"buffer_load_dwordx4 a[152:155], v220, %[srdz], s50 offen offset:1024 sc1\n" \
"v_mfma_f32_16x16x32_bf16 v[28:31], v[188:191], a[176:179], v[28:31]\n" \
"v_mfma_f32_16x16x32_bf16 v[60:63], v[184:187], a[164:167], v[60:63]\n" \
"v_mfma_f32_16x16x32_bf16 v[60:63], v[188:191], a[180:183], v[60:63]\n" \
"v_mfma_f32_16x16x32_bf16 v[92:95], v[184:187], a[168:171], v[92:95]\n" \
"buffer_load_dwordx4 a[156:159], v221, %[srdz], s50 offen offset:1024 sc1\n" \
"v_mfma_f32_16x16x32_bf16 v[92:95], v[188:191], a[184:187], v[92:95]\n" \
"v_mfma_f32_16x16x32_bf16 v[124:127], v[184:187], a[172:175], v[124:127]\n" \
"v_mfma_f32_16x16x32_bf16 v[124:127], v[188:191], a[188:191], v[124:127]\n" \
"s_waitcnt vmcnt(24) lgkmcnt(0)\n" \
"v_mfma_f32_16x16x32_bf16 v[0:3], v[128:131], a[192:195], v[0:3]\n" \
"ds_read_b64_tr_b16 v[160:161], v225 offset:33792\n" \
"v_mfma_f32_16x16x32_bf16 v[0:3], v[132:135], a[208:211], v[0:3]\n" \
"ds_read_b64_tr_b16 v[162:163], v225 offset:34048\n" \
"v_mfma_f32_16x16x32_bf16 v[32:35], v[128:131], a[196:199], v[32:35]\n" \
"s_mov_b32 m0, s45\n" \
"s_nop 0\n" \
"buffer_load_dwordx4 v217, %[srdw], s49 offen offset:2048 lds\n" \
"v_mfma_f32_16x16x32_bf16 v[32:35], v[132:135], a[212:215], v[32:35]\n" \
"ds_read_b64_tr_b16 v[164:165], v225 offset:41984\n" \
"v_mfma_f32_16x16x32_bf16 v[64:67], v[128:131], a[200:203], v[64:67]\n" \
"ds_read_b64_tr_b16 v[166:167], v225 offset:42240\n" \
"v_mfma_f32_16x16x32_bf16 v[64:67], v[132:135], a[216:219], v[64:67]\n" \
"v_mfma_f32_16x16x32_bf16 v[96:99], v[128:131], a[204:207], v[96:99]\n" \
"ds_read_b64_tr_b16 v[168:169], v225 offset:33800\n" \
"v_mfma_f32_16x16x32_bf16 v[96:99], v[132:135], a[220:223], v[96:99]\n" \
"ds_read_b64_tr_b16 v[170:171], v225 offset:34056\n" \
"v_mfma_f32_16x16x32_bf16 v[4:7], v[136:139], a[192:195], v[4:7]\n" \
"buffer_load_dwordx4 v218, %[srdw], s49 offen offset:3072 lds\n" \
"v_mfma_f32_16x16x32_bf16 v[4:7], v[140:143], a[208:211], v[4:7]\n" \
"ds_read_b64_tr_b16 v[172:173], v225 offset:41992\n" \
"v_mfma_f32_16x16x32_bf16 v[36:39], v[136:139], a[196:199], v[36:39]\n" \
"ds_read_b64_tr_b16 v[174:175], v225 offset:42248\n" \
"v_mfma_f32_16x16x32_bf16 v[36:39], v[140:143], a[212:215], v[36:39]\n" \
"v_mfma_f32_16x16x32_bf16 v[68:71], v[136:139], a[200:203], v[68:71]\n" \
"ds_read_b64_tr_b16 v[176:177], v225 offset:34304\n" \
"v_mfma_f32_16x16x32_bf16 v[68:71], v[140:143], a[216:219], v[68:71]\n" \
"ds_read_b64_tr_b16 v[178:179], v225 offset:34560\n" \
"v_mfma_f32_16x16x32_bf16 v[100:103], v[136:139], a[204:207], v[100:103]\n" \
"buffer_load_dwordx4 a[160:163], v227, %[srdz], s50 offen offset:2048 sc1\n" \
"v_mfma_f32_16x16x32_bf16 v[100:103], v[140:143], a[220:223], v[100:103]\n" \
"ds_read_b64_tr_b16 v[180:181], v225 offset:42496\n" \
"v_mfma_f32_16x16x32_bf16 v[8:11], v[144:147], a[192:195], v[8:11]\n" \
"ds_read_b64_tr_b16 v[182:183], v225 offset:42752\n" \
"v_mfma_f32_16x16x32_bf16 v[8:11], v[148:151], a[208:211], v[8:11]\n" \
"v_mfma_f32_16x16x32_bf16 v[40:43], v[144:147], a[196:199], v[40:43]\n" \
"ds_read_b64_tr_b16 v[184:185], v225 offset:34312\n" \
"v_mfma_f32_16x16x32_bf16 v[40:43], v[148:151], a[212:215], v[40:43]\n" \
"ds_read_b64_tr_b16 v[186:187], v225 offset:34568\n" \
"v_mfma_f32_16x16x32_bf16 v[72:75], v[144:147], a[200:203], v[72:75]\n" \
"buffer_load_dwordx4 a[164:167], v219, %[srdz], s50 offen offset:2048 sc1\n" \
"v_mfma_f32_16x16x32_bf16 v[72:75], v[148:151], a[216:219], v[72:75]\n" \
"ds_read_b64_tr_b16 v[188:189], v225 offset:42504\n" \
"v_mfma_f32_16x16x32_bf16 v[104:107], v[144:147], a[204:207], v[104:107]\n" \
"ds_read_b64_tr_b16 v[190:191], v225 offset:42760\n" \
"v_mfma_f32_16x16x32_bf16 v[104:107], v[148:151], a[220:223], v[104:107]\n" \
"v_mfma_f32_16x16x32_bf16 v[12:15], v[152:155], a[192:195], v[12:15]\n" \
"buffer_load_dwordx4 a[168:171], v220, %[srdz], s50 offen offset:2048 sc1\n" \
"v_mfma_f32_16x16x32_bf16 v[12:15], v[156:159], a[208:211], v[12:15]\n" \
"v_mfma_f32_16x16x32_bf16 v[44:47], v[152:155], a[196:199], v[44:47]\n" \
"v_mfma_f32_16x16x32_bf16 v[44:47], v[156:159], a[212:215], v[44:47]\n" \
"v_mfma_f32_16x16x32_bf16 v[76:79], v[152:155], a[200:203], v[76:79]\n" \
"buffer_load_dwordx4 a[172:175], v221, %[srdz], s50 offen offset:2048 sc1\n" \
"v_mfma_f32_16x16x32_bf16 v[76:79], v[156:159], a[216:219], v[76:79]\n" \
"v_mfma_f32_16x16x32_bf16 v[108:111], v[152:155], a[204:207], v[108:111]\n" \
"v_mfma_f32_16x16x32_bf16 v[108:111], v[156:159], a[220:223], v[108:111]\n" \
"s_waitcnt vmcnt(28) lgkmcnt(0)\n" \
"s_barrier\n" \
"v_mfma_f32_16x16x32_bf16 v[16:19], v[160:163], a[192:195], v[16:19]\n" \
"ds_read_b64_tr_b16 v[128:129], v225 offset:49152\n" \
"v_mfma_f32_16x16x32_bf16 v[16:19], v[164:167], a[208:211], v[16:19]\n" \
"ds_read_b64_tr_b16 v[130:131], v225 offset:49408\n" \
"v_mfma_f32_16x16x32_bf16 v[48:51], v[160:163], a[196:199], v[48:51]\n" \
"s_add_u32 s49, s49, s55\n" \
"s_mov_b32 m0, s46\n" \
"s_nop 0\n" \
"buffer_load_dwordx4 v226, %[srdw], s49 offen offset:0 lds\n" \
"v_mfma_f32_16x16x32_bf16 v[48:51], v[164:167], a[212:215], v[48:51]\n" \
"ds_read_b64_tr_b16 v[132:133], v225 offset:57344\n" \
"v_mfma_f32_16x16x32_bf16 v[80:83], v[160:163], a[200:203], v[80:83]\n" \
"ds_read_b64_tr_b16 v[134:135], v225 offset:57600\n" \
"v_mfma_f32_16x16x32_bf16 v[80:83], v[164:167], a[216:219], v[80:83]\n" \
"v_mfma_f32_16x16x32_bf16 v[112:115], v[160:163], a[204:207], v[112:115]\n" \
"ds_read_b64_tr_b16 v[136:137], v225 offset:49160\n" \
"v_mfma_f32_16x16x32_bf16 v[112:115], v[164:167], a[220:223], v[112:115]\n" \
"ds_read_b64_tr_b16 v[138:139], v225 offset:49416\n" \
"v_mfma_f32_16x16x32_bf16 v[20:23], v[168:171], a[192:195], v[20:23]\n" \
"buffer_load_dwordx4 v216, %[srdw], s49 offen offset:1024 lds\n" \
"v_mfma_f32_16x16x32_bf16 v[20:23], v[172:175], a[208:211], v[20:23]\n" \
"ds_read_b64_tr_b16 v[140:141], v225 offset:57352\n" \
"v_mfma_f32_16x16x32_bf16 v[52:55], v[168:171], a[196:199], v[52:55]\n" \
"ds_read_b64_tr_b16 v[142:143], v225 offset:57608\n" \
"v_mfma_f32_16x16x32_bf16 v[52:55], v[172:175], a[212:215], v[52:55]\n" \
"v_mfma_f32_16x16x32_bf16 v[84:87], v[168:171], a[200:203], v[84:87]\n" \
"ds_read_b64_tr_b16 v[144:145], v225 offset:49664\n" \
"v_mfma_f32_16x16x32_bf16 v[84:87], v[172:175], a[216:219], v[84:87]\n" \
"ds_read_b64_tr_b16 v[146:147], v225 offset:49920\n" \
"v_mfma_f32_16x16x32_bf16 v[116:119], v[168:171], a[204:207], v[116:119]\n" \
"buffer_load_dwordx4 a[176:179], v227, %[srdz], s50 offen offset:3072 sc1\n" \
"v_mfma_f32_16x16x32_bf16 v[116:119], v[172:175], a[220:223], v[116:119]\n" \
"ds_read_b64_tr_b16 v[148:149], v225 offset:57856\n" \
"v_mfma_f32_16x16x32_bf16 v[24:27], v[176:179], a[192:195], v[24:27]\n" \
"ds_read_b64_tr_b16 v[150:151], v225 offset:58112\n" \
"v_mfma_f32_16x16x32_bf16 v[24:27], v[180:183], a[208:211], v[24:27]\n" \
"v_mfma_f32_16x16x32_bf16 v[56:59], v[176:179], a[196:199], v[56:59]\n" \
"ds_read_b64_tr_b16 v[152:153], v225 offset:49672\n" \
"v_mfma_f32_16x16x32_bf16 v[56:59], v[180:183], a[212:215], v[56:59]\n" \
"ds_read_b64_tr_b16 v[154:155], v225 offset:49928\n" \
"v_mfma_f32_16x16x32_bf16 v[88:91], v[176:179], a[200:203], v[88:91]\n" \
"buffer_load_dwordx4 a[180:183], v219, %[srdz], s50 offen offset:3072 sc1\n" \
"v_mfma_f32_16x16x32_bf16 v[88:91], v[180:183], a[216:219], v[88:91]\n" \
"ds_read_b64_tr_b16 v[156:157], v225 offset:57864\n" \
"v_mfma_f32_16x16x32_bf16 v[120:123], v[176:179], a[204:207], v[120:123]\n" \
"ds_read_b64_tr_b16 v[158:159], v225 offset:58120\n" \
"v_mfma_f32_16x16x32_bf16 v[120:123], v[180:183], a[220:223], v[120:123]\n" \
"v_mfma_f32_16x16x32_bf16 v[28:31], v[184:187], a[192:195], v[28:31]\n" \
"buffer_load_dwordx4 a[184:187], v220, %[srdz], s50 offen offset:3072 sc1\n" \
"v_mfma_f32_16x16x32_bf16 v[28:31], v[188:191], a[208:211], v[28:31]\n" \
"v_mfma_f32_16x16x32_bf16 v[60:63], v[184:187], a[196:199], v[60:63]\n" \
"v_mfma_f32_16x16x32_bf16 v[60:63], v[188:191], a[212:215], v[60:63]\n" \
"v_mfma_f32_16x16x32_bf16 v[92:95], v[184:187], a[200:203], v[92:95]\n" \
"buffer_load_dwordx4 a[188:191], v221, %[srdz], s50 offen offset:3072 sc1\n" \
"v_mfma_f32_16x16x32_bf16 v[92:95], v[188:191], a[216:219], v[92:95]\n" \
"v_mfma_f32_16x16x32_bf16 v[124:127], v[184:187], a[204:207], v[124:127]\n" \
"v_mfma_f32_16x16x32_bf16 v[124:127], v[188:191], a[220:223], v[124:127]\n" \
"s_waitcnt vmcnt(24) lgkmcnt(0)\n" \
"v_mfma_f32_16x16x32_bf16 v[0:3], v[128:131], a[224:227], v[0:3]\n" \
"ds_read_b64_tr_b16 v[160:161], v225 offset:50176\n" \
"v_mfma_f32_16x16x32_bf16 v[0:3], v[132:135], a[240:243], v[0:3]\n" \
"ds_read_b64_tr_b16 v[162:163], v225 offset:50432\n" \
"v_mfma_f32_16x16x32_bf16 v[32:35], v[128:131], a[228:231], v[32:35]\n" \
"s_mov_b32 m0, s46\n" \
"s_nop 0\n" \
"buffer_load_dwordx4 v217, %[srdw], s49 offen offset:2048 lds\n" \
"v_mfma_f32_16x16x32_bf16 v[32:35], v[132:135], a[244:247], v[32:35]\n" \
"ds_read_b64_tr_b16 v[164:165], v225 offset:58368\n" \
"v_mfma_f32_16x16x32_bf16 v[64:67], v[128:131], a[232:235], v[64:67]\n" \
"ds_read_b64_tr_b16 v[166:167], v225 offset:58624\n" \
"v_mfma_f32_16x16x32_bf16 v[64:67], v[132:135], a[248:251], v[64:67]\n" \
"v_mfma_f32_16x16x32_bf16 v[96:99], v[128:131], a[236:239], v[96:99]\n" \
"ds_read_b64_tr_b16 v[168:169], v225 offset:50184\n" \
"v_mfma_f32_16x16x32_bf16 v[96:99], v[132:135], a[252:255], v[96:99]\n" \
"ds_read_b64_tr_b16 v[170:171], v225 offset:50440\n" \
"v_mfma_f32_16x16x32_bf16 v[4:7], v[136:139], a[224:227], v[4:7]\n" \
"buffer_load_dwordx4 v218, %[srdw], s49 offen offset:3072 lds\n" \
"v_mfma_f32_16x16x32_bf16 v[4:7], v[140:143], a[240:243], v[4:7]\n" \
"ds_read_b64_tr_b16 v[172:173], v225 offset:58376\n" \
"v_mfma_f32_16x16x32_bf16 v[36:39], v[136:139], a[228:231], v[36:39]\n" \
"ds_read_b64_tr_b16 v[174:175], v225 offset:58632\n" \
"v_mfma_f32_16x16x32_bf16 v[36:39], v[140:143], a[244:247], v[36:39]\n" \
"v_mfma_f32_16x16x32_bf16 v[68:71], v[136:139], a[232:235], v[68:71]\n" \
"ds_read_b64_tr_b16 v[176:177], v225 offset:50688\n" \
"v_mfma_f32_16x16x32_bf16 v[68:71], v[140:143], a[248:251], v[68:71]\n" \
"ds_read_b64_tr_b16 v[178:179], v225 offset:50944\n" \
"v_mfma_f32_16x16x32_bf16 v[100:103], v[136:139], a[236:239], v[100:103]\n" \
"s_add_u32 s50, s50, s53\n" \
"buffer_load_dwordx4 a[192:195], v227, %[srdz], s50 offen offset:0 sc1\n" \
"v_mfma_f32_16x16x32_bf16 v[100:103], v[140:143], a[252:255], v[100:103]\n" \
"ds_read_b64_tr_b16 v[180:181], v225 offset:58880\n" \
"v_mfma_f32_16x16x32_bf16 v[8:11], v[144:147], a[224:227], v[8:11]\n" \
"ds_read_b64_tr_b16 v[182:183], v225 offset:59136\n" \
"v_mfma_f32_16x16x32_bf16 v[8:11], v[148:151], a[240:243], v[8:11]\n" \
"v_mfma_f32_16x16x32_bf16 v[40:43], v[144:147], a[228:231], v[40:43]\n" \
"ds_read_b64_tr_b16 v[184:185], v225 offset:50696\n" \
"v_mfma_f32_16x16x32_bf16 v[40:43], v[148:151], a[244:247], v[40:43]\n" \
"ds_read_b64_tr_b16 v[186:187], v225 offset:50952\n" \
"v_mfma_f32_16x16x32_bf16 v[72:75], v[144:147], a[232:235], v[72:75]\n" \
"buffer_load_dwordx4 a[196:199], v219, %[srdz], s50 offen offset:0 sc1\n" \
"v_mfma_f32_16x16x32_bf16 v[72:75], v[148:151], a[248:251], v[72:75]\n" \
"ds_read_b64_tr_b16 v[188:189], v225 offset:58888\n" \
"v_mfma_f32_16x16x32_bf16 v[104:107], v[144:147], a[236:239], v[104:107]\n" \
"ds_read_b64_tr_b16 v[190:191], v225 offset:59144\n" \
"v_mfma_f32_16x16x32_bf16 v[104:107], v[148:151], a[252:255], v[104:107]\n" \
"v_mfma_f32_16x16x32_bf16 v[12:15], v[152:155], a[224:227], v[12:15]\n" \
"buffer_load_dwordx4 a[200:203], v220, %[srdz], s50 offen offset:0 sc1\n" \
"v_mfma_f32_16x16x32_bf16 v[12:15], v[156:159], a[240:243], v[12:15]\n" \
"v_mfma_f32_16x16x32_bf16 v[44:47], v[152:155], a[228:231], v[44:47]\n" \
"buffer_load_dword v237, v236, %[srdw], s60 offen\n" \
"s_add_u32 s60, s60, 0x200000\n" \
"v_mfma_f32_16x16x32_bf16 v[44:47], v[156:159], a[244:247], v[44:47]\n" \
"v_mfma_f32_16x16x32_bf16 v[76:79], v[152:155], a[232:235], v[76:79]\n" \
"buffer_load_dwordx4 a[204:207], v221, %[srdz], s50 offen offset:0 sc1\n" \
"v_mfma_f32_16x16x32_bf16 v[76:79], v[156:159], a[248:251], v[76:79]\n" \
".if . - adaln_body != 4096\n" \
".error \"size model\"\n" \
".endif\n" \
"Lk0_%=:\n" \
"s_cbranch_execz Lk1_%=\n" \
"s_nop 0\n" \
"v_mfma_f32_16x16x32_bf16 v[108:111], v[152:155], a[236:239], v[108:111]\n" \
"v_mfma_f32_16x16x32_bf16 v[108:111], v[156:159], a[252:255], v[108:111]\n" \
"s_waitcnt vmcnt(29) lgkmcnt(0)\n" \
"s_barrier\n" \
"v_mfma_f32_16x16x32_bf16 v[16:19], v[160:163], a[224:227], v[16:19]\n" \
"ds_read_b64_tr_b16 v[128:129], v225 offset:0\n" \
"v_mfma_f32_16x16x32_bf16 v[16:19], v[164:167], a[240:243], v[16:19]\n" \
"ds_read_b64_tr_b16 v[130:131], v225 offset:256\n" \
"v_mfma_f32_16x16x32_bf16 v[48:51], v[160:163], a[228:231], v[48:51]\n" \
"s_add_u32 s49, s49, s55\n" \
"s_mov_b32 m0, s47\n" \
"s_nop 0\n" \
"buffer_load_dwordx4 v226, %[srdw], s49 offen offset:0 lds\n" \
"v_mfma_f32_16x16x32_bf16 v[48:51], v[164:167], a[244:247], v[48:51]\n" \
"ds_read_b64_tr_b16 v[132:133], v225 offset:8192\n" \
"v_mfma_f32_16x16x32_bf16 v[80:83], v[160:163], a[232:235], v[80:83]\n" \
"ds_read_b64_tr_b16 v[134:135], v225 offset:8448\n" \
"v_mfma_f32_16x16x32_bf16 v[80:83], v[164:167], a[248:251], v[80:83]\n" \
"v_mfma_f32_16x16x32_bf16 v[112:115], v[160:163], a[236:239], v[112:115]\n" \
"ds_read_b64_tr_b16 v[136:137], v225 offset:8\n" \
"v_mfma_f32_16x16x32_bf16 v[112:115], v[164:167], a[252:255], v[112:115]\n" \
"ds_read_b64_tr_b16 v[138:139], v225 offset:264\n" \
"v_mfma_f32_16x16x32_bf16 v[20:23], v[168:171], a[224:227], v[20:23]\n" \
"buffer_load_dwordx4 v216, %[srdw], s49 offen offset:1024 lds\n" \
"v_mfma_f32_16x16x32_bf16 v[20:23], v[172:175], a[240:243], v[20:23]\n" \
"ds_read_b64_tr_b16 v[140:141], v225 offset:8200\n" \
"v_mfma_f32_16x16x32_bf16 v[52:55], v[168:171], a[228:231], v[52:55]\n" \
"ds_read_b64_tr_b16 v[142:143], v225 offset:8456\n" \
"v_mfma_f32_16x16x32_bf16 v[52:55], v[172:175], a[244:247], v[52:55]\n" \
"v_mfma_f32_16x16x32_bf16 v[84:87], v[168:171], a[232:235], v[84:87]\n" \
"ds_read_b64_tr_b16 v[144:145], v225 offset:512\n" \
"v_mfma_f32_16x16x32_bf16 v[84:87], v[172:175], a[248:251], v[84:87]\n" \
"ds_read_b64_tr_b16 v[146:147], v225 offset:768\n" \
"v_mfma_f32_16x16x32_bf16 v[116:119], v[168:171], a[236:239], v[116:119]\n" \
"buffer_load_dwordx4 a[208:211], v227, %[srdz], s50 offen offset:1024 sc1\n" \
"v_mfma_f32_16x16x32_bf16 v[116:119], v[172:175], a[252:255], v[116:119]\n" \
"ds_read_b64_tr_b16 v[148:149], v225 offset:8704\n" \
"v_mfma_f32_16x16x32_bf16 v[24:27], v[176:179], a[224:227], v[24:27]\n" \
"ds_read_b64_tr_b16 v[150:151], v225 offset:8960\n" \
"v_mfma_f32_16x16x32_bf16 v[24:27], v[180:183], a[240:243], v[24:27]\n" \
"v_mfma_f32_16x16x32_bf16 v[56:59], v[176:179], a[228:231], v[56:59]\n" \
"ds_read_b64_tr_b16 v[152:153], v225 offset:520\n" \
"v_mfma_f32_16x16x32_bf16 v[56:59], v[180:183], a[244:247], v[56:59]\n" \
"ds_read_b64_tr_b16 v[154:155], v225 offset:776\n" \
"v_mfma_f32_16x16x32_bf16 v[88:91], v[176:179], a[232:235], v[88:91]\n" \
"buffer_load_dwordx4 a[212:215], v219, %[srdz], s50 offen offset:1024 sc1\n" \
"v_mfma_f32_16x16x32_bf16 v[88:91], v[180:183], a[248:251], v[88:91]\n" \
"ds_read_b64_tr_b16 v[156:157], v225 offset:8712\n" \
"v_mfma_f32_16x16x32_bf16 v[120:123], v[176:179], a[236:239], v[120:123]\n" \
"ds_read_b64_tr_b16 v[158:159], v225 offset:8968\n" \
"v_mfma_f32_16x16x32_bf16 v[120:123], v[180:183], a[252:255], v[120:123]\n" \
"v_mfma_f32_16x16x32_bf16 v[28:31], v[184:187], a[224:227], v[28:31]\n" \
"buffer_load_dwordx4 a[216:219], v220, %[srdz], s50 offen offset:1024 sc1\n" \
"v_mfma_f32_16x16x32_bf16 v[28:31], v[188:191], a[240:243], v[28:31]\n" \
"v_mfma_f32_16x16x32_bf16 v[60:63], v[184:187], a[228:231], v[60:63]\n" \
"v_mfma_f32_16x16x32_bf16 v[60:63], v[188:191], a[244:247], v[60:63]\n" \
"v_mfma_f32_16x16x32_bf16 v[92:95], v[184:187], a[232:235], v[92:95]\n" \
"buffer_load_dwordx4 a[220:223], v221, %[srdz], s50 offen offset:1024 sc1\n" \
"v_mfma_f32_16x16x32_bf16 v[92:95], v[188:191], a[248:251], v[92:95]\n" \
"v_mfma_f32_16x16x32_bf16 v[124:127], v[184:187], a[236:239], v[124:127]\n" \
"v_mfma_f32_16x16x32_bf16 v[124:127], v[188:191], a[252:255], v[124:127]\n" \
"s_waitcnt vmcnt(25) lgkmcnt(0)\n" \
"s_sub_u32 s51, s51, 1\n" \
"s_cmp_lg_u32 s51, 0\n" \
"s_cbranch_scc1 Lq_%=\n" \
"s_nop 0\n" \
"v_mfma_f32_16x16x32_bf16 v[0:3], v[128:131], a[128:131], v[0:3]\n" \
"ds_read_b64_tr_b16 v[160:161], v225 offset:1024\n" \
"v_mfma_f32_16x16x32_bf16 v[0:3], v[132:135], a[144:147], v[0:3]\n" \
"ds_read_b64_tr_b16 v[162:163], v225 offset:1280\n" \
"v_mfma_f32_16x16x32_bf16 v[32:35], v[128:131], a[132:135], v[32:35]\n" \
"s_mov_b32 m0, s47\n" \
"s_nop 0\n" \
"buffer_load_dwordx4 v217, %[srdw], s49 offen offset:2048 lds\n" \
"v_mfma_f32_16x16x32_bf16 v[32:35], v[132:135], a[148:151], v[32:35]\n" \
"ds_read_b64_tr_b16 v[164:165], v225 offset:9216\n" \
"v_mfma_f32_16x16x32_bf16 v[64:67], v[128:131], a[136:139], v[64:67]\n" \
"ds_read_b64_tr_b16 v[166:167], v225 offset:9472\n" \
"v_mfma_f32_16x16x32_bf16 v[64:67], v[132:135], a[152:155], v[64:67]\n" \
"v_mfma_f32_16x16x32_bf16 v[96:99], v[128:131], a[140:143], v[96:99]\n" \
"ds_read_b64_tr_b16 v[168:169], v225 offset:1032\n" \
"v_mfma_f32_16x16x32_bf16 v[96:99], v[132:135], a[156:159], v[96:99]\n" \
"ds_read_b64_tr_b16 v[170:171], v225 offset:1288\n" \
"v_mfma_f32_16x16x32_bf16 v[4:7], v[136:139], a[128:131], v[4:7]\n" \
"buffer_load_dwordx4 v218, %[srdw], s49 offen offset:3072 lds\n" \
"v_mfma_f32_16x16x32_bf16 v[4:7], v[140:143], a[144:147], v[4:7]\n" \
"ds_read_b64_tr_b16 v[172:173], v225 offset:9224\n" \
"v_mfma_f32_16x16x32_bf16 v[36:39], v[136:139], a[132:135], v[36:39]\n" \
"ds_read_b64_tr_b16 v[174:175], v225 offset:9480\n" \
"v_mfma_f32_16x16x32_bf16 v[36:39], v[140:143], a[148:151], v[36:39]\n" \
"v_mfma_f32_16x16x32_bf16 v[68:71], v[136:139], a[136:139], v[68:71]\n" \
"ds_read_b64_tr_b16 v[176:177], v225 offset:1536\n" \
"v_mfma_f32_16x16x32_bf16 v[68:71], v[140:143], a[152:155], v[68:71]\n" \
"ds_read_b64_tr_b16 v[178:179], v225 offset:1792\n" \
"v_mfma_f32_16x16x32_bf16 v[100:103], v[136:139], a[140:143], v[100:103]\n" \
"buffer_load_dwordx4 a[224:227], v227, %[srdz], s50 offen offset:2048 sc1\n" \
"v_mfma_f32_16x16x32_bf16 v[100:103], v[140:143], a[156:159], v[100:103]\n" \
"ds_read_b64_tr_b16 v[180:181], v225 offset:9728\n" \
"v_mfma_f32_16x16x32_bf16 v[8:11], v[144:147], a[128:131], v[8:11]\n" \
"ds_read_b64_tr_b16 v[182:183], v225 offset:9984\n" \
"v_mfma_f32_16x16x32_bf16 v[8:11], v[148:151], a[144:147], v[8:11]\n" \
"v_mfma_f32_16x16x32_bf16 v[40:43], v[144:147], a[132:135], v[40:43]\n" \
"ds_read_b64_tr_b16 v[184:185], v225 offset:1544\n" \
"v_mfma_f32_16x16x32_bf16 v[40:43], v[148:151], a[148:151], v[40:43]\n" \
"ds_read_b64_tr_b16 v[186:187], v225 offset:1800\n" \
"v_mfma_f32_16x16x32_bf16 v[72:75], v[144:147], a[136:139], v[72:75]\n" \
"buffer_load_dwordx4 a[228:231], v219, %[srdz], s50 offen offset:2048 sc1\n" \
"v_mfma_f32_16x16x32_bf16 v[72:75], v[148:151], a[152:155], v[72:75]\n" \
"ds_read_b64_tr_b16 v[188:189], v225 offset:9736\n" \
"v_mfma_f32_16x16x32_bf16 v[104:107], v[144:147], a[140:143], v[104:107]\n" \
"ds_read_b64_tr_b16 v[190:191], v225 offset:9992\n" \
"v_mfma_f32_16x16x32_bf16 v[104:107], v[148:151], a[156:159], v[104:107]\n" \
"v_mfma_f32_16x16x32_bf16 v[12:15], v[152:155], a[128:131], v[12:15]\n" \
"buffer_load_dwordx4 a[232:235], v220, %[srdz], s50 offen offset:2048 sc1\n" \
"v_mfma_f32_16x16x32_bf16 v[12:15], v[156:159], a[144:147], v[12:15]\n" \
"v_mfma_f32_16x16x32_bf16 v[44:47], v[152:155], a[132:135], v[44:47]\n" \
"v_mfma_f32_16x16x32_bf16 v[44:47], v[156:159], a[148:151], v[44:47]\n" \
"v_mfma_f32_16x16x32_bf16 v[76:79], v[152:155], a[136:139], v[76:79]\n" \
"buffer_load_dwordx4 a[236:239], v221, %[srdz], s50 offen offset:2048 sc1\n" \
"v_mfma_f32_16x16x32_bf16 v[76:79], v[156:159], a[152:155], v[76:79]\n" \
"v_mfma_f32_16x16x32_bf16 v[108:111], v[152:155], a[140:143], v[108:111]\n" \
"v_mfma_f32_16x16x32_bf16 v[108:111], v[156:159], a[156:159], v[108:111]\n" \
"s_waitcnt vmcnt(29) lgkmcnt(0)\n" \
"s_barrier\n" \
"v_mfma_f32_16x16x32_bf16 v[16:19], v[160:163], a[128:131], v[16:19]\n" \
"ds_read_b64_tr_b16 v[128:129], v225 offset:16384\n" \
"v_mfma_f32_16x16x32_bf16 v[16:19], v[164:167], a[144:147], v[16:19]\n" \
"ds_read_b64_tr_b16 v[130:131], v225 offset:16640\n" \
"v_mfma_f32_16x16x32_bf16 v[48:51], v[160:163], a[132:135], v[48:51]\n" \
"s_mov_b32 s48, 16384\n" \
"s_mov_b32 m0, s44\n" \
"s_nop 0\n" \
"buffer_load_dwordx4 %[vd0], %[srdv], s48 offen offset:0 lds\n" \
"v_mfma_f32_16x16x32_bf16 v[48:51], v[164:167], a[148:151], v[48:51]\n" \
"ds_read_b64_tr_b16 v[132:133], v225 offset:24576\n" \
"v_mfma_f32_16x16x32_bf16 v[80:83], v[160:163], a[136:139], v[80:83]\n" \
"ds_read_b64_tr_b16 v[134:135], v225 offset:24832\n" \
"v_mfma_f32_16x16x32_bf16 v[80:83], v[164:167], a[152:155], v[80:83]\n" \
"v_mfma_f32_16x16x32_bf16 v[112:115], v[160:163], a[140:143], v[112:115]\n" \
"ds_read_b64_tr_b16 v[136:137], v225 offset:16392\n" \
"v_mfma_f32_16x16x32_bf16 v[112:115], v[164:167], a[156:159], v[112:115]\n" \
"ds_read_b64_tr_b16 v[138:139], v225 offset:16648\n" \
"v_mfma_f32_16x16x32_bf16 v[20:23], v[168:171], a[128:131], v[20:23]\n" \
"buffer_load_dwordx4 v215, %[srdv], s48 offen offset:1024 lds\n" \
"v_mfma_f32_16x16x32_bf16 v[20:23], v[172:175], a[144:147], v[20:23]\n" \
"ds_read_b64_tr_b16 v[140:141], v225 offset:24584\n" \
"v_mfma_f32_16x16x32_bf16 v[52:55], v[168:171], a[132:135], v[52:55]\n" \
"ds_read_b64_tr_b16 v[142:143], v225 offset:24840\n" \
"v_mfma_f32_16x16x32_bf16 v[52:55], v[172:175], a[148:151], v[52:55]\n" \
"v_mfma_f32_16x16x32_bf16 v[84:87], v[168:171], a[136:139], v[84:87]\n" \
"ds_read_b64_tr_b16 v[144:145], v225 offset:16896\n" \
"v_mfma_f32_16x16x32_bf16 v[84:87], v[172:175], a[152:155], v[84:87]\n" \
"ds_read_b64_tr_b16 v[146:147], v225 offset:17152\n" \
"v_mfma_f32_16x16x32_bf16 v[116:119], v[168:171], a[140:143], v[116:119]\n" \
"buffer_load_dwordx4 a[240:243], v227, %[srdz], s50 offen offset:3072 sc1\n" \
"v_mfma_f32_16x16x32_bf16 v[116:119], v[172:175], a[156:159], v[116:119]\n" \
"ds_read_b64_tr_b16 v[148:149], v225 offset:25088\n" \
"v_mfma_f32_16x16x32_bf16 v[24:27], v[176:179], a[128:131], v[24:27]\n" \
"ds_read_b64_tr_b16 v[150:151], v225 offset:25344\n" \
"v_mfma_f32_16x16x32_bf16 v[24:27], v[180:183], a[144:147], v[24:27]\n" \
"v_mfma_f32_16x16x32_bf16 v[56:59], v[176:179], a[132:135], v[56:59]\n" \
"ds_read_b64_tr_b16 v[152:153], v225 offset:16904\n" \
"v_mfma_f32_16x16x32_bf16 v[56:59], v[180:183], a[148:151], v[56:59]\n" \
"ds_read_b64_tr_b16 v[154:155], v225 offset:17160\n" \
"v_mfma_f32_16x16x32_bf16 v[88:91], v[176:179], a[136:139], v[88:91]\n" \
"buffer_load_dwordx4 a[244:247], v219, %[srdz], s50 offen offset:3072 sc1\n" \
"v_mfma_f32_16x16x32_bf16 v[88:91], v[180:183], a[152:155], v[88:91]\n" \
"ds_read_b64_tr_b16 v[156:157], v225 offset:25096\n" \
"v_mfma_f32_16x16x32_bf16 v[120:123], v[176:179], a[140:143], v[120:123]\n" \
"ds_read_b64_tr_b16 v[158:159], v225 offset:25352\n" \
"v_mfma_f32_16x16x32_bf16 v[120:123], v[180:183], a[156:159], v[120:123]\n" \
"v_mfma_f32_16x16x32_bf16 v[28:31], v[184:187], a[128:131], v[28:31]\n" \
"buffer_load_dwordx4 a[248:251], v220, %[srdz], s50 offen offset:3072 sc1\n" \
"v_mfma_f32_16x16x32_bf16 v[28:31], v[188:191], a[144:147], v[28:31]\n" \
"v_mfma_f32_16x16x32_bf16 v[60:63], v[184:187], a[132:135], v[60:63]\n" \
"v_mfma_f32_16x16x32_bf16 v[60:63], v[188:191], a[148:151], v[60:63]\n" \
"v_mfma_f32_16x16x32_bf16 v[92:95], v[184:187], a[136:139], v[92:95]\n" \
"buffer_load_dwordx4 a[252:255], v221, %[srdz], s50 offen offset:3072 sc1\n" \
"v_mfma_f32_16x16x32_bf16 v[92:95], v[188:191], a[152:155], v[92:95]\n" \
"v_mfma_f32_16x16x32_bf16 v[124:127], v[184:187], a[140:143], v[124:127]\n" \
"v_mfma_f32_16x16x32_bf16 v[124:127], v[188:191], a[156:159], v[124:127]\n" \
"s_waitcnt vmcnt(25) lgkmcnt(0)\n" \
"v_mfma_f32_16x16x32_bf16 v[0:3], v[128:131], a[160:163], v[0:3]\n" \
"ds_read_b64_tr_b16 v[160:161], v225 offset:17408\n" \
"v_mfma_f32_16x16x32_bf16 v[0:3], v[132:135], a[176:179], v[0:3]\n" \
"ds_read_b64_tr_b16 v[162:163], v225 offset:17664\n" \
"v_mfma_f32_16x16x32_bf16 v[32:35], v[128:131], a[164:167], v[32:35]\n" \
"s_mov_b32 s48, 16384\n" \
"s_mov_b32 m0, s44\n" \
"s_nop 0\n" \
"buffer_load_dwordx4 %[vd0], %[srdv], s48 offen offset:2048 lds\n" \
"v_mfma_f32_16x16x32_bf16 v[32:35], v[132:135], a[180:183], v[32:35]\n" \
"ds_read_b64_tr_b16 v[164:165], v225 offset:25600\n" \
"v_mfma_f32_16x16x32_bf16 v[64:67], v[128:131], a[168:171], v[64:67]\n" \
"ds_read_b64_tr_b16 v[166:167], v225 offset:25856\n" \
"v_mfma_f32_16x16x32_bf16 v[64:67], v[132:135], a[184:187], v[64:67]\n" \
"v_mfma_f32_16x16x32_bf16 v[96:99], v[128:131], a[172:175], v[96:99]\n" \
"ds_read_b64_tr_b16 v[168:169], v225 offset:17416\n" \
"v_mfma_f32_16x16x32_bf16 v[96:99], v[132:135], a[188:191], v[96:99]\n" \
"ds_read_b64_tr_b16 v[170:171], v225 offset:17672\n" \
"v_mfma_f32_16x16x32_bf16 v[4:7], v[136:139], a[160:163], v[4:7]\n" \
"buffer_load_dwordx4 v215, %[srdv], s48 offen offset:3072 lds\n" \
"v_mfma_f32_16x16x32_bf16 v[4:7], v[140:143], a[176:179], v[4:7]\n" \
"ds_read_b64_tr_b16 v[172:173], v225 offset:25608\n" \
"v_mfma_f32_16x16x32_bf16 v[36:39], v[136:139], a[164:167], v[36:39]\n" \
"ds_read_b64_tr_b16 v[174:175], v225 offset:25864\n" \
"v_mfma_f32_16x16x32_bf16 v[36:39], v[140:143], a[180:183], v[36:39]\n" \
"v_mfma_f32_16x16x32_bf16 v[68:71], v[136:139], a[168:171], v[68:71]\n" \
"ds_read_b64_tr_b16 v[176:177], v225 offset:17920\n" \
"v_mfma_f32_16x16x32_bf16 v[68:71], v[140:143], a[184:187], v[68:71]\n" \
"ds_read_b64_tr_b16 v[178:179], v225 offset:18176\n" \
"v_mfma_f32_16x16x32_bf16 v[100:103], v[136:139], a[172:175], v[100:103]\n" \
"s_mov_b32 s48, 0\n" \
"s_mov_b32 m0, s40\n" \
"s_nop 0\n" \
"buffer_load_dwordx4 %[kd0], %[srdk], s48 offen offset:0 lds\n" \
"v_mfma_f32_16x16x32_bf16 v[100:103], v[140:143], a[188:191], v[100:103]\n" \
"ds_read_b64_tr_b16 v[180:181], v225 offset:26112\n" \
"v_mfma_f32_16x16x32_bf16 v[8:11], v[144:147], a[160:163], v[8:11]\n" \
"ds_read_b64_tr_b16 v[182:183], v225 offset:26368\n" \
"v_mfma_f32_16x16x32_bf16 v[8:11], v[148:151], a[176:179], v[8:11]\n" \
"v_mfma_f32_16x16x32_bf16 v[40:43], v[144:147], a[164:167], v[40:43]\n" \
"ds_read_b64_tr_b16 v[184:185], v225 offset:17928\n" \
"v_mfma_f32_16x16x32_bf16 v[40:43], v[148:151], a[180:183], v[40:43]\n" \
"ds_read_b64_tr_b16 v[186:187], v225 offset:18184\n" \
"v_mfma_f32_16x16x32_bf16 v[72:75], v[144:147], a[168:171], v[72:75]\n" \
"buffer_load_dwordx4 v212, %[srdk], s48 offen offset:1024 lds\n" \
"v_mfma_f32_16x16x32_bf16 v[72:75], v[148:151], a[184:187], v[72:75]\n" \
"ds_read_b64_tr_b16 v[188:189], v225 offset:26120\n" \
"v_mfma_f32_16x16x32_bf16 v[104:107], v[144:147], a[172:175], v[104:107]\n" \
"ds_read_b64_tr_b16 v[190:191], v225 offset:26376\n" \
"v_mfma_f32_16x16x32_bf16 v[104:107], v[148:151], a[188:191], v[104:107]\n" \
"v_mfma_f32_16x16x32_bf16 v[12:15], v[152:155], a[160:163], v[12:15]\n" \
"buffer_load_dwordx4 v213, %[srdk], s48 offen offset:2048 lds\n" \
"v_mfma_f32_16x16x32_bf16 v[12:15], v[156:159], a[176:179], v[12:15]\n" \
"v_mfma_f32_16x16x32_bf16 v[44:47], v[152:155], a[164:167], v[44:47]\n" \
"v_mfma_f32_16x16x32_bf16 v[44:47], v[156:159], a[180:183], v[44:47]\n" \
"v_mfma_f32_16x16x32_bf16 v[76:79], v[152:155], a[168:171], v[76:79]\n" \
"buffer_load_dwordx4 v214, %[srdk], s48 offen offset:3072 lds\n" \
"v_mfma_f32_16x16x32_bf16 v[76:79], v[156:159], a[184:187], v[76:79]\n" \
"v_mfma_f32_16x16x32_bf16 v[108:111], v[152:155], a[172:175], v[108:111]\n" \
"v_mfma_f32_16x16x32_bf16 v[108:111], v[156:159], a[188:191], v[108:111]\n" \
"s_waitcnt vmcnt(29) lgkmcnt(0)\n" \
"s_barrier\n" \
"v_mfma_f32_16x16x32_bf16 v[16:19], v[160:163], a[160:163], v[16:19]\n" \
"ds_read_b64_tr_b16 v[128:129], v225 offset:32768\n" \
"v_mfma_f32_16x16x32_bf16 v[16:19], v[164:167], a[176:179], v[16:19]\n" \
"ds_read_b64_tr_b16 v[130:131], v225 offset:33024\n" \
"v_mfma_f32_16x16x32_bf16 v[48:51], v[160:163], a[164:167], v[48:51]\n" \
"s_mov_b32 s48, 32768\n" \
"s_mov_b32 m0, s45\n" \
"s_nop 0\n" \
"buffer_load_dwordx4 %[vd0], %[srdv], s48 offen offset:0 lds\n" \
"v_mfma_f32_16x16x32_bf16 v[48:51], v[164:167], a[180:183], v[48:51]\n" \
"ds_read_b64_tr_b16 v[132:133], v225 offset:40960\n" \
"v_mfma_f32_16x16x32_bf16 v[80:83], v[160:163], a[168:171], v[80:83]\n" \
"ds_read_b64_tr_b16 v[134:135], v225 offset:41216\n" \
"v_mfma_f32_16x16x32_bf16 v[80:83], v[164:167], a[184:187], v[80:83]\n" \
"v_mfma_f32_16x16x32_bf16 v[112:115], v[160:163], a[172:175], v[112:115]\n" \
"ds_read_b64_tr_b16 v[136:137], v225 offset:32776\n" \
"v_mfma_f32_16x16x32_bf16 v[112:115], v[164:167], a[188:191], v[112:115]\n" \
"ds_read_b64_tr_b16 v[138:139], v225 offset:33032\n" \
"v_mfma_f32_16x16x32_bf16 v[20:23], v[168:171], a[160:163], v[20:23]\n" \
"buffer_load_dwordx4 v215, %[srdv], s48 offen offset:1024 lds\n" \
"v_mfma_f32_16x16x32_bf16 v[20:23], v[172:175], a[176:179], v[20:23]\n" \
"ds_read_b64_tr_b16 v[140:141], v225 offset:40968\n" \
"v_mfma_f32_16x16x32_bf16 v[52:55], v[168:171], a[164:167], v[52:55]\n" \
"ds_read_b64_tr_b16 v[142:143], v225 offset:41224\n" \
"v_mfma_f32_16x16x32_bf16 v[52:55], v[172:175], a[180:183], v[52:55]\n" \
"v_mfma_f32_16x16x32_bf16 v[84:87], v[168:171], a[168:171], v[84:87]\n" \
"ds_read_b64_tr_b16 v[144:145], v225 offset:33280\n" \
"v_mfma_f32_16x16x32_bf16 v[84:87], v[172:175], a[184:187], v[84:87]\n" \
"ds_read_b64_tr_b16 v[146:147], v225 offset:33536\n" \
"v_mfma_f32_16x16x32_bf16 v[116:119], v[168:171], a[172:175], v[116:119]\n" \
"s_mov_b32 s48, 16384\n" \
"s_mov_b32 m0, s41\n" \
"s_nop 0\n" \
"buffer_load_dwordx4 %[kd0], %[srdk], s48 offen offset:0 lds\n" \
"v_mfma_f32_16x16x32_bf16 v[116:119], v[172:175], a[188:191], v[116:119]\n" \
"ds_read_b64_tr_b16 v[148:149], v225 offset:41472\n" \
"v_mfma_f32_16x16x32_bf16 v[24:27], v[176:179], a[160:163], v[24:27]\n" \
"ds_read_b64_tr_b16 v[150:151], v225 offset:41728\n" \
"v_mfma_f32_16x16x32_bf16 v[24:27], v[180:183], a[176:179], v[24:27]\n" \
"v_mfma_f32_16x16x32_bf16 v[56:59], v[176:179], a[164:167], v[56:59]\n" \
"ds_read_b64_tr_b16 v[152:153], v225 offset:33288\n" \
"v_mfma_f32_16x16x32_bf16 v[56:59], v[180:183], a[180:183], v[56:59]\n" \
"ds_read_b64_tr_b16 v[154:155], v225 offset:33544\n" \
"v_mfma_f32_16x16x32_bf16 v[88:91], v[176:179], a[168:171], v[88:91]\n" \
"buffer_load_dwordx4 v212, %[srdk], s48 offen offset:1024 lds\n" \
"v_mfma_f32_16x16x32_bf16 v[88:91], v[180:183], a[184:187], v[88:91]\n" \
"ds_read_b64_tr_b16 v[156:157], v225 offset:41480\n" \
"v_mfma_f32_16x16x32_bf16 v[120:123], v[176:179], a[172:175], v[120:123]\n" \
"ds_read_b64_tr_b16 v[158:159], v225 offset:41736\n" \
"v_mfma_f32_16x16x32_bf16 v[120:123], v[180:183], a[188:191], v[120:123]\n" \
"v_mfma_f32_16x16x32_bf16 v[28:31], v[184:187], a[160:163], v[28:31]\n" \
"buffer_load_dwordx4 v213, %[srdk], s48 offen offset:2048 lds\n" \
"v_mfma_f32_16x16x32_bf16 v[28:31], v[188:191], a[176:179], v[28:31]\n" \
"v_mfma_f32_16x16x32_bf16 v[60:63], v[184:187], a[164:167], v[60:63]\n" \
"v_mfma_f32_16x16x32_bf16 v[60:63], v[188:191], a[180:183], v[60:63]\n" \
"v_mfma_f32_16x16x32_bf16 v[92:95], v[184:187], a[168:171], v[92:95]\n" \
"buffer_load_dwordx4 v214, %[srdk], s48 offen offset:3072 lds\n" \
"v_mfma_f32_16x16x32_bf16 v[92:95], v[188:191], a[184:187], v[92:95]\n" \
"v_mfma_f32_16x16x32_bf16 v[124:127], v[184:187], a[172:175], v[124:127]\n" \
"v_mfma_f32_16x16x32_bf16 v[124:127], v[188:191], a[188:191], v[124:127]\n" \
"s_waitcnt vmcnt(24) lgkmcnt(0)\n" \
"v_mfma_f32_16x16x32_bf16 v[0:3], v[128:131], a[192:195], v[0:3]\n" \
"ds_read_b64_tr_b16 v[160:161], v225 offset:33792\n" \
"v_mfma_f32_16x16x32_bf16 v[0:3], v[132:135], a[208:211], v[0:3]\n" \
"ds_read_b64_tr_b16 v[162:163], v225 offset:34048\n" \
"v_mfma_f32_16x16x32_bf16 v[32:35], v[128:131], a[196:199], v[32:35]\n" \
"s_mov_b32 s48, 32768\n" \
"s_mov_b32 m0, s45\n" \
"s_nop 0\n" \
"buffer_load_dwordx4 %[vd0], %[srdv], s48 offen offset:2048 lds\n" \
"v_mfma_f32_16x16x32_bf16 v[32:35], v[132:135], a[212:215], v[32:35]\n" \
"ds_read_b64_tr_b16 v[164:165], v225 offset:41984\n" \
"v_mfma_f32_16x16x32_bf16 v[64:67], v[128:131], a[200:203], v[64:67]\n" \
"ds_read_b64_tr_b16 v[166:167], v225 offset:42240\n" \
"v_mfma_f32_16x16x32_bf16 v[64:67], v[132:135], a[216:219], v[64:67]\n" \
"v_mfma_f32_16x16x32_bf16 v[96:99], v[128:131], a[204:207], v[96:99]\n" \
"ds_read_b64_tr_b16 v[168:169], v225 offset:33800\n" \
"v_mfma_f32_16x16x32_bf16 v[96:99], v[132:135], a[220:223], v[96:99]\n" \
"ds_read_b64_tr_b16 v[170:171], v225 offset:34056\n" \
"v_mfma_f32_16x16x32_bf16 v[4:7], v[136:139], a[192:195], v[4:7]\n" \
"buffer_load_dwordx4 v215, %[srdv], s48 offen offset:3072 lds\n" \
"v_mfma_f32_16x16x32_bf16 v[4:7], v[140:143], a[208:211], v[4:7]\n" \
"ds_read_b64_tr_b16 v[172:173], v225 offset:41992\n" \
"v_mfma_f32_16x16x32_bf16 v[36:39], v[136:139], a[196:199], v[36:39]\n" \
"ds_read_b64_tr_b16 v[174:175], v225 offset:42248\n" \
"v_mfma_f32_16x16x32_bf16 v[36:39], v[140:143], a[212:215], v[36:39]\n" \
"v_mfma_f32_16x16x32_bf16 v[68:71], v[136:139], a[200:203], v[68:71]\n" \
"ds_read_b64_tr_b16 v[176:177], v225 offset:34304\n" \
"v_mfma_f32_16x16x32_bf16 v[68:71], v[140:143], a[216:219], v[68:71]\n" \
"ds_read_b64_tr_b16 v[178:179], v225 offset:34560\n" \
"v_mfma_f32_16x16x32_bf16 v[100:103], v[136:139], a[204:207], v[100:103]\n" \
"s_mov_b32 s48, 32768\n" \
"s_mov_b32 m0, s42\n" \
"s_nop 0\n" \
"buffer_load_dwordx4 %[kd0], %[srdk], s48 offen offset:0 lds\n" \
"v_mfma_f32_16x16x32_bf16 v[100:103], v[140:143], a[220:223], v[100:103]\n" \
"ds_read_b64_tr_b16 v[180:181], v225 offset:42496\n" \
"v_mfma_f32_16x16x32_bf16 v[8:11], v[144:147], a[192:195], v[8:11]\n" \
"ds_read_b64_tr_b16 v[182:183], v225 offset:42752\n" \
"v_mfma_f32_16x16x32_bf16 v[8:11], v[148:151], a[208:211], v[8:11]\n" \
"v_mfma_f32_16x16x32_bf16 v[40:43], v[144:147], a[196:199], v[40:43]\n" \
"ds_read_b64_tr_b16 v[184:185], v225 offset:34312\n" \
"v_mfma_f32_16x16x32_bf16 v[40:43], v[148:151], a[212:215], v[40:43]\n" \
"ds_read_b64_tr_b16 v[186:187], v225 offset:34568\n" \
"v_mfma_f32_16x16x32_bf16 v[72:75], v[144:147], a[200:203], v[72:75]\n" \
"buffer_load_dwordx4 v212, %[srdk], s48 offen offset:1024 lds\n" \
"v_mfma_f32_16x16x32_bf16 v[72:75], v[148:151], a[216:219], v[72:75]\n" \
"ds_read_b64_tr_b16 v[188:189], v225 offset:42504\n" \
"v_mfma_f32_16x16x32_bf16 v[104:107], v[144:147], a[204:207], v[104:107]\n" \
"ds_read_b64_tr_b16 v[190:191], v225 offset:42760\n" \
"v_mfma_f32_16x16x32_bf16 v[104:107], v[148:151], a[220:223], v[104:107]\n" \
"v_mfma_f32_16x16x32_bf16 v[12:15], v[152:155], a[192:195], v[12:15]\n" \
"buffer_load_dwordx4 v213, %[srdk], s48 offen offset:2048 lds\n" \
"v_mfma_f32_16x16x32_bf16 v[12:15], v[156:159], a[208:211], v[12:15]\n" \
"v_mfma_f32_16x16x32_bf16 v[44:47], v[152:155], a[196:199], v[44:47]\n" \
"v_mfma_f32_16x16x32_bf16 v[44:47], v[156:159], a[212:215], v[44:47]\n" \
"v_mfma_f32_16x16x32_bf16 v[76:79], v[152:155], a[200:203], v[76:79]\n" \
"buffer_load_dwordx4 v214, %[srdk], s48 offen offset:3072 lds\n" \
"v_mfma_f32_16x16x32_bf16 v[76:79], v[156:159], a[216:219], v[76:79]\n" \
"v_mfma_f32_16x16x32_bf16 v[108:111], v[152:155], a[204:207], v[108:111]\n" \
"v_mfma_f32_16x16x32_bf16 v[108:111], v[156:159], a[220:223], v[108:111]\n" \
"s_waitcnt vmcnt(28) lgkmcnt(0)\n" \
"s_barrier\n" \
"v_mfma_f32_16x16x32_bf16 v[16:19], v[160:163], a[192:195], v[16:19]\n" \
"ds_read_b64_tr_b16 v[128:129], v225 offset:49152\n" \
"s_mov_b32 s54, 0x4000\n" \
"v_mfma_f32_16x16x32_bf16 v[16:19], v[164:167], a[208:211], v[16:19]\n" \
"ds_read_b64_tr_b16 v[130:131], v225 offset:49408\n" \
"v_mfma_f32_16x16x32_bf16 v[48:51], v[160:163], a[196:199], v[48:51]\n" \
"s_mov_b32 s48, 49152\n" \
"s_mov_b32 m0, s46\n" \
"s_nop 0\n" \
"buffer_load_dwordx4 %[vd0], %[srdv], s48 offen offset:0 lds\n" \
"v_mbcnt_lo_u32_b32 v231, -1, 0\n" \
"v_mfma_f32_16x16x32_bf16 v[48:51], v[164:167], a[212:215], v[48:51]\n" \
"ds_read_b64_tr_b16 v[132:133], v225 offset:57344\n" \
"v_mfma_f32_16x16x32_bf16 v[80:83], v[160:163], a[200:203], v[80:83]\n" \
"ds_read_b64_tr_b16 v[134:135], v225 offset:57600\n" \
"v_mfma_f32_16x16x32_bf16 v[80:83], v[164:167], a[216:219], v[80:83]\n" \
"v_mbcnt_hi_u32_b32 v231, -1, v231\n" \
"v_mfma_f32_16x16x32_bf16 v[112:115], v[160:163], a[204:207], v[112:115]\n" \
"ds_read_b64_tr_b16 v[136:137], v225 offset:49160\n" \
"v_mfma_f32_16x16x32_bf16 v[112:115], v[164:167], a[220:223], v[112:115]\n" \
"ds_read_b64_tr_b16 v[138:139], v225 offset:49416\n" \
"v_mfma_f32_16x16x32_bf16 v[20:23], v[168:171], a[192:195], v[20:23]\n" \
"buffer_load_dwordx4 v215, %[srdv], s48 offen offset:1024 lds\n" \
"v_lshrrev_b32_e32 v232, 4, v231\n" \
"v_mfma_f32_16x16x32_bf16 v[20:23], v[172:175], a[208:211], v[20:23]\n" \
"ds_read_b64_tr_b16 v[140:141], v225 offset:57352\n" \
"v_mfma_f32_16x16x32_bf16 v[52:55], v[168:171], a[196:199], v[52:55]\n" \
"ds_read_b64_tr_b16 v[142:143], v225 offset:57608\n" \
"v_xor_b32_e64 v232, v232, v231\n" \
"v_mfma_f32_16x16x32_bf16 v[52:55], v[172:175], a[212:215], v[52:55]\n" \
"v_mfma_f32_16x16x32_bf16 v[84:87], v[168:171], a[200:203], v[84:87]\n" \
"ds_read_b64_tr_b16 v[144:145], v225 offset:49664\n" \
"v_mfma_f32_16x16x32_bf16 v[84:87], v[172:175], a[216:219], v[84:87]\n" \
"ds_read_b64_tr_b16 v[146:147], v225 offset:49920\n" \
"v_bfe_i32 v232, v232, 1, 1\n" \
"v_mfma_f32_16x16x32_bf16 v[116:119], v[168:171], a[204:207], v[116:119]\n" \
"s_mov_b32 s48, 49152\n" \
"s_mov_b32 m0, s43\n" \
"s_nop 0\n" \
"buffer_load_dwordx4 %[kd0], %[srdk], s48 offen offset:0 lds\n" \
"v_mfma_f32_16x16x32_bf16 v[116:119], v[172:175], a[220:223], v[116:119]\n" \
"ds_read_b64_tr_b16 v[148:149], v225 offset:57856\n" \
"v_mfma_f32_16x16x32_bf16 v[24:27], v[176:179], a[192:195], v[24:27]\n" \
"ds_read_b64_tr_b16 v[150:151], v225 offset:58112\n" \
"v_not_b32_e64 v232, v232\n" \
"v_mfma_f32_16x16x32_bf16 v[24:27], v[180:183], a[208:211], v[24:27]\n" \
"v_mfma_f32_16x16x32_bf16 v[56:59], v[176:179], a[196:199], v[56:59]\n" \
"ds_read_b64_tr_b16 v[152:153], v225 offset:49672\n" \
"v_mfma_f32_16x16x32_bf16 v[56:59], v[180:183], a[212:215], v[56:59]\n" \
"ds_read_b64_tr_b16 v[154:155], v225 offset:49928\n" \
"v_and_b32_e32 v230, 0x3f803f80, v232\n" \
"v_mfma_f32_16x16x32_bf16 v[88:91], v[176:179], a[200:203], v[88:91]\n" \
"buffer_load_dwordx4 v212, %[srdk], s48 offen offset:1024 lds\n" \
"v_mfma_f32_16x16x32_bf16 v[88:91], v[180:183], a[216:219], v[88:91]\n" \
"ds_read_b64_tr_b16 v[156:157], v225 offset:57864\n" \
"v_readlane_b32 s62, %[tlo], 18\n" \
"v_mfma_f32_16x16x32_bf16 v[120:123], v[176:179], a[204:207], v[120:123]\n" \
"ds_read_b64_tr_b16 v[158:159], v225 offset:58120\n" \
"v_mfma_f32_16x16x32_bf16 v[120:123], v[180:183], a[220:223], v[120:123]\n" \
"v_mfma_f32_16x16x32_bf16 v[28:31], v[184:187], a[192:195], v[28:31]\n" \
"buffer_load_dwordx4 v213, %[srdk], s48 offen offset:2048 lds\n" \
"v_readlane_b32 s61, %[tlo], 21\n" \
"v_mfma_f32_16x16x32_bf16 v[28:31], v[188:191], a[208:211], v[28:31]\n" \
"v_mfma_f32_16x16x32_bf16 v[60:63], v[184:187], a[196:199], v[60:63]\n" \
"v_mfma_f32_16x16x32_bf16 v[60:63], v[188:191], a[212:215], v[60:63]\n" \
"v_lshlrev_b32_e64 v224, 7, v231\n" \
"v_mfma_f32_16x16x32_bf16 v[92:95], v[184:187], a[200:203], v[92:95]\n" \
"buffer_load_dwordx4 v214, %[srdk], s48 offen offset:3072 lds\n" \
"v_mfma_f32_16x16x32_bf16 v[92:95], v[188:191], a[216:219], v[92:95]\n" \
"v_mfma_f32_16x16x32_bf16 v[124:127], v[184:187], a[204:207], v[124:127]\n" \
"v_mfma_f32_16x16x32_bf16 v[124:127], v[188:191], a[220:223], v[124:127]\n" \
"s_waitcnt vmcnt(24) lgkmcnt(0)\n" \
"v_mfma_f32_16x16x32_bf16 v[0:3], v[128:131], a[224:227], v[0:3]\n" \
"ds_read_b64_tr_b16 v[160:161], v225 offset:50176\n" \
"v_accvgpr_write_b32 a192, v230\n" \
"v_mfma_f32_16x16x32_bf16 v[0:3], v[132:135], a[240:243], v[0:3]\n" \
"ds_read_b64_tr_b16 v[162:163], v225 offset:50432\n" \
"v_mfma_f32_16x16x32_bf16 v[32:35], v[128:131], a[228:231], v[32:35]\n" \
"s_mov_b32 s48, 49152\n" \
"s_mov_b32 m0, s46\n" \
"s_nop 0\n" \
"buffer_load_dwordx4 %[vd0], %[srdv], s48 offen offset:2048 lds\n" \
"v_accvgpr_write_b32 a193, v230\n" \
"v_mfma_f32_16x16x32_bf16 v[32:35], v[132:135], a[244:247], v[32:35]\n" \
"ds_read_b64_tr_b16 v[164:165], v225 offset:58368\n" \
"v_mfma_f32_16x16x32_bf16 v[64:67], v[128:131], a[232:235], v[64:67]\n" \
"ds_read_b64_tr_b16 v[166:167], v225 offset:58624\n" \
"v_accvgpr_write_b32 a194, v230\n" \
"v_mfma_f32_16x16x32_bf16 v[64:67], v[132:135], a[248:251], v[64:67]\n" \
"v_mfma_f32_16x16x32_bf16 v[96:99], v[128:131], a[236:239], v[96:99]\n" \
"ds_read_b64_tr_b16 v[168:169], v225 offset:50184\n" \
"v_accvgpr_write_b32 a195, v230\n" \
"v_mfma_f32_16x16x32_bf16 v[96:99], v[132:135], a[252:255], v[96:99]\n" \
"ds_read_b64_tr_b16 v[170:171], v225 offset:50440\n" \
"v_mfma_f32_16x16x32_bf16 v[4:7], v[136:139], a[224:227], v[4:7]\n" \
"buffer_load_dwordx4 v215, %[srdv], s48 offen offset:3072 lds\n" \
"v_accvgpr_write_b32 a196, -1\n" \
"v_mfma_f32_16x16x32_bf16 v[4:7], v[140:143], a[240:243], v[4:7]\n" \
"ds_read_b64_tr_b16 v[172:173], v225 offset:58376\n" \
"v_mfma_f32_16x16x32_bf16 v[36:39], v[136:139], a[228:231], v[36:39]\n" \
"ds_read_b64_tr_b16 v[174:175], v225 offset:58632\n" \
"v_accvgpr_write_b32 a197, -1\n" \
"v_mfma_f32_16x16x32_bf16 v[36:39], v[140:143], a[244:247], v[36:39]\n" \
"v_mfma_f32_16x16x32_bf16 v[68:71], v[136:139], a[232:235], v[68:71]\n" \
"ds_read_b64_tr_b16 v[176:177], v225 offset:50688\n" \
"v_accvgpr_write_b32 a198, -1\n" \
"v_mfma_f32_16x16x32_bf16 v[68:71], v[140:143], a[248:251], v[68:71]\n" \
"ds_read_b64_tr_b16 v[178:179], v225 offset:50944\n" \
"v_mfma_f32_16x16x32_bf16 v[100:103], v[136:139], a[236:239], v[100:103]\n" \
"v_accvgpr_write_b32 a199, -1\n" \
"v_mfma_f32_16x16x32_bf16 v[100:103], v[140:143], a[252:255], v[100:103]\n" \
"ds_read_b64_tr_b16 v[180:181], v225 offset:58880\n" \
"v_mfma_f32_16x16x32_bf16 v[8:11], v[144:147], a[224:227], v[8:11]\n" \
"ds_read_b64_tr_b16 v[182:183], v225 offset:59136\n" \
"v_lshrrev_b32_e32 v231, 3, v224\n" \
"v_mfma_f32_16x16x32_bf16 v[8:11], v[148:151], a[240:243], v[8:11]\n" \
"v_mfma_f32_16x16x32_bf16 v[40:43], v[144:147], a[228:231], v[40:43]\n" \
"ds_read_b64_tr_b16 v[184:185], v225 offset:50696\n" \
"v_mov_b32_e64 v232, 0\n" \
"v_mfma_f32_16x16x32_bf16 v[40:43], v[148:151], a[244:247], v[40:43]\n" \
"ds_read_b64_tr_b16 v[186:187], v225 offset:50952\n" \
"v_mfma_f32_16x16x32_bf16 v[72:75], v[144:147], a[232:235], v[72:75]\n" \
"v_mov_b32_e64 v233, 0\n" \
"v_mfma_f32_16x16x32_bf16 v[72:75], v[148:151], a[248:251], v[72:75]\n" \
"ds_read_b64_tr_b16 v[188:189], v225 offset:58888\n" \
"v_mfma_f32_16x16x32_bf16 v[104:107], v[144:147], a[236:239], v[104:107]\n" \
"ds_read_b64_tr_b16 v[190:191], v225 offset:59144\n" \
"v_mov_b32_e64 v234, 0\n" \
"v_mfma_f32_16x16x32_bf16 v[104:107], v[148:151], a[252:255], v[104:107]\n" \
"v_mfma_f32_16x16x32_bf16 v[12:15], v[152:155], a[224:227], v[12:15]\n" \
"v_mov_b32_e64 v235, 0\n" \
"v_mfma_f32_16x16x32_bf16 v[12:15], v[156:159], a[240:243], v[12:15]\n" \
"v_mfma_f32_16x16x32_bf16 v[44:47], v[152:155], a[228:231], v[44:47]\n" \
"buffer_store_dwordx4 v[232:235], v231, %[srdz], s61 offen offset:0 sc1\n" \
"v_mfma_f32_16x16x32_bf16 v[44:47], v[156:159], a[244:247], v[44:47]\n" \
"v_mfma_f32_16x16x32_bf16 v[76:79], v[152:155], a[232:235], v[76:79]\n" \
"buffer_store_dwordx4 v[232:235], v231, %[srdz], s61 offen offset:1024 sc1\n" \
"v_mfma_f32_16x16x32_bf16 v[76:79], v[156:159], a[248:251], v[76:79]\n" \
"v_mfma_f32_16x16x32_bf16 v[108:111], v[152:155], a[236:239], v[108:111]\n" \
"v_mfma_f32_16x16x32_bf16 v[108:111], v[156:159], a[252:255], v[108:111]\n" \
"s_waitcnt vmcnt(22) lgkmcnt(0)\n" \
"s_barrier\n" \
"v_mfma_f32_16x16x32_bf16 v[16:19], v[160:163], a[224:227], v[16:19]\n" \
"ds_read_b128 a[64:67], %[kb0] offset:0\n" \
"v_mfma_f32_16x16x32_bf16 v[16:19], v[164:167], a[240:243], v[16:19]\n" \
"v_mfma_f32_16x16x32_bf16 v[20:23], v[168:171], a[224:227], v[20:23]\n" \
"v_cvt_pk_bf16_f32 v230, v0, v1\n" \
"v_cvt_pk_bf16_f32 v231, v2, v3\n" \
"v_cvt_pk_bf16_f32 v232, v4, v5\n" \
"ds_read_b128 a[68:71], v208 offset:0\n" \
"v_mfma_f32_16x16x32_bf16 v[20:23], v[172:175], a[240:243], v[20:23]\n" \
"v_cvt_pk_bf16_f32 v233, v6, v7\n" \
"v_accvgpr_write_b32 a0, v230\n" \
"v_accvgpr_write_b32 a1, v231\n" \
"v_mfma_f32_16x16x32_bf16 v[24:27], v[176:179], a[224:227], v[24:27]\n" \
"v_accvgpr_write_b32 a2, v232\n" \
"v_accvgpr_write_b32 a3, v233\n" \
"v_cvt_pk_bf16_f32 v234, v8, v9\n" \
"ds_read_b128 a[72:75], v209 offset:0\n" \
"v_mfma_f32_16x16x32_bf16 v[24:27], v[180:183], a[240:243], v[24:27]\n" \
"v_cvt_pk_bf16_f32 v235, v10, v11\n" \
"v_cvt_pk_bf16_f32 v236, v12, v13\n" \
"v_cvt_pk_bf16_f32 v237, v14, v15\n" \
"v_mfma_f32_16x16x32_bf16 v[28:31], v[184:187], a[224:227], v[28:31]\n" \
"v_accvgpr_write_b32 a4, v234\n" \
"v_accvgpr_write_b32 a5, v235\n" \
"v_accvgpr_write_b32 a6, v236\n" \
".if . - adaln_body != 8192\n" \
".error \"size model\"\n" \
".endif\n" \
"Lk1_%=:\n" \
"s_cbranch_execz Lk2_%=\n" \
"s_nop 0\n" \
"ds_read_b128 a[76:79], v210 offset:0\n" \
"v_mfma_f32_16x16x32_bf16 v[28:31], v[188:191], a[240:243], v[28:31]\n" \
"v_accvgpr_write_b32 a7, v237\n" \
"v_cvt_pk_bf16_f32 v230, v32, v33\n" \
"v_cvt_pk_bf16_f32 v231, v34, v35\n" \
"v_mfma_f32_16x16x32_bf16 v[48:51], v[160:163], a[228:231], v[48:51]\n" \
"v_cvt_pk_bf16_f32 v232, v36, v37\n" \
"v_cvt_pk_bf16_f32 v233, v38, v39\n" \
"v_accvgpr_write_b32 a16, v230\n" \
"ds_read_b128 a[80:83], %[kb0] offset:4096\n" \
"v_mfma_f32_16x16x32_bf16 v[48:51], v[164:167], a[244:247], v[48:51]\n" \
"v_accvgpr_write_b32 a17, v231\n" \
"v_accvgpr_write_b32 a18, v232\n" \
"v_accvgpr_write_b32 a19, v233\n" \
"v_mfma_f32_16x16x32_bf16 v[52:55], v[168:171], a[228:231], v[52:55]\n" \
"v_cvt_pk_bf16_f32 v234, v40, v41\n" \
"v_cvt_pk_bf16_f32 v235, v42, v43\n" \
"v_cvt_pk_bf16_f32 v236, v44, v45\n" \
"ds_read_b128 a[84:87], v208 offset:4096\n" \
"v_mfma_f32_16x16x32_bf16 v[52:55], v[172:175], a[244:247], v[52:55]\n" \
"v_cvt_pk_bf16_f32 v237, v46, v47\n" \
"v_accvgpr_write_b32 a20, v234\n" \
"v_accvgpr_write_b32 a21, v235\n" \
"v_mfma_f32_16x16x32_bf16 v[56:59], v[176:179], a[228:231], v[56:59]\n" \
"v_accvgpr_write_b32 a22, v236\n" \
"v_accvgpr_write_b32 a23, v237\n" \
"v_cvt_pk_bf16_f32 v230, v16, v17\n" \
"ds_read_b128 a[88:91], v209 offset:4096\n" \
"v_mfma_f32_16x16x32_bf16 v[56:59], v[180:183], a[244:247], v[56:59]\n" \
"v_cvt_pk_bf16_f32 v231, v18, v19\n" \
"v_cvt_pk_bf16_f32 v232, v20, v21\n" \
"v_cvt_pk_bf16_f32 v233, v22, v23\n" \
"v_mfma_f32_16x16x32_bf16 v[60:63], v[184:187], a[228:231], v[60:63]\n" \
"v_accvgpr_write_b32 a8, v230\n" \
"v_accvgpr_write_b32 a9, v231\n" \
"v_accvgpr_write_b32 a10, v232\n" \
"ds_read_b128 a[92:95], v210 offset:4096\n" \
"v_mfma_f32_16x16x32_bf16 v[60:63], v[188:191], a[244:247], v[60:63]\n" \
"v_accvgpr_write_b32 a11, v233\n" \
"v_cvt_pk_bf16_f32 v234, v24, v25\n" \
"v_cvt_pk_bf16_f32 v235, v26, v27\n" \
"v_mfma_f32_16x16x32_bf16 v[80:83], v[160:163], a[232:235], v[80:83]\n" \
"v_cvt_pk_bf16_f32 v236, v28, v29\n" \
"v_cvt_pk_bf16_f32 v237, v30, v31\n" \
"v_accvgpr_write_b32 a12, v234\n" \
"v_mfma_f32_16x16x32_bf16 v[80:83], v[164:167], a[248:251], v[80:83]\n" \
"v_accvgpr_write_b32 a13, v235\n" \
"v_accvgpr_write_b32 a14, v236\n" \
"v_accvgpr_write_b32 a15, v237\n" \
"v_mfma_f32_16x16x32_bf16 v[84:87], v[168:171], a[232:235], v[84:87]\n" \
"v_cvt_pk_bf16_f32 v230, v64, v65\n" \
"v_cvt_pk_bf16_f32 v231, v66, v67\n" \
"v_cvt_pk_bf16_f32 v232, v68, v69\n" \
"v_mfma_f32_16x16x32_bf16 v[84:87], v[172:175], a[248:251], v[84:87]\n" \
"v_cvt_pk_bf16_f32 v233, v70, v71\n" \
"v_accvgpr_write_b32 a32, v230\n" \
"v_accvgpr_write_b32 a33, v231\n" \
"v_mfma_f32_16x16x32_bf16 v[88:91], v[176:179], a[232:235], v[88:91]\n" \
"v_accvgpr_write_b32 a34, v232\n" \
"v_accvgpr_write_b32 a35, v233\n" \
"v_cvt_pk_bf16_f32 v234, v72, v73\n" \
"v_mfma_f32_16x16x32_bf16 v[88:91], v[180:183], a[248:251], v[88:91]\n" \
"v_cvt_pk_bf16_f32 v235, v74, v75\n" \
"v_cvt_pk_bf16_f32 v236, v76, v77\n" \
"v_cvt_pk_bf16_f32 v237, v78, v79\n" \
"v_mfma_f32_16x16x32_bf16 v[92:95], v[184:187], a[232:235], v[92:95]\n" \
"v_accvgpr_write_b32 a36, v234\n" \
"v_accvgpr_write_b32 a37, v235\n" \
"v_accvgpr_write_b32 a38, v236\n" \
"v_mfma_f32_16x16x32_bf16 v[92:95], v[188:191], a[248:251], v[92:95]\n" \
"v_accvgpr_write_b32 a39, v237\n" \
"v_cvt_pk_bf16_f32 v230, v48, v49\n" \
"v_cvt_pk_bf16_f32 v231, v50, v51\n" \
"v_mfma_f32_16x16x32_bf16 v[112:115], v[160:163], a[236:239], v[112:115]\n" \
"v_cvt_pk_bf16_f32 v232, v52, v53\n" \
"v_cvt_pk_bf16_f32 v233, v54, v55\n" \
"v_accvgpr_write_b32 a24, v230\n" \
"v_mfma_f32_16x16x32_bf16 v[112:115], v[164:167], a[252:255], v[112:115]\n" \
"v_accvgpr_write_b32 a25, v231\n" \
"v_accvgpr_write_b32 a26, v232\n" \
"v_accvgpr_write_b32 a27, v233\n" \
"v_mfma_f32_16x16x32_bf16 v[116:119], v[168:171], a[236:239], v[116:119]\n" \
"v_cvt_pk_bf16_f32 v234, v56, v57\n" \
"v_cvt_pk_bf16_f32 v235, v58, v59\n" \
"v_cvt_pk_bf16_f32 v236, v60, v61\n" \
"v_mfma_f32_16x16x32_bf16 v[116:119], v[172:175], a[252:255], v[116:119]\n" \
"v_cvt_pk_bf16_f32 v237, v62, v63\n" \
"v_accvgpr_write_b32 a28, v234\n" \
"v_accvgpr_write_b32 a29, v235\n" \
"v_mfma_f32_16x16x32_bf16 v[120:123], v[176:179], a[236:239], v[120:123]\n" \
"v_accvgpr_write_b32 a30, v236\n" \
"v_accvgpr_write_b32 a31, v237\n" \
"v_cvt_pk_bf16_f32 v230, v96, v97\n" \
"v_mfma_f32_16x16x32_bf16 v[120:123], v[180:183], a[252:255], v[120:123]\n" \
"v_cvt_pk_bf16_f32 v231, v98, v99\n" \
"v_cvt_pk_bf16_f32 v232, v100, v101\n" \
"v_cvt_pk_bf16_f32 v233, v102, v103\n" \
"v_mfma_f32_16x16x32_bf16 v[124:127], v[184:187], a[236:239], v[124:127]\n" \
"v_accvgpr_write_b32 a48, v230\n" \
"v_accvgpr_write_b32 a49, v231\n" \
"v_accvgpr_write_b32 a50, v232\n" \
"v_mfma_f32_16x16x32_bf16 v[124:127], v[188:191], a[252:255], v[124:127]\n" \
"v_accvgpr_write_b32 a51, v233\n" \
"s_waitcnt vmcnt(63) lgkmcnt(0)\n" \
"s_nop 0\n" \
"s_mov_b32 s48, 65536\n" \
"s_mov_b32 s60, 0\n" \
"s_waitcnt lgkmcnt(0)\n" \
"v_mfma_f32_16x16x32_bf16 v[128:131], a[64:67], a[0:3], 0\n" \
"ds_read_b128 a[96:99], %[kb0] offset:8192\n" \
"v_cvt_pk_bf16_f32 v234, v80, v81\n" \
"v_cvt_pk_bf16_f32 v235, v82, v83\n" \
"v_mfma_f32_16x16x32_bf16 v[128:131], a[68:71], a[4:7], v[128:131]\n" \
"ds_read_b128 a[100:103], v208 offset:8192\n" \
"v_cvt_pk_bf16_f32 v236, v84, v85\n" \
"v_cvt_pk_bf16_f32 v237, v86, v87\n" \
"v_mfma_f32_16x16x32_bf16 v[128:131], a[72:75], a[8:11], v[128:131]\n" \
"ds_read_b128 a[104:107], v209 offset:8192\n" \
"v_accvgpr_write_b32 a40, v234\n" \
"v_accvgpr_write_b32 a41, v235\n" \
"v_mfma_f32_16x16x32_bf16 v[128:131], a[76:79], a[12:15], v[128:131]\n" \
"ds_read_b128 a[108:111], v210 offset:8192\n" \
"v_accvgpr_write_b32 a42, v236\n" \
"v_accvgpr_write_b32 a43, v237\n" \
"v_mfma_f32_16x16x32_bf16 v[132:135], a[80:83], a[0:3], 0\n" \
"ds_read_b128 a[112:115], %[kb0] offset:12288\n" \
"v_cvt_pk_bf16_f32 v230, v88, v89\n" \
"v_cvt_pk_bf16_f32 v231, v90, v91\n" \
"v_mfma_f32_16x16x32_bf16 v[132:135], a[84:87], a[4:7], v[132:135]\n" \
"ds_read_b128 a[116:119], v208 offset:12288\n" \
"v_cvt_pk_bf16_f32 v232, v92, v93\n" \
"v_cvt_pk_bf16_f32 v233, v94, v95\n" \
"v_mfma_f32_16x16x32_bf16 v[132:135], a[88:91], a[8:11], v[132:135]\n" \
"ds_read_b128 a[120:123], v209 offset:12288\n" \
"v_accvgpr_write_b32 a44, v230\n" \
"v_accvgpr_write_b32 a45, v231\n" \
"v_mfma_f32_16x16x32_bf16 v[132:135], a[92:95], a[12:15], v[132:135]\n" \
"ds_read_b128 a[124:127], v210 offset:12288\n" \
"v_accvgpr_write_b32 a46, v232\n" \
"v_accvgpr_write_b32 a47, v233\n" \
"v_mfma_f32_16x16x32_bf16 v[136:139], a[64:67], a[16:19], 0\n" \
"ds_read_b64_tr_b16 a[128:129], %[vvb] offset:0\n" \
"v_cvt_pk_bf16_f32 v234, v104, v105\n" \
"v_cvt_pk_bf16_f32 v235, v106, v107\n" \
"v_mfma_f32_16x16x32_bf16 v[136:139], a[68:71], a[20:23], v[136:139]\n" \
"ds_read_b64_tr_b16 a[130:131], %[vvb] offset:4096\n" \
"v_cvt_pk_bf16_f32 v236, v108, v109\n" \
"v_cvt_pk_bf16_f32 v237, v110, v111\n" \
"v_mfma_f32_16x16x32_bf16 v[136:139], a[72:75], a[24:27], v[136:139]\n" \
"ds_read_b64_tr_b16 a[132:133], v211 offset:0\n" \
"v_accvgpr_write_b32 a52, v234\n" \
"v_accvgpr_write_b32 a53, v235\n" \
"v_mfma_f32_16x16x32_bf16 v[136:139], a[76:79], a[28:31], v[136:139]\n" \
"ds_read_b64_tr_b16 a[134:135], v211 offset:4096\n" \
"v_accvgpr_write_b32 a54, v236\n" \
"v_accvgpr_write_b32 a55, v237\n" \
"v_mfma_f32_16x16x32_bf16 v[140:143], a[80:83], a[16:19], 0\n" \
"ds_read_b64_tr_b16 a[136:137], %[vvb] offset:512\n" \
"v_cvt_pk_bf16_f32 v230, v112, v113\n" \
"v_cvt_pk_bf16_f32 v231, v114, v115\n" \
"v_mfma_f32_16x16x32_bf16 v[140:143], a[84:87], a[20:23], v[140:143]\n" \
"ds_read_b64_tr_b16 a[138:139], %[vvb] offset:4608\n" \
"v_cvt_pk_bf16_f32 v232, v116, v117\n" \
"v_cvt_pk_bf16_f32 v233, v118, v119\n" \
"v_mfma_f32_16x16x32_bf16 v[140:143], a[88:91], a[24:27], v[140:143]\n" \
"ds_read_b64_tr_b16 a[140:141], v211 offset:512\n" \
"v_accvgpr_write_b32 a56, v230\n" \
"v_accvgpr_write_b32 a57, v231\n" \
"v_mfma_f32_16x16x32_bf16 v[140:143], a[92:95], a[28:31], v[140:143]\n" \
"ds_read_b64_tr_b16 a[142:143], v211 offset:4608\n" \
"v_accvgpr_write_b32 a58, v232\n" \
"v_accvgpr_write_b32 a59, v233\n" \
"v_mfma_f32_16x16x32_bf16 v[144:147], a[64:67], a[32:35], 0\n" \
"ds_read_b64_tr_b16 a[144:145], %[vvb] offset:1024\n" \
"v_cvt_pk_bf16_f32 v234, v120, v121\n" \
"v_cvt_pk_bf16_f32 v235, v122, v123\n" \
"v_mfma_f32_16x16x32_bf16 v[144:147], a[68:71], a[36:39], v[144:147]\n" \
"ds_read_b64_tr_b16 a[146:147], %[vvb] offset:5120\n" \
"v_cvt_pk_bf16_f32 v236, v124, v125\n" \
"v_cvt_pk_bf16_f32 v237, v126, v127\n" \
"v_mfma_f32_16x16x32_bf16 v[144:147], a[72:75], a[40:43], v[144:147]\n" \
"ds_read_b64_tr_b16 a[148:149], v211 offset:1024\n" \
"v_accvgpr_write_b32 a60, v234\n" \
"v_accvgpr_write_b32 a61, v235\n" \
"v_mfma_f32_16x16x32_bf16 v[144:147], a[76:79], a[44:47], v[144:147]\n" \
"ds_read_b64_tr_b16 a[150:151], v211 offset:5120\n" \
"v_accvgpr_write_b32 a62, v236\n" \
"v_accvgpr_write_b32 a63, v237\n" \
"v_mfma_f32_16x16x32_bf16 v[148:151], a[80:83], a[32:35], 0\n" \
"ds_read_b64_tr_b16 a[152:153], %[vvb] offset:1536\n" \
"v_max3_f32 v192, v128, v129, v130\n" \
"v_max3_f32 v193, v131, v132, v133\n" \
"v_mfma_f32_16x16x32_bf16 v[148:151], a[84:87], a[36:39], v[148:151]\n" \
"ds_read_b64_tr_b16 a[154:155], %[vvb] offset:5632\n" \
"v_max3_f32 v192, v192, v134, v135\n" \
"v_max_f32_e64 v225, v192, v193\n" \
"v_mfma_f32_16x16x32_bf16 v[148:151], a[88:91], a[40:43], v[148:151]\n" \
"ds_read_b64_tr_b16 a[156:157], v211 offset:1536\n" \
"v_max3_f32 v192, v136, v137, v138\n" \
"v_max3_f32 v193, v139, v140, v141\n" \
"v_mfma_f32_16x16x32_bf16 v[148:151], a[92:95], a[44:47], v[148:151]\n" \
"ds_read_b64_tr_b16 a[158:159], v211 offset:5632\n" \
"v_max3_f32 v192, v192, v142, v143\n" \
"v_max_f32_e64 v226, v192, v193\n" \
"v_mfma_f32_16x16x32_bf16 v[152:155], a[64:67], a[48:51], 0\n" \
"ds_read_b128 a[200:203], v228 offset:0\n" \
"s_mov_b32 s44, %[m0t]\n" \
"s_mov_b32 s45, 0\n" \
"v_mfma_f32_16x16x32_bf16 v[152:155], a[68:71], a[52:55], v[152:155]\n" \
"ds_read_b128 a[204:207], v228 offset:64\n" \
"v_mfma_f32_16x16x32_bf16 v[152:155], a[72:75], a[56:59], v[152:155]\n" \
"ds_read_b128 a[208:211], v228 offset:128\n" \
"v_mfma_f32_16x16x32_bf16 v[152:155], a[76:79], a[60:63], v[152:155]\n" \
"ds_read_b128 a[212:215], v228 offset:192\n" \
"v_mfma_f32_16x16x32_bf16 v[156:159], a[80:83], a[48:51], 0\n" \
"ds_read_b128 a[216:219], v228 offset:256\n" \
"v_mfma_f32_16x16x32_bf16 v[156:159], a[84:87], a[52:55], v[156:159]\n" \
"ds_read_b128 a[220:223], v228 offset:320\n" \
"v_mfma_f32_16x16x32_bf16 v[156:159], a[88:91], a[56:59], v[156:159]\n" \
"ds_read_b128 a[224:227], v228 offset:384\n" \
"v_max3_f32 v192, v144, v145, v146\n" \
"v_max3_f32 v193, v147, v148, v149\n" \
"v_mfma_f32_16x16x32_bf16 v[156:159], a[92:95], a[60:63], v[156:159]\n" \
"ds_read_b128 a[228:231], v228 offset:448\n" \
"v_max3_f32 v192, v192, v150, v151\n" \
"v_max_f32_e64 v227, v192, v193\n" \
"s_nop 3\n" \
"s_nop 0\n" \
"v_max3_f32 v230, v152, v153, v154\n" \
"v_max3_f32 v231, v155, v156, v157\n" \
"v_max3_f32 v230, v230, v158, v159\n" \
"v_max_f32_e32 v228, v230, v231\n" \
"v_mov_b32_e32 v232, v225\n" \
"v_mov_b32_e32 v233, v226\n" \
"v_mov_b32_e32 v234, v227\n" \
"v_mov_b32_e32 v235, v228\n" \
"s_nop 0\n" \
"v_permlane16_swap_b32_e32 v225, v232\n" \
"v_permlane16_swap_b32_e32 v226, v233\n" \
"v_permlane16_swap_b32_e32 v227, v234\n" \
"v_permlane16_swap_b32_e32 v228, v235\n" \
"v_max_f32_e32 v225, v225, v232\n" \
"v_max_f32_e32 v226, v226, v233\n" \
"v_max_f32_e32 v227, v227, v234\n" \
"v_max_f32_e32 v228, v228, v235\n" \
"v_mov_b32_e32 v232, v225\n" \
"v_mov_b32_e32 v233, v226\n" \
"v_mov_b32_e32 v234, v227\n" \
"v_mov_b32_e32 v235, v228\n" \
"s_nop 0\n" \
"v_permlane32_swap_b32_e32 v225, v232\n" \
"v_permlane32_swap_b32_e32 v226, v233\n" \
"v_permlane32_swap_b32_e32 v227, v234\n" \
"v_permlane32_swap_b32_e32 v228, v235\n" \
"v_max_f32_e32 v225, v225, v232\n" \
"v_max_f32_e32 v226, v226, v233\n" \
"v_max_f32_e32 v227, v227, v234\n" \
"v_max_f32_e64 v228, v228, v235\n" \
"Lhavem_%=:\n" \
"s_nop 3\n" \
"v_mov_b64_e32 v[176:177], 0\n" \
"v_mov_b64_e32 v[178:179], 0\n" \
"v_mov_b64_e32 v[180:181], 0\n" \
"v_mov_b64_e32 v[182:183], 0\n" \
"v_sub_f32_e32 v160, 0, v225\n" \
"v_sub_f32_e32 v161, 0, v225\n" \
"v_sub_f32_e32 v162, 0, v225\n" \
"v_sub_f32_e32 v163, 0, v225\n" \
"v_sub_f32_e32 v164, 0, v226\n" \
"v_sub_f32_e32 v165, 0, v226\n" \
"v_sub_f32_e32 v166, 0, v226\n" \
"v_sub_f32_e32 v167, 0, v226\n" \
"v_sub_f32_e32 v168, 0, v227\n" \
"v_sub_f32_e32 v169, 0, v227\n" \
"v_sub_f32_e32 v170, 0, v227\n" \
"v_sub_f32_e32 v171, 0, v227\n" \
"v_sub_f32_e32 v172, 0, v228\n" \
"v_sub_f32_e32 v173, 0, v228\n" \
"v_sub_f32_e32 v174, 0, v228\n" \
"v_sub_f32_e32 v175, 0, v228\n" \
"v_sub_f32_e32 v128, v128, v225\n" \
"v_sub_f32_e32 v129, v129, v225\n" \
"v_sub_f32_e32 v130, v130, v225\n" \
"v_sub_f32_e32 v131, v131, v225\n" \
"v_sub_f32_e32 v132, v132, v225\n" \
"v_sub_f32_e32 v133, v133, v225\n" \
"v_sub_f32_e32 v134, v134, v225\n" \
"v_sub_f32_e32 v135, v135, v225\n" \
"v_sub_f32_e32 v136, v136, v226\n" \
"v_sub_f32_e32 v137, v137, v226\n" \
"v_sub_f32_e32 v138, v138, v226\n" \
"v_sub_f32_e32 v139, v139, v226\n" \
"v_sub_f32_e32 v140, v140, v226\n" \
"v_sub_f32_e32 v141, v141, v226\n" \
"v_sub_f32_e32 v142, v142, v226\n" \
"v_sub_f32_e32 v143, v143, v226\n" \
"v_sub_f32_e32 v144, v144, v227\n" \
"v_sub_f32_e32 v145, v145, v227\n" \
"v_sub_f32_e32 v146, v146, v227\n" \
"v_sub_f32_e32 v147, v147, v227\n" \
"v_sub_f32_e32 v148, v148, v227\n" \
"v_sub_f32_e32 v149, v149, v227\n" \
"v_sub_f32_e32 v150, v150, v227\n" \
"v_sub_f32_e32 v151, v151, v227\n" \
"v_sub_f32_e32 v152, v152, v228\n" \
"v_sub_f32_e32 v153, v153, v228\n" \
"v_sub_f32_e32 v154, v154, v228\n" \
"v_sub_f32_e32 v155, v155, v228\n" \
"v_sub_f32_e32 v156, v156, v228\n" \
"v_sub_f32_e32 v157, v157, v228\n" \
"v_sub_f32_e32 v158, v158, v228\n" \
"v_sub_f32_e32 v159, v159, v228\n" \
"v_exp_f32_e32 v128, v128\n" \
"v_exp_f32_e32 v129, v129\n" \
"v_exp_f32_e32 v130, v130\n" \
"v_exp_f32_e32 v131, v131\n" \
"v_exp_f32_e32 v132, v132\n" \
"v_exp_f32_e32 v133, v133\n" \
"v_exp_f32_e32 v134, v134\n" \
"v_exp_f32_e32 v135, v135\n" \
"v_exp_f32_e32 v136, v136\n" \
"v_exp_f32_e32 v137, v137\n" \
"v_exp_f32_e32 v138, v138\n" \
"v_exp_f32_e32 v139, v139\n" \
"v_exp_f32_e32 v140, v140\n" \
"v_exp_f32_e32 v141, v141\n" \
"v_exp_f32_e32 v142, v142\n" \
"v_exp_f32_e32 v143, v143\n" \
"v_exp_f32_e32 v144, v144\n" \
"v_exp_f32_e32 v145, v145\n" \
"v_exp_f32_e32 v146, v146\n" \
"v_exp_f32_e32 v147, v147\n" \
"v_exp_f32_e32 v148, v148\n" \
"v_exp_f32_e32 v149, v149\n" \
"v_exp_f32_e32 v150, v150\n" \
"v_exp_f32_e32 v151, v151\n" \
"v_exp_f32_e32 v152, v152\n" \
"v_exp_f32_e32 v153, v153\n" \
"v_exp_f32_e32 v154, v154\n" \
"v_exp_f32_e64 v155, v155\n" \
"v_bfe_i32 v228, v224, 7, 1\n" \
"v_and_b32_e32 v228, 0xaaaa, v228\n" \
"v_or_b32_e32 v228, 0x4444, v228\n" \
"v_cvt_pk_bf16_f32 v184, v128, v129\n" \
"v_cvt_pk_bf16_f32 v185, v130, v131\n" \
"v_cvt_pk_bf16_f32 v186, v132, v133\n" \
"v_cvt_pk_bf16_f32 v187, v134, v135\n" \
"v_cvt_pk_bf16_f32 v188, v136, v137\n" \
"v_cvt_pk_bf16_f32 v189, v138, v139\n" \
"v_cvt_pk_bf16_f32 v190, v140, v141\n" \
"v_cvt_pk_bf16_f32 v191, v142, v143\n" \
"v_cvt_pk_bf16_f32 v216, v144, v145\n" \
"v_cvt_pk_bf16_f32 v217, v146, v147\n" \
"v_cvt_pk_bf16_f32 v218, v148, v149\n" \
"v_cvt_pk_bf16_f32 v219, v150, v151\n" \
"v_lshrrev_b32_e64 v235, 3, v224\n" \
"s_waitcnt vmcnt(2) lgkmcnt(0)\n" \
"s_barrier\n" \
"v_mfma_f32_16x16x32_bf16 v[128:131], a[96:99], a[0:3], v[160:163]\n" \
"v_exp_f32_e64 v156, v156\n" \
"v_mfma_f32_16x16x32_bf16 v[128:131], a[100:103], a[4:7], v[128:131]\n" \
"s_add_u32 m0, %[m0b], 114688\n" \
"v_mfma_f32_16x16x32_bf16 v[128:131], a[104:107], a[8:11], v[128:131]\n" \
"v_exp_f32_e64 v157, v157\n" \
"v_mfma_f32_16x16x32_bf16 v[128:131], a[108:111], a[12:15], v[128:131]\n" \
"v_cvt_pk_bf16_f32 v220, v152, v153\n" \
"v_cvt_pk_bf16_f32 v221, v154, v155\n" \
"v_mfma_f32_16x16x32_bf16 v[132:135], a[112:115], a[0:3], v[160:163]\n" \
"v_exp_f32_e64 v158, v158\n" \
"v_mfma_f32_16x16x32_bf16 v[132:135], a[116:119], a[4:7], v[132:135]\n" \
"v_mfma_f32_16x16x32_bf16 v[132:135], a[120:123], a[8:11], v[132:135]\n" \
"buffer_load_dwordx4 %[vd0], %[srdv], s48 offen offset:0 lds\n" \
"v_mfma_f32_16x16x32_bf16 v[132:135], a[124:127], a[12:15], v[132:135]\n" \
"v_exp_f32_e64 v159, v159\n" \
"v_mfma_f32_16x16x32_bf16 v[0:3], a[128:131], v[184:187], 0\n" \
"ds_read_b128 a[64:67], %[kb0] offset:16384\n" \
"v_mfma_f32_16x16x32_bf16 v[32:35], a[128:131], v[188:191], 0\n" \
"v_exp_f32_e64 v128, v128\n" \
"v_mfma_f32_16x16x32_bf16 v[4:7], a[132:135], v[184:187], 0\n" \
"v_cvt_pk_bf16_f32 v222, v156, v157\n" \
"v_cvt_pk_bf16_f32 v223, v158, v159\n" \
"v_mfma_f32_16x16x32_bf16 v[36:39], a[132:135], v[188:191], 0\n" \
"v_exp_f32_e64 v129, v129\n" \
"v_mfma_f32_16x16x32_bf16 v[8:11], a[136:139], v[184:187], 0\n" \
"ds_read_b128 a[68:71], v208 offset:16384\n" \
"v_mfma_f32_16x16x32_bf16 v[40:43], a[136:139], v[188:191], 0\n" \
"v_exp_f32_e64 v130, v130\n" \
"v_mfma_f32_16x16x32_bf16 v[12:15], a[140:143], v[184:187], 0\n" \
"ds_read_b128 a[72:75], v209 offset:16384\n" \
"v_mfma_f32_16x16x32_bf16 v[44:47], a[140:143], v[188:191], 0\n" \
"v_exp_f32_e64 v131, v131\n" \
"v_smfmac_f32_16x16x64_bf16 v[176:179], a[192:195], v[184:191], v228\n" \
"v_mfma_f32_16x16x32_bf16 v[136:139], a[96:99], a[16:19], v[164:167]\n" \
"v_exp_f32_e64 v132, v132\n" \
"v_mfma_f32_16x16x32_bf16 v[136:139], a[100:103], a[20:23], v[136:139]\n" \
"ds_read_b128 a[76:79], v210 offset:16384\n" \
"v_mfma_f32_16x16x32_bf16 v[136:139], a[104:107], a[24:27], v[136:139]\n" \
"v_exp_f32_e64 v133, v133\n" \
"v_mfma_f32_16x16x32_bf16 v[136:139], a[108:111], a[28:31], v[136:139]\n" \
"v_cvt_pk_bf16_f32 v192, v128, v129\n" \
"v_cvt_pk_bf16_f32 v193, v130, v131\n" \
"v_mfma_f32_16x16x32_bf16 v[140:143], a[112:115], a[16:19], v[164:167]\n" \
"v_exp_f32_e64 v134, v134\n" \
"v_mfma_f32_16x16x32_bf16 v[140:143], a[116:119], a[20:23], v[140:143]\n" \
"ds_read_b128 a[80:83], %[kb0] offset:20480\n" \
"v_mfma_f32_16x16x32_bf16 v[140:143], a[120:123], a[24:27], v[140:143]\n" \
"buffer_load_dwordx4 v215, %[srdv], s48 offen offset:1024 lds\n" \
"v_mfma_f32_16x16x32_bf16 v[140:143], a[124:127], a[28:31], v[140:143]\n" \
"v_exp_f32_e64 v135, v135\n" \
"v_mfma_f32_16x16x32_bf16 v[16:19], a[144:147], v[184:187], 0\n" \
"ds_read_b128 a[84:87], v208 offset:20480\n" \
"v_mfma_f32_16x16x32_bf16 v[48:51], a[144:147], v[188:191], 0\n" \
"v_exp_f32_e64 v136, v136\n" \
"v_mfma_f32_16x16x32_bf16 v[20:23], a[148:151], v[184:187], 0\n" \
"v_cvt_pk_bf16_f32 v194, v132, v133\n" \
"v_cvt_pk_bf16_f32 v195, v134, v135\n" \
"v_mfma_f32_16x16x32_bf16 v[52:55], a[148:151], v[188:191], 0\n" \
"v_exp_f32_e64 v137, v137\n" \
"v_mfma_f32_16x16x32_bf16 v[24:27], a[152:155], v[184:187], 0\n" \
"v_exp_f32_e64 v138, v138\n" \
"v_mfma_f32_16x16x32_bf16 v[56:59], a[152:155], v[188:191], 0\n" \
"ds_read_b128 a[88:91], v209 offset:20480\n" \
"v_mfma_f32_16x16x32_bf16 v[28:31], a[156:159], v[184:187], 0\n" \
"v_exp_f32_e64 v139, v139\n" \
"v_mfma_f32_16x16x32_bf16 v[60:63], a[156:159], v[188:191], 0\n" \
"v_mfma_f32_16x16x32_bf16 v[144:147], a[96:99], a[32:35], v[168:171]\n" \
"v_exp_f32_e64 v140, v140\n" \
"v_mfma_f32_16x16x32_bf16 v[144:147], a[100:103], a[36:39], v[144:147]\n" \
"ds_read_b128 a[92:95], v210 offset:20480\n" \
"v_mfma_f32_16x16x32_bf16 v[144:147], a[104:107], a[40:43], v[144:147]\n" \
"v_exp_f32_e64 v141, v141\n" \
"v_mfma_f32_16x16x32_bf16 v[144:147], a[108:111], a[44:47], v[144:147]\n" \
"v_cvt_pk_bf16_f32 v196, v136, v137\n" \
"v_cvt_pk_bf16_f32 v197, v138, v139\n" \
"v_mfma_f32_16x16x32_bf16 v[148:151], a[112:115], a[32:35], v[168:171]\n" \
"v_exp_f32_e64 v142, v142\n" \
"v_mfma_f32_16x16x32_bf16 v[148:151], a[116:119], a[36:39], v[148:151]\n" \
"v_mfma_f32_16x16x32_bf16 v[148:151], a[120:123], a[40:43], v[148:151]\n" \
"buffer_load_dwordx4 %[vd0], %[srdv], s48 offen offset:2048 lds\n" \
"v_mfma_f32_16x16x32_bf16 v[148:151], a[124:127], a[44:47], v[148:151]\n" \
"v_exp_f32_e64 v143, v143\n" \
"v_mfma_f32_16x16x32_bf16 v[64:67], a[128:131], v[216:219], 0\n" \
"ds_read_b64_tr_b16 a[160:161], %[vvb] offset:8192\n" \
"v_mfma_f32_16x16x32_bf16 v[96:99], a[128:131], v[220:223], 0\n" \
"v_exp_f32_e64 v144, v144\n" \
"v_mfma_f32_16x16x32_bf16 v[68:71], a[132:135], v[216:219], 0\n" \
"v_cvt_pk_bf16_f32 v198, v140, v141\n" \
"v_cvt_pk_bf16_f32 v199, v142, v143\n" \
"v_mfma_f32_16x16x32_bf16 v[100:103], a[132:135], v[220:223], 0\n" \
"v_exp_f32_e64 v145, v145\n" \
"v_mfma_f32_16x16x32_bf16 v[72:75], a[136:139], v[216:219], 0\n" \
"ds_read_b64_tr_b16 a[162:163], %[vvb] offset:12288\n" \
"ds_read_b64_tr_b16 a[164:165], v211 offset:8192\n" \
"v_mfma_f32_16x16x32_bf16 v[104:107], a[136:139], v[220:223], 0\n" \
"v_exp_f32_e64 v146, v146\n" \
"v_mfma_f32_16x16x32_bf16 v[76:79], a[140:143], v[216:219], 0\n" \
"ds_read_b64_tr_b16 a[166:167], v211 offset:12288\n" \
"v_mfma_f32_16x16x32_bf16 v[108:111], a[140:143], v[220:223], 0\n" \
"v_exp_f32_e64 v147, v147\n" \
"v_smfmac_f32_16x16x64_bf16 v[180:183], a[192:195], v[216:223], v228\n" \
"ds_read_b64_tr_b16 a[168:169], %[vvb] offset:8704\n" \
"v_mfma_f32_16x16x32_bf16 v[152:155], a[96:99], a[48:51], v[172:175]\n" \
"v_exp_f32_e64 v148, v148\n" \
"v_mfma_f32_16x16x32_bf16 v[152:155], a[100:103], a[52:55], v[152:155]\n" \
"ds_read_b64_tr_b16 a[170:171], %[vvb] offset:12800\n" \
"v_mfma_f32_16x16x32_bf16 v[152:155], a[104:107], a[56:59], v[152:155]\n" \
"v_exp_f32_e64 v149, v149\n" \
"v_mfma_f32_16x16x32_bf16 v[152:155], a[108:111], a[60:63], v[152:155]\n" \
"v_cvt_pk_bf16_f32 v200, v144, v145\n" \
"v_cvt_pk_bf16_f32 v201, v146, v147\n" \
"v_mfma_f32_16x16x32_bf16 v[156:159], a[112:115], a[48:51], v[172:175]\n" \
"v_exp_f32_e64 v150, v150\n" \
"v_mfma_f32_16x16x32_bf16 v[156:159], a[116:119], a[52:55], v[156:159]\n" \
"ds_read_b64_tr_b16 a[172:173], v211 offset:8704\n" \
"v_mfma_f32_16x16x32_bf16 v[156:159], a[120:123], a[56:59], v[156:159]\n" \
"buffer_load_dwordx4 v215, %[srdv], s48 offen offset:3072 lds\n" \
"v_mfma_f32_16x16x32_bf16 v[156:159], a[124:127], a[60:63], v[156:159]\n" \
"v_exp_f32_e64 v151, v151\n" \
"v_mfma_f32_16x16x32_bf16 v[80:83], a[144:147], v[216:219], 0\n" \
"ds_read_b64_tr_b16 a[174:175], v211 offset:12800\n" \
"ds_read_b64_tr_b16 a[176:177], %[vvb] offset:9216\n" \
"v_mfma_f32_16x16x32_bf16 v[112:115], a[144:147], v[220:223], 0\n" \
"v_exp_f32_e64 v152, v152\n" \
"v_mfma_f32_16x16x32_bf16 v[84:87], a[148:151], v[216:219], 0\n" \
"v_cvt_pk_bf16_f32 v202, v148, v149\n" \
"v_cvt_pk_bf16_f32 v203, v150, v151\n" \
"v_mfma_f32_16x16x32_bf16 v[116:119], a[148:151], v[220:223], 0\n" \
"v_exp_f32_e64 v153, v153\n" \
"v_mfma_f32_16x16x32_bf16 v[88:91], a[152:155], v[216:219], 0\n" \
"v_exp_f32_e64 v154, v154\n" \
"v_mfma_f32_16x16x32_bf16 v[120:123], a[152:155], v[220:223], 0\n" \
"ds_read_b64_tr_b16 a[178:179], %[vvb] offset:13312\n" \
"v_mfma_f32_16x16x32_bf16 v[92:95], a[156:159], v[216:219], 0\n" \
"v_exp_f32_e64 v155, v155\n" \
"v_mov_b64_e32 v[216:217], 0\n" \
"v_mov_b64_e32 v[218:219], 0\n" \
"v_mfma_f32_16x16x32_bf16 v[124:127], a[156:159], v[220:223], 0\n" \
"ds_read_b64_tr_b16 a[180:181], v211 offset:9216\n" \
"s_waitcnt lgkmcnt(13)\n" \
"s_nop 0\n" \
"LmA_%=:\n" \
"v_mfma_f32_16x16x32_bf16 v[128:131], a[64:67], a[0:3], v[160:163]\n" \
"v_exp_f32_e64 v156, v156\n" \
"v_mfma_f32_16x16x32_bf16 v[128:131], a[68:71], a[4:7], v[128:131]\n" \
"ds_read_b64_tr_b16 a[182:183], v211 offset:13312\n" \
"s_add_u32 m0, %[m0b], 0\n" \
"v_mfma_f32_16x16x32_bf16 v[128:131], a[72:75], a[8:11], v[128:131]\n" \
"v_exp_f32_e32 v157, v157\n" \
"v_mfma_f32_16x16x32_bf16 v[128:131], a[76:79], a[12:15], v[128:131]\n" \
"v_cvt_pk_bf16_f32 v204, v152, v153\n" \
"v_cvt_pk_bf16_f32 v205, v154, v155\n" \
"v_mfma_f32_16x16x32_bf16 v[132:135], a[80:83], a[0:3], v[160:163]\n" \
"v_exp_f32_e64 v158, v158\n" \
"v_mfma_f32_16x16x32_bf16 v[132:135], a[84:87], a[4:7], v[132:135]\n" \
"ds_read_b64_tr_b16 a[184:185], %[vvb] offset:9728\n" \
"s_waitcnt lgkmcnt(5)\n" \
"v_mfma_f32_16x16x32_bf16 v[132:135], a[88:91], a[8:11], v[132:135]\n" \
"buffer_load_dwordx4 %[kd0], %[srdk], s48 offen offset:0 lds\n" \
"v_mfma_f32_16x16x32_bf16 v[132:135], a[92:95], a[12:15], v[132:135]\n" \
"v_exp_f32_e32 v159, v159\n" \
"v_mfma_f32_16x16x32_bf16 v[64:67], a[128:131], v[216:219], v[64:67]\n" \
"ds_read_b64_tr_b16 a[186:187], %[vvb] offset:13824\n" \
"ds_read_b64_tr_b16 a[188:189], v211 offset:9728\n" \
"v_mfma_f32_16x16x32_bf16 v[64:67], a[160:163], v[200:203], v[64:67]\n" \
"v_exp_f32_e64 v128, v128\n" \
"v_mfma_f32_16x16x32_bf16 v[68:71], a[132:135], v[216:219], v[68:71]\n" \
"v_cvt_pk_bf16_f32 v206, v156, v157\n" \
"v_cvt_pk_bf16_f32 v207, v158, v159\n" \
"v_mfma_f32_16x16x32_bf16 v[68:71], a[164:167], v[200:203], v[68:71]\n" \
"v_exp_f32_e64 v129, v129\n" \
"v_mfma_f32_16x16x32_bf16 v[72:75], a[136:139], v[216:219], v[72:75]\n" \
"ds_read_b64_tr_b16 a[190:191], v211 offset:13824\n" \
"ds_read_b64_tr_b16 a[128:129], %[vvb] offset:16384\n" \
"s_add_u32 s60, s60, 1\n" \
"s_nop 0\n" \
"v_readlane_b32 s58, %[tlo], s60\n" \
"v_mfma_f32_16x16x32_bf16 v[72:75], a[168:171], v[200:203], v[72:75]\n" \
"v_exp_f32_e64 v130, v130\n" \
"v_mfma_f32_16x16x32_bf16 v[76:79], a[140:143], v[216:219], v[76:79]\n" \
"ds_read_b64_tr_b16 a[130:131], %[vvb] offset:20480\n" \
"ds_read_b64_tr_b16 a[132:133], v211 offset:16384\n" \
"v_readlane_b32 s59, %[thi], s60\n" \
"v_mfma_f32_16x16x32_bf16 v[76:79], a[172:175], v[200:203], v[76:79]\n" \
"v_exp_f32_e64 v131, v131\n" \
"v_smfmac_f32_16x16x64_bf16 v[176:179], a[192:195], v[192:199], v228\n" \
"ds_read_b64_tr_b16 a[134:135], v211 offset:20480\n" \
"ds_read_b64_tr_b16 a[136:137], %[vvb] offset:16896\n" \
"v_mfma_f32_16x16x32_bf16 v[136:139], a[64:67], a[16:19], v[164:167]\n" \
"v_exp_f32_e64 v132, v132\n" \
"v_mfma_f32_16x16x32_bf16 v[136:139], a[68:71], a[20:23], v[136:139]\n" \
"ds_read_b64_tr_b16 a[138:139], %[vvb] offset:20992\n" \
"ds_read_b64_tr_b16 a[140:141], v211 offset:16896\n" \
"v_mfma_f32_16x16x32_bf16 v[136:139], a[72:75], a[24:27], v[136:139]\n" \
"v_exp_f32_e64 v133, v133\n" \
"v_mfma_f32_16x16x32_bf16 v[136:139], a[76:79], a[28:31], v[136:139]\n" \
"v_cvt_pk_bf16_f32 v184, v128, v129\n" \
"v_cvt_pk_bf16_f32 v185, v130, v131\n" \
"v_mfma_f32_16x16x32_bf16 v[140:143], a[80:83], a[16:19], v[164:167]\n" \
"v_exp_f32_e64 v134, v134\n" \
"v_mfma_f32_16x16x32_bf16 v[140:143], a[84:87], a[20:23], v[140:143]\n" \
"ds_read_b64_tr_b16 a[142:143], v211 offset:20992\n" \
"s_waitcnt lgkmcnt(8)\n" \
"v_mfma_f32_16x16x32_bf16 v[140:143], a[88:91], a[24:27], v[140:143]\n" \
"buffer_load_dwordx4 v212, %[srdk], s48 offen offset:1024 lds\n" \
"v_mfma_f32_16x16x32_bf16 v[140:143], a[92:95], a[28:31], v[140:143]\n" \
"v_exp_f32_e32 v135, v135\n" \
"v_mfma_f32_16x16x32_bf16 v[80:83], a[144:147], v[216:219], v[80:83]\n" \
"ds_read_b64_tr_b16 a[144:145], %[vvb] offset:17408\n" \
"ds_read_b64_tr_b16 a[146:147], %[vvb] offset:21504\n" \
"v_mfma_f32_16x16x32_bf16 v[80:83], a[176:179], v[200:203], v[80:83]\n" \
"v_exp_f32_e64 v136, v136\n" \
"v_mfma_f32_16x16x32_bf16 v[84:87], a[148:151], v[216:219], v[84:87]\n" \
"v_cvt_pk_bf16_f32 v186, v132, v133\n" \
"v_cvt_pk_bf16_f32 v187, v134, v135\n" \
"v_mfma_f32_16x16x32_bf16 v[84:87], a[180:183], v[200:203], v[84:87]\n" \
"v_exp_f32_e64 v137, v137\n" \
"v_mfma_f32_16x16x32_bf16 v[88:91], a[152:155], v[216:219], v[88:91]\n" \
".if . - adaln_body != 12288\n" \
".error \"size model\"\n" \
".endif\n" \
"Lk2_%=:\n" \
"s_cbranch_execz Lk3_%=\n" \
"s_nop 0\n" \
"v_exp_f32_e64 v138, v138\n" \
"v_mfma_f32_16x16x32_bf16 v[88:91], a[184:187], v[200:203], v[88:91]\n" \
"ds_read_b64_tr_b16 a[148:149], v211 offset:17408\n" \
"ds_read_b64_tr_b16 a[150:151], v211 offset:21504\n" \
"v_mfma_f32_16x16x32_bf16 v[92:95], a[156:159], v[216:219], v[92:95]\n" \
"v_exp_f32_e64 v139, v139\n" \
"v_mfma_f32_16x16x32_bf16 v[92:95], a[188:191], v[200:203], v[92:95]\n" \
"ds_read_b64_tr_b16 a[152:153], %[vvb] offset:17920\n" \
"v_mfma_f32_16x16x32_bf16 v[144:147], a[64:67], a[32:35], v[168:171]\n" \
"v_exp_f32_e64 v140, v140\n" \
"v_mfma_f32_16x16x32_bf16 v[144:147], a[68:71], a[36:39], v[144:147]\n" \
"ds_read_b64_tr_b16 a[154:155], %[vvb] offset:22016\n" \
"ds_read_b64_tr_b16 a[156:157], v211 offset:17920\n" \
"v_mfma_f32_16x16x32_bf16 v[144:147], a[72:75], a[40:43], v[144:147]\n" \
"v_exp_f32_e64 v141, v141\n" \
"v_mfma_f32_16x16x32_bf16 v[144:147], a[76:79], a[44:47], v[144:147]\n" \
"v_cvt_pk_bf16_f32 v188, v136, v137\n" \
"v_cvt_pk_bf16_f32 v189, v138, v139\n" \
"v_mfma_f32_16x16x32_bf16 v[148:151], a[80:83], a[32:35], v[168:171]\n" \
"v_exp_f32_e64 v142, v142\n" \
"v_mfma_f32_16x16x32_bf16 v[148:151], a[84:87], a[36:39], v[148:151]\n" \
"ds_read_b64_tr_b16 a[158:159], v211 offset:22016\n" \
"s_waitcnt lgkmcnt(8)\n" \
"v_mfma_f32_16x16x32_bf16 v[148:151], a[88:91], a[40:43], v[148:151]\n" \
"buffer_load_dwordx4 v213, %[srdk], s48 offen offset:2048 lds\n" \
"v_mfma_f32_16x16x32_bf16 v[148:151], a[92:95], a[44:47], v[148:151]\n" \
"v_exp_f32_e32 v143, v143\n" \
"v_mfma_f32_16x16x32_bf16 v[0:3], a[160:163], v[192:195], v[0:3]\n" \
"ds_read_b128 a[96:99], %[kb0] offset:24576\n" \
"v_mfma_f32_16x16x32_bf16 v[0:3], a[128:131], v[184:187], v[0:3]\n" \
"v_exp_f32_e64 v144, v144\n" \
"v_mfma_f32_16x16x32_bf16 v[4:7], a[164:167], v[192:195], v[4:7]\n" \
"v_cvt_pk_bf16_f32 v190, v140, v141\n" \
"v_cvt_pk_bf16_f32 v191, v142, v143\n" \
"v_mfma_f32_16x16x32_bf16 v[4:7], a[132:135], v[184:187], v[4:7]\n" \
"v_exp_f32_e64 v145, v145\n" \
"v_mfma_f32_16x16x32_bf16 v[8:11], a[168:171], v[192:195], v[8:11]\n" \
"ds_read_b128 a[100:103], v208 offset:24576\n" \
"v_mfma_f32_16x16x32_bf16 v[8:11], a[136:139], v[184:187], v[8:11]\n" \
"v_exp_f32_e64 v146, v146\n" \
"v_mfma_f32_16x16x32_bf16 v[12:15], a[172:175], v[192:195], v[12:15]\n" \
"ds_read_b128 a[104:107], v209 offset:24576\n" \
"v_mfma_f32_16x16x32_bf16 v[12:15], a[140:143], v[184:187], v[12:15]\n" \
"v_exp_f32_e64 v147, v147\n" \
"v_smfmac_f32_16x16x64_bf16 v[180:183], a[192:195], v[200:207], v228\n" \
"ds_read_b128 a[108:111], v210 offset:24576\n" \
"v_mfma_f32_16x16x32_bf16 v[152:155], a[64:67], a[48:51], v[172:175]\n" \
"v_exp_f32_e64 v148, v148\n" \
"v_mfma_f32_16x16x32_bf16 v[152:155], a[68:71], a[52:55], v[152:155]\n" \
"ds_read_b128 a[112:115], %[kb0] offset:28672\n" \
"v_mfma_f32_16x16x32_bf16 v[152:155], a[72:75], a[56:59], v[152:155]\n" \
"v_exp_f32_e64 v149, v149\n" \
"v_mfma_f32_16x16x32_bf16 v[152:155], a[76:79], a[60:63], v[152:155]\n" \
"v_cvt_pk_bf16_f32 v216, v144, v145\n" \
"v_cvt_pk_bf16_f32 v217, v146, v147\n" \
"v_mfma_f32_16x16x32_bf16 v[156:159], a[80:83], a[48:51], v[172:175]\n" \
"v_exp_f32_e64 v150, v150\n" \
"v_mfma_f32_16x16x32_bf16 v[156:159], a[84:87], a[52:55], v[156:159]\n" \
"ds_read_b128 a[116:119], v208 offset:28672\n" \
"s_waitcnt lgkmcnt(6)\n" \
"v_mfma_f32_16x16x32_bf16 v[156:159], a[88:91], a[56:59], v[156:159]\n" \
"buffer_load_dwordx4 v214, %[srdk], s48 offen offset:3072 lds\n" \
"v_mfma_f32_16x16x32_bf16 v[156:159], a[92:95], a[60:63], v[156:159]\n" \
"v_exp_f32_e32 v151, v151\n" \
"v_mfma_f32_16x16x32_bf16 v[16:19], a[176:179], v[192:195], v[16:19]\n" \
"ds_read_b128 a[120:123], v209 offset:28672\n" \
"v_mfma_f32_16x16x32_bf16 v[16:19], a[144:147], v[184:187], v[16:19]\n" \
"v_exp_f32_e64 v152, v152\n" \
"v_mfma_f32_16x16x32_bf16 v[20:23], a[180:183], v[192:195], v[20:23]\n" \
"v_cvt_pk_bf16_f32 v218, v148, v149\n" \
"v_cvt_pk_bf16_f32 v219, v150, v151\n" \
"v_mfma_f32_16x16x32_bf16 v[20:23], a[148:151], v[184:187], v[20:23]\n" \
"v_exp_f32_e64 v153, v153\n" \
"v_mfma_f32_16x16x32_bf16 v[24:27], a[184:187], v[192:195], v[24:27]\n" \
"v_exp_f32_e64 v154, v154\n" \
"v_mfma_f32_16x16x32_bf16 v[24:27], a[152:155], v[184:187], v[24:27]\n" \
"ds_read_b128 a[124:127], v210 offset:28672\n" \
"s_add_u32 s48, s48, 0x4000\n" \
"s_mov_b32 m0, s44\n" \
"s_addk_i32 s44, 0x400\n" \
"buffer_load_dwordx4 v229, s[64:67], s45 offen offset:0 nt lds\n" \
"s_add_u32 s45, s45, 393216\n" \
"v_mfma_f32_16x16x32_bf16 v[28:31], a[188:191], v[192:195], v[28:31]\n" \
"v_exp_f32_e64 v155, v155\n" \
"v_mfma_f32_16x16x32_bf16 v[28:31], a[156:159], v[184:187], v[28:31]\n" \
"s_waitcnt vmcnt(14) lgkmcnt(2)\n" \
"s_barrier\n" \
"v_mfma_f32_16x16x32_bf16 v[128:131], a[96:99], a[0:3], v[160:163]\n" \
"v_exp_f32_e64 v156, v156\n" \
"v_mfma_f32_16x16x32_bf16 v[128:131], a[100:103], a[4:7], v[128:131]\n" \
"s_add_u32 m0, %[m0b], 65536\n" \
"v_mfma_f32_16x16x32_bf16 v[128:131], a[104:107], a[8:11], v[128:131]\n" \
"v_exp_f32_e64 v157, v157\n" \
"v_mfma_f32_16x16x32_bf16 v[128:131], a[108:111], a[12:15], v[128:131]\n" \
"v_cvt_pk_bf16_f32 v220, v152, v153\n" \
"v_cvt_pk_bf16_f32 v221, v154, v155\n" \
"v_mfma_f32_16x16x32_bf16 v[132:135], a[112:115], a[0:3], v[160:163]\n" \
"v_exp_f32_e64 v158, v158\n" \
"v_mfma_f32_16x16x32_bf16 v[132:135], a[116:119], a[4:7], v[132:135]\n" \
"s_waitcnt lgkmcnt(0)\n" \
"v_mfma_f32_16x16x32_bf16 v[132:135], a[120:123], a[8:11], v[132:135]\n" \
"buffer_load_dwordx4 %[vd0], %[srdv], s48 offen offset:0 lds\n" \
"v_mfma_f32_16x16x32_bf16 v[132:135], a[124:127], a[12:15], v[132:135]\n" \
"v_exp_f32_e32 v159, v159\n" \
"v_mfma_f32_16x16x32_bf16 v[32:35], a[160:163], v[196:199], v[32:35]\n" \
"ds_read_b128 a[64:67], %[kb0] offset:32768\n" \
"v_mfma_f32_16x16x32_bf16 v[32:35], a[128:131], v[188:191], v[32:35]\n" \
"v_exp_f32_e64 v128, v128\n" \
"v_mfma_f32_16x16x32_bf16 v[36:39], a[164:167], v[196:199], v[36:39]\n" \
"v_cvt_pk_bf16_f32 v222, v156, v157\n" \
"v_cvt_pk_bf16_f32 v223, v158, v159\n" \
"v_mfma_f32_16x16x32_bf16 v[36:39], a[132:135], v[188:191], v[36:39]\n" \
"v_exp_f32_e64 v129, v129\n" \
"v_mfma_f32_16x16x32_bf16 v[40:43], a[168:171], v[196:199], v[40:43]\n" \
"ds_read_b128 a[68:71], v208 offset:32768\n" \
"v_mfma_f32_16x16x32_bf16 v[40:43], a[136:139], v[188:191], v[40:43]\n" \
"v_exp_f32_e64 v130, v130\n" \
"v_mfma_f32_16x16x32_bf16 v[44:47], a[172:175], v[196:199], v[44:47]\n" \
"ds_read_b128 a[72:75], v209 offset:32768\n" \
"v_mfma_f32_16x16x32_bf16 v[44:47], a[140:143], v[188:191], v[44:47]\n" \
"v_exp_f32_e64 v131, v131\n" \
"v_smfmac_f32_16x16x64_bf16 v[176:179], a[192:195], v[184:191], v228\n" \
"v_mfma_f32_16x16x32_bf16 v[136:139], a[96:99], a[16:19], v[164:167]\n" \
"v_exp_f32_e64 v132, v132\n" \
"v_mfma_f32_16x16x32_bf16 v[136:139], a[100:103], a[20:23], v[136:139]\n" \
"ds_read_b128 a[76:79], v210 offset:32768\n" \
"v_mfma_f32_16x16x32_bf16 v[136:139], a[104:107], a[24:27], v[136:139]\n" \
"v_exp_f32_e64 v133, v133\n" \
"v_mfma_f32_16x16x32_bf16 v[136:139], a[108:111], a[28:31], v[136:139]\n" \
"v_cvt_pk_bf16_f32 v192, v128, v129\n" \
"v_cvt_pk_bf16_f32 v193, v130, v131\n" \
"v_mfma_f32_16x16x32_bf16 v[140:143], a[112:115], a[16:19], v[164:167]\n" \
"v_exp_f32_e64 v134, v134\n" \
"v_mfma_f32_16x16x32_bf16 v[140:143], a[116:119], a[20:23], v[140:143]\n" \
"ds_read_b128 a[80:83], %[kb0] offset:36864\n" \
"v_mfma_f32_16x16x32_bf16 v[140:143], a[120:123], a[24:27], v[140:143]\n" \
"buffer_load_dwordx4 v215, %[srdv], s48 offen offset:1024 lds\n" \
"v_mfma_f32_16x16x32_bf16 v[140:143], a[124:127], a[28:31], v[140:143]\n" \
"v_exp_f32_e64 v135, v135\n" \
"v_mfma_f32_16x16x32_bf16 v[48:51], a[176:179], v[196:199], v[48:51]\n" \
"ds_read_b128 a[84:87], v208 offset:36864\n" \
"v_mfma_f32_16x16x32_bf16 v[48:51], a[144:147], v[188:191], v[48:51]\n" \
"v_exp_f32_e64 v136, v136\n" \
"v_mfma_f32_16x16x32_bf16 v[52:55], a[180:183], v[196:199], v[52:55]\n" \
"v_cvt_pk_bf16_f32 v194, v132, v133\n" \
"v_cvt_pk_bf16_f32 v195, v134, v135\n" \
"v_mfma_f32_16x16x32_bf16 v[52:55], a[148:151], v[188:191], v[52:55]\n" \
"v_exp_f32_e64 v137, v137\n" \
"v_mfma_f32_16x16x32_bf16 v[56:59], a[184:187], v[196:199], v[56:59]\n" \
"v_exp_f32_e64 v138, v138\n" \
"v_mfma_f32_16x16x32_bf16 v[56:59], a[152:155], v[188:191], v[56:59]\n" \
"ds_read_b128 a[88:91], v209 offset:36864\n" \
"v_mfma_f32_16x16x32_bf16 v[60:63], a[188:191], v[196:199], v[60:63]\n" \
"v_exp_f32_e64 v139, v139\n" \
"v_mfma_f32_16x16x32_bf16 v[60:63], a[156:159], v[188:191], v[60:63]\n" \
"s_cmp_lg_u32 %[wid], 0\n" \
"s_cbranch_scc1 LtA3_%=\n" \
"global_load_dword v237, v224, s[58:59]\n" \
"s_add_u32 s61, s48, 49152\n" \
"s_mov_b32 exec_hi, 0\n" \
"s_nop 0\n" \
"buffer_load_dword v237, v224, %[srdp], s61 offen\n" \
"s_mov_b32 exec_hi, -1\n" \
"s_nop 0\n" \
"LtA3_%=:\n" \
"v_mfma_f32_16x16x32_bf16 v[144:147], a[96:99], a[32:35], v[168:171]\n" \
"v_exp_f32_e64 v140, v140\n" \
"v_mfma_f32_16x16x32_bf16 v[144:147], a[100:103], a[36:39], v[144:147]\n" \
"ds_read_b128 a[92:95], v210 offset:36864\n" \
"v_mfma_f32_16x16x32_bf16 v[144:147], a[104:107], a[40:43], v[144:147]\n" \
"v_exp_f32_e64 v141, v141\n" \
"v_mfma_f32_16x16x32_bf16 v[144:147], a[108:111], a[44:47], v[144:147]\n" \
"v_cvt_pk_bf16_f32 v196, v136, v137\n" \
"v_cvt_pk_bf16_f32 v197, v138, v139\n" \
"v_mfma_f32_16x16x32_bf16 v[148:151], a[112:115], a[32:35], v[168:171]\n" \
"v_exp_f32_e64 v142, v142\n" \
"v_mfma_f32_16x16x32_bf16 v[148:151], a[116:119], a[36:39], v[148:151]\n" \
"v_mfma_f32_16x16x32_bf16 v[148:151], a[120:123], a[40:43], v[148:151]\n" \
"buffer_load_dwordx4 %[vd0], %[srdv], s48 offen offset:2048 lds\n" \
"v_mfma_f32_16x16x32_bf16 v[148:151], a[124:127], a[44:47], v[148:151]\n" \
"v_exp_f32_e64 v143, v143\n" \
"v_mfma_f32_16x16x32_bf16 v[96:99], a[160:163], v[204:207], v[96:99]\n" \
"ds_read_b64_tr_b16 a[160:161], %[vvb] offset:24576\n" \
"v_mfma_f32_16x16x32_bf16 v[96:99], a[128:131], v[220:223], v[96:99]\n" \
"v_exp_f32_e64 v144, v144\n" \
"v_mfma_f32_16x16x32_bf16 v[100:103], a[164:167], v[204:207], v[100:103]\n" \
"v_cvt_pk_bf16_f32 v198, v140, v141\n" \
"v_cvt_pk_bf16_f32 v199, v142, v143\n" \
"v_mfma_f32_16x16x32_bf16 v[100:103], a[132:135], v[220:223], v[100:103]\n" \
"v_exp_f32_e64 v145, v145\n" \
"v_mfma_f32_16x16x32_bf16 v[104:107], a[168:171], v[204:207], v[104:107]\n" \
"ds_read_b64_tr_b16 a[162:163], %[vvb] offset:28672\n" \
"ds_read_b64_tr_b16 a[164:165], v211 offset:24576\n" \
"v_mfma_f32_16x16x32_bf16 v[104:107], a[136:139], v[220:223], v[104:107]\n" \
"v_exp_f32_e64 v146, v146\n" \
"v_mfma_f32_16x16x32_bf16 v[108:111], a[172:175], v[204:207], v[108:111]\n" \
"ds_read_b64_tr_b16 a[166:167], v211 offset:28672\n" \
"v_mfma_f32_16x16x32_bf16 v[108:111], a[140:143], v[220:223], v[108:111]\n" \
"v_exp_f32_e64 v147, v147\n" \
"v_smfmac_f32_16x16x64_bf16 v[180:183], a[192:195], v[216:223], v228\n" \
"ds_read_b64_tr_b16 a[168:169], %[vvb] offset:25088\n" \
"v_mfma_f32_16x16x32_bf16 v[152:155], a[96:99], a[48:51], v[172:175]\n" \
"v_exp_f32_e64 v148, v148\n" \
"v_mfma_f32_16x16x32_bf16 v[152:155], a[100:103], a[52:55], v[152:155]\n" \
"ds_read_b64_tr_b16 a[170:171], %[vvb] offset:29184\n" \
"v_mfma_f32_16x16x32_bf16 v[152:155], a[104:107], a[56:59], v[152:155]\n" \
"v_exp_f32_e64 v149, v149\n" \
"v_mfma_f32_16x16x32_bf16 v[152:155], a[108:111], a[60:63], v[152:155]\n" \
"v_cvt_pk_bf16_f32 v200, v144, v145\n" \
"v_cvt_pk_bf16_f32 v201, v146, v147\n" \
"v_mfma_f32_16x16x32_bf16 v[156:159], a[112:115], a[48:51], v[172:175]\n" \
"v_exp_f32_e64 v150, v150\n" \
"v_mfma_f32_16x16x32_bf16 v[156:159], a[116:119], a[52:55], v[156:159]\n" \
"ds_read_b64_tr_b16 a[172:173], v211 offset:25088\n" \
"v_mfma_f32_16x16x32_bf16 v[156:159], a[120:123], a[56:59], v[156:159]\n" \
"buffer_load_dwordx4 v215, %[srdv], s48 offen offset:3072 lds\n" \
"v_mfma_f32_16x16x32_bf16 v[156:159], a[124:127], a[60:63], v[156:159]\n" \
"v_exp_f32_e64 v151, v151\n" \
"v_mfma_f32_16x16x32_bf16 v[112:115], a[176:179], v[204:207], v[112:115]\n" \
"ds_read_b64_tr_b16 a[174:175], v211 offset:29184\n" \
"ds_read_b64_tr_b16 a[176:177], %[vvb] offset:25600\n" \
"v_mfma_f32_16x16x32_bf16 v[112:115], a[144:147], v[220:223], v[112:115]\n" \
"v_exp_f32_e64 v152, v152\n" \
"v_mfma_f32_16x16x32_bf16 v[116:119], a[180:183], v[204:207], v[116:119]\n" \
"v_cvt_pk_bf16_f32 v202, v148, v149\n" \
"v_cvt_pk_bf16_f32 v203, v150, v151\n" \
"v_mfma_f32_16x16x32_bf16 v[116:119], a[148:151], v[220:223], v[116:119]\n" \
"v_exp_f32_e64 v153, v153\n" \
"v_mfma_f32_16x16x32_bf16 v[120:123], a[184:187], v[204:207], v[120:123]\n" \
"v_exp_f32_e64 v154, v154\n" \
"v_mfma_f32_16x16x32_bf16 v[120:123], a[152:155], v[220:223], v[120:123]\n" \
"ds_read_b64_tr_b16 a[178:179], %[vvb] offset:29696\n" \
"v_mfma_f32_16x16x32_bf16 v[124:127], a[188:191], v[204:207], v[124:127]\n" \
"v_exp_f32_e64 v155, v155\n" \
"v_mfma_f32_16x16x32_bf16 v[124:127], a[156:159], v[220:223], v[124:127]\n" \
"ds_read_b64_tr_b16 a[180:181], v211 offset:25600\n" \
"s_waitcnt lgkmcnt(13)\n" \
"v_mfma_f32_16x16x32_bf16 v[128:131], a[64:67], a[0:3], v[160:163]\n" \
"v_exp_f32_e32 v156, v156\n" \
"v_mfma_f32_16x16x32_bf16 v[128:131], a[68:71], a[4:7], v[128:131]\n" \
"ds_read_b64_tr_b16 a[182:183], v211 offset:29696\n" \
"s_add_u32 m0, %[m0b], 16384\n" \
"v_mfma_f32_16x16x32_bf16 v[128:131], a[72:75], a[8:11], v[128:131]\n" \
"v_exp_f32_e64 v157, v157\n" \
"v_mfma_f32_16x16x32_bf16 v[128:131], a[76:79], a[12:15], v[128:131]\n" \
"v_cvt_pk_bf16_f32 v204, v152, v153\n" \
"v_cvt_pk_bf16_f32 v205, v154, v155\n" \
"v_mfma_f32_16x16x32_bf16 v[132:135], a[80:83], a[0:3], v[160:163]\n" \
"v_exp_f32_e64 v158, v158\n" \
"v_mfma_f32_16x16x32_bf16 v[132:135], a[84:87], a[4:7], v[132:135]\n" \
"ds_read_b64_tr_b16 a[184:185], %[vvb] offset:26112\n" \
"s_waitcnt lgkmcnt(5)\n" \
"v_mfma_f32_16x16x32_bf16 v[132:135], a[88:91], a[8:11], v[132:135]\n" \
"buffer_load_dwordx4 %[kd0], %[srdk], s48 offen offset:0 lds\n" \
"v_mfma_f32_16x16x32_bf16 v[132:135], a[92:95], a[12:15], v[132:135]\n" \
"v_exp_f32_e32 v159, v159\n" \
"v_mfma_f32_16x16x32_bf16 v[64:67], a[128:131], v[216:219], v[64:67]\n" \
"ds_read_b64_tr_b16 a[186:187], %[vvb] offset:30208\n" \
"ds_read_b64_tr_b16 a[188:189], v211 offset:26112\n" \
"v_mfma_f32_16x16x32_bf16 v[64:67], a[160:163], v[200:203], v[64:67]\n" \
"v_exp_f32_e64 v128, v128\n" \
"v_mfma_f32_16x16x32_bf16 v[68:71], a[132:135], v[216:219], v[68:71]\n" \
"v_cvt_pk_bf16_f32 v206, v156, v157\n" \
"v_cvt_pk_bf16_f32 v207, v158, v159\n" \
"v_mfma_f32_16x16x32_bf16 v[68:71], a[164:167], v[200:203], v[68:71]\n" \
"v_exp_f32_e64 v129, v129\n" \
"v_mfma_f32_16x16x32_bf16 v[72:75], a[136:139], v[216:219], v[72:75]\n" \
"ds_read_b64_tr_b16 a[190:191], v211 offset:30208\n" \
"ds_read_b64_tr_b16 a[128:129], %[vvb] offset:32768\n" \
"v_mfma_f32_16x16x32_bf16 v[72:75], a[168:171], v[200:203], v[72:75]\n" \
"v_exp_f32_e64 v130, v130\n" \
"v_mfma_f32_16x16x32_bf16 v[76:79], a[140:143], v[216:219], v[76:79]\n" \
"ds_read_b64_tr_b16 a[130:131], %[vvb] offset:36864\n" \
"ds_read_b64_tr_b16 a[132:133], v211 offset:32768\n" \
"v_mfma_f32_16x16x32_bf16 v[76:79], a[172:175], v[200:203], v[76:79]\n" \
"v_exp_f32_e64 v131, v131\n" \
"v_smfmac_f32_16x16x64_bf16 v[176:179], a[192:195], v[192:199], v228\n" \
"ds_read_b64_tr_b16 a[134:135], v211 offset:36864\n" \
"ds_read_b64_tr_b16 a[136:137], %[vvb] offset:33280\n" \
"v_mfma_f32_16x16x32_bf16 v[136:139], a[64:67], a[16:19], v[164:167]\n" \
"v_exp_f32_e64 v132, v132\n" \
"v_mfma_f32_16x16x32_bf16 v[136:139], a[68:71], a[20:23], v[136:139]\n" \
"ds_read_b64_tr_b16 a[138:139], %[vvb] offset:37376\n" \
"ds_read_b64_tr_b16 a[140:141], v211 offset:33280\n" \
"v_mfma_f32_16x16x32_bf16 v[136:139], a[72:75], a[24:27], v[136:139]\n" \
"v_exp_f32_e64 v133, v133\n" \
"v_mfma_f32_16x16x32_bf16 v[136:139], a[76:79], a[28:31], v[136:139]\n" \
"v_cvt_pk_bf16_f32 v184, v128, v129\n" \
"v_cvt_pk_bf16_f32 v185, v130, v131\n" \
"v_mfma_f32_16x16x32_bf16 v[140:143], a[80:83], a[16:19], v[164:167]\n" \
"v_exp_f32_e64 v134, v134\n" \
"v_mfma_f32_16x16x32_bf16 v[140:143], a[84:87], a[20:23], v[140:143]\n" \
"ds_read_b64_tr_b16 a[142:143], v211 offset:37376\n" \
"s_waitcnt lgkmcnt(8)\n" \
"v_mfma_f32_16x16x32_bf16 v[140:143], a[88:91], a[24:27], v[140:143]\n" \
"buffer_load_dwordx4 v212, %[srdk], s48 offen offset:1024 lds\n" \
"v_mfma_f32_16x16x32_bf16 v[140:143], a[92:95], a[28:31], v[140:143]\n" \
"v_exp_f32_e32 v135, v135\n" \
"v_mfma_f32_16x16x32_bf16 v[80:83], a[144:147], v[216:219], v[80:83]\n" \
"ds_read_b64_tr_b16 a[144:145], %[vvb] offset:33792\n" \
"ds_read_b64_tr_b16 a[146:147], %[vvb] offset:37888\n" \
"v_mfma_f32_16x16x32_bf16 v[80:83], a[176:179], v[200:203], v[80:83]\n" \
"v_exp_f32_e64 v136, v136\n" \
"v_mfma_f32_16x16x32_bf16 v[84:87], a[148:151], v[216:219], v[84:87]\n" \
"v_cvt_pk_bf16_f32 v186, v132, v133\n" \
"v_cvt_pk_bf16_f32 v187, v134, v135\n" \
"v_mfma_f32_16x16x32_bf16 v[84:87], a[180:183], v[200:203], v[84:87]\n" \
"v_exp_f32_e64 v137, v137\n" \
"v_mfma_f32_16x16x32_bf16 v[88:91], a[152:155], v[216:219], v[88:91]\n" \
"v_exp_f32_e64 v138, v138\n" \
"v_mfma_f32_16x16x32_bf16 v[88:91], a[184:187], v[200:203], v[88:91]\n" \
"ds_read_b64_tr_b16 a[148:149], v211 offset:33792\n" \
"ds_read_b64_tr_b16 a[150:151], v211 offset:37888\n" \
"v_mfma_f32_16x16x32_bf16 v[92:95], a[156:159], v[216:219], v[92:95]\n" \
"v_exp_f32_e64 v139, v139\n" \
"v_mfma_f32_16x16x32_bf16 v[92:95], a[188:191], v[200:203], v[92:95]\n" \
"ds_read_b64_tr_b16 a[152:153], %[vvb] offset:34304\n" \
"buffer_store_dwordx4 a[196:199], v235, %[srdz], s62 offen offset:0 sc1\n" \
"v_mfma_f32_16x16x32_bf16 v[144:147], a[64:67], a[32:35], v[168:171]\n" \
"v_exp_f32_e64 v140, v140\n" \
"v_mfma_f32_16x16x32_bf16 v[144:147], a[68:71], a[36:39], v[144:147]\n" \
"ds_read_b64_tr_b16 a[154:155], %[vvb] offset:38400\n" \
"ds_read_b64_tr_b16 a[156:157], v211 offset:34304\n" \
"v_mfma_f32_16x16x32_bf16 v[144:147], a[72:75], a[40:43], v[144:147]\n" \
"v_exp_f32_e64 v141, v141\n" \
"v_mfma_f32_16x16x32_bf16 v[144:147], a[76:79], a[44:47], v[144:147]\n" \
"v_cvt_pk_bf16_f32 v188, v136, v137\n" \
"v_cvt_pk_bf16_f32 v189, v138, v139\n" \
"v_mfma_f32_16x16x32_bf16 v[148:151], a[80:83], a[32:35], v[168:171]\n" \
"v_exp_f32_e64 v142, v142\n" \
"v_mfma_f32_16x16x32_bf16 v[148:151], a[84:87], a[36:39], v[148:151]\n" \
"ds_read_b64_tr_b16 a[158:159], v211 offset:38400\n" \
"s_waitcnt lgkmcnt(8)\n" \
"v_mfma_f32_16x16x32_bf16 v[148:151], a[88:91], a[40:43], v[148:151]\n" \
"buffer_load_dwordx4 v213, %[srdk], s48 offen offset:2048 lds\n" \
"v_mfma_f32_16x16x32_bf16 v[148:151], a[92:95], a[44:47], v[148:151]\n" \
"v_exp_f32_e32 v143, v143\n" \
"v_mfma_f32_16x16x32_bf16 v[0:3], a[160:163], v[192:195], v[0:3]\n" \
"ds_read_b128 a[96:99], %[kb0] offset:40960\n" \
"v_mfma_f32_16x16x32_bf16 v[0:3], a[128:131], v[184:187], v[0:3]\n" \
"v_exp_f32_e64 v144, v144\n" \
"v_mfma_f32_16x16x32_bf16 v[4:7], a[164:167], v[192:195], v[4:7]\n" \
"v_cvt_pk_bf16_f32 v190, v140, v141\n" \
"v_cvt_pk_bf16_f32 v191, v142, v143\n" \
"v_mfma_f32_16x16x32_bf16 v[4:7], a[132:135], v[184:187], v[4:7]\n" \
"v_exp_f32_e64 v145, v145\n" \
"v_mfma_f32_16x16x32_bf16 v[8:11], a[168:171], v[192:195], v[8:11]\n" \
"ds_read_b128 a[100:103], v208 offset:40960\n" \
"v_mfma_f32_16x16x32_bf16 v[8:11], a[136:139], v[184:187], v[8:11]\n" \
"v_exp_f32_e64 v146, v146\n" \
"v_mfma_f32_16x16x32_bf16 v[12:15], a[172:175], v[192:195], v[12:15]\n" \
"ds_read_b128 a[104:107], v209 offset:40960\n" \
"v_mfma_f32_16x16x32_bf16 v[12:15], a[140:143], v[184:187], v[12:15]\n" \
"v_exp_f32_e64 v147, v147\n" \
"v_smfmac_f32_16x16x64_bf16 v[180:183], a[192:195], v[200:207], v228\n" \
"ds_read_b128 a[108:111], v210 offset:40960\n" \
"v_mfma_f32_16x16x32_bf16 v[152:155], a[64:67], a[48:51], v[172:175]\n" \
"v_exp_f32_e64 v148, v148\n" \
"v_mfma_f32_16x16x32_bf16 v[152:155], a[68:71], a[52:55], v[152:155]\n" \
"ds_read_b128 a[112:115], %[kb0] offset:45056\n" \
"v_mfma_f32_16x16x32_bf16 v[152:155], a[72:75], a[56:59], v[152:155]\n" \
"v_exp_f32_e64 v149, v149\n" \
"v_mfma_f32_16x16x32_bf16 v[152:155], a[76:79], a[60:63], v[152:155]\n" \
"v_cvt_pk_bf16_f32 v216, v144, v145\n" \
"v_cvt_pk_bf16_f32 v217, v146, v147\n" \
"v_mfma_f32_16x16x32_bf16 v[156:159], a[80:83], a[48:51], v[172:175]\n" \
"v_exp_f32_e64 v150, v150\n" \
"v_mfma_f32_16x16x32_bf16 v[156:159], a[84:87], a[52:55], v[156:159]\n" \
"ds_read_b128 a[116:119], v208 offset:45056\n" \
"s_waitcnt lgkmcnt(6)\n" \
"v_mfma_f32_16x16x32_bf16 v[156:159], a[88:91], a[56:59], v[156:159]\n" \
"buffer_load_dwordx4 v214, %[srdk], s48 offen offset:3072 lds\n" \
"v_mfma_f32_16x16x32_bf16 v[156:159], a[92:95], a[60:63], v[156:159]\n" \
"v_exp_f32_e32 v151, v151\n" \
"v_mfma_f32_16x16x32_bf16 v[16:19], a[176:179], v[192:195], v[16:19]\n" \
"ds_read_b128 a[120:123], v209 offset:45056\n" \
"v_mfma_f32_16x16x32_bf16 v[16:19], a[144:147], v[184:187], v[16:19]\n" \
"v_exp_f32_e64 v152, v152\n" \
"v_mfma_f32_16x16x32_bf16 v[20:23], a[180:183], v[192:195], v[20:23]\n" \
"v_cvt_pk_bf16_f32 v218, v148, v149\n" \
"v_cvt_pk_bf16_f32 v219, v150, v151\n" \
"v_mfma_f32_16x16x32_bf16 v[20:23], a[148:151], v[184:187], v[20:23]\n" \
"v_exp_f32_e64 v153, v153\n" \
"v_mfma_f32_16x16x32_bf16 v[24:27], a[184:187], v[192:195], v[24:27]\n" \
"v_exp_f32_e64 v154, v154\n" \
"v_mfma_f32_16x16x32_bf16 v[24:27], a[152:155], v[184:187], v[24:27]\n" \
"ds_read_b128 a[124:127], v210 offset:45056\n" \
"s_add_u32 s48, s48, 0x4000\n" \
"v_mfma_f32_16x16x32_bf16 v[28:31], a[188:191], v[192:195], v[28:31]\n" \
"v_exp_f32_e64 v155, v155\n" \
"v_mfma_f32_16x16x32_bf16 v[28:31], a[156:159], v[184:187], v[28:31]\n" \
"s_waitcnt vmcnt(14) lgkmcnt(2)\n" \
"s_barrier\n" \
"v_mfma_f32_16x16x32_bf16 v[128:131], a[96:99], a[0:3], v[160:163]\n" \
"v_exp_f32_e64 v156, v156\n" \
"v_mfma_f32_16x16x32_bf16 v[128:131], a[100:103], a[4:7], v[128:131]\n" \
"s_add_u32 m0, %[m0b], 81920\n" \
"v_mfma_f32_16x16x32_bf16 v[128:131], a[104:107], a[8:11], v[128:131]\n" \
"v_exp_f32_e64 v157, v157\n" \
"v_mfma_f32_16x16x32_bf16 v[128:131], a[108:111], a[12:15], v[128:131]\n" \
"v_cvt_pk_bf16_f32 v220, v152, v153\n" \
"v_cvt_pk_bf16_f32 v221, v154, v155\n" \
"v_mfma_f32_16x16x32_bf16 v[132:135], a[112:115], a[0:3], v[160:163]\n" \
"v_exp_f32_e64 v158, v158\n" \
"v_mfma_f32_16x16x32_bf16 v[132:135], a[116:119], a[4:7], v[132:135]\n" \
"s_waitcnt lgkmcnt(0)\n" \
"v_mfma_f32_16x16x32_bf16 v[132:135], a[120:123], a[8:11], v[132:135]\n" \
"buffer_load_dwordx4 %[vd0], %[srdv], s48 offen offset:0 lds\n" \
"v_mfma_f32_16x16x32_bf16 v[132:135], a[124:127], a[12:15], v[132:135]\n" \
"v_exp_f32_e32 v159, v159\n" \
"v_mfma_f32_16x16x32_bf16 v[32:35], a[160:163], v[196:199], v[32:35]\n" \
"ds_read_b128 a[64:67], %[kb0] offset:49152\n" \
"v_mfma_f32_16x16x32_bf16 v[32:35], a[128:131], v[188:191], v[32:35]\n" \
"v_exp_f32_e64 v128, v128\n" \
"v_mfma_f32_16x16x32_bf16 v[36:39], a[164:167], v[196:199], v[36:39]\n" \
"v_cvt_pk_bf16_f32 v222, v156, v157\n" \
"v_cvt_pk_bf16_f32 v223, v158, v159\n" \
"v_mfma_f32_16x16x32_bf16 v[36:39], a[132:135], v[188:191], v[36:39]\n" \
"v_exp_f32_e64 v129, v129\n" \
"v_mfma_f32_16x16x32_bf16 v[40:43], a[168:171], v[196:199], v[40:43]\n" \
"ds_read_b128 a[68:71], v208 offset:49152\n" \
"v_mfma_f32_16x16x32_bf16 v[40:43], a[136:139], v[188:191], v[40:43]\n" \
"v_exp_f32_e64 v130, v130\n" \
"v_mfma_f32_16x16x32_bf16 v[44:47], a[172:175], v[196:199], v[44:47]\n" \
"ds_read_b128 a[72:75], v209 offset:49152\n" \
"v_mfma_f32_16x16x32_bf16 v[44:47], a[140:143], v[188:191], v[44:47]\n" \
"v_exp_f32_e64 v131, v131\n" \
"v_smfmac_f32_16x16x64_bf16 v[176:179], a[192:195], v[184:191], v228\n" \
"v_mfma_f32_16x16x32_bf16 v[136:139], a[96:99], a[16:19], v[164:167]\n" \
"v_exp_f32_e64 v132, v132\n" \
"v_mfma_f32_16x16x32_bf16 v[136:139], a[100:103], a[20:23], v[136:139]\n" \
"ds_read_b128 a[76:79], v210 offset:49152\n" \
"v_mfma_f32_16x16x32_bf16 v[136:139], a[104:107], a[24:27], v[136:139]\n" \
"v_exp_f32_e64 v133, v133\n" \
"v_mfma_f32_16x16x32_bf16 v[136:139], a[108:111], a[28:31], v[136:139]\n" \
"v_cvt_pk_bf16_f32 v192, v128, v129\n" \
"v_cvt_pk_bf16_f32 v193, v130, v131\n" \
"v_mfma_f32_16x16x32_bf16 v[140:143], a[112:115], a[16:19], v[164:167]\n" \
"v_exp_f32_e64 v134, v134\n" \
"v_mfma_f32_16x16x32_bf16 v[140:143], a[116:119], a[20:23], v[140:143]\n" \
"ds_read_b128 a[80:83], %[kb0] offset:53248\n" \
"v_mfma_f32_16x16x32_bf16 v[140:143], a[120:123], a[24:27], v[140:143]\n" \
"buffer_load_dwordx4 v215, %[srdv], s48 offen offset:1024 lds\n" \
"v_mfma_f32_16x16x32_bf16 v[140:143], a[124:127], a[28:31], v[140:143]\n" \
"v_exp_f32_e64 v135, v135\n" \
"v_mfma_f32_16x16x32_bf16 v[48:51], a[176:179], v[196:199], v[48:51]\n" \
"ds_read_b128 a[84:87], v208 offset:53248\n" \
"v_mfma_f32_16x16x32_bf16 v[48:51], a[144:147], v[188:191], v[48:51]\n" \
"v_exp_f32_e64 v136, v136\n" \
"v_mfma_f32_16x16x32_bf16 v[52:55], a[180:183], v[196:199], v[52:55]\n" \
"v_cvt_pk_bf16_f32 v194, v132, v133\n" \
"v_cvt_pk_bf16_f32 v195, v134, v135\n" \
"v_mfma_f32_16x16x32_bf16 v[52:55], a[148:151], v[188:191], v[52:55]\n" \
"v_exp_f32_e64 v137, v137\n" \
"v_mfma_f32_16x16x32_bf16 v[56:59], a[184:187], v[196:199], v[56:59]\n" \
"v_exp_f32_e64 v138, v138\n" \
"v_mfma_f32_16x16x32_bf16 v[56:59], a[152:155], v[188:191], v[56:59]\n" \
"ds_read_b128 a[88:91], v209 offset:53248\n" \
"v_mfma_f32_16x16x32_bf16 v[60:63], a[188:191], v[196:199], v[60:63]\n" \
"v_exp_f32_e64 v139, v139\n" \
"v_mfma_f32_16x16x32_bf16 v[60:63], a[156:159], v[188:191], v[60:63]\n" \
"s_cmp_lg_u32 %[wid], 1\n" \
"s_cbranch_scc1 LtA5_%=\n" \
"global_load_dword v237, v224, s[58:59]\n" \
"s_add_u32 s61, s48, 49152\n" \
"s_mov_b32 exec_hi, 0\n" \
"s_nop 0\n" \
"buffer_load_dword v237, v224, %[srdp], s61 offen\n" \
"s_mov_b32 exec_hi, -1\n" \
"s_nop 0\n" \
"LtA5_%=:\n" \
"v_mfma_f32_16x16x32_bf16 v[144:147], a[96:99], a[32:35], v[168:171]\n" \
"v_exp_f32_e64 v140, v140\n" \
"v_mfma_f32_16x16x32_bf16 v[144:147], a[100:103], a[36:39], v[144:147]\n" \
"ds_read_b128 a[92:95], v210 offset:53248\n" \
"v_mfma_f32_16x16x32_bf16 v[144:147], a[104:107], a[40:43], v[144:147]\n" \
"v_exp_f32_e64 v141, v141\n" \
"v_mfma_f32_16x16x32_bf16 v[144:147], a[108:111], a[44:47], v[144:147]\n" \
"v_cvt_pk_bf16_f32 v196, v136, v137\n" \
"v_cvt_pk_bf16_f32 v197, v138, v139\n" \
"v_mfma_f32_16x16x32_bf16 v[148:151], a[112:115], a[32:35], v[168:171]\n" \
"v_exp_f32_e64 v142, v142\n" \
"v_mfma_f32_16x16x32_bf16 v[148:151], a[116:119], a[36:39], v[148:151]\n" \
"v_mfma_f32_16x16x32_bf16 v[148:151], a[120:123], a[40:43], v[148:151]\n" \
"buffer_load_dwordx4 %[vd0], %[srdv], s48 offen offset:2048 lds\n" \
"v_mfma_f32_16x16x32_bf16 v[148:151], a[124:127], a[44:47], v[148:151]\n" \
"v_exp_f32_e64 v143, v143\n" \
"v_mfma_f32_16x16x32_bf16 v[96:99], a[160:163], v[204:207], v[96:99]\n" \
"ds_read_b64_tr_b16 a[160:161], %[vvb] offset:40960\n" \
"v_mfma_f32_16x16x32_bf16 v[96:99], a[128:131], v[220:223], v[96:99]\n" \
"v_exp_f32_e64 v144, v144\n" \
"v_mfma_f32_16x16x32_bf16 v[100:103], a[164:167], v[204:207], v[100:103]\n" \
"v_cvt_pk_bf16_f32 v198, v140, v141\n" \
"v_cvt_pk_bf16_f32 v199, v142, v143\n" \
"v_mfma_f32_16x16x32_bf16 v[100:103], a[132:135], v[220:223], v[100:103]\n" \
"v_exp_f32_e64 v145, v145\n" \
"v_mfma_f32_16x16x32_bf16 v[104:107], a[168:171], v[204:207], v[104:107]\n" \
"ds_read_b64_tr_b16 a[162:163], %[vvb] offset:45056\n" \
"ds_read_b64_tr_b16 a[164:165], v211 offset:40960\n" \
"v_mfma_f32_16x16x32_bf16 v[104:107], a[136:139], v[220:223], v[104:107]\n" \
"v_exp_f32_e64 v146, v146\n" \
"v_mfma_f32_16x16x32_bf16 v[108:111], a[172:175], v[204:207], v[108:111]\n" \
"ds_read_b64_tr_b16 a[166:167], v211 offset:45056\n" \
"v_mfma_f32_16x16x32_bf16 v[108:111], a[140:143], v[220:223], v[108:111]\n" \
"v_exp_f32_e64 v147, v147\n" \
"v_smfmac_f32_16x16x64_bf16 v[180:183], a[192:195], v[216:223], v228\n" \
"ds_read_b64_tr_b16 a[168:169], %[vvb] offset:41472\n" \
"v_mfma_f32_16x16x32_bf16 v[152:155], a[96:99], a[48:51], v[172:175]\n" \
"v_exp_f32_e64 v148, v148\n" \
"v_mfma_f32_16x16x32_bf16 v[152:155], a[100:103], a[52:55], v[152:155]\n" \
"ds_read_b64_tr_b16 a[170:171], %[vvb] offset:45568\n" \
"v_mfma_f32_16x16x32_bf16 v[152:155], a[104:107], a[56:59], v[152:155]\n" \
"v_exp_f32_e64 v149, v149\n" \
"v_mfma_f32_16x16x32_bf16 v[152:155], a[108:111], a[60:63], v[152:155]\n" \
"v_cvt_pk_bf16_f32 v200, v144, v145\n" \
"v_cvt_pk_bf16_f32 v201, v146, v147\n" \
"v_mfma_f32_16x16x32_bf16 v[156:159], a[112:115], a[48:51], v[172:175]\n" \
"v_exp_f32_e64 v150, v150\n" \
"v_mfma_f32_16x16x32_bf16 v[156:159], a[116:119], a[52:55], v[156:159]\n" \
"ds_read_b64_tr_b16 a[172:173], v211 offset:41472\n" \
"v_mfma_f32_16x16x32_bf16 v[156:159], a[120:123], a[56:59], v[156:159]\n" \
"buffer_load_dwordx4 v215, %[srdv], s48 offen offset:3072 lds\n" \
"v_mfma_f32_16x16x32_bf16 v[156:159], a[124:127], a[60:63], v[156:159]\n" \
"v_exp_f32_e64 v151, v151\n" \
"v_mfma_f32_16x16x32_bf16 v[112:115], a[176:179], v[204:207], v[112:115]\n" \
"ds_read_b64_tr_b16 a[174:175], v211 offset:45568\n" \
"ds_read_b64_tr_b16 a[176:177], %[vvb] offset:41984\n" \
"v_mfma_f32_16x16x32_bf16 v[112:115], a[144:147], v[220:223], v[112:115]\n" \
"v_exp_f32_e64 v152, v152\n" \
"v_mfma_f32_16x16x32_bf16 v[116:119], a[180:183], v[204:207], v[116:119]\n" \
"v_cvt_pk_bf16_f32 v202, v148, v149\n" \
".if . - adaln_body != 16384\n" \
".error \"size model\"\n" \
".endif\n" \
"Lk3_%=:\n" \
"s_cbranch_execz Lk4_%=\n" \
"s_nop 0\n" \
"v_cvt_pk_bf16_f32 v203, v150, v151\n" \
"v_mfma_f32_16x16x32_bf16 v[116:119], a[148:151], v[220:223], v[116:119]\n" \
"v_exp_f32_e64 v153, v153\n" \
"v_mfma_f32_16x16x32_bf16 v[120:123], a[184:187], v[204:207], v[120:123]\n" \
"v_exp_f32_e64 v154, v154\n" \
"v_mfma_f32_16x16x32_bf16 v[120:123], a[152:155], v[220:223], v[120:123]\n" \
"ds_read_b64_tr_b16 a[178:179], %[vvb] offset:46080\n" \
"v_mfma_f32_16x16x32_bf16 v[124:127], a[188:191], v[204:207], v[124:127]\n" \
"v_exp_f32_e64 v155, v155\n" \
"v_mfma_f32_16x16x32_bf16 v[124:127], a[156:159], v[220:223], v[124:127]\n" \
"ds_read_b64_tr_b16 a[180:181], v211 offset:41984\n" \
"s_waitcnt lgkmcnt(13)\n" \
"v_mfma_f32_16x16x32_bf16 v[128:131], a[64:67], a[0:3], v[160:163]\n" \
"v_exp_f32_e32 v156, v156\n" \
"v_mfma_f32_16x16x32_bf16 v[128:131], a[68:71], a[4:7], v[128:131]\n" \
"ds_read_b64_tr_b16 a[182:183], v211 offset:46080\n" \
"s_add_u32 m0, %[m0b], 32768\n" \
"v_mfma_f32_16x16x32_bf16 v[128:131], a[72:75], a[8:11], v[128:131]\n" \
"v_exp_f32_e64 v157, v157\n" \
"v_mfma_f32_16x16x32_bf16 v[128:131], a[76:79], a[12:15], v[128:131]\n" \
"v_cvt_pk_bf16_f32 v204, v152, v153\n" \
"v_cvt_pk_bf16_f32 v205, v154, v155\n" \
"v_mfma_f32_16x16x32_bf16 v[132:135], a[80:83], a[0:3], v[160:163]\n" \
"v_exp_f32_e64 v158, v158\n" \
"v_mfma_f32_16x16x32_bf16 v[132:135], a[84:87], a[4:7], v[132:135]\n" \
"ds_read_b64_tr_b16 a[184:185], %[vvb] offset:42496\n" \
"s_waitcnt lgkmcnt(5)\n" \
"v_mfma_f32_16x16x32_bf16 v[132:135], a[88:91], a[8:11], v[132:135]\n" \
"buffer_load_dwordx4 %[kd0], %[srdk], s48 offen offset:0 lds\n" \
"v_mfma_f32_16x16x32_bf16 v[132:135], a[92:95], a[12:15], v[132:135]\n" \
"v_exp_f32_e32 v159, v159\n" \
"v_mfma_f32_16x16x32_bf16 v[64:67], a[128:131], v[216:219], v[64:67]\n" \
"ds_read_b64_tr_b16 a[186:187], %[vvb] offset:46592\n" \
"ds_read_b64_tr_b16 a[188:189], v211 offset:42496\n" \
"v_mfma_f32_16x16x32_bf16 v[64:67], a[160:163], v[200:203], v[64:67]\n" \
"v_exp_f32_e64 v128, v128\n" \
"v_mfma_f32_16x16x32_bf16 v[68:71], a[132:135], v[216:219], v[68:71]\n" \
"v_cvt_pk_bf16_f32 v206, v156, v157\n" \
"v_cvt_pk_bf16_f32 v207, v158, v159\n" \
"v_mfma_f32_16x16x32_bf16 v[68:71], a[164:167], v[200:203], v[68:71]\n" \
"v_exp_f32_e64 v129, v129\n" \
"v_mfma_f32_16x16x32_bf16 v[72:75], a[136:139], v[216:219], v[72:75]\n" \
"ds_read_b64_tr_b16 a[190:191], v211 offset:46592\n" \
"ds_read_b64_tr_b16 a[128:129], %[vvb] offset:49152\n" \
"v_mfma_f32_16x16x32_bf16 v[72:75], a[168:171], v[200:203], v[72:75]\n" \
"v_exp_f32_e64 v130, v130\n" \
"v_mfma_f32_16x16x32_bf16 v[76:79], a[140:143], v[216:219], v[76:79]\n" \
"ds_read_b64_tr_b16 a[130:131], %[vvb] offset:53248\n" \
"ds_read_b64_tr_b16 a[132:133], v211 offset:49152\n" \
"v_mfma_f32_16x16x32_bf16 v[76:79], a[172:175], v[200:203], v[76:79]\n" \
"v_exp_f32_e64 v131, v131\n" \
"v_smfmac_f32_16x16x64_bf16 v[176:179], a[192:195], v[192:199], v228\n" \
"ds_read_b64_tr_b16 a[134:135], v211 offset:53248\n" \
"ds_read_b64_tr_b16 a[136:137], %[vvb] offset:49664\n" \
"v_mfma_f32_16x16x32_bf16 v[136:139], a[64:67], a[16:19], v[164:167]\n" \
"v_exp_f32_e64 v132, v132\n" \
"v_mfma_f32_16x16x32_bf16 v[136:139], a[68:71], a[20:23], v[136:139]\n" \
"ds_read_b64_tr_b16 a[138:139], %[vvb] offset:53760\n" \
"ds_read_b64_tr_b16 a[140:141], v211 offset:49664\n" \
"v_mfma_f32_16x16x32_bf16 v[136:139], a[72:75], a[24:27], v[136:139]\n" \
"v_exp_f32_e64 v133, v133\n" \
"v_mfma_f32_16x16x32_bf16 v[136:139], a[76:79], a[28:31], v[136:139]\n" \
"v_cvt_pk_bf16_f32 v184, v128, v129\n" \
"v_cvt_pk_bf16_f32 v185, v130, v131\n" \
"v_mfma_f32_16x16x32_bf16 v[140:143], a[80:83], a[16:19], v[164:167]\n" \
"v_exp_f32_e64 v134, v134\n" \
"v_mfma_f32_16x16x32_bf16 v[140:143], a[84:87], a[20:23], v[140:143]\n" \
"ds_read_b64_tr_b16 a[142:143], v211 offset:53760\n" \
"s_waitcnt lgkmcnt(8)\n" \
"v_mfma_f32_16x16x32_bf16 v[140:143], a[88:91], a[24:27], v[140:143]\n" \
"buffer_load_dwordx4 v212, %[srdk], s48 offen offset:1024 lds\n" \
"v_mfma_f32_16x16x32_bf16 v[140:143], a[92:95], a[28:31], v[140:143]\n" \
"v_exp_f32_e32 v135, v135\n" \
"v_mfma_f32_16x16x32_bf16 v[80:83], a[144:147], v[216:219], v[80:83]\n" \
"ds_read_b64_tr_b16 a[144:145], %[vvb] offset:50176\n" \
"ds_read_b64_tr_b16 a[146:147], %[vvb] offset:54272\n" \
"v_mfma_f32_16x16x32_bf16 v[80:83], a[176:179], v[200:203], v[80:83]\n" \
"v_exp_f32_e64 v136, v136\n" \
"v_mfma_f32_16x16x32_bf16 v[84:87], a[148:151], v[216:219], v[84:87]\n" \
"v_cvt_pk_bf16_f32 v186, v132, v133\n" \
"v_cvt_pk_bf16_f32 v187, v134, v135\n" \
"v_mfma_f32_16x16x32_bf16 v[84:87], a[180:183], v[200:203], v[84:87]\n" \
"v_exp_f32_e64 v137, v137\n" \
"v_mfma_f32_16x16x32_bf16 v[88:91], a[152:155], v[216:219], v[88:91]\n" \
"v_exp_f32_e64 v138, v138\n" \
"v_mfma_f32_16x16x32_bf16 v[88:91], a[184:187], v[200:203], v[88:91]\n" \
"ds_read_b64_tr_b16 a[148:149], v211 offset:50176\n" \
"ds_read_b64_tr_b16 a[150:151], v211 offset:54272\n" \
"v_mfma_f32_16x16x32_bf16 v[92:95], a[156:159], v[216:219], v[92:95]\n" \
"v_exp_f32_e64 v139, v139\n" \
"v_mfma_f32_16x16x32_bf16 v[92:95], a[188:191], v[200:203], v[92:95]\n" \
"ds_read_b64_tr_b16 a[152:153], %[vvb] offset:50688\n" \
"global_load_dword v237, %[vct], %[pct]\n" \
"v_mfma_f32_16x16x32_bf16 v[144:147], a[64:67], a[32:35], v[168:171]\n" \
"v_exp_f32_e64 v140, v140\n" \
"v_mfma_f32_16x16x32_bf16 v[144:147], a[68:71], a[36:39], v[144:147]\n" \
"ds_read_b64_tr_b16 a[154:155], %[vvb] offset:54784\n" \
"ds_read_b64_tr_b16 a[156:157], v211 offset:50688\n" \
"v_mfma_f32_16x16x32_bf16 v[144:147], a[72:75], a[40:43], v[144:147]\n" \
"v_exp_f32_e64 v141, v141\n" \
"v_mfma_f32_16x16x32_bf16 v[144:147], a[76:79], a[44:47], v[144:147]\n" \
"v_cvt_pk_bf16_f32 v188, v136, v137\n" \
"v_cvt_pk_bf16_f32 v189, v138, v139\n" \
"v_mfma_f32_16x16x32_bf16 v[148:151], a[80:83], a[32:35], v[168:171]\n" \
"v_exp_f32_e64 v142, v142\n" \
"v_mfma_f32_16x16x32_bf16 v[148:151], a[84:87], a[36:39], v[148:151]\n" \
"ds_read_b64_tr_b16 a[158:159], v211 offset:54784\n" \
"s_waitcnt lgkmcnt(8)\n" \
"v_mfma_f32_16x16x32_bf16 v[148:151], a[88:91], a[40:43], v[148:151]\n" \
"buffer_load_dwordx4 v213, %[srdk], s48 offen offset:2048 lds\n" \
"v_mfma_f32_16x16x32_bf16 v[148:151], a[92:95], a[44:47], v[148:151]\n" \
"v_exp_f32_e32 v143, v143\n" \
"v_mfma_f32_16x16x32_bf16 v[0:3], a[160:163], v[192:195], v[0:3]\n" \
"ds_read_b128 a[96:99], %[kb0] offset:57344\n" \
"v_mfma_f32_16x16x32_bf16 v[0:3], a[128:131], v[184:187], v[0:3]\n" \
"v_exp_f32_e64 v144, v144\n" \
"v_mfma_f32_16x16x32_bf16 v[4:7], a[164:167], v[192:195], v[4:7]\n" \
"v_cvt_pk_bf16_f32 v190, v140, v141\n" \
"v_cvt_pk_bf16_f32 v191, v142, v143\n" \
"v_mfma_f32_16x16x32_bf16 v[4:7], a[132:135], v[184:187], v[4:7]\n" \
"v_exp_f32_e64 v145, v145\n" \
"v_mfma_f32_16x16x32_bf16 v[8:11], a[168:171], v[192:195], v[8:11]\n" \
"ds_read_b128 a[100:103], v208 offset:57344\n" \
"v_mfma_f32_16x16x32_bf16 v[8:11], a[136:139], v[184:187], v[8:11]\n" \
"v_exp_f32_e64 v146, v146\n" \
"v_mfma_f32_16x16x32_bf16 v[12:15], a[172:175], v[192:195], v[12:15]\n" \
"ds_read_b128 a[104:107], v209 offset:57344\n" \
"v_mfma_f32_16x16x32_bf16 v[12:15], a[140:143], v[184:187], v[12:15]\n" \
"v_exp_f32_e64 v147, v147\n" \
"v_smfmac_f32_16x16x64_bf16 v[180:183], a[192:195], v[200:207], v228\n" \
"ds_read_b128 a[108:111], v210 offset:57344\n" \
"v_mfma_f32_16x16x32_bf16 v[152:155], a[64:67], a[48:51], v[172:175]\n" \
"v_exp_f32_e64 v148, v148\n" \
"v_mfma_f32_16x16x32_bf16 v[152:155], a[68:71], a[52:55], v[152:155]\n" \
"ds_read_b128 a[112:115], %[kb0] offset:61440\n" \
"v_mfma_f32_16x16x32_bf16 v[152:155], a[72:75], a[56:59], v[152:155]\n" \
"v_exp_f32_e64 v149, v149\n" \
"v_mfma_f32_16x16x32_bf16 v[152:155], a[76:79], a[60:63], v[152:155]\n" \
"v_cvt_pk_bf16_f32 v216, v144, v145\n" \
"v_cvt_pk_bf16_f32 v217, v146, v147\n" \
"v_mfma_f32_16x16x32_bf16 v[156:159], a[80:83], a[48:51], v[172:175]\n" \
"v_exp_f32_e64 v150, v150\n" \
"v_mfma_f32_16x16x32_bf16 v[156:159], a[84:87], a[52:55], v[156:159]\n" \
"ds_read_b128 a[116:119], v208 offset:61440\n" \
"s_waitcnt lgkmcnt(6)\n" \
"v_mfma_f32_16x16x32_bf16 v[156:159], a[88:91], a[56:59], v[156:159]\n" \
"buffer_load_dwordx4 v214, %[srdk], s48 offen offset:3072 lds\n" \
"v_mfma_f32_16x16x32_bf16 v[156:159], a[92:95], a[60:63], v[156:159]\n" \
"v_exp_f32_e32 v151, v151\n" \
"v_mfma_f32_16x16x32_bf16 v[16:19], a[176:179], v[192:195], v[16:19]\n" \
"ds_read_b128 a[120:123], v209 offset:61440\n" \
"v_mfma_f32_16x16x32_bf16 v[16:19], a[144:147], v[184:187], v[16:19]\n" \
"v_exp_f32_e64 v152, v152\n" \
"v_mfma_f32_16x16x32_bf16 v[20:23], a[180:183], v[192:195], v[20:23]\n" \
"v_cvt_pk_bf16_f32 v218, v148, v149\n" \
"v_cvt_pk_bf16_f32 v219, v150, v151\n" \
"v_mfma_f32_16x16x32_bf16 v[20:23], a[148:151], v[184:187], v[20:23]\n" \
"v_exp_f32_e64 v153, v153\n" \
"v_mfma_f32_16x16x32_bf16 v[24:27], a[184:187], v[192:195], v[24:27]\n" \
"v_exp_f32_e64 v154, v154\n" \
"v_mfma_f32_16x16x32_bf16 v[24:27], a[152:155], v[184:187], v[24:27]\n" \
"ds_read_b128 a[124:127], v210 offset:61440\n" \
"s_add_u32 s48, s48, 0x4000\n" \
"s_mov_b32 m0, s44\n" \
"s_addk_i32 s44, 0x400\n" \
"buffer_load_dwordx4 v229, s[64:67], s45 offen offset:0 nt lds\n" \
"s_add_u32 s45, s45, 393216\n" \
"v_mfma_f32_16x16x32_bf16 v[28:31], a[188:191], v[192:195], v[28:31]\n" \
"v_exp_f32_e64 v155, v155\n" \
"v_mfma_f32_16x16x32_bf16 v[28:31], a[156:159], v[184:187], v[28:31]\n" \
"s_waitcnt vmcnt(15) lgkmcnt(2)\n" \
"s_barrier\n" \
"v_mfma_f32_16x16x32_bf16 v[128:131], a[96:99], a[0:3], v[160:163]\n" \
"v_exp_f32_e64 v156, v156\n" \
"v_mfma_f32_16x16x32_bf16 v[128:131], a[100:103], a[4:7], v[128:131]\n" \
"s_add_u32 m0, %[m0b], 98304\n" \
"v_mfma_f32_16x16x32_bf16 v[128:131], a[104:107], a[8:11], v[128:131]\n" \
"v_exp_f32_e64 v157, v157\n" \
"v_mfma_f32_16x16x32_bf16 v[128:131], a[108:111], a[12:15], v[128:131]\n" \
"v_cvt_pk_bf16_f32 v220, v152, v153\n" \
"v_cvt_pk_bf16_f32 v221, v154, v155\n" \
"v_mfma_f32_16x16x32_bf16 v[132:135], a[112:115], a[0:3], v[160:163]\n" \
"v_exp_f32_e64 v158, v158\n" \
"v_mfma_f32_16x16x32_bf16 v[132:135], a[116:119], a[4:7], v[132:135]\n" \
"s_waitcnt lgkmcnt(0)\n" \
"v_mfma_f32_16x16x32_bf16 v[132:135], a[120:123], a[8:11], v[132:135]\n" \
"buffer_load_dwordx4 %[vd0], %[srdv], s48 offen offset:0 lds\n" \
"v_mfma_f32_16x16x32_bf16 v[132:135], a[124:127], a[12:15], v[132:135]\n" \
"v_exp_f32_e32 v159, v159\n" \
"v_mfma_f32_16x16x32_bf16 v[32:35], a[160:163], v[196:199], v[32:35]\n" \
"ds_read_b128 a[64:67], %[kb0] offset:0\n" \
"v_mfma_f32_16x16x32_bf16 v[32:35], a[128:131], v[188:191], v[32:35]\n" \
"v_exp_f32_e64 v128, v128\n" \
"v_mfma_f32_16x16x32_bf16 v[36:39], a[164:167], v[196:199], v[36:39]\n" \
"v_cvt_pk_bf16_f32 v222, v156, v157\n" \
"v_cvt_pk_bf16_f32 v223, v158, v159\n" \
"v_mfma_f32_16x16x32_bf16 v[36:39], a[132:135], v[188:191], v[36:39]\n" \
"v_exp_f32_e64 v129, v129\n" \
"v_mfma_f32_16x16x32_bf16 v[40:43], a[168:171], v[196:199], v[40:43]\n" \
"ds_read_b128 a[68:71], v208 offset:0\n" \
"v_mfma_f32_16x16x32_bf16 v[40:43], a[136:139], v[188:191], v[40:43]\n" \
"v_exp_f32_e64 v130, v130\n" \
"v_mfma_f32_16x16x32_bf16 v[44:47], a[172:175], v[196:199], v[44:47]\n" \
"ds_read_b128 a[72:75], v209 offset:0\n" \
"v_mfma_f32_16x16x32_bf16 v[44:47], a[140:143], v[188:191], v[44:47]\n" \
"v_exp_f32_e64 v131, v131\n" \
"v_smfmac_f32_16x16x64_bf16 v[176:179], a[192:195], v[184:191], v228\n" \
"v_mfma_f32_16x16x32_bf16 v[136:139], a[96:99], a[16:19], v[164:167]\n" \
"v_exp_f32_e64 v132, v132\n" \
"v_mfma_f32_16x16x32_bf16 v[136:139], a[100:103], a[20:23], v[136:139]\n" \
"ds_read_b128 a[76:79], v210 offset:0\n" \
"v_mfma_f32_16x16x32_bf16 v[136:139], a[104:107], a[24:27], v[136:139]\n" \
"v_exp_f32_e64 v133, v133\n" \
"v_mfma_f32_16x16x32_bf16 v[136:139], a[108:111], a[28:31], v[136:139]\n" \
"v_cvt_pk_bf16_f32 v192, v128, v129\n" \
"v_cvt_pk_bf16_f32 v193, v130, v131\n" \
"v_mfma_f32_16x16x32_bf16 v[140:143], a[112:115], a[16:19], v[164:167]\n" \
"v_exp_f32_e64 v134, v134\n" \
"v_mfma_f32_16x16x32_bf16 v[140:143], a[116:119], a[20:23], v[140:143]\n" \
"ds_read_b128 a[80:83], %[kb0] offset:4096\n" \
"v_mfma_f32_16x16x32_bf16 v[140:143], a[120:123], a[24:27], v[140:143]\n" \
"buffer_load_dwordx4 v215, %[srdv], s48 offen offset:1024 lds\n" \
"v_mfma_f32_16x16x32_bf16 v[140:143], a[124:127], a[28:31], v[140:143]\n" \
"v_exp_f32_e64 v135, v135\n" \
"v_mfma_f32_16x16x32_bf16 v[48:51], a[176:179], v[196:199], v[48:51]\n" \
"ds_read_b128 a[84:87], v208 offset:4096\n" \
"v_mfma_f32_16x16x32_bf16 v[48:51], a[144:147], v[188:191], v[48:51]\n" \
"v_exp_f32_e64 v136, v136\n" \
"v_mfma_f32_16x16x32_bf16 v[52:55], a[180:183], v[196:199], v[52:55]\n" \
"v_cvt_pk_bf16_f32 v194, v132, v133\n" \
"v_cvt_pk_bf16_f32 v195, v134, v135\n" \
"v_mfma_f32_16x16x32_bf16 v[52:55], a[148:151], v[188:191], v[52:55]\n" \
"v_exp_f32_e64 v137, v137\n" \
"v_mfma_f32_16x16x32_bf16 v[56:59], a[184:187], v[196:199], v[56:59]\n" \
"v_exp_f32_e64 v138, v138\n" \
"v_mfma_f32_16x16x32_bf16 v[56:59], a[152:155], v[188:191], v[56:59]\n" \
"ds_read_b128 a[88:91], v209 offset:4096\n" \
"v_mfma_f32_16x16x32_bf16 v[60:63], a[188:191], v[196:199], v[60:63]\n" \
"v_exp_f32_e64 v139, v139\n" \
"v_mfma_f32_16x16x32_bf16 v[60:63], a[156:159], v[188:191], v[60:63]\n" \
"s_cmp_lg_u32 %[wid], 2\n" \
"s_cbranch_scc1 LtA7_%=\n" \
"global_load_dword v237, v224, s[58:59]\n" \
"s_add_u32 s61, s48, 49152\n" \
"s_mov_b32 exec_hi, 0\n" \
"s_nop 0\n" \
"buffer_load_dword v237, v224, %[srdp], s61 offen\n" \
"s_mov_b32 exec_hi, -1\n" \
"s_nop 0\n" \
"LtA7_%=:\n" \
"v_mfma_f32_16x16x32_bf16 v[144:147], a[96:99], a[32:35], v[168:171]\n" \
"v_exp_f32_e64 v140, v140\n" \
"v_mfma_f32_16x16x32_bf16 v[144:147], a[100:103], a[36:39], v[144:147]\n" \
"ds_read_b128 a[92:95], v210 offset:4096\n" \
"v_mfma_f32_16x16x32_bf16 v[144:147], a[104:107], a[40:43], v[144:147]\n" \
"v_exp_f32_e64 v141, v141\n" \
"v_mfma_f32_16x16x32_bf16 v[144:147], a[108:111], a[44:47], v[144:147]\n" \
"v_cvt_pk_bf16_f32 v196, v136, v137\n" \
"v_cvt_pk_bf16_f32 v197, v138, v139\n" \
"v_mfma_f32_16x16x32_bf16 v[148:151], a[112:115], a[32:35], v[168:171]\n" \
"v_exp_f32_e64 v142, v142\n" \
"v_mfma_f32_16x16x32_bf16 v[148:151], a[116:119], a[36:39], v[148:151]\n" \
"v_mfma_f32_16x16x32_bf16 v[148:151], a[120:123], a[40:43], v[148:151]\n" \
"buffer_load_dwordx4 %[vd0], %[srdv], s48 offen offset:2048 lds\n" \
"v_mfma_f32_16x16x32_bf16 v[148:151], a[124:127], a[44:47], v[148:151]\n" \
"v_exp_f32_e64 v143, v143\n" \
"v_mfma_f32_16x16x32_bf16 v[96:99], a[160:163], v[204:207], v[96:99]\n" \
"ds_read_b64_tr_b16 a[160:161], %[vvb] offset:57344\n" \
"v_mfma_f32_16x16x32_bf16 v[96:99], a[128:131], v[220:223], v[96:99]\n" \
"v_exp_f32_e64 v144, v144\n" \
"v_mfma_f32_16x16x32_bf16 v[100:103], a[164:167], v[204:207], v[100:103]\n" \
"v_cvt_pk_bf16_f32 v198, v140, v141\n" \
"v_cvt_pk_bf16_f32 v199, v142, v143\n" \
"v_mfma_f32_16x16x32_bf16 v[100:103], a[132:135], v[220:223], v[100:103]\n" \
"v_exp_f32_e64 v145, v145\n" \
"v_mfma_f32_16x16x32_bf16 v[104:107], a[168:171], v[204:207], v[104:107]\n" \
"ds_read_b64_tr_b16 a[162:163], %[vvb] offset:61440\n" \
"ds_read_b64_tr_b16 a[164:165], v211 offset:57344\n" \
"v_mfma_f32_16x16x32_bf16 v[104:107], a[136:139], v[220:223], v[104:107]\n" \
"v_exp_f32_e64 v146, v146\n" \
"v_mfma_f32_16x16x32_bf16 v[108:111], a[172:175], v[204:207], v[108:111]\n" \
"ds_read_b64_tr_b16 a[166:167], v211 offset:61440\n" \
"v_mfma_f32_16x16x32_bf16 v[108:111], a[140:143], v[220:223], v[108:111]\n" \
"v_exp_f32_e64 v147, v147\n" \
"v_smfmac_f32_16x16x64_bf16 v[180:183], a[192:195], v[216:223], v228\n" \
"ds_read_b64_tr_b16 a[168:169], %[vvb] offset:57856\n" \
"v_mfma_f32_16x16x32_bf16 v[152:155], a[96:99], a[48:51], v[172:175]\n" \
"v_exp_f32_e64 v148, v148\n" \
"v_mfma_f32_16x16x32_bf16 v[152:155], a[100:103], a[52:55], v[152:155]\n" \
"ds_read_b64_tr_b16 a[170:171], %[vvb] offset:61952\n" \
"v_mfma_f32_16x16x32_bf16 v[152:155], a[104:107], a[56:59], v[152:155]\n" \
"v_exp_f32_e64 v149, v149\n" \
"v_mfma_f32_16x16x32_bf16 v[152:155], a[108:111], a[60:63], v[152:155]\n" \
"v_cvt_pk_bf16_f32 v200, v144, v145\n" \
"v_cvt_pk_bf16_f32 v201, v146, v147\n" \
"v_mfma_f32_16x16x32_bf16 v[156:159], a[112:115], a[48:51], v[172:175]\n" \
"v_exp_f32_e64 v150, v150\n" \
"v_mfma_f32_16x16x32_bf16 v[156:159], a[116:119], a[52:55], v[156:159]\n" \
"ds_read_b64_tr_b16 a[172:173], v211 offset:57856\n" \
"v_mfma_f32_16x16x32_bf16 v[156:159], a[120:123], a[56:59], v[156:159]\n" \
"buffer_load_dwordx4 v215, %[srdv], s48 offen offset:3072 lds\n" \
"v_mfma_f32_16x16x32_bf16 v[156:159], a[124:127], a[60:63], v[156:159]\n" \
"v_exp_f32_e64 v151, v151\n" \
"v_mfma_f32_16x16x32_bf16 v[112:115], a[176:179], v[204:207], v[112:115]\n" \
"ds_read_b64_tr_b16 a[174:175], v211 offset:61952\n" \
"ds_read_b64_tr_b16 a[176:177], %[vvb] offset:58368\n" \
"v_mfma_f32_16x16x32_bf16 v[112:115], a[144:147], v[220:223], v[112:115]\n" \
"v_exp_f32_e64 v152, v152\n" \
"v_mfma_f32_16x16x32_bf16 v[116:119], a[180:183], v[204:207], v[116:119]\n" \
"v_cvt_pk_bf16_f32 v202, v148, v149\n" \
"v_cvt_pk_bf16_f32 v203, v150, v151\n" \
"v_mfma_f32_16x16x32_bf16 v[116:119], a[148:151], v[220:223], v[116:119]\n" \
"v_exp_f32_e64 v153, v153\n" \
"v_mfma_f32_16x16x32_bf16 v[120:123], a[184:187], v[204:207], v[120:123]\n" \
"v_exp_f32_e64 v154, v154\n" \
"v_mfma_f32_16x16x32_bf16 v[120:123], a[152:155], v[220:223], v[120:123]\n" \
"ds_read_b64_tr_b16 a[178:179], %[vvb] offset:62464\n" \
"v_mfma_f32_16x16x32_bf16 v[124:127], a[188:191], v[204:207], v[124:127]\n" \
"v_exp_f32_e64 v155, v155\n" \
"v_mfma_f32_16x16x32_bf16 v[124:127], a[156:159], v[220:223], v[124:127]\n" \
"ds_read_b64_tr_b16 a[180:181], v211 offset:58368\n" \
"s_waitcnt lgkmcnt(13)\n" \
"v_mfma_f32_16x16x32_bf16 v[128:131], a[64:67], a[0:3], v[160:163]\n" \
"v_exp_f32_e32 v156, v156\n" \
"v_mfma_f32_16x16x32_bf16 v[128:131], a[68:71], a[4:7], v[128:131]\n" \
"ds_read_b64_tr_b16 a[182:183], v211 offset:62464\n" \
"s_add_u32 m0, %[m0b], 49152\n" \
"v_mfma_f32_16x16x32_bf16 v[128:131], a[72:75], a[8:11], v[128:131]\n" \
"v_exp_f32_e64 v157, v157\n" \
"v_mfma_f32_16x16x32_bf16 v[128:131], a[76:79], a[12:15], v[128:131]\n" \
"v_cvt_pk_bf16_f32 v204, v152, v153\n" \
"v_cvt_pk_bf16_f32 v205, v154, v155\n" \
"v_mfma_f32_16x16x32_bf16 v[132:135], a[80:83], a[0:3], v[160:163]\n" \
"v_exp_f32_e64 v158, v158\n" \
"v_mfma_f32_16x16x32_bf16 v[132:135], a[84:87], a[4:7], v[132:135]\n" \
"ds_read_b64_tr_b16 a[184:185], %[vvb] offset:58880\n" \
"s_waitcnt lgkmcnt(5)\n" \
"v_mfma_f32_16x16x32_bf16 v[132:135], a[88:91], a[8:11], v[132:135]\n" \
"buffer_load_dwordx4 %[kd0], %[srdk], s48 offen offset:0 lds\n" \
"v_mfma_f32_16x16x32_bf16 v[132:135], a[92:95], a[12:15], v[132:135]\n" \
"v_exp_f32_e32 v159, v159\n" \
"v_mfma_f32_16x16x32_bf16 v[64:67], a[128:131], v[216:219], v[64:67]\n" \
"ds_read_b64_tr_b16 a[186:187], %[vvb] offset:62976\n" \
"ds_read_b64_tr_b16 a[188:189], v211 offset:58880\n" \
"v_mfma_f32_16x16x32_bf16 v[64:67], a[160:163], v[200:203], v[64:67]\n" \
"v_exp_f32_e64 v128, v128\n" \
"v_mfma_f32_16x16x32_bf16 v[68:71], a[132:135], v[216:219], v[68:71]\n" \
"v_cvt_pk_bf16_f32 v206, v156, v157\n" \
"v_cvt_pk_bf16_f32 v207, v158, v159\n" \
"v_mfma_f32_16x16x32_bf16 v[68:71], a[164:167], v[200:203], v[68:71]\n" \
"v_exp_f32_e64 v129, v129\n" \
"v_mfma_f32_16x16x32_bf16 v[72:75], a[136:139], v[216:219], v[72:75]\n" \
"ds_read_b64_tr_b16 a[190:191], v211 offset:62976\n" \
"ds_read_b64_tr_b16 a[128:129], %[vvb] offset:0\n" \
"v_mfma_f32_16x16x32_bf16 v[72:75], a[168:171], v[200:203], v[72:75]\n" \
"v_exp_f32_e64 v130, v130\n" \
"v_mfma_f32_16x16x32_bf16 v[76:79], a[140:143], v[216:219], v[76:79]\n" \
"ds_read_b64_tr_b16 a[130:131], %[vvb] offset:4096\n" \
"ds_read_b64_tr_b16 a[132:133], v211 offset:0\n" \
"v_mfma_f32_16x16x32_bf16 v[76:79], a[172:175], v[200:203], v[76:79]\n" \
"v_exp_f32_e64 v131, v131\n" \
"v_smfmac_f32_16x16x64_bf16 v[176:179], a[192:195], v[192:199], v228\n" \
"ds_read_b64_tr_b16 a[134:135], v211 offset:4096\n" \
"ds_read_b64_tr_b16 a[136:137], %[vvb] offset:512\n" \
"v_mfma_f32_16x16x32_bf16 v[136:139], a[64:67], a[16:19], v[164:167]\n" \
"v_exp_f32_e64 v132, v132\n" \
"v_mfma_f32_16x16x32_bf16 v[136:139], a[68:71], a[20:23], v[136:139]\n" \
"ds_read_b64_tr_b16 a[138:139], %[vvb] offset:4608\n" \
"ds_read_b64_tr_b16 a[140:141], v211 offset:512\n" \
"v_mfma_f32_16x16x32_bf16 v[136:139], a[72:75], a[24:27], v[136:139]\n" \
"v_exp_f32_e64 v133, v133\n" \
"v_mfma_f32_16x16x32_bf16 v[136:139], a[76:79], a[28:31], v[136:139]\n" \
"v_cvt_pk_bf16_f32 v184, v128, v129\n" \
"v_cvt_pk_bf16_f32 v185, v130, v131\n" \
"v_mfma_f32_16x16x32_bf16 v[140:143], a[80:83], a[16:19], v[164:167]\n" \
"v_exp_f32_e64 v134, v134\n" \
"v_mfma_f32_16x16x32_bf16 v[140:143], a[84:87], a[20:23], v[140:143]\n" \
"ds_read_b64_tr_b16 a[142:143], v211 offset:4608\n" \
"s_waitcnt lgkmcnt(8)\n" \
"v_mfma_f32_16x16x32_bf16 v[140:143], a[88:91], a[24:27], v[140:143]\n" \
"buffer_load_dwordx4 v212, %[srdk], s48 offen offset:1024 lds\n" \
"v_mfma_f32_16x16x32_bf16 v[140:143], a[92:95], a[28:31], v[140:143]\n" \
"v_exp_f32_e32 v135, v135\n" \
"v_mfma_f32_16x16x32_bf16 v[80:83], a[144:147], v[216:219], v[80:83]\n" \
"ds_read_b64_tr_b16 a[144:145], %[vvb] offset:1024\n" \
"ds_read_b64_tr_b16 a[146:147], %[vvb] offset:5120\n" \
"v_mfma_f32_16x16x32_bf16 v[80:83], a[176:179], v[200:203], v[80:83]\n" \
"v_exp_f32_e64 v136, v136\n" \
"v_mfma_f32_16x16x32_bf16 v[84:87], a[148:151], v[216:219], v[84:87]\n" \
"v_cvt_pk_bf16_f32 v186, v132, v133\n" \
"v_cvt_pk_bf16_f32 v187, v134, v135\n" \
"v_mfma_f32_16x16x32_bf16 v[84:87], a[180:183], v[200:203], v[84:87]\n" \
"v_exp_f32_e64 v137, v137\n" \
"v_mfma_f32_16x16x32_bf16 v[88:91], a[152:155], v[216:219], v[88:91]\n" \
"v_exp_f32_e64 v138, v138\n" \
"v_mfma_f32_16x16x32_bf16 v[88:91], a[184:187], v[200:203], v[88:91]\n" \
"ds_read_b64_tr_b16 a[148:149], v211 offset:1024\n" \
"ds_read_b64_tr_b16 a[150:151], v211 offset:5120\n" \
"v_mfma_f32_16x16x32_bf16 v[92:95], a[156:159], v[216:219], v[92:95]\n" \
"v_exp_f32_e64 v139, v139\n" \
"v_mfma_f32_16x16x32_bf16 v[92:95], a[188:191], v[200:203], v[92:95]\n" \
"ds_read_b64_tr_b16 a[152:153], %[vvb] offset:1536\n" \
"buffer_store_dwordx4 a[196:199], v235, %[srdz], s62 offen offset:1024 sc1\n" \
"s_add_u32 s62, s62, 2048\n" \
"v_mfma_f32_16x16x32_bf16 v[144:147], a[64:67], a[32:35], v[168:171]\n" \
"v_exp_f32_e64 v140, v140\n" \
"v_mfma_f32_16x16x32_bf16 v[144:147], a[68:71], a[36:39], v[144:147]\n" \
"ds_read_b64_tr_b16 a[154:155], %[vvb] offset:5632\n" \
"ds_read_b64_tr_b16 a[156:157], v211 offset:1536\n" \
"v_mfma_f32_16x16x32_bf16 v[144:147], a[72:75], a[40:43], v[144:147]\n" \
"v_exp_f32_e64 v141, v141\n" \
"v_mfma_f32_16x16x32_bf16 v[144:147], a[76:79], a[44:47], v[144:147]\n" \
"v_cvt_pk_bf16_f32 v188, v136, v137\n" \
"v_cvt_pk_bf16_f32 v189, v138, v139\n" \
"v_mfma_f32_16x16x32_bf16 v[148:151], a[80:83], a[32:35], v[168:171]\n" \
"v_exp_f32_e64 v142, v142\n" \
"v_mfma_f32_16x16x32_bf16 v[148:151], a[84:87], a[36:39], v[148:151]\n" \
"ds_read_b64_tr_b16 a[158:159], v211 offset:5632\n" \
"s_waitcnt lgkmcnt(8)\n" \
"v_mfma_f32_16x16x32_bf16 v[148:151], a[88:91], a[40:43], v[148:151]\n" \
"buffer_load_dwordx4 v213, %[srdk], s48 offen offset:2048 lds\n" \
"v_mfma_f32_16x16x32_bf16 v[148:151], a[92:95], a[44:47], v[148:151]\n" \
"v_exp_f32_e32 v143, v143\n" \
"v_mfma_f32_16x16x32_bf16 v[0:3], a[160:163], v[192:195], v[0:3]\n" \
"ds_read_b128 a[96:99], %[kb0] offset:8192\n" \
"v_mfma_f32_16x16x32_bf16 v[0:3], a[128:131], v[184:187], v[0:3]\n" \
"v_exp_f32_e64 v144, v144\n" \
"v_mfma_f32_16x16x32_bf16 v[4:7], a[164:167], v[192:195], v[4:7]\n" \
"v_cvt_pk_bf16_f32 v190, v140, v141\n" \
"v_cvt_pk_bf16_f32 v191, v142, v143\n" \
"v_mfma_f32_16x16x32_bf16 v[4:7], a[132:135], v[184:187], v[4:7]\n" \
"v_exp_f32_e64 v145, v145\n" \
"v_mfma_f32_16x16x32_bf16 v[8:11], a[168:171], v[192:195], v[8:11]\n" \
"ds_read_b128 a[100:103], v208 offset:8192\n" \
"v_mfma_f32_16x16x32_bf16 v[8:11], a[136:139], v[184:187], v[8:11]\n" \
"v_exp_f32_e64 v146, v146\n" \
"v_mfma_f32_16x16x32_bf16 v[12:15], a[172:175], v[192:195], v[12:15]\n" \
"ds_read_b128 a[104:107], v209 offset:8192\n" \
"v_mfma_f32_16x16x32_bf16 v[12:15], a[140:143], v[184:187], v[12:15]\n" \
"v_exp_f32_e64 v147, v147\n" \
"v_smfmac_f32_16x16x64_bf16 v[180:183], a[192:195], v[200:207], v228\n" \
"ds_read_b128 a[108:111], v210 offset:8192\n" \
"v_mfma_f32_16x16x32_bf16 v[152:155], a[64:67], a[48:51], v[172:175]\n" \
"v_exp_f32_e64 v148, v148\n" \
"v_mfma_f32_16x16x32_bf16 v[152:155], a[68:71], a[52:55], v[152:155]\n" \
"ds_read_b128 a[112:115], %[kb0] offset:12288\n" \
"v_mfma_f32_16x16x32_bf16 v[152:155], a[72:75], a[56:59], v[152:155]\n" \
"v_exp_f32_e64 v149, v149\n" \
"v_mfma_f32_16x16x32_bf16 v[152:155], a[76:79], a[60:63], v[152:155]\n" \
"v_cvt_pk_bf16_f32 v216, v144, v145\n" \
"v_cvt_pk_bf16_f32 v217, v146, v147\n" \
"v_mfma_f32_16x16x32_bf16 v[156:159], a[80:83], a[48:51], v[172:175]\n" \
"v_exp_f32_e64 v150, v150\n" \
"v_mfma_f32_16x16x32_bf16 v[156:159], a[84:87], a[52:55], v[156:159]\n" \
"ds_read_b128 a[116:119], v208 offset:12288\n" \
"s_waitcnt lgkmcnt(6)\n" \
"v_mfma_f32_16x16x32_bf16 v[156:159], a[88:91], a[56:59], v[156:159]\n" \
"buffer_load_dwordx4 v214, %[srdk], s48 offen offset:3072 lds\n" \
"v_mfma_f32_16x16x32_bf16 v[156:159], a[92:95], a[60:63], v[156:159]\n" \
"v_exp_f32_e32 v151, v151\n" \
"v_mfma_f32_16x16x32_bf16 v[16:19], a[176:179], v[192:195], v[16:19]\n" \
"ds_read_b128 a[120:123], v209 offset:12288\n" \
"v_mfma_f32_16x16x32_bf16 v[16:19], a[144:147], v[184:187], v[16:19]\n" \
"v_exp_f32_e64 v152, v152\n" \
"v_mfma_f32_16x16x32_bf16 v[20:23], a[180:183], v[192:195], v[20:23]\n" \
"v_cvt_pk_bf16_f32 v218, v148, v149\n" \
"v_cvt_pk_bf16_f32 v219, v150, v151\n" \
"v_mfma_f32_16x16x32_bf16 v[20:23], a[148:151], v[184:187], v[20:23]\n" \
"v_exp_f32_e64 v153, v153\n" \
"v_mfma_f32_16x16x32_bf16 v[24:27], a[184:187], v[192:195], v[24:27]\n" \
"v_exp_f32_e64 v154, v154\n" \
"v_mfma_f32_16x16x32_bf16 v[24:27], a[152:155], v[184:187], v[24:27]\n" \
"ds_read_b128 a[124:127], v210 offset:12288\n" \
"s_add_u32 s48, s48, 0x4000\n" \
"v_mfma_f32_16x16x32_bf16 v[28:31], a[188:191], v[192:195], v[28:31]\n" \
"v_exp_f32_e64 v155, v155\n" \
"v_mfma_f32_16x16x32_bf16 v[28:31], a[156:159], v[184:187], v[28:31]\n" \
"s_waitcnt vmcnt(15) lgkmcnt(2)\n" \
"s_barrier\n" \
"v_mfma_f32_16x16x32_bf16 v[128:131], a[96:99], a[0:3], v[160:163]\n" \
"v_exp_f32_e64 v156, v156\n" \
"v_mfma_f32_16x16x32_bf16 v[128:131], a[100:103], a[4:7], v[128:131]\n" \
"s_add_u32 m0, %[m0b], 114688\n" \
"v_mfma_f32_16x16x32_bf16 v[128:131], a[104:107], a[8:11], v[128:131]\n" \
"v_exp_f32_e64 v157, v157\n" \
"v_mfma_f32_16x16x32_bf16 v[128:131], a[108:111], a[12:15], v[128:131]\n" \
"v_cvt_pk_bf16_f32 v220, v152, v153\n" \
"v_cvt_pk_bf16_f32 v221, v154, v155\n" \
"v_mfma_f32_16x16x32_bf16 v[132:135], a[112:115], a[0:3], v[160:163]\n" \
"v_exp_f32_e64 v158, v158\n" \
"v_mfma_f32_16x16x32_bf16 v[132:135], a[116:119], a[4:7], v[132:135]\n" \
"s_waitcnt lgkmcnt(0)\n" \
"v_mfma_f32_16x16x32_bf16 v[132:135], a[120:123], a[8:11], v[132:135]\n" \
"buffer_load_dwordx4 %[vd0], %[srdv], s48 offen offset:0 lds\n" \
"v_mfma_f32_16x16x32_bf16 v[132:135], a[124:127], a[12:15], v[132:135]\n" \
"v_exp_f32_e32 v159, v159\n" \
"v_mfma_f32_16x16x32_bf16 v[32:35], a[160:163], v[196:199], v[32:35]\n" \
"ds_read_b128 a[64:67], %[kb0] offset:16384\n" \
"v_mfma_f32_16x16x32_bf16 v[32:35], a[128:131], v[188:191], v[32:35]\n" \
"v_exp_f32_e64 v128, v128\n" \
"v_mfma_f32_16x16x32_bf16 v[36:39], a[164:167], v[196:199], v[36:39]\n" \
"v_cvt_pk_bf16_f32 v222, v156, v157\n" \
"v_cvt_pk_bf16_f32 v223, v158, v159\n" \
"v_mfma_f32_16x16x32_bf16 v[36:39], a[132:135], v[188:191], v[36:39]\n" \
"v_exp_f32_e64 v129, v129\n" \
"v_mfma_f32_16x16x32_bf16 v[40:43], a[168:171], v[196:199], v[40:43]\n" \
"ds_read_b128 a[68:71], v208 offset:16384\n" \
"v_mfma_f32_16x16x32_bf16 v[40:43], a[136:139], v[188:191], v[40:43]\n" \
"v_exp_f32_e64 v130, v130\n" \
"v_mfma_f32_16x16x32_bf16 v[44:47], a[172:175], v[196:199], v[44:47]\n" \
"ds_read_b128 a[72:75], v209 offset:16384\n" \
"v_mfma_f32_16x16x32_bf16 v[44:47], a[140:143], v[188:191], v[44:47]\n" \
"v_exp_f32_e64 v131, v131\n" \
"v_smfmac_f32_16x16x64_bf16 v[176:179], a[192:195], v[184:191], v228\n" \
"v_mfma_f32_16x16x32_bf16 v[136:139], a[96:99], a[16:19], v[164:167]\n" \
"v_exp_f32_e64 v132, v132\n" \
"v_mfma_f32_16x16x32_bf16 v[136:139], a[100:103], a[20:23], v[136:139]\n" \
"ds_read_b128 a[76:79], v210 offset:16384\n" \
"v_mfma_f32_16x16x32_bf16 v[136:139], a[104:107], a[24:27], v[136:139]\n" \
"v_exp_f32_e64 v133, v133\n" \
"v_mfma_f32_16x16x32_bf16 v[136:139], a[108:111], a[28:31], v[136:139]\n" \
"v_cvt_pk_bf16_f32 v192, v128, v129\n" \
"v_cvt_pk_bf16_f32 v193, v130, v131\n" \
"v_mfma_f32_16x16x32_bf16 v[140:143], a[112:115], a[16:19], v[164:167]\n" \
"v_exp_f32_e64 v134, v134\n" \
"v_mfma_f32_16x16x32_bf16 v[140:143], a[116:119], a[20:23], v[140:143]\n" \
"ds_read_b128 a[80:83], %[kb0] offset:20480\n" \
"v_mfma_f32_16x16x32_bf16 v[140:143], a[120:123], a[24:27], v[140:143]\n" \
"buffer_load_dwordx4 v215, %[srdv], s48 offen offset:1024 lds\n" \
"v_mfma_f32_16x16x32_bf16 v[140:143], a[124:127], a[28:31], v[140:143]\n" \
".if . - adaln_body != 20480\n" \
".error \"size model\"\n" \
".endif\n" \
"Lk4_%=:\n" \
"s_cbranch_execz Lk5_%=\n" \
"s_nop 0\n" \
"v_exp_f32_e64 v135, v135\n" \
"v_mfma_f32_16x16x32_bf16 v[48:51], a[176:179], v[196:199], v[48:51]\n" \
"ds_read_b128 a[84:87], v208 offset:20480\n" \
"v_mfma_f32_16x16x32_bf16 v[48:51], a[144:147], v[188:191], v[48:51]\n" \
"v_exp_f32_e64 v136, v136\n" \
"v_mfma_f32_16x16x32_bf16 v[52:55], a[180:183], v[196:199], v[52:55]\n" \
"v_cvt_pk_bf16_f32 v194, v132, v133\n" \
"v_cvt_pk_bf16_f32 v195, v134, v135\n" \
"v_mfma_f32_16x16x32_bf16 v[52:55], a[148:151], v[188:191], v[52:55]\n" \
"v_exp_f32_e64 v137, v137\n" \
"v_mfma_f32_16x16x32_bf16 v[56:59], a[184:187], v[196:199], v[56:59]\n" \
"v_exp_f32_e64 v138, v138\n" \
"v_mfma_f32_16x16x32_bf16 v[56:59], a[152:155], v[188:191], v[56:59]\n" \
"ds_read_b128 a[88:91], v209 offset:20480\n" \
"v_mfma_f32_16x16x32_bf16 v[60:63], a[188:191], v[196:199], v[60:63]\n" \
"v_exp_f32_e64 v139, v139\n" \
"v_mfma_f32_16x16x32_bf16 v[60:63], a[156:159], v[188:191], v[60:63]\n" \
"s_cmp_lg_u32 %[wid], 3\n" \
"s_cbranch_scc1 LtA1_%=\n" \
"global_load_dword v237, v224, s[58:59]\n" \
"s_add_u32 s61, s48, 49152\n" \
"s_mov_b32 exec_hi, 0\n" \
"s_nop 0\n" \
"buffer_load_dword v237, v224, %[srdp], s61 offen\n" \
"s_mov_b32 exec_hi, -1\n" \
"s_nop 0\n" \
"LtA1_%=:\n" \
"v_mfma_f32_16x16x32_bf16 v[144:147], a[96:99], a[32:35], v[168:171]\n" \
"v_exp_f32_e64 v140, v140\n" \
"v_mfma_f32_16x16x32_bf16 v[144:147], a[100:103], a[36:39], v[144:147]\n" \
"ds_read_b128 a[92:95], v210 offset:20480\n" \
"v_mfma_f32_16x16x32_bf16 v[144:147], a[104:107], a[40:43], v[144:147]\n" \
"v_exp_f32_e64 v141, v141\n" \
"v_mfma_f32_16x16x32_bf16 v[144:147], a[108:111], a[44:47], v[144:147]\n" \
"v_cvt_pk_bf16_f32 v196, v136, v137\n" \
"v_cvt_pk_bf16_f32 v197, v138, v139\n" \
"v_mfma_f32_16x16x32_bf16 v[148:151], a[112:115], a[32:35], v[168:171]\n" \
"v_exp_f32_e64 v142, v142\n" \
"v_mfma_f32_16x16x32_bf16 v[148:151], a[116:119], a[36:39], v[148:151]\n" \
"v_mfma_f32_16x16x32_bf16 v[148:151], a[120:123], a[40:43], v[148:151]\n" \
"buffer_load_dwordx4 %[vd0], %[srdv], s48 offen offset:2048 lds\n" \
"v_mfma_f32_16x16x32_bf16 v[148:151], a[124:127], a[44:47], v[148:151]\n" \
"v_exp_f32_e64 v143, v143\n" \
"v_mfma_f32_16x16x32_bf16 v[96:99], a[160:163], v[204:207], v[96:99]\n" \
"ds_read_b64_tr_b16 a[160:161], %[vvb] offset:8192\n" \
"v_mfma_f32_16x16x32_bf16 v[96:99], a[128:131], v[220:223], v[96:99]\n" \
"v_exp_f32_e64 v144, v144\n" \
"v_mfma_f32_16x16x32_bf16 v[100:103], a[164:167], v[204:207], v[100:103]\n" \
"v_cvt_pk_bf16_f32 v198, v140, v141\n" \
"v_cvt_pk_bf16_f32 v199, v142, v143\n" \
"v_mfma_f32_16x16x32_bf16 v[100:103], a[132:135], v[220:223], v[100:103]\n" \
"v_exp_f32_e64 v145, v145\n" \
"v_mfma_f32_16x16x32_bf16 v[104:107], a[168:171], v[204:207], v[104:107]\n" \
"ds_read_b64_tr_b16 a[162:163], %[vvb] offset:12288\n" \
"ds_read_b64_tr_b16 a[164:165], v211 offset:8192\n" \
"v_mfma_f32_16x16x32_bf16 v[104:107], a[136:139], v[220:223], v[104:107]\n" \
"v_exp_f32_e64 v146, v146\n" \
"v_mfma_f32_16x16x32_bf16 v[108:111], a[172:175], v[204:207], v[108:111]\n" \
"ds_read_b64_tr_b16 a[166:167], v211 offset:12288\n" \
"v_mfma_f32_16x16x32_bf16 v[108:111], a[140:143], v[220:223], v[108:111]\n" \
"v_exp_f32_e64 v147, v147\n" \
"v_smfmac_f32_16x16x64_bf16 v[180:183], a[192:195], v[216:223], v228\n" \
"ds_read_b64_tr_b16 a[168:169], %[vvb] offset:8704\n" \
"v_mfma_f32_16x16x32_bf16 v[152:155], a[96:99], a[48:51], v[172:175]\n" \
"v_exp_f32_e64 v148, v148\n" \
"v_mfma_f32_16x16x32_bf16 v[152:155], a[100:103], a[52:55], v[152:155]\n" \
"ds_read_b64_tr_b16 a[170:171], %[vvb] offset:12800\n" \
"v_mfma_f32_16x16x32_bf16 v[152:155], a[104:107], a[56:59], v[152:155]\n" \
"v_exp_f32_e64 v149, v149\n" \
"v_mfma_f32_16x16x32_bf16 v[152:155], a[108:111], a[60:63], v[152:155]\n" \
"v_cvt_pk_bf16_f32 v200, v144, v145\n" \
"v_cvt_pk_bf16_f32 v201, v146, v147\n" \
"v_mfma_f32_16x16x32_bf16 v[156:159], a[112:115], a[48:51], v[172:175]\n" \
"v_exp_f32_e64 v150, v150\n" \
"v_mfma_f32_16x16x32_bf16 v[156:159], a[116:119], a[52:55], v[156:159]\n" \
"ds_read_b64_tr_b16 a[172:173], v211 offset:8704\n" \
"v_mfma_f32_16x16x32_bf16 v[156:159], a[120:123], a[56:59], v[156:159]\n" \
"buffer_load_dwordx4 v215, %[srdv], s48 offen offset:3072 lds\n" \
"v_mfma_f32_16x16x32_bf16 v[156:159], a[124:127], a[60:63], v[156:159]\n" \
"v_exp_f32_e64 v151, v151\n" \
"v_mfma_f32_16x16x32_bf16 v[112:115], a[176:179], v[204:207], v[112:115]\n" \
"ds_read_b64_tr_b16 a[174:175], v211 offset:12800\n" \
"ds_read_b64_tr_b16 a[176:177], %[vvb] offset:9216\n" \
"v_mfma_f32_16x16x32_bf16 v[112:115], a[144:147], v[220:223], v[112:115]\n" \
"v_exp_f32_e64 v152, v152\n" \
"v_mfma_f32_16x16x32_bf16 v[116:119], a[180:183], v[204:207], v[116:119]\n" \
"v_cvt_pk_bf16_f32 v202, v148, v149\n" \
"v_cvt_pk_bf16_f32 v203, v150, v151\n" \
"v_mfma_f32_16x16x32_bf16 v[116:119], a[148:151], v[220:223], v[116:119]\n" \
"v_exp_f32_e64 v153, v153\n" \
"v_mfma_f32_16x16x32_bf16 v[120:123], a[184:187], v[204:207], v[120:123]\n" \
"v_exp_f32_e64 v154, v154\n" \
"v_mfma_f32_16x16x32_bf16 v[120:123], a[152:155], v[220:223], v[120:123]\n" \
"ds_read_b64_tr_b16 a[178:179], %[vvb] offset:13312\n" \
"v_mfma_f32_16x16x32_bf16 v[124:127], a[188:191], v[204:207], v[124:127]\n" \
"v_exp_f32_e64 v155, v155\n" \
"v_mfma_f32_16x16x32_bf16 v[124:127], a[156:159], v[220:223], v[124:127]\n" \
"ds_read_b64_tr_b16 a[180:181], v211 offset:9216\n" \
"s_waitcnt lgkmcnt(13)\n" \
"s_cmp_lg_u32 s60, 4\n" \
"s_cbranch_scc1 LmA_%=\n" \
"s_nop 0\n" \
"v_readlane_b32 s64, %[tlo], 9\n" \
"v_readlane_b32 s65, %[thi], 9\n" \
"s_mov_b32 s66, %[wbytes]\n" \
"v_mov_b32_e32 v229, %[vg0]\n" \
"s_mov_b32 s44, %[m0t]\n" \
"s_mov_b32 s45, 0\n" \
"s_cmp_lg_u32 s52, 0\n" \
"s_cbranch_scc1 LmB_%=\n" \
"s_add_u32 s62, %[sin], 8192\n" \
"s_add_u32 s63, %[sin], 28672\n" \
"s_waitcnt vmcnt(13)\n" \
"s_nop 0\n" \
"ds_read_b64_tr_b16 a[232:233], %[ta0] offset:0\n" \
"ds_read_b64_tr_b16 a[234:235], %[ta0] offset:128\n" \
"ds_read_b64_tr_b16 a[236:237], %[ta0] offset:1024\n" \
"ds_read_b64_tr_b16 a[238:239], %[ta0] offset:1152\n" \
"ds_read_b64_tr_b16 a[240:241], %[ta0] offset:2048\n" \
"ds_read_b64_tr_b16 a[242:243], %[ta0] offset:2176\n" \
"ds_read_b64_tr_b16 a[244:245], %[ta0] offset:3072\n" \
"ds_read_b64_tr_b16 a[246:247], %[ta0] offset:3200\n" \
"ds_read_b64_tr_b16 a[248:249], %[ta0] offset:4096\n" \
"ds_read_b64_tr_b16 a[250:251], %[ta0] offset:4224\n" \
"ds_read_b64_tr_b16 a[252:253], %[ta0] offset:5120\n" \
"ds_read_b64_tr_b16 a[254:255], %[ta0] offset:5248\n" \
"v_add_u32_e32 v234, %[m0t], v235\n" \
"s_waitcnt lgkmcnt(8)\n" \
"v_mfma_f32_16x16x32_bf16 v[230:233], a[232:235], a[200:203], 0\n" \
"v_mfma_f32_16x16x32_bf16 v[230:233], a[236:239], a[204:207], v[230:233]\n" \
"ds_read_b64_tr_b16 a[232:233], %[ta0] offset:6144\n" \
"ds_read_b64_tr_b16 a[234:235], %[ta0] offset:6272\n" \
"ds_read_b64_tr_b16 a[236:237], %[ta0] offset:7168\n" \
"ds_read_b64_tr_b16 a[238:239], %[ta0] offset:7296\n" \
"s_waitcnt lgkmcnt(4)\n" \
"s_nop 0\n" \
"v_mfma_f32_16x16x32_bf16 v[230:233], a[240:243], a[208:211], v[230:233]\n" \
"v_mfma_f32_16x16x32_bf16 v[230:233], a[244:247], a[212:215], v[230:233]\n" \
"v_mfma_f32_16x16x32_bf16 v[230:233], a[248:251], a[216:219], v[230:233]\n" \
"v_mfma_f32_16x16x32_bf16 v[230:233], a[252:255], a[220:223], v[230:233]\n" \
"s_waitcnt lgkmcnt(0)\n" \
"s_nop 0\n" \
"v_mfma_f32_16x16x32_bf16 v[230:233], a[232:235], a[224:227], v[230:233]\n" \
"v_mfma_f32_16x16x32_bf16 v[230:233], a[236:239], a[228:231], v[230:233]\n" \
"s_nop 7\n" \
"s_nop 0\n" \
"ds_write_b128 v234, v[230:233] offset:7168\n" \
"LmB_%=:\n" \
"v_mfma_f32_16x16x32_bf16 v[128:131], a[64:67], a[0:3], v[160:163]\n" \
"v_exp_f32_e64 v156, v156\n" \
"v_mfma_f32_16x16x32_bf16 v[128:131], a[68:71], a[4:7], v[128:131]\n" \
"ds_read_b64_tr_b16 a[182:183], v211 offset:13312\n" \
"s_add_u32 m0, %[m0b], 0\n" \
"v_mfma_f32_16x16x32_bf16 v[128:131], a[72:75], a[8:11], v[128:131]\n" \
"v_exp_f32_e32 v157, v157\n" \
"v_mfma_f32_16x16x32_bf16 v[128:131], a[76:79], a[12:15], v[128:131]\n" \
"v_cvt_pk_bf16_f32 v204, v152, v153\n" \
"v_cvt_pk_bf16_f32 v205, v154, v155\n" \
"v_mfma_f32_16x16x32_bf16 v[132:135], a[80:83], a[0:3], v[160:163]\n" \
"v_exp_f32_e64 v158, v158\n" \
"v_mfma_f32_16x16x32_bf16 v[132:135], a[84:87], a[4:7], v[132:135]\n" \
"ds_read_b64_tr_b16 a[184:185], %[vvb] offset:9728\n" \
"s_waitcnt lgkmcnt(5)\n" \
"v_mfma_f32_16x16x32_bf16 v[132:135], a[88:91], a[8:11], v[132:135]\n" \
"buffer_load_dwordx4 %[kd0], %[srdk], s48 offen offset:0 lds\n" \
"v_mfma_f32_16x16x32_bf16 v[132:135], a[92:95], a[12:15], v[132:135]\n" \
"v_exp_f32_e32 v159, v159\n" \
"v_mfma_f32_16x16x32_bf16 v[64:67], a[128:131], v[216:219], v[64:67]\n" \
"ds_read_b64_tr_b16 a[186:187], %[vvb] offset:13824\n" \
"ds_read_b64_tr_b16 a[188:189], v211 offset:9728\n" \
"v_mfma_f32_16x16x32_bf16 v[64:67], a[160:163], v[200:203], v[64:67]\n" \
"v_exp_f32_e64 v128, v128\n" \
"v_mfma_f32_16x16x32_bf16 v[68:71], a[132:135], v[216:219], v[68:71]\n" \
"v_cvt_pk_bf16_f32 v206, v156, v157\n" \
"v_cvt_pk_bf16_f32 v207, v158, v159\n" \
"v_mfma_f32_16x16x32_bf16 v[68:71], a[164:167], v[200:203], v[68:71]\n" \
"v_exp_f32_e64 v129, v129\n" \
"v_mfma_f32_16x16x32_bf16 v[72:75], a[136:139], v[216:219], v[72:75]\n" \
"ds_read_b64_tr_b16 a[190:191], v211 offset:13824\n" \
"ds_read_b64_tr_b16 a[128:129], %[vvb] offset:16384\n" \
"s_add_u32 s60, s60, 1\n" \
"s_nop 0\n" \
"v_readlane_b32 s58, %[tlo], s60\n" \
"v_mfma_f32_16x16x32_bf16 v[72:75], a[168:171], v[200:203], v[72:75]\n" \
"v_exp_f32_e64 v130, v130\n" \
"v_mfma_f32_16x16x32_bf16 v[76:79], a[140:143], v[216:219], v[76:79]\n" \
"ds_read_b64_tr_b16 a[130:131], %[vvb] offset:20480\n" \
"ds_read_b64_tr_b16 a[132:133], v211 offset:16384\n" \
"v_readlane_b32 s59, %[thi], s60\n" \
"v_mfma_f32_16x16x32_bf16 v[76:79], a[172:175], v[200:203], v[76:79]\n" \
"v_exp_f32_e64 v131, v131\n" \
"v_smfmac_f32_16x16x64_bf16 v[176:179], a[192:195], v[192:199], v228\n" \
"ds_read_b64_tr_b16 a[134:135], v211 offset:20480\n" \
"ds_read_b64_tr_b16 a[136:137], %[vvb] offset:16896\n" \
"s_cmp_eq_u32 s60, 7\n" \
"s_cselect_b32 s61, 56, 1\n" \
"v_mul_u32_u24_e64 v236, s61, v224\n" \
"v_mfma_f32_16x16x32_bf16 v[136:139], a[64:67], a[16:19], v[164:167]\n" \
"v_exp_f32_e64 v132, v132\n" \
"v_mfma_f32_16x16x32_bf16 v[136:139], a[68:71], a[20:23], v[136:139]\n" \
"ds_read_b64_tr_b16 a[138:139], %[vvb] offset:20992\n" \
"ds_read_b64_tr_b16 a[140:141], v211 offset:16896\n" \
"v_mfma_f32_16x16x32_bf16 v[136:139], a[72:75], a[24:27], v[136:139]\n" \
"v_exp_f32_e64 v133, v133\n" \
"v_mfma_f32_16x16x32_bf16 v[136:139], a[76:79], a[28:31], v[136:139]\n" \
"v_cvt_pk_bf16_f32 v184, v128, v129\n" \
"v_cvt_pk_bf16_f32 v185, v130, v131\n" \
"v_mfma_f32_16x16x32_bf16 v[140:143], a[80:83], a[16:19], v[164:167]\n" \
"v_exp_f32_e64 v134, v134\n" \
"v_mfma_f32_16x16x32_bf16 v[140:143], a[84:87], a[20:23], v[140:143]\n" \
"ds_read_b64_tr_b16 a[142:143], v211 offset:20992\n" \
"s_waitcnt lgkmcnt(8)\n" \
"v_mfma_f32_16x16x32_bf16 v[140:143], a[88:91], a[24:27], v[140:143]\n" \
"buffer_load_dwordx4 v212, %[srdk], s48 offen offset:1024 lds\n" \
"v_mfma_f32_16x16x32_bf16 v[140:143], a[92:95], a[28:31], v[140:143]\n" \
"v_exp_f32_e32 v135, v135\n" \
"v_mfma_f32_16x16x32_bf16 v[80:83], a[144:147], v[216:219], v[80:83]\n" \
"ds_read_b64_tr_b16 a[144:145], %[vvb] offset:17408\n" \
"ds_read_b64_tr_b16 a[146:147], %[vvb] offset:21504\n" \
"v_mfma_f32_16x16x32_bf16 v[80:83], a[176:179], v[200:203], v[80:83]\n" \
"v_exp_f32_e64 v136, v136\n" \
"v_mfma_f32_16x16x32_bf16 v[84:87], a[148:151], v[216:219], v[84:87]\n" \
"v_cvt_pk_bf16_f32 v186, v132, v133\n" \
"v_cvt_pk_bf16_f32 v187, v134, v135\n" \
"v_mfma_f32_16x16x32_bf16 v[84:87], a[180:183], v[200:203], v[84:87]\n" \
"v_exp_f32_e64 v137, v137\n" \
"v_mfma_f32_16x16x32_bf16 v[88:91], a[152:155], v[216:219], v[88:91]\n" \
"v_exp_f32_e64 v138, v138\n" \
"v_mfma_f32_16x16x32_bf16 v[88:91], a[184:187], v[200:203], v[88:91]\n" \
"ds_read_b64_tr_b16 a[148:149], v211 offset:17408\n" \
"ds_read_b64_tr_b16 a[150:151], v211 offset:21504\n" \
"v_mfma_f32_16x16x32_bf16 v[92:95], a[156:159], v[216:219], v[92:95]\n" \
"v_exp_f32_e64 v139, v139\n" \
"v_mfma_f32_16x16x32_bf16 v[92:95], a[188:191], v[200:203], v[92:95]\n" \
"ds_read_b64_tr_b16 a[152:153], %[vvb] offset:17920\n" \
"buffer_store_dwordx4 a[196:199], v235, %[srdz], s63 offen offset:0 sc1\n" \
"v_mfma_f32_16x16x32_bf16 v[144:147], a[64:67], a[32:35], v[168:171]\n" \
"v_exp_f32_e64 v140, v140\n" \
"v_mfma_f32_16x16x32_bf16 v[144:147], a[68:71], a[36:39], v[144:147]\n" \
"ds_read_b64_tr_b16 a[154:155], %[vvb] offset:22016\n" \
"ds_read_b64_tr_b16 a[156:157], v211 offset:17920\n" \
"v_mfma_f32_16x16x32_bf16 v[144:147], a[72:75], a[40:43], v[144:147]\n" \
"v_exp_f32_e64 v141, v141\n" \
"v_mfma_f32_16x16x32_bf16 v[144:147], a[76:79], a[44:47], v[144:147]\n" \
"v_cvt_pk_bf16_f32 v188, v136, v137\n" \
"v_cvt_pk_bf16_f32 v189, v138, v139\n" \
"v_mfma_f32_16x16x32_bf16 v[148:151], a[80:83], a[32:35], v[168:171]\n" \
"v_exp_f32_e64 v142, v142\n" \
"v_mfma_f32_16x16x32_bf16 v[148:151], a[84:87], a[36:39], v[148:151]\n" \
"ds_read_b64_tr_b16 a[158:159], v211 offset:22016\n" \
"s_waitcnt lgkmcnt(8)\n" \
"v_mfma_f32_16x16x32_bf16 v[148:151], a[88:91], a[40:43], v[148:151]\n" \
"buffer_load_dwordx4 v213, %[srdk], s48 offen offset:2048 lds\n" \
"v_mfma_f32_16x16x32_bf16 v[148:151], a[92:95], a[44:47], v[148:151]\n" \
"v_exp_f32_e32 v143, v143\n" \
"v_mfma_f32_16x16x32_bf16 v[0:3], a[160:163], v[192:195], v[0:3]\n" \
"ds_read_b128 a[96:99], %[kb0] offset:24576\n" \
"v_mfma_f32_16x16x32_bf16 v[0:3], a[128:131], v[184:187], v[0:3]\n" \
"v_exp_f32_e64 v144, v144\n" \
"v_mfma_f32_16x16x32_bf16 v[4:7], a[164:167], v[192:195], v[4:7]\n" \
"v_cvt_pk_bf16_f32 v190, v140, v141\n" \
"v_cvt_pk_bf16_f32 v191, v142, v143\n" \
"v_mfma_f32_16x16x32_bf16 v[4:7], a[132:135], v[184:187], v[4:7]\n" \
"v_exp_f32_e64 v145, v145\n" \
"v_mfma_f32_16x16x32_bf16 v[8:11], a[168:171], v[192:195], v[8:11]\n" \
"ds_read_b128 a[100:103], v208 offset:24576\n" \
"v_mfma_f32_16x16x32_bf16 v[8:11], a[136:139], v[184:187], v[8:11]\n" \
"v_exp_f32_e64 v146, v146\n" \
"v_mfma_f32_16x16x32_bf16 v[12:15], a[172:175], v[192:195], v[12:15]\n" \
"ds_read_b128 a[104:107], v209 offset:24576\n" \
"v_mfma_f32_16x16x32_bf16 v[12:15], a[140:143], v[184:187], v[12:15]\n" \
"v_exp_f32_e64 v147, v147\n" \
"v_smfmac_f32_16x16x64_bf16 v[180:183], a[192:195], v[200:207], v228\n" \
"ds_read_b128 a[108:111], v210 offset:24576\n" \
"v_mfma_f32_16x16x32_bf16 v[152:155], a[64:67], a[48:51], v[172:175]\n" \
"v_exp_f32_e64 v148, v148\n" \
"v_mfma_f32_16x16x32_bf16 v[152:155], a[68:71], a[52:55], v[152:155]\n" \
"ds_read_b128 a[112:115], %[kb0] offset:28672\n" \
"v_mfma_f32_16x16x32_bf16 v[152:155], a[72:75], a[56:59], v[152:155]\n" \
"v_exp_f32_e64 v149, v149\n" \
"v_mfma_f32_16x16x32_bf16 v[152:155], a[76:79], a[60:63], v[152:155]\n" \
"v_cvt_pk_bf16_f32 v216, v144, v145\n" \
"v_cvt_pk_bf16_f32 v217, v146, v147\n" \
"v_mfma_f32_16x16x32_bf16 v[156:159], a[80:83], a[48:51], v[172:175]\n" \
"v_exp_f32_e64 v150, v150\n" \
"v_mfma_f32_16x16x32_bf16 v[156:159], a[84:87], a[52:55], v[156:159]\n" \
"ds_read_b128 a[116:119], v208 offset:28672\n" \
"s_waitcnt lgkmcnt(6)\n" \
"v_mfma_f32_16x16x32_bf16 v[156:159], a[88:91], a[56:59], v[156:159]\n" \
"buffer_load_dwordx4 v214, %[srdk], s48 offen offset:3072 lds\n" \
"v_mfma_f32_16x16x32_bf16 v[156:159], a[92:95], a[60:63], v[156:159]\n" \
"v_exp_f32_e32 v151, v151\n" \
"v_mfma_f32_16x16x32_bf16 v[16:19], a[176:179], v[192:195], v[16:19]\n" \
"ds_read_b128 a[120:123], v209 offset:28672\n" \
"v_mfma_f32_16x16x32_bf16 v[16:19], a[144:147], v[184:187], v[16:19]\n" \
"v_exp_f32_e64 v152, v152\n" \
"v_mfma_f32_16x16x32_bf16 v[20:23], a[180:183], v[192:195], v[20:23]\n" \
"v_cvt_pk_bf16_f32 v218, v148, v149\n" \
"v_cvt_pk_bf16_f32 v219, v150, v151\n" \
"v_mfma_f32_16x16x32_bf16 v[20:23], a[148:151], v[184:187], v[20:23]\n" \
"v_exp_f32_e64 v153, v153\n" \
"v_mfma_f32_16x16x32_bf16 v[24:27], a[184:187], v[192:195], v[24:27]\n" \
"v_exp_f32_e64 v154, v154\n" \
"v_mfma_f32_16x16x32_bf16 v[24:27], a[152:155], v[184:187], v[24:27]\n" \
"ds_read_b128 a[124:127], v210 offset:28672\n" \
"s_add_u32 s48, s48, 0x4000\n" \
"s_mov_b32 m0, s44\n" \
"s_addk_i32 s44, 0x400\n" \
"buffer_load_dwordx4 v229, s[64:67], s45 offen offset:0 lds\n" \
"s_add_u32 s45, s45, 229376\n" \
"v_mfma_f32_16x16x32_bf16 v[28:31], a[188:191], v[192:195], v[28:31]\n" \
"v_exp_f32_e64 v155, v155\n" \
"v_mfma_f32_16x16x32_bf16 v[28:31], a[156:159], v[184:187], v[28:31]\n" \
"s_waitcnt vmcnt(15) lgkmcnt(2)\n" \
"s_barrier\n" \
"v_mfma_f32_16x16x32_bf16 v[128:131], a[96:99], a[0:3], v[160:163]\n" \
"v_exp_f32_e64 v156, v156\n" \
"v_mfma_f32_16x16x32_bf16 v[128:131], a[100:103], a[4:7], v[128:131]\n" \
"s_add_u32 m0, %[m0b], 65536\n" \
"v_mfma_f32_16x16x32_bf16 v[128:131], a[104:107], a[8:11], v[128:131]\n" \
"v_exp_f32_e64 v157, v157\n" \
"v_mfma_f32_16x16x32_bf16 v[128:131], a[108:111], a[12:15], v[128:131]\n" \
"v_cvt_pk_bf16_f32 v220, v152, v153\n" \
"v_cvt_pk_bf16_f32 v221, v154, v155\n" \
"v_mfma_f32_16x16x32_bf16 v[132:135], a[112:115], a[0:3], v[160:163]\n" \
"v_exp_f32_e64 v158, v158\n" \
"v_mfma_f32_16x16x32_bf16 v[132:135], a[116:119], a[4:7], v[132:135]\n" \
"s_cmp_lg_u32 s60, 5\n" \
"s_cbranch_scc1 Lsjb_%=\n" \
"ds_read_b32 v230, %[rd0]\n" \
"v_readlane_b32 s55, %[thi], 15\n" \
"v_lshrrev_b32_e32 v234, 5, v224\n" \
"s_waitcnt lgkmcnt(0)\n" \
"v_add_f32_dpp v230, v230, v230 row_ror:4 row_mask:0xf bank_mask:0xf\n" \
"s_nop 1\n" \
"v_add_f32_dpp v230, v230, v230 row_ror:8 row_mask:0xf bank_mask:0xf\n" \
"v_cvt_pk_bf16_f32 v230, v230, v230\n" \
"v_lshlrev_b32_e32 v230, 16, v230\n" \
"v_add_f32_e64 v230, s55, v230\n" \
"v_cvt_pk_bf16_f32 v230, v230, v230\n" \
"v_readlane_b32 s55, %[tlo], 15\n" \
"v_lshl_or_b32 v230, v230, 16, 1\n" \
"s_cmp_lg_u32 s52, 0\n" \
"s_cselect_b32 s55, 0x80000000, s55\n" \
"s_mov_b64 exec, 15\n" \
"buffer_store_dword v230, v234, %[srdz], s55 offen sc1\n" \
"s_mov_b64 exec, -1\n" \
"Lsjb_%=:\n" \
"v_mfma_f32_16x16x32_bf16 v[132:135], a[120:123], a[8:11], v[132:135]\n" \
"buffer_load_dwordx4 %[vd0], %[srdv], s48 offen offset:0 lds\n" \
"v_mfma_f32_16x16x32_bf16 v[132:135], a[124:127], a[12:15], v[132:135]\n" \
"v_exp_f32_e32 v159, v159\n" \
"v_mfma_f32_16x16x32_bf16 v[32:35], a[160:163], v[196:199], v[32:35]\n" \
"ds_read_b128 a[64:67], %[kb0] offset:32768\n" \
"v_mfma_f32_16x16x32_bf16 v[32:35], a[128:131], v[188:191], v[32:35]\n" \
"v_exp_f32_e64 v128, v128\n" \
"v_mfma_f32_16x16x32_bf16 v[36:39], a[164:167], v[196:199], v[36:39]\n" \
"v_cvt_pk_bf16_f32 v222, v156, v157\n" \
"v_cvt_pk_bf16_f32 v223, v158, v159\n" \
"v_mfma_f32_16x16x32_bf16 v[36:39], a[132:135], v[188:191], v[36:39]\n" \
"v_exp_f32_e64 v129, v129\n" \
"v_mfma_f32_16x16x32_bf16 v[40:43], a[168:171], v[196:199], v[40:43]\n" \
"ds_read_b128 a[68:71], v208 offset:32768\n" \
"v_mfma_f32_16x16x32_bf16 v[40:43], a[136:139], v[188:191], v[40:43]\n" \
"v_exp_f32_e64 v130, v130\n" \
"v_mfma_f32_16x16x32_bf16 v[44:47], a[172:175], v[196:199], v[44:47]\n" \
"ds_read_b128 a[72:75], v209 offset:32768\n" \
"v_mfma_f32_16x16x32_bf16 v[44:47], a[140:143], v[188:191], v[44:47]\n" \
"v_exp_f32_e64 v131, v131\n" \
"v_smfmac_f32_16x16x64_bf16 v[176:179], a[192:195], v[184:191], v228\n" \
"v_mfma_f32_16x16x32_bf16 v[136:139], a[96:99], a[16:19], v[164:167]\n" \
"v_exp_f32_e64 v132, v132\n" \
"v_mfma_f32_16x16x32_bf16 v[136:139], a[100:103], a[20:23], v[136:139]\n" \
"ds_read_b128 a[76:79], v210 offset:32768\n" \
"v_mfma_f32_16x16x32_bf16 v[136:139], a[104:107], a[24:27], v[136:139]\n" \
"v_exp_f32_e64 v133, v133\n" \
"v_mfma_f32_16x16x32_bf16 v[136:139], a[108:111], a[28:31], v[136:139]\n" \
"v_cvt_pk_bf16_f32 v192, v128, v129\n" \
"v_cvt_pk_bf16_f32 v193, v130, v131\n" \
"v_mfma_f32_16x16x32_bf16 v[140:143], a[112:115], a[16:19], v[164:167]\n" \
"v_exp_f32_e64 v134, v134\n" \
"v_mfma_f32_16x16x32_bf16 v[140:143], a[116:119], a[20:23], v[140:143]\n" \
"ds_read_b128 a[80:83], %[kb0] offset:36864\n" \
"v_mfma_f32_16x16x32_bf16 v[140:143], a[120:123], a[24:27], v[140:143]\n" \
"buffer_load_dwordx4 v215, %[srdv], s48 offen offset:1024 lds\n" \
"v_mfma_f32_16x16x32_bf16 v[140:143], a[124:127], a[28:31], v[140:143]\n" \
"v_exp_f32_e64 v135, v135\n" \
"v_mfma_f32_16x16x32_bf16 v[48:51], a[176:179], v[196:199], v[48:51]\n" \
"ds_read_b128 a[84:87], v208 offset:36864\n" \
"v_mfma_f32_16x16x32_bf16 v[48:51], a[144:147], v[188:191], v[48:51]\n" \
"v_exp_f32_e64 v136, v136\n" \
"v_mfma_f32_16x16x32_bf16 v[52:55], a[180:183], v[196:199], v[52:55]\n" \
"v_cvt_pk_bf16_f32 v194, v132, v133\n" \
"v_cvt_pk_bf16_f32 v195, v134, v135\n" \
"v_mfma_f32_16x16x32_bf16 v[52:55], a[148:151], v[188:191], v[52:55]\n" \
"v_exp_f32_e64 v137, v137\n" \
"v_mfma_f32_16x16x32_bf16 v[56:59], a[184:187], v[196:199], v[56:59]\n" \
"v_exp_f32_e64 v138, v138\n" \
"v_mfma_f32_16x16x32_bf16 v[56:59], a[152:155], v[188:191], v[56:59]\n" \
"ds_read_b128 a[88:91], v209 offset:36864\n" \
"v_mfma_f32_16x16x32_bf16 v[60:63], a[188:191], v[196:199], v[60:63]\n" \
"v_exp_f32_e64 v139, v139\n" \
"v_mfma_f32_16x16x32_bf16 v[60:63], a[156:159], v[188:191], v[60:63]\n" \
"s_cmp_lg_u32 %[wid], 0\n" \
"s_cbranch_scc1 LtB3_%=\n" \
"global_load_dword v237, v236, s[58:59]\n" \
"s_add_u32 s61, s48, 49152\n" \
"s_mov_b32 exec_hi, 0\n" \
"s_nop 0\n" \
"buffer_load_dword v237, v224, %[srdp], s61 offen\n" \
"s_mov_b32 exec_hi, -1\n" \
"s_nop 0\n" \
"LtB3_%=:\n" \
"v_mfma_f32_16x16x32_bf16 v[144:147], a[96:99], a[32:35], v[168:171]\n" \
"v_exp_f32_e64 v140, v140\n" \
"v_mfma_f32_16x16x32_bf16 v[144:147], a[100:103], a[36:39], v[144:147]\n" \
"ds_read_b128 a[92:95], v210 offset:36864\n" \
"v_mfma_f32_16x16x32_bf16 v[144:147], a[104:107], a[40:43], v[144:147]\n" \
"v_exp_f32_e64 v141, v141\n" \
"v_mfma_f32_16x16x32_bf16 v[144:147], a[108:111], a[44:47], v[144:147]\n" \
"v_cvt_pk_bf16_f32 v196, v136, v137\n" \
"v_cvt_pk_bf16_f32 v197, v138, v139\n" \
"v_mfma_f32_16x16x32_bf16 v[148:151], a[112:115], a[32:35], v[168:171]\n" \
"v_exp_f32_e64 v142, v142\n" \
"v_mfma_f32_16x16x32_bf16 v[148:151], a[116:119], a[36:39], v[148:151]\n" \
"v_mfma_f32_16x16x32_bf16 v[148:151], a[120:123], a[40:43], v[148:151]\n" \
"buffer_load_dwordx4 %[vd0], %[srdv], s48 offen offset:2048 lds\n" \
"v_mfma_f32_16x16x32_bf16 v[148:151], a[124:127], a[44:47], v[148:151]\n" \
"v_exp_f32_e64 v143, v143\n" \
"v_mfma_f32_16x16x32_bf16 v[96:99], a[160:163], v[204:207], v[96:99]\n" \
"ds_read_b64_tr_b16 a[160:161], %[vvb] offset:24576\n" \
"v_mfma_f32_16x16x32_bf16 v[96:99], a[128:131], v[220:223], v[96:99]\n" \
"v_exp_f32_e64 v144, v144\n" \
"v_mfma_f32_16x16x32_bf16 v[100:103], a[164:167], v[204:207], v[100:103]\n" \
"v_cvt_pk_bf16_f32 v198, v140, v141\n" \
"v_cvt_pk_bf16_f32 v199, v142, v143\n" \
"v_mfma_f32_16x16x32_bf16 v[100:103], a[132:135], v[220:223], v[100:103]\n" \
"v_exp_f32_e64 v145, v145\n" \
"v_mfma_f32_16x16x32_bf16 v[104:107], a[168:171], v[204:207], v[104:107]\n" \
"ds_read_b64_tr_b16 a[162:163], %[vvb] offset:28672\n" \
"ds_read_b64_tr_b16 a[164:165], v211 offset:24576\n" \
"v_mfma_f32_16x16x32_bf16 v[104:107], a[136:139], v[220:223], v[104:107]\n" \
"v_exp_f32_e64 v146, v146\n" \
"v_mfma_f32_16x16x32_bf16 v[108:111], a[172:175], v[204:207], v[108:111]\n" \
"ds_read_b64_tr_b16 a[166:167], v211 offset:28672\n" \
"v_mfma_f32_16x16x32_bf16 v[108:111], a[140:143], v[220:223], v[108:111]\n" \
"v_exp_f32_e64 v147, v147\n" \
"v_smfmac_f32_16x16x64_bf16 v[180:183], a[192:195], v[216:223], v228\n" \
"ds_read_b64_tr_b16 a[168:169], %[vvb] offset:25088\n" \
"buffer_store_dwordx4 a[196:199], v235, %[srdz], s62 offen offset:0 sc1\n" \
"v_mfma_f32_16x16x32_bf16 v[152:155], a[96:99], a[48:51], v[172:175]\n" \
"v_exp_f32_e64 v148, v148\n" \
"v_mfma_f32_16x16x32_bf16 v[152:155], a[100:103], a[52:55], v[152:155]\n" \
"ds_read_b64_tr_b16 a[170:171], %[vvb] offset:29184\n" \
"v_mfma_f32_16x16x32_bf16 v[152:155], a[104:107], a[56:59], v[152:155]\n" \
"v_exp_f32_e64 v149, v149\n" \
"v_mfma_f32_16x16x32_bf16 v[152:155], a[108:111], a[60:63], v[152:155]\n" \
"v_cvt_pk_bf16_f32 v200, v144, v145\n" \
"v_cvt_pk_bf16_f32 v201, v146, v147\n" \
"v_mfma_f32_16x16x32_bf16 v[156:159], a[112:115], a[48:51], v[172:175]\n" \
"v_exp_f32_e64 v150, v150\n" \
"v_mfma_f32_16x16x32_bf16 v[156:159], a[116:119], a[52:55], v[156:159]\n" \
"ds_read_b64_tr_b16 a[172:173], v211 offset:25088\n" \
"v_mfma_f32_16x16x32_bf16 v[156:159], a[120:123], a[56:59], v[156:159]\n" \
"buffer_load_dwordx4 v215, %[srdv], s48 offen offset:3072 lds\n" \
"v_mfma_f32_16x16x32_bf16 v[156:159], a[124:127], a[60:63], v[156:159]\n" \
"v_exp_f32_e64 v151, v151\n" \
"v_mfma_f32_16x16x32_bf16 v[112:115], a[176:179], v[204:207], v[112:115]\n" \
"ds_read_b64_tr_b16 a[174:175], v211 offset:29184\n" \
"ds_read_b64_tr_b16 a[176:177], %[vvb] offset:25600\n" \
"v_mfma_f32_16x16x32_bf16 v[112:115], a[144:147], v[220:223], v[112:115]\n" \
"v_exp_f32_e64 v152, v152\n" \
"v_mfma_f32_16x16x32_bf16 v[116:119], a[180:183], v[204:207], v[116:119]\n" \
"v_cvt_pk_bf16_f32 v202, v148, v149\n" \
"v_cvt_pk_bf16_f32 v203, v150, v151\n" \
"v_mfma_f32_16x16x32_bf16 v[116:119], a[148:151], v[220:223], v[116:119]\n" \
"v_exp_f32_e64 v153, v153\n" \
"v_mfma_f32_16x16x32_bf16 v[120:123], a[184:187], v[204:207], v[120:123]\n" \
"v_exp_f32_e64 v154, v154\n" \
"v_mfma_f32_16x16x32_bf16 v[120:123], a[152:155], v[220:223], v[120:123]\n" \
"ds_read_b64_tr_b16 a[178:179], %[vvb] offset:29696\n" \
"v_mfma_f32_16x16x32_bf16 v[124:127], a[188:191], v[204:207], v[124:127]\n" \
"v_exp_f32_e64 v155, v155\n" \
"v_mfma_f32_16x16x32_bf16 v[124:127], a[156:159], v[220:223], v[124:127]\n" \
"ds_read_b64_tr_b16 a[180:181], v211 offset:25600\n" \
"s_waitcnt lgkmcnt(13)\n" \
"v_mfma_f32_16x16x32_bf16 v[128:131], a[64:67], a[0:3], v[160:163]\n" \
"v_exp_f32_e32 v156, v156\n" \
"v_mfma_f32_16x16x32_bf16 v[128:131], a[68:71], a[4:7], v[128:131]\n" \
"ds_read_b64_tr_b16 a[182:183], v211 offset:29696\n" \
"s_add_u32 m0, %[m0b], 16384\n" \
"v_mfma_f32_16x16x32_bf16 v[128:131], a[72:75], a[8:11], v[128:131]\n" \
"v_exp_f32_e64 v157, v157\n" \
"v_mfma_f32_16x16x32_bf16 v[128:131], a[76:79], a[12:15], v[128:131]\n" \
"v_cvt_pk_bf16_f32 v204, v152, v153\n" \
"v_cvt_pk_bf16_f32 v205, v154, v155\n" \
"v_mfma_f32_16x16x32_bf16 v[132:135], a[80:83], a[0:3], v[160:163]\n" \
"v_exp_f32_e64 v158, v158\n" \
"v_mfma_f32_16x16x32_bf16 v[132:135], a[84:87], a[4:7], v[132:135]\n" \
"ds_read_b64_tr_b16 a[184:185], %[vvb] offset:26112\n" \
"s_waitcnt lgkmcnt(5)\n" \
"v_mfma_f32_16x16x32_bf16 v[132:135], a[88:91], a[8:11], v[132:135]\n" \
"buffer_load_dwordx4 %[kd0], %[srdk], s48 offen offset:0 lds\n" \
"v_mfma_f32_16x16x32_bf16 v[132:135], a[92:95], a[12:15], v[132:135]\n" \
"v_exp_f32_e32 v159, v159\n" \
"v_mfma_f32_16x16x32_bf16 v[64:67], a[128:131], v[216:219], v[64:67]\n" \
"ds_read_b64_tr_b16 a[186:187], %[vvb] offset:30208\n" \
"ds_read_b64_tr_b16 a[188:189], v211 offset:26112\n" \
"v_mfma_f32_16x16x32_bf16 v[64:67], a[160:163], v[200:203], v[64:67]\n" \
"v_exp_f32_e64 v128, v128\n" \
"v_mfma_f32_16x16x32_bf16 v[68:71], a[132:135], v[216:219], v[68:71]\n" \
"v_cvt_pk_bf16_f32 v206, v156, v157\n" \
"v_cvt_pk_bf16_f32 v207, v158, v159\n" \
"v_mfma_f32_16x16x32_bf16 v[68:71], a[164:167], v[200:203], v[68:71]\n" \
"v_exp_f32_e64 v129, v129\n" \
"v_mfma_f32_16x16x32_bf16 v[72:75], a[136:139], v[216:219], v[72:75]\n" \
"ds_read_b64_tr_b16 a[190:191], v211 offset:30208\n" \
"ds_read_b64_tr_b16 a[128:129], %[vvb] offset:32768\n" \
"v_mfma_f32_16x16x32_bf16 v[72:75], a[168:171], v[200:203], v[72:75]\n" \
"v_exp_f32_e64 v130, v130\n" \
"v_mfma_f32_16x16x32_bf16 v[76:79], a[140:143], v[216:219], v[76:79]\n" \
"ds_read_b64_tr_b16 a[130:131], %[vvb] offset:36864\n" \
"ds_read_b64_tr_b16 a[132:133], v211 offset:32768\n" \
"v_mfma_f32_16x16x32_bf16 v[76:79], a[172:175], v[200:203], v[76:79]\n" \
"v_exp_f32_e64 v131, v131\n" \
"v_smfmac_f32_16x16x64_bf16 v[176:179], a[192:195], v[192:199], v228\n" \
"ds_read_b64_tr_b16 a[134:135], v211 offset:36864\n" \
"ds_read_b64_tr_b16 a[136:137], %[vvb] offset:33280\n" \
"v_mfma_f32_16x16x32_bf16 v[136:139], a[64:67], a[16:19], v[164:167]\n" \
"v_exp_f32_e64 v132, v132\n" \
"v_mfma_f32_16x16x32_bf16 v[136:139], a[68:71], a[20:23], v[136:139]\n" \
"ds_read_b64_tr_b16 a[138:139], %[vvb] offset:37376\n" \
"ds_read_b64_tr_b16 a[140:141], v211 offset:33280\n" \
"v_mfma_f32_16x16x32_bf16 v[136:139], a[72:75], a[24:27], v[136:139]\n" \
"v_exp_f32_e64 v133, v133\n" \
"v_mfma_f32_16x16x32_bf16 v[136:139], a[76:79], a[28:31], v[136:139]\n" \
"v_cvt_pk_bf16_f32 v184, v128, v129\n" \
"v_cvt_pk_bf16_f32 v185, v130, v131\n" \
"v_mfma_f32_16x16x32_bf16 v[140:143], a[80:83], a[16:19], v[164:167]\n" \
"v_exp_f32_e64 v134, v134\n" \
"v_mfma_f32_16x16x32_bf16 v[140:143], a[84:87], a[20:23], v[140:143]\n" \
"ds_read_b64_tr_b16 a[142:143], v211 offset:37376\n" \
"s_waitcnt lgkmcnt(8)\n" \
"v_mfma_f32_16x16x32_bf16 v[140:143], a[88:91], a[24:27], v[140:143]\n" \
"buffer_load_dwordx4 v212, %[srdk], s48 offen offset:1024 lds\n" \
"v_mfma_f32_16x16x32_bf16 v[140:143], a[92:95], a[28:31], v[140:143]\n" \
"v_exp_f32_e32 v135, v135\n" \
"v_mfma_f32_16x16x32_bf16 v[80:83], a[144:147], v[216:219], v[80:83]\n" \
".if . - adaln_body != 24576\n" \
".error \"size model\"\n" \
".endif\n" \
"Lk5_%=:\n" \
"s_cbranch_execz adaln_kr\n" \
"s_nop 0\n" \
"ds_read_b64_tr_b16 a[144:145], %[vvb] offset:33792\n" \
"ds_read_b64_tr_b16 a[146:147], %[vvb] offset:37888\n" \
"v_mfma_f32_16x16x32_bf16 v[80:83], a[176:179], v[200:203], v[80:83]\n" \
"v_exp_f32_e64 v136, v136\n" \
"v_mfma_f32_16x16x32_bf16 v[84:87], a[148:151], v[216:219], v[84:87]\n" \
"v_cvt_pk_bf16_f32 v186, v132, v133\n" \
"v_cvt_pk_bf16_f32 v187, v134, v135\n" \
"v_mfma_f32_16x16x32_bf16 v[84:87], a[180:183], v[200:203], v[84:87]\n" \
"v_exp_f32_e64 v137, v137\n" \
"v_mfma_f32_16x16x32_bf16 v[88:91], a[152:155], v[216:219], v[88:91]\n" \
"v_exp_f32_e64 v138, v138\n" \
"v_mfma_f32_16x16x32_bf16 v[88:91], a[184:187], v[200:203], v[88:91]\n" \
"ds_read_b64_tr_b16 a[148:149], v211 offset:33792\n" \
"ds_read_b64_tr_b16 a[150:151], v211 offset:37888\n" \
"v_mfma_f32_16x16x32_bf16 v[92:95], a[156:159], v[216:219], v[92:95]\n" \
"v_exp_f32_e64 v139, v139\n" \
"v_mfma_f32_16x16x32_bf16 v[92:95], a[188:191], v[200:203], v[92:95]\n" \
"ds_read_b64_tr_b16 a[152:153], %[vvb] offset:34304\n" \
"buffer_store_dwordx4 a[196:199], v235, %[srdz], s63 offen offset:1024 sc1\n" \
"v_mfma_f32_16x16x32_bf16 v[144:147], a[64:67], a[32:35], v[168:171]\n" \
"v_exp_f32_e64 v140, v140\n" \
"v_mfma_f32_16x16x32_bf16 v[144:147], a[68:71], a[36:39], v[144:147]\n" \
"ds_read_b64_tr_b16 a[154:155], %[vvb] offset:38400\n" \
"ds_read_b64_tr_b16 a[156:157], v211 offset:34304\n" \
"v_mfma_f32_16x16x32_bf16 v[144:147], a[72:75], a[40:43], v[144:147]\n" \
"v_exp_f32_e64 v141, v141\n" \
"v_mfma_f32_16x16x32_bf16 v[144:147], a[76:79], a[44:47], v[144:147]\n" \
"v_cvt_pk_bf16_f32 v188, v136, v137\n" \
"v_cvt_pk_bf16_f32 v189, v138, v139\n" \
"v_mfma_f32_16x16x32_bf16 v[148:151], a[80:83], a[32:35], v[168:171]\n" \
"v_exp_f32_e64 v142, v142\n" \
"v_mfma_f32_16x16x32_bf16 v[148:151], a[84:87], a[36:39], v[148:151]\n" \
"ds_read_b64_tr_b16 a[158:159], v211 offset:38400\n" \
"s_waitcnt lgkmcnt(8)\n" \
"v_mfma_f32_16x16x32_bf16 v[148:151], a[88:91], a[40:43], v[148:151]\n" \
"buffer_load_dwordx4 v213, %[srdk], s48 offen offset:2048 lds\n" \
"v_mfma_f32_16x16x32_bf16 v[148:151], a[92:95], a[44:47], v[148:151]\n" \
"v_exp_f32_e32 v143, v143\n" \
"v_mfma_f32_16x16x32_bf16 v[0:3], a[160:163], v[192:195], v[0:3]\n" \
"ds_read_b128 a[96:99], %[kb0] offset:40960\n" \
"v_mfma_f32_16x16x32_bf16 v[0:3], a[128:131], v[184:187], v[0:3]\n" \
"v_exp_f32_e64 v144, v144\n" \
"v_mfma_f32_16x16x32_bf16 v[4:7], a[164:167], v[192:195], v[4:7]\n" \
"v_cvt_pk_bf16_f32 v190, v140, v141\n" \
"v_cvt_pk_bf16_f32 v191, v142, v143\n" \
"v_mfma_f32_16x16x32_bf16 v[4:7], a[132:135], v[184:187], v[4:7]\n" \
"v_exp_f32_e64 v145, v145\n" \
"v_mfma_f32_16x16x32_bf16 v[8:11], a[168:171], v[192:195], v[8:11]\n" \
"ds_read_b128 a[100:103], v208 offset:40960\n" \
"v_mfma_f32_16x16x32_bf16 v[8:11], a[136:139], v[184:187], v[8:11]\n" \
"v_exp_f32_e64 v146, v146\n" \
"v_mfma_f32_16x16x32_bf16 v[12:15], a[172:175], v[192:195], v[12:15]\n" \
"ds_read_b128 a[104:107], v209 offset:40960\n" \
"v_mfma_f32_16x16x32_bf16 v[12:15], a[140:143], v[184:187], v[12:15]\n" \
"v_exp_f32_e64 v147, v147\n" \
"v_smfmac_f32_16x16x64_bf16 v[180:183], a[192:195], v[200:207], v228\n" \
"ds_read_b128 a[108:111], v210 offset:40960\n" \
"v_mfma_f32_16x16x32_bf16 v[152:155], a[64:67], a[48:51], v[172:175]\n" \
"v_exp_f32_e64 v148, v148\n" \
"v_mfma_f32_16x16x32_bf16 v[152:155], a[68:71], a[52:55], v[152:155]\n" \
"ds_read_b128 a[112:115], %[kb0] offset:45056\n" \
"v_mfma_f32_16x16x32_bf16 v[152:155], a[72:75], a[56:59], v[152:155]\n" \
"v_exp_f32_e64 v149, v149\n" \
"v_mfma_f32_16x16x32_bf16 v[152:155], a[76:79], a[60:63], v[152:155]\n" \
"v_cvt_pk_bf16_f32 v216, v144, v145\n" \
"v_cvt_pk_bf16_f32 v217, v146, v147\n" \
"v_mfma_f32_16x16x32_bf16 v[156:159], a[80:83], a[48:51], v[172:175]\n" \
"v_exp_f32_e64 v150, v150\n" \
"v_mfma_f32_16x16x32_bf16 v[156:159], a[84:87], a[52:55], v[156:159]\n" \
"ds_read_b128 a[116:119], v208 offset:45056\n" \
"s_waitcnt lgkmcnt(6)\n" \
"v_mfma_f32_16x16x32_bf16 v[156:159], a[88:91], a[56:59], v[156:159]\n" \
"buffer_load_dwordx4 v214, %[srdk], s48 offen offset:3072 lds\n" \
"v_mfma_f32_16x16x32_bf16 v[156:159], a[92:95], a[60:63], v[156:159]\n" \
"v_exp_f32_e32 v151, v151\n" \
"v_mfma_f32_16x16x32_bf16 v[16:19], a[176:179], v[192:195], v[16:19]\n" \
"ds_read_b128 a[120:123], v209 offset:45056\n" \
"v_mfma_f32_16x16x32_bf16 v[16:19], a[144:147], v[184:187], v[16:19]\n" \
"v_exp_f32_e64 v152, v152\n" \
"v_mfma_f32_16x16x32_bf16 v[20:23], a[180:183], v[192:195], v[20:23]\n" \
"v_cvt_pk_bf16_f32 v218, v148, v149\n" \
"v_cvt_pk_bf16_f32 v219, v150, v151\n" \
"v_mfma_f32_16x16x32_bf16 v[20:23], a[148:151], v[184:187], v[20:23]\n" \
"v_exp_f32_e64 v153, v153\n" \
"v_mfma_f32_16x16x32_bf16 v[24:27], a[184:187], v[192:195], v[24:27]\n" \
"v_exp_f32_e64 v154, v154\n" \
"v_mfma_f32_16x16x32_bf16 v[24:27], a[152:155], v[184:187], v[24:27]\n" \
"ds_read_b128 a[124:127], v210 offset:45056\n" \
"s_add_u32 s48, s48, 0x4000\n" \
"v_mfma_f32_16x16x32_bf16 v[28:31], a[188:191], v[192:195], v[28:31]\n" \
"v_exp_f32_e64 v155, v155\n" \
"v_mfma_f32_16x16x32_bf16 v[28:31], a[156:159], v[184:187], v[28:31]\n" \
"s_waitcnt vmcnt(16) lgkmcnt(2)\n" \
"s_barrier\n" \
"v_mfma_f32_16x16x32_bf16 v[128:131], a[96:99], a[0:3], v[160:163]\n" \
"v_exp_f32_e64 v156, v156\n" \
"v_mfma_f32_16x16x32_bf16 v[128:131], a[100:103], a[4:7], v[128:131]\n" \
"s_add_u32 m0, %[m0b], 81920\n" \
"v_mfma_f32_16x16x32_bf16 v[128:131], a[104:107], a[8:11], v[128:131]\n" \
"v_exp_f32_e64 v157, v157\n" \
"v_mfma_f32_16x16x32_bf16 v[128:131], a[108:111], a[12:15], v[128:131]\n" \
"v_cvt_pk_bf16_f32 v220, v152, v153\n" \
"v_cvt_pk_bf16_f32 v221, v154, v155\n" \
"v_mfma_f32_16x16x32_bf16 v[132:135], a[112:115], a[0:3], v[160:163]\n" \
"v_exp_f32_e64 v158, v158\n" \
"v_mfma_f32_16x16x32_bf16 v[132:135], a[116:119], a[4:7], v[132:135]\n" \
"s_waitcnt lgkmcnt(0)\n" \
"v_mfma_f32_16x16x32_bf16 v[132:135], a[120:123], a[8:11], v[132:135]\n" \
"buffer_load_dwordx4 %[vd0], %[srdv], s48 offen offset:0 lds\n" \
"v_mfma_f32_16x16x32_bf16 v[132:135], a[124:127], a[12:15], v[132:135]\n" \
"v_exp_f32_e32 v159, v159\n" \
"v_mfma_f32_16x16x32_bf16 v[32:35], a[160:163], v[196:199], v[32:35]\n" \
"ds_read_b128 a[64:67], %[kb0] offset:49152\n" \
"v_mfma_f32_16x16x32_bf16 v[32:35], a[128:131], v[188:191], v[32:35]\n" \
"v_exp_f32_e64 v128, v128\n" \
"v_mfma_f32_16x16x32_bf16 v[36:39], a[164:167], v[196:199], v[36:39]\n" \
"v_cvt_pk_bf16_f32 v222, v156, v157\n" \
"v_cvt_pk_bf16_f32 v223, v158, v159\n" \
"v_mfma_f32_16x16x32_bf16 v[36:39], a[132:135], v[188:191], v[36:39]\n" \
"v_exp_f32_e64 v129, v129\n" \
"v_mfma_f32_16x16x32_bf16 v[40:43], a[168:171], v[196:199], v[40:43]\n" \
"ds_read_b128 a[68:71], v208 offset:49152\n" \
"v_mfma_f32_16x16x32_bf16 v[40:43], a[136:139], v[188:191], v[40:43]\n" \
"v_exp_f32_e64 v130, v130\n" \
"v_mfma_f32_16x16x32_bf16 v[44:47], a[172:175], v[196:199], v[44:47]\n" \
"ds_read_b128 a[72:75], v209 offset:49152\n" \
"v_mfma_f32_16x16x32_bf16 v[44:47], a[140:143], v[188:191], v[44:47]\n" \
"v_exp_f32_e64 v131, v131\n" \
"v_smfmac_f32_16x16x64_bf16 v[176:179], a[192:195], v[184:191], v228\n" \
"v_mfma_f32_16x16x32_bf16 v[136:139], a[96:99], a[16:19], v[164:167]\n" \
"v_exp_f32_e64 v132, v132\n" \
"v_mfma_f32_16x16x32_bf16 v[136:139], a[100:103], a[20:23], v[136:139]\n" \
"ds_read_b128 a[76:79], v210 offset:49152\n" \
"v_mfma_f32_16x16x32_bf16 v[136:139], a[104:107], a[24:27], v[136:139]\n" \
"v_exp_f32_e64 v133, v133\n" \
"v_mfma_f32_16x16x32_bf16 v[136:139], a[108:111], a[28:31], v[136:139]\n" \
"v_cvt_pk_bf16_f32 v192, v128, v129\n" \
"v_cvt_pk_bf16_f32 v193, v130, v131\n" \
"v_mfma_f32_16x16x32_bf16 v[140:143], a[112:115], a[16:19], v[164:167]\n" \
"v_exp_f32_e64 v134, v134\n" \
"v_mfma_f32_16x16x32_bf16 v[140:143], a[116:119], a[20:23], v[140:143]\n" \
"ds_read_b128 a[80:83], %[kb0] offset:53248\n" \
"v_mfma_f32_16x16x32_bf16 v[140:143], a[120:123], a[24:27], v[140:143]\n" \
"buffer_load_dwordx4 v215, %[srdv], s48 offen offset:1024 lds\n" \
"v_mfma_f32_16x16x32_bf16 v[140:143], a[124:127], a[28:31], v[140:143]\n" \
"v_exp_f32_e64 v135, v135\n" \
"v_mfma_f32_16x16x32_bf16 v[48:51], a[176:179], v[196:199], v[48:51]\n" \
"ds_read_b128 a[84:87], v208 offset:53248\n" \
"v_mfma_f32_16x16x32_bf16 v[48:51], a[144:147], v[188:191], v[48:51]\n" \
"v_exp_f32_e64 v136, v136\n" \
"v_mfma_f32_16x16x32_bf16 v[52:55], a[180:183], v[196:199], v[52:55]\n" \
"v_cvt_pk_bf16_f32 v194, v132, v133\n" \
"v_cvt_pk_bf16_f32 v195, v134, v135\n" \
"v_mfma_f32_16x16x32_bf16 v[52:55], a[148:151], v[188:191], v[52:55]\n" \
"v_exp_f32_e64 v137, v137\n" \
"v_mfma_f32_16x16x32_bf16 v[56:59], a[184:187], v[196:199], v[56:59]\n" \
"v_exp_f32_e64 v138, v138\n" \
"v_mfma_f32_16x16x32_bf16 v[56:59], a[152:155], v[188:191], v[56:59]\n" \
"ds_read_b128 a[88:91], v209 offset:53248\n" \
"v_mfma_f32_16x16x32_bf16 v[60:63], a[188:191], v[196:199], v[60:63]\n" \
"v_exp_f32_e64 v139, v139\n" \
"v_mfma_f32_16x16x32_bf16 v[60:63], a[156:159], v[188:191], v[60:63]\n" \
"s_cmp_lg_u32 %[wid], 1\n" \
"s_cbranch_scc1 LtB5_%=\n" \
"global_load_dword v237, v236, s[58:59]\n" \
"s_add_u32 s61, s48, 49152\n" \
"s_mov_b32 exec_hi, 0\n" \
"s_nop 0\n" \
"buffer_load_dword v237, v224, %[srdp], s61 offen\n" \
"s_mov_b32 exec_hi, -1\n" \
"s_nop 0\n" \
"LtB5_%=:\n" \
"v_mfma_f32_16x16x32_bf16 v[144:147], a[96:99], a[32:35], v[168:171]\n" \
"v_exp_f32_e64 v140, v140\n" \
"v_mfma_f32_16x16x32_bf16 v[144:147], a[100:103], a[36:39], v[144:147]\n" \
"ds_read_b128 a[92:95], v210 offset:53248\n" \
"v_mfma_f32_16x16x32_bf16 v[144:147], a[104:107], a[40:43], v[144:147]\n" \
"v_exp_f32_e64 v141, v141\n" \
"v_mfma_f32_16x16x32_bf16 v[144:147], a[108:111], a[44:47], v[144:147]\n" \
"v_cvt_pk_bf16_f32 v196, v136, v137\n" \
"v_cvt_pk_bf16_f32 v197, v138, v139\n" \
"v_mfma_f32_16x16x32_bf16 v[148:151], a[112:115], a[32:35], v[168:171]\n" \
"v_exp_f32_e64 v142, v142\n" \
"v_mfma_f32_16x16x32_bf16 v[148:151], a[116:119], a[36:39], v[148:151]\n" \
"v_mfma_f32_16x16x32_bf16 v[148:151], a[120:123], a[40:43], v[148:151]\n" \
"buffer_load_dwordx4 %[vd0], %[srdv], s48 offen offset:2048 lds\n" \
"v_mfma_f32_16x16x32_bf16 v[148:151], a[124:127], a[44:47], v[148:151]\n" \
"v_exp_f32_e64 v143, v143\n" \
"v_mfma_f32_16x16x32_bf16 v[96:99], a[160:163], v[204:207], v[96:99]\n" \
"ds_read_b64_tr_b16 a[160:161], %[vvb] offset:40960\n" \
"v_mfma_f32_16x16x32_bf16 v[96:99], a[128:131], v[220:223], v[96:99]\n" \
"v_exp_f32_e64 v144, v144\n" \
"v_mfma_f32_16x16x32_bf16 v[100:103], a[164:167], v[204:207], v[100:103]\n" \
"v_cvt_pk_bf16_f32 v198, v140, v141\n" \
"v_cvt_pk_bf16_f32 v199, v142, v143\n" \
"v_mfma_f32_16x16x32_bf16 v[100:103], a[132:135], v[220:223], v[100:103]\n" \
"v_exp_f32_e64 v145, v145\n" \
"v_mfma_f32_16x16x32_bf16 v[104:107], a[168:171], v[204:207], v[104:107]\n" \
"ds_read_b64_tr_b16 a[162:163], %[vvb] offset:45056\n" \
"ds_read_b64_tr_b16 a[164:165], v211 offset:40960\n" \
"v_mfma_f32_16x16x32_bf16 v[104:107], a[136:139], v[220:223], v[104:107]\n" \
"v_exp_f32_e64 v146, v146\n" \
"v_mfma_f32_16x16x32_bf16 v[108:111], a[172:175], v[204:207], v[108:111]\n" \
"ds_read_b64_tr_b16 a[166:167], v211 offset:45056\n" \
"v_mfma_f32_16x16x32_bf16 v[108:111], a[140:143], v[220:223], v[108:111]\n" \
"v_exp_f32_e64 v147, v147\n" \
"v_smfmac_f32_16x16x64_bf16 v[180:183], a[192:195], v[216:223], v228\n" \
"ds_read_b64_tr_b16 a[168:169], %[vvb] offset:41472\n" \
"v_mfma_f32_16x16x32_bf16 v[152:155], a[96:99], a[48:51], v[172:175]\n" \
"v_exp_f32_e64 v148, v148\n" \
"v_mfma_f32_16x16x32_bf16 v[152:155], a[100:103], a[52:55], v[152:155]\n" \
"ds_read_b64_tr_b16 a[170:171], %[vvb] offset:45568\n" \
"v_mfma_f32_16x16x32_bf16 v[152:155], a[104:107], a[56:59], v[152:155]\n" \
"v_exp_f32_e64 v149, v149\n" \
"v_mfma_f32_16x16x32_bf16 v[152:155], a[108:111], a[60:63], v[152:155]\n" \
"v_cvt_pk_bf16_f32 v200, v144, v145\n" \
"v_cvt_pk_bf16_f32 v201, v146, v147\n" \
"v_mfma_f32_16x16x32_bf16 v[156:159], a[112:115], a[48:51], v[172:175]\n" \
"v_exp_f32_e64 v150, v150\n" \
"v_mfma_f32_16x16x32_bf16 v[156:159], a[116:119], a[52:55], v[156:159]\n" \
"ds_read_b64_tr_b16 a[172:173], v211 offset:41472\n" \
"v_mfma_f32_16x16x32_bf16 v[156:159], a[120:123], a[56:59], v[156:159]\n" \
"buffer_load_dwordx4 v215, %[srdv], s48 offen offset:3072 lds\n" \
"v_mfma_f32_16x16x32_bf16 v[156:159], a[124:127], a[60:63], v[156:159]\n" \
"v_exp_f32_e64 v151, v151\n" \
"v_mfma_f32_16x16x32_bf16 v[112:115], a[176:179], v[204:207], v[112:115]\n" \
"ds_read_b64_tr_b16 a[174:175], v211 offset:45568\n" \
"ds_read_b64_tr_b16 a[176:177], %[vvb] offset:41984\n" \
"v_mfma_f32_16x16x32_bf16 v[112:115], a[144:147], v[220:223], v[112:115]\n" \
"v_exp_f32_e64 v152, v152\n" \
"v_mfma_f32_16x16x32_bf16 v[116:119], a[180:183], v[204:207], v[116:119]\n" \
"v_cvt_pk_bf16_f32 v202, v148, v149\n" \
"v_cvt_pk_bf16_f32 v203, v150, v151\n" \
"v_mfma_f32_16x16x32_bf16 v[116:119], a[148:151], v[220:223], v[116:119]\n" \
"v_exp_f32_e64 v153, v153\n" \
"v_mfma_f32_16x16x32_bf16 v[120:123], a[184:187], v[204:207], v[120:123]\n" \
"v_exp_f32_e64 v154, v154\n" \
"v_mfma_f32_16x16x32_bf16 v[120:123], a[152:155], v[220:223], v[120:123]\n" \
"ds_read_b64_tr_b16 a[178:179], %[vvb] offset:46080\n" \
"v_mfma_f32_16x16x32_bf16 v[124:127], a[188:191], v[204:207], v[124:127]\n" \
"v_exp_f32_e64 v155, v155\n" \
"v_mfma_f32_16x16x32_bf16 v[124:127], a[156:159], v[220:223], v[124:127]\n" \
"ds_read_b64_tr_b16 a[180:181], v211 offset:41984\n" \
"s_waitcnt lgkmcnt(13)\n" \
"v_mfma_f32_16x16x32_bf16 v[128:131], a[64:67], a[0:3], v[160:163]\n" \
"v_exp_f32_e32 v156, v156\n" \
"v_mfma_f32_16x16x32_bf16 v[128:131], a[68:71], a[4:7], v[128:131]\n" \
"ds_read_b64_tr_b16 a[182:183], v211 offset:46080\n" \
"s_add_u32 m0, %[m0b], 32768\n" \
"v_mfma_f32_16x16x32_bf16 v[128:131], a[72:75], a[8:11], v[128:131]\n" \
"v_exp_f32_e64 v157, v157\n" \
"v_mfma_f32_16x16x32_bf16 v[128:131], a[76:79], a[12:15], v[128:131]\n" \
"v_cvt_pk_bf16_f32 v204, v152, v153\n" \
"v_cvt_pk_bf16_f32 v205, v154, v155\n" \
"v_mfma_f32_16x16x32_bf16 v[132:135], a[80:83], a[0:3], v[160:163]\n" \
"v_exp_f32_e64 v158, v158\n" \
"v_mfma_f32_16x16x32_bf16 v[132:135], a[84:87], a[4:7], v[132:135]\n" \
"ds_read_b64_tr_b16 a[184:185], %[vvb] offset:42496\n" \
"s_waitcnt lgkmcnt(5)\n" \
"v_mfma_f32_16x16x32_bf16 v[132:135], a[88:91], a[8:11], v[132:135]\n" \
"buffer_load_dwordx4 %[kd0], %[srdk], s48 offen offset:0 lds\n" \
"v_mfma_f32_16x16x32_bf16 v[132:135], a[92:95], a[12:15], v[132:135]\n" \
"v_exp_f32_e32 v159, v159\n" \
"v_mfma_f32_16x16x32_bf16 v[64:67], a[128:131], v[216:219], v[64:67]\n" \
"ds_read_b64_tr_b16 a[186:187], %[vvb] offset:46592\n" \
"ds_read_b64_tr_b16 a[188:189], v211 offset:42496\n" \
"v_mfma_f32_16x16x32_bf16 v[64:67], a[160:163], v[200:203], v[64:67]\n" \
"v_exp_f32_e64 v128, v128\n" \
"v_mfma_f32_16x16x32_bf16 v[68:71], a[132:135], v[216:219], v[68:71]\n" \
"v_cvt_pk_bf16_f32 v206, v156, v157\n" \
"v_cvt_pk_bf16_f32 v207, v158, v159\n" \
"v_mfma_f32_16x16x32_bf16 v[68:71], a[164:167], v[200:203], v[68:71]\n" \
"v_exp_f32_e64 v129, v129\n" \
"v_mfma_f32_16x16x32_bf16 v[72:75], a[136:139], v[216:219], v[72:75]\n" \
"ds_read_b64_tr_b16 a[190:191], v211 offset:46592\n" \
"ds_read_b64_tr_b16 a[128:129], %[vvb] offset:49152\n" \
"v_mfma_f32_16x16x32_bf16 v[72:75], a[168:171], v[200:203], v[72:75]\n" \
"v_exp_f32_e64 v130, v130\n" \
"v_mfma_f32_16x16x32_bf16 v[76:79], a[140:143], v[216:219], v[76:79]\n" \
"ds_read_b64_tr_b16 a[130:131], %[vvb] offset:53248\n" \
"ds_read_b64_tr_b16 a[132:133], v211 offset:49152\n" \
"v_mfma_f32_16x16x32_bf16 v[76:79], a[172:175], v[200:203], v[76:79]\n" \
"v_exp_f32_e64 v131, v131\n" \
"v_smfmac_f32_16x16x64_bf16 v[176:179], a[192:195], v[192:199], v228\n" \
"ds_read_b64_tr_b16 a[134:135], v211 offset:53248\n" \
"ds_read_b64_tr_b16 a[136:137], %[vvb] offset:49664\n" \
"buffer_store_dwordx4 a[196:199], v235, %[srdz], s62 offen offset:1024 sc1\n" \
"v_mfma_f32_16x16x32_bf16 v[136:139], a[64:67], a[16:19], v[164:167]\n" \
"v_exp_f32_e64 v132, v132\n" \
"v_mfma_f32_16x16x32_bf16 v[136:139], a[68:71], a[20:23], v[136:139]\n" \
"ds_read_b64_tr_b16 a[138:139], %[vvb] offset:53760\n" \
"ds_read_b64_tr_b16 a[140:141], v211 offset:49664\n" \
"v_mfma_f32_16x16x32_bf16 v[136:139], a[72:75], a[24:27], v[136:139]\n" \
"v_exp_f32_e64 v133, v133\n" \
"v_mfma_f32_16x16x32_bf16 v[136:139], a[76:79], a[28:31], v[136:139]\n" \
"v_cvt_pk_bf16_f32 v184, v128, v129\n" \
"v_cvt_pk_bf16_f32 v185, v130, v131\n" \
"v_mfma_f32_16x16x32_bf16 v[140:143], a[80:83], a[16:19], v[164:167]\n" \
"v_exp_f32_e64 v134, v134\n" \
"v_mfma_f32_16x16x32_bf16 v[140:143], a[84:87], a[20:23], v[140:143]\n" \
"ds_read_b64_tr_b16 a[142:143], v211 offset:53760\n" \
"s_waitcnt lgkmcnt(8)\n" \
"v_mfma_f32_16x16x32_bf16 v[140:143], a[88:91], a[24:27], v[140:143]\n" \
"buffer_load_dwordx4 v212, %[srdk], s48 offen offset:1024 lds\n" \
"v_mfma_f32_16x16x32_bf16 v[140:143], a[92:95], a[28:31], v[140:143]\n" \
"v_exp_f32_e32 v135, v135\n" \
"v_mfma_f32_16x16x32_bf16 v[80:83], a[144:147], v[216:219], v[80:83]\n" \
"ds_read_b64_tr_b16 a[144:145], %[vvb] offset:50176\n" \
"ds_read_b64_tr_b16 a[146:147], %[vvb] offset:54272\n" \
"v_mfma_f32_16x16x32_bf16 v[80:83], a[176:179], v[200:203], v[80:83]\n" \
"v_exp_f32_e64 v136, v136\n" \
"v_mfma_f32_16x16x32_bf16 v[84:87], a[148:151], v[216:219], v[84:87]\n" \
"v_cvt_pk_bf16_f32 v186, v132, v133\n" \
"v_cvt_pk_bf16_f32 v187, v134, v135\n" \
"v_mfma_f32_16x16x32_bf16 v[84:87], a[180:183], v[200:203], v[84:87]\n" \
"v_exp_f32_e64 v137, v137\n" \
"v_mfma_f32_16x16x32_bf16 v[88:91], a[152:155], v[216:219], v[88:91]\n" \
"v_exp_f32_e64 v138, v138\n" \
"v_mfma_f32_16x16x32_bf16 v[88:91], a[184:187], v[200:203], v[88:91]\n" \
"ds_read_b64_tr_b16 a[148:149], v211 offset:50176\n" \
"ds_read_b64_tr_b16 a[150:151], v211 offset:54272\n" \
"v_mfma_f32_16x16x32_bf16 v[92:95], a[156:159], v[216:219], v[92:95]\n" \
"v_exp_f32_e64 v139, v139\n" \
"v_mfma_f32_16x16x32_bf16 v[92:95], a[188:191], v[200:203], v[92:95]\n" \
"ds_read_b64_tr_b16 a[152:153], %[vvb] offset:50688\n" \
"global_load_dword v237, %[vct], %[pct]\n" \
"v_mfma_f32_16x16x32_bf16 v[144:147], a[64:67], a[32:35], v[168:171]\n" \
"v_exp_f32_e64 v140, v140\n" \
"v_mfma_f32_16x16x32_bf16 v[144:147], a[68:71], a[36:39], v[144:147]\n" \
"ds_read_b64_tr_b16 a[154:155], %[vvb] offset:54784\n" \
"ds_read_b64_tr_b16 a[156:157], v211 offset:50688\n" \
"v_mfma_f32_16x16x32_bf16 v[144:147], a[72:75], a[40:43], v[144:147]\n" \
"v_exp_f32_e64 v141, v141\n" \
"v_mfma_f32_16x16x32_bf16 v[144:147], a[76:79], a[44:47], v[144:147]\n" \
"v_cvt_pk_bf16_f32 v188, v136, v137\n" \
"v_cvt_pk_bf16_f32 v189, v138, v139\n" \
"v_mfma_f32_16x16x32_bf16 v[148:151], a[80:83], a[32:35], v[168:171]\n" \
"v_exp_f32_e64 v142, v142\n" \
"v_mfma_f32_16x16x32_bf16 v[148:151], a[84:87], a[36:39], v[148:151]\n" \
"ds_read_b64_tr_b16 a[158:159], v211 offset:54784\n" \
"s_waitcnt lgkmcnt(8)\n" \
"v_mfma_f32_16x16x32_bf16 v[148:151], a[88:91], a[40:43], v[148:151]\n" \
"buffer_load_dwordx4 v213, %[srdk], s48 offen offset:2048 lds\n" \
"v_mfma_f32_16x16x32_bf16 v[148:151], a[92:95], a[44:47], v[148:151]\n" \
"v_exp_f32_e32 v143, v143\n" \
"v_mfma_f32_16x16x32_bf16 v[0:3], a[160:163], v[192:195], v[0:3]\n" \
"ds_read_b128 a[96:99], %[kb0] offset:57344\n" \
"v_mfma_f32_16x16x32_bf16 v[0:3], a[128:131], v[184:187], v[0:3]\n" \
"v_exp_f32_e64 v144, v144\n" \
"v_mfma_f32_16x16x32_bf16 v[4:7], a[164:167], v[192:195], v[4:7]\n" \
"v_cvt_pk_bf16_f32 v190, v140, v141\n" \
"v_cvt_pk_bf16_f32 v191, v142, v143\n" \
"v_mfma_f32_16x16x32_bf16 v[4:7], a[132:135], v[184:187], v[4:7]\n" \
"v_exp_f32_e64 v145, v145\n" \
"v_mfma_f32_16x16x32_bf16 v[8:11], a[168:171], v[192:195], v[8:11]\n" \
"ds_read_b128 a[100:103], v208 offset:57344\n" \
"v_mfma_f32_16x16x32_bf16 v[8:11], a[136:139], v[184:187], v[8:11]\n" \
"v_exp_f32_e64 v146, v146\n" \
"v_mfma_f32_16x16x32_bf16 v[12:15], a[172:175], v[192:195], v[12:15]\n" \
"ds_read_b128 a[104:107], v209 offset:57344\n" \
"v_mfma_f32_16x16x32_bf16 v[12:15], a[140:143], v[184:187], v[12:15]\n" \
"v_exp_f32_e64 v147, v147\n" \
"v_smfmac_f32_16x16x64_bf16 v[180:183], a[192:195], v[200:207], v228\n" \
"ds_read_b128 a[108:111], v210 offset:57344\n" \
"v_mfma_f32_16x16x32_bf16 v[152:155], a[64:67], a[48:51], v[172:175]\n" \
"v_exp_f32_e64 v148, v148\n" \
"v_mfma_f32_16x16x32_bf16 v[152:155], a[68:71], a[52:55], v[152:155]\n" \
"ds_read_b128 a[112:115], %[kb0] offset:61440\n" \
"v_mfma_f32_16x16x32_bf16 v[152:155], a[72:75], a[56:59], v[152:155]\n" \
"v_exp_f32_e64 v149, v149\n" \
"v_mfma_f32_16x16x32_bf16 v[152:155], a[76:79], a[60:63], v[152:155]\n" \
"v_cvt_pk_bf16_f32 v216, v144, v145\n" \
"v_cvt_pk_bf16_f32 v217, v146, v147\n" \
"v_mfma_f32_16x16x32_bf16 v[156:159], a[80:83], a[48:51], v[172:175]\n" \
"v_exp_f32_e64 v150, v150\n" \
"v_mfma_f32_16x16x32_bf16 v[156:159], a[84:87], a[52:55], v[156:159]\n" \
"ds_read_b128 a[116:119], v208 offset:61440\n" \
"s_waitcnt lgkmcnt(6)\n" \
"v_mfma_f32_16x16x32_bf16 v[156:159], a[88:91], a[56:59], v[156:159]\n" \
"buffer_load_dwordx4 v214, %[srdk], s48 offen offset:3072 lds\n" \
"v_mfma_f32_16x16x32_bf16 v[156:159], a[92:95], a[60:63], v[156:159]\n" \
"v_exp_f32_e32 v151, v151\n" \
"v_mfma_f32_16x16x32_bf16 v[16:19], a[176:179], v[192:195], v[16:19]\n" \
"ds_read_b128 a[120:123], v209 offset:61440\n" \
"v_mfma_f32_16x16x32_bf16 v[16:19], a[144:147], v[184:187], v[16:19]\n" \
"v_exp_f32_e64 v152, v152\n" \
"v_mfma_f32_16x16x32_bf16 v[20:23], a[180:183], v[192:195], v[20:23]\n" \
"v_cvt_pk_bf16_f32 v218, v148, v149\n" \
"v_cvt_pk_bf16_f32 v219, v150, v151\n" \
"v_mfma_f32_16x16x32_bf16 v[20:23], a[148:151], v[184:187], v[20:23]\n" \
"v_exp_f32_e64 v153, v153\n" \
"v_mfma_f32_16x16x32_bf16 v[24:27], a[184:187], v[192:195], v[24:27]\n" \
"v_exp_f32_e64 v154, v154\n" \
"v_mfma_f32_16x16x32_bf16 v[24:27], a[152:155], v[184:187], v[24:27]\n" \
"ds_read_b128 a[124:127], v210 offset:61440\n" \
"s_add_u32 s48, s48, 0x4000\n" \
"s_mov_b32 m0, s44\n" \
"s_addk_i32 s44, 0x400\n" \
"buffer_load_dwordx4 v229, s[64:67], s45 offen offset:0 lds\n" \
"s_add_u32 s45, s45, 229376\n" \
"v_mfma_f32_16x16x32_bf16 v[28:31], a[188:191], v[192:195], v[28:31]\n" \
"v_exp_f32_e64 v155, v155\n" \
"v_mfma_f32_16x16x32_bf16 v[28:31], a[156:159], v[184:187], v[28:31]\n" \
"s_waitcnt vmcnt(16) lgkmcnt(2)\n" \
"s_barrier\n" \
"v_mfma_f32_16x16x32_bf16 v[128:131], a[96:99], a[0:3], v[160:163]\n" \
"v_exp_f32_e64 v156, v156\n" \
"v_mfma_f32_16x16x32_bf16 v[128:131], a[100:103], a[4:7], v[128:131]\n" \
"s_add_u32 m0, %[m0b], 98304\n" \
"v_mfma_f32_16x16x32_bf16 v[128:131], a[104:107], a[8:11], v[128:131]\n" \
"v_exp_f32_e64 v157, v157\n" \
"v_mfma_f32_16x16x32_bf16 v[128:131], a[108:111], a[12:15], v[128:131]\n" \
"v_cvt_pk_bf16_f32 v220, v152, v153\n" \
"v_cvt_pk_bf16_f32 v221, v154, v155\n" \
"v_mfma_f32_16x16x32_bf16 v[132:135], a[112:115], a[0:3], v[160:163]\n" \
"v_exp_f32_e64 v158, v158\n" \
"v_mfma_f32_16x16x32_bf16 v[132:135], a[116:119], a[4:7], v[132:135]\n" \
"s_waitcnt lgkmcnt(0)\n" \
"v_mfma_f32_16x16x32_bf16 v[132:135], a[120:123], a[8:11], v[132:135]\n" \
"buffer_load_dwordx4 %[vd0], %[srdv], s48 offen offset:0 lds\n" \
"v_mfma_f32_16x16x32_bf16 v[132:135], a[124:127], a[12:15], v[132:135]\n" \
"v_exp_f32_e32 v159, v159\n" \
"v_mfma_f32_16x16x32_bf16 v[32:35], a[160:163], v[196:199], v[32:35]\n" \
"ds_read_b128 a[64:67], %[kb0] offset:0\n" \
"v_mfma_f32_16x16x32_bf16 v[32:35], a[128:131], v[188:191], v[32:35]\n" \
"v_exp_f32_e64 v128, v128\n" \
"v_mfma_f32_16x16x32_bf16 v[36:39], a[164:167], v[196:199], v[36:39]\n" \
"v_cvt_pk_bf16_f32 v222, v156, v157\n" \
"v_cvt_pk_bf16_f32 v223, v158, v159\n" \
"v_mfma_f32_16x16x32_bf16 v[36:39], a[132:135], v[188:191], v[36:39]\n" \
"v_exp_f32_e64 v129, v129\n" \
"v_mfma_f32_16x16x32_bf16 v[40:43], a[168:171], v[196:199], v[40:43]\n" \
"ds_read_b128 a[68:71], v208 offset:0\n" \
"v_mfma_f32_16x16x32_bf16 v[40:43], a[136:139], v[188:191], v[40:43]\n" \
"v_exp_f32_e64 v130, v130\n" \
"v_mfma_f32_16x16x32_bf16 v[44:47], a[172:175], v[196:199], v[44:47]\n" \
"ds_read_b128 a[72:75], v209 offset:0\n" \
"v_mfma_f32_16x16x32_bf16 v[44:47], a[140:143], v[188:191], v[44:47]\n" \
"v_exp_f32_e64 v131, v131\n" \
"v_smfmac_f32_16x16x64_bf16 v[176:179], a[192:195], v[184:191], v228\n" \
"buffer_store_dwordx4 a[196:199], v235, %[srdz], s63 offen offset:2048 sc1\n" \
"s_add_u32 s63, s63, 3072\n" \
"v_mfma_f32_16x16x32_bf16 v[136:139], a[96:99], a[16:19], v[164:167]\n" \
"v_exp_f32_e64 v132, v132\n" \
"v_mfma_f32_16x16x32_bf16 v[136:139], a[100:103], a[20:23], v[136:139]\n" \
"ds_read_b128 a[76:79], v210 offset:0\n" \
"v_mfma_f32_16x16x32_bf16 v[136:139], a[104:107], a[24:27], v[136:139]\n" \
"v_exp_f32_e64 v133, v133\n" \
"v_mfma_f32_16x16x32_bf16 v[136:139], a[108:111], a[28:31], v[136:139]\n" \
"v_cvt_pk_bf16_f32 v192, v128, v129\n" \
"v_cvt_pk_bf16_f32 v193, v130, v131\n" \
"v_mfma_f32_16x16x32_bf16 v[140:143], a[112:115], a[16:19], v[164:167]\n" \
"v_exp_f32_e64 v134, v134\n" \
"v_mfma_f32_16x16x32_bf16 v[140:143], a[116:119], a[20:23], v[140:143]\n" \
"ds_read_b128 a[80:83], %[kb0] offset:4096\n" \
"v_mfma_f32_16x16x32_bf16 v[140:143], a[120:123], a[24:27], v[140:143]\n" \
"buffer_load_dwordx4 v215, %[srdv], s48 offen offset:1024 lds\n" \
"v_mfma_f32_16x16x32_bf16 v[140:143], a[124:127], a[28:31], v[140:143]\n" \
"v_exp_f32_e64 v135, v135\n" \
"v_mfma_f32_16x16x32_bf16 v[48:51], a[176:179], v[196:199], v[48:51]\n" \
"ds_read_b128 a[84:87], v208 offset:4096\n" \
"v_mfma_f32_16x16x32_bf16 v[48:51], a[144:147], v[188:191], v[48:51]\n" \
"v_exp_f32_e64 v136, v136\n" \
"v_mfma_f32_16x16x32_bf16 v[52:55], a[180:183], v[196:199], v[52:55]\n" \
"v_cvt_pk_bf16_f32 v194, v132, v133\n" \
"v_cvt_pk_bf16_f32 v195, v134, v135\n" \
"v_mfma_f32_16x16x32_bf16 v[52:55], a[148:151], v[188:191], v[52:55]\n" \
"v_exp_f32_e64 v137, v137\n" \
"v_mfma_f32_16x16x32_bf16 v[56:59], a[184:187], v[196:199], v[56:59]\n" \
"v_exp_f32_e64 v138, v138\n" \
"v_mfma_f32_16x16x32_bf16 v[56:59], a[152:155], v[188:191], v[56:59]\n" \
"ds_read_b128 a[88:91], v209 offset:4096\n" \
"v_mfma_f32_16x16x32_bf16 v[60:63], a[188:191], v[196:199], v[60:63]\n" \
"v_exp_f32_e64 v139, v139\n" \
"v_mfma_f32_16x16x32_bf16 v[60:63], a[156:159], v[188:191], v[60:63]\n" \
"s_cmp_lg_u32 %[wid], 2\n" \
"s_cbranch_scc1 LtB7_%=\n" \
"global_load_dword v237, v236, s[58:59]\n" \
"s_add_u32 s61, s48, 49152\n" \
"s_mov_b32 exec_hi, 0\n" \
"s_nop 0\n" \
"buffer_load_dword v237, v224, %[srdp], s61 offen\n" \
"s_mov_b32 exec_hi, -1\n" \
"s_nop 0\n" \
"LtB7_%=:\n" \
"v_mfma_f32_16x16x32_bf16 v[144:147], a[96:99], a[32:35], v[168:171]\n" \
"v_exp_f32_e64 v140, v140\n" \
"v_mfma_f32_16x16x32_bf16 v[144:147], a[100:103], a[36:39], v[144:147]\n" \
"ds_read_b128 a[92:95], v210 offset:4096\n" \
"v_mfma_f32_16x16x32_bf16 v[144:147], a[104:107], a[40:43], v[144:147]\n" \
"v_exp_f32_e64 v141, v141\n" \
"v_mfma_f32_16x16x32_bf16 v[144:147], a[108:111], a[44:47], v[144:147]\n" \
"v_cvt_pk_bf16_f32 v196, v136, v137\n" \
"v_cvt_pk_bf16_f32 v197, v138, v139\n" \
"v_mfma_f32_16x16x32_bf16 v[148:151], a[112:115], a[32:35], v[168:171]\n" \
"v_exp_f32_e64 v142, v142\n" \
"v_mfma_f32_16x16x32_bf16 v[148:151], a[116:119], a[36:39], v[148:151]\n" \
"v_mfma_f32_16x16x32_bf16 v[148:151], a[120:123], a[40:43], v[148:151]\n" \
"buffer_load_dwordx4 %[vd0], %[srdv], s48 offen offset:2048 lds\n" \
"v_mfma_f32_16x16x32_bf16 v[148:151], a[124:127], a[44:47], v[148:151]\n" \
"v_exp_f32_e64 v143, v143\n" \
"v_mfma_f32_16x16x32_bf16 v[96:99], a[160:163], v[204:207], v[96:99]\n" \
"ds_read_b64_tr_b16 a[160:161], %[vvb] offset:57344\n" \
"v_mfma_f32_16x16x32_bf16 v[96:99], a[128:131], v[220:223], v[96:99]\n" \
"v_exp_f32_e64 v144, v144\n" \
"v_mfma_f32_16x16x32_bf16 v[100:103], a[164:167], v[204:207], v[100:103]\n" \
"v_cvt_pk_bf16_f32 v198, v140, v141\n" \
"v_cvt_pk_bf16_f32 v199, v142, v143\n" \
"v_mfma_f32_16x16x32_bf16 v[100:103], a[132:135], v[220:223], v[100:103]\n" \
"v_exp_f32_e64 v145, v145\n" \
"v_mfma_f32_16x16x32_bf16 v[104:107], a[168:171], v[204:207], v[104:107]\n" \
"ds_read_b64_tr_b16 a[162:163], %[vvb] offset:61440\n" \
"ds_read_b64_tr_b16 a[164:165], v211 offset:57344\n" \
"v_mfma_f32_16x16x32_bf16 v[104:107], a[136:139], v[220:223], v[104:107]\n" \
"v_exp_f32_e64 v146, v146\n" \
"v_mfma_f32_16x16x32_bf16 v[108:111], a[172:175], v[204:207], v[108:111]\n" \
"ds_read_b64_tr_b16 a[166:167], v211 offset:61440\n" \
"v_mfma_f32_16x16x32_bf16 v[108:111], a[140:143], v[220:223], v[108:111]\n" \
"v_exp_f32_e64 v147, v147\n" \
"v_smfmac_f32_16x16x64_bf16 v[180:183], a[192:195], v[216:223], v228\n" \
"ds_read_b64_tr_b16 a[168:169], %[vvb] offset:57856\n" \
"v_mfma_f32_16x16x32_bf16 v[152:155], a[96:99], a[48:51], v[172:175]\n" \
"v_exp_f32_e64 v148, v148\n" \
"v_mfma_f32_16x16x32_bf16 v[152:155], a[100:103], a[52:55], v[152:155]\n" \
"ds_read_b64_tr_b16 a[170:171], %[vvb] offset:61952\n" \
"v_mfma_f32_16x16x32_bf16 v[152:155], a[104:107], a[56:59], v[152:155]\n" \
"v_exp_f32_e64 v149, v149\n" \
"v_mfma_f32_16x16x32_bf16 v[152:155], a[108:111], a[60:63], v[152:155]\n" \
"v_cvt_pk_bf16_f32 v200, v144, v145\n" \
"v_cvt_pk_bf16_f32 v201, v146, v147\n" \
"v_mfma_f32_16x16x32_bf16 v[156:159], a[112:115], a[48:51], v[172:175]\n" \
".if . - adaln_body != 28672\n" \
".error \"size model\"\n" \
".endif\n" \
"Lk6_%=:\n" \
"s_cbranch_execz Lk7_%=\n" \
"s_nop 0\n" \
"v_exp_f32_e64 v150, v150\n" \
"v_mfma_f32_16x16x32_bf16 v[156:159], a[116:119], a[52:55], v[156:159]\n" \
"ds_read_b64_tr_b16 a[172:173], v211 offset:57856\n" \
"v_mfma_f32_16x16x32_bf16 v[156:159], a[120:123], a[56:59], v[156:159]\n" \
"buffer_load_dwordx4 v215, %[srdv], s48 offen offset:3072 lds\n" \
"v_mfma_f32_16x16x32_bf16 v[156:159], a[124:127], a[60:63], v[156:159]\n" \
"v_exp_f32_e64 v151, v151\n" \
"v_mfma_f32_16x16x32_bf16 v[112:115], a[176:179], v[204:207], v[112:115]\n" \
"ds_read_b64_tr_b16 a[174:175], v211 offset:61952\n" \
"ds_read_b64_tr_b16 a[176:177], %[vvb] offset:58368\n" \
"v_mfma_f32_16x16x32_bf16 v[112:115], a[144:147], v[220:223], v[112:115]\n" \
"v_exp_f32_e64 v152, v152\n" \
"v_mfma_f32_16x16x32_bf16 v[116:119], a[180:183], v[204:207], v[116:119]\n" \
"v_cvt_pk_bf16_f32 v202, v148, v149\n" \
"v_cvt_pk_bf16_f32 v203, v150, v151\n" \
"v_mfma_f32_16x16x32_bf16 v[116:119], a[148:151], v[220:223], v[116:119]\n" \
"v_exp_f32_e64 v153, v153\n" \
"v_mfma_f32_16x16x32_bf16 v[120:123], a[184:187], v[204:207], v[120:123]\n" \
"v_exp_f32_e64 v154, v154\n" \
"v_mfma_f32_16x16x32_bf16 v[120:123], a[152:155], v[220:223], v[120:123]\n" \
"ds_read_b64_tr_b16 a[178:179], %[vvb] offset:62464\n" \
"v_mfma_f32_16x16x32_bf16 v[124:127], a[188:191], v[204:207], v[124:127]\n" \
"v_exp_f32_e64 v155, v155\n" \
"v_mfma_f32_16x16x32_bf16 v[124:127], a[156:159], v[220:223], v[124:127]\n" \
"ds_read_b64_tr_b16 a[180:181], v211 offset:58368\n" \
"s_waitcnt lgkmcnt(13)\n" \
"v_mfma_f32_16x16x32_bf16 v[128:131], a[64:67], a[0:3], v[160:163]\n" \
"v_exp_f32_e32 v156, v156\n" \
"v_mfma_f32_16x16x32_bf16 v[128:131], a[68:71], a[4:7], v[128:131]\n" \
"ds_read_b64_tr_b16 a[182:183], v211 offset:62464\n" \
"s_add_u32 m0, %[m0b], 49152\n" \
"v_mfma_f32_16x16x32_bf16 v[128:131], a[72:75], a[8:11], v[128:131]\n" \
"v_exp_f32_e64 v157, v157\n" \
"v_mfma_f32_16x16x32_bf16 v[128:131], a[76:79], a[12:15], v[128:131]\n" \
"v_cvt_pk_bf16_f32 v204, v152, v153\n" \
"v_cvt_pk_bf16_f32 v205, v154, v155\n" \
"v_mfma_f32_16x16x32_bf16 v[132:135], a[80:83], a[0:3], v[160:163]\n" \
"v_exp_f32_e64 v158, v158\n" \
"v_mfma_f32_16x16x32_bf16 v[132:135], a[84:87], a[4:7], v[132:135]\n" \
"ds_read_b64_tr_b16 a[184:185], %[vvb] offset:58880\n" \
"s_waitcnt lgkmcnt(5)\n" \
"v_mfma_f32_16x16x32_bf16 v[132:135], a[88:91], a[8:11], v[132:135]\n" \
"buffer_load_dwordx4 %[kd0], %[srdk], s48 offen offset:0 lds\n" \
"v_mfma_f32_16x16x32_bf16 v[132:135], a[92:95], a[12:15], v[132:135]\n" \
"v_exp_f32_e32 v159, v159\n" \
"v_mfma_f32_16x16x32_bf16 v[64:67], a[128:131], v[216:219], v[64:67]\n" \
"ds_read_b64_tr_b16 a[186:187], %[vvb] offset:62976\n" \
"ds_read_b64_tr_b16 a[188:189], v211 offset:58880\n" \
"v_mfma_f32_16x16x32_bf16 v[64:67], a[160:163], v[200:203], v[64:67]\n" \
"v_exp_f32_e64 v128, v128\n" \
"v_mfma_f32_16x16x32_bf16 v[68:71], a[132:135], v[216:219], v[68:71]\n" \
"v_cvt_pk_bf16_f32 v206, v156, v157\n" \
"v_cvt_pk_bf16_f32 v207, v158, v159\n" \
"v_mfma_f32_16x16x32_bf16 v[68:71], a[164:167], v[200:203], v[68:71]\n" \
"v_exp_f32_e64 v129, v129\n" \
"v_mfma_f32_16x16x32_bf16 v[72:75], a[136:139], v[216:219], v[72:75]\n" \
"ds_read_b64_tr_b16 a[190:191], v211 offset:62976\n" \
"ds_read_b64_tr_b16 a[128:129], %[vvb] offset:0\n" \
"v_mfma_f32_16x16x32_bf16 v[72:75], a[168:171], v[200:203], v[72:75]\n" \
"v_exp_f32_e64 v130, v130\n" \
"v_mfma_f32_16x16x32_bf16 v[76:79], a[140:143], v[216:219], v[76:79]\n" \
"ds_read_b64_tr_b16 a[130:131], %[vvb] offset:4096\n" \
"ds_read_b64_tr_b16 a[132:133], v211 offset:0\n" \
"v_mfma_f32_16x16x32_bf16 v[76:79], a[172:175], v[200:203], v[76:79]\n" \
"v_exp_f32_e64 v131, v131\n" \
"v_smfmac_f32_16x16x64_bf16 v[176:179], a[192:195], v[192:199], v228\n" \
"ds_read_b64_tr_b16 a[134:135], v211 offset:4096\n" \
"ds_read_b64_tr_b16 a[136:137], %[vvb] offset:512\n" \
"v_mfma_f32_16x16x32_bf16 v[136:139], a[64:67], a[16:19], v[164:167]\n" \
"v_exp_f32_e64 v132, v132\n" \
"v_mfma_f32_16x16x32_bf16 v[136:139], a[68:71], a[20:23], v[136:139]\n" \
"ds_read_b64_tr_b16 a[138:139], %[vvb] offset:4608\n" \
"ds_read_b64_tr_b16 a[140:141], v211 offset:512\n" \
"v_mfma_f32_16x16x32_bf16 v[136:139], a[72:75], a[24:27], v[136:139]\n" \
"v_exp_f32_e64 v133, v133\n" \
"v_mfma_f32_16x16x32_bf16 v[136:139], a[76:79], a[28:31], v[136:139]\n" \
"v_cvt_pk_bf16_f32 v184, v128, v129\n" \
"v_cvt_pk_bf16_f32 v185, v130, v131\n" \
"v_mfma_f32_16x16x32_bf16 v[140:143], a[80:83], a[16:19], v[164:167]\n" \
"v_exp_f32_e64 v134, v134\n" \
"v_mfma_f32_16x16x32_bf16 v[140:143], a[84:87], a[20:23], v[140:143]\n" \
"ds_read_b64_tr_b16 a[142:143], v211 offset:4608\n" \
"s_waitcnt lgkmcnt(8)\n" \
"v_mfma_f32_16x16x32_bf16 v[140:143], a[88:91], a[24:27], v[140:143]\n" \
"buffer_load_dwordx4 v212, %[srdk], s48 offen offset:1024 lds\n" \
"v_mfma_f32_16x16x32_bf16 v[140:143], a[92:95], a[28:31], v[140:143]\n" \
"v_exp_f32_e32 v135, v135\n" \
"v_mfma_f32_16x16x32_bf16 v[80:83], a[144:147], v[216:219], v[80:83]\n" \
"ds_read_b64_tr_b16 a[144:145], %[vvb] offset:1024\n" \
"ds_read_b64_tr_b16 a[146:147], %[vvb] offset:5120\n" \
"v_mfma_f32_16x16x32_bf16 v[80:83], a[176:179], v[200:203], v[80:83]\n" \
"v_exp_f32_e64 v136, v136\n" \
"v_mfma_f32_16x16x32_bf16 v[84:87], a[148:151], v[216:219], v[84:87]\n" \
"v_cvt_pk_bf16_f32 v186, v132, v133\n" \
"v_cvt_pk_bf16_f32 v187, v134, v135\n" \
"v_mfma_f32_16x16x32_bf16 v[84:87], a[180:183], v[200:203], v[84:87]\n" \
"v_exp_f32_e64 v137, v137\n" \
"v_mfma_f32_16x16x32_bf16 v[88:91], a[152:155], v[216:219], v[88:91]\n" \
"v_exp_f32_e64 v138, v138\n" \
"v_mfma_f32_16x16x32_bf16 v[88:91], a[184:187], v[200:203], v[88:91]\n" \
"ds_read_b64_tr_b16 a[148:149], v211 offset:1024\n" \
"ds_read_b64_tr_b16 a[150:151], v211 offset:5120\n" \
"v_mfma_f32_16x16x32_bf16 v[92:95], a[156:159], v[216:219], v[92:95]\n" \
"v_exp_f32_e64 v139, v139\n" \
"v_mfma_f32_16x16x32_bf16 v[92:95], a[188:191], v[200:203], v[92:95]\n" \
"ds_read_b64_tr_b16 a[152:153], %[vvb] offset:1536\n" \
"buffer_store_dwordx4 a[196:199], v235, %[srdz], s62 offen offset:2048 sc1\n" \
"s_add_u32 s62, s62, 3072\n" \
"v_mfma_f32_16x16x32_bf16 v[144:147], a[64:67], a[32:35], v[168:171]\n" \
"v_exp_f32_e64 v140, v140\n" \
"v_mfma_f32_16x16x32_bf16 v[144:147], a[68:71], a[36:39], v[144:147]\n" \
"ds_read_b64_tr_b16 a[154:155], %[vvb] offset:5632\n" \
"ds_read_b64_tr_b16 a[156:157], v211 offset:1536\n" \
"v_mfma_f32_16x16x32_bf16 v[144:147], a[72:75], a[40:43], v[144:147]\n" \
"v_exp_f32_e64 v141, v141\n" \
"v_mfma_f32_16x16x32_bf16 v[144:147], a[76:79], a[44:47], v[144:147]\n" \
"v_cvt_pk_bf16_f32 v188, v136, v137\n" \
"v_cvt_pk_bf16_f32 v189, v138, v139\n" \
"v_mfma_f32_16x16x32_bf16 v[148:151], a[80:83], a[32:35], v[168:171]\n" \
"v_exp_f32_e64 v142, v142\n" \
"v_mfma_f32_16x16x32_bf16 v[148:151], a[84:87], a[36:39], v[148:151]\n" \
"ds_read_b64_tr_b16 a[158:159], v211 offset:5632\n" \
"s_waitcnt lgkmcnt(8)\n" \
"v_mfma_f32_16x16x32_bf16 v[148:151], a[88:91], a[40:43], v[148:151]\n" \
"buffer_load_dwordx4 v213, %[srdk], s48 offen offset:2048 lds\n" \
"v_mfma_f32_16x16x32_bf16 v[148:151], a[92:95], a[44:47], v[148:151]\n" \
"v_exp_f32_e32 v143, v143\n" \
"v_mfma_f32_16x16x32_bf16 v[0:3], a[160:163], v[192:195], v[0:3]\n" \
"ds_read_b128 a[96:99], %[kb0] offset:8192\n" \
"v_mfma_f32_16x16x32_bf16 v[0:3], a[128:131], v[184:187], v[0:3]\n" \
"v_exp_f32_e64 v144, v144\n" \
"v_mfma_f32_16x16x32_bf16 v[4:7], a[164:167], v[192:195], v[4:7]\n" \
"v_cvt_pk_bf16_f32 v190, v140, v141\n" \
"v_cvt_pk_bf16_f32 v191, v142, v143\n" \
"v_mfma_f32_16x16x32_bf16 v[4:7], a[132:135], v[184:187], v[4:7]\n" \
"v_exp_f32_e64 v145, v145\n" \
"v_mfma_f32_16x16x32_bf16 v[8:11], a[168:171], v[192:195], v[8:11]\n" \
"ds_read_b128 a[100:103], v208 offset:8192\n" \
"v_mfma_f32_16x16x32_bf16 v[8:11], a[136:139], v[184:187], v[8:11]\n" \
"v_exp_f32_e64 v146, v146\n" \
"v_mfma_f32_16x16x32_bf16 v[12:15], a[172:175], v[192:195], v[12:15]\n" \
"ds_read_b128 a[104:107], v209 offset:8192\n" \
"v_mfma_f32_16x16x32_bf16 v[12:15], a[140:143], v[184:187], v[12:15]\n" \
"v_exp_f32_e64 v147, v147\n" \
"v_smfmac_f32_16x16x64_bf16 v[180:183], a[192:195], v[200:207], v228\n" \
"ds_read_b128 a[108:111], v210 offset:8192\n" \
"v_mfma_f32_16x16x32_bf16 v[152:155], a[64:67], a[48:51], v[172:175]\n" \
"v_exp_f32_e64 v148, v148\n" \
"v_mfma_f32_16x16x32_bf16 v[152:155], a[68:71], a[52:55], v[152:155]\n" \
"ds_read_b128 a[112:115], %[kb0] offset:12288\n" \
"v_mfma_f32_16x16x32_bf16 v[152:155], a[72:75], a[56:59], v[152:155]\n" \
"v_exp_f32_e64 v149, v149\n" \
"v_mfma_f32_16x16x32_bf16 v[152:155], a[76:79], a[60:63], v[152:155]\n" \
"v_cvt_pk_bf16_f32 v216, v144, v145\n" \
"v_cvt_pk_bf16_f32 v217, v146, v147\n" \
"v_mfma_f32_16x16x32_bf16 v[156:159], a[80:83], a[48:51], v[172:175]\n" \
"v_exp_f32_e64 v150, v150\n" \
"v_mfma_f32_16x16x32_bf16 v[156:159], a[84:87], a[52:55], v[156:159]\n" \
"ds_read_b128 a[116:119], v208 offset:12288\n" \
"s_waitcnt lgkmcnt(6)\n" \
"v_mfma_f32_16x16x32_bf16 v[156:159], a[88:91], a[56:59], v[156:159]\n" \
"buffer_load_dwordx4 v214, %[srdk], s48 offen offset:3072 lds\n" \
"v_mfma_f32_16x16x32_bf16 v[156:159], a[92:95], a[60:63], v[156:159]\n" \
"v_exp_f32_e32 v151, v151\n" \
"v_mfma_f32_16x16x32_bf16 v[16:19], a[176:179], v[192:195], v[16:19]\n" \
"ds_read_b128 a[120:123], v209 offset:12288\n" \
"v_mfma_f32_16x16x32_bf16 v[16:19], a[144:147], v[184:187], v[16:19]\n" \
"v_exp_f32_e64 v152, v152\n" \
"v_mfma_f32_16x16x32_bf16 v[20:23], a[180:183], v[192:195], v[20:23]\n" \
"v_cvt_pk_bf16_f32 v218, v148, v149\n" \
"v_cvt_pk_bf16_f32 v219, v150, v151\n" \
"v_mfma_f32_16x16x32_bf16 v[20:23], a[148:151], v[184:187], v[20:23]\n" \
"v_exp_f32_e64 v153, v153\n" \
"v_mfma_f32_16x16x32_bf16 v[24:27], a[184:187], v[192:195], v[24:27]\n" \
"v_exp_f32_e64 v154, v154\n" \
"v_mfma_f32_16x16x32_bf16 v[24:27], a[152:155], v[184:187], v[24:27]\n" \
"ds_read_b128 a[124:127], v210 offset:12288\n" \
"s_add_u32 s48, s48, 0x4000\n" \
"v_mfma_f32_16x16x32_bf16 v[28:31], a[188:191], v[192:195], v[28:31]\n" \
"v_exp_f32_e64 v155, v155\n" \
"v_mfma_f32_16x16x32_bf16 v[28:31], a[156:159], v[184:187], v[28:31]\n" \
"s_waitcnt vmcnt(17) lgkmcnt(2)\n" \
"s_barrier\n" \
"v_mfma_f32_16x16x32_bf16 v[128:131], a[96:99], a[0:3], v[160:163]\n" \
"v_exp_f32_e64 v156, v156\n" \
"v_mfma_f32_16x16x32_bf16 v[128:131], a[100:103], a[4:7], v[128:131]\n" \
"s_add_u32 m0, %[m0b], 114688\n" \
"v_mfma_f32_16x16x32_bf16 v[128:131], a[104:107], a[8:11], v[128:131]\n" \
"v_exp_f32_e64 v157, v157\n" \
"v_mfma_f32_16x16x32_bf16 v[128:131], a[108:111], a[12:15], v[128:131]\n" \
"v_cvt_pk_bf16_f32 v220, v152, v153\n" \
"v_cvt_pk_bf16_f32 v221, v154, v155\n" \
"v_mfma_f32_16x16x32_bf16 v[132:135], a[112:115], a[0:3], v[160:163]\n" \
"v_exp_f32_e64 v158, v158\n" \
"v_mfma_f32_16x16x32_bf16 v[132:135], a[116:119], a[4:7], v[132:135]\n" \
"s_waitcnt lgkmcnt(0)\n" \
"v_mfma_f32_16x16x32_bf16 v[132:135], a[120:123], a[8:11], v[132:135]\n" \
"buffer_load_dwordx4 %[vd0], %[srdv], s48 offen offset:0 lds\n" \
"v_mfma_f32_16x16x32_bf16 v[132:135], a[124:127], a[12:15], v[132:135]\n" \
"v_exp_f32_e32 v159, v159\n" \
"v_mfma_f32_16x16x32_bf16 v[32:35], a[160:163], v[196:199], v[32:35]\n" \
"ds_read_b128 a[64:67], %[kb0] offset:16384\n" \
"v_mfma_f32_16x16x32_bf16 v[32:35], a[128:131], v[188:191], v[32:35]\n" \
"v_exp_f32_e64 v128, v128\n" \
"v_mfma_f32_16x16x32_bf16 v[36:39], a[164:167], v[196:199], v[36:39]\n" \
"v_cvt_pk_bf16_f32 v222, v156, v157\n" \
"v_cvt_pk_bf16_f32 v223, v158, v159\n" \
"v_mfma_f32_16x16x32_bf16 v[36:39], a[132:135], v[188:191], v[36:39]\n" \
"v_exp_f32_e64 v129, v129\n" \
"v_mfma_f32_16x16x32_bf16 v[40:43], a[168:171], v[196:199], v[40:43]\n" \
"ds_read_b128 a[68:71], v208 offset:16384\n" \
"v_mfma_f32_16x16x32_bf16 v[40:43], a[136:139], v[188:191], v[40:43]\n" \
"v_exp_f32_e64 v130, v130\n" \
"v_mfma_f32_16x16x32_bf16 v[44:47], a[172:175], v[196:199], v[44:47]\n" \
"ds_read_b128 a[72:75], v209 offset:16384\n" \
"v_mfma_f32_16x16x32_bf16 v[44:47], a[140:143], v[188:191], v[44:47]\n" \
"v_exp_f32_e64 v131, v131\n" \
"v_smfmac_f32_16x16x64_bf16 v[176:179], a[192:195], v[184:191], v228\n" \
"v_mfma_f32_16x16x32_bf16 v[136:139], a[96:99], a[16:19], v[164:167]\n" \
"v_exp_f32_e64 v132, v132\n" \
"v_mfma_f32_16x16x32_bf16 v[136:139], a[100:103], a[20:23], v[136:139]\n" \
"ds_read_b128 a[76:79], v210 offset:16384\n" \
"v_mfma_f32_16x16x32_bf16 v[136:139], a[104:107], a[24:27], v[136:139]\n" \
"v_exp_f32_e64 v133, v133\n" \
"v_mfma_f32_16x16x32_bf16 v[136:139], a[108:111], a[28:31], v[136:139]\n" \
"v_cvt_pk_bf16_f32 v192, v128, v129\n" \
"v_cvt_pk_bf16_f32 v193, v130, v131\n" \
"v_mfma_f32_16x16x32_bf16 v[140:143], a[112:115], a[16:19], v[164:167]\n" \
"v_exp_f32_e64 v134, v134\n" \
"v_mfma_f32_16x16x32_bf16 v[140:143], a[116:119], a[20:23], v[140:143]\n" \
"ds_read_b128 a[80:83], %[kb0] offset:20480\n" \
"v_mfma_f32_16x16x32_bf16 v[140:143], a[120:123], a[24:27], v[140:143]\n" \
"buffer_load_dwordx4 v215, %[srdv], s48 offen offset:1024 lds\n" \
"v_mfma_f32_16x16x32_bf16 v[140:143], a[124:127], a[28:31], v[140:143]\n" \
"v_exp_f32_e64 v135, v135\n" \
"v_mfma_f32_16x16x32_bf16 v[48:51], a[176:179], v[196:199], v[48:51]\n" \
"ds_read_b128 a[84:87], v208 offset:20480\n" \
"v_mfma_f32_16x16x32_bf16 v[48:51], a[144:147], v[188:191], v[48:51]\n" \
"v_exp_f32_e64 v136, v136\n" \
"v_mfma_f32_16x16x32_bf16 v[52:55], a[180:183], v[196:199], v[52:55]\n" \
"v_cvt_pk_bf16_f32 v194, v132, v133\n" \
"v_cvt_pk_bf16_f32 v195, v134, v135\n" \
"v_mfma_f32_16x16x32_bf16 v[52:55], a[148:151], v[188:191], v[52:55]\n" \
"v_exp_f32_e64 v137, v137\n" \
"v_mfma_f32_16x16x32_bf16 v[56:59], a[184:187], v[196:199], v[56:59]\n" \
"v_exp_f32_e64 v138, v138\n" \
"v_mfma_f32_16x16x32_bf16 v[56:59], a[152:155], v[188:191], v[56:59]\n" \
"ds_read_b128 a[88:91], v209 offset:20480\n" \
"v_mfma_f32_16x16x32_bf16 v[60:63], a[188:191], v[196:199], v[60:63]\n" \
"v_exp_f32_e64 v139, v139\n" \
"v_mfma_f32_16x16x32_bf16 v[60:63], a[156:159], v[188:191], v[60:63]\n" \
"s_cmp_lg_u32 %[wid], 3\n" \
"s_cbranch_scc1 LtB1_%=\n" \
"global_load_dword v237, v236, s[58:59]\n" \
"s_add_u32 s61, s48, 49152\n" \
"s_mov_b32 exec_hi, 0\n" \
"s_nop 0\n" \
"buffer_load_dword v237, v224, %[srdp], s61 offen\n" \
"s_mov_b32 exec_hi, -1\n" \
"s_nop 0\n" \
"LtB1_%=:\n" \
"v_mfma_f32_16x16x32_bf16 v[144:147], a[96:99], a[32:35], v[168:171]\n" \
"v_exp_f32_e64 v140, v140\n" \
"v_mfma_f32_16x16x32_bf16 v[144:147], a[100:103], a[36:39], v[144:147]\n" \
"ds_read_b128 a[92:95], v210 offset:20480\n" \
"v_mfma_f32_16x16x32_bf16 v[144:147], a[104:107], a[40:43], v[144:147]\n" \
"v_exp_f32_e64 v141, v141\n" \
"v_mfma_f32_16x16x32_bf16 v[144:147], a[108:111], a[44:47], v[144:147]\n" \
"v_cvt_pk_bf16_f32 v196, v136, v137\n" \
"v_cvt_pk_bf16_f32 v197, v138, v139\n" \
"v_mfma_f32_16x16x32_bf16 v[148:151], a[112:115], a[32:35], v[168:171]\n" \
"v_exp_f32_e64 v142, v142\n" \
"v_mfma_f32_16x16x32_bf16 v[148:151], a[116:119], a[36:39], v[148:151]\n" \
"v_mfma_f32_16x16x32_bf16 v[148:151], a[120:123], a[40:43], v[148:151]\n" \
"buffer_load_dwordx4 %[vd0], %[srdv], s48 offen offset:2048 lds\n" \
"v_mfma_f32_16x16x32_bf16 v[148:151], a[124:127], a[44:47], v[148:151]\n" \
"v_exp_f32_e64 v143, v143\n" \
"v_mfma_f32_16x16x32_bf16 v[96:99], a[160:163], v[204:207], v[96:99]\n" \
"ds_read_b64_tr_b16 a[160:161], %[vvb] offset:8192\n" \
"v_mfma_f32_16x16x32_bf16 v[96:99], a[128:131], v[220:223], v[96:99]\n" \
"v_exp_f32_e64 v144, v144\n" \
"v_mfma_f32_16x16x32_bf16 v[100:103], a[164:167], v[204:207], v[100:103]\n" \
"v_cvt_pk_bf16_f32 v198, v140, v141\n" \
"v_cvt_pk_bf16_f32 v199, v142, v143\n" \
"v_mfma_f32_16x16x32_bf16 v[100:103], a[132:135], v[220:223], v[100:103]\n" \
"v_exp_f32_e64 v145, v145\n" \
"v_mfma_f32_16x16x32_bf16 v[104:107], a[168:171], v[204:207], v[104:107]\n" \
"ds_read_b64_tr_b16 a[162:163], %[vvb] offset:12288\n" \
"ds_read_b64_tr_b16 a[164:165], v211 offset:8192\n" \
"v_mfma_f32_16x16x32_bf16 v[104:107], a[136:139], v[220:223], v[104:107]\n" \
"v_exp_f32_e64 v146, v146\n" \
"v_mfma_f32_16x16x32_bf16 v[108:111], a[172:175], v[204:207], v[108:111]\n" \
"ds_read_b64_tr_b16 a[166:167], v211 offset:12288\n" \
"v_mfma_f32_16x16x32_bf16 v[108:111], a[140:143], v[220:223], v[108:111]\n" \
"v_exp_f32_e64 v147, v147\n" \
"v_smfmac_f32_16x16x64_bf16 v[180:183], a[192:195], v[216:223], v228\n" \
"ds_read_b64_tr_b16 a[168:169], %[vvb] offset:8704\n" \
"v_mfma_f32_16x16x32_bf16 v[152:155], a[96:99], a[48:51], v[172:175]\n" \
"v_exp_f32_e64 v148, v148\n" \
"v_mfma_f32_16x16x32_bf16 v[152:155], a[100:103], a[52:55], v[152:155]\n" \
"ds_read_b64_tr_b16 a[170:171], %[vvb] offset:12800\n" \
"v_mfma_f32_16x16x32_bf16 v[152:155], a[104:107], a[56:59], v[152:155]\n" \
"v_exp_f32_e64 v149, v149\n" \
"v_mfma_f32_16x16x32_bf16 v[152:155], a[108:111], a[60:63], v[152:155]\n" \
"v_cvt_pk_bf16_f32 v200, v144, v145\n" \
"v_cvt_pk_bf16_f32 v201, v146, v147\n" \
"v_mfma_f32_16x16x32_bf16 v[156:159], a[112:115], a[48:51], v[172:175]\n" \
"v_exp_f32_e64 v150, v150\n" \
"v_mfma_f32_16x16x32_bf16 v[156:159], a[116:119], a[52:55], v[156:159]\n" \
"ds_read_b64_tr_b16 a[172:173], v211 offset:8704\n" \
"v_mfma_f32_16x16x32_bf16 v[156:159], a[120:123], a[56:59], v[156:159]\n" \
"buffer_load_dwordx4 v215, %[srdv], s48 offen offset:3072 lds\n" \
"v_mfma_f32_16x16x32_bf16 v[156:159], a[124:127], a[60:63], v[156:159]\n" \
"v_exp_f32_e64 v151, v151\n" \
"v_mfma_f32_16x16x32_bf16 v[112:115], a[176:179], v[204:207], v[112:115]\n" \
"ds_read_b64_tr_b16 a[174:175], v211 offset:12800\n" \
"ds_read_b64_tr_b16 a[176:177], %[vvb] offset:9216\n" \
"v_mfma_f32_16x16x32_bf16 v[112:115], a[144:147], v[220:223], v[112:115]\n" \
"v_exp_f32_e64 v152, v152\n" \
"v_mfma_f32_16x16x32_bf16 v[116:119], a[180:183], v[204:207], v[116:119]\n" \
"v_cvt_pk_bf16_f32 v202, v148, v149\n" \
"v_cvt_pk_bf16_f32 v203, v150, v151\n" \
"v_mfma_f32_16x16x32_bf16 v[116:119], a[148:151], v[220:223], v[116:119]\n" \
"v_exp_f32_e64 v153, v153\n" \
"v_mfma_f32_16x16x32_bf16 v[120:123], a[184:187], v[204:207], v[120:123]\n" \
"v_exp_f32_e64 v154, v154\n" \
"v_mfma_f32_16x16x32_bf16 v[120:123], a[152:155], v[220:223], v[120:123]\n" \
"ds_read_b64_tr_b16 a[178:179], %[vvb] offset:13312\n" \
"v_mfma_f32_16x16x32_bf16 v[124:127], a[188:191], v[204:207], v[124:127]\n" \
"v_exp_f32_e64 v155, v155\n" \
"v_mfma_f32_16x16x32_bf16 v[124:127], a[156:159], v[220:223], v[124:127]\n" \
"ds_read_b64_tr_b16 a[180:181], v211 offset:9216\n" \
"s_waitcnt lgkmcnt(13)\n" \
"s_cmp_lg_u32 s60, 7\n" \
"s_cbranch_scc1 LmB_%=\n" \
"s_nop 0\n" \
"v_readlane_b32 s48, %[tlo], 11\n" \
"v_readlane_b32 s58, %[tlo], 12\n" \
"v_mfma_f32_16x16x32_bf16 v[128:131], a[64:67], a[0:3], v[160:163]\n" \
"v_exp_f32_e32 v156, v156\n" \
"v_lshlrev_b32_e32 v229, 2, v224\n" \
"v_mfma_f32_16x16x32_bf16 v[128:131], a[68:71], a[4:7], v[128:131]\n" \
"ds_read_b64_tr_b16 a[182:183], v211 offset:13312\n" \
"s_add_u32 m0, %[m0b], 0\n" \
"s_mov_b32 s49, s48\n" \
"v_mfma_f32_16x16x32_bf16 v[128:131], a[72:75], a[8:11], v[128:131]\n" \
"v_exp_f32_e64 v157, v157\n" \
"v_and_b32_e32 v229, 0x1800, v229\n" \
"v_mfma_f32_16x16x32_bf16 v[128:131], a[76:79], a[12:15], v[128:131]\n" \
"v_cvt_pk_bf16_f32 v204, v152, v153\n" \
"v_cvt_pk_bf16_f32 v205, v154, v155\n" \
"v_bfe_u32 %[ta0], v224, 11, 1\n" \
"v_mfma_f32_16x16x32_bf16 v[132:135], a[80:83], a[0:3], v[160:163]\n" \
"v_exp_f32_e64 v158, v158\n" \
"v_lshl_or_b32 v229, %[ta0], 15, v229\n" \
"v_lshrrev_b32_e64 %[ta0], 12, v224\n" \
"v_lshl_or_b32 v229, %[ta0], 13, v229\n" \
"v_mfma_f32_16x16x32_bf16 v[132:135], a[84:87], a[4:7], v[132:135]\n" \
"ds_read_b64_tr_b16 a[184:185], %[vvb] offset:9728\n" \
"s_lshl_b32 s49, s49, 18\n" \
"s_or_b32 s49, s49, s58\n" \
"v_and_or_b32 v229, v235, 48, v229\n" \
"s_waitcnt lgkmcnt(5)\n" \
"v_mfma_f32_16x16x32_bf16 v[132:135], a[88:91], a[8:11], v[132:135]\n" \
"buffer_load_dwordx4 %[wo0], %[srdt], s49 offen offset:0 lds\n" \
"v_mfma_f32_16x16x32_bf16 v[132:135], a[92:95], a[12:15], v[132:135]\n" \
"v_exp_f32_e32 v159, v159\n" \
"v_readlane_b32 s51, %[tlo], 31\n" \
"v_mfma_f32_16x16x32_bf16 v[64:67], a[128:131], v[216:219], v[64:67]\n" \
"ds_read_b64_tr_b16 a[186:187], %[vvb] offset:13824\n" \
"ds_read_b64_tr_b16 a[188:189], v211 offset:9728\n" \
"v_readlane_b32 s60, %[thi], 11\n" \
"v_mfma_f32_16x16x32_bf16 v[64:67], a[160:163], v[200:203], v[64:67]\n" \
"v_exp_f32_e32 v128, v128\n" \
"s_lshl_b32 s62, %[wid], 10\n" \
"v_mfma_f32_16x16x32_bf16 v[68:71], a[132:135], v[216:219], v[68:71]\n" \
"v_cvt_pk_bf16_f32 v206, v156, v157\n" \
"v_cvt_pk_bf16_f32 v207, v158, v159\n" \
"s_add_u32 s62, s62, 262144\n" \
"v_mfma_f32_16x16x32_bf16 v[68:71], a[164:167], v[200:203], v[68:71]\n" \
"v_exp_f32_e64 v129, v129\n" \
"s_add_u32 s55, s62, 8192\n" \
"v_mfma_f32_16x16x32_bf16 v[72:75], a[136:139], v[216:219], v[72:75]\n" \
"ds_read_b64_tr_b16 a[190:191], v211 offset:13824\n" \
"ds_read_b64_tr_b16 a[128:129], %[vvb] offset:16384\n" \
"v_readlane_b32 s59, %[thi], 12\n" \
"v_mfma_f32_16x16x32_bf16 v[72:75], a[168:171], v[200:203], v[72:75]\n" \
"v_exp_f32_e64 v130, v130\n" \
"v_readlane_b32 s68, %[tlo], 20\n" \
"v_mfma_f32_16x16x32_bf16 v[76:79], a[140:143], v[216:219], v[76:79]\n" \
"ds_read_b64_tr_b16 a[130:131], %[vvb] offset:20480\n" \
"ds_read_b64_tr_b16 a[132:133], v211 offset:16384\n" \
"v_readlane_b32 s53, %[tlo], 13\n" \
"v_mfma_f32_16x16x32_bf16 v[76:79], a[172:175], v[200:203], v[76:79]\n" \
"v_exp_f32_e64 v131, v131\n" \
"v_readlane_b32 s69, %[thi], 20\n" \
"s_lshl_b32 s63, %[wid], 9\n" \
"v_smfmac_f32_16x16x64_bf16 v[176:179], a[192:195], v[192:199], v228\n" \
"ds_read_b64_tr_b16 a[134:135], v211 offset:20480\n" \
"ds_read_b64_tr_b16 a[136:137], %[vvb] offset:16896\n" \
"v_readlane_b32 s50, %[thi], 13\n" \
"v_mfma_f32_16x16x32_bf16 v[136:139], a[64:67], a[16:19], v[164:167]\n" \
"v_exp_f32_e32 v132, v132\n" \
"v_lshrrev_b32_e32 v225, 4, v224\n" \
"v_add_u32_e32 v225, s63, v225\n" \
"v_mfma_f32_16x16x32_bf16 v[136:139], a[68:71], a[20:23], v[136:139]\n" \
"ds_read_b64_tr_b16 a[138:139], %[vvb] offset:20992\n" \
"ds_read_b64_tr_b16 a[140:141], v211 offset:16896\n" \
"v_readlane_b32 s46, %[tlo], 14\n" \
"v_add_u32_e32 v212, 0x3c00, %[wo0]\n" \
"v_mfma_f32_16x16x32_bf16 v[136:139], a[72:75], a[24:27], v[136:139]\n" \
"v_exp_f32_e64 v133, v133\n" \
"buffer_load_dwordx4 v[230:233], v235, %[srdz], s62 offen sc1\n" \
"v_mfma_f32_16x16x32_bf16 v[136:139], a[76:79], a[28:31], v[136:139]\n" \
"v_cvt_pk_bf16_f32 v184, v128, v129\n" \
"v_cvt_pk_bf16_f32 v185, v130, v131\n" \
"global_load_dwordx2 v[226:227], v225, s[68:69]\n" \
"v_mfma_f32_16x16x32_bf16 v[140:143], a[80:83], a[16:19], v[164:167]\n" \
"v_exp_f32_e64 v134, v134\n" \
"buffer_load_dwordx4 v[234:237], v235, %[srdz], s55 offen sc1\n" \
"v_mfma_f32_16x16x32_bf16 v[140:143], a[84:87], a[20:23], v[140:143]\n" \
"ds_read_b64_tr_b16 a[142:143], v211 offset:20992\n" \
"v_readlane_b32 s47, %[thi], 14\n" \
"v_add_u32_e32 v213, 0x7800, %[wo0]\n" \
"s_waitcnt lgkmcnt(8)\n" \
"v_mfma_f32_16x16x32_bf16 v[140:143], a[88:91], a[24:27], v[140:143]\n" \
"buffer_load_dwordx4 v212, %[srdt], s49 offen offset:1024 lds\n" \
"v_mfma_f32_16x16x32_bf16 v[140:143], a[92:95], a[28:31], v[140:143]\n" \
"v_exp_f32_e32 v135, v135\n" \
"v_mfma_f32_16x16x32_bf16 v[80:83], a[144:147], v[216:219], v[80:83]\n" \
"ds_read_b64_tr_b16 a[144:145], %[vvb] offset:17408\n" \
"ds_read_b64_tr_b16 a[146:147], %[vvb] offset:21504\n" \
"v_mfma_f32_16x16x32_bf16 v[80:83], a[176:179], v[200:203], v[80:83]\n" \
"v_exp_f32_e64 v136, v136\n" \
"v_mfma_f32_16x16x32_bf16 v[84:87], a[148:151], v[216:219], v[84:87]\n" \
"v_cvt_pk_bf16_f32 v186, v132, v133\n" \
"v_cvt_pk_bf16_f32 v187, v134, v135\n" \
"v_mfma_f32_16x16x32_bf16 v[84:87], a[180:183], v[200:203], v[84:87]\n" \
"v_exp_f32_e64 v137, v137\n" \
"v_mfma_f32_16x16x32_bf16 v[88:91], a[152:155], v[216:219], v[88:91]\n" \
"v_exp_f32_e64 v138, v138\n" \
"v_mfma_f32_16x16x32_bf16 v[88:91], a[184:187], v[200:203], v[88:91]\n" \
"ds_read_b64_tr_b16 a[148:149], v211 offset:17408\n" \
"ds_read_b64_tr_b16 a[150:151], v211 offset:21504\n" \
"v_mfma_f32_16x16x32_bf16 v[92:95], a[156:159], v[216:219], v[92:95]\n" \
"v_exp_f32_e64 v139, v139\n" \
"v_mfma_f32_16x16x32_bf16 v[92:95], a[188:191], v[200:203], v[92:95]\n" \
"ds_read_b64_tr_b16 a[152:153], %[vvb] offset:17920\n" \
"buffer_load_dwordx4 a[212:215], v229, %[srdt], s51 offen offset:0\n" \
"v_mfma_f32_16x16x32_bf16 v[144:147], a[64:67], a[32:35], v[168:171]\n" \
"v_exp_f32_e64 v140, v140\n" \
"v_mfma_f32_16x16x32_bf16 v[144:147], a[68:71], a[36:39], v[144:147]\n" \
"ds_read_b64_tr_b16 a[154:155], %[vvb] offset:22016\n" \
"ds_read_b64_tr_b16 a[156:157], v211 offset:17920\n" \
"v_mfma_f32_16x16x32_bf16 v[144:147], a[72:75], a[40:43], v[144:147]\n" \
"v_exp_f32_e64 v141, v141\n" \
"v_mfma_f32_16x16x32_bf16 v[144:147], a[76:79], a[44:47], v[144:147]\n" \
"v_cvt_pk_bf16_f32 v188, v136, v137\n" \
"v_cvt_pk_bf16_f32 v189, v138, v139\n" \
"v_mfma_f32_16x16x32_bf16 v[148:151], a[80:83], a[32:35], v[168:171]\n" \
"v_exp_f32_e64 v142, v142\n" \
"v_mfma_f32_16x16x32_bf16 v[148:151], a[84:87], a[36:39], v[148:151]\n" \
"ds_read_b64_tr_b16 a[158:159], v211 offset:22016\n" \
"s_waitcnt lgkmcnt(8)\n" \
"v_mfma_f32_16x16x32_bf16 v[148:151], a[88:91], a[40:43], v[148:151]\n" \
"buffer_load_dwordx4 v213, %[srdt], s49 offen offset:2048 lds\n" \
"v_mfma_f32_16x16x32_bf16 v[148:151], a[92:95], a[44:47], v[148:151]\n" \
"v_exp_f32_e32 v143, v143\n" \
"v_mfma_f32_16x16x32_bf16 v[0:3], a[160:163], v[192:195], v[0:3]\n" \
"ds_read_b128 a[96:99], %[kb0] offset:24576\n" \
"buffer_load_dwordx4 a[216:219], v229, %[srdt], s51 offen offset:64\n" \
"v_mfma_f32_16x16x32_bf16 v[0:3], a[128:131], v[184:187], v[0:3]\n" \
"v_exp_f32_e64 v144, v144\n" \
"v_mfma_f32_16x16x32_bf16 v[4:7], a[164:167], v[192:195], v[4:7]\n" \
"v_cvt_pk_bf16_f32 v190, v140, v141\n" \
"v_cvt_pk_bf16_f32 v191, v142, v143\n" \
"v_mfma_f32_16x16x32_bf16 v[4:7], a[132:135], v[184:187], v[4:7]\n" \
"v_exp_f32_e64 v145, v145\n" \
"v_mfma_f32_16x16x32_bf16 v[8:11], a[168:171], v[192:195], v[8:11]\n" \
"ds_read_b128 a[100:103], v208 offset:24576\n" \
"buffer_load_dwordx4 a[220:223], v229, %[srdt], s51 offen offset:128\n" \
"v_mfma_f32_16x16x32_bf16 v[8:11], a[136:139], v[184:187], v[8:11]\n" \
"v_exp_f32_e64 v146, v146\n" \
"v_mfma_f32_16x16x32_bf16 v[12:15], a[172:175], v[192:195], v[12:15]\n" \
"ds_read_b128 a[104:107], v209 offset:24576\n" \
"v_add_u32_e32 v214, 0xb400, %[wo0]\n" \
"v_mfma_f32_16x16x32_bf16 v[12:15], a[140:143], v[184:187], v[12:15]\n" \
"v_exp_f32_e64 v147, v147\n" \
"v_smfmac_f32_16x16x64_bf16 v[180:183], a[192:195], v[200:207], v228\n" \
"ds_read_b128 a[108:111], v210 offset:24576\n" \
"buffer_load_dwordx4 a[224:227], v229, %[srdt], s51 offen offset:192\n" \
"v_mfma_f32_16x16x32_bf16 v[152:155], a[64:67], a[48:51], v[172:175]\n" \
"v_exp_f32_e64 v148, v148\n" \
"v_mfma_f32_16x16x32_bf16 v[152:155], a[68:71], a[52:55], v[152:155]\n" \
"ds_read_b128 a[112:115], %[kb0] offset:28672\n" \
"buffer_load_dwordx4 a[228:231], v229, %[srdt], s51 offen offset:256\n" \
"v_mfma_f32_16x16x32_bf16 v[152:155], a[72:75], a[56:59], v[152:155]\n" \
"v_exp_f32_e64 v149, v149\n" \
"v_mfma_f32_16x16x32_bf16 v[152:155], a[76:79], a[60:63], v[152:155]\n" \
"v_cvt_pk_bf16_f32 v216, v144, v145\n" \
"v_cvt_pk_bf16_f32 v217, v146, v147\n" \
"v_mfma_f32_16x16x32_bf16 v[156:159], a[80:83], a[48:51], v[172:175]\n" \
"v_exp_f32_e64 v150, v150\n" \
"v_mfma_f32_16x16x32_bf16 v[156:159], a[84:87], a[52:55], v[156:159]\n" \
"ds_read_b128 a[116:119], v208 offset:28672\n" \
"s_waitcnt lgkmcnt(6)\n" \
"v_mfma_f32_16x16x32_bf16 v[156:159], a[88:91], a[56:59], v[156:159]\n" \
"buffer_load_dwordx4 v214, %[srdt], s49 offen offset:3072 lds\n" \
"v_mfma_f32_16x16x32_bf16 v[156:159], a[92:95], a[60:63], v[156:159]\n" \
"v_exp_f32_e32 v151, v151\n" \
"v_mfma_f32_16x16x32_bf16 v[16:19], a[176:179], v[192:195], v[16:19]\n" \
"ds_read_b128 a[120:123], v209 offset:28672\n" \
"buffer_load_dwordx4 a[232:235], v229, %[srdt], s51 offen offset:320\n" \
"v_mfma_f32_16x16x32_bf16 v[16:19], a[144:147], v[184:187], v[16:19]\n" \
"v_exp_f32_e64 v152, v152\n" \
"buffer_load_dwordx4 a[236:239], v229, %[srdt], s51 offen offset:384\n" \
"v_mfma_f32_16x16x32_bf16 v[20:23], a[180:183], v[192:195], v[20:23]\n" \
"v_cvt_pk_bf16_f32 v218, v148, v149\n" \
"v_cvt_pk_bf16_f32 v219, v150, v151\n" \
"buffer_load_dwordx4 a[196:199], v229, %[srdt], s51 offen offset:448\n" \
"v_mfma_f32_16x16x32_bf16 v[20:23], a[148:151], v[184:187], v[20:23]\n" \
"v_exp_f32_e64 v153, v153\n" \
"v_mfma_f32_16x16x32_bf16 v[24:27], a[184:187], v[192:195], v[24:27]\n" \
"v_exp_f32_e64 v154, v154\n" \
"v_mfma_f32_16x16x32_bf16 v[24:27], a[152:155], v[184:187], v[24:27]\n" \
"ds_read_b128 a[124:127], v210 offset:28672\n" \
"s_waitcnt vmcnt(11)\n" \
"v_bitop3_b32 v225, v230, v231, v232 bitop3:0x80\n" \
"v_mfma_f32_16x16x32_bf16 v[28:31], a[188:191], v[192:195], v[28:31]\n" \
"v_exp_f32_e32 v155, v155\n" \
"v_bitop3_b32 v225, v225, v233, v234 bitop3:0x80\n" \
"v_bitop3_b32 v225, v225, v235, v236 bitop3:0x80\n" \
"v_bitop3_b32 v225, v225, v237, 1 bitop3:0x80\n" \
"v_mfma_f32_16x16x32_bf16 v[28:31], a[156:159], v[184:187], v[28:31]\n" \
"s_waitcnt vmcnt(12) lgkmcnt(2)\n" \
"s_barrier\n" \
".if . - adaln_body != 32768\n" \
".error \"size model\"\n" \
".endif\n" \
"Lk7_%=:\n" \
"s_cbranch_execz Lk8_%=\n" \
"s_nop 0\n" \
"v_mfma_f32_16x16x32_bf16 v[128:131], a[96:99], a[0:3], v[160:163]\n" \
"v_exp_f32_e64 v156, v156\n" \
"v_readlane_b32 s64, %[tlo], 8\n" \
"v_readlane_b32 s65, %[thi], 8\n" \
"v_cmp_eq_u32_e32 vcc, 0, v225\n" \
"v_mfma_f32_16x16x32_bf16 v[128:131], a[100:103], a[4:7], v[128:131]\n" \
"s_cbranch_vccnz Lus_%=\n" \
"Lub_%=:\n" \
"v_mfma_f32_16x16x32_bf16 v[128:131], a[104:107], a[8:11], v[128:131]\n" \
"v_exp_f32_e32 v157, v157\n" \
"v_lshlrev_b32_e32 v225, 16, v226\n" \
"v_mfma_f32_16x16x32_bf16 v[128:131], a[108:111], a[12:15], v[128:131]\n" \
"v_cvt_pk_bf16_f32 v220, v152, v153\n" \
"v_cvt_pk_bf16_f32 v221, v154, v155\n" \
"v_and_b32_e32 v226, 0xffff0000, v226\n" \
"v_mfma_f32_16x16x32_bf16 v[132:135], a[112:115], a[0:3], v[160:163]\n" \
"v_exp_f32_e32 v158, v158\n" \
"v_mul_f32_e32 v234, v234, v225\n" \
"v_mul_f32_e64 v235, v235, v226\n" \
"v_mfma_f32_16x16x32_bf16 v[132:135], a[116:119], a[4:7], v[132:135]\n" \
"v_mul_f32_e32 v230, v230, v234\n" \
"s_waitcnt lgkmcnt(0)\n" \
"v_mfma_f32_16x16x32_bf16 v[132:135], a[120:123], a[8:11], v[132:135]\n" \
"s_add_u32 m0, %[m0b], 65536\n" \
"s_mov_b32 s61, 0\n" \
"buffer_load_dwordx4 %[vg0], s[64:67], s61 offen offset:0 lds\n" \
"v_mfma_f32_16x16x32_bf16 v[132:135], a[124:127], a[12:15], v[132:135]\n" \
"v_exp_f32_e32 v159, v159\n" \
"v_mul_f32_e64 v231, v231, v235\n" \
"v_mfma_f32_16x16x32_bf16 v[32:35], a[160:163], v[196:199], v[32:35]\n" \
"ds_read_b128 a[64:67], %[kb0] offset:32768\n" \
"v_cvt_pk_bf16_f32 v230, v230, v231\n" \
"v_cvt_pk_bf16_f32 v234, v234, v235\n" \
"v_mfma_f32_16x16x32_bf16 v[32:35], a[128:131], v[188:191], v[32:35]\n" \
"v_exp_f32_e32 v128, v128\n" \
"v_lshlrev_b32_e32 v225, 16, v227\n" \
"v_mfma_f32_16x16x32_bf16 v[36:39], a[164:167], v[196:199], v[36:39]\n" \
"v_cvt_pk_bf16_f32 v222, v156, v157\n" \
"v_cvt_pk_bf16_f32 v223, v158, v159\n" \
"v_and_b32_e32 v227, 0xffff0000, v227\n" \
"v_mfma_f32_16x16x32_bf16 v[36:39], a[132:135], v[188:191], v[36:39]\n" \
"v_exp_f32_e64 v129, v129\n" \
"v_mfma_f32_16x16x32_bf16 v[40:43], a[168:171], v[196:199], v[40:43]\n" \
"ds_read_b128 a[68:71], v208 offset:32768\n" \
"v_mul_f32_e64 v236, v236, v225\n" \
"v_mfma_f32_16x16x32_bf16 v[40:43], a[136:139], v[188:191], v[40:43]\n" \
"v_exp_f32_e64 v130, v130\n" \
"v_mfma_f32_16x16x32_bf16 v[44:47], a[172:175], v[196:199], v[44:47]\n" \
"ds_read_b128 a[72:75], v209 offset:32768\n" \
"v_mul_f32_e64 v237, v237, v227\n" \
"v_mfma_f32_16x16x32_bf16 v[44:47], a[140:143], v[188:191], v[44:47]\n" \
"v_exp_f32_e64 v131, v131\n" \
"v_smfmac_f32_16x16x64_bf16 v[176:179], a[192:195], v[184:191], v228\n" \
"v_mul_f32_e32 v232, v232, v236\n" \
"v_mul_f32_e32 v233, v233, v237\n" \
"v_mfma_f32_16x16x32_bf16 v[136:139], a[96:99], a[16:19], v[164:167]\n" \
"v_exp_f32_e64 v132, v132\n" \
"v_mfma_f32_16x16x32_bf16 v[136:139], a[100:103], a[20:23], v[136:139]\n" \
"ds_read_b128 a[76:79], v210 offset:32768\n" \
"v_cvt_pk_bf16_f32 v231, v232, v233\n" \
"v_cvt_pk_bf16_f32 v235, v236, v237\n" \
"v_mfma_f32_16x16x32_bf16 v[136:139], a[104:107], a[24:27], v[136:139]\n" \
"v_exp_f32_e64 v133, v133\n" \
"s_add_u32 s62, %[m0t], 6144\n" \
"v_mfma_f32_16x16x32_bf16 v[136:139], a[108:111], a[28:31], v[136:139]\n" \
"v_cvt_pk_bf16_f32 v192, v128, v129\n" \
"v_cvt_pk_bf16_f32 v193, v130, v131\n" \
"v_lshrrev_b32_e64 v232, 4, v224\n" \
"v_mfma_f32_16x16x32_bf16 v[140:143], a[112:115], a[16:19], v[164:167]\n" \
"v_exp_f32_e32 v134, v134\n" \
"v_add_u32_e32 v232, s62, v232\n" \
"v_mfma_f32_16x16x32_bf16 v[140:143], a[116:119], a[20:23], v[140:143]\n" \
"ds_read_b128 a[80:83], %[kb0] offset:36864\n" \
"ds_write_b64 v232, v[230:231] offset:0\n" \
"v_mfma_f32_16x16x32_bf16 v[140:143], a[120:123], a[24:27], v[140:143]\n" \
"s_add_u32 m0, %[m0b], 66560\n" \
"s_mov_b32 s61, 229376\n" \
"buffer_load_dwordx4 %[vg0], s[64:67], s61 offen offset:0 lds\n" \
"v_mfma_f32_16x16x32_bf16 v[140:143], a[124:127], a[28:31], v[140:143]\n" \
"v_exp_f32_e32 v135, v135\n" \
"v_mov_b64_e32 v[236:237], 0\n" \
"v_mfma_f32_16x16x32_bf16 v[48:51], a[176:179], v[196:199], v[48:51]\n" \
"ds_read_b128 a[84:87], v208 offset:36864\n" \
"ds_write_b64 v232, v[236:237] offset:1024\n" \
"v_lshrrev_b32_e64 v233, 3, v224\n" \
"v_mfma_f32_16x16x32_bf16 v[48:51], a[144:147], v[188:191], v[48:51]\n" \
"v_exp_f32_e64 v136, v136\n" \
"v_and_b32_e32 v232, 0x170, v233\n" \
"v_mfma_f32_16x16x32_bf16 v[52:55], a[180:183], v[196:199], v[52:55]\n" \
"v_cvt_pk_bf16_f32 v194, v132, v133\n" \
"v_cvt_pk_bf16_f32 v195, v134, v135\n" \
"v_lshrrev_b32_e64 v225, 7, v224\n" \
"v_mfma_f32_16x16x32_bf16 v[52:55], a[148:151], v[188:191], v[52:55]\n" \
"v_exp_f32_e64 v137, v137\n" \
"v_and_or_b32 v232, v225, 8, v232\n" \
"v_mfma_f32_16x16x32_bf16 v[56:59], a[184:187], v[196:199], v[56:59]\n" \
"v_exp_f32_e32 v138, v138\n" \
"v_lshrrev_b32_e32 v225, 12, v224\n" \
"v_mfma_f32_16x16x32_bf16 v[56:59], a[152:155], v[188:191], v[56:59]\n" \
"ds_read_b128 a[88:91], v209 offset:36864\n" \
"v_lshl_or_b32 v232, v225, 7, v232\n" \
"v_mfma_f32_16x16x32_bf16 v[60:63], a[188:191], v[196:199], v[60:63]\n" \
"v_exp_f32_e64 v139, v139\n" \
"v_mfma_f32_16x16x32_bf16 v[60:63], a[156:159], v[188:191], v[60:63]\n" \
"v_add_u32_e64 v232, s62, v232\n" \
"ds_write_b64 v232, v[234:235] offset:512\n" \
"v_mfma_f32_16x16x32_bf16 v[144:147], a[96:99], a[32:35], v[168:171]\n" \
"v_exp_f32_e64 v140, v140\n" \
"v_mfma_f32_16x16x32_bf16 v[144:147], a[100:103], a[36:39], v[144:147]\n" \
"ds_read_b128 a[92:95], v210 offset:36864\n" \
"s_mov_b32 s68, 0xf0000f\n" \
"s_mov_b32 s69, 0xf0000f00\n" \
"v_mfma_f32_16x16x32_bf16 v[144:147], a[104:107], a[40:43], v[144:147]\n" \
"v_exp_f32_e64 v141, v141\n" \
"v_mov_b32_e32 v230, 0x400\n" \
"v_mfma_f32_16x16x32_bf16 v[144:147], a[108:111], a[44:47], v[144:147]\n" \
"v_cvt_pk_bf16_f32 v196, v136, v137\n" \
"v_cvt_pk_bf16_f32 v197, v138, v139\n" \
"v_mfma_f32_16x16x32_bf16 v[148:151], a[112:115], a[32:35], v[168:171]\n" \
"v_exp_f32_e64 v142, v142\n" \
"v_mfma_f32_16x16x32_bf16 v[148:151], a[116:119], a[36:39], v[148:151]\n" \
"v_cndmask_b32_e64 v230, v230, 0, s[68:69]\n" \
"v_and_or_b32 v230, v233, 48, v230\n" \
"v_add_u32_e64 v230, s62, v230\n" \
"v_mfma_f32_16x16x32_bf16 v[148:151], a[120:123], a[40:43], v[148:151]\n" \
"s_add_u32 m0, %[m0b], 67584\n" \
"s_mov_b32 s61, 458752\n" \
"buffer_load_dwordx4 %[vg0], s[64:67], s61 offen offset:0 lds\n" \
"v_mfma_f32_16x16x32_bf16 v[148:151], a[124:127], a[44:47], v[148:151]\n" \
"v_exp_f32_e64 v143, v143\n" \
"v_mfma_f32_16x16x32_bf16 v[96:99], a[160:163], v[204:207], v[96:99]\n" \
"ds_read_b64_tr_b16 a[160:161], %[vvb] offset:24576\n" \
"ds_read_b128 a[200:203], v230 offset:0\n" \
"ds_read_b128 a[204:207], v230 offset:64\n" \
"v_mfma_f32_16x16x32_bf16 v[96:99], a[128:131], v[220:223], v[96:99]\n" \
"v_exp_f32_e64 v144, v144\n" \
"ds_read_b128 a[208:211], v230 offset:128\n" \
"v_mfma_f32_16x16x32_bf16 v[100:103], a[164:167], v[204:207], v[100:103]\n" \
"v_cvt_pk_bf16_f32 v198, v140, v141\n" \
"v_cvt_pk_bf16_f32 v199, v142, v143\n" \
"v_mfma_f32_16x16x32_bf16 v[100:103], a[132:135], v[220:223], v[100:103]\n" \
"v_exp_f32_e64 v145, v145\n" \
"v_mfma_f32_16x16x32_bf16 v[104:107], a[168:171], v[204:207], v[104:107]\n" \
"ds_read_b64_tr_b16 a[162:163], %[vvb] offset:28672\n" \
"ds_read_b64_tr_b16 a[164:165], v211 offset:24576\n" \
"v_mfma_f32_16x16x32_bf16 v[104:107], a[136:139], v[220:223], v[104:107]\n" \
"v_exp_f32_e64 v146, v146\n" \
"v_mfma_f32_16x16x32_bf16 v[108:111], a[172:175], v[204:207], v[108:111]\n" \
"ds_read_b64_tr_b16 a[166:167], v211 offset:28672\n" \
"s_waitcnt vmcnt(12) lgkmcnt(5)\n" \
"v_mfma_f32_16x16x32_bf16 v[234:237], a[212:215], a[200:203], 0\n" \
"ds_read_b128 a[200:203], v230 offset:192\n" \
"v_mfma_f32_16x16x32_bf16 v[108:111], a[140:143], v[220:223], v[108:111]\n" \
"v_exp_f32_e32 v147, v147\n" \
"v_smfmac_f32_16x16x64_bf16 v[180:183], a[192:195], v[216:223], v228\n" \
"ds_read_b64_tr_b16 a[168:169], %[vvb] offset:25088\n" \
"v_mfma_f32_16x16x32_bf16 v[152:155], a[96:99], a[48:51], v[172:175]\n" \
"v_exp_f32_e32 v148, v148\n" \
"s_waitcnt vmcnt(10) lgkmcnt(6)\n" \
"v_mfma_f32_16x16x32_bf16 v[234:237], a[216:219], a[204:207], v[234:237]\n" \
"ds_read_b128 a[204:207], v230 offset:256\n" \
"v_mfma_f32_16x16x32_bf16 v[152:155], a[100:103], a[52:55], v[152:155]\n" \
"ds_read_b64_tr_b16 a[170:171], %[vvb] offset:29184\n" \
"v_mfma_f32_16x16x32_bf16 v[152:155], a[104:107], a[56:59], v[152:155]\n" \
"v_exp_f32_e64 v149, v149\n" \
"v_mfma_f32_16x16x32_bf16 v[152:155], a[108:111], a[60:63], v[152:155]\n" \
"v_cvt_pk_bf16_f32 v200, v144, v145\n" \
"v_cvt_pk_bf16_f32 v201, v146, v147\n" \
"s_waitcnt vmcnt(9) lgkmcnt(7)\n" \
"v_mfma_f32_16x16x32_bf16 v[234:237], a[220:223], a[208:211], v[234:237]\n" \
"ds_read_b128 a[208:211], v230 offset:320\n" \
"v_mfma_f32_16x16x32_bf16 v[156:159], a[112:115], a[48:51], v[172:175]\n" \
"v_exp_f32_e32 v150, v150\n" \
"v_mfma_f32_16x16x32_bf16 v[156:159], a[116:119], a[52:55], v[156:159]\n" \
"ds_read_b64_tr_b16 a[172:173], v211 offset:25088\n" \
"v_mfma_f32_16x16x32_bf16 v[156:159], a[120:123], a[56:59], v[156:159]\n" \
"s_add_u32 m0, %[m0b], 68608\n" \
"s_mov_b32 s61, 688128\n" \
"buffer_load_dwordx4 %[vg0], s[64:67], s61 offen offset:0 lds\n" \
"s_waitcnt vmcnt(9) lgkmcnt(5)\n" \
"v_mfma_f32_16x16x32_bf16 v[234:237], a[224:227], a[200:203], v[234:237]\n" \
"ds_read_b128 a[200:203], v230 offset:384\n" \
"v_mfma_f32_16x16x32_bf16 v[156:159], a[124:127], a[60:63], v[156:159]\n" \
"v_exp_f32_e32 v151, v151\n" \
"v_mfma_f32_16x16x32_bf16 v[112:115], a[176:179], v[204:207], v[112:115]\n" \
"ds_read_b64_tr_b16 a[174:175], v211 offset:29184\n" \
"ds_read_b64_tr_b16 a[176:177], %[vvb] offset:25600\n" \
"v_mfma_f32_16x16x32_bf16 v[112:115], a[144:147], v[220:223], v[112:115]\n" \
"v_exp_f32_e32 v152, v152\n" \
"s_waitcnt vmcnt(8) lgkmcnt(6)\n" \
"v_mfma_f32_16x16x32_bf16 v[234:237], a[228:231], a[204:207], v[234:237]\n" \
"ds_read_b128 a[204:207], v230 offset:448\n" \
"v_mfma_f32_16x16x32_bf16 v[116:119], a[180:183], v[204:207], v[116:119]\n" \
"v_cvt_pk_bf16_f32 v202, v148, v149\n" \
"v_cvt_pk_bf16_f32 v203, v150, v151\n" \
"v_mfma_f32_16x16x32_bf16 v[116:119], a[148:151], v[220:223], v[116:119]\n" \
"v_exp_f32_e64 v153, v153\n" \
"v_mfma_f32_16x16x32_bf16 v[120:123], a[184:187], v[204:207], v[120:123]\n" \
"v_exp_f32_e32 v154, v154\n" \
"s_waitcnt vmcnt(6) lgkmcnt(5)\n" \
"v_mfma_f32_16x16x32_bf16 v[234:237], a[232:235], a[208:211], v[234:237]\n" \
"v_mfma_f32_16x16x32_bf16 v[120:123], a[152:155], v[220:223], v[120:123]\n" \
"ds_read_b64_tr_b16 a[178:179], %[vvb] offset:29696\n" \
"v_mfma_f32_16x16x32_bf16 v[124:127], a[188:191], v[204:207], v[124:127]\n" \
"v_exp_f32_e64 v155, v155\n" \
"v_mfma_f32_16x16x32_bf16 v[124:127], a[156:159], v[220:223], v[124:127]\n" \
"ds_read_b64_tr_b16 a[180:181], v211 offset:25600\n" \
"s_waitcnt lgkmcnt(14)\n" \
"v_mfma_f32_16x16x32_bf16 v[128:131], a[64:67], a[0:3], v[160:163]\n" \
"v_exp_f32_e32 v156, v156\n" \
"s_waitcnt vmcnt(5) lgkmcnt(5)\n" \
"v_mfma_f32_16x16x32_bf16 v[234:237], a[236:239], a[200:203], v[234:237]\n" \
"v_mfma_f32_16x16x32_bf16 v[128:131], a[68:71], a[4:7], v[128:131]\n" \
"ds_read_b64_tr_b16 a[182:183], v211 offset:29696\n" \
"s_add_u32 m0, %[m0b], 16384\n" \
"s_mov_b32 s49, s48\n" \
"v_mbcnt_lo_u32_b32 v231, -1, 0\n" \
"v_mfma_f32_16x16x32_bf16 v[128:131], a[72:75], a[8:11], v[128:131]\n" \
"v_exp_f32_e64 v157, v157\n" \
"v_mfma_f32_16x16x32_bf16 v[128:131], a[76:79], a[12:15], v[128:131]\n" \
"v_cvt_pk_bf16_f32 v204, v152, v153\n" \
"v_cvt_pk_bf16_f32 v205, v154, v155\n" \
"s_waitcnt vmcnt(4) lgkmcnt(3)\n" \
"v_mfma_f32_16x16x32_bf16 v[234:237], a[196:199], a[204:207], v[234:237]\n" \
"v_mfma_f32_16x16x32_bf16 v[132:135], a[80:83], a[0:3], v[160:163]\n" \
"v_exp_f32_e32 v158, v158\n" \
"v_mfma_f32_16x16x32_bf16 v[132:135], a[84:87], a[4:7], v[132:135]\n" \
"ds_read_b64_tr_b16 a[184:185], %[vvb] offset:26112\n" \
"s_lshl_b32 s49, s49, 18\n" \
"s_or_b32 s49, s49, s59\n" \
"v_mbcnt_hi_u32_b32 v231, -1, v231\n" \
"s_waitcnt lgkmcnt(6)\n" \
"v_mfma_f32_16x16x32_bf16 v[132:135], a[88:91], a[8:11], v[132:135]\n" \
"buffer_load_dwordx4 %[wo0], %[srdt], s49 offen offset:0 lds\n" \
"v_mfma_f32_16x16x32_bf16 v[132:135], a[92:95], a[12:15], v[132:135]\n" \
"v_exp_f32_e32 v159, v159\n" \
"v_mfma_f32_16x16x32_bf16 v[64:67], a[128:131], v[216:219], v[64:67]\n" \
"ds_read_b64_tr_b16 a[186:187], %[vvb] offset:30208\n" \
"ds_read_b64_tr_b16 a[188:189], v211 offset:26112\n" \
"v_xor_b32_e32 v229, 0x80, %[vx0]\n" \
"v_and_b32_e64 v226, 31, v231\n" \
"v_mfma_f32_16x16x32_bf16 v[64:67], a[160:163], v[200:203], v[64:67]\n" \
"v_exp_f32_e64 v128, v128\n" \
"v_mfma_f32_16x16x32_bf16 v[68:71], a[132:135], v[216:219], v[68:71]\n" \
"v_cvt_pk_bf16_f32 v206, v156, v157\n" \
"v_cvt_pk_bf16_f32 v207, v158, v159\n" \
"v_mfma_f32_16x16x32_bf16 v[68:71], a[164:167], v[200:203], v[68:71]\n" \
"v_exp_f32_e64 v129, v129\n" \
"v_mfma_f32_16x16x32_bf16 v[72:75], a[136:139], v[216:219], v[72:75]\n" \
"ds_read_b64_tr_b16 a[190:191], v211 offset:30208\n" \
"ds_read_b64_tr_b16 a[128:129], %[vvb] offset:32768\n" \
"v_lshlrev_b32_e64 v226, 2, v226\n" \
"v_mfma_f32_16x16x32_bf16 v[72:75], a[168:171], v[200:203], v[72:75]\n" \
"v_exp_f32_e64 v130, v130\n" \
"s_mov_b32 s68, 0xaaaaaaaa\n" \
"v_mfma_f32_16x16x32_bf16 v[76:79], a[140:143], v[216:219], v[76:79]\n" \
"ds_read_b64_tr_b16 a[130:131], %[vvb] offset:36864\n" \
"ds_read_b64_tr_b16 a[132:133], v211 offset:32768\n" \
"v_cmp_gt_u32_e32 vcc, 1, v231\n" \
"v_mfma_f32_16x16x32_bf16 v[76:79], a[172:175], v[200:203], v[76:79]\n" \
"v_exp_f32_e32 v131, v131\n" \
"s_mov_b32 s69, 0xaaaaaaaa\n" \
"v_smfmac_f32_16x16x64_bf16 v[176:179], a[192:195], v[192:199], v228\n" \
"ds_read_b64_tr_b16 a[134:135], v211 offset:36864\n" \
"ds_read_b64_tr_b16 a[136:137], %[vvb] offset:33280\n" \
"global_load_dwordx2 a[240:241], %[vx0], %[px] offset:0\n" \
"v_mov_b32_e32 v225, 0x80000000\n" \
"v_mfma_f32_16x16x32_bf16 v[136:139], a[64:67], a[16:19], v[164:167]\n" \
"v_exp_f32_e64 v132, v132\n" \
"v_cndmask_b32_e64 v234, v234, v235, s[68:69]\n" \
"v_mfma_f32_16x16x32_bf16 v[136:139], a[68:71], a[20:23], v[136:139]\n" \
"ds_read_b64_tr_b16 a[138:139], %[vvb] offset:37376\n" \
"ds_read_b64_tr_b16 a[140:141], v211 offset:33280\n" \
"v_cndmask_b32_e64 v236, v236, v237, s[68:69]\n" \
"v_mfma_f32_16x16x32_bf16 v[136:139], a[72:75], a[24:27], v[136:139]\n" \
"v_exp_f32_e64 v133, v133\n" \
"s_mov_b32 s68, 0xcccccccc\n" \
"v_mfma_f32_16x16x32_bf16 v[136:139], a[76:79], a[28:31], v[136:139]\n" \
"v_cvt_pk_bf16_f32 v184, v128, v129\n" \
"v_cvt_pk_bf16_f32 v185, v130, v131\n" \
"s_mov_b32 s69, 0xcccccccc\n" \
"v_mfma_f32_16x16x32_bf16 v[140:143], a[80:83], a[16:19], v[164:167]\n" \
"v_exp_f32_e64 v134, v134\n" \
"v_cndmask_b32_e64 v234, v234, v236, s[68:69]\n" \
"v_mfma_f32_16x16x32_bf16 v[140:143], a[84:87], a[20:23], v[140:143]\n" \
"ds_read_b64_tr_b16 a[142:143], v211 offset:37376\n" \
"v_cndmask_b32_e32 v225, v225, v224, vcc\n" \
"s_waitcnt lgkmcnt(8)\n" \
"v_mfma_f32_16x16x32_bf16 v[140:143], a[88:91], a[24:27], v[140:143]\n" \
"buffer_load_dwordx4 v212, %[srdt], s49 offen offset:1024 lds\n" \
"v_mfma_f32_16x16x32_bf16 v[140:143], a[92:95], a[28:31], v[140:143]\n" \
"v_exp_f32_e32 v135, v135\n" \
"v_lshrrev_b32_e32 v235, 3, v224\n" \
"v_mfma_f32_16x16x32_bf16 v[80:83], a[144:147], v[216:219], v[80:83]\n" \
"ds_read_b64_tr_b16 a[144:145], %[vvb] offset:33792\n" \
"ds_read_b64_tr_b16 a[146:147], %[vvb] offset:37888\n" \
"v_and_b32_e32 v236, 0xc0, v235\n" \
"v_mfma_f32_16x16x32_bf16 v[80:83], a[176:179], v[200:203], v[80:83]\n" \
"v_exp_f32_e64 v136, v136\n" \
"v_add_f32_dpp v234, v234, v234 quad_perm:[1,0,3,2] row_mask:0xf bank_mask:0xf\n" \
"v_mfma_f32_16x16x32_bf16 v[84:87], a[148:151], v[216:219], v[84:87]\n" \
"v_cvt_pk_bf16_f32 v186, v132, v133\n" \
"v_cvt_pk_bf16_f32 v187, v134, v135\n" \
"v_and_or_b32 v236, v231, 48, v236\n" \
"v_mfma_f32_16x16x32_bf16 v[84:87], a[180:183], v[200:203], v[84:87]\n" \
"v_exp_f32_e64 v137, v137\n" \
"s_mul_i32 s62, %[wid], 8188\n" \
"v_mfma_f32_16x16x32_bf16 v[88:91], a[152:155], v[216:219], v[88:91]\n" \
"v_exp_f32_e32 v138, v138\n" \
"s_sub_u32 s62, %[m0t], s62\n" \
"v_mfma_f32_16x16x32_bf16 v[88:91], a[184:187], v[200:203], v[88:91]\n" \
"ds_read_b64_tr_b16 a[148:149], v211 offset:33792\n" \
"ds_read_b64_tr_b16 a[150:151], v211 offset:37888\n" \
"v_add_f32_dpp v234, v234, v234 quad_perm:[2,3,0,1] row_mask:0xf bank_mask:0xf\n" \
"v_mfma_f32_16x16x32_bf16 v[92:95], a[156:159], v[216:219], v[92:95]\n" \
"v_exp_f32_e32 v139, v139\n" \
"v_add_u32_e32 v236, s62, v236\n" \
"v_mfma_f32_16x16x32_bf16 v[92:95], a[188:191], v[200:203], v[92:95]\n" \
"ds_read_b64_tr_b16 a[152:153], %[vvb] offset:34304\n" \
"global_load_dwordx2 a[244:245], %[vx0], %[px] offset:1024\n" \
"v_mfma_f32_16x16x32_bf16 v[144:147], a[64:67], a[32:35], v[168:171]\n" \
"v_exp_f32_e64 v140, v140\n" \
"ds_write_b32 v236, v234 offset:7680\n" \
"v_mfma_f32_16x16x32_bf16 v[144:147], a[68:71], a[36:39], v[144:147]\n" \
"ds_read_b64_tr_b16 a[154:155], %[vvb] offset:38400\n" \
"ds_read_b64_tr_b16 a[156:157], v211 offset:34304\n" \
"s_sub_u32 s61, s48, 0\n" \
"s_cmp_lt_u32 s61, 4\n" \
"s_cselect_b64 vcc, -1, 0\n" \
"v_mfma_f32_16x16x32_bf16 v[144:147], a[72:75], a[40:43], v[144:147]\n" \
"v_exp_f32_e32 v141, v141\n" \
"s_lshl_b32 s62, %[wid], 13\n" \
"v_mfma_f32_16x16x32_bf16 v[144:147], a[76:79], a[44:47], v[144:147]\n" \
"v_cvt_pk_bf16_f32 v188, v136, v137\n" \
"v_cvt_pk_bf16_f32 v189, v138, v139\n" \
"s_sub_u32 s62, %[m0t], s62\n" \
"v_mfma_f32_16x16x32_bf16 v[148:151], a[80:83], a[32:35], v[168:171]\n" \
"v_exp_f32_e64 v142, v142\n" \
"v_and_b32_e32 v230, 0xf0, v235\n" \
"v_mfma_f32_16x16x32_bf16 v[148:151], a[84:87], a[36:39], v[148:151]\n" \
"ds_read_b64_tr_b16 a[158:159], v211 offset:38400\n" \
"v_readlane_b32 s55, %[tlo], 16\n" \
"s_waitcnt lgkmcnt(9)\n" \
"v_mfma_f32_16x16x32_bf16 v[148:151], a[88:91], a[40:43], v[148:151]\n" \
"buffer_load_dwordx4 v213, %[srdt], s49 offen offset:2048 lds\n" \
"v_mfma_f32_16x16x32_bf16 v[148:151], a[92:95], a[44:47], v[148:151]\n" \
"v_exp_f32_e32 v143, v143\n" \
"v_mfma_f32_16x16x32_bf16 v[0:3], a[160:163], v[192:195], v[0:3]\n" \
"ds_read_b128 a[96:99], %[kb0] offset:40960\n" \
"v_cmp_gt_u32_e64 s[56:57], 32, v231\n" \
"v_mfma_f32_16x16x32_bf16 v[0:3], a[128:131], v[184:187], v[0:3]\n" \
"v_exp_f32_e64 v144, v144\n" \
"v_mfma_f32_16x16x32_bf16 v[4:7], a[164:167], v[192:195], v[4:7]\n" \
"v_cvt_pk_bf16_f32 v190, v140, v141\n" \
"v_cvt_pk_bf16_f32 v191, v142, v143\n" \
"v_mfma_f32_16x16x32_bf16 v[4:7], a[132:135], v[184:187], v[4:7]\n" \
"v_exp_f32_e64 v145, v145\n" \
"v_mfma_f32_16x16x32_bf16 v[8:11], a[168:171], v[192:195], v[8:11]\n" \
"ds_read_b128 a[100:103], v208 offset:40960\n" \
"v_lshlrev_b32_e64 v232, 2, v224\n" \
"v_mfma_f32_16x16x32_bf16 v[8:11], a[136:139], v[184:187], v[8:11]\n" \
"v_exp_f32_e64 v146, v146\n" \
"v_mfma_f32_16x16x32_bf16 v[12:15], a[172:175], v[192:195], v[12:15]\n" \
"ds_read_b128 a[104:107], v209 offset:40960\n" \
"v_mov_b32_e32 v233, 0x80000000\n" \
"v_mfma_f32_16x16x32_bf16 v[12:15], a[140:143], v[184:187], v[12:15]\n" \
"v_exp_f32_e64 v147, v147\n" \
"v_smfmac_f32_16x16x64_bf16 v[180:183], a[192:195], v[200:207], v228\n" \
"ds_read_b128 a[108:111], v210 offset:40960\n" \
"global_load_dwordx2 a[248:249], v229, %[px] offset:0\n" \
"v_mfma_f32_16x16x32_bf16 v[152:155], a[64:67], a[48:51], v[172:175]\n" \
"v_exp_f32_e64 v148, v148\n" \
"v_mfma_f32_16x16x32_bf16 v[152:155], a[68:71], a[52:55], v[152:155]\n" \
"ds_read_b128 a[112:115], %[kb0] offset:45056\n" \
"v_cndmask_b32_e64 v232, v233, v232, s[56:57]\n" \
"v_mfma_f32_16x16x32_bf16 v[152:155], a[72:75], a[56:59], v[152:155]\n" \
"v_exp_f32_e64 v149, v149\n" \
"v_mfma_f32_16x16x32_bf16 v[152:155], a[76:79], a[60:63], v[152:155]\n" \
"v_cvt_pk_bf16_f32 v216, v144, v145\n" \
"v_cvt_pk_bf16_f32 v217, v146, v147\n" \
"v_mfma_f32_16x16x32_bf16 v[156:159], a[80:83], a[48:51], v[172:175]\n" \
"v_exp_f32_e64 v150, v150\n" \
"v_mfma_f32_16x16x32_bf16 v[156:159], a[84:87], a[52:55], v[156:159]\n" \
"ds_read_b128 a[116:119], v208 offset:45056\n" \
"s_waitcnt lgkmcnt(6)\n" \
"v_mfma_f32_16x16x32_bf16 v[156:159], a[88:91], a[56:59], v[156:159]\n" \
"buffer_load_dwordx4 v214, %[srdt], s49 offen offset:3072 lds\n" \
"v_mfma_f32_16x16x32_bf16 v[156:159], a[92:95], a[60:63], v[156:159]\n" \
"v_exp_f32_e32 v151, v151\n" \
"v_mfma_f32_16x16x32_bf16 v[16:19], a[176:179], v[192:195], v[16:19]\n" \
"ds_read_b128 a[120:123], v209 offset:45056\n" \
"v_add_u32_e64 v230, s62, v230\n" \
"v_mfma_f32_16x16x32_bf16 v[16:19], a[144:147], v[184:187], v[16:19]\n" \
"v_exp_f32_e64 v152, v152\n" \
"v_readlane_b32 s51, %[thi], 31\n" \
"v_mfma_f32_16x16x32_bf16 v[20:23], a[180:183], v[192:195], v[20:23]\n" \
"v_cvt_pk_bf16_f32 v218, v148, v149\n" \
"v_cvt_pk_bf16_f32 v219, v150, v151\n" \
"s_cmp_eq_u32 %[wid], 0\n" \
"s_cselect_b32 s51, s51, 0x80000000\n" \
"v_mfma_f32_16x16x32_bf16 v[20:23], a[148:151], v[184:187], v[20:23]\n" \
"v_exp_f32_e32 v153, v153\n" \
"v_mfma_f32_16x16x32_bf16 v[24:27], a[184:187], v[192:195], v[24:27]\n" \
"v_exp_f32_e64 v154, v154\n" \
"v_mfma_f32_16x16x32_bf16 v[24:27], a[152:155], v[184:187], v[24:27]\n" \
"ds_read_b128 a[124:127], v210 offset:45056\n" \
"buffer_store_dword v233, v232, %[srdz], s55 offen sc1\n" \
"global_load_dwordx2 a[252:253], v229, %[px] offset:1024\n" \
"v_mfma_f32_16x16x32_bf16 v[28:31], a[188:191], v[192:195], v[28:31]\n" \
"v_exp_f32_e64 v155, v155\n" \
"v_mfma_f32_16x16x32_bf16 v[28:31], a[156:159], v[184:187], v[28:31]\n" \
"s_waitcnt vmcnt(63) lgkmcnt(2)\n" \
"s_barrier\n" \
"v_mfma_f32_16x16x32_bf16 v[128:131], a[96:99], a[0:3], v[160:163]\n" \
"v_exp_f32_e64 v156, v156\n" \
"ds_read_b128 v[232:235], v230 offset:7680\n" \
"v_mfma_f32_16x16x32_bf16 v[128:131], a[100:103], a[4:7], v[128:131]\n" \
"v_lshrrev_b32_e64 v227, 5, v231\n" \
"v_mfma_f32_16x16x32_bf16 v[128:131], a[104:107], a[8:11], v[128:131]\n" \
"v_exp_f32_e32 v157, v157\n" \
"v_lshrrev_b32_e32 v230, 6, v224\n" \
"v_mfma_f32_16x16x32_bf16 v[128:131], a[108:111], a[12:15], v[128:131]\n" \
"v_cvt_pk_bf16_f32 v220, v152, v153\n" \
"v_cvt_pk_bf16_f32 v221, v154, v155\n" \
"v_mfma_f32_16x16x32_bf16 v[132:135], a[112:115], a[0:3], v[160:163]\n" \
"v_exp_f32_e64 v158, v158\n" \
"v_mfma_f32_16x16x32_bf16 v[132:135], a[116:119], a[4:7], v[132:135]\n" \
"v_lshlrev_b32_e32 v227, 10, v227\n" \
"s_waitcnt lgkmcnt(1)\n" \
"v_mfma_f32_16x16x32_bf16 v[132:135], a[120:123], a[8:11], v[132:135]\n" \
"s_add_u32 m0, %[m0b], 81920\n" \
"s_mov_b32 s61, 917504\n" \
"buffer_load_dwordx4 %[vg0], s[64:67], s61 offen offset:0 lds\n" \
"v_mfma_f32_16x16x32_bf16 v[132:135], a[124:127], a[12:15], v[132:135]\n" \
"v_exp_f32_e32 v159, v159\n" \
"s_waitcnt lgkmcnt(0)\n" \
"v_add_f32_e64 v232, v232, v233\n" \
"v_mfma_f32_16x16x32_bf16 v[32:35], a[160:163], v[196:199], v[32:35]\n" \
"ds_read_b128 a[64:67], %[kb0] offset:49152\n" \
"v_add_u32_e32 v227, 0x10000, v227\n" \
"v_add_f32_e64 v234, v234, v235\n" \
"v_mfma_f32_16x16x32_bf16 v[32:35], a[128:131], v[188:191], v[32:35]\n" \
"v_exp_f32_e64 v128, v128\n" \
"v_mfma_f32_16x16x32_bf16 v[36:39], a[164:167], v[196:199], v[36:39]\n" \
"v_cvt_pk_bf16_f32 v222, v156, v157\n" \
"v_cvt_pk_bf16_f32 v223, v158, v159\n" \
"v_mfma_f32_16x16x32_bf16 v[36:39], a[132:135], v[188:191], v[36:39]\n" \
"v_exp_f32_e64 v129, v129\n" \
"v_mfma_f32_16x16x32_bf16 v[40:43], a[168:171], v[196:199], v[40:43]\n" \
"ds_read_b128 a[68:71], v208 offset:49152\n" \
"v_sub_u32_e64 v215, %[vvb], v227\n" \
"v_mfma_f32_16x16x32_bf16 v[40:43], a[136:139], v[188:191], v[40:43]\n" \
"v_exp_f32_e64 v130, v130\n" \
"v_mfma_f32_16x16x32_bf16 v[44:47], a[172:175], v[196:199], v[44:47]\n" \
"ds_read_b128 a[72:75], v209 offset:49152\n" \
"v_xor_b32_e64 v227, 32, v215\n" \
"v_mfma_f32_16x16x32_bf16 v[44:47], a[140:143], v[188:191], v[44:47]\n" \
"v_exp_f32_e64 v131, v131\n" \
"v_smfmac_f32_16x16x64_bf16 v[176:179], a[192:195], v[184:191], v228\n" \
"global_load_dwordx2 a[242:243], %[vx0], %[px] offset:64\n" \
"v_mfma_f32_16x16x32_bf16 v[136:139], a[96:99], a[16:19], v[164:167]\n" \
"v_exp_f32_e64 v132, v132\n" \
"v_mfma_f32_16x16x32_bf16 v[136:139], a[100:103], a[20:23], v[136:139]\n" \
"ds_read_b128 a[76:79], v210 offset:49152\n" \
"v_add_f32_e64 v232, v232, v234\n" \
"v_mfma_f32_16x16x32_bf16 v[136:139], a[104:107], a[24:27], v[136:139]\n" \
"v_exp_f32_e64 v133, v133\n" \
"v_mfma_f32_16x16x32_bf16 v[136:139], a[108:111], a[28:31], v[136:139]\n" \
"v_cvt_pk_bf16_f32 v192, v128, v129\n" \
"v_cvt_pk_bf16_f32 v193, v130, v131\n" \
"v_mfma_f32_16x16x32_bf16 v[140:143], a[112:115], a[16:19], v[164:167]\n" \
"v_exp_f32_e64 v134, v134\n" \
"v_mfma_f32_16x16x32_bf16 v[140:143], a[116:119], a[20:23], v[140:143]\n" \
"ds_read_b128 a[80:83], %[kb0] offset:53248\n" \
"v_mfma_f32_16x16x32_bf16 v[140:143], a[120:123], a[24:27], v[140:143]\n" \
"s_add_u32 m0, %[m0b], 82944\n" \
"s_mov_b32 s61, 1146880\n" \
"buffer_load_dwordx4 %[vg0], s[64:67], s61 offen offset:0 lds\n" \
"v_mfma_f32_16x16x32_bf16 v[140:143], a[124:127], a[28:31], v[140:143]\n" \
"v_exp_f32_e64 v135, v135\n" \
"v_mfma_f32_16x16x32_bf16 v[48:51], a[176:179], v[196:199], v[48:51]\n" \
"ds_read_b128 a[84:87], v208 offset:53248\n" \
"v_mov_b32_dpp v233, v232 quad_perm:[1,0,3,2] row_mask:0xf bank_mask:0xf\n" \
"v_cvt_pk_bf16_f32 v232, v232, v233\n" \
"v_mfma_f32_16x16x32_bf16 v[48:51], a[144:147], v[188:191], v[48:51]\n" \
"v_exp_f32_e64 v136, v136\n" \
"s_mov_b64 exec, 0x5555\n" \
"buffer_store_dword v232, v230, %[srdz], s51 offen sc1\n" \
"s_mov_b64 exec, -1\n" \
"v_mfma_f32_16x16x32_bf16 v[52:55], a[180:183], v[196:199], v[52:55]\n" \
"v_cvt_pk_bf16_f32 v194, v132, v133\n" \
"v_cvt_pk_bf16_f32 v195, v134, v135\n" \
"v_mfma_f32_16x16x32_bf16 v[52:55], a[148:151], v[188:191], v[52:55]\n" \
"v_exp_f32_e32 v137, v137\n" \
"v_mfma_f32_16x16x32_bf16 v[56:59], a[184:187], v[196:199], v[56:59]\n" \
"v_exp_f32_e64 v138, v138\n" \
"v_mfma_f32_16x16x32_bf16 v[56:59], a[152:155], v[188:191], v[56:59]\n" \
"ds_read_b128 a[88:91], v209 offset:53248\n" \
"v_lshrrev_b32_e64 v236, 3, v224\n" \
"v_mfma_f32_16x16x32_bf16 v[60:63], a[188:191], v[196:199], v[60:63]\n" \
"v_exp_f32_e64 v139, v139\n" \
"v_mfma_f32_16x16x32_bf16 v[60:63], a[156:159], v[188:191], v[60:63]\n" \
"global_load_dwordx2 a[246:247], %[vx0], %[px] offset:1088\n" \
"v_mfma_f32_16x16x32_bf16 v[144:147], a[96:99], a[32:35], v[168:171]\n" \
"v_exp_f32_e64 v140, v140\n" \
"v_mfma_f32_16x16x32_bf16 v[144:147], a[100:103], a[36:39], v[144:147]\n" \
"ds_read_b128 a[92:95], v210 offset:53248\n" \
"v_and_b32_e32 v230, 0xb0, v236\n" \
"v_mfma_f32_16x16x32_bf16 v[144:147], a[104:107], a[40:43], v[144:147]\n" \
"v_exp_f32_e64 v141, v141\n" \
"v_mfma_f32_16x16x32_bf16 v[144:147], a[108:111], a[44:47], v[144:147]\n" \
"v_cvt_pk_bf16_f32 v196, v136, v137\n" \
"v_cvt_pk_bf16_f32 v197, v138, v139\n" \
"v_mfma_f32_16x16x32_bf16 v[148:151], a[112:115], a[32:35], v[168:171]\n" \
"v_exp_f32_e64 v142, v142\n" \
"v_mfma_f32_16x16x32_bf16 v[148:151], a[116:119], a[36:39], v[148:151]\n" \
"v_lshrrev_b32_e64 v236, 2, v236\n" \
"v_mfma_f32_16x16x32_bf16 v[148:151], a[120:123], a[40:43], v[148:151]\n" \
"s_add_u32 m0, %[m0b], 83968\n" \
"s_mov_b32 s61, 1376256\n" \
"buffer_load_dwordx4 %[vg0], s[64:67], s61 offen offset:0 lds\n" \
"v_mfma_f32_16x16x32_bf16 v[148:151], a[124:127], a[44:47], v[148:151]\n" \
"v_exp_f32_e64 v143, v143\n" \
"v_mfma_f32_16x16x32_bf16 v[96:99], a[160:163], v[204:207], v[96:99]\n" \
"ds_read_b64_tr_b16 a[160:161], %[vvb] offset:40960\n" \
"v_and_or_b32 v230, v236, 64, v230\n" \
"v_mfma_f32_16x16x32_bf16 v[96:99], a[128:131], v[220:223], v[96:99]\n" \
"v_exp_f32_e64 v144, v144\n" \
"v_mfma_f32_16x16x32_bf16 v[100:103], a[164:167], v[204:207], v[100:103]\n" \
"v_cvt_pk_bf16_f32 v198, v140, v141\n" \
"v_cvt_pk_bf16_f32 v199, v142, v143\n" \
"v_mfma_f32_16x16x32_bf16 v[100:103], a[132:135], v[220:223], v[100:103]\n" \
"v_exp_f32_e64 v145, v145\n" \
"v_mfma_f32_16x16x32_bf16 v[104:107], a[168:171], v[204:207], v[104:107]\n" \
"ds_read_b64_tr_b16 a[162:163], %[vvb] offset:45056\n" \
"ds_read_b64_tr_b16 a[164:165], v211 offset:40960\n" \
"v_mfma_f32_16x16x32_bf16 v[104:107], a[136:139], v[220:223], v[104:107]\n" \
"v_exp_f32_e64 v146, v146\n" \
"v_mfma_f32_16x16x32_bf16 v[108:111], a[172:175], v[204:207], v[108:111]\n" \
"ds_read_b64_tr_b16 a[166:167], v211 offset:45056\n" \
"v_lshrrev_b32_e64 v236, 12, v224\n" \
"v_mfma_f32_16x16x32_bf16 v[108:111], a[140:143], v[220:223], v[108:111]\n" \
"v_exp_f32_e64 v147, v147\n" \
"v_smfmac_f32_16x16x64_bf16 v[180:183], a[192:195], v[216:223], v228\n" \
"ds_read_b64_tr_b16 a[168:169], %[vvb] offset:41472\n" \
"global_load_dwordx2 a[250:251], v229, %[px] offset:64\n" \
"v_mfma_f32_16x16x32_bf16 v[152:155], a[96:99], a[48:51], v[172:175]\n" \
"v_exp_f32_e64 v148, v148\n" \
"v_mfma_f32_16x16x32_bf16 v[152:155], a[100:103], a[52:55], v[152:155]\n" \
".if . - adaln_body != 36864\n" \
".error \"size model\"\n" \
".endif\n" \
"Lk8_%=:\n" \
"s_cbranch_execz Lk9_%=\n" \
"s_nop 0\n" \
"ds_read_b64_tr_b16 a[170:171], %[vvb] offset:45568\n" \
"v_lshl_or_b32 v230, v236, 13, v230\n" \
"v_mfma_f32_16x16x32_bf16 v[152:155], a[104:107], a[56:59], v[152:155]\n" \
"v_exp_f32_e64 v149, v149\n" \
"v_readlane_b32 s51, %[thi], 26\n" \
"v_mfma_f32_16x16x32_bf16 v[152:155], a[108:111], a[60:63], v[152:155]\n" \
"v_cvt_pk_bf16_f32 v200, v144, v145\n" \
"v_cvt_pk_bf16_f32 v201, v146, v147\n" \
"v_add_u32_e64 v230, s62, v230\n" \
"v_mfma_f32_16x16x32_bf16 v[156:159], a[112:115], a[48:51], v[172:175]\n" \
"v_exp_f32_e32 v150, v150\n" \
"v_add_u32_e32 v230, s51, v230\n" \
"v_mfma_f32_16x16x32_bf16 v[156:159], a[116:119], a[52:55], v[156:159]\n" \
"ds_read_b64_tr_b16 a[172:173], v211 offset:41472\n" \
"v_xor_b32_e32 v236, 0x100, v230\n" \
"v_mfma_f32_16x16x32_bf16 v[156:159], a[120:123], a[56:59], v[156:159]\n" \
"s_add_u32 m0, %[m0b], 84992\n" \
"s_mov_b32 s61, 1605632\n" \
"buffer_load_dwordx4 %[vg0], s[64:67], s61 offen offset:0 lds\n" \
"v_mfma_f32_16x16x32_bf16 v[156:159], a[124:127], a[60:63], v[156:159]\n" \
"v_exp_f32_e64 v151, v151\n" \
"ds_read_b128 a[212:215], v230 offset:6656\n" \
"v_mfma_f32_16x16x32_bf16 v[112:115], a[176:179], v[204:207], v[112:115]\n" \
"ds_read_b64_tr_b16 a[174:175], v211 offset:45568\n" \
"ds_read_b64_tr_b16 a[176:177], %[vvb] offset:41984\n" \
"ds_read_b128 a[216:219], v230 offset:23040\n" \
"v_mfma_f32_16x16x32_bf16 v[112:115], a[144:147], v[220:223], v[112:115]\n" \
"v_exp_f32_e64 v152, v152\n" \
"ds_read_b128 a[220:223], v236 offset:6656\n" \
"v_mfma_f32_16x16x32_bf16 v[116:119], a[180:183], v[204:207], v[116:119]\n" \
"v_cvt_pk_bf16_f32 v202, v148, v149\n" \
"v_cvt_pk_bf16_f32 v203, v150, v151\n" \
"ds_read_b128 a[224:227], v236 offset:23040\n" \
"v_mfma_f32_16x16x32_bf16 v[116:119], a[148:151], v[220:223], v[116:119]\n" \
"v_exp_f32_e64 v153, v153\n" \
"v_mfma_f32_16x16x32_bf16 v[120:123], a[184:187], v[204:207], v[120:123]\n" \
"v_exp_f32_e64 v154, v154\n" \
"v_mfma_f32_16x16x32_bf16 v[120:123], a[152:155], v[220:223], v[120:123]\n" \
"ds_read_b64_tr_b16 a[178:179], %[vvb] offset:46080\n" \
"global_load_dwordx2 a[254:255], v229, %[px] offset:1088\n" \
"v_mfma_f32_16x16x32_bf16 v[124:127], a[188:191], v[204:207], v[124:127]\n" \
"v_exp_f32_e64 v155, v155\n" \
"v_mfma_f32_16x16x32_bf16 v[124:127], a[156:159], v[220:223], v[124:127]\n" \
"ds_read_b64_tr_b16 a[180:181], v211 offset:41984\n" \
"s_waitcnt lgkmcnt(14)\n" \
"v_mfma_f32_16x16x32_bf16 v[128:131], a[64:67], a[0:3], v[160:163]\n" \
"v_exp_f32_e32 v156, v156\n" \
"v_mfma_f32_16x16x32_bf16 v[128:131], a[68:71], a[4:7], v[128:131]\n" \
"ds_read_b64_tr_b16 a[182:183], v211 offset:46080\n" \
"s_add_u32 m0, %[m0b], 32768\n" \
"s_cmp_gt_u32 s48, 0\n" \
"v_mfma_f32_16x16x32_bf16 v[128:131], a[72:75], a[8:11], v[128:131]\n" \
"v_exp_f32_e32 v157, v157\n" \
"v_mfma_f32_16x16x32_bf16 v[128:131], a[76:79], a[12:15], v[128:131]\n" \
"v_cvt_pk_bf16_f32 v204, v152, v153\n" \
"v_cvt_pk_bf16_f32 v205, v154, v155\n" \
"v_mfma_f32_16x16x32_bf16 v[132:135], a[80:83], a[0:3], v[160:163]\n" \
"v_exp_f32_e64 v158, v158\n" \
"v_mfma_f32_16x16x32_bf16 v[132:135], a[84:87], a[4:7], v[132:135]\n" \
"ds_read_b64_tr_b16 a[184:185], %[vvb] offset:42496\n" \
"s_cselect_b32 s49, 0, 1\n" \
"s_lshl_b32 s49, s49, 18\n" \
"s_or_b32 s49, s49, s58\n" \
"s_waitcnt lgkmcnt(8)\n" \
"v_mfma_f32_16x16x32_bf16 v[132:135], a[88:91], a[8:11], v[132:135]\n" \
"buffer_load_dwordx4 %[wo0], %[srdt], s49 offen offset:0 lds\n" \
"v_mfma_f32_16x16x32_bf16 v[132:135], a[92:95], a[12:15], v[132:135]\n" \
"v_exp_f32_e64 v159, v159\n" \
"v_mfma_f32_16x16x32_bf16 v[64:67], a[128:131], v[216:219], v[64:67]\n" \
"ds_read_b64_tr_b16 a[186:187], %[vvb] offset:46592\n" \
"ds_read_b64_tr_b16 a[188:189], v211 offset:42496\n" \
"v_mfma_f32_16x16x32_bf16 v[64:67], a[160:163], v[200:203], v[64:67]\n" \
"v_exp_f32_e64 v128, v128\n" \
"v_mfma_f32_16x16x32_bf16 v[68:71], a[132:135], v[216:219], v[68:71]\n" \
"v_cvt_pk_bf16_f32 v206, v156, v157\n" \
"v_cvt_pk_bf16_f32 v207, v158, v159\n" \
"v_mfma_f32_16x16x32_bf16 v[68:71], a[164:167], v[200:203], v[68:71]\n" \
"v_exp_f32_e64 v129, v129\n" \
"v_mfma_f32_16x16x32_bf16 v[72:75], a[136:139], v[216:219], v[72:75]\n" \
"ds_read_b64_tr_b16 a[190:191], v211 offset:46592\n" \
"ds_read_b64_tr_b16 a[128:129], %[vvb] offset:49152\n" \
"v_mfma_f32_16x16x32_bf16 v[72:75], a[168:171], v[200:203], v[72:75]\n" \
"v_exp_f32_e64 v130, v130\n" \
"v_mfma_f32_16x16x32_bf16 v[76:79], a[140:143], v[216:219], v[76:79]\n" \
"ds_read_b64_tr_b16 a[130:131], %[vvb] offset:53248\n" \
"ds_read_b64_tr_b16 a[132:133], v211 offset:49152\n" \
"v_mfma_f32_16x16x32_bf16 v[76:79], a[172:175], v[200:203], v[76:79]\n" \
"v_exp_f32_e64 v131, v131\n" \
"v_smfmac_f32_16x16x64_bf16 v[176:179], a[192:195], v[192:199], v228\n" \
"ds_read_b64_tr_b16 a[134:135], v211 offset:53248\n" \
"ds_read_b64_tr_b16 a[136:137], %[vvb] offset:49664\n" \
"v_mfma_f32_16x16x32_bf16 v[136:139], a[64:67], a[16:19], v[164:167]\n" \
"v_exp_f32_e64 v132, v132\n" \
"v_mfma_f32_16x16x32_bf16 v[136:139], a[68:71], a[20:23], v[136:139]\n" \
"ds_read_b64_tr_b16 a[138:139], %[vvb] offset:53760\n" \
"ds_read_b64_tr_b16 a[140:141], v211 offset:49664\n" \
"v_mfma_f32_16x16x32_bf16 v[136:139], a[72:75], a[24:27], v[136:139]\n" \
"v_exp_f32_e64 v133, v133\n" \
"v_mfma_f32_16x16x32_bf16 v[136:139], a[76:79], a[28:31], v[136:139]\n" \
"v_cvt_pk_bf16_f32 v184, v128, v129\n" \
"v_cvt_pk_bf16_f32 v185, v130, v131\n" \
"v_mfma_f32_16x16x32_bf16 v[140:143], a[80:83], a[16:19], v[164:167]\n" \
"v_exp_f32_e64 v134, v134\n" \
"v_mfma_f32_16x16x32_bf16 v[140:143], a[84:87], a[20:23], v[140:143]\n" \
"ds_read_b64_tr_b16 a[142:143], v211 offset:53760\n" \
"s_waitcnt lgkmcnt(8)\n" \
"v_mfma_f32_16x16x32_bf16 v[140:143], a[88:91], a[24:27], v[140:143]\n" \
"buffer_load_dwordx4 v212, %[srdt], s49 offen offset:1024 lds\n" \
"v_mfma_f32_16x16x32_bf16 v[140:143], a[92:95], a[28:31], v[140:143]\n" \
"v_exp_f32_e32 v135, v135\n" \
"v_mfma_f32_16x16x32_bf16 v[80:83], a[144:147], v[216:219], v[80:83]\n" \
"ds_read_b64_tr_b16 a[144:145], %[vvb] offset:50176\n" \
"ds_read_b64_tr_b16 a[146:147], %[vvb] offset:54272\n" \
"v_mfma_f32_16x16x32_bf16 v[80:83], a[176:179], v[200:203], v[80:83]\n" \
"v_exp_f32_e64 v136, v136\n" \
"v_mfma_f32_16x16x32_bf16 v[84:87], a[148:151], v[216:219], v[84:87]\n" \
"v_cvt_pk_bf16_f32 v186, v132, v133\n" \
"v_cvt_pk_bf16_f32 v187, v134, v135\n" \
"v_mfma_f32_16x16x32_bf16 v[84:87], a[180:183], v[200:203], v[84:87]\n" \
"v_exp_f32_e64 v137, v137\n" \
"v_mfma_f32_16x16x32_bf16 v[88:91], a[152:155], v[216:219], v[88:91]\n" \
"v_exp_f32_e64 v138, v138\n" \
"v_mfma_f32_16x16x32_bf16 v[88:91], a[184:187], v[200:203], v[88:91]\n" \
"ds_read_b64_tr_b16 a[148:149], v211 offset:50176\n" \
"ds_read_b64_tr_b16 a[150:151], v211 offset:54272\n" \
"v_readlane_b32 s64, %[tlo], 9\n" \
"v_mfma_f32_16x16x32_bf16 v[92:95], a[156:159], v[216:219], v[92:95]\n" \
"v_exp_f32_e64 v139, v139\n" \
"v_mfma_f32_16x16x32_bf16 v[92:95], a[188:191], v[200:203], v[92:95]\n" \
"ds_read_b64_tr_b16 a[152:153], %[vvb] offset:50688\n" \
"v_readlane_b32 s65, %[thi], 9\n" \
"v_mfma_f32_16x16x32_bf16 v[144:147], a[64:67], a[32:35], v[168:171]\n" \
"v_exp_f32_e64 v140, v140\n" \
"v_mfma_f32_16x16x32_bf16 v[144:147], a[68:71], a[36:39], v[144:147]\n" \
"ds_read_b64_tr_b16 a[154:155], %[vvb] offset:54784\n" \
"ds_read_b64_tr_b16 a[156:157], v211 offset:50688\n" \
"v_mfma_f32_16x16x32_bf16 v[144:147], a[72:75], a[40:43], v[144:147]\n" \
"v_exp_f32_e64 v141, v141\n" \
"v_mfma_f32_16x16x32_bf16 v[144:147], a[76:79], a[44:47], v[144:147]\n" \
"v_cvt_pk_bf16_f32 v188, v136, v137\n" \
"v_cvt_pk_bf16_f32 v189, v138, v139\n" \
"v_mfma_f32_16x16x32_bf16 v[148:151], a[80:83], a[32:35], v[168:171]\n" \
"v_exp_f32_e64 v142, v142\n" \
"v_mfma_f32_16x16x32_bf16 v[148:151], a[84:87], a[36:39], v[148:151]\n" \
"ds_read_b64_tr_b16 a[158:159], v211 offset:54784\n" \
"s_waitcnt lgkmcnt(10)\n" \
"v_mfma_f32_16x16x32_bf16 v[148:151], a[88:91], a[40:43], v[148:151]\n" \
"buffer_load_dwordx4 v213, %[srdt], s49 offen offset:2048 lds\n" \
"v_mfma_f32_16x16x32_bf16 v[148:151], a[92:95], a[44:47], v[148:151]\n" \
"v_exp_f32_e32 v143, v143\n" \
"v_mfma_f32_16x16x32_bf16 v[0:3], a[160:163], v[192:195], v[0:3]\n" \
"ds_read_b128 a[96:99], %[kb0] offset:57344\n" \
"v_mfma_f32_16x16x32_bf16 v[0:3], a[128:131], v[184:187], v[0:3]\n" \
"v_exp_f32_e64 v144, v144\n" \
"v_mfma_f32_16x16x32_bf16 v[4:7], a[164:167], v[192:195], v[4:7]\n" \
"v_cvt_pk_bf16_f32 v190, v140, v141\n" \
"v_cvt_pk_bf16_f32 v191, v142, v143\n" \
"v_mfma_f32_16x16x32_bf16 v[4:7], a[132:135], v[184:187], v[4:7]\n" \
"v_exp_f32_e64 v145, v145\n" \
"v_mfma_f32_16x16x32_bf16 v[8:11], a[168:171], v[192:195], v[8:11]\n" \
"ds_read_b128 a[100:103], v208 offset:57344\n" \
"v_mfma_f32_16x16x32_bf16 v[8:11], a[136:139], v[184:187], v[8:11]\n" \
"v_exp_f32_e32 v146, v146\n" \
"s_waitcnt vmcnt(3) lgkmcnt(10)\n" \
"v_mfma_f32_16x16x32_bf16 v[232:235], a[240:243], a[212:215], 0\n" \
"v_mfma_f32_16x16x32_bf16 v[232:235], a[244:247], a[216:219], v[232:235]\n" \
"v_mfma_f32_16x16x32_bf16 v[232:235], a[248:251], a[220:223], v[232:235]\n" \
"v_mfma_f32_16x16x32_bf16 v[232:235], a[252:255], a[224:227], v[232:235]\n" \
"s_mov_b32 s68, 0xaaaaaaaa\n" \
"s_mov_b32 s69, 0xaaaaaaaa\n" \
"v_mfma_f32_16x16x32_bf16 v[12:15], a[172:175], v[192:195], v[12:15]\n" \
"ds_read_b128 a[104:107], v209 offset:57344\n" \
"v_mfma_f32_16x16x32_bf16 v[12:15], a[140:143], v[184:187], v[12:15]\n" \
"v_exp_f32_e64 v147, v147\n" \
"v_smfmac_f32_16x16x64_bf16 v[180:183], a[192:195], v[200:207], v228\n" \
"ds_read_b128 a[108:111], v210 offset:57344\n" \
"buffer_load_dword v229, v226, %[srdz], s50 offen sc1\n" \
"v_mfma_f32_16x16x32_bf16 v[152:155], a[64:67], a[48:51], v[172:175]\n" \
"v_exp_f32_e64 v148, v148\n" \
"v_mfma_f32_16x16x32_bf16 v[152:155], a[68:71], a[52:55], v[152:155]\n" \
"ds_read_b128 a[112:115], %[kb0] offset:61440\n" \
"v_cndmask_b32_e64 v232, v232, v233, s[68:69]\n" \
"v_mfma_f32_16x16x32_bf16 v[152:155], a[72:75], a[56:59], v[152:155]\n" \
"v_exp_f32_e64 v149, v149\n" \
"v_mfma_f32_16x16x32_bf16 v[152:155], a[76:79], a[60:63], v[152:155]\n" \
"v_cvt_pk_bf16_f32 v216, v144, v145\n" \
"v_cvt_pk_bf16_f32 v217, v146, v147\n" \
"v_mfma_f32_16x16x32_bf16 v[156:159], a[80:83], a[48:51], v[172:175]\n" \
"v_exp_f32_e64 v150, v150\n" \
"v_mfma_f32_16x16x32_bf16 v[156:159], a[84:87], a[52:55], v[156:159]\n" \
"ds_read_b128 a[116:119], v208 offset:61440\n" \
"s_waitcnt lgkmcnt(6)\n" \
"v_mfma_f32_16x16x32_bf16 v[156:159], a[88:91], a[56:59], v[156:159]\n" \
"buffer_load_dwordx4 v214, %[srdt], s49 offen offset:3072 lds\n" \
"v_mfma_f32_16x16x32_bf16 v[156:159], a[92:95], a[60:63], v[156:159]\n" \
"v_exp_f32_e32 v151, v151\n" \
"v_mfma_f32_16x16x32_bf16 v[16:19], a[176:179], v[192:195], v[16:19]\n" \
"ds_read_b128 a[120:123], v209 offset:61440\n" \
"v_cndmask_b32_e64 v234, v234, v235, s[68:69]\n" \
"v_mfma_f32_16x16x32_bf16 v[16:19], a[144:147], v[184:187], v[16:19]\n" \
"v_exp_f32_e64 v152, v152\n" \
"v_mfma_f32_16x16x32_bf16 v[20:23], a[180:183], v[192:195], v[20:23]\n" \
"v_cvt_pk_bf16_f32 v218, v148, v149\n" \
"v_cvt_pk_bf16_f32 v219, v150, v151\n" \
"v_mfma_f32_16x16x32_bf16 v[20:23], a[148:151], v[184:187], v[20:23]\n" \
"v_exp_f32_e64 v153, v153\n" \
"v_mfma_f32_16x16x32_bf16 v[24:27], a[184:187], v[192:195], v[24:27]\n" \
"v_exp_f32_e64 v154, v154\n" \
"v_mfma_f32_16x16x32_bf16 v[24:27], a[152:155], v[184:187], v[24:27]\n" \
"ds_read_b128 a[124:127], v210 offset:61440\n" \
"s_mov_b32 s68, 0xcccccccc\n" \
"v_mfma_f32_16x16x32_bf16 v[28:31], a[188:191], v[192:195], v[28:31]\n" \
"v_exp_f32_e64 v155, v155\n" \
"v_mfma_f32_16x16x32_bf16 v[28:31], a[156:159], v[184:187], v[28:31]\n" \
"s_waitcnt vmcnt(30) lgkmcnt(2)\n" \
"s_barrier\n" \
"v_mfma_f32_16x16x32_bf16 v[128:131], a[96:99], a[0:3], v[160:163]\n" \
"v_exp_f32_e64 v156, v156\n" \
"s_mov_b32 m0, s44\n" \
"s_addk_i32 s44, 0x400\n" \
"buffer_load_dwordx4 %[vg0], s[64:67], s45 offen offset:0 lds\n" \
"s_add_u32 s45, s45, 229376\n" \
"v_mfma_f32_16x16x32_bf16 v[128:131], a[100:103], a[4:7], v[128:131]\n" \
"s_add_u32 m0, %[m0b], 98304\n" \
"s_cmp_gt_u32 s48, 0\n" \
"v_mfma_f32_16x16x32_bf16 v[128:131], a[104:107], a[8:11], v[128:131]\n" \
"v_exp_f32_e32 v157, v157\n" \
"v_mfma_f32_16x16x32_bf16 v[128:131], a[108:111], a[12:15], v[128:131]\n" \
"v_cvt_pk_bf16_f32 v220, v152, v153\n" \
"v_cvt_pk_bf16_f32 v221, v154, v155\n" \
"v_mfma_f32_16x16x32_bf16 v[132:135], a[112:115], a[0:3], v[160:163]\n" \
"v_exp_f32_e64 v158, v158\n" \
"v_mfma_f32_16x16x32_bf16 v[132:135], a[116:119], a[4:7], v[132:135]\n" \
"s_cselect_b32 s49, 0, 1\n" \
"s_lshl_b32 s49, s49, 18\n" \
"s_or_b32 s49, s49, s59\n" \
"s_waitcnt lgkmcnt(0)\n" \
"v_mfma_f32_16x16x32_bf16 v[132:135], a[120:123], a[8:11], v[132:135]\n" \
"buffer_load_dwordx4 %[wo0], %[srdt], s49 offen offset:0 lds\n" \
"v_mfma_f32_16x16x32_bf16 v[132:135], a[124:127], a[12:15], v[132:135]\n" \
"v_exp_f32_e64 v159, v159\n" \
"v_mfma_f32_16x16x32_bf16 v[32:35], a[160:163], v[196:199], v[32:35]\n" \
"ds_read_b64_tr_b16 a[196:197], v215 offset:0\n" \
"s_mov_b32 s69, 0xcccccccc\n" \
"v_mfma_f32_16x16x32_bf16 v[32:35], a[128:131], v[188:191], v[32:35]\n" \
"v_exp_f32_e64 v128, v128\n" \
"v_mfma_f32_16x16x32_bf16 v[36:39], a[164:167], v[196:199], v[36:39]\n" \
"v_cvt_pk_bf16_f32 v222, v156, v157\n" \
"v_cvt_pk_bf16_f32 v223, v158, v159\n" \
"v_mfma_f32_16x16x32_bf16 v[36:39], a[132:135], v[188:191], v[36:39]\n" \
"v_exp_f32_e64 v129, v129\n" \
"v_mfma_f32_16x16x32_bf16 v[40:43], a[168:171], v[196:199], v[40:43]\n" \
"ds_read_b64_tr_b16 a[198:199], v215 offset:2048\n" \
"v_cndmask_b32_e64 v232, v232, v234, s[68:69]\n" \
"v_mfma_f32_16x16x32_bf16 v[40:43], a[136:139], v[188:191], v[40:43]\n" \
"v_exp_f32_e64 v130, v130\n" \
"v_mfma_f32_16x16x32_bf16 v[44:47], a[172:175], v[196:199], v[44:47]\n" \
"ds_read_b64_tr_b16 a[200:201], v215 offset:4096\n" \
"s_mov_b32 s68, 0xf0000f\n" \
"v_mfma_f32_16x16x32_bf16 v[44:47], a[140:143], v[188:191], v[44:47]\n" \
"v_exp_f32_e64 v131, v131\n" \
"v_smfmac_f32_16x16x64_bf16 v[176:179], a[192:195], v[184:191], v228\n" \
"s_mov_b32 s69, 0xf0000f00\n" \
"v_cndmask_b32_e64 %[rd0], 0, v232, s[68:69]\n" \
"v_mfma_f32_16x16x32_bf16 v[136:139], a[96:99], a[16:19], v[164:167]\n" \
"v_exp_f32_e64 v132, v132\n" \
"v_mfma_f32_16x16x32_bf16 v[136:139], a[100:103], a[20:23], v[136:139]\n" \
"ds_read_b64_tr_b16 a[202:203], v215 offset:6144\n" \
"v_readlane_b32 s62, %[tlo], 32\n" \
"v_mfma_f32_16x16x32_bf16 v[136:139], a[104:107], a[24:27], v[136:139]\n" \
"v_exp_f32_e64 v133, v133\n" \
"v_mfma_f32_16x16x32_bf16 v[136:139], a[108:111], a[28:31], v[136:139]\n" \
"v_cvt_pk_bf16_f32 v192, v128, v129\n" \
"v_cvt_pk_bf16_f32 v193, v130, v131\n" \
"v_mfma_f32_16x16x32_bf16 v[140:143], a[112:115], a[16:19], v[164:167]\n" \
"v_exp_f32_e64 v134, v134\n" \
"v_mfma_f32_16x16x32_bf16 v[140:143], a[116:119], a[20:23], v[140:143]\n" \
"ds_read_b64_tr_b16 a[204:205], v215 offset:8192\n" \
"v_mfma_f32_16x16x32_bf16 v[140:143], a[120:123], a[24:27], v[140:143]\n" \
"buffer_load_dwordx4 v212, %[srdt], s49 offen offset:1024 lds\n" \
"v_mfma_f32_16x16x32_bf16 v[140:143], a[124:127], a[28:31], v[140:143]\n" \
"v_exp_f32_e64 v135, v135\n" \
"v_mfma_f32_16x16x32_bf16 v[48:51], a[176:179], v[196:199], v[48:51]\n" \
"ds_read_b64_tr_b16 a[206:207], v215 offset:10240\n" \
"v_add_u32_e32 v208, 0x1000, %[oq0]\n" \
"v_mfma_f32_16x16x32_bf16 v[48:51], a[144:147], v[188:191], v[48:51]\n" \
"v_exp_f32_e64 v136, v136\n" \
"v_mfma_f32_16x16x32_bf16 v[52:55], a[180:183], v[196:199], v[52:55]\n" \
"v_cvt_pk_bf16_f32 v194, v132, v133\n" \
"v_cvt_pk_bf16_f32 v195, v134, v135\n" \
"v_mfma_f32_16x16x32_bf16 v[52:55], a[148:151], v[188:191], v[52:55]\n" \
"v_exp_f32_e64 v137, v137\n" \
"v_mfma_f32_16x16x32_bf16 v[56:59], a[184:187], v[196:199], v[56:59]\n" \
"v_exp_f32_e64 v138, v138\n" \
"v_mfma_f32_16x16x32_bf16 v[56:59], a[152:155], v[188:191], v[56:59]\n" \
"ds_read_b64_tr_b16 a[208:209], v215 offset:12288\n" \
"v_add_u32_e32 v209, 0x2000, %[oq0]\n" \
"v_mfma_f32_16x16x32_bf16 v[60:63], a[188:191], v[196:199], v[60:63]\n" \
"v_exp_f32_e64 v139, v139\n" \
"v_mfma_f32_16x16x32_bf16 v[60:63], a[156:159], v[188:191], v[60:63]\n" \
"v_add_u32_e32 v210, 0x3000, %[oq0]\n" \
"v_lshrrev_b32_e64 v237, 7, v224\n" \
"v_mfma_f32_16x16x32_bf16 v[144:147], a[96:99], a[32:35], v[168:171]\n" \
"v_exp_f32_e64 v140, v140\n" \
"v_mfma_f32_16x16x32_bf16 v[144:147], a[100:103], a[36:39], v[144:147]\n" \
"ds_read_b64_tr_b16 a[210:211], v215 offset:14336\n" \
"v_and_b32_e64 v237, 48, v237\n" \
"v_mfma_f32_16x16x32_bf16 v[144:147], a[104:107], a[40:43], v[144:147]\n" \
"v_exp_f32_e64 v141, v141\n" \
"v_mfma_f32_16x16x32_bf16 v[144:147], a[108:111], a[44:47], v[144:147]\n" \
"v_cvt_pk_bf16_f32 v196, v136, v137\n" \
"v_cvt_pk_bf16_f32 v197, v138, v139\n" \
"v_mfma_f32_16x16x32_bf16 v[148:151], a[112:115], a[32:35], v[168:171]\n" \
"v_exp_f32_e64 v142, v142\n" \
"v_mfma_f32_16x16x32_bf16 v[148:151], a[116:119], a[36:39], v[148:151]\n" \
"v_add_u32_e64 v237, s62, v237\n" \
"v_mfma_f32_16x16x32_bf16 v[148:151], a[120:123], a[40:43], v[148:151]\n" \
"buffer_load_dwordx4 v213, %[srdt], s49 offen offset:2048 lds\n" \
"v_mfma_f32_16x16x32_bf16 v[148:151], a[124:127], a[44:47], v[148:151]\n" \
"v_exp_f32_e64 v143, v143\n" \
"v_mfma_f32_16x16x32_bf16 v[96:99], a[160:163], v[204:207], v[96:99]\n" \
"ds_read_b64_tr_b16 a[160:161], %[vvb] offset:57344\n" \
"v_mfma_f32_16x16x32_bf16 v[96:99], a[128:131], v[220:223], v[96:99]\n" \
"v_exp_f32_e64 v144, v144\n" \
"v_mfma_f32_16x16x32_bf16 v[100:103], a[164:167], v[204:207], v[100:103]\n" \
"v_cvt_pk_bf16_f32 v198, v140, v141\n" \
"v_cvt_pk_bf16_f32 v199, v142, v143\n" \
"v_mfma_f32_16x16x32_bf16 v[100:103], a[132:135], v[220:223], v[100:103]\n" \
"v_exp_f32_e64 v145, v145\n" \
"v_mfma_f32_16x16x32_bf16 v[104:107], a[168:171], v[204:207], v[104:107]\n" \
"ds_read_b64_tr_b16 a[162:163], %[vvb] offset:61440\n" \
"ds_read_b64_tr_b16 a[164:165], v211 offset:57344\n" \
"v_mfma_f32_16x16x32_bf16 v[104:107], a[136:139], v[220:223], v[104:107]\n" \
"v_exp_f32_e64 v146, v146\n" \
"v_mfma_f32_16x16x32_bf16 v[108:111], a[172:175], v[204:207], v[108:111]\n" \
"ds_read_b64_tr_b16 a[166:167], v211 offset:61440\n" \
"v_mfma_f32_16x16x32_bf16 v[108:111], a[140:143], v[220:223], v[108:111]\n" \
"v_exp_f32_e64 v147, v147\n" \
"v_smfmac_f32_16x16x64_bf16 v[180:183], a[192:195], v[216:223], v228\n" \
"ds_read_b64_tr_b16 a[168:169], %[vvb] offset:57856\n" \
"v_mfma_f32_16x16x32_bf16 v[152:155], a[96:99], a[48:51], v[172:175]\n" \
"v_exp_f32_e64 v148, v148\n" \
"v_mfma_f32_16x16x32_bf16 v[152:155], a[100:103], a[52:55], v[152:155]\n" \
"ds_read_b64_tr_b16 a[170:171], %[vvb] offset:61952\n" \
"v_mfma_f32_16x16x32_bf16 v[152:155], a[104:107], a[56:59], v[152:155]\n" \
"v_exp_f32_e64 v149, v149\n" \
"v_mfma_f32_16x16x32_bf16 v[152:155], a[108:111], a[60:63], v[152:155]\n" \
"v_cvt_pk_bf16_f32 v200, v144, v145\n" \
"v_cvt_pk_bf16_f32 v201, v146, v147\n" \
"v_mfma_f32_16x16x32_bf16 v[156:159], a[112:115], a[48:51], v[172:175]\n" \
"v_exp_f32_e64 v150, v150\n" \
"v_mfma_f32_16x16x32_bf16 v[156:159], a[116:119], a[52:55], v[156:159]\n" \
"ds_read_b64_tr_b16 a[172:173], v211 offset:57856\n" \
"v_mfma_f32_16x16x32_bf16 v[156:159], a[120:123], a[56:59], v[156:159]\n" \
"buffer_load_dwordx4 v214, %[srdt], s49 offen offset:3072 lds\n" \
"v_mfma_f32_16x16x32_bf16 v[156:159], a[124:127], a[60:63], v[156:159]\n" \
"v_exp_f32_e64 v151, v151\n" \
"v_mfma_f32_16x16x32_bf16 v[112:115], a[176:179], v[204:207], v[112:115]\n" \
"ds_read_b64_tr_b16 a[174:175], v211 offset:61952\n" \
"ds_read_b64_tr_b16 a[176:177], %[vvb] offset:58368\n" \
"v_mfma_f32_16x16x32_bf16 v[112:115], a[144:147], v[220:223], v[112:115]\n" \
"v_exp_f32_e64 v152, v152\n" \
"s_mov_b32 m0, s44\n" \
"s_addk_i32 s44, 0x400\n" \
"buffer_load_dwordx4 %[vg0], s[64:67], s45 offen offset:0 lds\n" \
"s_add_u32 s45, s45, 229376\n" \
"v_mfma_f32_16x16x32_bf16 v[116:119], a[180:183], v[204:207], v[116:119]\n" \
"v_cvt_pk_bf16_f32 v202, v148, v149\n" \
"v_cvt_pk_bf16_f32 v203, v150, v151\n" \
"v_mfma_f32_16x16x32_bf16 v[116:119], a[148:151], v[220:223], v[116:119]\n" \
"v_exp_f32_e64 v153, v153\n" \
"v_mfma_f32_16x16x32_bf16 v[120:123], a[184:187], v[204:207], v[120:123]\n" \
"v_exp_f32_e64 v154, v154\n" \
"v_mfma_f32_16x16x32_bf16 v[120:123], a[152:155], v[220:223], v[120:123]\n" \
"ds_read_b64_tr_b16 a[178:179], %[vvb] offset:62464\n" \
"s_waitcnt vmcnt(7)\n" \
"v_cmp_eq_u32_e64 s[56:57], 0, v229\n" \
"s_cmp_lg_u64 s[56:57], 0\n" \
"s_cbranch_scc1 Lspk_%=\n" \
"Lbkk_%=:\n" \
"v_mfma_f32_16x16x32_bf16 v[124:127], a[188:191], v[204:207], v[124:127]\n" \
"v_exp_f32_e32 v155, v155\n" \
"v_mfma_f32_16x16x32_bf16 v[124:127], a[156:159], v[220:223], v[124:127]\n" \
"ds_read_b64_tr_b16 a[180:181], v211 offset:58368\n" \
"ds_read_b64_tr_b16 a[182:183], v211 offset:62464\n" \
"ds_read_b64_tr_b16 a[184:185], %[vvb] offset:58880\n" \
"ds_read_b64_tr_b16 a[186:187], %[vvb] offset:62976\n" \
"ds_read_b64_tr_b16 a[188:189], v211 offset:58880\n" \
"ds_read_b64_tr_b16 a[190:191], v211 offset:62976\n" \
"s_waitcnt lgkmcnt(8)\n" \
"s_nop 0\n" \
"v_smfmac_f32_16x16x64_bf16 v[176:179], a[192:195], v[192:199], v228\n" \
"v_exp_f32_e64 v156, v156\n" \
"ds_read_b64_tr_b16 a[212:213], v227 offset:0\n" \
"v_mfma_f32_16x16x32_bf16 v[0:3], a[160:163], v[192:195], v[0:3]\n" \
"v_exp_f32_e64 v157, v157\n" \
"ds_read_b64_tr_b16 a[214:215], v227 offset:2048\n" \
"v_mfma_f32_16x16x32_bf16 v[4:7], a[164:167], v[192:195], v[4:7]\n" \
"v_cvt_pk_bf16_f32 v204, v152, v153\n" \
"ds_read_b64_tr_b16 a[216:217], v227 offset:4096\n" \
"v_mfma_f32_16x16x32_bf16 v[8:11], a[168:171], v[192:195], v[8:11]\n" \
"v_cvt_pk_bf16_f32 v205, v154, v155\n" \
"ds_read_b64_tr_b16 a[218:219], v227 offset:6144\n" \
"v_mfma_f32_16x16x32_bf16 v[12:15], a[172:175], v[192:195], v[12:15]\n" \
"v_exp_f32_e64 v158, v158\n" \
"ds_read_b64_tr_b16 a[220:221], v227 offset:8192\n" \
"s_waitcnt lgkmcnt(5)\n" \
"s_nop 0\n" \
"v_mfma_f32_16x16x32_bf16 v[16:19], a[176:179], v[192:195], v[16:19]\n" \
"v_exp_f32_e64 v159, v159\n" \
"ds_read_b64_tr_b16 a[222:223], v227 offset:10240\n" \
"v_mfma_f32_16x16x32_bf16 v[20:23], a[180:183], v[192:195], v[20:23]\n" \
"v_cvt_pk_bf16_f32 v206, v156, v157\n" \
"ds_read_b64_tr_b16 a[224:225], v227 offset:12288\n" \
"v_mfma_f32_16x16x32_bf16 v[24:27], a[184:187], v[192:195], v[24:27]\n" \
"v_cvt_pk_bf16_f32 v207, v158, v159\n" \
"ds_read_b64_tr_b16 a[226:227], v227 offset:14336\n" \
"v_mfma_f32_16x16x32_bf16 v[28:31], a[188:191], v[192:195], v[28:31]\n" \
"v_add_f32_e64 v176, v176, v177\n" \
"v_smfmac_f32_16x16x64_bf16 v[180:183], a[192:195], v[200:207], v228\n" \
"v_add_f32_e32 v178, v178, v179\n" \
"v_add_f32_e32 v231, v176, v178\n" \
"v_rcp_f32_e32 v176, v176\n" \
"v_rcp_f32_e32 v178, v178\n" \
"v_mfma_f32_16x16x32_bf16 v[32:35], a[160:163], v[196:199], v[32:35]\n" \
"v_pk_mul_f32 v[0:1], v[0:1], v[176:177] op_sel_hi:[1,0]\n" \
"v_pk_mul_f32 v[2:3], v[2:3], v[176:177] op_sel_hi:[1,0]\n" \
"v_pk_mul_f32 v[4:5], v[4:5], v[176:177] op_sel_hi:[1,0]\n" \
"v_pk_mul_f32 v[6:7], v[6:7], v[176:177] op_sel_hi:[1,0]\n" \
"v_cvt_pk_bf16_f32 v0, v0, v1\n" \
"v_cvt_pk_bf16_f32 v1, v2, v3\n" \
"v_cvt_pk_bf16_f32 v2, v4, v5\n" \
"v_mfma_f32_16x16x32_bf16 v[36:39], a[164:167], v[196:199], v[36:39]\n" \
"v_cvt_pk_bf16_f32 v3, v6, v7\n" \
"s_mov_b32 s55, 0x4e800000\n" \
"v_mov_b32_e32 v236, %[flag]\n" \
"v_add_f32_e32 v180, v180, v181\n" \
"v_add_f32_e32 v182, v182, v183\n" \
"v_add_f32_e32 v231, v231, v180\n" \
"v_add_f32_e64 v231, v231, v182\n" \
"v_mfma_f32_16x16x32_bf16 v[40:43], a[168:171], v[196:199], v[40:43]\n" \
"v_rcp_f32_e64 v180, v180\n" \
"v_cmp_nle_f32_e64 s[56:57], v231, s55\n" \
"v_rcp_f32_e32 v182, v182\n" \
"s_nop 3\n" \
"s_cmp_lg_u64 s[56:57], 0\n" \
"s_cselect_b32 s55, 1, 0\n" \
"v_mov_b32_e64 v230, s55\n" \
"v_mfma_f32_16x16x32_bf16 v[44:47], a[172:175], v[196:199], v[44:47]\n" \
"s_mov_b32 s69, s55\n" \
"s_andn2_b32 s55, s55, s52\n" \
"s_cselect_b64 vcc, 0, vcc\n" \
"s_nop 0\n" \
"ds_write_b32 v236, v230\n" \
"v_and_b32_e32 v236, -16, v236\n" \
"s_cbranch_vccz Lg37_%=\n" \
"buffer_store_dwordx4 v[0:3], %[oq0], %[srdz], s60 offen offset:0 sc1\n" \
"Lg37_%=:\n" \
"v_pk_mul_f32 v[8:9], v[8:9], v[176:177] op_sel_hi:[1,0]\n" \
"v_mfma_f32_16x16x32_bf16 v[48:51], a[176:179], v[196:199], v[48:51]\n" \
"v_pk_mul_f32 v[10:11], v[10:11], v[176:177] op_sel_hi:[1,0]\n" \
"v_pk_mul_f32 v[12:13], v[12:13], v[176:177] op_sel_hi:[1,0]\n" \
"v_pk_mul_f32 v[14:15], v[14:15], v[176:177] op_sel_hi:[1,0]\n" \
"v_cvt_pk_bf16_f32 v8, v8, v9\n" \
"v_cvt_pk_bf16_f32 v9, v10, v11\n" \
"v_cvt_pk_bf16_f32 v10, v12, v13\n" \
"v_cvt_pk_bf16_f32 v11, v14, v15\n" \
"v_mfma_f32_16x16x32_bf16 v[52:55], a[180:183], v[196:199], v[52:55]\n" \
"s_cbranch_vccz Lg38_%=\n" \
"s_nop 0\n" \
"buffer_store_dwordx4 v[8:11], %[oq0], %[srdz], s60 offen offset:1024 sc1\n" \
"Lg38_%=:\n" \
"v_pk_mul_f32 v[16:17], v[16:17], v[176:177] op_sel_hi:[1,0]\n" \
"v_pk_mul_f32 v[18:19], v[18:19], v[176:177] op_sel_hi:[1,0]\n" \
"v_pk_mul_f32 v[20:21], v[20:21], v[176:177] op_sel_hi:[1,0]\n" \
"v_pk_mul_f32 v[22:23], v[22:23], v[176:177] op_sel_hi:[1,0]\n" \
"v_cvt_pk_bf16_f32 v16, v16, v17\n" \
"v_cvt_pk_bf16_f32 v17, v18, v19\n" \
"v_mfma_f32_16x16x32_bf16 v[56:59], a[184:187], v[196:199], v[56:59]\n" \
"v_cvt_pk_bf16_f32 v18, v20, v21\n" \
"v_cvt_pk_bf16_f32 v19, v22, v23\n" \
"s_cbranch_vccz Lg39_%=\n" \
"s_nop 0\n" \
"buffer_store_dwordx4 v[16:19], %[oq0], %[srdz], s60 offen offset:2048 sc1\n" \
"Lg39_%=:\n" \
"v_pk_mul_f32 v[24:25], v[24:25], v[176:177] op_sel_hi:[1,0]\n" \
"v_pk_mul_f32 v[26:27], v[26:27], v[176:177] op_sel_hi:[1,0]\n" \
"v_pk_mul_f32 v[28:29], v[28:29], v[176:177] op_sel_hi:[1,0]\n" \
"v_pk_mul_f32 v[30:31], v[30:31], v[176:177] op_sel_hi:[1,0]\n" \
"v_mfma_f32_16x16x32_bf16 v[60:63], a[188:191], v[196:199], v[60:63]\n" \
"v_cvt_pk_bf16_f32 v24, v24, v25\n" \
"v_cvt_pk_bf16_f32 v25, v26, v27\n" \
"v_cvt_pk_bf16_f32 v26, v28, v29\n" \
"v_cvt_pk_bf16_f32 v27, v30, v31\n" \
"s_cbranch_vccz Lg40_%=\n" \
"s_nop 0\n" \
"buffer_store_dwordx4 v[24:27], %[oq0], %[srdz], s60 offen offset:3072 sc1\n" \
"Lg40_%=:\n" \
"v_mfma_f32_16x16x32_bf16 v[64:67], a[128:131], v[216:219], v[64:67]\n" \
"v_pk_mul_f32 v[32:33], v[32:33], v[178:179] op_sel_hi:[1,0]\n" \
"v_pk_mul_f32 v[34:35], v[34:35], v[178:179] op_sel_hi:[1,0]\n" \
"v_pk_mul_f32 v[36:37], v[36:37], v[178:179] op_sel_hi:[1,0]\n" \
"v_mfma_f32_16x16x32_bf16 v[64:67], a[160:163], v[200:203], v[64:67]\n" \
"v_pk_mul_f32 v[38:39], v[38:39], v[178:179] op_sel_hi:[1,0]\n" \
"v_cvt_pk_bf16_f32 v32, v32, v33\n" \
"v_cvt_pk_bf16_f32 v33, v34, v35\n" \
"v_mfma_f32_16x16x32_bf16 v[68:71], a[132:135], v[216:219], v[68:71]\n" \
"v_cvt_pk_bf16_f32 v34, v36, v37\n" \
"v_cvt_pk_bf16_f32 v35, v38, v39\n" \
"s_cbranch_vccz Lg41_%=\n" \
"s_nop 0\n" \
"buffer_store_dwordx4 v[32:35], v208, %[srdz], s60 offen offset:0 sc1\n" \
"Lg41_%=:\n" \
"v_mfma_f32_16x16x32_bf16 v[68:71], a[164:167], v[200:203], v[68:71]\n" \
"v_pk_mul_f32 v[40:41], v[40:41], v[178:179] op_sel_hi:[1,0]\n" \
"v_pk_mul_f32 v[42:43], v[42:43], v[178:179] op_sel_hi:[1,0]\n" \
"v_pk_mul_f32 v[44:45], v[44:45], v[178:179] op_sel_hi:[1,0]\n" \
"v_mfma_f32_16x16x32_bf16 v[72:75], a[136:139], v[216:219], v[72:75]\n" \
"v_pk_mul_f32 v[46:47], v[46:47], v[178:179] op_sel_hi:[1,0]\n" \
"v_cvt_pk_bf16_f32 v40, v40, v41\n" \
"v_cvt_pk_bf16_f32 v41, v42, v43\n" \
"v_mfma_f32_16x16x32_bf16 v[72:75], a[168:171], v[200:203], v[72:75]\n" \
"v_cvt_pk_bf16_f32 v42, v44, v45\n" \
"v_cvt_pk_bf16_f32 v43, v46, v47\n" \
"s_cbranch_vccz Lg42_%=\n" \
"s_nop 0\n" \
"buffer_store_dwordx4 v[40:43], v208, %[srdz], s60 offen offset:1024 sc1\n" \
"Lg42_%=:\n" \
"v_mfma_f32_16x16x32_bf16 v[76:79], a[140:143], v[216:219], v[76:79]\n" \
"v_pk_mul_f32 v[48:49], v[48:49], v[178:179] op_sel_hi:[1,0]\n" \
"v_pk_mul_f32 v[50:51], v[50:51], v[178:179] op_sel_hi:[1,0]\n" \
"v_pk_mul_f32 v[52:53], v[52:53], v[178:179] op_sel_hi:[1,0]\n" \
"v_mfma_f32_16x16x32_bf16 v[76:79], a[172:175], v[200:203], v[76:79]\n" \
"v_pk_mul_f32 v[54:55], v[54:55], v[178:179] op_sel_hi:[1,0]\n" \
"v_cvt_pk_bf16_f32 v48, v48, v49\n" \
"v_cvt_pk_bf16_f32 v49, v50, v51\n" \
"v_mfma_f32_16x16x32_bf16 v[80:83], a[144:147], v[216:219], v[80:83]\n" \
"v_cvt_pk_bf16_f32 v50, v52, v53\n" \
"v_cvt_pk_bf16_f32 v51, v54, v55\n" \
"s_cbranch_vccz Lg43_%=\n" \
"s_nop 0\n" \
"buffer_store_dwordx4 v[48:51], v208, %[srdz], s60 offen offset:2048 sc1\n" \
"Lg43_%=:\n" \
"v_mfma_f32_16x16x32_bf16 v[80:83], a[176:179], v[200:203], v[80:83]\n" \
"v_pk_mul_f32 v[56:57], v[56:57], v[178:179] op_sel_hi:[1,0]\n" \
"v_pk_mul_f32 v[58:59], v[58:59], v[178:179] op_sel_hi:[1,0]\n" \
"v_pk_mul_f32 v[60:61], v[60:61], v[178:179] op_sel_hi:[1,0]\n" \
"v_mfma_f32_16x16x32_bf16 v[84:87], a[148:151], v[216:219], v[84:87]\n" \
"v_pk_mul_f32 v[62:63], v[62:63], v[178:179] op_sel_hi:[1,0]\n" \
"v_cvt_pk_bf16_f32 v56, v56, v57\n" \
"v_cvt_pk_bf16_f32 v57, v58, v59\n" \
"v_mfma_f32_16x16x32_bf16 v[84:87], a[180:183], v[200:203], v[84:87]\n" \
"v_cvt_pk_bf16_f32 v58, v60, v61\n" \
".if . - adaln_body != 40960\n" \
".error \"size model\"\n" \
".endif\n" \
"Lk9_%=:\n" \
"s_cbranch_execz Lk10_%=\n" \
"s_nop 0\n" \
"v_cvt_pk_bf16_f32 v59, v62, v63\n" \
"s_cbranch_vccz Lg44_%=\n" \
"s_nop 0\n" \
"buffer_store_dwordx4 v[56:59], v208, %[srdz], s60 offen offset:3072 sc1\n" \
"Lg44_%=:\n" \
"v_mfma_f32_16x16x32_bf16 v[88:91], a[152:155], v[216:219], v[88:91]\n" \
"v_mfma_f32_16x16x32_bf16 v[88:91], a[184:187], v[200:203], v[88:91]\n" \
"v_mfma_f32_16x16x32_bf16 v[92:95], a[156:159], v[216:219], v[92:95]\n" \
"v_mfma_f32_16x16x32_bf16 v[92:95], a[188:191], v[200:203], v[92:95]\n" \
"s_waitcnt vmcnt(14) lgkmcnt(0)\n" \
"s_bitcmp1_b32 0xf, s48\n" \
"s_cbranch_scc1 Lcw1_%=\n" \
"s_waitcnt vmcnt(6)\n" \
"Lcw1_%=:\n" \
"s_barrier\n" \
"s_nop 0\n" \
"ds_read_b128 v[232:235], v236\n" \
"v_mfma_f32_16x16x32_bf16 v[96:99], a[160:163], v[204:207], v[96:99]\n" \
"v_pk_mul_f32 v[64:65], v[64:65], v[180:181] op_sel_hi:[1,0]\n" \
"v_pk_mul_f32 v[66:67], v[66:67], v[180:181] op_sel_hi:[1,0]\n" \
"v_pk_mul_f32 v[68:69], v[68:69], v[180:181] op_sel_hi:[1,0]\n" \
"v_pk_mul_f32 v[70:71], v[70:71], v[180:181] op_sel_hi:[1,0]\n" \
"v_cvt_pk_bf16_f32 v64, v64, v65\n" \
"v_mfma_f32_16x16x32_bf16 v[100:103], a[164:167], v[204:207], v[100:103]\n" \
"v_cvt_pk_bf16_f32 v65, v66, v67\n" \
"v_cvt_pk_bf16_f32 v66, v68, v69\n" \
"v_cvt_pk_bf16_f32 v67, v70, v71\n" \
"s_cbranch_vccz Lg45_%=\n" \
"s_nop 0\n" \
"buffer_store_dwordx4 v[64:67], v209, %[srdz], s60 offen offset:0 sc1\n" \
"Lg45_%=:\n" \
"v_pk_mul_f32 v[72:73], v[72:73], v[180:181] op_sel_hi:[1,0]\n" \
"v_mfma_f32_16x16x32_bf16 v[104:107], a[168:171], v[204:207], v[104:107]\n" \
"v_pk_mul_f32 v[74:75], v[74:75], v[180:181] op_sel_hi:[1,0]\n" \
"v_pk_mul_f32 v[76:77], v[76:77], v[180:181] op_sel_hi:[1,0]\n" \
"v_pk_mul_f32 v[78:79], v[78:79], v[180:181] op_sel_hi:[1,0]\n" \
"v_cvt_pk_bf16_f32 v72, v72, v73\n" \
"v_cvt_pk_bf16_f32 v73, v74, v75\n" \
"v_mfma_f32_16x16x32_bf16 v[108:111], a[172:175], v[204:207], v[108:111]\n" \
"v_cvt_pk_bf16_f32 v74, v76, v77\n" \
"v_cvt_pk_bf16_f32 v75, v78, v79\n" \
"s_cbranch_vccz Lg46_%=\n" \
"s_nop 0\n" \
"buffer_store_dwordx4 v[72:75], v209, %[srdz], s60 offen offset:1024 sc1\n" \
"Lg46_%=:\n" \
"v_pk_mul_f32 v[80:81], v[80:81], v[180:181] op_sel_hi:[1,0]\n" \
"v_pk_mul_f32 v[82:83], v[82:83], v[180:181] op_sel_hi:[1,0]\n" \
"v_mfma_f32_16x16x32_bf16 v[112:115], a[176:179], v[204:207], v[112:115]\n" \
"v_pk_mul_f32 v[84:85], v[84:85], v[180:181] op_sel_hi:[1,0]\n" \
"v_pk_mul_f32 v[86:87], v[86:87], v[180:181] op_sel_hi:[1,0]\n" \
"v_cvt_pk_bf16_f32 v80, v80, v81\n" \
"v_cvt_pk_bf16_f32 v81, v82, v83\n" \
"v_cvt_pk_bf16_f32 v82, v84, v85\n" \
"v_mfma_f32_16x16x32_bf16 v[116:119], a[180:183], v[204:207], v[116:119]\n" \
"v_cvt_pk_bf16_f32 v83, v86, v87\n" \
"s_cbranch_vccz Lg47_%=\n" \
"s_nop 0\n" \
"buffer_store_dwordx4 v[80:83], v209, %[srdz], s60 offen offset:2048 sc1\n" \
"Lg47_%=:\n" \
"v_pk_mul_f32 v[88:89], v[88:89], v[180:181] op_sel_hi:[1,0]\n" \
"v_pk_mul_f32 v[90:91], v[90:91], v[180:181] op_sel_hi:[1,0]\n" \
"v_pk_mul_f32 v[92:93], v[92:93], v[180:181] op_sel_hi:[1,0]\n" \
"v_mfma_f32_16x16x32_bf16 v[120:123], a[184:187], v[204:207], v[120:123]\n" \
"v_pk_mul_f32 v[94:95], v[94:95], v[180:181] op_sel_hi:[1,0]\n" \
"v_cvt_pk_bf16_f32 v88, v88, v89\n" \
"v_cvt_pk_bf16_f32 v89, v90, v91\n" \
"v_cvt_pk_bf16_f32 v90, v92, v93\n" \
"v_cvt_pk_bf16_f32 v91, v94, v95\n" \
"v_mfma_f32_16x16x32_bf16 v[124:127], a[188:191], v[204:207], v[124:127]\n" \
"s_cbranch_vccz Lg48_%=\n" \
"s_nop 0\n" \
"buffer_store_dwordx4 v[88:91], v209, %[srdz], s60 offen offset:3072 sc1\n" \
"Lg48_%=:\n" \
"s_waitcnt lgkmcnt(0)\n" \
"s_nop 0\n" \
"v_or3_b32 v232, v232, v233, v234\n" \
"v_or_b32_e32 v232, v232, v235\n" \
"s_nop 0\n" \
"v_readfirstlane_b32 s55, v232\n" \
"s_nop 3\n" \
"s_andn2_b32 s55, s55, s52\n" \
"s_cbranch_scc1 Lslow_%=\n" \
"v_mfma_f32_16x16x32_bf16 v[192:195], a[196:199], v[0:3], 0\n" \
"v_mfma_f32_16x16x32_bf16 v[192:195], a[200:203], v[8:11], v[192:195]\n" \
"v_pk_mul_f32 v[96:97], v[96:97], v[182:183] op_sel_hi:[1,0]\n" \
"v_pk_mul_f32 v[98:99], v[98:99], v[182:183] op_sel_hi:[1,0]\n" \
"v_mfma_f32_16x16x32_bf16 v[192:195], a[204:207], v[16:19], v[192:195]\n" \
"v_pk_mul_f32 v[100:101], v[100:101], v[182:183] op_sel_hi:[1,0]\n" \
"v_pk_mul_f32 v[102:103], v[102:103], v[182:183] op_sel_hi:[1,0]\n" \
"s_add_u32 m0, %[m0b], 114688\n" \
"s_cmp_gt_u32 s48, 1\n" \
"s_cselect_b32 s49, 1, 2\n" \
"s_lshl_b32 s49, s49, 18\n" \
"s_or_b32 s49, s49, s58\n" \
"buffer_load_dwordx4 %[wo0], %[srdt], s49 offen offset:0 lds\n" \
"v_mfma_f32_16x16x32_bf16 v[192:195], a[208:211], v[24:27], v[192:195]\n" \
"v_cvt_pk_bf16_f32 v96, v96, v97\n" \
"v_cvt_pk_bf16_f32 v97, v98, v99\n" \
"v_mfma_f32_16x16x32_bf16 v[144:147], a[212:215], v[0:3], 0\n" \
"v_cvt_pk_bf16_f32 v98, v100, v101\n" \
"v_mfma_f32_16x16x32_bf16 v[144:147], a[216:219], v[8:11], v[144:147]\n" \
"v_cvt_pk_bf16_f32 v99, v102, v103\n" \
"s_cbranch_vccz Lg1_%=\n" \
"s_nop 0\n" \
"buffer_store_dwordx4 v[96:99], v210, %[srdz], s60 offen offset:0 sc1\n" \
"Lg1_%=:\n" \
"v_mfma_f32_16x16x32_bf16 v[144:147], a[220:223], v[16:19], v[144:147]\n" \
"ds_read_b64_tr_b16 a[228:229], v215 offset:512\n" \
"v_pk_mul_f32 v[104:105], v[104:105], v[182:183] op_sel_hi:[1,0]\n" \
"v_pk_mul_f32 v[106:107], v[106:107], v[182:183] op_sel_hi:[1,0]\n" \
"v_mfma_f32_16x16x32_bf16 v[144:147], a[224:227], v[24:27], v[144:147]\n" \
"ds_read_b64_tr_b16 a[230:231], v215 offset:2560\n" \
"v_pk_mul_f32 v[108:109], v[108:109], v[182:183] op_sel_hi:[1,0]\n" \
"v_mfma_f32_16x16x32_bf16 v[196:199], a[196:199], v[32:35], 0\n" \
"ds_read_b64_tr_b16 a[232:233], v215 offset:4608\n" \
"v_pk_mul_f32 v[110:111], v[110:111], v[182:183] op_sel_hi:[1,0]\n" \
"v_cvt_pk_bf16_f32 v104, v104, v105\n" \
"v_mfma_f32_16x16x32_bf16 v[196:199], a[200:203], v[40:43], v[196:199]\n" \
"ds_read_b64_tr_b16 a[234:235], v215 offset:6656\n" \
"v_cvt_pk_bf16_f32 v105, v106, v107\n" \
"v_cvt_pk_bf16_f32 v106, v108, v109\n" \
"v_mfma_f32_16x16x32_bf16 v[196:199], a[204:207], v[48:51], v[196:199]\n" \
"v_cvt_pk_bf16_f32 v107, v110, v111\n" \
"s_cbranch_vccz Lg2_%=\n" \
"s_nop 0\n" \
"buffer_store_dwordx4 v[104:107], v210, %[srdz], s60 offen offset:1024 sc1\n" \
"Lg2_%=:\n" \
"v_mfma_f32_16x16x32_bf16 v[196:199], a[208:211], v[56:59], v[196:199]\n" \
"v_pk_mul_f32 v[112:113], v[112:113], v[182:183] op_sel_hi:[1,0]\n" \
"v_mfma_f32_16x16x32_bf16 v[148:151], a[212:215], v[32:35], 0\n" \
"v_pk_mul_f32 v[114:115], v[114:115], v[182:183] op_sel_hi:[1,0]\n" \
"v_pk_mul_f32 v[116:117], v[116:117], v[182:183] op_sel_hi:[1,0]\n" \
"buffer_load_dwordx4 v212, %[srdt], s49 offen offset:1024 lds\n" \
"v_mfma_f32_16x16x32_bf16 v[148:151], a[216:219], v[40:43], v[148:151]\n" \
"v_pk_mul_f32 v[118:119], v[118:119], v[182:183] op_sel_hi:[1,0]\n" \
"v_cvt_pk_bf16_f32 v112, v112, v113\n" \
"v_mfma_f32_16x16x32_bf16 v[148:151], a[220:223], v[48:51], v[148:151]\n" \
"ds_read_b64_tr_b16 a[236:237], v215 offset:8704\n" \
"v_cvt_pk_bf16_f32 v113, v114, v115\n" \
"v_mfma_f32_16x16x32_bf16 v[148:151], a[224:227], v[56:59], v[148:151]\n" \
"v_cvt_pk_bf16_f32 v114, v116, v117\n" \
"v_cvt_pk_bf16_f32 v115, v118, v119\n" \
"v_mfma_f32_16x16x32_bf16 v[200:203], a[196:199], v[64:67], 0\n" \
"ds_read_b64_tr_b16 a[238:239], v215 offset:10752\n" \
"s_cbranch_vccz Lg3_%=\n" \
"s_nop 0\n" \
"buffer_store_dwordx4 v[112:115], v210, %[srdz], s60 offen offset:2048 sc1\n" \
"Lg3_%=:\n" \
"v_pk_mul_f32 v[120:121], v[120:121], v[182:183] op_sel_hi:[1,0]\n" \
"v_mfma_f32_16x16x32_bf16 v[200:203], a[200:203], v[72:75], v[200:203]\n" \
"v_pk_mul_f32 v[122:123], v[122:123], v[182:183] op_sel_hi:[1,0]\n" \
"v_pk_mul_f32 v[124:125], v[124:125], v[182:183] op_sel_hi:[1,0]\n" \
"v_mfma_f32_16x16x32_bf16 v[200:203], a[204:207], v[80:83], v[200:203]\n" \
"v_pk_mul_f32 v[126:127], v[126:127], v[182:183] op_sel_hi:[1,0]\n" \
"v_mfma_f32_16x16x32_bf16 v[200:203], a[208:211], v[88:91], v[200:203]\n" \
"v_cvt_pk_bf16_f32 v120, v120, v121\n" \
"v_cvt_pk_bf16_f32 v121, v122, v123\n" \
"v_mfma_f32_16x16x32_bf16 v[152:155], a[212:215], v[64:67], 0\n" \
"v_cvt_pk_bf16_f32 v122, v124, v125\n" \
"v_cvt_pk_bf16_f32 v123, v126, v127\n" \
"v_mfma_f32_16x16x32_bf16 v[152:155], a[216:219], v[72:75], v[152:155]\n" \
"s_cbranch_vccz Lg4_%=\n" \
"s_nop 0\n" \
"buffer_store_dwordx4 v[120:123], v210, %[srdz], s60 offen offset:3072 sc1\n" \
"Lg4_%=:\n" \
"v_mfma_f32_16x16x32_bf16 v[152:155], a[220:223], v[80:83], v[152:155]\n" \
"v_add_u32_e32 v220, 0x10000, v215\n" \
"v_mfma_f32_16x16x32_bf16 v[152:155], a[224:227], v[88:91], v[152:155]\n" \
"v_readlane_b32 s62, %[tlo], 19\n" \
"buffer_load_dwordx4 v213, %[srdt], s49 offen offset:2048 lds\n" \
"v_mfma_f32_16x16x32_bf16 v[204:207], a[196:199], v[96:99], 0\n" \
"v_add_u32_e32 v221, 0x10000, v227\n" \
"v_mfma_f32_16x16x32_bf16 v[204:207], a[200:203], v[104:107], v[204:207]\n" \
"ds_read_b64_tr_b16 a[196:197], v215 offset:12800\n" \
"v_mfma_f32_16x16x32_bf16 v[204:207], a[204:207], v[112:115], v[204:207]\n" \
"ds_read_b64_tr_b16 a[198:199], v215 offset:14848\n" \
"v_mfma_f32_16x16x32_bf16 v[204:207], a[208:211], v[120:123], v[204:207]\n" \
"ds_read_b64_tr_b16 a[200:201], v227 offset:512\n" \
"v_mfma_f32_16x16x32_bf16 v[156:159], a[212:215], v[96:99], 0\n" \
"ds_read_b64_tr_b16 a[202:203], v227 offset:2560\n" \
"v_mfma_f32_16x16x32_bf16 v[156:159], a[216:219], v[104:107], v[156:159]\n" \
"ds_read_b64_tr_b16 a[204:205], v227 offset:4608\n" \
"v_mfma_f32_16x16x32_bf16 v[156:159], a[220:223], v[112:115], v[156:159]\n" \
"ds_read_b64_tr_b16 a[206:207], v227 offset:6656\n" \
"v_mfma_f32_16x16x32_bf16 v[156:159], a[224:227], v[120:123], v[156:159]\n" \
"s_waitcnt vmcnt(63) lgkmcnt(4)\n" \
"s_nop 0\n" \
"v_mfma_f32_16x16x32_bf16 v[160:163], a[228:231], v[0:3], 0\n" \
"ds_read_b64_tr_b16 a[208:209], v227 offset:8704\n" \
"v_mfma_f32_16x16x32_bf16 v[160:163], a[232:235], v[8:11], v[160:163]\n" \
"ds_read_b64_tr_b16 a[210:211], v227 offset:10752\n" \
"s_mov_b32 s40, 606208\n" \
"buffer_load_dwordx4 a[176:179], v237, %[srdz], s40 offen sc1\n" \
"v_mfma_f32_16x16x32_bf16 v[164:167], a[228:231], v[32:35], 0\n" \
"ds_read_b64_tr_b16 a[212:213], v227 offset:12800\n" \
"v_mfma_f32_16x16x32_bf16 v[164:167], a[232:235], v[40:43], v[164:167]\n" \
"ds_read_b64_tr_b16 a[214:215], v227 offset:14848\n" \
"buffer_load_dwordx4 v214, %[srdt], s49 offen offset:3072 lds\n" \
"v_mfma_f32_16x16x32_bf16 v[168:171], a[228:231], v[64:67], 0\n" \
"v_mfma_f32_16x16x32_bf16 v[168:171], a[232:235], v[72:75], v[168:171]\n" \
"v_mfma_f32_16x16x32_bf16 v[172:175], a[228:231], v[96:99], 0\n" \
"ds_read_b64_tr_b16 a[216:217], v215 offset:16384\n" \
"v_mfma_f32_16x16x32_bf16 v[172:175], a[232:235], v[104:107], v[172:175]\n" \
"ds_read_b64_tr_b16 a[218:219], v215 offset:18432\n" \
"v_mfma_f32_16x16x32_bf16 v[160:163], a[236:239], v[16:19], v[160:163]\n" \
"ds_read_b64_tr_b16 a[220:221], v215 offset:20480\n" \
"v_mfma_f32_16x16x32_bf16 v[160:163], a[196:199], v[24:27], v[160:163]\n" \
"ds_read_b64_tr_b16 a[222:223], v215 offset:22528\n" \
"s_mov_b32 s40, 610304\n" \
"buffer_load_dwordx4 a[180:183], v237, %[srdz], s40 offen sc1\n" \
"v_mfma_f32_16x16x32_bf16 v[164:167], a[236:239], v[48:51], v[164:167]\n" \
"v_mfma_f32_16x16x32_bf16 v[164:167], a[196:199], v[56:59], v[164:167]\n" \
"v_mfma_f32_16x16x32_bf16 v[168:171], a[236:239], v[80:83], v[168:171]\n" \
"s_waitcnt vmcnt(63) lgkmcnt(0)\n" \
"s_barrier\n" \
"v_mfma_f32_16x16x32_bf16 v[168:171], a[196:199], v[88:91], v[168:171]\n" \
"v_mfma_f32_16x16x32_bf16 v[172:175], a[236:239], v[112:115], v[172:175]\n" \
"ds_read_b64_tr_b16 a[224:225], v215 offset:24576\n" \
"v_mfma_f32_16x16x32_bf16 v[172:175], a[196:199], v[120:123], v[172:175]\n" \
"s_waitcnt vmcnt(63) lgkmcnt(5)\n" \
"s_nop 0\n" \
"v_mfma_f32_16x16x32_bf16 v[176:179], a[200:203], v[0:3], 0\n" \
"ds_read_b64_tr_b16 a[226:227], v215 offset:26624\n" \
"v_mfma_f32_16x16x32_bf16 v[176:179], a[204:207], v[8:11], v[176:179]\n" \
"ds_read_b64_tr_b16 a[228:229], v215 offset:28672\n" \
"s_mov_b32 s40, 614400\n" \
"buffer_load_dwordx4 a[184:187], v237, %[srdz], s40 offen sc1\n" \
"v_mfma_f32_16x16x32_bf16 v[180:183], a[200:203], v[32:35], 0\n" \
"ds_read_b64_tr_b16 a[230:231], v215 offset:30720\n" \
"v_mfma_f32_16x16x32_bf16 v[180:183], a[204:207], v[40:43], v[180:183]\n" \
"s_add_u32 m0, %[m0b], 49152\n" \
"s_cmp_gt_u32 s48, 1\n" \
"s_cselect_b32 s49, 1, 2\n" \
"s_lshl_b32 s49, s49, 18\n" \
"s_or_b32 s49, s49, s59\n" \
"buffer_load_dwordx4 %[wo0], %[srdt], s49 offen offset:0 lds\n" \
"v_mfma_f32_16x16x32_bf16 v[184:187], a[200:203], v[64:67], 0\n" \
"v_mfma_f32_16x16x32_bf16 v[184:187], a[204:207], v[72:75], v[184:187]\n" \
"buffer_load_dwordx4 v212, %[srdt], s49 offen offset:1024 lds\n" \
"v_mfma_f32_16x16x32_bf16 v[188:191], a[200:203], v[96:99], 0\n" \
"ds_read_b64_tr_b16 a[232:233], v227 offset:16384\n" \
"v_mfma_f32_16x16x32_bf16 v[188:191], a[204:207], v[104:107], v[188:191]\n" \
"ds_read_b64_tr_b16 a[234:235], v227 offset:18432\n" \
"v_mfma_f32_16x16x32_bf16 v[176:179], a[208:211], v[16:19], v[176:179]\n" \
"ds_read_b64_tr_b16 a[236:237], v227 offset:20480\n" \
"v_mfma_f32_16x16x32_bf16 v[176:179], a[212:215], v[24:27], v[176:179]\n" \
"ds_read_b64_tr_b16 a[238:239], v227 offset:22528\n" \
"s_mov_b32 s40, 618496\n" \
"buffer_load_dwordx4 a[188:191], v237, %[srdz], s40 offen sc1\n" \
"v_mfma_f32_16x16x32_bf16 v[180:183], a[208:211], v[48:51], v[180:183]\n" \
"v_mfma_f32_16x16x32_bf16 v[180:183], a[212:215], v[56:59], v[180:183]\n" \
"v_mfma_f32_16x16x32_bf16 v[184:187], a[208:211], v[80:83], v[184:187]\n" \
"v_mfma_f32_16x16x32_bf16 v[184:187], a[212:215], v[88:91], v[184:187]\n" \
"buffer_load_dwordx4 v213, %[srdt], s49 offen offset:2048 lds\n" \
"v_mfma_f32_16x16x32_bf16 v[188:191], a[208:211], v[112:115], v[188:191]\n" \
"ds_read_b64_tr_b16 a[196:197], v227 offset:24576\n" \
"v_mfma_f32_16x16x32_bf16 v[188:191], a[212:215], v[120:123], v[188:191]\n" \
"s_waitcnt vmcnt(63) lgkmcnt(5)\n" \
"s_nop 0\n" \
"v_mfma_f32_16x16x32_bf16 v[4:7], a[216:219], v[0:3], 0\n" \
"ds_read_b64_tr_b16 a[198:199], v227 offset:26624\n" \
"v_mfma_f32_16x16x32_bf16 v[4:7], a[220:223], v[8:11], v[4:7]\n" \
"ds_read_b64_tr_b16 a[200:201], v227 offset:28672\n" \
"v_mfma_f32_16x16x32_bf16 v[36:39], a[216:219], v[32:35], 0\n" \
"ds_read_b64_tr_b16 a[202:203], v227 offset:30720\n" \
"v_mfma_f32_16x16x32_bf16 v[36:39], a[220:223], v[40:43], v[36:39]\n" \
"v_mfma_f32_16x16x32_bf16 v[68:71], a[216:219], v[64:67], 0\n" \
"buffer_load_dwordx4 v214, %[srdt], s49 offen offset:3072 lds\n" \
"v_mfma_f32_16x16x32_bf16 v[68:71], a[220:223], v[72:75], v[68:71]\n" \
"v_mfma_f32_16x16x32_bf16 v[100:103], a[216:219], v[96:99], 0\n" \
"ds_read_b64_tr_b16 a[204:205], v215 offset:16896\n" \
"v_mfma_f32_16x16x32_bf16 v[100:103], a[220:223], v[104:107], v[100:103]\n" \
"ds_read_b64_tr_b16 a[206:207], v215 offset:18944\n" \
"v_mfma_f32_16x16x32_bf16 v[4:7], a[224:227], v[16:19], v[4:7]\n" \
"ds_read_b64_tr_b16 a[208:209], v215 offset:20992\n" \
"v_mfma_f32_16x16x32_bf16 v[4:7], a[228:231], v[24:27], v[4:7]\n" \
"ds_read_b64_tr_b16 a[210:211], v215 offset:23040\n" \
"v_mfma_f32_16x16x32_bf16 v[36:39], a[224:227], v[48:51], v[36:39]\n" \
"v_mfma_f32_16x16x32_bf16 v[36:39], a[228:231], v[56:59], v[36:39]\n" \
"v_mfma_f32_16x16x32_bf16 v[68:71], a[224:227], v[80:83], v[68:71]\n" \
"v_mfma_f32_16x16x32_bf16 v[68:71], a[228:231], v[88:91], v[68:71]\n" \
"v_mfma_f32_16x16x32_bf16 v[100:103], a[224:227], v[112:115], v[100:103]\n" \
"ds_read_b64_tr_b16 a[212:213], v215 offset:25088\n" \
"v_mfma_f32_16x16x32_bf16 v[100:103], a[228:231], v[120:123], v[100:103]\n" \
"s_waitcnt vmcnt(63) lgkmcnt(5)\n" \
"s_nop 0\n" \
"v_mfma_f32_16x16x32_bf16 v[12:15], a[232:235], v[0:3], 0\n" \
"ds_read_b64_tr_b16 a[214:215], v215 offset:27136\n" \
"s_cmp_gt_u32 s48, 0\n" \
"s_cselect_b32 s50, 0, 1\n" \
"s_mul_i32 s50, s50, 152064\n" \
"buffer_load_dwordx4 a[64:67], %[oq0], %[srdz], s50 offen offset:0 sc1\n" \
"v_mfma_f32_16x16x32_bf16 v[12:15], a[236:239], v[8:11], v[12:15]\n" \
"ds_read_b64_tr_b16 a[216:217], v215 offset:29184\n" \
"v_mfma_f32_16x16x32_bf16 v[44:47], a[232:235], v[32:35], 0\n" \
"ds_read_b64_tr_b16 a[218:219], v215 offset:31232\n" \
"buffer_load_dwordx4 a[68:71], %[oq0], %[srdz], s50 offen offset:1024 sc1\n" \
"v_mfma_f32_16x16x32_bf16 v[44:47], a[236:239], v[40:43], v[44:47]\n" \
"v_mfma_f32_16x16x32_bf16 v[76:79], a[232:235], v[64:67], 0\n" \
"buffer_load_dwordx4 a[80:83], v208, %[srdz], s50 offen offset:0 sc1\n" \
"v_mfma_f32_16x16x32_bf16 v[76:79], a[236:239], v[72:75], v[76:79]\n" \
"v_mfma_f32_16x16x32_bf16 v[108:111], a[232:235], v[96:99], 0\n" \
"ds_read_b64_tr_b16 a[220:221], v227 offset:16896\n" \
"buffer_load_dwordx4 a[84:87], v208, %[srdz], s50 offen offset:1024 sc1\n" \
"v_mfma_f32_16x16x32_bf16 v[108:111], a[236:239], v[104:107], v[108:111]\n" \
"ds_read_b64_tr_b16 a[222:223], v227 offset:18944\n" \
"v_mfma_f32_16x16x32_bf16 v[12:15], a[196:199], v[16:19], v[12:15]\n" \
"ds_read_b64_tr_b16 a[224:225], v227 offset:25088\n" \
"buffer_load_dwordx4 a[96:99], v209, %[srdz], s50 offen offset:0 sc1\n" \
"v_mfma_f32_16x16x32_bf16 v[12:15], a[200:203], v[24:27], v[12:15]\n" \
"ds_read_b64_tr_b16 a[226:227], v227 offset:27136\n" \
"v_mfma_f32_16x16x32_bf16 v[44:47], a[196:199], v[48:51], v[44:47]\n" \
"ds_read_b64_tr_b16 a[228:229], v227 offset:20992\n" \
"buffer_load_dwordx4 a[100:103], v209, %[srdz], s50 offen offset:1024 sc1\n" \
"v_mfma_f32_16x16x32_bf16 v[44:47], a[200:203], v[56:59], v[44:47]\n" \
"ds_read_b64_tr_b16 a[230:231], v227 offset:23040\n" \
"v_mfma_f32_16x16x32_bf16 v[76:79], a[196:199], v[80:83], v[76:79]\n" \
"ds_read_b64_tr_b16 a[232:233], v227 offset:29184\n" \
"buffer_load_dwordx4 a[112:115], v210, %[srdz], s50 offen offset:0 sc1\n" \
"v_mfma_f32_16x16x32_bf16 v[76:79], a[200:203], v[88:91], v[76:79]\n" \
"ds_read_b64_tr_b16 a[234:235], v227 offset:31232\n" \
"v_mfma_f32_16x16x32_bf16 v[108:111], a[196:199], v[112:115], v[108:111]\n" \
"ds_read_b64_tr_b16 a[236:237], v215 offset:32768\n" \
"buffer_load_dwordx4 a[116:119], v210, %[srdz], s50 offen offset:1024 sc1\n" \
"v_mfma_f32_16x16x32_bf16 v[108:111], a[200:203], v[120:123], v[108:111]\n" \
"s_waitcnt vmcnt(63) lgkmcnt(13)\n" \
"s_nop 0\n" \
"v_mfma_f32_16x16x32_bf16 v[20:23], a[204:207], v[0:3], 0\n" \
"ds_read_b64_tr_b16 a[238:239], v215 offset:34816\n" \
"buffer_load_dwordx4 a[72:75], %[oq0], %[srdz], s50 offen offset:2048 sc1\n" \
"v_mfma_f32_16x16x32_bf16 v[20:23], a[208:211], v[8:11], v[20:23]\n" \
"ds_read_b64_tr_b16 a[196:197], v215 offset:36864\n" \
"v_mfma_f32_16x16x32_bf16 v[52:55], a[204:207], v[32:35], 0\n" \
"ds_read_b64_tr_b16 a[198:199], v215 offset:38912\n" \
"buffer_load_dwordx4 a[76:79], %[oq0], %[srdz], s50 offen offset:3072 sc1\n" \
"v_mfma_f32_16x16x32_bf16 v[52:55], a[208:211], v[40:43], v[52:55]\n" \
"v_mfma_f32_16x16x32_bf16 v[84:87], a[204:207], v[64:67], 0\n" \
"s_waitcnt vmcnt(63) lgkmcnt(4)\n" \
"s_barrier\n" \
"v_mfma_f32_16x16x32_bf16 v[84:87], a[208:211], v[72:75], v[84:87]\n" \
"buffer_load_dwordx4 a[88:91], v208, %[srdz], s50 offen offset:2048 sc1\n" \
"v_mfma_f32_16x16x32_bf16 v[116:119], a[204:207], v[96:99], 0\n" \
"ds_read_b64_tr_b16 a[200:201], v215 offset:40960\n" \
"v_mfma_f32_16x16x32_bf16 v[116:119], a[208:211], v[104:107], v[116:119]\n" \
"ds_read_b64_tr_b16 a[202:203], v215 offset:43008\n" \
"buffer_load_dwordx4 a[92:95], v208, %[srdz], s50 offen offset:3072 sc1\n" \
"v_mfma_f32_16x16x32_bf16 v[20:23], a[212:215], v[16:19], v[20:23]\n" \
"ds_read_b64_tr_b16 a[204:205], v215 offset:45056\n" \
"v_mfma_f32_16x16x32_bf16 v[20:23], a[216:219], v[24:27], v[20:23]\n" \
"ds_read_b64_tr_b16 a[206:207], v215 offset:47104\n" \
"buffer_load_dwordx4 a[104:107], v209, %[srdz], s50 offen offset:2048 sc1\n" \
"s_waitcnt vmcnt(15)\n" \
"s_nop 0\n" \
"v_mfma_f32_16x16x32_bf16 v[52:55], a[212:215], v[48:51], v[52:55]\n" \
"v_mfma_f32_16x16x32_bf16 v[216:219], a[176:179], v[0:3], 0\n" \
"v_mfma_f32_16x16x32_bf16 v[216:219], a[180:183], v[8:11], v[216:219]\n" \
"v_mfma_f32_16x16x32_bf16 v[216:219], a[184:187], v[16:19], v[216:219]\n" \
"v_mfma_f32_16x16x32_bf16 v[216:219], a[188:191], v[24:27], v[216:219]\n" \
"v_mfma_f32_16x16x32_bf16 v[52:55], a[216:219], v[56:59], v[52:55]\n" \
"buffer_load_dwordx4 a[108:111], v209, %[srdz], s50 offen offset:3072 sc1\n" \
"v_mfma_f32_16x16x32_bf16 v[84:87], a[212:215], v[80:83], v[84:87]\n" \
"v_mfma_f32_16x16x32_bf16 v[84:87], a[216:219], v[88:91], v[84:87]\n" \
"buffer_load_dwordx4 a[120:123], v210, %[srdz], s50 offen offset:2048 sc1\n" \
"v_mfma_f32_16x16x32_bf16 v[116:119], a[212:215], v[112:115], v[116:119]\n" \
"v_mfma_f32_16x16x32_bf16 v[232:235], a[176:179], v[32:35], 0\n" \
"v_mfma_f32_16x16x32_bf16 v[232:235], a[180:183], v[40:43], v[232:235]\n" \
"v_mfma_f32_16x16x32_bf16 v[232:235], a[184:187], v[48:51], v[232:235]\n" \
"v_mfma_f32_16x16x32_bf16 v[232:235], a[188:191], v[56:59], v[232:235]\n" \
"v_mfma_f32_16x16x32_bf16 v[116:119], a[216:219], v[120:123], v[116:119]\n" \
"v_mov_b32_dpp v231, v216 quad_perm:[0,1,2,3] row_mask:0x1 bank_mask:0xf\n" \
"s_waitcnt vmcnt(63) lgkmcnt(10)\n" \
"s_nop 0\n" \
"v_mfma_f32_16x16x32_bf16 v[28:31], a[220:223], v[0:3], 0\n" \
"buffer_load_dwordx4 a[124:127], v210, %[srdz], s50 offen offset:3072 sc1\n" \
"v_mfma_f32_16x16x32_bf16 v[28:31], a[228:231], v[8:11], v[28:31]\n" \
"s_sub_u32 s61, s48, 4\n" \
"s_cmp_lt_u32 s61, 2\n" \
"s_cselect_b64 vcc, -1, 0\n" \
"s_nop 0\n" \
"v_mfma_f32_16x16x32_bf16 v[60:63], a[220:223], v[32:35], 0\n" \
"s_cbranch_vccz Lg5_%=\n" \
"s_nop 0\n" \
"buffer_store_dwordx4 v[0:3], %[oq0], %[srdz], s60 offen offset:0 sc1\n" \
"Lg5_%=:\n" \
"v_mfma_f32_16x16x32_bf16 v[216:219], a[176:179], v[64:67], 0\n" \
"v_mfma_f32_16x16x32_bf16 v[216:219], a[180:183], v[72:75], v[216:219]\n" \
"v_mfma_f32_16x16x32_bf16 v[216:219], a[184:187], v[80:83], v[216:219]\n" \
"v_mfma_f32_16x16x32_bf16 v[216:219], a[188:191], v[88:91], v[216:219]\n" \
"v_mfma_f32_16x16x32_bf16 v[60:63], a[228:231], v[40:43], v[60:63]\n" \
"v_mov_b32_dpp v231, v232 quad_perm:[0,1,2,3] row_mask:0x2 bank_mask:0xf\n" \
"v_mfma_f32_16x16x32_bf16 v[92:95], a[220:223], v[64:67], 0\n" \
"s_cmp_gt_u32 s48, 1\n" \
"s_cselect_b32 s50, 1, 2\n" \
"s_mul_i32 s50, s50, 152064\n" \
"buffer_load_dwordx4 a[0:3], %[oq0], %[srdz], s50 offen offset:0 sc1\n" \
"v_mfma_f32_16x16x32_bf16 v[92:95], a[228:231], v[72:75], v[92:95]\n" \
"v_mfma_f32_16x16x32_bf16 v[124:127], a[220:223], v[96:99], 0\n" \
"ds_read_b64_tr_b16 a[208:209], v227 offset:32768\n" \
"v_mfma_f32_16x16x32_bf16 v[232:235], a[176:179], v[96:99], 0\n" \
"v_mfma_f32_16x16x32_bf16 v[232:235], a[180:183], v[104:107], v[232:235]\n" \
"v_mfma_f32_16x16x32_bf16 v[232:235], a[184:187], v[112:115], v[232:235]\n" \
"v_mfma_f32_16x16x32_bf16 v[232:235], a[188:191], v[120:123], v[232:235]\n" \
"v_mfma_f32_16x16x32_bf16 v[124:127], a[228:231], v[104:107], v[124:127]\n" \
"ds_read_b64_tr_b16 a[210:211], v227 offset:34816\n" \
"v_mov_b32_dpp v231, v216 quad_perm:[0,1,2,3] row_mask:0x4 bank_mask:0xf\n" \
"s_waitcnt vmcnt(15) lgkmcnt(6)\n" \
"s_nop 0\n" \
"v_mfma_f32_16x16x32_bf16 v[128:131], a[236:239], a[64:67], v[192:195]\n" \
"ds_read_b64_tr_b16 a[212:213], v227 offset:36864\n" \
"v_mfma_f32_16x16x32_bf16 v[128:131], a[196:199], a[68:71], v[128:131]\n" \
"ds_read_b64_tr_b16 a[214:215], v227 offset:38912\n" \
"s_waitcnt vmcnt(13) lgkmcnt(15)\n" \
"s_nop 0\n" \
"v_mfma_f32_16x16x32_bf16 v[132:135], a[236:239], a[80:83], v[196:199]\n" \
"s_cbranch_vccz Lg6_%=\n" \
"s_nop 0\n" \
"buffer_store_dwordx4 v[8:11], %[oq0], %[srdz], s60 offen offset:1024 sc1\n" \
"Lg6_%=:\n" \
"v_mfma_f32_16x16x32_bf16 v[132:135], a[196:199], a[84:87], v[132:135]\n" \
"s_waitcnt vmcnt(13) lgkmcnt(15)\n" \
"s_bitcmp1_b32 0x30, s48\n" \
"s_cbranch_scc1 Lcw2_%=\n" \
"s_waitcnt vmcnt(11)\n" \
"Lcw2_%=:\n" \
"v_mfma_f32_16x16x32_bf16 v[136:139], a[236:239], a[96:99], v[200:203]\n" \
"v_mov_b32_dpp v231, v232 quad_perm:[0,1,2,3] row_mask:0x8 bank_mask:0xf\n" \
"buffer_load_dwordx4 a[4:7], %[oq0], %[srdz], s50 offen offset:1024 sc1\n" \
"v_mfma_f32_16x16x32_bf16 v[136:139], a[196:199], a[100:103], v[136:139]\n" \
"s_waitcnt vmcnt(12) lgkmcnt(15)\n" \
"s_bitcmp1_b32 0x30, s48\n" \
"s_cbranch_scc1 Lcw3_%=\n" \
"s_waitcnt vmcnt(10)\n" \
"Lcw3_%=:\n" \
"v_mfma_f32_16x16x32_bf16 v[140:143], a[236:239], a[112:115], v[204:207]\n" \
"ds_read_b64_tr_b16 a[216:217], v227 offset:40960\n" \
"v_cmp_u_f32_e64 s[42:43], v231, v231\n" \
"v_readlane_b32 s41, %[tlo], 33\n" \
"v_mfma_f32_16x16x32_bf16 v[140:143], a[196:199], a[116:119], v[140:143]\n" \
"s_waitcnt vmcnt(10) lgkmcnt(5)\n" \
"s_bitcmp1_b32 0x30, s48\n" \
"s_cbranch_scc1 Lcw4_%=\n" \
"s_waitcnt vmcnt(8)\n" \
"Lcw4_%=:\n" \
"v_mfma_f32_16x16x32_bf16 v[128:131], a[200:203], a[72:75], v[128:131]\n" \
"ds_read_b64_tr_b16 a[218:219], v227 offset:43008\n" \
"buffer_load_dwordx4 a[8:11], %[oq0], %[srdz], s50 offen offset:2048 sc1\n" \
"v_mfma_f32_16x16x32_bf16 v[128:131], a[204:207], a[76:79], v[128:131]\n" \
"ds_read_b64_tr_b16 a[220:221], v227 offset:45056\n" \
"v_lshrrev_b32_e64 v222, 5, v224\n" \
"s_waitcnt vmcnt(9) lgkmcnt(15)\n" \
"s_bitcmp1_b32 0x30, s48\n" \
"s_cbranch_scc1 Lcw5_%=\n" \
"s_waitcnt vmcnt(7)\n" \
"Lcw5_%=:\n" \
"v_mfma_f32_16x16x32_bf16 v[132:135], a[200:203], a[88:91], v[132:135]\n" \
"ds_read_b64_tr_b16 a[222:223], v227 offset:47104\n" \
"v_mfma_f32_16x16x32_bf16 v[132:135], a[204:207], a[92:95], v[132:135]\n" \
"s_cbranch_vccz Lg7_%=\n" \
"s_nop 0\n" \
"buffer_store_dwordx4 v[16:19], %[oq0], %[srdz], s60 offen offset:2048 sc1\n" \
"Lg7_%=:\n" \
"s_waitcnt vmcnt(6) lgkmcnt(14)\n" \
"s_bitcmp1_b32 0x30, s48\n" \
"s_cbranch_scc1 Lcw6_%=\n" \
"s_waitcnt vmcnt(3)\n" \
"Lcw6_%=:\n" \
"v_mfma_f32_16x16x32_bf16 v[136:139], a[200:203], a[104:107], v[136:139]\n" \
"s_cmp_lg_u64 s[42:43], 0\n" \
"s_cbranch_scc1 Ls0_%=\n" \
"Ls0b_%=:\n" \
"v_mfma_f32_16x16x32_bf16 v[136:139], a[204:207], a[108:111], v[136:139]\n" \
"v_or_b32_e64 v231, 1, v231\n" \
"buffer_load_dwordx4 a[12:15], %[oq0], %[srdz], s50 offen offset:3072 sc1\n" \
"v_mfma_f32_16x16x32_bf16 v[140:143], a[200:203], a[120:123], v[140:143]\n" \
"ds_read_b64_tr_b16 a[228:229], v215 offset:33280\n" \
"v_mfma_f32_16x16x32_bf16 v[140:143], a[204:207], a[124:127], v[140:143]\n" \
"ds_read_b64_tr_b16 a[230:231], v215 offset:35328\n" \
"v_mfma_f32_16x16x32_bf16 v[28:31], a[224:227], v[16:19], v[28:31]\n" \
"buffer_store_dword v231, v222, %[srdz], s41 offen sc1\n" \
"v_mfma_f32_16x16x32_bf16 v[28:31], a[232:235], v[24:27], v[28:31]\n" \
"v_mfma_f32_16x16x32_bf16 v[60:63], a[224:227], v[48:51], v[60:63]\n" \
"v_mfma_f32_16x16x32_bf16 v[60:63], a[232:235], v[56:59], v[60:63]\n" \
"s_cbranch_vccz Lg8_%=\n" \
"s_nop 0\n" \
"buffer_store_dwordx4 v[24:27], %[oq0], %[srdz], s60 offen offset:3072 sc1\n" \
"Lg8_%=:\n" \
"v_mfma_f32_16x16x32_bf16 v[92:95], a[224:227], v[80:83], v[92:95]\n" \
"v_mfma_f32_16x16x32_bf16 v[92:95], a[232:235], v[88:91], v[92:95]\n" \
"v_cmp_u_f32_e64 s[44:45], v128, v132\n" \
"buffer_load_dwordx4 a[16:19], v208, %[srdz], s50 offen offset:0 sc1\n" \
"v_mfma_f32_16x16x32_bf16 v[124:127], a[224:227], v[112:115], v[124:127]\n" \
"v_mfma_f32_16x16x32_bf16 v[124:127], a[232:235], v[120:123], v[124:127]\n" \
"v_cmp_u_f32_e64 s[56:57], v136, v140\n" \
"s_or_b64 s[56:57], s[56:57], s[44:45]\n" \
"s_cbranch_scc1 Lsp1_%=\n" \
"Lbk1_%=:\n" \
"s_waitcnt vmcnt(63) lgkmcnt(2)\n" \
"s_nop 0\n" \
"v_mfma_f32_16x16x32_bf16 v[144:147], a[208:211], a[64:67], v[144:147]\n" \
"ds_read_b64_tr_b16 a[224:225], v215 offset:37376\n" \
"v_mfma_f32_16x16x32_bf16 v[144:147], a[212:215], a[68:71], v[144:147]\n" \
"ds_read_b64_tr_b16 a[226:227], v215 offset:39424\n" \
"buffer_load_dwordx4 a[20:23], v208, %[srdz], s50 offen offset:1024 sc1\n" \
"v_mfma_f32_16x16x32_bf16 v[148:151], a[208:211], a[80:83], v[148:151]\n" \
"ds_read_b64_tr_b16 a[196:197], v215 offset:41472\n" \
"v_mfma_f32_16x16x32_bf16 v[148:151], a[212:215], a[84:87], v[148:151]\n" \
"ds_read_b64_tr_b16 a[198:199], v215 offset:43520\n" \
"v_mfma_f32_16x16x32_bf16 v[152:155], a[208:211], a[96:99], v[152:155]\n" \
"ds_read_b64_tr_b16 a[200:201], v215 offset:45568\n" \
"s_cbranch_vccz Lg9_%=\n" \
"s_nop 0\n" \
"buffer_store_dwordx4 v[32:35], v208, %[srdz], s60 offen offset:0 sc1\n" \
"Lg9_%=:\n" \
"v_mfma_f32_16x16x32_bf16 v[152:155], a[212:215], a[100:103], v[152:155]\n" \
"ds_read_b64_tr_b16 a[202:203], v215 offset:47616\n" \
"v_mfma_f32_16x16x32_bf16 v[156:159], a[208:211], a[112:115], v[156:159]\n" \
"ds_read_b64_tr_b16 a[204:205], v227 offset:33280\n" \
"buffer_load_dwordx4 a[24:27], v208, %[srdz], s50 offen offset:2048 sc1\n" \
"v_mfma_f32_16x16x32_bf16 v[156:159], a[212:215], a[116:119], v[156:159]\n" \
"ds_read_b64_tr_b16 a[206:207], v227 offset:35328\n" \
"v_mfma_f32_16x16x32_bf16 v[144:147], a[216:219], a[72:75], v[144:147]\n" \
"ds_read_b64_tr_b16 a[232:233], v227 offset:37376\n" \
"v_mfma_f32_16x16x32_bf16 v[144:147], a[220:223], a[76:79], v[144:147]\n" \
"ds_read_b64_tr_b16 a[234:235], v227 offset:39424\n" \
"v_mfma_f32_16x16x32_bf16 v[148:151], a[216:219], a[88:91], v[148:151]\n" \
"buffer_load_dwordx4 a[28:31], v208, %[srdz], s50 offen offset:3072 sc1\n" \
"v_mfma_f32_16x16x32_bf16 v[148:151], a[220:223], a[92:95], v[148:151]\n" \
"v_mfma_f32_16x16x32_bf16 v[152:155], a[216:219], a[104:107], v[152:155]\n" \
"s_cbranch_vccz Lg10_%=\n" \
"s_nop 0\n" \
"buffer_store_dwordx4 v[40:43], v208, %[srdz], s60 offen offset:1024 sc1\n" \
"Lg10_%=:\n" \
"v_mfma_f32_16x16x32_bf16 v[152:155], a[220:223], a[108:111], v[152:155]\n" \
"v_mfma_f32_16x16x32_bf16 v[156:159], a[216:219], a[120:123], v[156:159]\n" \
"ds_read_b64_tr_b16 a[236:237], v227 offset:41472\n" \
"buffer_load_dwordx4 a[32:35], v209, %[srdz], s50 offen offset:0 sc1\n" \
"v_mfma_f32_16x16x32_bf16 v[156:159], a[220:223], a[124:127], v[156:159]\n" \
"s_waitcnt vmcnt(63) lgkmcnt(5)\n" \
"s_nop 0\n" \
"v_mfma_f32_16x16x32_bf16 v[160:163], a[228:231], a[64:67], v[160:163]\n" \
"ds_read_b64_tr_b16 a[238:239], v227 offset:43520\n" \
"v_mfma_f32_16x16x32_bf16 v[160:163], a[224:227], a[68:71], v[160:163]\n" \
"ds_read_b64_tr_b16 a[208:209], v227 offset:45568\n" \
"v_mfma_f32_16x16x32_bf16 v[164:167], a[228:231], a[80:83], v[164:167]\n" \
"ds_read_b64_tr_b16 a[210:211], v227 offset:47616\n" \
"buffer_load_dwordx4 a[36:39], v209, %[srdz], s50 offen offset:1024 sc1\n" \
"v_mfma_f32_16x16x32_bf16 v[164:167], a[224:227], a[84:87], v[164:167]\n" \
"v_mfma_f32_16x16x32_bf16 v[168:171], a[228:231], a[96:99], v[168:171]\n" \
"v_mfma_f32_16x16x32_bf16 v[168:171], a[224:227], a[100:103], v[168:171]\n" \
"s_cbranch_vccz Lg11_%=\n" \
"s_nop 0\n" \
"buffer_store_dwordx4 v[48:51], v208, %[srdz], s60 offen offset:2048 sc1\n" \
"Lg11_%=:\n" \
"v_mfma_f32_16x16x32_bf16 v[172:175], a[228:231], a[112:115], v[172:175]\n" \
"v_mfma_f32_16x16x32_bf16 v[172:175], a[224:227], a[116:119], v[172:175]\n" \
"buffer_load_dwordx4 a[40:43], v209, %[srdz], s50 offen offset:2048 sc1\n" \
"v_mfma_f32_16x16x32_bf16 v[160:163], a[196:199], a[72:75], v[160:163]\n" \
"v_mfma_f32_16x16x32_bf16 v[160:163], a[200:203], a[76:79], v[160:163]\n" \
"buffer_load_dwordx4 a[44:47], v209, %[srdz], s50 offen offset:3072 sc1\n" \
"v_mfma_f32_16x16x32_bf16 v[164:167], a[196:199], a[88:91], v[164:167]\n" \
"v_mfma_f32_16x16x32_bf16 v[164:167], a[200:203], a[92:95], v[164:167]\n" \
"buffer_load_dwordx4 a[48:51], v210, %[srdz], s50 offen offset:0 sc1\n" \
"v_mfma_f32_16x16x32_bf16 v[168:171], a[196:199], a[104:107], v[168:171]\n" \
"s_waitcnt vmcnt(59) lgkmcnt(0)\n" \
"s_bitcmp1_b32 0xf, s48\n" \
"s_cbranch_scc1 Lcw7_%=\n" \
"s_waitcnt vmcnt(50)\n" \
"s_bitcmp1_b32 0x30, s48\n" \
"s_cbranch_scc1 Lcw7_%=\n" \
"s_waitcnt vmcnt(43)\n" \
"Lcw7_%=:\n" \
"s_barrier\n" \
"v_mfma_f32_16x16x32_bf16 v[168:171], a[200:203], a[108:111], v[168:171]\n" \
"ds_read_b64_tr_b16 a[212:213], v220 offset:32768\n" \
"s_cbranch_vccz Lg12_%=\n" \
"s_nop 0\n" \
"buffer_store_dwordx4 v[56:59], v208, %[srdz], s60 offen offset:3072 sc1\n" \
"Lg12_%=:\n" \
"v_mfma_f32_16x16x32_bf16 v[172:175], a[196:199], a[120:123], v[172:175]\n" \
"ds_read_b64_tr_b16 a[214:215], v220 offset:34816\n" \
"v_mfma_f32_16x16x32_bf16 v[172:175], a[200:203], a[124:127], v[172:175]\n" \
"s_waitcnt vmcnt(63) lgkmcnt(2)\n" \
"s_nop 0\n" \
"v_mfma_f32_16x16x32_bf16 v[176:179], a[204:207], a[64:67], v[176:179]\n" \
"ds_read_b64_tr_b16 a[216:217], v220 offset:36864\n" \
"buffer_load_dwordx4 a[52:55], v210, %[srdz], s50 offen offset:1024 sc1\n" \
"v_mfma_f32_16x16x32_bf16 v[176:179], a[232:235], a[68:71], v[176:179]\n" \
"ds_read_b64_tr_b16 a[218:219], v220 offset:38912\n" \
"v_mfma_f32_16x16x32_bf16 v[180:183], a[204:207], a[80:83], v[180:183]\n" \
"ds_read_b64_tr_b16 a[220:221], v220 offset:40960\n" \
"v_mfma_f32_16x16x32_bf16 v[180:183], a[232:235], a[84:87], v[180:183]\n" \
"ds_read_b64_tr_b16 a[222:223], v220 offset:43008\n" \
"v_mfma_f32_16x16x32_bf16 v[184:187], a[204:207], a[96:99], v[184:187]\n" \
"ds_read_b64_tr_b16 a[228:229], v220 offset:45056\n" \
"buffer_load_dwordx4 a[56:59], v210, %[srdz], s50 offen offset:2048 sc1\n" \
"v_mfma_f32_16x16x32_bf16 v[184:187], a[232:235], a[100:103], v[184:187]\n" \
"ds_read_b64_tr_b16 a[230:231], v220 offset:47104\n" \
"v_mfma_f32_16x16x32_bf16 v[188:191], a[204:207], a[112:115], v[188:191]\n" \
"ds_read_b64_tr_b16 a[224:225], v221 offset:32768\n" \
".if . - adaln_body != 45056\n" \
".error \"size model\"\n" \
".endif\n" \
"Lk10_%=:\n" \
"s_cbranch_execz Lk11_%=\n" \
"s_nop 0\n" \
"s_cbranch_vccz Lg13_%=\n" \
"s_nop 0\n" \
"buffer_store_dwordx4 v[64:67], v209, %[srdz], s60 offen offset:0 sc1\n" \
"Lg13_%=:\n" \
"v_mfma_f32_16x16x32_bf16 v[188:191], a[232:235], a[116:119], v[188:191]\n" \
"ds_read_b64_tr_b16 a[226:227], v221 offset:34816\n" \
"v_mfma_f32_16x16x32_bf16 v[176:179], a[236:239], a[72:75], v[176:179]\n" \
"ds_read_b64_tr_b16 a[196:197], v221 offset:36864\n" \
"buffer_load_dwordx4 a[60:63], v210, %[srdz], s50 offen offset:3072 sc1\n" \
"v_mfma_f32_16x16x32_bf16 v[176:179], a[208:211], a[76:79], v[176:179]\n" \
"ds_read_b64_tr_b16 a[198:199], v221 offset:38912\n" \
"v_mfma_f32_16x16x32_bf16 v[180:183], a[236:239], a[88:91], v[180:183]\n" \
"s_cmp_gt_u32 s48, 2\n" \
"s_cselect_b32 s50, 2, 3\n" \
"s_mul_i32 s50, s50, 152064\n" \
"buffer_load_dwordx4 a[128:131], %[oq0], %[srdz], s50 offen offset:0 sc1\n" \
"v_mfma_f32_16x16x32_bf16 v[180:183], a[208:211], a[92:95], v[180:183]\n" \
"v_mfma_f32_16x16x32_bf16 v[184:187], a[236:239], a[104:107], v[184:187]\n" \
"v_mfma_f32_16x16x32_bf16 v[184:187], a[208:211], a[108:111], v[184:187]\n" \
"v_mfma_f32_16x16x32_bf16 v[188:191], a[236:239], a[120:123], v[188:191]\n" \
"ds_read_b64_tr_b16 a[200:201], v221 offset:40960\n" \
"s_cbranch_vccz Lg14_%=\n" \
"s_nop 0\n" \
"buffer_store_dwordx4 v[72:75], v209, %[srdz], s60 offen offset:1024 sc1\n" \
"Lg14_%=:\n" \
"v_mfma_f32_16x16x32_bf16 v[188:191], a[208:211], a[124:127], v[188:191]\n" \
"s_waitcnt vmcnt(63) lgkmcnt(5)\n" \
"s_nop 0\n" \
"v_mfma_f32_16x16x32_bf16 v[4:7], a[212:215], a[64:67], v[4:7]\n" \
"ds_read_b64_tr_b16 a[202:203], v221 offset:43008\n" \
"v_mfma_f32_16x16x32_bf16 v[4:7], a[216:219], a[68:71], v[4:7]\n" \
"ds_read_b64_tr_b16 a[204:205], v221 offset:45056\n" \
"s_add_u32 m0, %[m0b], 0\n" \
"s_cmp_gt_u32 s48, 2\n" \
"s_cselect_b32 s49, 2, 3\n" \
"s_lshl_b32 s49, s49, 18\n" \
"s_or_b32 s49, s49, s58\n" \
"s_nop 0\n" \
"buffer_load_dwordx4 %[wo0], %[srdt], s49 offen offset:0 lds\n" \
"v_mfma_f32_16x16x32_bf16 v[36:39], a[212:215], a[80:83], v[36:39]\n" \
"ds_read_b64_tr_b16 a[206:207], v221 offset:47104\n" \
"v_mfma_f32_16x16x32_bf16 v[36:39], a[216:219], a[84:87], v[36:39]\n" \
"buffer_load_dwordx4 a[132:135], %[oq0], %[srdz], s50 offen offset:1024 sc1\n" \
"v_mfma_f32_16x16x32_bf16 v[68:71], a[212:215], a[96:99], v[68:71]\n" \
"v_mfma_f32_16x16x32_bf16 v[68:71], a[216:219], a[100:103], v[68:71]\n" \
"v_mfma_f32_16x16x32_bf16 v[100:103], a[212:215], a[112:115], v[100:103]\n" \
"ds_read_b64_tr_b16 a[232:233], v220 offset:33280\n" \
"v_mfma_f32_16x16x32_bf16 v[100:103], a[216:219], a[116:119], v[100:103]\n" \
"ds_read_b64_tr_b16 a[234:235], v220 offset:35328\n" \
"s_cbranch_vccz Lg15_%=\n" \
"s_nop 0\n" \
"buffer_store_dwordx4 v[80:83], v209, %[srdz], s60 offen offset:2048 sc1\n" \
"Lg15_%=:\n" \
"v_mfma_f32_16x16x32_bf16 v[4:7], a[220:223], a[72:75], v[4:7]\n" \
"ds_read_b64_tr_b16 a[236:237], v220 offset:37376\n" \
"s_lshl_b32 s40, %[wid], 12\n" \
"s_nop 0\n" \
"s_mov_b32 s43, 0xffff0000\n" \
"v_mfma_f32_16x16x32_bf16 v[4:7], a[228:231], a[76:79], v[4:7]\n" \
"ds_read_b64_tr_b16 a[238:239], v220 offset:39424\n" \
"buffer_load_dwordx4 a[136:139], %[oq0], %[srdz], s50 offen offset:2048 sc1\n" \
"v_mfma_f32_16x16x32_bf16 v[36:39], a[220:223], a[88:91], v[36:39]\n" \
"v_lshrrev_b32_e32 v222, 7, v224\n" \
"s_sub_u32 s40, %[m0b], s40\n" \
"v_mfma_f32_16x16x32_bf16 v[36:39], a[228:231], a[92:95], v[36:39]\n" \
"v_readlane_b32 s41, %[tlo], 26\n" \
"v_mfma_f32_16x16x32_bf16 v[68:71], a[220:223], a[104:107], v[68:71]\n" \
"v_readlane_b32 s42, %[thi], 26\n" \
"v_mfma_f32_16x16x32_bf16 v[68:71], a[228:231], a[108:111], v[68:71]\n" \
"buffer_load_dwordx4 a[140:143], %[oq0], %[srdz], s50 offen offset:3072 sc1\n" \
"v_mfma_f32_16x16x32_bf16 v[100:103], a[220:223], a[120:123], v[100:103]\n" \
"ds_read_b64_tr_b16 a[208:209], v220 offset:41472\n" \
"v_lshrrev_b32_e64 v217, 3, v224\n" \
"v_mfma_f32_16x16x32_bf16 v[100:103], a[228:231], a[124:127], v[100:103]\n" \
"s_waitcnt vmcnt(63) lgkmcnt(5)\n" \
"s_nop 0\n" \
"v_mfma_f32_16x16x32_bf16 v[12:15], a[224:227], a[64:67], v[12:15]\n" \
"ds_read_b64_tr_b16 a[210:211], v220 offset:43520\n" \
"s_cbranch_vccz Lg16_%=\n" \
"s_nop 0\n" \
"buffer_store_dwordx4 v[88:91], v209, %[srdz], s60 offen offset:3072 sc1\n" \
"Lg16_%=:\n" \
"v_mfma_f32_16x16x32_bf16 v[12:15], a[196:199], a[68:71], v[12:15]\n" \
"ds_read_b64_tr_b16 a[212:213], v220 offset:45568\n" \
"v_lshrrev_b32_e64 v219, 5, v222\n" \
"v_mfma_f32_16x16x32_bf16 v[44:47], a[224:227], a[80:83], v[44:47]\n" \
"ds_read_b64_tr_b16 a[214:215], v220 offset:47616\n" \
"buffer_load_dwordx4 v212, %[srdt], s49 offen offset:1024 lds\n" \
"v_mfma_f32_16x16x32_bf16 v[44:47], a[196:199], a[84:87], v[44:47]\n" \
"v_bfe_u32 v235, v222, 2, 1\n" \
"v_mfma_f32_16x16x32_bf16 v[76:79], a[224:227], a[96:99], v[76:79]\n" \
"v_lshl_or_b32 v219, v219, 1, v235\n" \
"v_mfma_f32_16x16x32_bf16 v[76:79], a[196:199], a[100:103], v[76:79]\n" \
"buffer_load_dwordx4 a[144:147], v208, %[srdz], s50 offen offset:0 sc1\n" \
"v_mfma_f32_16x16x32_bf16 v[108:111], a[224:227], a[112:115], v[108:111]\n" \
"ds_read_b64_tr_b16 a[216:217], v221 offset:33280\n" \
"v_add_u32_e64 v235, s41, v235\n" \
"v_mfma_f32_16x16x32_bf16 v[108:111], a[196:199], a[116:119], v[108:111]\n" \
"ds_read_b64_tr_b16 a[218:219], v221 offset:35328\n" \
"v_sub_u32_e32 %[ta0], 0x7ff, v235\n" \
"v_mfma_f32_16x16x32_bf16 v[12:15], a[200:203], a[72:75], v[12:15]\n" \
"ds_read_b64_tr_b16 a[220:221], v221 offset:41472\n" \
"s_cbranch_vccz Lg17_%=\n" \
"s_nop 0\n" \
"buffer_store_dwordx4 v[96:99], v210, %[srdz], s60 offen offset:0 sc1\n" \
"Lg17_%=:\n" \
"v_mfma_f32_16x16x32_bf16 v[12:15], a[204:207], a[76:79], v[12:15]\n" \
"ds_read_b64_tr_b16 a[222:223], v221 offset:43520\n" \
"v_and_b32_e64 v234, 16, v222\n" \
"v_mfma_f32_16x16x32_bf16 v[44:47], a[200:203], a[88:91], v[44:47]\n" \
"ds_read_b64_tr_b16 a[228:229], v221 offset:37376\n" \
"v_and_b32_e64 v233, 1, v222\n" \
"v_mfma_f32_16x16x32_bf16 v[44:47], a[204:207], a[92:95], v[44:47]\n" \
"ds_read_b64_tr_b16 a[230:231], v221 offset:39424\n" \
"v_cmp_eq_u32_e64 s[68:69], 1, v233\n" \
"v_mfma_f32_16x16x32_bf16 v[76:79], a[200:203], a[104:107], v[76:79]\n" \
"ds_read_b64_tr_b16 a[224:225], v221 offset:45568\n" \
"s_lshl_b32 s52, %[wid], 2\n" \
"s_nop 0\n" \
"v_mfma_f32_16x16x32_bf16 v[76:79], a[204:207], a[108:111], v[76:79]\n" \
"ds_read_b64_tr_b16 a[226:227], v221 offset:47616\n" \
"v_cndmask_b32_e64 v234, v234, 32, s[68:69]\n" \
"v_mfma_f32_16x16x32_bf16 v[108:111], a[200:203], a[120:123], v[108:111]\n" \
"buffer_load_dwordx4 a[148:151], v208, %[srdz], s50 offen offset:1024 sc1\n" \
"v_mfma_f32_16x16x32_bf16 v[108:111], a[204:207], a[124:127], v[108:111]\n" \
"s_waitcnt vmcnt(63) lgkmcnt(12)\n" \
"s_nop 0\n" \
"v_mfma_f32_16x16x32_bf16 v[20:23], a[232:235], a[64:67], v[20:23]\n" \
"s_cbranch_vccz Lg18_%=\n" \
"s_nop 0\n" \
"buffer_store_dwordx4 v[104:107], v210, %[srdz], s60 offen offset:1024 sc1\n" \
"Lg18_%=:\n" \
"v_mfma_f32_16x16x32_bf16 v[20:23], a[236:239], a[68:71], v[20:23]\n" \
"s_add_u32 s52, s52, s40\n" \
"s_nop 0\n" \
"v_mfma_f32_16x16x32_bf16 v[52:55], a[232:235], a[80:83], v[52:55]\n" \
"buffer_load_dwordx4 v213, %[srdt], s49 offen offset:2048 lds\n" \
"v_mfma_f32_16x16x32_bf16 v[52:55], a[236:239], a[84:87], v[52:55]\n" \
"v_mad_u32_u24 v228, v219, 48, v234\n" \
"s_add_u32 s63, s52, 29184\n" \
"v_mfma_f32_16x16x32_bf16 v[84:87], a[232:235], a[96:99], v[84:87]\n" \
"s_waitcnt vmcnt(63) lgkmcnt(0)\n" \
"s_bitcmp1_b32 0x30, s48\n" \
"s_cbranch_scc1 Lcw8_%=\n" \
"s_waitcnt vmcnt(49)\n" \
"Lcw8_%=:\n" \
"s_barrier\n" \
"s_nop 0\n" \
"v_mfma_f32_16x16x32_bf16 v[84:87], a[236:239], a[100:103], v[84:87]\n" \
"ds_read_b64_tr_b16 a[196:197], v220 offset:49152\n" \
"v_add_u32_e64 v228, s63, v228\n" \
"v_mfma_f32_16x16x32_bf16 v[116:119], a[232:235], a[112:115], v[116:119]\n" \
"ds_read_b64_tr_b16 a[198:199], v220 offset:51200\n" \
"buffer_load_dwordx4 a[152:155], v208, %[srdz], s50 offen offset:2048 sc1\n" \
"v_mfma_f32_16x16x32_bf16 v[116:119], a[236:239], a[116:119], v[116:119]\n" \
"ds_read_b64_tr_b16 a[200:201], v220 offset:53248\n" \
"v_and_b32_e64 v216, 15, v222\n" \
"v_mfma_f32_16x16x32_bf16 v[20:23], a[208:211], a[72:75], v[20:23]\n" \
"ds_read_b64_tr_b16 a[202:203], v220 offset:55296\n" \
"v_lshrrev_b32_e64 v218, 4, v222\n" \
"v_mfma_f32_16x16x32_bf16 v[20:23], a[212:215], a[76:79], v[20:23]\n" \
"ds_read_b64_tr_b16 a[204:205], v220 offset:57344\n" \
"s_cbranch_vccz Lg19_%=\n" \
"s_nop 0\n" \
"buffer_store_dwordx4 v[112:115], v210, %[srdz], s60 offen offset:2048 sc1\n" \
"Lg19_%=:\n" \
"v_mfma_f32_16x16x32_bf16 v[52:55], a[208:211], a[88:91], v[52:55]\n" \
"ds_read_b64_tr_b16 a[206:207], v220 offset:59392\n" \
"v_lshrrev_b32_e64 v230, 3, v216\n" \
"v_mfma_f32_16x16x32_bf16 v[52:55], a[212:215], a[92:95], v[52:55]\n" \
"ds_read_b64_tr_b16 a[232:233], v220 offset:61440\n" \
"v_lshl_or_b32 v230, v230, 1, v233\n" \
"v_mfma_f32_16x16x32_bf16 v[84:87], a[208:211], a[104:107], v[84:87]\n" \
"ds_read_b64_tr_b16 a[234:235], v220 offset:63488\n" \
"s_add_u32 s63, s40, 29184\n" \
"v_mfma_f32_16x16x32_bf16 v[84:87], a[212:215], a[108:111], v[84:87]\n" \
"buffer_load_dwordx4 a[156:159], v208, %[srdz], s50 offen offset:3072 sc1\n" \
"v_mfma_f32_16x16x32_bf16 v[116:119], a[208:211], a[120:123], v[116:119]\n" \
"v_mad_u32_u24 %[vg0], v230, 48, s63\n" \
"s_add_u32 s63, s52, 25600\n" \
"v_mfma_f32_16x16x32_bf16 v[116:119], a[212:215], a[124:127], v[116:119]\n" \
"s_waitcnt vmcnt(20) lgkmcnt(4)\n" \
"s_bitcmp1_b32 0x30, s48\n" \
"s_cbranch_scc1 Lcw9_%=\n" \
"s_waitcnt vmcnt(13)\n" \
"Lcw9_%=:\n" \
"v_mfma_f32_16x16x32_bf16 v[28:31], a[216:219], a[64:67], v[28:31]\n" \
"v_lshlrev_b32_e64 v235, 6, v218\n" \
"v_mfma_f32_16x16x32_bf16 v[28:31], a[228:231], a[68:71], v[28:31]\n" \
"s_cbranch_vccz Lg20_%=\n" \
"s_nop 0\n" \
"buffer_store_dwordx4 v[120:123], v210, %[srdz], s60 offen offset:3072 sc1\n" \
"Lg20_%=:\n" \
"v_mfma_f32_16x16x32_bf16 v[60:63], a[216:219], a[80:83], v[60:63]\n" \
"v_add_u32_e32 v234, s63, v235\n" \
"s_movk_i32 s52, 272\n" \
"v_mfma_f32_16x16x32_bf16 v[60:63], a[228:231], a[84:87], v[60:63]\n" \
"buffer_load_dwordx4 v214, %[srdt], s49 offen offset:3072 lds\n" \
"v_mfma_f32_16x16x32_bf16 v[92:95], a[216:219], a[96:99], v[92:95]\n" \
"v_mad_u32_u24 %[kd0], v216, s52, v234\n" \
"v_mfma_f32_16x16x32_bf16 v[92:95], a[228:231], a[100:103], v[92:95]\n" \
"buffer_load_dwordx4 a[160:163], v209, %[srdz], s50 offen offset:0 sc1\n" \
"v_mfma_f32_16x16x32_bf16 v[124:127], a[216:219], a[112:115], v[124:127]\n" \
"ds_read_b64_tr_b16 a[236:237], v221 offset:49152\n" \
"v_bfe_u32 v234, v222, 1, 2\n" \
"v_mfma_f32_16x16x32_bf16 v[124:127], a[228:231], a[116:119], v[124:127]\n" \
"ds_read_b64_tr_b16 a[238:239], v221 offset:51200\n" \
"v_lshl_add_u32 %[vx0], v234, 4, v235\n" \
"v_mfma_f32_16x16x32_bf16 v[192:195], a[196:199], a[0:3], v[128:131]\n" \
"ds_read_b64_tr_b16 a[208:209], v221 offset:53248\n" \
"s_add_u32 s63, s40, 25600\n" \
"v_mfma_f32_16x16x32_bf16 v[192:195], a[200:203], a[4:7], v[192:195]\n" \
"ds_read_b64_tr_b16 a[210:211], v221 offset:55296\n" \
"v_add_u32_e64 %[vx0], s63, %[vx0]\n" \
"v_mfma_f32_16x16x32_bf16 v[196:199], a[196:199], a[16:19], v[132:135]\n" \
"buffer_load_dwordx4 a[164:167], v209, %[srdz], s50 offen offset:1024 sc1\n" \
"v_mfma_f32_16x16x32_bf16 v[196:199], a[200:203], a[20:23], v[196:199]\n" \
"s_movk_i32 s52, 2112\n" \
"s_nop 0\n" \
"v_mfma_f32_16x16x32_bf16 v[200:203], a[196:199], a[32:35], v[136:139]\n" \
"v_mul_u32_u24_e64 v211, s52, v216\n" \
"v_mfma_f32_16x16x32_bf16 v[200:203], a[200:203], a[36:39], v[200:203]\n" \
"v_add_u32_e32 v211, 16384, v211\n" \
"v_mfma_f32_16x16x32_bf16 v[204:207], a[196:199], a[48:51], v[140:143]\n" \
"ds_read_b64_tr_b16 a[212:213], v221 offset:57344\n" \
"s_sub_u32 s61, s48, 6\n" \
"s_cmp_lt_u32 s61, 2\n" \
"s_cselect_b64 vcc, -1, 0\n" \
"s_nop 0\n" \
"v_mov_b32_e32 v235, 2048\n" \
"v_mfma_f32_16x16x32_bf16 v[204:207], a[200:203], a[52:55], v[204:207]\n" \
"s_waitcnt vmcnt(21) lgkmcnt(5)\n" \
"s_bitcmp1_b32 0x30, s48\n" \
"s_cbranch_scc1 Lcw10_%=\n" \
"s_waitcnt vmcnt(14)\n" \
"Lcw10_%=:\n" \
"v_mfma_f32_16x16x32_bf16 v[192:195], a[204:207], a[8:11], v[192:195]\n" \
"ds_read_b64_tr_b16 a[214:215], v221 offset:59392\n" \
"s_cbranch_vccz Lg21_%=\n" \
"s_nop 0\n" \
"buffer_store_dwordx4 v[0:3], %[oq0], %[srdz], s60 offen offset:0 sc1\n" \
"Lg21_%=:\n" \
"v_mfma_f32_16x16x32_bf16 v[192:195], a[232:235], a[12:15], v[192:195]\n" \
"ds_read_b64_tr_b16 a[216:217], v221 offset:61440\n" \
"v_cmp_eq_u32_e64 s[68:69], 4, v216\n" \
"v_mfma_f32_16x16x32_bf16 v[196:199], a[204:207], a[24:27], v[196:199]\n" \
"ds_read_b64_tr_b16 a[218:219], v221 offset:63488\n" \
"s_lshl_b32 s52, %[wid], 9\n" \
"s_nop 0\n" \
"v_mfma_f32_16x16x32_bf16 v[196:199], a[232:235], a[28:31], v[196:199]\n" \
"buffer_load_dwordx4 a[168:171], v209, %[srdz], s50 offen offset:2048 sc1\n" \
"v_mfma_f32_16x16x32_bf16 v[200:203], a[204:207], a[40:43], v[200:203]\n" \
"v_cndmask_b32_e64 v235, 0, v235, s[68:69]\n" \
"v_mfma_f32_16x16x32_bf16 v[200:203], a[232:235], a[44:47], v[200:203]\n" \
"v_cmp_gt_u32_e64 s[68:69], 4, v216\n" \
"s_add_u32 s52, s52, s40\n" \
"s_nop 0\n" \
"v_mfma_f32_16x16x32_bf16 v[204:207], a[204:207], a[56:59], v[204:207]\n" \
"ds_read_b64_tr_b16 a[228:229], v220 offset:49664\n" \
"v_cndmask_b32_e64 v211, v235, v211, s[68:69]\n" \
"v_mfma_f32_16x16x32_bf16 v[204:207], a[232:235], a[60:63], v[204:207]\n" \
"ds_read_b64_tr_b16 a[230:231], v220 offset:51712\n" \
"v_lshlrev_b32_e64 v235, 4, v218\n" \
"v_mfma_f32_16x16x32_bf16 v[28:31], a[220:223], a[72:75], v[28:31]\n" \
"s_cbranch_vccz Lg22_%=\n" \
"s_nop 0\n" \
"buffer_store_dwordx4 v[8:11], %[oq0], %[srdz], s60 offen offset:1024 sc1\n" \
"Lg22_%=:\n" \
"v_mfma_f32_16x16x32_bf16 v[28:31], a[224:227], a[76:79], v[28:31]\n" \
"v_add3_u32 v211, v211, v235, s52\n" \
"v_mfma_f32_16x16x32_bf16 v[60:63], a[220:223], a[88:91], v[60:63]\n" \
"buffer_load_dwordx4 a[172:175], v209, %[srdz], s50 offen offset:3072 sc1\n" \
"v_mfma_f32_16x16x32_bf16 v[60:63], a[224:227], a[92:95], v[60:63]\n" \
"v_and_b32_e32 v216, 0x2b0, v217\n" \
"v_mfma_f32_16x16x32_bf16 v[92:95], a[220:223], a[104:107], v[92:95]\n" \
"v_lshrrev_b32_e64 v218, 2, v217\n" \
"v_mfma_f32_16x16x32_bf16 v[92:95], a[224:227], a[108:111], v[92:95]\n" \
"v_cmp_u_f32_e64 s[44:45], v192, v196\n" \
"v_and_or_b32 v216, v218, 64, v216\n" \
"s_add_u32 s52, s40, s42\n" \
"s_nop 0\n" \
"v_mfma_f32_16x16x32_bf16 v[124:127], a[220:223], a[120:123], v[124:127]\n" \
"s_add_u32 m0, %[m0b], 16384\n" \
"s_cmp_gt_u32 s48, 2\n" \
"s_cselect_b32 s49, 2, 3\n" \
"s_lshl_b32 s49, s49, 18\n" \
"s_or_b32 s49, s49, s59\n" \
"buffer_load_dwordx4 %[wo0], %[srdt], s49 offen offset:0 lds\n" \
"v_mfma_f32_16x16x32_bf16 v[124:127], a[224:227], a[124:127], v[124:127]\n" \
"v_cmp_u_f32_e64 s[56:57], v200, v204\n" \
"s_or_b64 s[56:57], s[56:57], s[44:45]\n" \
"s_cbranch_scc1 Lsp2_%=\n" \
"Lbk2_%=:\n" \
"s_waitcnt vmcnt(63) lgkmcnt(2)\n" \
"s_nop 0\n" \
"v_mfma_f32_16x16x32_bf16 v[144:147], a[236:239], a[0:3], v[144:147]\n" \
"ds_read_b64_tr_b16 a[220:221], v220 offset:53760\n" \
"v_add_u32_e64 %[kb0], s52, v216\n" \
"v_mfma_f32_16x16x32_bf16 v[144:147], a[208:211], a[4:7], v[144:147]\n" \
"ds_read_b64_tr_b16 a[222:223], v220 offset:55808\n" \
"s_cbranch_vccz Lg23_%=\n" \
"s_nop 0\n" \
"buffer_store_dwordx4 v[16:19], %[oq0], %[srdz], s60 offen offset:2048 sc1\n" \
"Lg23_%=:\n" \
"v_mfma_f32_16x16x32_bf16 v[148:151], a[236:239], a[16:19], v[148:151]\n" \
"ds_read_b64_tr_b16 a[196:197], v220 offset:57856\n" \
"v_xor_b32_e32 %[vvb], 0x100, %[kb0]\n" \
"v_mfma_f32_16x16x32_bf16 v[148:151], a[208:211], a[20:23], v[148:151]\n" \
"ds_read_b64_tr_b16 a[198:199], v220 offset:59904\n" \
"buffer_load_dwordx4 a[176:179], v210, %[srdz], s50 offen offset:0 sc1\n" \
"v_mfma_f32_16x16x32_bf16 v[152:155], a[236:239], a[32:35], v[152:155]\n" \
"ds_read_b64_tr_b16 a[200:201], v220 offset:61952\n" \
"v_and_b32_e32 v235, 0x200, v217\n" \
"v_mfma_f32_16x16x32_bf16 v[152:155], a[208:211], a[36:39], v[152:155]\n" \
"ds_read_b64_tr_b16 a[202:203], v220 offset:64000\n" \
"v_and_b32_e64 v234, 8, v222\n" \
"v_mfma_f32_16x16x32_bf16 v[156:159], a[236:239], a[48:51], v[156:159]\n" \
"ds_read_b64_tr_b16 a[204:205], v221 offset:49664\n" \
"v_lshl_or_b32 v235, v234, 5, v235\n" \
"v_mfma_f32_16x16x32_bf16 v[156:159], a[208:211], a[52:55], v[156:159]\n" \
"ds_read_b64_tr_b16 a[206:207], v221 offset:51712\n" \
"v_and_b32_e64 v234, 16, v222\n" \
"v_mfma_f32_16x16x32_bf16 v[144:147], a[212:215], a[8:11], v[144:147]\n" \
"ds_read_b64_tr_b16 a[224:225], v221 offset:53760\n" \
"v_lshl_or_b32 v235, v234, 1, v235\n" \
"v_mfma_f32_16x16x32_bf16 v[144:147], a[216:219], a[12:15], v[144:147]\n" \
"ds_read_b64_tr_b16 a[226:227], v221 offset:55808\n" \
"v_lshl_or_b32 v235, v233, 6, v235\n" \
"v_mfma_f32_16x16x32_bf16 v[148:151], a[212:215], a[24:27], v[148:151]\n" \
"s_cbranch_vccz Lg24_%=\n" \
"s_nop 0\n" \
"buffer_store_dwordx4 v[24:27], %[oq0], %[srdz], s60 offen offset:3072 sc1\n" \
"Lg24_%=:\n" \
"v_mfma_f32_16x16x32_bf16 v[148:151], a[216:219], a[28:31], v[148:151]\n" \
"v_and_b32_e64 v216, 2, v222\n" \
"v_mfma_f32_16x16x32_bf16 v[152:155], a[212:215], a[40:43], v[152:155]\n" \
"buffer_load_dwordx4 a[180:183], v210, %[srdz], s50 offen offset:1024 sc1\n" \
"v_mfma_f32_16x16x32_bf16 v[152:155], a[216:219], a[44:47], v[152:155]\n" \
"v_lshl_or_b32 v235, v216, 3, v235\n" \
"s_lshr_b32 s52, s42, 1\n" \
"s_nop 0\n" \
"v_mfma_f32_16x16x32_bf16 v[156:159], a[212:215], a[56:59], v[156:159]\n" \
"ds_read_b64_tr_b16 a[232:233], v221 offset:57856\n" \
"v_sub_u32_e32 v234, 0x7ff, %[ta0]\n" \
"v_mfma_f32_16x16x32_bf16 v[156:159], a[216:219], a[60:63], v[156:159]\n" \
"s_waitcnt vmcnt(63) lgkmcnt(5)\n" \
"s_nop 0\n" \
"v_mfma_f32_16x16x32_bf16 v[160:163], a[228:231], a[0:3], v[160:163]\n" \
"ds_read_b64_tr_b16 a[234:235], v221 offset:59904\n" \
"buffer_load_dwordx4 v212, %[srdt], s49 offen offset:1024 lds\n" \
"v_mfma_f32_16x16x32_bf16 v[160:163], a[220:223], a[4:7], v[160:163]\n" \
"ds_read_b64_tr_b16 a[236:237], v221 offset:61952\n" \
"v_lshlrev_b32_e64 v234, 11, v234\n" \
"v_mfma_f32_16x16x32_bf16 v[164:167], a[228:231], a[16:19], v[164:167]\n" \
"ds_read_b64_tr_b16 a[238:239], v221 offset:64000\n" \
"v_add3_u32 %[vd0], v234, v235, s52\n" \
"v_mfma_f32_16x16x32_bf16 v[164:167], a[220:223], a[20:23], v[164:167]\n" \
"s_cbranch_vccz Lg25_%=\n" \
"s_nop 0\n" \
"buffer_store_dwordx4 v[32:35], v208, %[srdz], s60 offen offset:0 sc1\n" \
"Lg25_%=:\n" \
"v_mfma_f32_16x16x32_bf16 v[168:171], a[228:231], a[32:35], v[168:171]\n" \
"v_mfma_f32_16x16x32_bf16 v[168:171], a[220:223], a[36:39], v[168:171]\n" \
"buffer_load_dwordx4 a[184:187], v210, %[srdz], s50 offen offset:2048 sc1\n" \
"v_mfma_f32_16x16x32_bf16 v[172:175], a[228:231], a[48:51], v[172:175]\n" \
"v_mfma_f32_16x16x32_bf16 v[172:175], a[220:223], a[52:55], v[172:175]\n" \
"buffer_load_dwordx4 a[188:191], v210, %[srdz], s50 offen offset:3072 sc1\n" \
"v_mfma_f32_16x16x32_bf16 v[160:163], a[196:199], a[8:11], v[160:163]\n" \
"v_mfma_f32_16x16x32_bf16 v[160:163], a[200:203], a[12:15], v[160:163]\n" \
"v_mfma_f32_16x16x32_bf16 v[164:167], a[196:199], a[24:27], v[164:167]\n" \
"v_mfma_f32_16x16x32_bf16 v[164:167], a[200:203], a[28:31], v[164:167]\n" \
"v_mfma_f32_16x16x32_bf16 v[168:171], a[196:199], a[40:43], v[168:171]\n" \
"s_waitcnt vmcnt(63) lgkmcnt(0)\n" \
"s_bitcmp1_b32 0x30, s48\n" \
"s_cbranch_scc1 Lcw11_%=\n" \
"s_waitcnt vmcnt(60)\n" \
"s_bitcmp1_b32 0xc0, s48\n" \
"s_cbranch_scc1 Lcw11_%=\n" \
"s_waitcnt vmcnt(55)\n" \
"Lcw11_%=:\n" \
"s_barrier\n" \
"s_nop 0\n" \
"v_mfma_f32_16x16x32_bf16 v[168:171], a[200:203], a[44:47], v[168:171]\n" \
"ds_read_b64_tr_b16 a[208:209], v215 offset:49152\n" \
"s_cbranch_vccz Lg26_%=\n" \
"s_nop 0\n" \
"buffer_store_dwordx4 v[40:43], v208, %[srdz], s60 offen offset:1024 sc1\n" \
"Lg26_%=:\n" \
"v_mfma_f32_16x16x32_bf16 v[172:175], a[196:199], a[56:59], v[172:175]\n" \
"ds_read_b64_tr_b16 a[210:211], v215 offset:51200\n" \
"v_mfma_f32_16x16x32_bf16 v[172:175], a[200:203], a[60:63], v[172:175]\n" \
"s_waitcnt vmcnt(63) lgkmcnt(2)\n" \
"s_nop 0\n" \
"v_mfma_f32_16x16x32_bf16 v[176:179], a[204:207], a[0:3], v[176:179]\n" \
"ds_read_b64_tr_b16 a[212:213], v215 offset:53248\n" \
"global_load_dword v237, %[vct], %[pct]\n" \
"v_mfma_f32_16x16x32_bf16 v[176:179], a[224:227], a[4:7], v[176:179]\n" \
"ds_read_b64_tr_b16 a[214:215], v215 offset:55296\n" \
"v_mfma_f32_16x16x32_bf16 v[180:183], a[204:207], a[16:19], v[180:183]\n" \
"ds_read_b64_tr_b16 a[216:217], v215 offset:57344\n" \
"buffer_load_dwordx4 v213, %[srdt], s49 offen offset:2048 lds\n" \
"v_mfma_f32_16x16x32_bf16 v[180:183], a[224:227], a[20:23], v[180:183]\n" \
"ds_read_b64_tr_b16 a[218:219], v215 offset:59392\n" \
"v_mfma_f32_16x16x32_bf16 v[184:187], a[204:207], a[32:35], v[184:187]\n" \
"ds_read_b64_tr_b16 a[228:229], v215 offset:61440\n" \
"v_mfma_f32_16x16x32_bf16 v[184:187], a[224:227], a[36:39], v[184:187]\n" \
"ds_read_b64_tr_b16 a[230:231], v215 offset:63488\n" \
"s_cbranch_vccz Lg27_%=\n" \
"s_nop 0\n" \
"buffer_store_dwordx4 v[48:51], v208, %[srdz], s60 offen offset:2048 sc1\n" \
"Lg27_%=:\n" \
"v_mfma_f32_16x16x32_bf16 v[188:191], a[204:207], a[48:51], v[188:191]\n" \
"ds_read_b64_tr_b16 a[220:221], v227 offset:49152\n" \
"v_mfma_f32_16x16x32_bf16 v[188:191], a[224:227], a[52:55], v[188:191]\n" \
"ds_read_b64_tr_b16 a[222:223], v227 offset:51200\n" \
"v_mfma_f32_16x16x32_bf16 v[176:179], a[232:235], a[8:11], v[176:179]\n" \
"ds_read_b64_tr_b16 a[196:197], v227 offset:53248\n" \
"v_mfma_f32_16x16x32_bf16 v[176:179], a[236:239], a[12:15], v[176:179]\n" \
"ds_read_b64_tr_b16 a[198:199], v227 offset:55296\n" \
"s_cmp_gt_u32 s48, 3\n" \
"s_cselect_b32 s50, 3, 4\n" \
"s_mul_i32 s50, s50, 152064\n" \
"buffer_load_dwordx4 a[64:67], %[oq0], %[srdz], s50 offen offset:0 sc1\n" \
"v_mfma_f32_16x16x32_bf16 v[180:183], a[232:235], a[24:27], v[180:183]\n" \
"v_mfma_f32_16x16x32_bf16 v[180:183], a[236:239], a[28:31], v[180:183]\n" \
"buffer_load_dwordx4 v214, %[srdt], s49 offen offset:3072 lds\n" \
"v_mfma_f32_16x16x32_bf16 v[184:187], a[232:235], a[40:43], v[184:187]\n" \
"v_mfma_f32_16x16x32_bf16 v[184:187], a[236:239], a[44:47], v[184:187]\n" \
"v_mfma_f32_16x16x32_bf16 v[188:191], a[232:235], a[56:59], v[188:191]\n" \
"ds_read_b64_tr_b16 a[200:201], v227 offset:57344\n" \
"s_cbranch_vccz Lg28_%=\n" \
"s_nop 0\n" \
"buffer_store_dwordx4 v[56:59], v208, %[srdz], s60 offen offset:3072 sc1\n" \
"Lg28_%=:\n" \
"v_mfma_f32_16x16x32_bf16 v[188:191], a[236:239], a[60:63], v[188:191]\n" \
"s_waitcnt vmcnt(63) lgkmcnt(5)\n" \
"s_nop 0\n" \
"v_mfma_f32_16x16x32_bf16 v[4:7], a[208:211], a[0:3], v[4:7]\n" \
"ds_read_b64_tr_b16 a[202:203], v227 offset:59392\n" \
"buffer_load_dwordx4 a[68:71], %[oq0], %[srdz], s50 offen offset:1024 sc1\n" \
"v_mfma_f32_16x16x32_bf16 v[4:7], a[212:215], a[4:7], v[4:7]\n" \
"ds_read_b64_tr_b16 a[204:205], v227 offset:61440\n" \
"v_mfma_f32_16x16x32_bf16 v[36:39], a[208:211], a[16:19], v[36:39]\n" \
"ds_read_b64_tr_b16 a[206:207], v227 offset:63488\n" \
"v_mfma_f32_16x16x32_bf16 v[36:39], a[212:215], a[20:23], v[36:39]\n" \
"v_mfma_f32_16x16x32_bf16 v[68:71], a[208:211], a[32:35], v[68:71]\n" \
"buffer_load_dwordx4 a[72:75], %[oq0], %[srdz], s50 offen offset:2048 sc1\n" \
"v_mfma_f32_16x16x32_bf16 v[68:71], a[212:215], a[36:39], v[68:71]\n" \
"v_mfma_f32_16x16x32_bf16 v[100:103], a[208:211], a[48:51], v[100:103]\n" \
"ds_read_b64_tr_b16 a[224:225], v215 offset:49664\n" \
"v_mfma_f32_16x16x32_bf16 v[100:103], a[212:215], a[52:55], v[100:103]\n" \
"ds_read_b64_tr_b16 a[226:227], v215 offset:51712\n" \
"s_cbranch_vccz Lg29_%=\n" \
"s_nop 0\n" \
"buffer_store_dwordx4 v[64:67], v209, %[srdz], s60 offen offset:0 sc1\n" \
"Lg29_%=:\n" \
"v_mfma_f32_16x16x32_bf16 v[4:7], a[216:219], a[8:11], v[4:7]\n" \
"ds_read_b64_tr_b16 a[232:233], v215 offset:53760\n" \
"v_mfma_f32_16x16x32_bf16 v[4:7], a[228:231], a[12:15], v[4:7]\n" \
"ds_read_b64_tr_b16 a[234:235], v215 offset:55808\n" \
"v_mfma_f32_16x16x32_bf16 v[36:39], a[216:219], a[24:27], v[36:39]\n" \
"v_mfma_f32_16x16x32_bf16 v[36:39], a[228:231], a[28:31], v[36:39]\n" \
"buffer_load_dwordx4 a[76:79], %[oq0], %[srdz], s50 offen offset:3072 sc1\n" \
"v_mfma_f32_16x16x32_bf16 v[68:71], a[216:219], a[40:43], v[68:71]\n" \
"v_mfma_f32_16x16x32_bf16 v[68:71], a[228:231], a[44:47], v[68:71]\n" \
"v_mfma_f32_16x16x32_bf16 v[100:103], a[216:219], a[56:59], v[100:103]\n" \
"ds_read_b64_tr_b16 a[236:237], v215 offset:57856\n" \
"v_mfma_f32_16x16x32_bf16 v[100:103], a[228:231], a[60:63], v[100:103]\n" \
"s_waitcnt vmcnt(63) lgkmcnt(5)\n" \
"s_nop 0\n" \
"v_mfma_f32_16x16x32_bf16 v[12:15], a[220:223], a[0:3], v[12:15]\n" \
"ds_read_b64_tr_b16 a[238:239], v215 offset:59904\n" \
"s_cbranch_vccz Lg30_%=\n" \
"s_nop 0\n" \
"buffer_store_dwordx4 v[72:75], v209, %[srdz], s60 offen offset:1024 sc1\n" \
"Lg30_%=:\n" \
"v_mfma_f32_16x16x32_bf16 v[12:15], a[196:199], a[4:7], v[12:15]\n" \
"ds_read_b64_tr_b16 a[208:209], v215 offset:61952\n" \
"v_mfma_f32_16x16x32_bf16 v[44:47], a[220:223], a[16:19], v[44:47]\n" \
"ds_read_b64_tr_b16 a[210:211], v215 offset:64000\n" \
"v_mfma_f32_16x16x32_bf16 v[44:47], a[196:199], a[20:23], v[44:47]\n" \
"buffer_load_dwordx4 a[80:83], v208, %[srdz], s50 offen offset:0 sc1\n" \
"v_mfma_f32_16x16x32_bf16 v[76:79], a[220:223], a[32:35], v[76:79]\n" \
"v_mfma_f32_16x16x32_bf16 v[76:79], a[196:199], a[36:39], v[76:79]\n" \
"s_add_u32 m0, %[m0b], 32768\n" \
"s_cmp_gt_u32 s48, 3\n" \
"s_cselect_b32 s49, 3, 4\n" \
"s_lshl_b32 s49, s49, 18\n" \
"s_or_b32 s49, s49, s58\n" \
"buffer_load_dwordx4 %[wo0], %[srdt], s49 offen offset:0 lds\n" \
"v_mfma_f32_16x16x32_bf16 v[108:111], a[220:223], a[48:51], v[108:111]\n" \
"ds_read_b64_tr_b16 a[212:213], v227 offset:49664\n" \
"v_mfma_f32_16x16x32_bf16 v[108:111], a[196:199], a[52:55], v[108:111]\n" \
"ds_read_b64_tr_b16 a[214:215], v227 offset:51712\n" \
"v_mfma_f32_16x16x32_bf16 v[12:15], a[200:203], a[8:11], v[12:15]\n" \
"ds_read_b64_tr_b16 a[216:217], v227 offset:57856\n" \
"v_mfma_f32_16x16x32_bf16 v[12:15], a[204:207], a[12:15], v[12:15]\n" \
"ds_read_b64_tr_b16 a[218:219], v227 offset:59904\n" \
"s_cbranch_vccz Lg31_%=\n" \
"s_nop 0\n" \
"buffer_store_dwordx4 v[80:83], v209, %[srdz], s60 offen offset:2048 sc1\n" \
"Lg31_%=:\n" \
"v_mfma_f32_16x16x32_bf16 v[44:47], a[200:203], a[24:27], v[44:47]\n" \
"ds_read_b64_tr_b16 a[228:229], v227 offset:53760\n" \
"v_mfma_f32_16x16x32_bf16 v[44:47], a[204:207], a[28:31], v[44:47]\n" \
"ds_read_b64_tr_b16 a[230:231], v227 offset:55808\n" \
"v_mfma_f32_16x16x32_bf16 v[76:79], a[200:203], a[40:43], v[76:79]\n" \
"ds_read_b64_tr_b16 a[220:221], v227 offset:61952\n" \
"buffer_load_dwordx4 a[84:87], v208, %[srdz], s50 offen offset:1024 sc1\n" \
"v_mfma_f32_16x16x32_bf16 v[76:79], a[204:207], a[44:47], v[76:79]\n" \
"ds_read_b64_tr_b16 a[222:223], v227 offset:64000\n" \
"v_mfma_f32_16x16x32_bf16 v[108:111], a[200:203], a[56:59], v[108:111]\n" \
"v_mfma_f32_16x16x32_bf16 v[108:111], a[204:207], a[60:63], v[108:111]\n" \
"s_waitcnt vmcnt(63) lgkmcnt(12)\n" \
"s_nop 0\n" \
"v_mfma_f32_16x16x32_bf16 v[20:23], a[224:227], a[0:3], v[20:23]\n" \
"v_mfma_f32_16x16x32_bf16 v[20:23], a[232:235], a[4:7], v[20:23]\n" \
"v_mfma_f32_16x16x32_bf16 v[52:55], a[224:227], a[16:19], v[52:55]\n" \
"s_cbranch_vccz Lg32_%=\n" \
"s_nop 0\n" \
"buffer_store_dwordx4 v[88:91], v209, %[srdz], s60 offen offset:3072 sc1\n" \
"Lg32_%=:\n" \
"v_mfma_f32_16x16x32_bf16 v[52:55], a[232:235], a[20:23], v[52:55]\n" \
"v_mfma_f32_16x16x32_bf16 v[84:87], a[224:227], a[32:35], v[84:87]\n" \
"s_waitcnt vmcnt(32) lgkmcnt(0)\n" \
"s_nop 0\n" \
"s_bitcmp1_b32 0xc0, s48\n" \
"s_cbranch_scc1 Lcw12_%=\n" \
"s_waitcnt vmcnt(20)\n" \
"Lcw12_%=:\n" \
"s_barrier\n" \
"s_nop 0\n" \
"v_mfma_f32_16x16x32_bf16 v[84:87], a[232:235], a[36:39], v[84:87]\n" \
"ds_read_b64_tr_b16 a[196:197], v215 offset:0\n" \
"buffer_load_dwordx4 a[88:91], v208, %[srdz], s50 offen offset:2048 sc1\n" \
"v_mfma_f32_16x16x32_bf16 v[116:119], a[224:227], a[48:51], v[116:119]\n" \
"ds_read_b64_tr_b16 a[198:199], v215 offset:2048\n" \
"v_mfma_f32_16x16x32_bf16 v[116:119], a[232:235], a[52:55], v[116:119]\n" \
"ds_read_b64_tr_b16 a[200:201], v215 offset:4096\n" \
"buffer_load_dwordx4 v212, %[srdt], s49 offen offset:1024 lds\n" \
"v_mfma_f32_16x16x32_bf16 v[20:23], a[236:239], a[8:11], v[20:23]\n" \
"ds_read_b64_tr_b16 a[202:203], v215 offset:6144\n" \
"v_mfma_f32_16x16x32_bf16 v[20:23], a[208:211], a[12:15], v[20:23]\n" \
"ds_read_b64_tr_b16 a[204:205], v215 offset:8192\n" \
"v_mfma_f32_16x16x32_bf16 v[52:55], a[236:239], a[24:27], v[52:55]\n" \
"ds_read_b64_tr_b16 a[206:207], v215 offset:10240\n" \
"v_mfma_f32_16x16x32_bf16 v[52:55], a[208:211], a[28:31], v[52:55]\n" \
"ds_read_b64_tr_b16 a[224:225], v215 offset:12288\n" \
"s_cbranch_vccz Lg33_%=\n" \
"s_nop 0\n" \
"buffer_store_dwordx4 v[96:99], v210, %[srdz], s60 offen offset:0 sc1\n" \
"Lg33_%=:\n" \
"v_mfma_f32_16x16x32_bf16 v[84:87], a[236:239], a[40:43], v[84:87]\n" \
"ds_read_b64_tr_b16 a[226:227], v215 offset:14336\n" \
"v_mfma_f32_16x16x32_bf16 v[84:87], a[208:211], a[44:47], v[84:87]\n" \
"buffer_load_dwordx4 a[92:95], v208, %[srdz], s50 offen offset:3072 sc1\n" \
"v_mfma_f32_16x16x32_bf16 v[116:119], a[236:239], a[56:59], v[116:119]\n" \
"v_mfma_f32_16x16x32_bf16 v[116:119], a[208:211], a[60:63], v[116:119]\n" \
"s_waitcnt vmcnt(25) lgkmcnt(4)\n" \
"s_nop 0\n" \
"s_bitcmp1_b32 0xc0, s48\n" \
"s_cbranch_scc1 Lcw13_%=\n" \
"s_waitcnt vmcnt(16)\n" \
"Lcw13_%=:\n" \
"v_mfma_f32_16x16x32_bf16 v[28:31], a[212:215], a[0:3], v[28:31]\n" \
"buffer_load_dwordx4 a[96:99], v209, %[srdz], s50 offen offset:0 sc1\n" \
"v_mfma_f32_16x16x32_bf16 v[28:31], a[228:231], a[4:7], v[28:31]\n" \
"v_mfma_f32_16x16x32_bf16 v[60:63], a[212:215], a[16:19], v[60:63]\n" \
"buffer_load_dwordx4 v213, %[srdt], s49 offen offset:2048 lds\n" \
"v_mfma_f32_16x16x32_bf16 v[60:63], a[228:231], a[20:23], v[60:63]\n" \
"v_mfma_f32_16x16x32_bf16 v[92:95], a[212:215], a[32:35], v[92:95]\n" \
"s_cbranch_vccz Lg34_%=\n" \
"s_nop 0\n" \
"buffer_store_dwordx4 v[104:107], v210, %[srdz], s60 offen offset:1024 sc1\n" \
"Lg34_%=:\n" \
"v_mfma_f32_16x16x32_bf16 v[92:95], a[228:231], a[36:39], v[92:95]\n" \
"v_mfma_f32_16x16x32_bf16 v[124:127], a[212:215], a[48:51], v[124:127]\n" \
"ds_read_b64_tr_b16 a[232:233], v227 offset:0\n" \
"v_mfma_f32_16x16x32_bf16 v[124:127], a[228:231], a[52:55], v[124:127]\n" \
"ds_read_b64_tr_b16 a[234:235], v227 offset:2048\n" \
"v_mfma_f32_16x16x32_bf16 v[128:131], a[196:199], a[128:131], v[192:195]\n" \
"ds_read_b64_tr_b16 a[236:237], v227 offset:4096\n" \
"v_mfma_f32_16x16x32_bf16 v[128:131], a[200:203], a[132:135], v[128:131]\n" \
"ds_read_b64_tr_b16 a[238:239], v227 offset:6144\n" \
"buffer_load_dwordx4 a[100:103], v209, %[srdz], s50 offen offset:1024 sc1\n" \
"v_mfma_f32_16x16x32_bf16 v[132:135], a[196:199], a[144:147], v[196:199]\n" \
"v_mfma_f32_16x16x32_bf16 v[132:135], a[200:203], a[148:151], v[132:135]\n" \
"v_mfma_f32_16x16x32_bf16 v[136:139], a[196:199], a[160:163], v[200:203]\n" \
"v_mfma_f32_16x16x32_bf16 v[136:139], a[200:203], a[164:167], v[136:139]\n" \
"s_cbranch_vccz Lg35_%=\n" \
"s_nop 0\n" \
"buffer_store_dwordx4 v[112:115], v210, %[srdz], s60 offen offset:2048 sc1\n" \
"Lg35_%=:\n" \
"v_mfma_f32_16x16x32_bf16 v[140:143], a[196:199], a[176:179], v[204:207]\n" \
"ds_read_b64_tr_b16 a[208:209], v227 offset:8192\n" \
"v_mfma_f32_16x16x32_bf16 v[140:143], a[200:203], a[180:183], v[140:143]\n" \
"s_waitcnt vmcnt(26) lgkmcnt(5)\n" \
"s_nop 0\n" \
"s_bitcmp1_b32 0xc0, s48\n" \
"s_cbranch_scc1 Lcw14_%=\n" \
"s_waitcnt vmcnt(16)\n" \
"Lcw14_%=:\n" \
"v_mfma_f32_16x16x32_bf16 v[128:131], a[204:207], a[136:139], v[128:131]\n" \
"ds_read_b64_tr_b16 a[210:211], v227 offset:10240\n" \
"buffer_load_dwordx4 a[104:107], v209, %[srdz], s50 offen offset:2048 sc1\n" \
"v_mfma_f32_16x16x32_bf16 v[128:131], a[224:227], a[140:143], v[128:131]\n" \
"ds_read_b64_tr_b16 a[212:213], v227 offset:12288\n" \
"v_mfma_f32_16x16x32_bf16 v[132:135], a[204:207], a[152:155], v[132:135]\n" \
"ds_read_b64_tr_b16 a[214:215], v227 offset:14336\n" \
"v_mfma_f32_16x16x32_bf16 v[132:135], a[224:227], a[156:159], v[132:135]\n" \
"buffer_load_dwordx4 v214, %[srdt], s49 offen offset:3072 lds\n" \
"v_mfma_f32_16x16x32_bf16 v[136:139], a[204:207], a[168:171], v[136:139]\n" \
"v_mfma_f32_16x16x32_bf16 v[136:139], a[224:227], a[172:175], v[136:139]\n" \
"v_mfma_f32_16x16x32_bf16 v[140:143], a[204:207], a[184:187], v[140:143]\n" \
".if . - adaln_body != 49152\n" \
".error \"size model\"\n" \
".endif\n" \
"Lk11_%=:\n" \
"s_cbranch_execz adaln_kr\n" \
"s_nop 0\n" \
"ds_read_b64_tr_b16 a[228:229], v215 offset:512\n" \
"s_cbranch_vccz Lg36_%=\n" \
"s_nop 0\n" \
"buffer_store_dwordx4 v[120:123], v210, %[srdz], s60 offen offset:3072 sc1\n" \
"Lg36_%=:\n" \
"v_mfma_f32_16x16x32_bf16 v[140:143], a[224:227], a[188:191], v[140:143]\n" \
"ds_read_b64_tr_b16 a[230:231], v215 offset:2560\n" \
"v_mfma_f32_16x16x32_bf16 v[28:31], a[216:219], a[8:11], v[28:31]\n" \
"buffer_load_dwordx4 a[108:111], v209, %[srdz], s50 offen offset:3072 sc1\n" \
"v_mfma_f32_16x16x32_bf16 v[28:31], a[220:223], a[12:15], v[28:31]\n" \
"v_mfma_f32_16x16x32_bf16 v[60:63], a[216:219], a[24:27], v[60:63]\n" \
"v_mfma_f32_16x16x32_bf16 v[60:63], a[220:223], a[28:31], v[60:63]\n" \
"v_mfma_f32_16x16x32_bf16 v[92:95], a[216:219], a[40:43], v[92:95]\n" \
"buffer_load_dwordx4 a[112:115], v210, %[srdz], s50 offen offset:0 sc1\n" \
"v_mfma_f32_16x16x32_bf16 v[92:95], a[220:223], a[44:47], v[92:95]\n" \
"v_cmp_u_f32_e64 s[44:45], v128, v132\n" \
"v_mfma_f32_16x16x32_bf16 v[124:127], a[216:219], a[56:59], v[124:127]\n" \
"v_mfma_f32_16x16x32_bf16 v[124:127], a[220:223], a[60:63], v[124:127]\n" \
"v_cmp_u_f32_e64 s[56:57], v136, v140\n" \
"s_or_b64 s[56:57], s[56:57], s[44:45]\n" \
"s_cbranch_scc1 Lsp3_%=\n" \
"Lbk3_%=:\n" \
"s_waitcnt vmcnt(63) lgkmcnt(2)\n" \
"s_nop 0\n" \
"v_mfma_f32_16x16x32_bf16 v[144:147], a[232:235], a[128:131], v[144:147]\n" \
"ds_read_b64_tr_b16 a[216:217], v215 offset:4608\n" \
"v_mfma_f32_16x16x32_bf16 v[144:147], a[236:239], a[132:135], v[144:147]\n" \
"ds_read_b64_tr_b16 a[218:219], v215 offset:6656\n" \
"v_mfma_f32_16x16x32_bf16 v[148:151], a[232:235], a[144:147], v[148:151]\n" \
"ds_read_b64_tr_b16 a[196:197], v215 offset:8704\n" \
"v_mfma_f32_16x16x32_bf16 v[148:151], a[236:239], a[148:151], v[148:151]\n" \
"ds_read_b64_tr_b16 a[198:199], v215 offset:10752\n" \
"v_mfma_f32_16x16x32_bf16 v[152:155], a[232:235], a[160:163], v[152:155]\n" \
"ds_read_b64_tr_b16 a[200:201], v215 offset:12800\n" \
"v_mfma_f32_16x16x32_bf16 v[152:155], a[236:239], a[164:167], v[152:155]\n" \
"ds_read_b64_tr_b16 a[202:203], v215 offset:14848\n" \
"v_mfma_f32_16x16x32_bf16 v[156:159], a[232:235], a[176:179], v[156:159]\n" \
"ds_read_b64_tr_b16 a[204:205], v227 offset:512\n" \
"buffer_load_dwordx4 a[116:119], v210, %[srdz], s50 offen offset:1024 sc1\n" \
"v_mfma_f32_16x16x32_bf16 v[156:159], a[236:239], a[180:183], v[156:159]\n" \
"ds_read_b64_tr_b16 a[206:207], v227 offset:2560\n" \
"v_mfma_f32_16x16x32_bf16 v[144:147], a[208:211], a[136:139], v[144:147]\n" \
"ds_read_b64_tr_b16 a[220:221], v227 offset:4608\n" \
"v_mfma_f32_16x16x32_bf16 v[144:147], a[212:215], a[140:143], v[144:147]\n" \
"ds_read_b64_tr_b16 a[222:223], v227 offset:6656\n" \
"v_mfma_f32_16x16x32_bf16 v[148:151], a[208:211], a[152:155], v[148:151]\n" \
"s_add_u32 m0, %[m0b], 98304\n" \
"s_cmp_gt_u32 s48, 4\n" \
"s_cselect_b32 s49, 4, 5\n" \
"s_lshl_b32 s49, s49, 18\n" \
"s_or_b32 s49, s49, s58\n" \
"buffer_load_dwordx4 %[wo0], %[srdt], s49 offen offset:0 lds\n" \
"v_mfma_f32_16x16x32_bf16 v[148:151], a[212:215], a[156:159], v[148:151]\n" \
"v_mfma_f32_16x16x32_bf16 v[152:155], a[208:211], a[168:171], v[152:155]\n" \
"buffer_load_dwordx4 a[120:123], v210, %[srdz], s50 offen offset:2048 sc1\n" \
"v_mfma_f32_16x16x32_bf16 v[152:155], a[212:215], a[172:175], v[152:155]\n" \
"v_mfma_f32_16x16x32_bf16 v[156:159], a[208:211], a[184:187], v[156:159]\n" \
"ds_read_b64_tr_b16 a[224:225], v227 offset:8704\n" \
"v_mfma_f32_16x16x32_bf16 v[156:159], a[212:215], a[188:191], v[156:159]\n" \
"s_waitcnt vmcnt(63) lgkmcnt(5)\n" \
"s_nop 0\n" \
"v_mfma_f32_16x16x32_bf16 v[160:163], a[228:231], a[128:131], v[160:163]\n" \
"ds_read_b64_tr_b16 a[226:227], v227 offset:10752\n" \
"v_mfma_f32_16x16x32_bf16 v[160:163], a[216:219], a[132:135], v[160:163]\n" \
"ds_read_b64_tr_b16 a[232:233], v227 offset:12800\n" \
"v_mfma_f32_16x16x32_bf16 v[164:167], a[228:231], a[144:147], v[164:167]\n" \
"ds_read_b64_tr_b16 a[234:235], v227 offset:14848\n" \
"v_mfma_f32_16x16x32_bf16 v[164:167], a[216:219], a[148:151], v[164:167]\n" \
"buffer_load_dwordx4 a[124:127], v210, %[srdz], s50 offen offset:3072 sc1\n" \
"v_mfma_f32_16x16x32_bf16 v[168:171], a[228:231], a[160:163], v[168:171]\n" \
"v_mfma_f32_16x16x32_bf16 v[168:171], a[216:219], a[164:167], v[168:171]\n" \
"v_mfma_f32_16x16x32_bf16 v[172:175], a[228:231], a[176:179], v[172:175]\n" \
"v_mfma_f32_16x16x32_bf16 v[172:175], a[216:219], a[180:183], v[172:175]\n" \
"v_mfma_f32_16x16x32_bf16 v[160:163], a[196:199], a[136:139], v[160:163]\n" \
"v_mfma_f32_16x16x32_bf16 v[160:163], a[200:203], a[140:143], v[160:163]\n" \
"buffer_load_dwordx4 v212, %[srdt], s49 offen offset:1024 lds\n" \
"v_mfma_f32_16x16x32_bf16 v[164:167], a[196:199], a[152:155], v[164:167]\n" \
"v_mfma_f32_16x16x32_bf16 v[164:167], a[200:203], a[156:159], v[164:167]\n" \
"v_mfma_f32_16x16x32_bf16 v[168:171], a[196:199], a[168:171], v[168:171]\n" \
"s_waitcnt vmcnt(30) lgkmcnt(0)\n" \
"s_nop 0\n" \
"s_bitcmp1_b32 0xc0, s48\n" \
"s_cbranch_scc1 Lcw15_%=\n" \
"s_waitcnt vmcnt(21)\n" \
"Lcw15_%=:\n" \
"s_barrier\n" \
"s_nop 0\n" \
"v_mfma_f32_16x16x32_bf16 v[168:171], a[200:203], a[172:175], v[168:171]\n" \
"ds_read_b64_tr_b16 a[236:237], v215 offset:16384\n" \
"v_mfma_f32_16x16x32_bf16 v[172:175], a[196:199], a[184:187], v[172:175]\n" \
"ds_read_b64_tr_b16 a[238:239], v215 offset:18432\n" \
"v_mfma_f32_16x16x32_bf16 v[172:175], a[200:203], a[188:191], v[172:175]\n" \
"s_waitcnt vmcnt(63) lgkmcnt(2)\n" \
"s_nop 0\n" \
"v_mfma_f32_16x16x32_bf16 v[176:179], a[204:207], a[128:131], v[176:179]\n" \
"ds_read_b64_tr_b16 a[208:209], v215 offset:20480\n" \
"s_cmp_gt_u32 s48, 4\n" \
"s_cselect_b32 s50, 4, 5\n" \
"s_mul_i32 s50, s50, 152064\n" \
"buffer_load_dwordx4 v[0:3], %[oq0], %[srdz], s50 offen offset:0 sc1\n" \
"v_mfma_f32_16x16x32_bf16 v[176:179], a[220:223], a[132:135], v[176:179]\n" \
"ds_read_b64_tr_b16 a[210:211], v215 offset:22528\n" \
"v_mfma_f32_16x16x32_bf16 v[180:183], a[204:207], a[144:147], v[180:183]\n" \
"ds_read_b64_tr_b16 a[212:213], v215 offset:24576\n" \
"v_mfma_f32_16x16x32_bf16 v[180:183], a[220:223], a[148:151], v[180:183]\n" \
"ds_read_b64_tr_b16 a[214:215], v215 offset:26624\n" \
"v_mfma_f32_16x16x32_bf16 v[184:187], a[204:207], a[160:163], v[184:187]\n" \
"ds_read_b64_tr_b16 a[228:229], v215 offset:28672\n" \
"buffer_load_dwordx4 v[8:11], %[oq0], %[srdz], s50 offen offset:1024 sc1\n" \
"v_mfma_f32_16x16x32_bf16 v[184:187], a[220:223], a[164:167], v[184:187]\n" \
"ds_read_b64_tr_b16 a[230:231], v215 offset:30720\n" \
"v_mfma_f32_16x16x32_bf16 v[188:191], a[204:207], a[176:179], v[188:191]\n" \
"ds_read_b64_tr_b16 a[216:217], v227 offset:16384\n" \
"v_mfma_f32_16x16x32_bf16 v[188:191], a[220:223], a[180:183], v[188:191]\n" \
"ds_read_b64_tr_b16 a[218:219], v227 offset:18432\n" \
"v_mfma_f32_16x16x32_bf16 v[176:179], a[224:227], a[136:139], v[176:179]\n" \
"ds_read_b64_tr_b16 a[196:197], v227 offset:20480\n" \
"buffer_load_dwordx4 v[16:19], %[oq0], %[srdz], s50 offen offset:2048 sc1\n" \
"v_mfma_f32_16x16x32_bf16 v[176:179], a[232:235], a[140:143], v[176:179]\n" \
"ds_read_b64_tr_b16 a[198:199], v227 offset:22528\n" \
"v_mfma_f32_16x16x32_bf16 v[180:183], a[224:227], a[152:155], v[180:183]\n" \
"buffer_load_dwordx4 v213, %[srdt], s49 offen offset:2048 lds\n" \
"v_mfma_f32_16x16x32_bf16 v[180:183], a[232:235], a[156:159], v[180:183]\n" \
"v_mfma_f32_16x16x32_bf16 v[184:187], a[224:227], a[168:171], v[184:187]\n" \
"buffer_load_dwordx4 v[24:27], %[oq0], %[srdz], s50 offen offset:3072 sc1\n" \
"v_mfma_f32_16x16x32_bf16 v[184:187], a[232:235], a[172:175], v[184:187]\n" \
"v_mfma_f32_16x16x32_bf16 v[188:191], a[224:227], a[184:187], v[188:191]\n" \
"ds_read_b64_tr_b16 a[200:201], v227 offset:24576\n" \
"v_mfma_f32_16x16x32_bf16 v[188:191], a[232:235], a[188:191], v[188:191]\n" \
"s_waitcnt vmcnt(63) lgkmcnt(5)\n" \
"s_nop 0\n" \
"v_mfma_f32_16x16x32_bf16 v[4:7], a[236:239], a[128:131], v[4:7]\n" \
"ds_read_b64_tr_b16 a[202:203], v227 offset:26624\n" \
"buffer_load_dwordx4 v[32:35], v208, %[srdz], s50 offen offset:0 sc1\n" \
"v_mfma_f32_16x16x32_bf16 v[4:7], a[208:211], a[132:135], v[4:7]\n" \
"ds_read_b64_tr_b16 a[204:205], v227 offset:28672\n" \
"v_mfma_f32_16x16x32_bf16 v[36:39], a[236:239], a[144:147], v[36:39]\n" \
"ds_read_b64_tr_b16 a[206:207], v227 offset:30720\n" \
"v_mfma_f32_16x16x32_bf16 v[36:39], a[208:211], a[148:151], v[36:39]\n" \
"buffer_load_dwordx4 v[40:43], v208, %[srdz], s50 offen offset:1024 sc1\n" \
"v_mfma_f32_16x16x32_bf16 v[68:71], a[236:239], a[160:163], v[68:71]\n" \
"v_mfma_f32_16x16x32_bf16 v[68:71], a[208:211], a[164:167], v[68:71]\n" \
"buffer_load_dwordx4 v[48:51], v208, %[srdz], s50 offen offset:2048 sc1\n" \
"v_mfma_f32_16x16x32_bf16 v[100:103], a[236:239], a[176:179], v[100:103]\n" \
"ds_read_b64_tr_b16 a[220:221], v215 offset:16896\n" \
"v_mfma_f32_16x16x32_bf16 v[100:103], a[208:211], a[180:183], v[100:103]\n" \
"ds_read_b64_tr_b16 a[222:223], v215 offset:18944\n" \
"v_mfma_f32_16x16x32_bf16 v[4:7], a[212:215], a[136:139], v[4:7]\n" \
"ds_read_b64_tr_b16 a[224:225], v215 offset:20992\n" \
"v_mfma_f32_16x16x32_bf16 v[4:7], a[228:231], a[140:143], v[4:7]\n" \
"ds_read_b64_tr_b16 a[226:227], v215 offset:23040\n" \
"v_mfma_f32_16x16x32_bf16 v[36:39], a[212:215], a[152:155], v[36:39]\n" \
"buffer_load_dwordx4 v214, %[srdt], s49 offen offset:3072 lds\n" \
"v_mfma_f32_16x16x32_bf16 v[36:39], a[228:231], a[156:159], v[36:39]\n" \
"v_mfma_f32_16x16x32_bf16 v[68:71], a[212:215], a[168:171], v[68:71]\n" \
"buffer_load_dwordx4 v[56:59], v208, %[srdz], s50 offen offset:3072 sc1\n" \
"v_mfma_f32_16x16x32_bf16 v[68:71], a[228:231], a[172:175], v[68:71]\n" \
"v_mfma_f32_16x16x32_bf16 v[100:103], a[212:215], a[184:187], v[100:103]\n" \
"ds_read_b64_tr_b16 a[232:233], v215 offset:25088\n" \
"v_mfma_f32_16x16x32_bf16 v[100:103], a[228:231], a[188:191], v[100:103]\n" \
"s_waitcnt vmcnt(63) lgkmcnt(5)\n" \
"s_nop 0\n" \
"v_mfma_f32_16x16x32_bf16 v[12:15], a[216:219], a[128:131], v[12:15]\n" \
"ds_read_b64_tr_b16 a[234:235], v215 offset:27136\n" \
"buffer_load_dwordx4 v[64:67], v209, %[srdz], s50 offen offset:0 sc1\n" \
"v_mfma_f32_16x16x32_bf16 v[12:15], a[196:199], a[132:135], v[12:15]\n" \
"ds_read_b64_tr_b16 a[236:237], v215 offset:29184\n" \
"v_mfma_f32_16x16x32_bf16 v[44:47], a[216:219], a[144:147], v[44:47]\n" \
"ds_read_b64_tr_b16 a[238:239], v215 offset:31232\n" \
"v_mfma_f32_16x16x32_bf16 v[44:47], a[196:199], a[148:151], v[44:47]\n" \
"buffer_load_dwordx4 v[72:75], v209, %[srdz], s50 offen offset:1024 sc1\n" \
"v_mfma_f32_16x16x32_bf16 v[76:79], a[216:219], a[160:163], v[76:79]\n" \
"v_mfma_f32_16x16x32_bf16 v[76:79], a[196:199], a[164:167], v[76:79]\n" \
"buffer_load_dwordx4 v[80:83], v209, %[srdz], s50 offen offset:2048 sc1\n" \
"v_mfma_f32_16x16x32_bf16 v[108:111], a[216:219], a[176:179], v[108:111]\n" \
"ds_read_b64_tr_b16 a[208:209], v227 offset:16896\n" \
"v_mfma_f32_16x16x32_bf16 v[108:111], a[196:199], a[180:183], v[108:111]\n" \
"ds_read_b64_tr_b16 a[210:211], v227 offset:18944\n" \
"v_mfma_f32_16x16x32_bf16 v[12:15], a[200:203], a[136:139], v[12:15]\n" \
"ds_read_b64_tr_b16 a[212:213], v227 offset:25088\n" \
"v_mfma_f32_16x16x32_bf16 v[12:15], a[204:207], a[140:143], v[12:15]\n" \
"ds_read_b64_tr_b16 a[214:215], v227 offset:27136\n" \
"v_mfma_f32_16x16x32_bf16 v[44:47], a[200:203], a[152:155], v[44:47]\n" \
"ds_read_b64_tr_b16 a[228:229], v227 offset:20992\n" \
"v_mfma_f32_16x16x32_bf16 v[44:47], a[204:207], a[156:159], v[44:47]\n" \
"ds_read_b64_tr_b16 a[230:231], v227 offset:23040\n" \
"buffer_load_dwordx4 v[88:91], v209, %[srdz], s50 offen offset:3072 sc1\n" \
"v_mfma_f32_16x16x32_bf16 v[76:79], a[200:203], a[168:171], v[76:79]\n" \
"ds_read_b64_tr_b16 a[216:217], v227 offset:29184\n" \
"v_mfma_f32_16x16x32_bf16 v[76:79], a[204:207], a[172:175], v[76:79]\n" \
"ds_read_b64_tr_b16 a[218:219], v227 offset:31232\n" \
"v_mfma_f32_16x16x32_bf16 v[108:111], a[200:203], a[184:187], v[108:111]\n" \
"buffer_load_dwordx4 v[96:99], v210, %[srdz], s50 offen offset:0 sc1\n" \
"v_mfma_f32_16x16x32_bf16 v[108:111], a[204:207], a[188:191], v[108:111]\n" \
"s_waitcnt vmcnt(63) lgkmcnt(12)\n" \
"s_nop 0\n" \
"v_mfma_f32_16x16x32_bf16 v[20:23], a[220:223], a[128:131], v[20:23]\n" \
"s_add_u32 m0, %[m0b], 114688\n" \
"s_cmp_gt_u32 s48, 5\n" \
"s_cselect_b32 s49, 5, 6\n" \
"s_lshl_b32 s49, s49, 18\n" \
"s_or_b32 s49, s49, s58\n" \
"buffer_load_dwordx4 %[wo0], %[srdt], s49 offen offset:0 lds\n" \
"v_mfma_f32_16x16x32_bf16 v[20:23], a[224:227], a[132:135], v[20:23]\n" \
"v_mfma_f32_16x16x32_bf16 v[52:55], a[220:223], a[144:147], v[52:55]\n" \
"buffer_load_dwordx4 v[104:107], v210, %[srdz], s50 offen offset:1024 sc1\n" \
"v_mfma_f32_16x16x32_bf16 v[52:55], a[224:227], a[148:151], v[52:55]\n" \
"v_mfma_f32_16x16x32_bf16 v[84:87], a[220:223], a[160:163], v[84:87]\n" \
"s_waitcnt vmcnt(24) lgkmcnt(0)\n" \
"s_barrier\n" \
"v_mfma_f32_16x16x32_bf16 v[84:87], a[224:227], a[164:167], v[84:87]\n" \
"ds_read_b64_tr_b16 a[196:197], v215 offset:32768\n" \
"v_mfma_f32_16x16x32_bf16 v[116:119], a[220:223], a[176:179], v[116:119]\n" \
"ds_read_b64_tr_b16 a[198:199], v215 offset:34816\n" \
"v_mfma_f32_16x16x32_bf16 v[116:119], a[224:227], a[180:183], v[116:119]\n" \
"ds_read_b64_tr_b16 a[200:201], v215 offset:36864\n" \
"buffer_load_dwordx4 v[112:115], v210, %[srdz], s50 offen offset:2048 sc1\n" \
"v_mfma_f32_16x16x32_bf16 v[20:23], a[232:235], a[136:139], v[20:23]\n" \
"ds_read_b64_tr_b16 a[202:203], v215 offset:38912\n" \
"v_mfma_f32_16x16x32_bf16 v[20:23], a[236:239], a[140:143], v[20:23]\n" \
"ds_read_b64_tr_b16 a[204:205], v215 offset:40960\n" \
"v_mfma_f32_16x16x32_bf16 v[52:55], a[232:235], a[152:155], v[52:55]\n" \
"ds_read_b64_tr_b16 a[206:207], v215 offset:43008\n" \
"v_mfma_f32_16x16x32_bf16 v[52:55], a[236:239], a[156:159], v[52:55]\n" \
"ds_read_b64_tr_b16 a[220:221], v215 offset:45056\n" \
"buffer_load_dwordx4 v[120:123], v210, %[srdz], s50 offen offset:3072 sc1\n" \
"v_mfma_f32_16x16x32_bf16 v[84:87], a[232:235], a[168:171], v[84:87]\n" \
"ds_read_b64_tr_b16 a[222:223], v215 offset:47104\n" \
"v_readlane_b32 s54, %[thi], 16\n" \
"v_mfma_f32_16x16x32_bf16 v[84:87], a[236:239], a[172:175], v[84:87]\n" \
"v_mfma_f32_16x16x32_bf16 v[116:119], a[232:235], a[184:187], v[116:119]\n" \
"v_mfma_f32_16x16x32_bf16 v[116:119], a[236:239], a[188:191], v[116:119]\n" \
"s_waitcnt vmcnt(23) lgkmcnt(4)\n" \
"s_nop 0\n" \
"v_mfma_f32_16x16x32_bf16 v[28:31], a[208:211], a[128:131], v[28:31]\n" \
"buffer_load_dwordx4 v212, %[srdt], s49 offen offset:1024 lds\n" \
"v_mfma_f32_16x16x32_bf16 v[28:31], a[228:231], a[132:135], v[28:31]\n" \
"v_mfma_f32_16x16x32_bf16 v[60:63], a[208:211], a[144:147], v[60:63]\n" \
"s_cmp_gt_u32 s48, 5\n" \
"s_cselect_b32 s50, 5, 6\n" \
"s_mul_i32 s50, s50, 152064\n" \
"buffer_load_dwordx4 a[0:3], %[oq0], %[srdz], s50 offen offset:0 sc1\n" \
"v_mfma_f32_16x16x32_bf16 v[60:63], a[228:231], a[148:151], v[60:63]\n" \
"v_mfma_f32_16x16x32_bf16 v[92:95], a[208:211], a[160:163], v[92:95]\n" \
"buffer_load_dword v232, v226, %[srdz], s54 offen sc1\n" \
"v_mfma_f32_16x16x32_bf16 v[92:95], a[228:231], a[164:167], v[92:95]\n" \
"v_mfma_f32_16x16x32_bf16 v[124:127], a[208:211], a[176:179], v[124:127]\n" \
"ds_read_b64_tr_b16 a[224:225], v227 offset:32768\n" \
"buffer_load_dwordx4 a[4:7], %[oq0], %[srdz], s50 offen offset:1024 sc1\n" \
"v_mfma_f32_16x16x32_bf16 v[124:127], a[228:231], a[180:183], v[124:127]\n" \
"ds_read_b64_tr_b16 a[226:227], v227 offset:34816\n" \
"v_mfma_f32_16x16x32_bf16 v[192:195], a[196:199], a[64:67], v[128:131]\n" \
"ds_read_b64_tr_b16 a[232:233], v227 offset:36864\n" \
"v_mfma_f32_16x16x32_bf16 v[192:195], a[200:203], a[68:71], v[192:195]\n" \
"ds_read_b64_tr_b16 a[234:235], v227 offset:38912\n" \
"v_mfma_f32_16x16x32_bf16 v[196:199], a[196:199], a[80:83], v[132:135]\n" \
"buffer_load_dwordx4 a[8:11], %[oq0], %[srdz], s50 offen offset:2048 sc1\n" \
"v_mfma_f32_16x16x32_bf16 v[196:199], a[200:203], a[84:87], v[196:199]\n" \
"v_mfma_f32_16x16x32_bf16 v[200:203], a[196:199], a[96:99], v[136:139]\n" \
"buffer_load_dwordx4 a[12:15], %[oq0], %[srdz], s50 offen offset:3072 sc1\n" \
"v_mfma_f32_16x16x32_bf16 v[200:203], a[200:203], a[100:103], v[200:203]\n" \
"v_mfma_f32_16x16x32_bf16 v[204:207], a[196:199], a[112:115], v[140:143]\n" \
"ds_read_b64_tr_b16 a[236:237], v227 offset:40960\n" \
"v_mfma_f32_16x16x32_bf16 v[204:207], a[200:203], a[116:119], v[204:207]\n" \
"s_waitcnt vmcnt(26) lgkmcnt(5)\n" \
"s_nop 0\n" \
"v_mfma_f32_16x16x32_bf16 v[192:195], a[204:207], a[72:75], v[192:195]\n" \
"ds_read_b64_tr_b16 a[238:239], v227 offset:43008\n" \
"v_mfma_f32_16x16x32_bf16 v[192:195], a[220:223], a[76:79], v[192:195]\n" \
"ds_read_b64_tr_b16 a[208:209], v227 offset:45056\n" \
"buffer_load_dwordx4 v213, %[srdt], s49 offen offset:2048 lds\n" \
"v_mfma_f32_16x16x32_bf16 v[196:199], a[204:207], a[88:91], v[196:199]\n" \
"ds_read_b64_tr_b16 a[210:211], v227 offset:47104\n" \
"v_mfma_f32_16x16x32_bf16 v[196:199], a[220:223], a[92:95], v[196:199]\n" \
"buffer_load_dwordx4 a[16:19], v208, %[srdz], s50 offen offset:0 sc1\n" \
"v_mfma_f32_16x16x32_bf16 v[200:203], a[204:207], a[104:107], v[200:203]\n" \
"v_mfma_f32_16x16x32_bf16 v[200:203], a[220:223], a[108:111], v[200:203]\n" \
"buffer_load_dwordx4 a[20:23], v208, %[srdz], s50 offen offset:1024 sc1\n" \
"v_mfma_f32_16x16x32_bf16 v[204:207], a[204:207], a[120:123], v[204:207]\n" \
"ds_read_b64_tr_b16 a[228:229], v215 offset:33280\n" \
"v_mfma_f32_16x16x32_bf16 v[204:207], a[220:223], a[124:127], v[204:207]\n" \
"ds_read_b64_tr_b16 a[230:231], v215 offset:35328\n" \
"v_mfma_f32_16x16x32_bf16 v[28:31], a[212:215], a[136:139], v[28:31]\n" \
"buffer_load_dwordx4 a[24:27], v208, %[srdz], s50 offen offset:2048 sc1\n" \
"v_mfma_f32_16x16x32_bf16 v[28:31], a[216:219], a[140:143], v[28:31]\n" \
"v_mfma_f32_16x16x32_bf16 v[60:63], a[212:215], a[152:155], v[60:63]\n" \
"buffer_load_dwordx4 a[28:31], v208, %[srdz], s50 offen offset:3072 sc1\n" \
"v_mfma_f32_16x16x32_bf16 v[60:63], a[216:219], a[156:159], v[60:63]\n" \
"v_mfma_f32_16x16x32_bf16 v[92:95], a[212:215], a[168:171], v[92:95]\n" \
"v_mfma_f32_16x16x32_bf16 v[92:95], a[216:219], a[172:175], v[92:95]\n" \
"v_cmp_u_f32_e64 s[44:45], v192, v196\n" \
"buffer_load_dwordx4 v214, %[srdt], s49 offen offset:3072 lds\n" \
"v_mfma_f32_16x16x32_bf16 v[124:127], a[212:215], a[184:187], v[124:127]\n" \
"v_mfma_f32_16x16x32_bf16 v[124:127], a[216:219], a[188:191], v[124:127]\n" \
"v_cmp_u_f32_e64 s[56:57], v200, v204\n" \
"s_or_b64 s[56:57], s[56:57], s[44:45]\n" \
"s_cbranch_scc1 Lsp4_%=\n" \
"Lbk4_%=:\n" \
"s_waitcnt vmcnt(63) lgkmcnt(2)\n" \
"s_nop 0\n" \
"v_mfma_f32_16x16x32_bf16 v[144:147], a[224:227], a[64:67], v[144:147]\n" \
"ds_read_b64_tr_b16 a[212:213], v215 offset:37376\n" \
"v_mfma_f32_16x16x32_bf16 v[144:147], a[232:235], a[68:71], v[144:147]\n" \
"ds_read_b64_tr_b16 a[214:215], v215 offset:39424\n" \
"buffer_load_dwordx4 a[32:35], v209, %[srdz], s50 offen offset:0 sc1\n" \
"v_mfma_f32_16x16x32_bf16 v[148:151], a[224:227], a[80:83], v[148:151]\n" \
"ds_read_b64_tr_b16 a[196:197], v215 offset:41472\n" \
"v_mfma_f32_16x16x32_bf16 v[148:151], a[232:235], a[84:87], v[148:151]\n" \
"ds_read_b64_tr_b16 a[198:199], v215 offset:43520\n" \
"v_mfma_f32_16x16x32_bf16 v[152:155], a[224:227], a[96:99], v[152:155]\n" \
"ds_read_b64_tr_b16 a[200:201], v215 offset:45568\n" \
"v_mfma_f32_16x16x32_bf16 v[152:155], a[232:235], a[100:103], v[152:155]\n" \
"ds_read_b64_tr_b16 a[202:203], v215 offset:47616\n" \
"buffer_load_dwordx4 a[36:39], v209, %[srdz], s50 offen offset:1024 sc1\n" \
"v_mfma_f32_16x16x32_bf16 v[156:159], a[224:227], a[112:115], v[156:159]\n" \
"ds_read_b64_tr_b16 a[204:205], v227 offset:33280\n" \
"v_mfma_f32_16x16x32_bf16 v[156:159], a[232:235], a[116:119], v[156:159]\n" \
"ds_read_b64_tr_b16 a[206:207], v227 offset:35328\n" \
"v_mfma_f32_16x16x32_bf16 v[144:147], a[236:239], a[72:75], v[144:147]\n" \
"ds_read_b64_tr_b16 a[216:217], v227 offset:41472\n" \
"v_mfma_f32_16x16x32_bf16 v[144:147], a[208:211], a[76:79], v[144:147]\n" \
"ds_read_b64_tr_b16 a[218:219], v227 offset:43520\n" \
"buffer_load_dwordx4 a[40:43], v209, %[srdz], s50 offen offset:2048 sc1\n" \
"v_mfma_f32_16x16x32_bf16 v[148:151], a[236:239], a[88:91], v[148:151]\n" \
"ds_read_b64_tr_b16 a[220:221], v227 offset:37376\n" \
"v_mfma_f32_16x16x32_bf16 v[148:151], a[208:211], a[92:95], v[148:151]\n" \
"ds_read_b64_tr_b16 a[222:223], v227 offset:39424\n" \
"v_mfma_f32_16x16x32_bf16 v[152:155], a[236:239], a[104:107], v[152:155]\n" \
"ds_read_b64_tr_b16 a[224:225], v227 offset:45568\n" \
"v_mfma_f32_16x16x32_bf16 v[152:155], a[208:211], a[108:111], v[152:155]\n" \
"ds_read_b64_tr_b16 a[226:227], v227 offset:47616\n" \
"buffer_load_dwordx4 a[44:47], v209, %[srdz], s50 offen offset:3072 sc1\n" \
"v_mfma_f32_16x16x32_bf16 v[156:159], a[236:239], a[120:123], v[156:159]\n" \
"v_mfma_f32_16x16x32_bf16 v[156:159], a[208:211], a[124:127], v[156:159]\n" \
"s_waitcnt vmcnt(63) lgkmcnt(12)\n" \
"s_nop 0\n" \
"v_mfma_f32_16x16x32_bf16 v[160:163], a[228:231], a[64:67], v[160:163]\n" \
"buffer_load_dwordx4 a[48:51], v210, %[srdz], s50 offen offset:0 sc1\n" \
"v_mfma_f32_16x16x32_bf16 v[160:163], a[212:215], a[68:71], v[160:163]\n" \
"v_mfma_f32_16x16x32_bf16 v[164:167], a[228:231], a[80:83], v[164:167]\n" \
"buffer_load_dwordx4 a[52:55], v210, %[srdz], s50 offen offset:1024 sc1\n" \
"v_mfma_f32_16x16x32_bf16 v[164:167], a[212:215], a[84:87], v[164:167]\n" \
"v_mfma_f32_16x16x32_bf16 v[168:171], a[228:231], a[96:99], v[168:171]\n" \
"s_waitcnt vmcnt(28) lgkmcnt(0)\n" \
"s_barrier\n" \
"v_mfma_f32_16x16x32_bf16 v[168:171], a[212:215], a[100:103], v[168:171]\n" \
"ds_read_b64_tr_b16 a[232:233], v220 offset:32768\n" \
"v_mfma_f32_16x16x32_bf16 v[172:175], a[228:231], a[112:115], v[172:175]\n" \
"ds_read_b64_tr_b16 a[234:235], v220 offset:34816\n" \
"v_mfma_f32_16x16x32_bf16 v[172:175], a[212:215], a[116:119], v[172:175]\n" \
"ds_read_b64_tr_b16 a[236:237], v220 offset:36864\n" \
"v_mfma_f32_16x16x32_bf16 v[160:163], a[196:199], a[72:75], v[160:163]\n" \
"ds_read_b64_tr_b16 a[238:239], v220 offset:38912\n" \
"v_mfma_f32_16x16x32_bf16 v[160:163], a[200:203], a[76:79], v[160:163]\n" \
"ds_read_b64_tr_b16 a[208:209], v220 offset:40960\n" \
"s_add_u32 m0, %[m0b], 49152\n" \
"s_cmp_gt_u32 s48, 6\n" \
"s_cselect_b32 s49, 6, 7\n" \
"s_lshl_b32 s49, s49, 18\n" \
"s_or_b32 s49, s49, s58\n" \
"buffer_load_dwordx4 %[wo0], %[srdt], s49 offen offset:0 lds\n" \
"v_mfma_f32_16x16x32_bf16 v[164:167], a[196:199], a[88:91], v[164:167]\n" \
"ds_read_b64_tr_b16 a[210:211], v220 offset:43008\n" \
"v_mfma_f32_16x16x32_bf16 v[164:167], a[200:203], a[92:95], v[164:167]\n" \
"ds_read_b64_tr_b16 a[228:229], v220 offset:45056\n" \
"buffer_load_dwordx4 a[56:59], v210, %[srdz], s50 offen offset:2048 sc1\n" \
"v_mfma_f32_16x16x32_bf16 v[168:171], a[196:199], a[104:107], v[168:171]\n" \
"ds_read_b64_tr_b16 a[230:231], v220 offset:47104\n" \
"v_mfma_f32_16x16x32_bf16 v[168:171], a[200:203], a[108:111], v[168:171]\n" \
"buffer_load_dwordx4 a[60:63], v210, %[srdz], s50 offen offset:3072 sc1\n" \
"v_mfma_f32_16x16x32_bf16 v[172:175], a[196:199], a[120:123], v[172:175]\n" \
"v_mfma_f32_16x16x32_bf16 v[172:175], a[200:203], a[124:127], v[172:175]\n" \
"s_waitcnt vmcnt(23) lgkmcnt(4)\n" \
"s_nop 0\n" \
"v_mfma_f32_16x16x32_bf16 v[176:179], a[204:207], a[64:67], v[176:179]\n" \
"v_mfma_f32_16x16x32_bf16 v[176:179], a[220:223], a[68:71], v[176:179]\n" \
"s_cmp_gt_u32 s48, 6\n" \
"s_cselect_b32 s50, 6, 7\n" \
"s_mul_i32 s50, s50, 152064\n" \
"buffer_load_dwordx4 a[128:131], %[oq0], %[srdz], s50 offen offset:0 sc1\n" \
"v_mfma_f32_16x16x32_bf16 v[180:183], a[204:207], a[80:83], v[180:183]\n" \
"v_mfma_f32_16x16x32_bf16 v[180:183], a[220:223], a[84:87], v[180:183]\n" \
"v_mfma_f32_16x16x32_bf16 v[184:187], a[204:207], a[96:99], v[184:187]\n" \
"v_mfma_f32_16x16x32_bf16 v[184:187], a[220:223], a[100:103], v[184:187]\n" \
"buffer_load_dwordx4 v212, %[srdt], s49 offen offset:1024 lds\n" \
"v_mfma_f32_16x16x32_bf16 v[188:191], a[204:207], a[112:115], v[188:191]\n" \
"ds_read_b64_tr_b16 a[212:213], v221 offset:32768\n" \
"v_mfma_f32_16x16x32_bf16 v[188:191], a[220:223], a[116:119], v[188:191]\n" \
"ds_read_b64_tr_b16 a[214:215], v221 offset:34816\n" \
"v_mfma_f32_16x16x32_bf16 v[128:131], a[232:235], v[0:3], v[192:195]\n" \
"ds_read_b64_tr_b16 a[196:197], v221 offset:36864\n" \
"buffer_load_dwordx4 a[132:135], %[oq0], %[srdz], s50 offen offset:1024 sc1\n" \
"v_mfma_f32_16x16x32_bf16 v[128:131], a[236:239], v[8:11], v[128:131]\n" \
"ds_read_b64_tr_b16 a[198:199], v221 offset:38912\n" \
"v_mfma_f32_16x16x32_bf16 v[132:135], a[232:235], v[32:35], v[196:199]\n" \
"buffer_load_dwordx4 a[136:139], %[oq0], %[srdz], s50 offen offset:2048 sc1\n" \
"v_mfma_f32_16x16x32_bf16 v[132:135], a[236:239], v[40:43], v[132:135]\n" \
"v_mfma_f32_16x16x32_bf16 v[136:139], a[232:235], v[64:67], v[200:203]\n" \
"v_mfma_f32_16x16x32_bf16 v[136:139], a[236:239], v[72:75], v[136:139]\n" \
"buffer_load_dwordx4 a[140:143], %[oq0], %[srdz], s50 offen offset:3072 sc1\n" \
"v_mfma_f32_16x16x32_bf16 v[140:143], a[232:235], v[96:99], v[204:207]\n" \
"ds_read_b64_tr_b16 a[200:201], v221 offset:40960\n" \
"v_mfma_f32_16x16x32_bf16 v[140:143], a[236:239], v[104:107], v[140:143]\n" \
"s_waitcnt vmcnt(26) lgkmcnt(5)\n" \
"s_nop 0\n" \
"v_mfma_f32_16x16x32_bf16 v[128:131], a[208:211], v[16:19], v[128:131]\n" \
"ds_read_b64_tr_b16 a[202:203], v221 offset:43008\n" \
"v_mfma_f32_16x16x32_bf16 v[128:131], a[228:231], v[24:27], v[128:131]\n" \
"ds_read_b64_tr_b16 a[204:205], v221 offset:45056\n" \
"v_mfma_f32_16x16x32_bf16 v[132:135], a[208:211], v[48:51], v[132:135]\n" \
"ds_read_b64_tr_b16 a[206:207], v221 offset:47104\n" \
"v_mfma_f32_16x16x32_bf16 v[132:135], a[228:231], v[56:59], v[132:135]\n" \
"buffer_load_dwordx4 a[144:147], v208, %[srdz], s50 offen offset:0 sc1\n" \
"v_mfma_f32_16x16x32_bf16 v[136:139], a[208:211], v[80:83], v[136:139]\n" \
"v_mfma_f32_16x16x32_bf16 v[136:139], a[228:231], v[88:91], v[136:139]\n" \
"buffer_load_dwordx4 v213, %[srdt], s49 offen offset:2048 lds\n" \
"v_mfma_f32_16x16x32_bf16 v[140:143], a[208:211], v[112:115], v[140:143]\n" \
"ds_read_b64_tr_b16 a[220:221], v220 offset:33280\n" \
"v_mfma_f32_16x16x32_bf16 v[140:143], a[228:231], v[120:123], v[140:143]\n" \
"ds_read_b64_tr_b16 a[222:223], v220 offset:35328\n" \
"v_mfma_f32_16x16x32_bf16 v[176:179], a[216:219], a[72:75], v[176:179]\n" \
"buffer_load_dwordx4 a[148:151], v208, %[srdz], s50 offen offset:1024 sc1\n" \
"v_mfma_f32_16x16x32_bf16 v[176:179], a[224:227], a[76:79], v[176:179]\n" \
"v_mfma_f32_16x16x32_bf16 v[180:183], a[216:219], a[88:91], v[180:183]\n" \
"buffer_load_dwordx4 a[152:155], v208, %[srdz], s50 offen offset:2048 sc1\n" \
"v_mfma_f32_16x16x32_bf16 v[180:183], a[224:227], a[92:95], v[180:183]\n" \
"v_mfma_f32_16x16x32_bf16 v[184:187], a[216:219], a[104:107], v[184:187]\n" \
"v_mfma_f32_16x16x32_bf16 v[184:187], a[224:227], a[108:111], v[184:187]\n" \
"v_cmp_u_f32_e64 s[44:45], v128, v132\n" \
"buffer_load_dwordx4 a[156:159], v208, %[srdz], s50 offen offset:3072 sc1\n" \
"v_mfma_f32_16x16x32_bf16 v[188:191], a[216:219], a[120:123], v[188:191]\n" \
"v_mfma_f32_16x16x32_bf16 v[188:191], a[224:227], a[124:127], v[188:191]\n" \
"v_cmp_u_f32_e64 s[56:57], v136, v140\n" \
"s_or_b64 s[56:57], s[56:57], s[44:45]\n" \
"s_cbranch_scc1 Lsp5_%=\n" \
"Lbk5_%=:\n" \
"s_waitcnt vmcnt(63) lgkmcnt(2)\n" \
"s_nop 0\n" \
"v_mfma_f32_16x16x32_bf16 v[144:147], a[212:215], v[0:3], v[144:147]\n" \
"ds_read_b64_tr_b16 a[216:217], v220 offset:37376\n" \
"v_mfma_f32_16x16x32_bf16 v[144:147], a[196:199], v[8:11], v[144:147]\n" \
"ds_read_b64_tr_b16 a[218:219], v220 offset:39424\n" \
"v_mfma_f32_16x16x32_bf16 v[148:151], a[212:215], v[32:35], v[148:151]\n" \
"ds_read_b64_tr_b16 a[208:209], v220 offset:41472\n" \
"v_mfma_f32_16x16x32_bf16 v[148:151], a[196:199], v[40:43], v[148:151]\n" \
"ds_read_b64_tr_b16 a[210:211], v220 offset:43520\n" \
"v_mfma_f32_16x16x32_bf16 v[152:155], a[212:215], v[64:67], v[152:155]\n" \
"ds_read_b64_tr_b16 a[224:225], v220 offset:45568\n" \
"buffer_load_dwordx4 a[160:163], v209, %[srdz], s50 offen offset:0 sc1\n" \
"v_mfma_f32_16x16x32_bf16 v[152:155], a[196:199], v[72:75], v[152:155]\n" \
"ds_read_b64_tr_b16 a[226:227], v220 offset:47616\n" \
"v_mfma_f32_16x16x32_bf16 v[156:159], a[212:215], v[96:99], v[156:159]\n" \
"ds_read_b64_tr_b16 a[228:229], v221 offset:33280\n" \
"buffer_load_dwordx4 v214, %[srdt], s49 offen offset:3072 lds\n" \
"v_mfma_f32_16x16x32_bf16 v[156:159], a[196:199], v[104:107], v[156:159]\n" \
"ds_read_b64_tr_b16 a[230:231], v221 offset:35328\n" \
"v_mfma_f32_16x16x32_bf16 v[144:147], a[200:203], v[16:19], v[144:147]\n" \
"ds_read_b64_tr_b16 a[232:233], v221 offset:41472\n" \
"v_readlane_b32 s64, %[tlo], 10\n" \
"v_readlane_b32 s65, %[thi], 10\n" \
"buffer_load_dwordx4 a[164:167], v209, %[srdz], s50 offen offset:1024 sc1\n" \
"v_mfma_f32_16x16x32_bf16 v[144:147], a[204:207], v[24:27], v[144:147]\n" \
"ds_read_b64_tr_b16 a[234:235], v221 offset:43520\n" \
"v_mfma_f32_16x16x32_bf16 v[148:151], a[200:203], v[48:51], v[148:151]\n" \
"ds_read_b64_tr_b16 a[236:237], v221 offset:37376\n" \
"v_mfma_f32_16x16x32_bf16 v[148:151], a[204:207], v[56:59], v[148:151]\n" \
"ds_read_b64_tr_b16 a[238:239], v221 offset:39424\n" \
"v_mfma_f32_16x16x32_bf16 v[152:155], a[200:203], v[80:83], v[152:155]\n" \
"ds_read_b64_tr_b16 a[212:213], v221 offset:45568\n" \
"buffer_load_dwordx4 a[168:171], v209, %[srdz], s50 offen offset:2048 sc1\n" \
"v_mfma_f32_16x16x32_bf16 v[152:155], a[204:207], v[88:91], v[152:155]\n" \
"ds_read_b64_tr_b16 a[214:215], v221 offset:47616\n" \
"v_mfma_f32_16x16x32_bf16 v[156:159], a[200:203], v[112:115], v[156:159]\n" \
"buffer_load_dwordx4 a[172:175], v209, %[srdz], s50 offen offset:3072 sc1\n" \
"v_mfma_f32_16x16x32_bf16 v[156:159], a[204:207], v[120:123], v[156:159]\n" \
"s_waitcnt vmcnt(63) lgkmcnt(12)\n" \
"s_nop 0\n" \
"v_mfma_f32_16x16x32_bf16 v[160:163], a[220:223], v[0:3], v[160:163]\n" \
"v_mfma_f32_16x16x32_bf16 v[160:163], a[216:219], v[8:11], v[160:163]\n" \
"buffer_load_dwordx4 a[176:179], v210, %[srdz], s50 offen offset:0 sc1\n" \
"v_mfma_f32_16x16x32_bf16 v[164:167], a[220:223], v[32:35], v[164:167]\n" \
"v_readlane_b32 s63, %[thi], 33\n" \
"v_lshlrev_b32_e64 v234, 3, v224\n" \
"v_mfma_f32_16x16x32_bf16 v[164:167], a[216:219], v[40:43], v[164:167]\n" \
"v_mfma_f32_16x16x32_bf16 v[168:171], a[220:223], v[64:67], v[168:171]\n" \
"v_and_b32_e32 v234, 0xc00, v234\n" \
"v_lshlrev_b32_e64 v235, 2, v224\n" \
"s_waitcnt vmcnt(25) lgkmcnt(0)\n" \
"s_barrier\n" \
"v_mfma_f32_16x16x32_bf16 v[168:171], a[216:219], v[72:75], v[168:171]\n" \
"ds_read_b64_tr_b16 a[196:197], v220 offset:49152\n" \
"v_mfma_f32_16x16x32_bf16 v[172:175], a[220:223], v[96:99], v[172:175]\n" \
"ds_read_b64_tr_b16 a[198:199], v220 offset:51200\n" \
"v_and_b32_e32 v235, 0x7000, v235\n" \
"v_bfe_u32 v230, v224, 9, 1\n" \
"v_mfma_f32_16x16x32_bf16 v[172:175], a[216:219], v[104:107], v[172:175]\n" \
"ds_read_b64_tr_b16 a[200:201], v220 offset:53248\n" \
"v_mfma_f32_16x16x32_bf16 v[160:163], a[208:211], v[16:19], v[160:163]\n" \
"ds_read_b64_tr_b16 a[202:203], v220 offset:55296\n" \
"v_lshl_or_b32 v235, v230, 2, v235\n" \
"v_readlane_b32 s52, %[thi], 35\n" \
"buffer_load_dwordx4 a[180:183], v210, %[srdz], s50 offen offset:1024 sc1\n" \
"v_mfma_f32_16x16x32_bf16 v[160:163], a[224:227], v[24:27], v[160:163]\n" \
"ds_read_b64_tr_b16 a[204:205], v220 offset:57344\n" \
"v_or_b32_e64 v234, v234, v235\n" \
"v_mfma_f32_16x16x32_bf16 v[164:167], a[208:211], v[48:51], v[164:167]\n" \
"ds_read_b64_tr_b16 a[206:207], v220 offset:59392\n" \
"v_mfma_f32_16x16x32_bf16 v[164:167], a[224:227], v[56:59], v[164:167]\n" \
"ds_read_b64_tr_b16 a[220:221], v220 offset:61440\n" \
"v_mfma_f32_16x16x32_bf16 v[168:171], a[208:211], v[80:83], v[168:171]\n" \
"ds_read_b64_tr_b16 a[222:223], v220 offset:63488\n" \
"v_mfma_f32_16x16x32_bf16 v[168:171], a[224:227], v[88:91], v[168:171]\n" \
"buffer_load_dword v235, v234, %[srdz], s63 offen sc1\n" \
"v_mfma_f32_16x16x32_bf16 v[172:175], a[208:211], v[112:115], v[172:175]\n" \
"v_mfma_f32_16x16x32_bf16 v[172:175], a[224:227], v[120:123], v[172:175]\n" \
"s_waitcnt vmcnt(63) lgkmcnt(4)\n" \
"s_nop 0\n" \
"v_mfma_f32_16x16x32_bf16 v[176:179], a[228:231], v[0:3], v[176:179]\n" \
"buffer_load_dwordx4 a[184:187], v210, %[srdz], s50 offen offset:2048 sc1\n" \
"v_mfma_f32_16x16x32_bf16 v[176:179], a[236:239], v[8:11], v[176:179]\n" \
"v_mfma_f32_16x16x32_bf16 v[180:183], a[228:231], v[32:35], v[180:183]\n" \
"buffer_load_dwordx4 a[188:191], v210, %[srdz], s50 offen offset:3072 sc1\n" \
"v_mfma_f32_16x16x32_bf16 v[180:183], a[236:239], v[40:43], v[180:183]\n" \
"v_mfma_f32_16x16x32_bf16 v[184:187], a[228:231], v[64:67], v[184:187]\n" \
"v_mfma_f32_16x16x32_bf16 v[184:187], a[236:239], v[72:75], v[184:187]\n" \
"v_mfma_f32_16x16x32_bf16 v[188:191], a[228:231], v[96:99], v[188:191]\n" \
"ds_read_b64_tr_b16 a[216:217], v221 offset:49152\n" \
"s_waitcnt vmcnt(23)\n" \
"v_cmp_eq_u32_e32 vcc, 0, v232\n" \
"s_cbranch_vccnz Lspp_%=\n" \
"Lbkp_%=:\n" \
"s_nop 0\n" \
"v_mfma_f32_16x16x32_bf16 v[188:191], a[236:239], v[104:107], v[188:191]\n" \
"ds_read_b64_tr_b16 a[218:219], v221 offset:51200\n" \
"v_mfma_f32_16x16x32_bf16 v[192:195], a[196:199], a[0:3], v[128:131]\n" \
"ds_read_b64_tr_b16 a[208:209], v221 offset:53248\n" \
"v_mfma_f32_16x16x32_bf16 v[192:195], a[200:203], a[4:7], v[192:195]\n" \
"ds_read_b64_tr_b16 a[210:211], v221 offset:55296\n" \
"s_add_u32 m0, %[m0b], 0\n" \
"s_cmp_gt_u32 s48, 3\n" \
"s_cselect_b32 s49, 3, 4\n" \
"s_lshl_b32 s49, s49, 18\n" \
"s_or_b32 s49, s49, s59\n" \
"s_nop 0\n" \
"buffer_load_dwordx4 %[wo0], %[srdt], s49 offen offset:0 lds\n" \
"v_mfma_f32_16x16x32_bf16 v[196:199], a[196:199], a[16:19], v[132:135]\n" \
"v_mfma_f32_16x16x32_bf16 v[196:199], a[200:203], a[20:23], v[196:199]\n" \
"v_mfma_f32_16x16x32_bf16 v[200:203], a[196:199], a[32:35], v[136:139]\n" \
"v_mfma_f32_16x16x32_bf16 v[200:203], a[200:203], a[36:39], v[200:203]\n" \
"v_mfma_f32_16x16x32_bf16 v[204:207], a[196:199], a[48:51], v[140:143]\n" \
"ds_read_b64_tr_b16 a[224:225], v221 offset:57344\n" \
"v_mfma_f32_16x16x32_bf16 v[204:207], a[200:203], a[52:55], v[204:207]\n" \
"s_waitcnt vmcnt(21) lgkmcnt(5)\n" \
"s_nop 0\n" \
"v_mfma_f32_16x16x32_bf16 v[192:195], a[204:207], a[8:11], v[192:195]\n" \
"ds_read_b64_tr_b16 a[226:227], v221 offset:59392\n" \
"v_mfma_f32_16x16x32_bf16 v[192:195], a[220:223], a[12:15], v[192:195]\n" \
".if . - adaln_body != 53248\n" \
".error \"size model\"\n" \
".endif\n" \
"Lk12_%=:\n" \
"s_cbranch_execz Lk13_%=\n" \
"s_nop 0\n" \
"ds_read_b64_tr_b16 a[228:229], v221 offset:61440\n" \
"v_mfma_f32_16x16x32_bf16 v[196:199], a[204:207], a[24:27], v[196:199]\n" \
"ds_read_b64_tr_b16 a[230:231], v221 offset:63488\n" \
"v_mfma_f32_16x16x32_bf16 v[196:199], a[220:223], a[28:31], v[196:199]\n" \
"buffer_load_dwordx4 v212, %[srdt], s49 offen offset:1024 lds\n" \
"v_mfma_f32_16x16x32_bf16 v[200:203], a[204:207], a[40:43], v[200:203]\n" \
"v_mfma_f32_16x16x32_bf16 v[200:203], a[220:223], a[44:47], v[200:203]\n" \
"v_mfma_f32_16x16x32_bf16 v[204:207], a[204:207], a[56:59], v[204:207]\n" \
"ds_read_b64_tr_b16 a[236:237], v220 offset:49664\n" \
"v_mfma_f32_16x16x32_bf16 v[204:207], a[220:223], a[60:63], v[204:207]\n" \
"ds_read_b64_tr_b16 a[238:239], v220 offset:51712\n" \
"v_mfma_f32_16x16x32_bf16 v[176:179], a[232:235], v[16:19], v[176:179]\n" \
"v_mfma_f32_16x16x32_bf16 v[176:179], a[212:215], v[24:27], v[176:179]\n" \
"v_mfma_f32_16x16x32_bf16 v[180:183], a[232:235], v[48:51], v[180:183]\n" \
"buffer_load_dwordx4 v213, %[srdt], s49 offen offset:2048 lds\n" \
"v_mfma_f32_16x16x32_bf16 v[180:183], a[212:215], v[56:59], v[180:183]\n" \
"v_mfma_f32_16x16x32_bf16 v[184:187], a[232:235], v[80:83], v[184:187]\n" \
"s_add_u32 m0, %[m0b], 98304\n" \
"s_mov_b32 s61, 0\n" \
"s_nop 0\n" \
"buffer_load_dwordx4 %[vdn], s[64:67], s61 offen offset:0 lds\n" \
"v_mfma_f32_16x16x32_bf16 v[184:187], a[212:215], v[88:91], v[184:187]\n" \
"v_cmp_u_f32_e64 s[44:45], v192, v196\n" \
"v_mfma_f32_16x16x32_bf16 v[188:191], a[232:235], v[112:115], v[188:191]\n" \
"v_mfma_f32_16x16x32_bf16 v[188:191], a[212:215], v[120:123], v[188:191]\n" \
"v_cmp_u_f32_e64 s[56:57], v200, v204\n" \
"s_or_b64 s[56:57], s[56:57], s[44:45]\n" \
"s_cbranch_scc1 Lsp6_%=\n" \
"Lbk6_%=:\n" \
"s_waitcnt vmcnt(63) lgkmcnt(2)\n" \
"s_nop 0\n" \
"v_mfma_f32_16x16x32_bf16 v[144:147], a[216:219], a[0:3], v[144:147]\n" \
"ds_read_b64_tr_b16 a[232:233], v220 offset:53760\n" \
"v_mfma_f32_16x16x32_bf16 v[144:147], a[208:211], a[4:7], v[144:147]\n" \
"ds_read_b64_tr_b16 a[234:235], v220 offset:55808\n" \
"v_mfma_f32_16x16x32_bf16 v[148:151], a[216:219], a[16:19], v[148:151]\n" \
"ds_read_b64_tr_b16 a[196:197], v220 offset:57856\n" \
"v_mfma_f32_16x16x32_bf16 v[148:151], a[208:211], a[20:23], v[148:151]\n" \
"ds_read_b64_tr_b16 a[198:199], v220 offset:59904\n" \
"v_mfma_f32_16x16x32_bf16 v[152:155], a[216:219], a[32:35], v[152:155]\n" \
"ds_read_b64_tr_b16 a[200:201], v220 offset:61952\n" \
"v_mfma_f32_16x16x32_bf16 v[152:155], a[208:211], a[36:39], v[152:155]\n" \
"ds_read_b64_tr_b16 a[202:203], v220 offset:64000\n" \
"v_mfma_f32_16x16x32_bf16 v[156:159], a[216:219], a[48:51], v[156:159]\n" \
"ds_read_b64_tr_b16 a[204:205], v221 offset:49664\n" \
"v_mfma_f32_16x16x32_bf16 v[156:159], a[208:211], a[52:55], v[156:159]\n" \
"ds_read_b64_tr_b16 a[206:207], v221 offset:51712\n" \
"s_add_u32 m0, %[m0b], 0\n" \
"s_nop 0\n" \
"buffer_load_dwordx4 v214, %[srdt], s49 offen offset:3072 lds\n" \
"v_mfma_f32_16x16x32_bf16 v[144:147], a[224:227], a[8:11], v[144:147]\n" \
"ds_read_b64_tr_b16 a[212:213], v221 offset:57856\n" \
"v_mfma_f32_16x16x32_bf16 v[144:147], a[228:231], a[12:15], v[144:147]\n" \
"ds_read_b64_tr_b16 a[214:215], v221 offset:59904\n" \
"v_mfma_f32_16x16x32_bf16 v[148:151], a[224:227], a[24:27], v[148:151]\n" \
"ds_read_b64_tr_b16 a[220:221], v221 offset:53760\n" \
"v_mfma_f32_16x16x32_bf16 v[148:151], a[228:231], a[28:31], v[148:151]\n" \
"ds_read_b64_tr_b16 a[222:223], v221 offset:55808\n" \
"v_mfma_f32_16x16x32_bf16 v[152:155], a[224:227], a[40:43], v[152:155]\n" \
"ds_read_b64_tr_b16 a[216:217], v221 offset:61952\n" \
"v_mfma_f32_16x16x32_bf16 v[152:155], a[228:231], a[44:47], v[152:155]\n" \
"ds_read_b64_tr_b16 a[218:219], v221 offset:64000\n" \
"v_mfma_f32_16x16x32_bf16 v[156:159], a[224:227], a[56:59], v[156:159]\n" \
"v_xor_b32_e64 v218, 64, %[vdn]\n" \
"s_add_u32 m0, %[m0b], 99328\n" \
"s_mov_b32 s61, 4096\n" \
"buffer_load_dwordx4 v218, s[64:67], s61 offen offset:0 lds\n" \
"v_mfma_f32_16x16x32_bf16 v[156:159], a[228:231], a[60:63], v[156:159]\n" \
"s_waitcnt vmcnt(63) lgkmcnt(12)\n" \
"s_nop 0\n" \
"v_mfma_f32_16x16x32_bf16 v[160:163], a[236:239], a[0:3], v[160:163]\n" \
"v_mfma_f32_16x16x32_bf16 v[160:163], a[232:235], a[4:7], v[160:163]\n" \
"v_mfma_f32_16x16x32_bf16 v[164:167], a[236:239], a[16:19], v[164:167]\n" \
"v_mfma_f32_16x16x32_bf16 v[164:167], a[232:235], a[20:23], v[164:167]\n" \
"s_add_u32 m0, %[m0b], 16384\n" \
"s_cmp_gt_u32 s48, 4\n" \
"s_cselect_b32 s49, 4, 5\n" \
"s_lshl_b32 s49, s49, 18\n" \
"s_or_b32 s49, s49, s59\n" \
"buffer_load_dwordx4 %[wo0], %[srdt], s49 offen offset:0 lds\n" \
"v_mfma_f32_16x16x32_bf16 v[168:171], a[236:239], a[32:35], v[168:171]\n" \
"s_waitcnt vmcnt(15) lgkmcnt(0)\n" \
"s_barrier\n" \
"v_mfma_f32_16x16x32_bf16 v[168:171], a[232:235], a[36:39], v[168:171]\n" \
"ds_read_b64_tr_b16 a[208:209], v215 offset:49152\n" \
"v_mfma_f32_16x16x32_bf16 v[172:175], a[236:239], a[48:51], v[172:175]\n" \
"ds_read_b64_tr_b16 a[210:211], v215 offset:51200\n" \
"v_mfma_f32_16x16x32_bf16 v[172:175], a[232:235], a[52:55], v[172:175]\n" \
"ds_read_b64_tr_b16 a[224:225], v215 offset:53248\n" \
"v_mfma_f32_16x16x32_bf16 v[160:163], a[196:199], a[8:11], v[160:163]\n" \
"ds_read_b64_tr_b16 a[226:227], v215 offset:55296\n" \
"v_mfma_f32_16x16x32_bf16 v[160:163], a[200:203], a[12:15], v[160:163]\n" \
"ds_read_b64_tr_b16 a[228:229], v215 offset:57344\n" \
"v_mfma_f32_16x16x32_bf16 v[164:167], a[196:199], a[24:27], v[164:167]\n" \
"ds_read_b64_tr_b16 a[230:231], v215 offset:59392\n" \
"v_mfma_f32_16x16x32_bf16 v[164:167], a[200:203], a[28:31], v[164:167]\n" \
"ds_read_b64_tr_b16 a[236:237], v215 offset:61440\n" \
"v_mfma_f32_16x16x32_bf16 v[168:171], a[196:199], a[40:43], v[168:171]\n" \
"ds_read_b64_tr_b16 a[238:239], v215 offset:63488\n" \
"v_mfma_f32_16x16x32_bf16 v[168:171], a[200:203], a[44:47], v[168:171]\n" \
"buffer_load_dwordx4 v212, %[srdt], s49 offen offset:1024 lds\n" \
"v_mfma_f32_16x16x32_bf16 v[172:175], a[196:199], a[56:59], v[172:175]\n" \
"v_mfma_f32_16x16x32_bf16 v[172:175], a[200:203], a[60:63], v[172:175]\n" \
"s_waitcnt vmcnt(63) lgkmcnt(4)\n" \
"s_nop 0\n" \
"v_mfma_f32_16x16x32_bf16 v[176:179], a[204:207], a[0:3], v[176:179]\n" \
"v_xor_b32_e32 v218, 0x80, %[vdn]\n" \
"s_add_u32 m0, %[m0b], 100352\n" \
"s_mov_b32 s61, 8192\n" \
"buffer_load_dwordx4 v218, s[64:67], s61 offen offset:0 lds\n" \
"v_mfma_f32_16x16x32_bf16 v[176:179], a[220:223], a[4:7], v[176:179]\n" \
"v_mfma_f32_16x16x32_bf16 v[180:183], a[204:207], a[16:19], v[180:183]\n" \
"s_waitcnt vmcnt(11)\n" \
"v_and_b32_e32 v230, 1, v235\n" \
"v_mfma_f32_16x16x32_bf16 v[180:183], a[220:223], a[20:23], v[180:183]\n" \
"v_cmp_eq_u32_e64 s[68:69], 0, v230\n" \
"v_mfma_f32_16x16x32_bf16 v[184:187], a[204:207], a[32:35], v[184:187]\n" \
"s_cmp_lg_u64 s[68:69], 0\n" \
"s_cbranch_scc1 Ls1_%=\n" \
"Ls1b_%=:\n" \
"v_add_f32_e64 v235, %[rd0], v235\n" \
"v_mfma_f32_16x16x32_bf16 v[184:187], a[220:223], a[36:39], v[184:187]\n" \
"v_bfe_u32 v234, v224, 9, 1\n" \
"v_mfma_f32_16x16x32_bf16 v[188:191], a[204:207], a[48:51], v[188:191]\n" \
"ds_read_b64_tr_b16 a[232:233], v227 offset:49152\n" \
"v_add_f32_dpp v235, v235, v235 quad_perm:[1,0,3,2] row_mask:0xf bank_mask:0xf\n" \
"v_add_u32_e64 v230, s52, v234\n" \
"v_mfma_f32_16x16x32_bf16 v[188:191], a[220:223], a[52:55], v[188:191]\n" \
"ds_read_b64_tr_b16 a[234:235], v227 offset:51200\n" \
"s_mul_i32 s63, %[wid], 4088\n" \
"v_mfma_f32_16x16x32_bf16 v[128:131], a[208:211], a[128:131], v[192:195]\n" \
"ds_read_b64_tr_b16 a[196:197], v227 offset:53248\n" \
"v_mfma_f32_16x16x32_bf16 v[128:131], a[224:227], a[132:135], v[128:131]\n" \
"ds_read_b64_tr_b16 a[198:199], v227 offset:55296\n" \
"v_add_f32_dpp v235, v235, v235 quad_perm:[2,3,0,1] row_mask:0xf bank_mask:0xf\n" \
"s_sub_u32 s63, %[m0b], s63\n" \
"s_nop 0\n" \
"v_mfma_f32_16x16x32_bf16 v[132:135], a[208:211], a[144:147], v[196:199]\n" \
"v_lshl_add_u32 v234, v234, 2, s63\n" \
"s_add_u32 m0, %[m0b], 16384\n" \
"s_nop 0\n" \
"s_nop 0\n" \
"buffer_load_dwordx4 v213, %[srdt], s49 offen offset:2048 lds\n" \
"v_mfma_f32_16x16x32_bf16 v[132:135], a[224:227], a[148:151], v[132:135]\n" \
"v_add_f32_dpp v235, v235, v235 row_ror:8 row_mask:0xf bank_mask:0xf\n" \
"v_sub_u32_e32 v230, 0x7ff, v230\n" \
"v_mfma_f32_16x16x32_bf16 v[136:139], a[208:211], a[160:163], v[200:203]\n" \
"v_mov_b32_e64 v216, v235\n" \
"v_mfma_f32_16x16x32_bf16 v[136:139], a[224:227], a[164:167], v[136:139]\n" \
"v_add_u32_e32 v234, 0xcc04, v234\n" \
"v_mfma_f32_16x16x32_bf16 v[140:143], a[208:211], a[176:179], v[204:207]\n" \
"ds_read_b64_tr_b16 a[200:201], v227 offset:57344\n" \
"v_permlane16_swap_b32_e32 v235, v216\n" \
"s_nop 0\n" \
"v_mfma_f32_16x16x32_bf16 v[140:143], a[224:227], a[180:183], v[140:143]\n" \
"s_waitcnt vmcnt(10) lgkmcnt(5)\n" \
"s_nop 0\n" \
"v_mfma_f32_16x16x32_bf16 v[128:131], a[228:231], a[136:139], v[128:131]\n" \
"ds_read_b64_tr_b16 a[202:203], v227 offset:59392\n" \
"v_add_f32_e64 v235, v235, v216\n" \
"v_mfma_f32_16x16x32_bf16 v[128:131], a[236:239], a[140:143], v[128:131]\n" \
"ds_read_b64_tr_b16 a[204:205], v227 offset:61440\n" \
"v_mov_b32_e64 v216, v235\n" \
"v_xor_b32_e32 v218, 0xc0, %[vdn]\n" \
"s_add_u32 m0, %[m0b], 101376\n" \
"s_mov_b32 s61, 12288\n" \
"buffer_load_dwordx4 v218, s[64:67], s61 offen offset:0 lds\n" \
"v_mfma_f32_16x16x32_bf16 v[132:135], a[228:231], a[152:155], v[132:135]\n" \
"ds_read_b64_tr_b16 a[206:207], v227 offset:63488\n" \
"s_nop 0\n" \
"s_nop 0\n" \
"v_mfma_f32_16x16x32_bf16 v[132:135], a[236:239], a[156:159], v[132:135]\n" \
"v_permlane32_swap_b32_e32 v235, v216\n" \
"s_nop 0\n" \
"s_add_u32 m0, %[m0b], 16384\n" \
"s_nop 0\n" \
"s_nop 0\n" \
"buffer_load_dwordx4 v214, %[srdt], s49 offen offset:3072 lds\n" \
"v_mfma_f32_16x16x32_bf16 v[136:139], a[228:231], a[168:171], v[136:139]\n" \
"v_add_f32_e64 v235, v235, v216\n" \
"v_mfma_f32_16x16x32_bf16 v[136:139], a[236:239], a[172:175], v[136:139]\n" \
"v_max_f32_e32 v235, 0xff800000, v235\n" \
"v_mfma_f32_16x16x32_bf16 v[140:143], a[228:231], a[184:187], v[140:143]\n" \
"ds_read_b64_tr_b16 a[220:221], v215 offset:49664\n" \
"v_mfma_f32_16x16x32_bf16 v[140:143], a[236:239], a[188:191], v[140:143]\n" \
"ds_read_b64_tr_b16 a[222:223], v215 offset:51712\n" \
"v_ashrrev_i32_e64 v216, 31, v235\n" \
"v_mfma_f32_16x16x32_bf16 v[176:179], a[212:215], a[8:11], v[176:179]\n" \
"v_or_b32_e32 v216, 0x80000000, v216\n" \
"v_mfma_f32_16x16x32_bf16 v[176:179], a[216:219], a[12:15], v[176:179]\n" \
"v_xor_b32_e64 v235, v235, v216\n" \
"v_mfma_f32_16x16x32_bf16 v[180:183], a[212:215], a[24:27], v[180:183]\n" \
"v_and_b32_e32 v235, 0xfffff800, v235\n" \
"v_mfma_f32_16x16x32_bf16 v[180:183], a[216:219], a[28:31], v[180:183]\n" \
"v_or_b32_e64 v235, v235, v230\n" \
"v_mfma_f32_16x16x32_bf16 v[184:187], a[212:215], a[40:43], v[184:187]\n" \
"ds_write_b32 v234, v235 offset:65532\n" \
"v_mfma_f32_16x16x32_bf16 v[184:187], a[216:219], a[44:47], v[184:187]\n" \
"v_cmp_u_f32_e64 s[44:45], v128, v132\n" \
"v_mfma_f32_16x16x32_bf16 v[188:191], a[212:215], a[56:59], v[188:191]\n" \
"v_mfma_f32_16x16x32_bf16 v[188:191], a[216:219], a[60:63], v[188:191]\n" \
"v_cmp_u_f32_e64 s[56:57], v136, v140\n" \
"s_or_b64 s[56:57], s[56:57], s[44:45]\n" \
"s_cbranch_scc1 Lsp7_%=\n" \
"Lbk7_%=:\n" \
"s_waitcnt vmcnt(63) lgkmcnt(3)\n" \
"s_nop 0\n" \
"v_mfma_f32_16x16x32_bf16 v[144:147], a[232:235], a[128:131], v[144:147]\n" \
"ds_read_b64_tr_b16 a[212:213], v215 offset:53760\n" \
"v_mfma_f32_16x16x32_bf16 v[144:147], a[196:199], a[132:135], v[144:147]\n" \
"ds_read_b64_tr_b16 a[214:215], v215 offset:55808\n" \
"v_mfma_f32_16x16x32_bf16 v[148:151], a[232:235], a[144:147], v[148:151]\n" \
"ds_read_b64_tr_b16 a[208:209], v215 offset:57856\n" \
"v_mfma_f32_16x16x32_bf16 v[148:151], a[196:199], a[148:151], v[148:151]\n" \
"ds_read_b64_tr_b16 a[210:211], v215 offset:59904\n" \
"s_lshl_b32 s61, %[wid], 10\n" \
"v_lshrrev_b32_e32 v231, 3, v224\n" \
"v_add_u32_e64 v231, s61, v231\n" \
"s_add_u32 m0, %[m0b], 32768\n" \
"s_cmp_gt_u32 s48, 5\n" \
"s_cselect_b32 s49, 5, 6\n" \
"s_lshl_b32 s49, s49, 18\n" \
"s_or_b32 s49, s49, s59\n" \
"buffer_load_dwordx4 %[wo0], %[srdt], s49 offen offset:0 lds\n" \
"v_mfma_f32_16x16x32_bf16 v[152:155], a[232:235], a[160:163], v[152:155]\n" \
"ds_read_b64_tr_b16 a[216:217], v215 offset:61952\n" \
"v_readlane_b32 s44, %[tlo], 20\n" \
"v_readlane_b32 s45, %[thi], 20\n" \
"v_lshrrev_b32_e64 v236, 1, v231\n" \
"v_mfma_f32_16x16x32_bf16 v[152:155], a[196:199], a[164:167], v[152:155]\n" \
"ds_read_b64_tr_b16 a[218:219], v215 offset:64000\n" \
"v_mfma_f32_16x16x32_bf16 v[156:159], a[232:235], a[176:179], v[156:159]\n" \
"ds_read_b64_tr_b16 a[224:225], v227 offset:49664\n" \
"v_mfma_f32_16x16x32_bf16 v[156:159], a[196:199], a[180:183], v[156:159]\n" \
"ds_read_b64_tr_b16 a[226:227], v227 offset:51712\n" \
"v_mfma_f32_16x16x32_bf16 v[144:147], a[200:203], a[136:139], v[144:147]\n" \
"ds_read_b64_tr_b16 a[228:229], v227 offset:53760\n" \
"v_mfma_f32_16x16x32_bf16 v[144:147], a[204:207], a[140:143], v[144:147]\n" \
"ds_read_b64_tr_b16 a[230:231], v227 offset:55808\n" \
"v_mfma_f32_16x16x32_bf16 v[148:151], a[200:203], a[152:155], v[148:151]\n" \
"s_mov_b32 s61, 262144\n" \
"buffer_load_dwordx4 v[192:195], v231, %[srdz], s61 offen offset:0 sc1\n" \
"v_mfma_f32_16x16x32_bf16 v[148:151], a[204:207], a[156:159], v[148:151]\n" \
"v_mfma_f32_16x16x32_bf16 v[152:155], a[200:203], a[168:171], v[152:155]\n" \
"buffer_load_dwordx4 v212, %[srdt], s49 offen offset:1024 lds\n" \
"v_mfma_f32_16x16x32_bf16 v[152:155], a[204:207], a[172:175], v[152:155]\n" \
"v_mfma_f32_16x16x32_bf16 v[156:159], a[200:203], a[184:187], v[156:159]\n" \
"ds_read_b64_tr_b16 a[236:237], v227 offset:57856\n" \
"v_mfma_f32_16x16x32_bf16 v[156:159], a[204:207], a[188:191], v[156:159]\n" \
"s_waitcnt vmcnt(63) lgkmcnt(5)\n" \
"s_nop 0\n" \
"v_mfma_f32_16x16x32_bf16 v[160:163], a[220:223], a[128:131], v[160:163]\n" \
"ds_read_b64_tr_b16 a[238:239], v227 offset:59904\n" \
"v_mfma_f32_16x16x32_bf16 v[160:163], a[212:215], a[132:135], v[160:163]\n" \
"ds_read_b64_tr_b16 a[232:233], v227 offset:61952\n" \
"v_mfma_f32_16x16x32_bf16 v[164:167], a[220:223], a[144:147], v[164:167]\n" \
"ds_read_b64_tr_b16 a[234:235], v227 offset:64000\n" \
"s_mov_b32 s61, 266240\n" \
"buffer_load_dwordx4 v[196:199], v231, %[srdz], s61 offen offset:0 sc1\n" \
"v_mfma_f32_16x16x32_bf16 v[164:167], a[212:215], a[148:151], v[164:167]\n" \
"v_mfma_f32_16x16x32_bf16 v[168:171], a[220:223], a[160:163], v[168:171]\n" \
"v_cvt_pk_bf16_f32 v128, v128, v129\n" \
"buffer_load_dwordx4 v213, %[srdt], s49 offen offset:2048 lds\n" \
"v_mfma_f32_16x16x32_bf16 v[168:171], a[212:215], a[164:167], v[168:171]\n" \
"v_cvt_pk_bf16_f32 v129, v130, v131\n" \
"v_mfma_f32_16x16x32_bf16 v[172:175], a[220:223], a[176:179], v[172:175]\n" \
"v_cvt_pk_bf16_f32 v130, v144, v145\n" \
"v_mfma_f32_16x16x32_bf16 v[172:175], a[212:215], a[180:183], v[172:175]\n" \
"v_cvt_pk_bf16_f32 v131, v146, v147\n" \
"v_mfma_f32_16x16x32_bf16 v[160:163], a[208:211], a[136:139], v[160:163]\n" \
"s_add_u32 s55, s46, 0\n" \
"s_nop 0\n" \
"buffer_store_dwordx4 v[128:131], %[vxs], %[srdz], s55 offen offset:0 sc1\n" \
"v_lshrrev_b32_e32 v230, 5, v224\n" \
"s_lshl_b32 s63, %[wid], 12\n" \
"v_mfma_f32_16x16x32_bf16 v[160:163], a[216:219], a[140:143], v[160:163]\n" \
"v_and_b32_e32 v230, 28, v230\n" \
"s_sub_u32 s63, %[m0b], s63\n" \
"v_mfma_f32_16x16x32_bf16 v[164:167], a[208:211], a[152:155], v[164:167]\n" \
"v_cvt_pk_bf16_f32 v132, v132, v133\n" \
"s_mov_b32 s61, 270336\n" \
"buffer_load_dwordx4 v[200:203], v231, %[srdz], s61 offen offset:0 sc1\n" \
"s_add_u32 s63, s63, 117760\n" \
"v_add_u32_e64 v230, s63, v230\n" \
"v_mfma_f32_16x16x32_bf16 v[164:167], a[216:219], a[156:159], v[164:167]\n" \
"v_cvt_pk_bf16_f32 v133, v134, v135\n" \
"v_mfma_f32_16x16x32_bf16 v[168:171], a[208:211], a[168:171], v[168:171]\n" \
"s_waitcnt vmcnt(14) lgkmcnt(0)\n" \
"s_barrier\n" \
"v_mfma_f32_16x16x32_bf16 v[168:171], a[216:219], a[172:175], v[168:171]\n" \
"ds_read_b64_tr_b16 a[196:197], v215 offset:0\n" \
"v_cvt_pk_bf16_f32 v134, v148, v149\n" \
"ds_read_b32 v235, v230 offset:0\n" \
"v_readlane_b32 s63, %[thi], 34\n" \
"v_mfma_f32_16x16x32_bf16 v[172:175], a[208:211], a[184:187], v[172:175]\n" \
"ds_read_b64_tr_b16 a[198:199], v215 offset:2048\n" \
"v_cvt_pk_bf16_f32 v135, v150, v151\n" \
"v_mfma_f32_16x16x32_bf16 v[172:175], a[216:219], a[188:191], v[172:175]\n" \
"s_waitcnt vmcnt(63) lgkmcnt(7)\n" \
"s_nop 0\n" \
"v_mfma_f32_16x16x32_bf16 v[176:179], a[224:227], a[128:131], v[176:179]\n" \
"ds_read_b64_tr_b16 a[200:201], v215 offset:4096\n" \
"v_cvt_pk_bf16_f32 v136, v136, v137\n" \
"v_mfma_f32_16x16x32_bf16 v[176:179], a[228:231], a[132:135], v[176:179]\n" \
"ds_read_b64_tr_b16 a[202:203], v215 offset:6144\n" \
"v_cvt_pk_bf16_f32 v137, v138, v139\n" \
"v_mfma_f32_16x16x32_bf16 v[180:183], a[224:227], a[144:147], v[180:183]\n" \
"ds_read_b64_tr_b16 a[204:205], v215 offset:8192\n" \
"s_add_u32 s55, s46, 304128\n" \
"buffer_store_dwordx4 v[132:135], %[vxs], %[srdz], s55 offen offset:0 sc1\n" \
"v_mfma_f32_16x16x32_bf16 v[180:183], a[228:231], a[148:151], v[180:183]\n" \
"ds_read_b64_tr_b16 a[206:207], v215 offset:10240\n" \
"v_cvt_pk_bf16_f32 v138, v152, v153\n" \
"v_mfma_f32_16x16x32_bf16 v[184:187], a[224:227], a[160:163], v[184:187]\n" \
"ds_read_b64_tr_b16 a[220:221], v215 offset:12288\n" \
"v_cvt_pk_bf16_f32 v139, v154, v155\n" \
"s_mov_b32 s61, 274432\n" \
"buffer_load_dwordx4 v[204:207], v231, %[srdz], s61 offen offset:0 sc1\n" \
"s_waitcnt lgkmcnt(6)\n" \
"s_nop 0\n" \
"v_min_u32_dpp v234, v235, v235 quad_perm:[1,0,3,2] row_mask:0xf bank_mask:0xf\n" \
"v_max_u32_dpp v235, v235, v235 quad_perm:[1,0,3,2] row_mask:0xf bank_mask:0xf\n" \
"v_mfma_f32_16x16x32_bf16 v[184:187], a[228:231], a[164:167], v[184:187]\n" \
"ds_read_b64_tr_b16 a[222:223], v215 offset:14336\n" \
"v_mfma_f32_16x16x32_bf16 v[188:191], a[224:227], a[176:179], v[188:191]\n" \
"ds_read_b64_tr_b16 a[212:213], v227 offset:0\n" \
"v_cvt_pk_bf16_f32 v140, v140, v141\n" \
"v_min_u32_dpp v216, v235, v235 quad_perm:[2,3,0,1] row_mask:0xf bank_mask:0xf\n" \
"v_max_u32_dpp v230, v234, v234 quad_perm:[2,3,0,1] row_mask:0xf bank_mask:0xf\n" \
"buffer_load_dwordx4 v214, %[srdt], s49 offen offset:3072 lds\n" \
"v_mfma_f32_16x16x32_bf16 v[188:191], a[228:231], a[180:183], v[188:191]\n" \
"ds_read_b64_tr_b16 a[214:215], v227 offset:2048\n" \
"v_cvt_pk_bf16_f32 v141, v142, v143\n" \
"v_max_u32_dpp v235, v235, v235 quad_perm:[2,3,0,1] row_mask:0xf bank_mask:0xf\n" \
"v_max_u32_e64 v234, v216, v230\n" \
"v_mfma_f32_16x16x32_bf16 v[176:179], a[236:239], a[136:139], v[176:179]\n" \
"ds_read_b64_tr_b16 a[208:209], v227 offset:4096\n" \
"s_add_u32 s55, s46, 608256\n" \
"buffer_store_dwordx4 v[136:139], %[vxs], %[srdz], s55 offen offset:0 sc1\n" \
"v_mfma_f32_16x16x32_bf16 v[176:179], a[232:235], a[140:143], v[176:179]\n" \
"ds_read_b64_tr_b16 a[210:211], v227 offset:6144\n" \
"v_cvt_pk_bf16_f32 v142, v156, v157\n" \
"v_min_u32_dpp v216, v235, v235 row_half_mirror row_mask:0xf bank_mask:0xf\n" \
"v_max_u32_dpp v230, v234, v234 row_half_mirror row_mask:0xf bank_mask:0xf\n" \
"v_mfma_f32_16x16x32_bf16 v[180:183], a[236:239], a[152:155], v[180:183]\n" \
"v_cvt_pk_bf16_f32 v143, v158, v159\n" \
"v_max_u32_dpp v235, v235, v235 row_half_mirror row_mask:0xf bank_mask:0xf\n" \
"v_max_u32_e64 v234, v216, v230\n" \
"v_mfma_f32_16x16x32_bf16 v[180:183], a[232:235], a[156:159], v[180:183]\n" \
"s_add_u32 s55, s46, 912384\n" \
"buffer_store_dwordx4 v[140:143], %[vxs], %[srdz], s55 offen offset:0 sc1\n" \
"v_lshlrev_b32_e64 v230, 4, v224\n" \
"v_mfma_f32_16x16x32_bf16 v[184:187], a[236:239], a[168:171], v[184:187]\n" \
"v_mfma_f32_16x16x32_bf16 v[184:187], a[232:235], a[172:175], v[184:187]\n" \
"global_load_dwordx2 v[232:233], v236, s[44:45]\n" \
"v_mfma_f32_16x16x32_bf16 v[188:191], a[236:239], a[184:187], v[188:191]\n" \
"ds_read_b64_tr_b16 a[216:217], v227 offset:8192\n" \
"v_mfma_f32_16x16x32_bf16 v[188:191], a[232:235], a[188:191], v[188:191]\n" \
"s_waitcnt vmcnt(63) lgkmcnt(5)\n" \
"s_nop 0\n" \
"v_mfma_f32_16x16x32_bf16 v[4:7], a[196:199], a[64:67], v[4:7]\n" \
"ds_read_b64_tr_b16 a[218:219], v227 offset:10240\n" \
"s_mov_b64 exec, 3\n" \
"s_nop 0\n" \
"buffer_atomic_or_x2 v[234:235], v230, %[srdz], s63 offen sc1\n" \
"s_mov_b64 exec, -1\n" \
"s_nop 0\n" \
"v_mfma_f32_16x16x32_bf16 v[4:7], a[200:203], a[68:71], v[4:7]\n" \
"ds_read_b64_tr_b16 a[224:225], v227 offset:12288\n" \
"v_mfma_f32_16x16x32_bf16 v[36:39], a[196:199], a[80:83], v[36:39]\n" \
"ds_read_b64_tr_b16 a[226:227], v227 offset:14336\n" \
"v_mfma_f32_16x16x32_bf16 v[36:39], a[200:203], a[84:87], v[36:39]\n" \
"v_mfma_f32_16x16x32_bf16 v[68:71], a[196:199], a[96:99], v[68:71]\n" \
"v_cvt_pk_bf16_f32 v160, v160, v161\n" \
"v_mfma_f32_16x16x32_bf16 v[68:71], a[200:203], a[100:103], v[68:71]\n" \
"v_cvt_pk_bf16_f32 v161, v162, v163\n" \
"v_mfma_f32_16x16x32_bf16 v[100:103], a[196:199], a[112:115], v[100:103]\n" \
"ds_read_b64_tr_b16 a[228:229], v215 offset:512\n" \
"v_cvt_pk_bf16_f32 v162, v176, v177\n" \
"v_mfma_f32_16x16x32_bf16 v[100:103], a[200:203], a[116:119], v[100:103]\n" \
"ds_read_b64_tr_b16 a[230:231], v215 offset:2560\n" \
"v_cvt_pk_bf16_f32 v163, v178, v179\n" \
"v_mfma_f32_16x16x32_bf16 v[4:7], a[204:207], a[72:75], v[4:7]\n" \
"ds_read_b64_tr_b16 a[236:237], v215 offset:4608\n" \
"v_mfma_f32_16x16x32_bf16 v[4:7], a[220:223], a[76:79], v[4:7]\n" \
"ds_read_b64_tr_b16 a[238:239], v215 offset:6656\n" \
"v_mfma_f32_16x16x32_bf16 v[36:39], a[204:207], a[88:91], v[36:39]\n" \
"s_add_u32 s55, s46, 4096\n" \
"buffer_store_dwordx4 v[160:163], %[vxs], %[srdz], s55 offen offset:0 sc1\n" \
"v_mfma_f32_16x16x32_bf16 v[36:39], a[220:223], a[92:95], v[36:39]\n" \
"v_cvt_pk_bf16_f32 v164, v164, v165\n" \
"v_cvt_pk_bf16_f32 v165, v166, v167\n" \
"v_mfma_f32_16x16x32_bf16 v[68:71], a[204:207], a[104:107], v[68:71]\n" \
"v_cvt_pk_bf16_f32 v166, v180, v181\n" \
"s_add_u32 m0, %[m0b], 49152\n" \
"s_cmp_gt_u32 s48, 6\n" \
"s_cselect_b32 s49, 6, 7\n" \
"s_lshl_b32 s49, s49, 18\n" \
"s_or_b32 s49, s49, s59\n" \
"buffer_load_dwordx4 %[wo0], %[srdt], s49 offen offset:0 lds\n" \
"v_mfma_f32_16x16x32_bf16 v[68:71], a[220:223], a[108:111], v[68:71]\n" \
"v_cvt_pk_bf16_f32 v167, v182, v183\n" \
"v_mfma_f32_16x16x32_bf16 v[100:103], a[204:207], a[120:123], v[100:103]\n" \
"ds_read_b64_tr_b16 a[232:233], v215 offset:8704\n" \
"v_mfma_f32_16x16x32_bf16 v[100:103], a[220:223], a[124:127], v[100:103]\n" \
"s_waitcnt vmcnt(63) lgkmcnt(5)\n" \
"s_nop 0\n" \
"v_mfma_f32_16x16x32_bf16 v[12:15], a[212:215], a[64:67], v[12:15]\n" \
"ds_read_b64_tr_b16 a[234:235], v215 offset:10752\n" \
"v_cvt_pk_bf16_f32 v168, v168, v169\n" \
"v_mfma_f32_16x16x32_bf16 v[12:15], a[208:211], a[68:71], v[12:15]\n" \
"ds_read_b64_tr_b16 a[196:197], v215 offset:12800\n" \
"s_add_u32 s55, s46, 308224\n" \
"buffer_store_dwordx4 v[164:167], %[vxs], %[srdz], s55 offen offset:0 sc1\n" \
"v_mfma_f32_16x16x32_bf16 v[44:47], a[212:215], a[80:83], v[44:47]\n" \
"ds_read_b64_tr_b16 a[198:199], v215 offset:14848\n" \
"v_cvt_pk_bf16_f32 v169, v170, v171\n" \
"v_mfma_f32_16x16x32_bf16 v[44:47], a[208:211], a[84:87], v[44:47]\n" \
"v_cvt_pk_bf16_f32 v170, v184, v185\n" \
"v_cvt_pk_bf16_f32 v171, v186, v187\n" \
"v_mfma_f32_16x16x32_bf16 v[76:79], a[212:215], a[96:99], v[76:79]\n" \
"s_add_u32 s55, s46, 612352\n" \
"buffer_store_dwordx4 v[168:171], %[vxs], %[srdz], s55 offen offset:0 sc1\n" \
"v_mfma_f32_16x16x32_bf16 v[76:79], a[208:211], a[100:103], v[76:79]\n" \
"v_mfma_f32_16x16x32_bf16 v[108:111], a[212:215], a[112:115], v[108:111]\n" \
"ds_read_b64_tr_b16 a[200:201], v227 offset:512\n" \
"v_cvt_pk_bf16_f32 v172, v172, v173\n" \
"buffer_load_dwordx4 v212, %[srdt], s49 offen offset:1024 lds\n" \
"v_mfma_f32_16x16x32_bf16 v[108:111], a[208:211], a[116:119], v[108:111]\n" \
"ds_read_b64_tr_b16 a[202:203], v227 offset:2560\n" \
"v_cvt_pk_bf16_f32 v173, v174, v175\n" \
"v_mfma_f32_16x16x32_bf16 v[12:15], a[216:219], a[72:75], v[12:15]\n" \
"ds_read_b64_tr_b16 a[204:205], v227 offset:4608\n" \
"v_cvt_pk_bf16_f32 v174, v188, v189\n" \
"v_mfma_f32_16x16x32_bf16 v[12:15], a[224:227], a[76:79], v[12:15]\n" \
"ds_read_b64_tr_b16 a[206:207], v227 offset:6656\n" \
"v_cvt_pk_bf16_f32 v175, v190, v191\n" \
"v_mfma_f32_16x16x32_bf16 v[44:47], a[216:219], a[88:91], v[44:47]\n" \
"s_add_u32 s55, s46, 916480\n" \
"buffer_store_dwordx4 v[172:175], %[vxs], %[srdz], s55 offen offset:0 sc1\n" \
"v_mfma_f32_16x16x32_bf16 v[44:47], a[224:227], a[92:95], v[44:47]\n" \
"v_mfma_f32_16x16x32_bf16 v[76:79], a[216:219], a[104:107], v[76:79]\n" \
"buffer_load_dwordx4 v213, %[srdt], s49 offen offset:2048 lds\n" \
"v_mfma_f32_16x16x32_bf16 v[76:79], a[224:227], a[108:111], v[76:79]\n" \
"v_mfma_f32_16x16x32_bf16 v[108:111], a[216:219], a[120:123], v[108:111]\n" \
"ds_read_b64_tr_b16 a[220:221], v227 offset:8704\n" \
"v_mfma_f32_16x16x32_bf16 v[108:111], a[224:227], a[124:127], v[108:111]\n" \
"s_waitcnt vmcnt(63) lgkmcnt(5)\n" \
"s_nop 0\n" \
"v_mfma_f32_16x16x32_bf16 v[20:23], a[228:231], a[64:67], v[20:23]\n" \
"ds_read_b64_tr_b16 a[222:223], v227 offset:10752\n" \
"v_mfma_f32_16x16x32_bf16 v[20:23], a[236:239], a[68:71], v[20:23]\n" \
"ds_read_b64_tr_b16 a[212:213], v227 offset:12800\n" \
"v_mfma_f32_16x16x32_bf16 v[52:55], a[228:231], a[80:83], v[52:55]\n" \
"ds_read_b64_tr_b16 a[214:215], v227 offset:14848\n" \
"v_mfma_f32_16x16x32_bf16 v[52:55], a[236:239], a[84:87], v[52:55]\n" \
"global_load_dword v237, %[vct], %[pct]\n" \
"v_mfma_f32_16x16x32_bf16 v[84:87], a[228:231], a[96:99], v[84:87]\n" \
"v_mfma_f32_16x16x32_bf16 v[84:87], a[236:239], a[100:103], v[84:87]\n" \
"v_xor_b32_e32 v218, 0x80, %[vdn]\n" \
"s_add_u32 m0, %[m0b], 114688\n" \
"s_mov_b32 s61, 16384\n" \
"buffer_load_dwordx4 v218, s[64:67], s61 offen offset:0 lds\n" \
"v_mfma_f32_16x16x32_bf16 v[116:119], a[228:231], a[112:115], v[116:119]\n" \
"v_mfma_f32_16x16x32_bf16 v[116:119], a[236:239], a[116:119], v[116:119]\n" \
"s_add_u32 m0, %[m0b], 49152\n" \
"s_nop 0\n" \
"s_nop 0\n" \
"buffer_load_dwordx4 v214, %[srdt], s49 offen offset:3072 lds\n" \
"v_mfma_f32_16x16x32_bf16 v[20:23], a[232:235], a[72:75], v[20:23]\n" \
"v_mfma_f32_16x16x32_bf16 v[20:23], a[196:199], a[76:79], v[20:23]\n" \
"v_mfma_f32_16x16x32_bf16 v[52:55], a[232:235], a[88:91], v[52:55]\n" \
"v_mfma_f32_16x16x32_bf16 v[52:55], a[196:199], a[92:95], v[52:55]\n" \
"v_mfma_f32_16x16x32_bf16 v[84:87], a[232:235], a[104:107], v[84:87]\n" \
"s_waitcnt vmcnt(24) lgkmcnt(0)\n" \
"s_barrier\n" \
"v_mfma_f32_16x16x32_bf16 v[84:87], a[196:199], a[108:111], v[84:87]\n" \
"ds_read_b64_tr_b16 a[208:209], v215 offset:16384\n" \
"v_mfma_f32_16x16x32_bf16 v[116:119], a[232:235], a[120:123], v[116:119]\n" \
"ds_read_b64_tr_b16 a[210:211], v215 offset:18432\n" \
"v_mfma_f32_16x16x32_bf16 v[116:119], a[196:199], a[124:127], v[116:119]\n" \
"s_waitcnt vmcnt(63) lgkmcnt(2)\n" \
"s_nop 0\n" \
"v_mfma_f32_16x16x32_bf16 v[28:31], a[200:203], a[64:67], v[28:31]\n" \
"ds_read_b64_tr_b16 a[216:217], v215 offset:20480\n" \
"v_mfma_f32_16x16x32_bf16 v[28:31], a[204:207], a[68:71], v[28:31]\n" \
"ds_read_b64_tr_b16 a[218:219], v215 offset:22528\n" \
"v_mfma_f32_16x16x32_bf16 v[60:63], a[200:203], a[80:83], v[60:63]\n" \
"ds_read_b64_tr_b16 a[224:225], v215 offset:24576\n" \
"v_mfma_f32_16x16x32_bf16 v[60:63], a[204:207], a[84:87], v[60:63]\n" \
"ds_read_b64_tr_b16 a[226:227], v215 offset:26624\n" \
"v_mfma_f32_16x16x32_bf16 v[92:95], a[200:203], a[96:99], v[92:95]\n" \
"ds_read_b64_tr_b16 a[228:229], v215 offset:28672\n" \
"v_xor_b32_e32 v218, 0xc0, %[vdn]\n" \
"s_add_u32 m0, %[m0b], 115712\n" \
"s_mov_b32 s61, 20480\n" \
"buffer_load_dwordx4 v218, s[64:67], s61 offen offset:0 lds\n" \
"v_mfma_f32_16x16x32_bf16 v[92:95], a[204:207], a[100:103], v[92:95]\n" \
"ds_read_b64_tr_b16 a[230:231], v215 offset:30720\n" \
"v_mfma_f32_16x16x32_bf16 v[124:127], a[200:203], a[112:115], v[124:127]\n" \
"ds_read_b64_tr_b16 a[236:237], v227 offset:16384\n" \
"v_mfma_f32_16x16x32_bf16 v[124:127], a[204:207], a[116:119], v[124:127]\n" \
"ds_read_b64_tr_b16 a[238:239], v227 offset:18432\n" \
"v_mfma_f32_16x16x32_bf16 v[28:31], a[220:223], a[72:75], v[28:31]\n" \
"ds_read_b64_tr_b16 a[232:233], v227 offset:20480\n" \
"v_mfma_f32_16x16x32_bf16 v[28:31], a[212:215], a[76:79], v[28:31]\n" \
"ds_read_b64_tr_b16 a[234:235], v227 offset:22528\n" \
"v_mfma_f32_16x16x32_bf16 v[60:63], a[220:223], a[88:91], v[60:63]\n" \
"v_mfma_f32_16x16x32_bf16 v[60:63], a[212:215], a[92:95], v[60:63]\n" \
"v_mfma_f32_16x16x32_bf16 v[92:95], a[220:223], a[104:107], v[92:95]\n" \
"v_mfma_f32_16x16x32_bf16 v[92:95], a[212:215], a[108:111], v[92:95]\n" \
"v_mfma_f32_16x16x32_bf16 v[124:127], a[220:223], a[120:123], v[124:127]\n" \
"ds_read_b64_tr_b16 a[196:197], v227 offset:24576\n" \
"v_mfma_f32_16x16x32_bf16 v[124:127], a[212:215], a[124:127], v[124:127]\n" \
"s_waitcnt vmcnt(63) lgkmcnt(5)\n" \
"s_nop 0\n" \
"v_mfma_f32_16x16x32_bf16 v[4:7], a[208:211], v[0:3], v[4:7]\n" \
"ds_read_b64_tr_b16 a[198:199], v227 offset:26624\n" \
"v_mfma_f32_16x16x32_bf16 v[4:7], a[216:219], v[8:11], v[4:7]\n" \
"ds_read_b64_tr_b16 a[200:201], v227 offset:28672\n" \
"v_mfma_f32_16x16x32_bf16 v[36:39], a[208:211], v[32:35], v[36:39]\n" \
"ds_read_b64_tr_b16 a[202:203], v227 offset:30720\n" \
"v_mfma_f32_16x16x32_bf16 v[36:39], a[216:219], v[40:43], v[36:39]\n" \
"s_waitcnt vmcnt(12)\n" \
"s_nop 0\n" \
"v_and_or_b32 v236, v192, v193, 0\n" \
"v_bitop3_b32 v236, v236, v194, v195 bitop3:0x80\n" \
"s_add_u32 m0, %[m0b], 116736\n" \
"s_mov_b32 s61, 24576\n" \
"buffer_load_dwordx4 %[vdn], s[64:67], s61 offen offset:0 lds\n" \
"v_mfma_f32_16x16x32_bf16 v[68:71], a[208:211], v[64:67], v[68:71]\n" \
"v_bitop3_b32 v236, v236, v196, v197 bitop3:0x80\n" \
"v_mfma_f32_16x16x32_bf16 v[68:71], a[216:219], v[72:75], v[68:71]\n" \
"v_bitop3_b32 v236, v236, v198, v199 bitop3:0x80\n" \
"v_mfma_f32_16x16x32_bf16 v[100:103], a[208:211], v[96:99], v[100:103]\n" \
"v_bitop3_b32 v236, v236, v200, v201 bitop3:0x80\n" \
"v_mfma_f32_16x16x32_bf16 v[100:103], a[216:219], v[104:107], v[100:103]\n" \
"v_bitop3_b32 v236, v236, v202, v203 bitop3:0x80\n" \
"v_mfma_f32_16x16x32_bf16 v[4:7], a[224:227], v[16:19], v[4:7]\n" \
"v_bitop3_b32 v236, v236, v204, v205 bitop3:0x80\n" \
"v_mfma_f32_16x16x32_bf16 v[4:7], a[228:231], v[24:27], v[4:7]\n" \
"v_bitop3_b32 v236, v236, v206, v207 bitop3:0x80\n" \
"v_mfma_f32_16x16x32_bf16 v[36:39], a[224:227], v[48:51], v[36:39]\n" \
"s_mov_b32 s54, 0x07060302\n" \
"v_and_b32_e64 v236, 1, v236\n" \
"v_mfma_f32_16x16x32_bf16 v[36:39], a[228:231], v[56:59], v[36:39]\n" \
"v_and_b32_e32 v231, 0x170, v231\n" \
"v_mfma_f32_16x16x32_bf16 v[68:71], a[224:227], v[80:83], v[68:71]\n" \
"s_waitcnt vmcnt(16) lgkmcnt(15)\n" \
"s_barrier\n" \
"v_mfma_f32_16x16x32_bf16 v[68:71], a[228:231], v[88:91], v[68:71]\n" \
"ds_read_b64_tr_b16 a[204:205], v215 offset:32768\n" \
"v_cmp_eq_u32_e64 s[56:57], 0, v236\n" \
"v_mfma_f32_16x16x32_bf16 v[100:103], a[224:227], v[112:115], v[100:103]\n" \
"ds_read_b64_tr_b16 a[206:207], v215 offset:34816\n" \
"v_lshrrev_b32_e64 v236, 7, v224\n" \
"v_mfma_f32_16x16x32_bf16 v[100:103], a[228:231], v[120:123], v[100:103]\n" \
".if . - adaln_body != 57344\n" \
".error \"size model\"\n" \
".endif\n" \
"Lk13_%=:\n" \
"s_cbranch_execz Lk14_%=\n" \
"s_nop 0\n" \
"s_waitcnt vmcnt(13) lgkmcnt(2)\n" \
"s_nop 0\n" \
"v_mfma_f32_16x16x32_bf16 v[12:15], a[236:239], v[0:3], v[12:15]\n" \
"ds_read_b64_tr_b16 a[220:221], v215 offset:36864\n" \
"v_and_or_b32 v231, v236, 8, v231\n" \
"v_mfma_f32_16x16x32_bf16 v[12:15], a[232:235], v[8:11], v[12:15]\n" \
"ds_read_b64_tr_b16 a[222:223], v215 offset:38912\n" \
"v_lshrrev_b32_e64 v236, 12, v224\n" \
"v_mfma_f32_16x16x32_bf16 v[44:47], a[236:239], v[32:35], v[44:47]\n" \
"ds_read_b64_tr_b16 a[212:213], v215 offset:40960\n" \
"v_lshl_or_b32 v231, v236, 7, v231\n" \
"v_mfma_f32_16x16x32_bf16 v[44:47], a[232:235], v[40:43], v[44:47]\n" \
"ds_read_b64_tr_b16 a[214:215], v215 offset:43008\n" \
"s_mul_i32 s61, %[wid], 3584\n" \
"s_sub_u32 s61, %[m0b], s61\n" \
"v_add_u32_e32 v231, s61, v231\n" \
"v_mfma_f32_16x16x32_bf16 v[76:79], a[236:239], v[64:67], v[76:79]\n" \
"ds_read_b64_tr_b16 a[208:209], v215 offset:45056\n" \
"s_cmp_lg_u64 s[56:57], 0\n" \
"s_cbranch_scc1 Lpars_%=\n" \
"Lparb_%=:\n" \
"v_mov_b32_e64 v234, 0\n" \
"v_mfma_f32_16x16x32_bf16 v[76:79], a[232:235], v[72:75], v[76:79]\n" \
"ds_read_b64_tr_b16 a[210:211], v215 offset:47104\n" \
"v_lshlrev_b32_e64 v184, 16, v232\n" \
"v_mfma_f32_16x16x32_bf16 v[108:111], a[236:239], v[96:99], v[108:111]\n" \
"ds_read_b64_tr_b16 a[216:217], v227 offset:32768\n" \
"v_and_b32_e32 v185, 0xffff0000, v232\n" \
"v_mfma_f32_16x16x32_bf16 v[108:111], a[232:235], v[104:107], v[108:111]\n" \
"ds_read_b64_tr_b16 a[218:219], v227 offset:34816\n" \
"v_lshlrev_b32_e32 v186, 16, v233\n" \
"v_xor_b32_e32 v218, 64, %[vdn]\n" \
"s_add_u32 m0, %[m0b], 117760\n" \
"s_mov_b32 s61, 28672\n" \
"buffer_load_dwordx4 v218, s[64:67], s61 offen offset:0 lds\n" \
"v_mfma_f32_16x16x32_bf16 v[12:15], a[196:199], v[16:19], v[12:15]\n" \
"ds_read_b64_tr_b16 a[224:225], v227 offset:36864\n" \
"v_and_b32_e32 v187, 0xffff0000, v233\n" \
"v_mfma_f32_16x16x32_bf16 v[12:15], a[200:203], v[24:27], v[12:15]\n" \
"ds_read_b64_tr_b16 a[226:227], v227 offset:38912\n" \
"v_mul_f32_e64 v188, v200, v184\n" \
"v_mfma_f32_16x16x32_bf16 v[44:47], a[196:199], v[48:51], v[44:47]\n" \
"v_mul_f32_e32 v189, v201, v185\n" \
"v_mul_f32_e32 v190, v202, v186\n" \
"v_mfma_f32_16x16x32_bf16 v[44:47], a[200:203], v[56:59], v[44:47]\n" \
"v_mul_f32_e32 v191, v203, v187\n" \
"v_mul_f32_e32 v235, v196, v184\n" \
"v_mfma_f32_16x16x32_bf16 v[76:79], a[196:199], v[80:83], v[76:79]\n" \
"v_fmac_f32_e32 v235, v197, v185\n" \
"s_nop 0\n" \
"v_mfma_f32_16x16x32_bf16 v[76:79], a[200:203], v[88:91], v[76:79]\n" \
"v_fmac_f32_e32 v235, v198, v186\n" \
"s_nop 0\n" \
"v_mfma_f32_16x16x32_bf16 v[108:111], a[196:199], v[112:115], v[108:111]\n" \
"ds_read_b64_tr_b16 a[228:229], v227 offset:40960\n" \
"v_fmac_f32_e32 v235, v199, v187\n" \
"s_nop 0\n" \
"v_mfma_f32_16x16x32_bf16 v[108:111], a[200:203], v[120:123], v[108:111]\n" \
"s_waitcnt vmcnt(63) lgkmcnt(5)\n" \
"s_nop 0\n" \
"v_mfma_f32_16x16x32_bf16 v[4:7], a[204:207], a[0:3], v[4:7]\n" \
"ds_read_b64_tr_b16 a[230:231], v227 offset:43008\n" \
"v_add_f32_e64 v184, v188, v189\n" \
"v_mfma_f32_16x16x32_bf16 v[4:7], a[220:223], a[4:7], v[4:7]\n" \
"ds_read_b64_tr_b16 a[236:237], v227 offset:45056\n" \
"v_add_f32_e64 v185, v190, v191\n" \
"v_mfma_f32_16x16x32_bf16 v[36:39], a[204:207], a[16:19], v[36:39]\n" \
"ds_read_b64_tr_b16 a[238:239], v227 offset:47104\n" \
"v_add_f32_e64 v184, v184, v185\n" \
"v_mfma_f32_16x16x32_bf16 v[36:39], a[220:223], a[20:23], v[36:39]\n" \
"s_waitcnt vmcnt(7)\n" \
"v_mov_b32_e32 v230, 1\n" \
"buffer_store_dword v230, v225, %[srdz], s62 offen offset:64 sc1\n" \
"v_mfma_f32_16x16x32_bf16 v[68:71], a[204:207], a[32:35], v[68:71]\n" \
"v_lshrrev_b32_e32 v236, 12, v224\n" \
"v_permlane32_swap_b32_e32 v184, v235\n" \
"v_mfma_f32_16x16x32_bf16 v[68:71], a[220:223], a[36:39], v[68:71]\n" \
"v_mov_b32_e64 v230, 1\n" \
"buffer_atomic_add v230, v225, %[srdz], s47 offen sc1\n" \
"v_mfma_f32_16x16x32_bf16 v[100:103], a[204:207], a[48:51], v[100:103]\n" \
"v_add_f32_e64 v184, v184, v235\n" \
"s_mul_i32 s61, %[wid], 4092\n" \
"v_mfma_f32_16x16x32_bf16 v[100:103], a[220:223], a[52:55], v[100:103]\n" \
"v_add_f32_dpp v184, v184, v184 quad_perm:[1,0,3,2] row_mask:0xf bank_mask:0xf\n" \
"s_sub_u32 s61, %[m0b], s61\n" \
"s_nop 0\n" \
"v_mfma_f32_16x16x32_bf16 v[4:7], a[212:215], a[8:11], v[4:7]\n" \
"v_add_f32_dpp v184, v184, v184 quad_perm:[2,3,0,1] row_mask:0xf bank_mask:0xf\n" \
"v_mfma_f32_16x16x32_bf16 v[4:7], a[208:211], a[12:15], v[4:7]\n" \
"v_lshl_add_u32 v236, v236, 4, s61\n" \
"v_mfma_f32_16x16x32_bf16 v[36:39], a[212:215], a[24:27], v[36:39]\n" \
"v_add_f32_dpp v184, v184, v184 row_ror:4 row_mask:0xf bank_mask:0xf\n" \
"v_mfma_f32_16x16x32_bf16 v[36:39], a[208:211], a[28:31], v[36:39]\n" \
"s_nop 0\n" \
"s_nop 0\n" \
"v_mfma_f32_16x16x32_bf16 v[68:71], a[212:215], a[40:43], v[68:71]\n" \
"s_waitcnt vmcnt(5) lgkmcnt(15)\n" \
"s_barrier\n" \
"v_mfma_f32_16x16x32_bf16 v[68:71], a[208:211], a[44:47], v[68:71]\n" \
"ds_read_b64_tr_b16 a[232:233], v215 offset:49152\n" \
"v_add_f32_dpp v184, v184, v184 row_ror:8 row_mask:0xf bank_mask:0xf\n" \
"v_mfma_f32_16x16x32_bf16 v[100:103], a[212:215], a[56:59], v[100:103]\n" \
"ds_read_b64_tr_b16 a[234:235], v215 offset:51200\n" \
"v_mov_b32_e64 v185, v184\n" \
"v_mfma_f32_16x16x32_bf16 v[100:103], a[208:211], a[60:63], v[100:103]\n" \
"s_waitcnt vmcnt(63) lgkmcnt(2)\n" \
"s_nop 0\n" \
"v_mfma_f32_16x16x32_bf16 v[12:15], a[216:219], a[0:3], v[12:15]\n" \
"ds_read_b64_tr_b16 a[196:197], v215 offset:53248\n" \
"s_nop 0\n" \
"s_nop 0\n" \
"v_mfma_f32_16x16x32_bf16 v[12:15], a[224:227], a[4:7], v[12:15]\n" \
"ds_read_b64_tr_b16 a[198:199], v215 offset:55296\n" \
"v_permlane16_swap_b32_e32 v184, v185\n" \
"s_nop 0\n" \
"v_mfma_f32_16x16x32_bf16 v[44:47], a[216:219], a[16:19], v[44:47]\n" \
"ds_read_b64_tr_b16 a[200:201], v215 offset:57344\n" \
"v_add_f32_e64 v184, v184, v185\n" \
"v_mfma_f32_16x16x32_bf16 v[44:47], a[224:227], a[20:23], v[44:47]\n" \
"ds_read_b64_tr_b16 a[202:203], v215 offset:59392\n" \
"v_bfe_u32 v186, v231, 3, 1\n" \
"v_mfma_f32_16x16x32_bf16 v[76:79], a[216:219], a[32:35], v[76:79]\n" \
"ds_read_b64_tr_b16 a[204:205], v215 offset:61440\n" \
"s_movk_i32 s61, 2040\n" \
"s_nop 0\n" \
"v_mfma_f32_16x16x32_bf16 v[76:79], a[224:227], a[36:39], v[76:79]\n" \
"ds_read_b64_tr_b16 a[206:207], v215 offset:63488\n" \
"v_mad_u32_u24 v186, v186, s61, v231\n" \
"v_mfma_f32_16x16x32_bf16 v[108:111], a[216:219], a[48:51], v[108:111]\n" \
"ds_read_b64_tr_b16 a[220:221], v227 offset:49152\n" \
"v_perm_b32 v196, v197, v196, s54\n" \
"v_mfma_f32_16x16x32_bf16 v[108:111], a[224:227], a[52:55], v[108:111]\n" \
"ds_read_b64_tr_b16 a[222:223], v227 offset:51200\n" \
"v_perm_b32 v197, v199, v198, s54\n" \
"v_mfma_f32_16x16x32_bf16 v[12:15], a[228:231], a[8:11], v[12:15]\n" \
"ds_read_b64_tr_b16 a[212:213], v227 offset:53248\n" \
"v_perm_b32 v200, v201, v200, s54\n" \
"v_mfma_f32_16x16x32_bf16 v[12:15], a[236:239], a[12:15], v[12:15]\n" \
"ds_read_b64_tr_b16 a[214:215], v227 offset:55296\n" \
"v_perm_b32 v201, v203, v202, s54\n" \
"v_mfma_f32_16x16x32_bf16 v[44:47], a[228:231], a[24:27], v[44:47]\n" \
"v_perm_b32 v204, v205, v204, s54\n" \
"v_perm_b32 v205, v207, v206, s54\n" \
"v_mfma_f32_16x16x32_bf16 v[44:47], a[236:239], a[28:31], v[44:47]\n" \
"ds_write_b32 v236, v184 offset:14368\n" \
"v_mfma_f32_16x16x32_bf16 v[76:79], a[228:231], a[40:43], v[76:79]\n" \
"ds_write_b32 v236, v234 offset:14336\n" \
"v_mfma_f32_16x16x32_bf16 v[76:79], a[236:239], a[44:47], v[76:79]\n" \
"ds_write_b128 v186, v[192:195] offset:6144\n" \
"v_mfma_f32_16x16x32_bf16 v[108:111], a[228:231], a[56:59], v[108:111]\n" \
"ds_read_b64_tr_b16 a[208:209], v227 offset:57344\n" \
"ds_write_b128 v186, v[188:191] offset:10240\n" \
"v_mfma_f32_16x16x32_bf16 v[108:111], a[236:239], a[60:63], v[108:111]\n" \
"s_waitcnt vmcnt(63) lgkmcnt(9)\n" \
"s_nop 0\n" \
"v_mfma_f32_16x16x32_bf16 v[4:7], a[232:235], a[128:131], v[4:7]\n" \
"ds_read_b64_tr_b16 a[210:211], v227 offset:59392\n" \
"ds_write_b64 v231, v[196:197] offset:0\n" \
"v_mfma_f32_16x16x32_bf16 v[4:7], a[196:199], a[132:135], v[4:7]\n" \
"ds_read_b64_tr_b16 a[216:217], v227 offset:61440\n" \
"ds_write_b64 v231, v[200:201] offset:2048\n" \
"v_mfma_f32_16x16x32_bf16 v[36:39], a[232:235], a[144:147], v[36:39]\n" \
"ds_read_b64_tr_b16 a[218:219], v227 offset:63488\n" \
"ds_write_b64 v231, v[204:205] offset:4096\n" \
"v_mfma_f32_16x16x32_bf16 v[36:39], a[196:199], a[148:151], v[36:39]\n" \
"v_mfma_f32_16x16x32_bf16 v[68:71], a[232:235], a[160:163], v[68:71]\n" \
"v_mfma_f32_16x16x32_bf16 v[68:71], a[196:199], a[164:167], v[68:71]\n" \
"v_mfma_f32_16x16x32_bf16 v[100:103], a[232:235], a[176:179], v[100:103]\n" \
"ds_read_b64_tr_b16 a[224:225], v215 offset:16896\n" \
"v_mfma_f32_16x16x32_bf16 v[100:103], a[196:199], a[180:183], v[100:103]\n" \
"ds_read_b64_tr_b16 a[226:227], v215 offset:18944\n" \
"v_mfma_f32_16x16x32_bf16 v[4:7], a[200:203], a[136:139], v[4:7]\n" \
"ds_read_b64_tr_b16 a[228:229], v215 offset:20992\n" \
"v_mfma_f32_16x16x32_bf16 v[4:7], a[204:207], a[140:143], v[4:7]\n" \
"ds_read_b64_tr_b16 a[230:231], v215 offset:23040\n" \
"v_mfma_f32_16x16x32_bf16 v[36:39], a[200:203], a[152:155], v[36:39]\n" \
"s_waitcnt vmcnt(63) lgkmcnt(4)\n" \
"s_barrier\n" \
"v_mfma_f32_16x16x32_bf16 v[36:39], a[204:207], a[156:159], v[36:39]\n" \
"v_mov_b32_e64 v192, s40\n" \
"v_mfma_f32_16x16x32_bf16 v[68:71], a[200:203], a[168:171], v[68:71]\n" \
"ds_read_b128 v[196:199], v192 offset:14368\n" \
"v_mfma_f32_16x16x32_bf16 v[68:71], a[204:207], a[172:175], v[68:71]\n" \
"ds_read_b128 v[128:131], %[kb0] offset:6144\n" \
"v_mfma_f32_16x16x32_bf16 v[100:103], a[200:203], a[184:187], v[100:103]\n" \
"ds_read_b64_tr_b16 a[236:237], v215 offset:25088\n" \
"ds_read_b128 v[132:135], %[kb0] offset:8192\n" \
"v_mfma_f32_16x16x32_bf16 v[100:103], a[204:207], a[188:191], v[100:103]\n" \
"s_waitcnt vmcnt(63) lgkmcnt(9)\n" \
"s_nop 0\n" \
"v_mfma_f32_16x16x32_bf16 v[12:15], a[220:223], a[128:131], v[12:15]\n" \
"ds_read_b64_tr_b16 a[238:239], v215 offset:27136\n" \
"ds_read_b128 v[136:139], %[kb0] offset:7168\n" \
"v_mfma_f32_16x16x32_bf16 v[12:15], a[212:215], a[132:135], v[12:15]\n" \
"ds_read_b64_tr_b16 a[232:233], v215 offset:29184\n" \
"ds_read_b128 v[140:143], %[kb0] offset:9216\n" \
"v_mfma_f32_16x16x32_bf16 v[44:47], a[220:223], a[144:147], v[44:47]\n" \
"ds_read_b64_tr_b16 a[234:235], v215 offset:31232\n" \
"ds_read_b128 v[144:147], %[vvb] offset:6144\n" \
"v_mfma_f32_16x16x32_bf16 v[44:47], a[212:215], a[148:151], v[44:47]\n" \
"ds_read_b128 v[148:151], %[vvb] offset:8192\n" \
"v_mfma_f32_16x16x32_bf16 v[76:79], a[220:223], a[160:163], v[76:79]\n" \
"ds_read_b128 v[152:155], %[vvb] offset:7168\n" \
"v_mfma_f32_16x16x32_bf16 v[76:79], a[212:215], a[164:167], v[76:79]\n" \
"ds_read_b128 v[156:159], %[vvb] offset:9216\n" \
"v_mfma_f32_16x16x32_bf16 v[108:111], a[220:223], a[176:179], v[108:111]\n" \
"ds_read_b64_tr_b16 a[196:197], v227 offset:16896\n" \
"ds_read_b128 v[160:163], %[kb0] offset:10240\n" \
"v_mfma_f32_16x16x32_bf16 v[108:111], a[212:215], a[180:183], v[108:111]\n" \
"ds_read_b64_tr_b16 a[198:199], v227 offset:18944\n" \
"ds_read_b128 v[164:167], %[kb0] offset:12288\n" \
"v_mfma_f32_16x16x32_bf16 v[12:15], a[208:211], a[136:139], v[12:15]\n" \
"ds_read_b64_tr_b16 a[200:201], v227 offset:20992\n" \
"ds_read_b128 v[168:171], %[kb0] offset:11264\n" \
"v_mfma_f32_16x16x32_bf16 v[12:15], a[216:219], a[140:143], v[12:15]\n" \
"ds_read_b64_tr_b16 a[202:203], v227 offset:23040\n" \
"ds_read_b128 v[172:175], %[kb0] offset:13312\n" \
"v_mfma_f32_16x16x32_bf16 v[44:47], a[208:211], a[152:155], v[44:47]\n" \
"ds_read_b128 v[176:179], %[vvb] offset:10240\n" \
"v_mfma_f32_16x16x32_bf16 v[44:47], a[216:219], a[156:159], v[44:47]\n" \
"ds_read_b128 v[180:183], %[vvb] offset:12288\n" \
"v_mfma_f32_16x16x32_bf16 v[76:79], a[208:211], a[168:171], v[76:79]\n" \
"v_mov_b32_e64 v230, 64\n" \
"buffer_atomic_add v230, v225, %[srdz], s47 offen sc1\n" \
"v_mfma_f32_16x16x32_bf16 v[76:79], a[216:219], a[172:175], v[76:79]\n" \
"ds_read_b128 v[184:187], %[vvb] offset:11264\n" \
"v_mfma_f32_16x16x32_bf16 v[108:111], a[208:211], a[184:187], v[108:111]\n" \
"ds_read_b64_tr_b16 a[204:205], v227 offset:25088\n" \
"ds_read_b128 v[188:191], %[vvb] offset:13312\n" \
"v_mfma_f32_16x16x32_bf16 v[108:111], a[216:219], a[188:191], v[108:111]\n" \
"s_waitcnt vmcnt(63) lgkmcnt(14)\n" \
"s_nop 0\n" \
"v_mfma_f32_16x16x32_bf16 v[20:23], a[224:227], v[0:3], v[20:23]\n" \
"ds_read_b64_tr_b16 a[206:207], v227 offset:27136\n" \
"s_waitcnt lgkmcnt(14)\n" \
"v_add_f32_e32 v223, v196, v197\n" \
"v_mfma_f32_16x16x32_bf16 v[20:23], a[228:231], v[8:11], v[20:23]\n" \
"ds_read_b64_tr_b16 a[220:221], v227 offset:29184\n" \
"v_add_f32_e64 v198, v198, v199\n" \
"v_mfma_f32_16x16x32_bf16 v[52:55], a[224:227], v[32:35], v[52:55]\n" \
"ds_read_b64_tr_b16 a[222:223], v227 offset:31232\n" \
"v_add_f32_e64 v223, v223, v198\n" \
"v_mfma_f32_16x16x32_bf16 v[52:55], a[228:231], v[40:43], v[52:55]\n" \
"ds_read_b128 v[192:195], %[kb0] offset:2048\n" \
"v_mfma_f32_16x16x32_bf16 v[84:87], a[224:227], v[64:67], v[84:87]\n" \
"v_cvt_pk_bf16_f32 v4, v4, v5\n" \
"ds_read_b128 v[196:199], %[kb0] offset:3072\n" \
"v_mfma_f32_16x16x32_bf16 v[84:87], a[228:231], v[72:75], v[84:87]\n" \
"v_cvt_pk_bf16_f32 v5, v6, v7\n" \
"ds_read_b128 v[200:203], %[vvb] offset:2048\n" \
"v_mfma_f32_16x16x32_bf16 v[116:119], a[224:227], v[96:99], v[116:119]\n" \
"ds_read_b64_tr_b16 a[212:213], v215 offset:33280\n" \
"v_cvt_pk_bf16_f32 v6, v12, v13\n" \
"v_mfma_f32_16x16x32_bf16 v[116:119], a[228:231], v[104:107], v[116:119]\n" \
"ds_read_b64_tr_b16 a[214:215], v215 offset:35328\n" \
"v_cvt_pk_bf16_f32 v7, v14, v15\n" \
"v_mfma_f32_16x16x32_bf16 v[20:23], a[236:239], v[16:19], v[20:23]\n" \
"ds_read_b64_tr_b16 a[208:209], v215 offset:37376\n" \
"ds_read_b128 v[204:207], %[vvb] offset:3072\n" \
"v_mfma_f32_16x16x32_bf16 v[20:23], a[232:235], v[24:27], v[20:23]\n" \
"ds_read_b64_tr_b16 a[210:211], v215 offset:39424\n" \
"v_mfma_f32_16x16x32_bf16 v[52:55], a[236:239], v[48:51], v[52:55]\n" \
"s_add_u32 s55, s46, 8192\n" \
"buffer_store_dwordx4 v[4:7], %[vxs], %[srdz], s55 offen offset:0 sc1\n" \
"v_mfma_f32_16x16x32_bf16 v[52:55], a[232:235], v[56:59], v[52:55]\n" \
"v_cvt_pk_bf16_f32 v36, v36, v37\n" \
"v_cvt_pk_bf16_f32 v37, v38, v39\n" \
"v_mfma_f32_16x16x32_bf16 v[84:87], a[236:239], v[80:83], v[84:87]\n" \
"v_cvt_pk_bf16_f32 v38, v44, v45\n" \
"v_mfma_f32_16x16x32_bf16 v[84:87], a[232:235], v[88:91], v[84:87]\n" \
"v_cvt_pk_bf16_f32 v39, v46, v47\n" \
"v_mfma_f32_16x16x32_bf16 v[116:119], a[236:239], v[112:115], v[116:119]\n" \
"ds_read_b64_tr_b16 a[216:217], v215 offset:41472\n" \
"v_mfma_f32_16x16x32_bf16 v[116:119], a[232:235], v[120:123], v[116:119]\n" \
"s_waitcnt vmcnt(63) lgkmcnt(9)\n" \
"s_nop 0\n" \
"v_mfma_f32_16x16x32_bf16 v[28:31], a[196:199], v[0:3], v[28:31]\n" \
"ds_read_b64_tr_b16 a[218:219], v215 offset:43520\n" \
"v_cvt_pk_bf16_f32 v68, v68, v69\n" \
"v_mfma_f32_16x16x32_bf16 v[28:31], a[200:203], v[8:11], v[28:31]\n" \
"ds_read_b64_tr_b16 a[224:225], v215 offset:45568\n" \
"s_add_u32 s55, s46, 312320\n" \
"buffer_store_dwordx4 v[36:39], %[vxs], %[srdz], s55 offen offset:0 sc1\n" \
"v_mfma_f32_16x16x32_bf16 v[60:63], a[196:199], v[32:35], v[60:63]\n" \
"ds_read_b64_tr_b16 a[226:227], v215 offset:47616\n" \
"v_cvt_pk_bf16_f32 v69, v70, v71\n" \
"v_mfma_f32_16x16x32_bf16 v[60:63], a[200:203], v[40:43], v[60:63]\n" \
"v_cvt_pk_bf16_f32 v70, v76, v77\n" \
"v_cvt_pk_bf16_f32 v71, v78, v79\n" \
"v_mfma_f32_16x16x32_bf16 v[92:95], a[196:199], v[64:67], v[92:95]\n" \
"s_add_u32 s55, s46, 616448\n" \
"buffer_store_dwordx4 v[68:71], %[vxs], %[srdz], s55 offen offset:0 sc1\n" \
"v_mfma_f32_16x16x32_bf16 v[92:95], a[200:203], v[72:75], v[92:95]\n" \
"v_accvgpr_read_b32 v1, a240\n" \
"v_mfma_f32_16x16x32_bf16 v[124:127], a[196:199], v[96:99], v[124:127]\n" \
"ds_read_b64_tr_b16 a[228:229], v227 offset:33280\n" \
"v_cvt_pk_bf16_f32 v100, v100, v101\n" \
"v_mfma_f32_16x16x32_bf16 v[124:127], a[200:203], v[104:107], v[124:127]\n" \
"ds_read_b64_tr_b16 a[230:231], v227 offset:35328\n" \
"v_cvt_pk_bf16_f32 v101, v102, v103\n" \
"v_mfma_f32_16x16x32_bf16 v[28:31], a[204:207], v[16:19], v[28:31]\n" \
"ds_read_b64_tr_b16 a[236:237], v227 offset:37376\n" \
"v_cvt_pk_bf16_f32 v102, v108, v109\n" \
"v_readlane_b32 s68, %[thi], 18\n" \
"v_lshlrev_b32_e64 v226, 7, v226\n" \
"v_mfma_f32_16x16x32_bf16 v[28:31], a[220:223], v[24:27], v[28:31]\n" \
"ds_read_b64_tr_b16 a[238:239], v227 offset:39424\n" \
"v_cvt_pk_bf16_f32 v103, v110, v111\n" \
"v_mfma_f32_16x16x32_bf16 v[60:63], a[204:207], v[48:51], v[60:63]\n" \
"s_add_u32 s55, s46, 920576\n" \
"buffer_store_dwordx4 v[100:103], %[vxs], %[srdz], s55 offen offset:0 sc1\n" \
"v_mfma_f32_16x16x32_bf16 v[60:63], a[220:223], v[56:59], v[60:63]\n" \
"v_lshlrev_b32_e64 v0, 16, v1\n" \
"v_mfma_f32_16x16x32_bf16 v[92:95], a[204:207], v[80:83], v[92:95]\n" \
"v_and_b32_e64 v1, s43, v1\n" \
"v_mfma_f32_16x16x32_bf16 v[92:95], a[220:223], v[88:91], v[92:95]\n" \
"v_accvgpr_read_b32 v3, a241\n" \
"v_mfma_f32_16x16x32_bf16 v[124:127], a[204:207], v[112:115], v[124:127]\n" \
"ds_read_b64_tr_b16 a[232:233], v227 offset:41472\n" \
"v_lshlrev_b32_e64 v2, 16, v3\n" \
"v_mfma_f32_16x16x32_bf16 v[124:127], a[220:223], v[120:123], v[124:127]\n" \
"s_waitcnt vmcnt(63) lgkmcnt(5)\n" \
"s_nop 0\n" \
"v_mfma_f32_16x16x32_bf16 v[20:23], a[212:215], a[0:3], v[20:23]\n" \
"ds_read_b64_tr_b16 a[234:235], v227 offset:43520\n" \
"v_and_b32_e64 v3, s43, v3\n" \
"v_mfma_f32_16x16x32_bf16 v[20:23], a[208:211], a[4:7], v[20:23]\n" \
"ds_read_b64_tr_b16 a[196:197], v227 offset:45568\n" \
"v_accvgpr_read_b32 v9, a244\n" \
"v_mfma_f32_16x16x32_bf16 v[52:55], a[212:215], a[16:19], v[52:55]\n" \
"ds_read_b64_tr_b16 a[198:199], v227 offset:47616\n" \
"v_lshlrev_b32_e64 v8, 16, v9\n" \
"v_mfma_f32_16x16x32_bf16 v[52:55], a[208:211], a[20:23], v[52:55]\n" \
"buffer_load_dword v216, v226, %[srdz], s68 offen offset:64 sc1\n" \
"v_mfma_f32_16x16x32_bf16 v[84:87], a[212:215], a[32:35], v[84:87]\n" \
"v_mov_b32_e32 v226, 0\n" \
"v_lshrrev_b32_e32 v217, 3, v224\n" \
"s_add_u32 s68, %[sin], 4096\n" \
"v_and_b32_e64 v9, s43, v9\n" \
"v_mfma_f32_16x16x32_bf16 v[84:87], a[208:211], a[36:39], v[84:87]\n" \
"v_accvgpr_read_b32 v11, a245\n" \
"v_mfma_f32_16x16x32_bf16 v[116:119], a[212:215], a[48:51], v[116:119]\n" \
"ds_read_b64_tr_b16 a[200:201], v215 offset:49664\n" \
"v_lshlrev_b32_e64 v10, 16, v11\n" \
"v_mfma_f32_16x16x32_bf16 v[116:119], a[208:211], a[52:55], v[116:119]\n" \
"ds_read_b64_tr_b16 a[202:203], v215 offset:51712\n" \
"v_and_b32_e64 v11, s43, v11\n" \
"v_mfma_f32_16x16x32_bf16 v[20:23], a[216:219], a[8:11], v[20:23]\n" \
"ds_read_b64_tr_b16 a[204:205], v215 offset:53760\n" \
"v_accvgpr_read_b32 v33, a248\n" \
"v_mfma_f32_16x16x32_bf16 v[20:23], a[224:227], a[12:15], v[20:23]\n" \
"ds_read_b64_tr_b16 a[206:207], v215 offset:55808\n" \
"v_lshlrev_b32_e64 v32, 16, v33\n" \
"v_mfma_f32_16x16x32_bf16 v[52:55], a[216:219], a[24:27], v[52:55]\n" \
"ds_read_b64_tr_b16 a[220:221], v215 offset:57856\n" \
"v_and_b32_e64 v33, s43, v33\n" \
"v_mfma_f32_16x16x32_bf16 v[52:55], a[224:227], a[28:31], v[52:55]\n" \
"ds_read_b64_tr_b16 a[222:223], v215 offset:59904\n" \
"v_accvgpr_read_b32 v35, a249\n" \
"v_mfma_f32_16x16x32_bf16 v[84:87], a[216:219], a[40:43], v[84:87]\n" \
"ds_read_b64_tr_b16 a[212:213], v215 offset:61952\n" \
"v_lshlrev_b32_e64 v34, 16, v35\n" \
"v_mfma_f32_16x16x32_bf16 v[84:87], a[224:227], a[44:47], v[84:87]\n" \
"ds_read_b64_tr_b16 a[214:215], v215 offset:64000\n" \
"v_and_b32_e64 v35, s43, v35\n" \
"v_mfma_f32_16x16x32_bf16 v[116:119], a[216:219], a[56:59], v[116:119]\n" \
"ds_read_b64_tr_b16 a[208:209], v227 offset:49664\n" \
"v_accvgpr_read_b32 v41, a252\n" \
"v_mfma_f32_16x16x32_bf16 v[116:119], a[224:227], a[60:63], v[116:119]\n" \
"s_waitcnt vmcnt(63) lgkmcnt(9)\n" \
"s_nop 0\n" \
"v_mfma_f32_16x16x32_bf16 v[28:31], a[228:231], a[0:3], v[28:31]\n" \
"ds_read_b64_tr_b16 a[210:211], v227 offset:51712\n" \
"v_lshlrev_b32_e64 v40, 16, v41\n" \
"v_mfma_f32_16x16x32_bf16 v[28:31], a[236:239], a[4:7], v[28:31]\n" \
"ds_read_b64_tr_b16 a[216:217], v227 offset:53760\n" \
"v_and_b32_e64 v41, s43, v41\n" \
"v_mfma_f32_16x16x32_bf16 v[60:63], a[228:231], a[16:19], v[60:63]\n" \
"ds_read_b64_tr_b16 a[218:219], v227 offset:55808\n" \
"v_accvgpr_read_b32 v43, a253\n" \
"v_mfma_f32_16x16x32_bf16 v[60:63], a[236:239], a[20:23], v[60:63]\n" \
"ds_read_b64_tr_b16 a[224:225], v227 offset:57856\n" \
"v_lshlrev_b32_e64 v42, 16, v43\n" \
"v_mfma_f32_16x16x32_bf16 v[92:95], a[228:231], a[32:35], v[92:95]\n" \
"ds_read_b64_tr_b16 a[226:227], v227 offset:59904\n" \
"v_and_b32_e64 v43, s43, v43\n" \
"v_mfma_f32_16x16x32_bf16 v[92:95], a[236:239], a[36:39], v[92:95]\n" \
"v_accvgpr_read_b32 v17, a242\n" \
"v_mfma_f32_16x16x32_bf16 v[124:127], a[228:231], a[48:51], v[124:127]\n" \
"v_lshlrev_b32_e64 v16, 16, v17\n" \
"v_mfma_f32_16x16x32_bf16 v[124:127], a[236:239], a[52:55], v[124:127]\n" \
"ds_read_b64_tr_b16 a[228:229], v227 offset:61952\n" \
"v_and_b32_e64 v17, s43, v17\n" \
"v_mfma_f32_16x16x32_bf16 v[28:31], a[232:235], a[8:11], v[28:31]\n" \
"ds_read_b64_tr_b16 a[230:231], v227 offset:64000\n" \
"v_accvgpr_read_b32 v19, a243\n" \
"v_mfma_f32_16x16x32_bf16 v[28:31], a[196:199], a[12:15], v[28:31]\n" \
"v_lshlrev_b32_e64 v18, 16, v19\n" \
"v_mfma_f32_16x16x32_bf16 v[60:63], a[232:235], a[24:27], v[60:63]\n" \
"v_and_b32_e64 v19, s43, v19\n" \
"v_mfma_f32_16x16x32_bf16 v[60:63], a[196:199], a[28:31], v[60:63]\n" \
"v_accvgpr_read_b32 v25, a246\n" \
"v_mfma_f32_16x16x32_bf16 v[92:95], a[232:235], a[40:43], v[92:95]\n" \
"v_lshlrev_b32_e64 v24, 16, v25\n" \
"v_mfma_f32_16x16x32_bf16 v[92:95], a[196:199], a[44:47], v[92:95]\n" \
"v_and_b32_e64 v25, s43, v25\n" \
"v_mfma_f32_16x16x32_bf16 v[124:127], a[232:235], a[56:59], v[124:127]\n" \
"v_accvgpr_read_b32 v27, a247\n" \
"v_mfma_f32_16x16x32_bf16 v[124:127], a[196:199], a[60:63], v[124:127]\n" \
"s_waitcnt vmcnt(63) lgkmcnt(0)\n" \
"s_nop 0\n" \
"v_mfma_f32_16x16x32_bf16 v[20:23], a[200:203], a[128:131], v[20:23]\n" \
"v_lshlrev_b32_e64 v26, 16, v27\n" \
"v_mfma_f32_16x16x32_bf16 v[20:23], a[204:207], a[132:135], v[20:23]\n" \
"v_and_b32_e64 v27, s43, v27\n" \
"v_mfma_f32_16x16x32_bf16 v[20:23], a[220:223], a[136:139], v[20:23]\n" \
"v_accvgpr_read_b32 v49, a250\n" \
"v_mfma_f32_16x16x32_bf16 v[20:23], a[212:215], a[140:143], v[20:23]\n" \
"v_lshlrev_b32_e64 v48, 16, v49\n" \
"v_mfma_f32_16x16x32_bf16 v[28:31], a[208:211], a[128:131], v[28:31]\n" \
"v_and_b32_e64 v49, s43, v49\n" \
"v_mfma_f32_16x16x32_bf16 v[28:31], a[216:219], a[132:135], v[28:31]\n" \
"v_accvgpr_read_b32 v51, a251\n" \
"v_mfma_f32_16x16x32_bf16 v[28:31], a[224:227], a[136:139], v[28:31]\n" \
"v_lshlrev_b32_e64 v50, 16, v51\n" \
"v_mfma_f32_16x16x32_bf16 v[28:31], a[228:231], a[140:143], v[28:31]\n" \
"v_and_b32_e64 v51, s43, v51\n" \
"v_mfma_f32_16x16x32_bf16 v[52:55], a[200:203], a[144:147], v[52:55]\n" \
"v_accvgpr_read_b32 v57, a254\n" \
"v_mfma_f32_16x16x32_bf16 v[52:55], a[204:207], a[148:151], v[52:55]\n" \
"v_lshlrev_b32_e64 v56, 16, v57\n" \
"v_mfma_f32_16x16x32_bf16 v[52:55], a[220:223], a[152:155], v[52:55]\n" \
"v_and_b32_e64 v57, s43, v57\n" \
"v_mfma_f32_16x16x32_bf16 v[52:55], a[212:215], a[156:159], v[52:55]\n" \
"v_cvt_pk_bf16_f32 v20, v20, v21\n" \
"v_cvt_pk_bf16_f32 v21, v22, v23\n" \
"v_accvgpr_read_b32 v59, a255\n" \
"v_mfma_f32_16x16x32_bf16 v[60:63], a[208:211], a[144:147], v[60:63]\n" \
"v_lshlrev_b32_e64 v58, 16, v59\n" \
"v_mfma_f32_16x16x32_bf16 v[60:63], a[216:219], a[148:151], v[60:63]\n" \
"v_cvt_pk_bf16_f32 v22, v28, v29\n" \
"v_cvt_pk_bf16_f32 v23, v30, v31\n" \
"v_and_b32_e64 v59, s43, v59\n" \
"v_mfma_f32_16x16x32_bf16 v[60:63], a[224:227], a[152:155], v[60:63]\n" \
"s_add_u32 s55, s46, 12288\n" \
"buffer_store_dwordx4 v[20:23], %[vxs], %[srdz], s55 offen offset:0 sc1\n" \
"v_mfma_f32_16x16x32_bf16 v[60:63], a[228:231], a[156:159], v[60:63]\n" \
"v_mfma_f32_16x16x32_bf16 v[84:87], a[200:203], a[160:163], v[84:87]\n" \
"v_mfma_f32_16x16x32_bf16 v[84:87], a[204:207], a[164:167], v[84:87]\n" \
"v_mfma_f32_16x16x32_bf16 v[84:87], a[220:223], a[168:171], v[84:87]\n" \
"v_mfma_f32_16x16x32_bf16 v[84:87], a[212:215], a[172:175], v[84:87]\n" \
"v_cvt_pk_bf16_f32 v52, v52, v53\n" \
"v_cvt_pk_bf16_f32 v53, v54, v55\n" \
"v_mfma_f32_16x16x32_bf16 v[92:95], a[208:211], a[160:163], v[92:95]\n" \
"v_mfma_f32_16x16x32_bf16 v[92:95], a[216:219], a[164:167], v[92:95]\n" \
"v_cvt_pk_bf16_f32 v54, v60, v61\n" \
"v_cvt_pk_bf16_f32 v55, v62, v63\n" \
"v_mfma_f32_16x16x32_bf16 v[92:95], a[224:227], a[168:171], v[92:95]\n" \
"s_add_u32 s55, s46, 316416\n" \
"buffer_store_dwordx4 v[52:55], %[vxs], %[srdz], s55 offen offset:0 sc1\n" \
"v_mfma_f32_16x16x32_bf16 v[92:95], a[228:231], a[172:175], v[92:95]\n" \
"v_mfma_f32_16x16x32_bf16 v[116:119], a[200:203], a[176:179], v[116:119]\n" \
"v_mfma_f32_16x16x32_bf16 v[116:119], a[204:207], a[180:183], v[116:119]\n" \
"v_mfma_f32_16x16x32_bf16 v[116:119], a[220:223], a[184:187], v[116:119]\n" \
"v_mfma_f32_16x16x32_bf16 v[116:119], a[212:215], a[188:191], v[116:119]\n" \
"v_cvt_pk_bf16_f32 v84, v84, v85\n" \
"v_cvt_pk_bf16_f32 v85, v86, v87\n" \
"v_mfma_f32_16x16x32_bf16 v[124:127], a[208:211], a[176:179], v[124:127]\n" \
"v_mfma_f32_16x16x32_bf16 v[124:127], a[216:219], a[180:183], v[124:127]\n" \
"v_cvt_pk_bf16_f32 v86, v92, v93\n" \
"v_cvt_pk_bf16_f32 v87, v94, v95\n" \
"v_mfma_f32_16x16x32_bf16 v[124:127], a[224:227], a[184:187], v[124:127]\n" \
"s_add_u32 s55, s46, 620544\n" \
"buffer_store_dwordx4 v[84:87], %[vxs], %[srdz], s55 offen offset:0 sc1\n" \
"v_mfma_f32_16x16x32_bf16 v[124:127], a[228:231], a[188:191], v[124:127]\n" \
"v_cvt_pk_bf16_f32 v116, v116, v117\n" \
"v_cvt_pk_bf16_f32 v117, v118, v119\n" \
"s_waitcnt lgkmcnt(0)\n" \
"s_nop 4\n" \
"v_cvt_pk_bf16_f32 v118, v124, v125\n" \
"v_cvt_pk_bf16_f32 v119, v126, v127\n" \
"s_add_u32 s55, s46, 924672\n" \
"buffer_store_dwordx4 v[116:119], %[vxs], %[srdz], s55 offen offset:0 sc1\n" \
"buffer_load_dword v229, v226, %[srdz], s53 offen sc1\n" \
"v_readlane_b32 s62, %[tlo], 35\n" \
"v_lshrrev_b32_e32 v72, 2, v224\n" \
"s_add_u32 s54, %[sin], 8192\n" \
"s_add_u32 s55, %[sin], 12288\n" \
"s_movk_i32 s56, 0x1000\n" \
"s_movk_i32 s57, 0x2000\n" \
"s_movk_i32 s58, 0x3000\n" \
"buffer_load_dwordx4 v[64:67], v72, %[srdz], s62 offen offset:0 sc1\n" \
"buffer_load_dwordx4 v[68:71], v72, %[srdz], s62 offen offset:16 sc1\n" \
"v_mov_b32_e32 v226, v211\n" \
"v_bfe_u32 v221, v222, 3, 1\n" \
"v_and_b32_e32 v208, 1, v222\n" \
"v_lshl_or_b32 v221, v221, 1, v208\n" \
"s_movk_i32 s63, 272\n" \
"v_mad_u32_u24 v221, v221, s63, %[vx0]\n" \
"v_readlane_b32 s64, %[tlo], 25\n" \
"v_readlane_b32 s65, %[thi], 25\n" \
"s_mov_b32 s66, 0x400000\n" \
"s_mov_b32 s67, 0x20000\n" \
"v_readlane_b32 s48, %[tlo], 27\n" \
"v_readlane_b32 s49, %[thi], 27\n" \
"v_readlane_b32 s69, %[tlo], 21\n" \
"v_accvgpr_write_b32 a128, -1\n" \
"v_lshrrev_b32_e32 v214, 4, v224\n" \
"s_add_u32 s62, %[m0b], 65536\n" \
"v_add_u32_e32 v215, %[m0t], v214\n" \
"v_add_u32_e32 v214, s62, v214\n" \
"ds_read_b64_tr_b16 a[64:65], v214 offset:0\n" \
"v_and_b32_e32 v4, 1, v222\n" \
"ds_read_b64_tr_b16 a[66:67], v214 offset:1024\n" \
"v_bfe_u32 v5, v222, 1, 3\n" \
"ds_read_b64_tr_b16 a[96:97], v215 offset:0\n" \
"v_lshlrev_b32_e32 v4, 4, v4\n" \
"ds_read_b64_tr_b16 a[98:99], v215 offset:1024\n" \
"v_mov_b32_e32 v6, 0x3f80\n" \
"ds_read_b64_tr_b16 a[68:69], v214 offset:512\n" \
"v_lshlrev_b32_e64 v5, v5, 1\n" \
"ds_read_b64_tr_b16 a[70:71], v214 offset:1536\n" \
"v_lshlrev_b32_e32 v4, v4, v6\n" \
"ds_read_b64_tr_b16 a[100:101], v215 offset:512\n" \
"v_bfe_i32 v230, v5, 0, 1\n" \
"ds_read_b64_tr_b16 a[102:103], v215 offset:1536\n" \
"v_bfe_i32 v231, v5, 1, 1\n" \
"ds_read_b64_tr_b16 a[72:73], v214 offset:16384\n" \
"v_bfe_i32 v232, v5, 2, 1\n" \
"ds_read_b64_tr_b16 a[74:75], v214 offset:17408\n" \
"v_bfe_i32 v233, v5, 3, 1\n" \
"ds_read_b64_tr_b16 a[104:105], v215 offset:4096\n" \
"v_bfe_i32 v234, v5, 4, 1\n" \
"ds_read_b64_tr_b16 a[106:107], v215 offset:5120\n" \
"v_bfe_i32 v235, v5, 5, 1\n" \
"ds_read_b64_tr_b16 a[76:77], v214 offset:16896\n" \
"v_bfe_i32 v236, v5, 6, 1\n" \
"ds_read_b64_tr_b16 a[78:79], v214 offset:17920\n" \
"v_bfe_i32 v237, v5, 7, 1\n" \
"ds_read_b64_tr_b16 a[108:109], v215 offset:4608\n" \
"v_and_b32_e32 v230, v4, v230\n" \
"ds_read_b64_tr_b16 a[110:111], v215 offset:5632\n" \
"v_and_b32_e32 v231, v4, v231\n" \
"ds_read_b64_tr_b16 a[80:81], v214 offset:2048\n" \
"v_and_b32_e32 v232, v4, v232\n" \
"ds_read_b64_tr_b16 a[82:83], v214 offset:3072\n" \
"v_and_b32_e32 v233, v4, v233\n" \
"ds_read_b64_tr_b16 a[112:113], v215 offset:2048\n" \
"v_and_b32_e32 v234, v4, v234\n" \
"ds_read_b64_tr_b16 a[114:115], v215 offset:3072\n" \
"v_and_b32_e32 v235, v4, v235\n" \
"ds_read_b64_tr_b16 a[84:85], v214 offset:2560\n" \
"v_and_b32_e32 v236, v4, v236\n" \
".if . - adaln_body != 61448\n" \
".error \"size model\"\n" \
".endif\n" \
"Lk14_%=:\n" \
"s_cbranch_execz Lk15_%=\n" \
"s_nop 0\n" \
"ds_read_b64_tr_b16 a[86:87], v214 offset:3584\n" \
"v_and_b32_e32 v237, v4, v237\n" \
"ds_read_b64_tr_b16 a[116:117], v215 offset:2560\n" \
"ds_read_b64_tr_b16 a[118:119], v215 offset:3584\n" \
"ds_read_b64_tr_b16 a[88:89], v214 offset:18432\n" \
"ds_read_b64_tr_b16 a[90:91], v214 offset:19456\n" \
"ds_read_b64_tr_b16 a[120:121], v215 offset:6144\n" \
"ds_read_b64_tr_b16 a[122:123], v215 offset:7168\n" \
"ds_read_b64_tr_b16 a[92:93], v214 offset:18944\n" \
"ds_read_b64_tr_b16 a[94:95], v214 offset:19968\n" \
"ds_read_b64_tr_b16 a[124:125], v215 offset:6656\n" \
"ds_read_b64_tr_b16 a[126:127], v215 offset:7680\n" \
"v_lshrrev_b32_e32 v208, 1, v217\n" \
"v_and_b32_e32 v210, 24, v208\n" \
"v_bfe_u32 v215, v222, 4, 1\n" \
"v_lshl_or_b32 v210, v215, 14, v210\n" \
"v_lshl_or_b32 v210, v215, 7, v210\n" \
"v_bfe_u32 v215, v222, 2, 2\n" \
"v_lshl_or_b32 v210, v215, 9, v210\n" \
"v_lshrrev_b32_e32 v211, 5, v222\n" \
"v_xor_b32_e32 v215, v215, v211\n" \
"v_lshl_or_b32 v210, v215, 5, v210\n" \
"s_add_u32 s62, %[m0b], 98304\n" \
"v_add_u32_e32 v210, s62, v210\n" \
"v_xor_b32_e32 v211, 64, v210\n" \
"v_xor_b32_e32 v213, 0x80, v210\n" \
"v_xor_b32_e32 v214, 0xc0, v210\n" \
"v_readlane_b32 s62, %[thi], 28\n" \
"v_and_b32_e32 v100, 7, v222\n" \
"v_lshrrev_b32_e32 v103, 1, v222\n" \
"v_and_b32_e32 v101, 1, v222\n" \
"v_lshlrev_b32_e32 v103, 13, v103\n" \
"v_lshl_or_b32 v103, v101, 4, v103\n" \
"v_add_u32_e32 v100, s62, v100\n" \
"v_accvgpr_write_b32 a34, v103\n" \
"v_lshrrev_b32_e32 v101, 8, v100\n" \
"v_lshlrev_b32_e32 v101, 7, v101\n" \
"v_bfe_u32 v103, v100, 6, 2\n" \
"v_lshl_or_b32 v101, v103, 5, v101\n" \
"v_bfe_u32 v103, v100, 1, 1\n" \
"v_lshl_or_b32 v101, v103, 4, v101\n" \
"v_bfe_u32 v103, v100, 2, 4\n" \
"v_or_b32_e32 v101, v103, v101\n" \
"v_lshrrev_b32_e32 v102, 8, v101\n" \
"v_lshlrev_b32_e32 v102, 9, v102\n" \
"v_accvgpr_write_b32 a32, v101\n" \
"v_bfe_u32 v103, v101, 6, 1\n" \
"v_lshl_or_b32 v102, v103, 8, v102\n" \
"v_bfe_u32 v103, v101, 7, 1\n" \
"v_lshl_or_b32 v102, v103, 7, v102\n" \
"v_bfe_u32 v103, v101, 4, 1\n" \
"v_lshl_or_b32 v102, v103, 6, v102\n" \
"v_bfe_u32 v103, v101, 2, 2\n" \
"v_lshl_or_b32 v102, v103, 4, v102\n" \
"v_bfe_u32 v103, v101, 5, 1\n" \
"v_lshl_or_b32 v102, v103, 3, v102\n" \
"v_bfe_u32 v103, v101, 0, 2\n" \
"v_lshl_or_b32 v102, v103, 1, v102\n" \
"v_accvgpr_write_b32 a33, v102\n" \
"s_mov_b32 s51, 0x10000\n" \
"s_mov_b32 s50, 0\n" \
"s_waitcnt vmcnt(7)\n" \
"v_cmp_eq_u32_e32 vcc, 0, v216\n" \
"s_mov_b32 %[got], 0\n" \
"s_cbranch_vccnz TLf1_%=\n" \
"s_mov_b32 %[got], 1\n" \
"buffer_load_dwordx4 a[196:199], v217, %[srdz], %[sin] offen offset:0 sc1\n" \
"buffer_load_dwordx4 a[200:203], v217, %[srdz], %[sin] offen offset:1024 sc1\n" \
"buffer_load_dwordx4 a[204:207], v217, %[srdz], %[sin] offen offset:2048 sc1\n" \
"buffer_load_dwordx4 a[208:211], v217, %[srdz], %[sin] offen offset:3072 sc1\n" \
"buffer_load_dwordx4 a[212:215], v217, %[srdz], s68 offen offset:0 sc1\n" \
"buffer_load_dwordx4 a[216:219], v217, %[srdz], s68 offen offset:1024 sc1\n" \
"buffer_load_dwordx4 a[220:223], v217, %[srdz], s68 offen offset:2048 sc1\n" \
"buffer_load_dwordx4 a[224:227], v217, %[srdz], s68 offen offset:3072 sc1\n" \
"buffer_load_dwordx4 a[164:167], v217, %[srdz], s54 offen offset:0 sc1\n" \
"buffer_load_dwordx4 a[168:171], v217, %[srdz], s54 offen offset:1024 sc1\n" \
"buffer_load_dwordx4 a[172:175], v217, %[srdz], s54 offen offset:2048 sc1\n" \
"buffer_load_dwordx4 a[176:179], v217, %[srdz], s54 offen offset:3072 sc1\n" \
"TLf1_%=:\n" \
"ds_read_b64_tr_b16 a[0:1], v210 offset:0\n" \
"ds_read_b64_tr_b16 a[2:3], v213 offset:2048\n" \
"ds_read_b64_tr_b16 a[4:5], v211 offset:0\n" \
"ds_read_b64_tr_b16 a[6:7], v214 offset:2048\n" \
"ds_read_b64_tr_b16 a[8:9], v213 offset:0\n" \
"ds_read_b64_tr_b16 a[10:11], v210 offset:2048\n" \
"ds_read_b64_tr_b16 a[12:13], v214 offset:0\n" \
"ds_read_b64_tr_b16 a[14:15], v211 offset:2048\n" \
"ds_read_b64_tr_b16 a[16:17], v210 offset:256\n" \
"ds_read_b64_tr_b16 a[18:19], v213 offset:2304\n" \
"ds_read_b64_tr_b16 a[20:21], v211 offset:256\n" \
"ds_read_b64_tr_b16 a[22:23], v214 offset:2304\n" \
"ds_read_b64_tr_b16 a[24:25], v213 offset:256\n" \
"ds_read_b64_tr_b16 a[26:27], v210 offset:2304\n" \
"ds_read_b64_tr_b16 a[28:29], v214 offset:256\n" \
"ds_read_b64_tr_b16 a[30:31], v211 offset:2304\n" \
"s_waitcnt vmcnt(12)\n" \
"s_cmp_lg_u32 %[got], 0\n" \
"s_cbranch_scc1 TLrp4_%=\n" \
"s_waitcnt vmcnt(0)\n" \
"v_and_b32_e32 v208, 32, v229\n" \
"v_cmp_eq_u32_e32 vcc, 0, v208\n" \
"s_cbranch_vccz TLrl2_%=\n" \
"v_mov_b32_e32 v216, s40\n" \
"TLrq2_%=:\n" \
"buffer_load_dword v225, off, %[srdz], s53 offset:0 sc1\n" \
"s_mov_b32 s52, 10\n" \
"TLrs2_%=:\n" \
"ds_read_b32 v229, v216 offset:14336\n" \
"s_waitcnt lgkmcnt(0)\n" \
"v_and_b32_e32 v208, 32, v229\n" \
"v_cmp_eq_u32_e32 vcc, 0, v208\n" \
"s_cbranch_vccz TLrd2_%=\n" \
"s_sub_u32 s52, s52, 1\n" \
"s_cbranch_scc0 TLrs2_%=\n" \
"s_sub_u32 s51, s51, 1\n" \
"s_cselect_b32 s51, 0, s51\n" \
"s_waitcnt vmcnt(0)\n" \
"v_and_b32_e32 v208, 32, v225\n" \
"v_cmp_eq_u32_e32 vcc, 0, v208\n" \
"s_cbranch_scc1 TLrl2_%=\n" \
"s_cbranch_vccnz TLrq2_%=\n" \
"v_mov_b32_e32 v229, v225\n" \
"s_mov_b64 exec, 1\n" \
"ds_max_u32 v216, v225 offset:14336\n" \
"s_mov_b64 exec, -1\n" \
"TLrd2_%=:\n" \
"s_sleep 0\n" \
"TLrl2_%=:\n" \
"buffer_load_dwordx4 a[196:199], v217, %[srdz], %[sin] offen offset:0 sc1\n" \
"buffer_load_dwordx4 a[200:203], v217, %[srdz], %[sin] offen offset:1024 sc1\n" \
"buffer_load_dwordx4 a[204:207], v217, %[srdz], %[sin] offen offset:2048 sc1\n" \
"buffer_load_dwordx4 a[208:211], v217, %[srdz], %[sin] offen offset:3072 sc1\n" \
"buffer_load_dwordx4 a[212:215], v217, %[srdz], s68 offen offset:0 sc1\n" \
"buffer_load_dwordx4 a[216:219], v217, %[srdz], s68 offen offset:1024 sc1\n" \
"buffer_load_dwordx4 a[220:223], v217, %[srdz], s68 offen offset:2048 sc1\n" \
"buffer_load_dwordx4 a[224:227], v217, %[srdz], s68 offen offset:3072 sc1\n" \
"v_and_b32_e32 v44, 0x800, v229\n" \
"v_cmp_eq_u32_e32 vcc, 0, v44\n" \
"s_cbranch_vccnz TLrn_%=\n" \
"buffer_load_dwordx4 a[164:167], v217, %[srdz], s54 offen offset:0 sc1\n" \
"buffer_load_dwordx4 a[168:171], v217, %[srdz], s54 offen offset:1024 sc1\n" \
"buffer_load_dwordx4 a[172:175], v217, %[srdz], s54 offen offset:2048 sc1\n" \
"buffer_load_dwordx4 a[176:179], v217, %[srdz], s54 offen offset:3072 sc1\n" \
"s_branch TLrp5_%=\n" \
"TLrp4_%=:\n" \
"v_and_b32_e32 v44, 0x800, v229\n" \
"v_cmp_eq_u32_e32 vcc, 0, v44\n" \
"s_cbranch_vccnz TLrn_%=\n" \
"TLrp5_%=:\n" \
"s_mov_b32 s50, 1\n" \
"buffer_load_dwordx4 a[180:183], v217, %[srdz], s55 offen offset:0 sc1\n" \
"buffer_load_dwordx4 a[184:187], v217, %[srdz], s55 offen offset:1024 sc1\n" \
"buffer_load_dwordx4 a[188:191], v217, %[srdz], s55 offen offset:2048 sc1\n" \
"buffer_load_dwordx4 a[192:195], v217, %[srdz], s55 offen offset:3072 sc1\n" \
"TLrn_%=:\n" \
"v_min3_u32 v208, v64, v65, v66\n" \
"v_min3_u32 v209, v67, v68, v69\n" \
"v_min3_u32 v208, v208, v70, v71\n" \
"v_min_u32_e32 v208, v208, v209\n" \
"v_cmp_eq_u32_e32 vcc, 0, v208\n" \
"s_cbranch_vccnz TLpk_%=\n" \
"TLpkb_%=:\n" \
"v_min_u32_e32 v208, v65, v67\n" \
"v_min_u32_e32 v209, v69, v71\n" \
"v_max_u32_e32 v65, v65, v67\n" \
"v_max_u32_e32 v69, v69, v71\n" \
"v_max3_u32 v64, v208, v64, v66\n" \
"v_max3_u32 v68, v209, v68, v70\n" \
"v_min_u32_e32 v208, v65, v69\n" \
"v_max_u32_e32 v65, v65, v69\n" \
"v_max3_u32 v64, v208, v64, v68\n" \
"v_mov_b64_e32 v[68:69], v[64:65]\n" \
"v_min_u32_dpp v208, v65, v65 quad_perm:[1,0,3,2] row_mask:0xf bank_mask:0xf\n" \
"v_max_u32_dpp v209, v64, v64 quad_perm:[1,0,3,2] row_mask:0xf bank_mask:0xf\n" \
"v_max_u32_dpp v65, v65, v65 quad_perm:[1,0,3,2] row_mask:0xf bank_mask:0xf\n" \
"v_max_u32_e32 v64, v208, v209\n" \
"s_nop 0\n" \
"v_min_u32_dpp v208, v65, v65 quad_perm:[2,3,0,1] row_mask:0xf bank_mask:0xf\n" \
"v_max_u32_dpp v209, v64, v64 quad_perm:[2,3,0,1] row_mask:0xf bank_mask:0xf\n" \
"v_max_u32_dpp v65, v65, v65 quad_perm:[2,3,0,1] row_mask:0xf bank_mask:0xf\n" \
"v_max_u32_e32 v64, v208, v209\n" \
"s_nop 0\n" \
"v_min_u32_dpp v208, v65, v65 row_half_mirror row_mask:0xf bank_mask:0xf\n" \
"v_max_u32_dpp v209, v64, v64 row_half_mirror row_mask:0xf bank_mask:0xf\n" \
"v_max_u32_dpp v65, v65, v65 row_half_mirror row_mask:0xf bank_mask:0xf\n" \
"v_max_u32_e32 v64, v208, v209\n" \
"s_nop 0\n" \
"v_min_u32_dpp v208, v65, v65 row_mirror row_mask:0xf bank_mask:0xf\n" \
"v_max_u32_dpp v209, v64, v64 row_mirror row_mask:0xf bank_mask:0xf\n" \
"v_max_u32_dpp v65, v65, v65 row_mirror row_mask:0xf bank_mask:0xf\n" \
"v_max_u32_e32 v64, v208, v209\n" \
"v_mov_b32_e32 v67, v65\n" \
"v_mov_b32_e32 v66, v64\n" \
"s_nop 0\n" \
"v_permlane16_swap_b32_e32 v65, v67\n" \
"v_permlane16_swap_b32_e32 v64, v66\n" \
"v_min_u32_e32 v208, v65, v67\n" \
"v_max_u32_e32 v65, v65, v67\n" \
"v_max3_u32 v64, v208, v64, v66\n" \
"v_mov_b32_e32 v67, v65\n" \
"v_mov_b32_e32 v66, v64\n" \
"s_nop 0\n" \
"v_permlane32_swap_b32_e32 v65, v67\n" \
"v_permlane32_swap_b32_e32 v64, v66\n" \
"v_min_u32_e32 v208, v65, v67\n" \
"v_max_u32_e32 v65, v65, v67\n" \
"v_max3_u32 v64, v208, v64, v66\n" \
"v_readfirstlane_b32 s44, v65\n" \
"v_readfirstlane_b32 s45, v64\n" \
"v_cmp_ge_u32_e32 vcc, v68, v64\n" \
"s_nop 1\n" \
"v_cndmask_b32_e64 v68, v68, 0, vcc\n" \
"v_cmp_ge_u32_e32 vcc, v69, v64\n" \
"s_nop 1\n" \
"v_cndmask_b32_e32 v69, v69, v68, vcc\n" \
"v_cndmask_b32_e64 v68, v68, 0, vcc\n" \
"s_nop 0\n" \
"v_min_u32_dpp v208, v69, v69 quad_perm:[1,0,3,2] row_mask:0xf bank_mask:0xf\n" \
"v_max_u32_dpp v209, v68, v68 quad_perm:[1,0,3,2] row_mask:0xf bank_mask:0xf\n" \
"v_max_u32_dpp v69, v69, v69 quad_perm:[1,0,3,2] row_mask:0xf bank_mask:0xf\n" \
"v_max_u32_e32 v68, v208, v209\n" \
"s_nop 0\n" \
"v_min_u32_dpp v208, v69, v69 quad_perm:[2,3,0,1] row_mask:0xf bank_mask:0xf\n" \
"v_max_u32_dpp v209, v68, v68 quad_perm:[2,3,0,1] row_mask:0xf bank_mask:0xf\n" \
"v_max_u32_dpp v69, v69, v69 quad_perm:[2,3,0,1] row_mask:0xf bank_mask:0xf\n" \
"v_max_u32_e32 v68, v208, v209\n" \
"s_nop 0\n" \
"v_min_u32_dpp v208, v69, v69 row_half_mirror row_mask:0xf bank_mask:0xf\n" \
"v_max_u32_dpp v209, v68, v68 row_half_mirror row_mask:0xf bank_mask:0xf\n" \
"v_max_u32_dpp v69, v69, v69 row_half_mirror row_mask:0xf bank_mask:0xf\n" \
"v_max_u32_e32 v68, v208, v209\n" \
"s_nop 0\n" \
"v_min_u32_dpp v208, v69, v69 row_mirror row_mask:0xf bank_mask:0xf\n" \
"v_max_u32_dpp v209, v68, v68 row_mirror row_mask:0xf bank_mask:0xf\n" \
"v_max_u32_dpp v69, v69, v69 row_mirror row_mask:0xf bank_mask:0xf\n" \
"v_max_u32_e32 v68, v208, v209\n" \
"v_mov_b32_e32 v67, v69\n" \
"v_mov_b32_e32 v66, v68\n" \
"s_nop 0\n" \
"v_permlane16_swap_b32_e32 v69, v67\n" \
"v_permlane16_swap_b32_e32 v68, v66\n" \
"v_min_u32_e32 v208, v69, v67\n" \
"v_max_u32_e32 v69, v69, v67\n" \
"v_max3_u32 v68, v208, v68, v66\n" \
"v_mov_b32_e32 v67, v69\n" \
"v_mov_b32_e32 v66, v68\n" \
"s_nop 0\n" \
"v_permlane32_swap_b32_e32 v69, v67\n" \
"v_permlane32_swap_b32_e32 v68, v66\n" \
"v_min_u32_e32 v208, v69, v67\n" \
"v_max_u32_e32 v69, v69, v67\n" \
"v_max3_u32 v68, v208, v68, v66\n" \
"v_readfirstlane_b32 s46, v69\n" \
"v_readfirstlane_b32 s47, v68\n" \
"s_andn2_b32 s44, 0x7ff, s44\n" \
"s_andn2_b32 s45, 0x7ff, s45\n" \
"s_andn2_b32 s46, 0x7ff, s46\n" \
"s_andn2_b32 s47, 0x7ff, s47\n" \
"v_and_b32_e32 v213, 4, v222\n" \
"v_mov_b32_e32 v208, s44\n" \
"v_cmp_ne_u32_e32 vcc, 0, v213\n" \
"v_mov_b32_e32 v214, s45\n" \
"v_mov_b32_e32 v209, s46\n" \
"v_mov_b32_e32 v215, s47\n" \
"v_cndmask_b32_e32 v208, v208, v214, vcc\n" \
"v_cndmask_b32_e32 v209, v209, v215, vcc\n" \
"v_cmp_lt_u32_e32 vcc, 31, v222\n" \
"v_and_b32_e32 v211, 0xf0, %[kb0]\n" \
"s_lshl_b32 s62, %[wid], 9\n" \
"v_cndmask_b32_e32 v210, v208, v209, vcc\n" \
"v_lshrrev_b32_e32 %[vdn], 6, v210\n" \
"v_lshlrev_b32_e32 %[vdn], 9, %[vdn]\n" \
"v_lshlrev_b32_e32 v213, 11, v210\n" \
"v_lshrrev_b32_e32 v214, 1, v210\n" \
"v_and_b32_e32 v214, 0x80, v214\n" \
"v_lshrrev_b32_e32 v215, 1, v211\n" \
"v_and_b32_e32 v215, 56, v215\n" \
"v_add3_u32 v213, v213, v214, v215\n" \
"v_and_b32_e32 v215, 0x80, v211\n" \
"v_lshlrev_b32_e32 v215, 1, v215\n" \
"v_add3_u32 v80, v213, v215, s62\n" \
"v_xor_b32_e32 v81, 0x80, v80\n" \
"s_mov_b32 s63, 278528\n" \
"buffer_load_dword %[vxs], %[vdn], %[srdz], s63 offen offset:0 sc1\n" \
"global_load_dwordx2 v[88:89], v80, %[px] offset:0\n" \
"global_load_dwordx2 v[90:91], v80, %[px] offset:64\n" \
"global_load_dwordx2 v[92:93], v81, %[px] offset:0\n" \
"global_load_dwordx2 v[94:95], v81, %[px] offset:64\n" \
"v_and_b32_e32 v213, 0x100, v210\n" \
"s_add_u32 s63, s62, s40\n" \
"v_add3_u32 v213, v213, v211, s63\n" \
"v_xor_b32_e32 v214, 0x100, v213\n" \
"ds_read_b128 v[104:107], v213 offset:2048\n" \
"ds_read_b128 v[108:111], v214 offset:2048\n" \
"ds_read_b128 v[64:67], v213 offset:6144\n" \
"ds_read_b128 v[68:71], v213 offset:8192\n" \
"ds_read_b128 v[72:75], v214 offset:6144\n" \
"ds_read_b128 v[76:79], v214 offset:8192\n" \
"ds_read_b128 v[112:115], v213 offset:10240\n" \
"ds_read_b128 v[116:119], v213 offset:12288\n" \
"ds_read_b128 v[120:123], v214 offset:10240\n" \
"ds_read_b128 v[124:127], v214 offset:12288\n" \
"s_movk_i32 s63, 2112\n" \
"v_mad_u32_u24 %[oq0], v219, s63, v213\n" \
"v_mad_u32_u24 %[wo0], v219, s63, v214\n" \
"s_lshl_b32 s62, %[wid], 10\n" \
"s_add_u32 s62, s62, 7340032\n" \
"v_and_b32_e32 v215, 0x3b0, v217\n" \
"v_add_u32_e32 v215, s62, v215\n" \
"s_mov_b32 s63, 38016\n" \
"v_lshrrev_b32_e32 %[vct], 1, v208\n" \
"v_and_b32_e32 v208, 1, v208\n" \
"v_mad_u32_u24 %[vct], %[vct], s63, v215\n" \
"v_lshl_add_u32 %[vct], v208, 6, %[vct]\n" \
"v_lshrrev_b32_e32 %[rd0], 1, v209\n" \
"v_and_b32_e32 v209, 1, v209\n" \
"v_mad_u32_u24 %[rd0], %[rd0], s63, v215\n" \
"v_lshl_add_u32 %[rd0], v209, 6, %[rd0]\n" \
"s_waitcnt vmcnt(13) lgkmcnt(0)\n" \
"s_cmp_lg_u32 s50, 0\n" \
"s_cbranch_scc1 TLrw_%=\n" \
"s_waitcnt vmcnt(5)\n" \
"TLrw_%=:\n" \
"v_mfma_f32_16x16x32_bf16 v[208:211], v[230:233], a[196:199], 0\n" \
"v_mfma_f32_16x16x32_bf16 v[212:215], v[230:233], a[204:207], 0\n" \
"v_mfma_f32_16x16x32_bf16 v[208:211], v[234:237], a[200:203], v[208:211]\n" \
"v_mfma_f32_16x16x32_bf16 v[212:215], v[234:237], a[208:211], v[212:215]\n" \
"v_lshlrev_b32_e32 v96, 16, v104\n" \
"v_and_b32_e32 v97, s43, v104\n" \
"v_lshlrev_b32_e32 v98, 16, v105\n" \
"v_and_b32_e32 v99, s43, v105\n" \
"v_lshlrev_b32_e32 v100, 16, v106\n" \
"v_and_b32_e32 v101, s43, v106\n" \
"v_lshlrev_b32_e32 v102, 16, v107\n" \
"v_and_b32_e32 v103, s43, v107\n" \
"v_pk_fma_f32 v[0:1], v[208:209], v[128:129], v[0:1]\n" \
"v_pk_fma_f32 v[2:3], v[210:211], v[130:131], v[2:3]\n" \
"v_pk_fma_f32 v[8:9], v[212:213], v[136:137], v[8:9]\n" \
"v_pk_fma_f32 v[10:11], v[214:215], v[138:139], v[10:11]\n" \
"v_pk_mul_f32 v[6:7], v[0:1], v[160:161]\n" \
"v_pk_mul_f32 v[12:13], v[0:1], v[0:1]\n" \
"v_pk_add_f32 v[4:5], v[0:1], v[2:3]\n" \
"v_pk_fma_f32 v[6:7], v[2:3], v[162:163], v[6:7]\n" \
"v_pk_fma_f32 v[12:13], v[2:3], v[2:3], v[12:13]\n" \
"v_pk_add_f32 v[4:5], v[4:5], v[8:9]\n" \
"v_pk_fma_f32 v[6:7], v[8:9], v[168:169], v[6:7]\n" \
"v_pk_fma_f32 v[12:13], v[8:9], v[8:9], v[12:13]\n" \
"v_pk_add_f32 v[4:5], v[4:5], v[10:11]\n" \
"v_pk_fma_f32 v[6:7], v[10:11], v[170:171], v[6:7]\n" \
"v_pk_fma_f32 v[12:13], v[10:11], v[10:11], v[12:13]\n" \
"v_mfma_f32_16x16x32_bf16 v[208:211], v[230:233], a[212:215], 0\n" \
"v_mfma_f32_16x16x32_bf16 v[212:215], v[230:233], a[220:223], 0\n" \
"v_mfma_f32_16x16x32_bf16 v[208:211], v[234:237], a[216:219], v[208:211]\n" \
"v_mfma_f32_16x16x32_bf16 v[212:215], v[234:237], a[224:227], v[212:215]\n" \
"v_lshlrev_b32_e32 v104, 16, v108\n" \
"v_and_b32_e32 v105, s43, v108\n" \
"v_lshlrev_b32_e32 v106, 16, v109\n" \
"v_and_b32_e32 v107, s43, v109\n" \
"v_lshlrev_b32_e32 v108, 16, v110\n" \
"v_and_b32_e32 v109, s43, v110\n" \
"v_lshlrev_b32_e32 v110, 16, v111\n" \
"v_and_b32_e32 v111, s43, v111\n" \
"v_pk_fma_f32 v[16:17], v[208:209], v[132:133], v[16:17]\n" \
"v_pk_fma_f32 v[18:19], v[210:211], v[134:135], v[18:19]\n" \
"v_pk_fma_f32 v[24:25], v[212:213], v[140:141], v[24:25]\n" \
"v_pk_fma_f32 v[26:27], v[214:215], v[142:143], v[26:27]\n" \
"v_pk_add_f32 v[4:5], v[4:5], v[16:17]\n" \
"v_pk_fma_f32 v[6:7], v[16:17], v[164:165], v[6:7]\n" \
"v_pk_fma_f32 v[12:13], v[16:17], v[16:17], v[12:13]\n" \
"v_pk_add_f32 v[4:5], v[4:5], v[18:19]\n" \
"v_pk_fma_f32 v[6:7], v[18:19], v[166:167], v[6:7]\n" \
"v_pk_fma_f32 v[12:13], v[18:19], v[18:19], v[12:13]\n" \
"v_pk_add_f32 v[4:5], v[4:5], v[24:25]\n" \
"v_pk_fma_f32 v[6:7], v[24:25], v[172:173], v[6:7]\n" \
"v_pk_fma_f32 v[12:13], v[24:25], v[24:25], v[12:13]\n" \
"v_pk_add_f32 v[4:5], v[4:5], v[26:27]\n" \
"v_pk_fma_f32 v[6:7], v[26:27], v[174:175], v[6:7]\n" \
"v_pk_fma_f32 v[12:13], v[26:27], v[26:27], v[12:13]\n" \
"s_cmp_lg_u32 s50, 0\n" \
"s_cbranch_scc1 TLre_%=\n" \
"v_and_b32_e32 v208, 0x800, v229\n" \
"v_cmp_eq_u32_e32 vcc, 0, v208\n" \
"s_cbranch_vccz TLrl4_%=\n" \
"v_mov_b32_e32 v216, s40\n" \
"TLrq4_%=:\n" \
"buffer_load_dword v225, off, %[srdz], s53 offset:0 sc1\n" \
"s_mov_b32 s52, 10\n" \
"TLrs4_%=:\n" \
"ds_read_b32 v229, v216 offset:14336\n" \
"s_waitcnt lgkmcnt(0)\n" \
"v_and_b32_e32 v208, 0x800, v229\n" \
"v_cmp_eq_u32_e32 vcc, 0, v208\n" \
"s_cbranch_vccz TLrd4_%=\n" \
"s_sub_u32 s52, s52, 1\n" \
"s_cbranch_scc0 TLrs4_%=\n" \
"s_sub_u32 s51, s51, 1\n" \
"s_cselect_b32 s51, 0, s51\n" \
"s_waitcnt vmcnt(0)\n" \
"v_and_b32_e32 v208, 0x800, v225\n" \
"v_cmp_eq_u32_e32 vcc, 0, v208\n" \
"s_cbranch_scc1 TLrl4_%=\n" \
"s_cbranch_vccnz TLrq4_%=\n" \
"v_mov_b32_e32 v229, v225\n" \
"s_mov_b64 exec, 1\n" \
"ds_max_u32 v216, v225 offset:14336\n" \
"s_mov_b64 exec, -1\n" \
"TLrd4_%=:\n" \
"s_sleep 4\n" \
"TLrl4_%=:\n" \
"s_mov_b32 s63, 278528\n" \
"s_nop 0\n" \
"buffer_load_dword %[vxs], %[vdn], %[srdz], s63 offen offset:0 sc1\n" \
"buffer_load_dwordx4 a[164:167], v217, %[srdz], s54 offen offset:0 sc1\n" \
"buffer_load_dwordx4 a[168:171], v217, %[srdz], s54 offen offset:1024 sc1\n" \
"buffer_load_dwordx4 a[172:175], v217, %[srdz], s54 offen offset:2048 sc1\n" \
"buffer_load_dwordx4 a[176:179], v217, %[srdz], s54 offen offset:3072 sc1\n" \
"buffer_load_dwordx4 a[180:183], v217, %[srdz], s55 offen offset:0 sc1\n" \
"buffer_load_dwordx4 a[184:187], v217, %[srdz], s55 offen offset:1024 sc1\n" \
"buffer_load_dwordx4 a[188:191], v217, %[srdz], s55 offen offset:2048 sc1\n" \
"buffer_load_dwordx4 a[192:195], v217, %[srdz], s55 offen offset:3072 sc1\n" \
"s_waitcnt vmcnt(4)\n" \
"s_branch TLrf_%=\n" \
"TLre_%=:\n" \
"s_waitcnt vmcnt(9)\n" \
"TLrf_%=:\n" \
"v_mfma_f32_16x16x32_bf16 v[208:211], v[230:233], a[164:167], 0\n" \
"v_mfma_f32_16x16x32_bf16 v[212:215], v[230:233], a[172:175], 0\n" \
"v_mfma_f32_16x16x32_bf16 v[208:211], v[234:237], a[168:171], v[208:211]\n" \
"v_mfma_f32_16x16x32_bf16 v[212:215], v[234:237], a[176:179], v[212:215]\n" \
"s_lshl_b32 s62, %[wid], 3\n" \
"v_and_b32_e32 v216, 4, v222\n" \
"s_add_u32 s62, s62, s40\n" \
"v_lshlrev_b32_e32 v229, 4, v224\n" \
"v_add_u32_e32 v216, s62, v216\n" \
"s_movk_i32 s52, 0x1000\n" \
"s_nop 0\n" \
"v_pk_fma_f32 v[32:33], v[208:209], v[144:145], v[32:33]\n" \
"v_pk_fma_f32 v[34:35], v[210:211], v[146:147], v[34:35]\n" \
"v_pk_fma_f32 v[40:41], v[212:213], v[152:153], v[40:41]\n" \
"v_pk_fma_f32 v[42:43], v[214:215], v[154:155], v[42:43]\n" \
"v_pk_mul_f32 v[36:37], v[32:33], v[176:177]\n" \
"v_pk_mul_f32 v[38:39], v[32:33], v[32:33]\n" \
"v_pk_add_f32 v[14:15], v[32:33], v[34:35]\n" \
"v_pk_fma_f32 v[36:37], v[34:35], v[178:179], v[36:37]\n" \
"v_pk_fma_f32 v[38:39], v[34:35], v[34:35], v[38:39]\n" \
"v_pk_add_f32 v[14:15], v[14:15], v[40:41]\n" \
"v_pk_fma_f32 v[36:37], v[40:41], v[184:185], v[36:37]\n" \
"v_pk_fma_f32 v[38:39], v[40:41], v[40:41], v[38:39]\n" \
"v_pk_add_f32 v[14:15], v[14:15], v[42:43]\n" \
"v_pk_fma_f32 v[36:37], v[42:43], v[186:187], v[36:37]\n" \
"v_pk_fma_f32 v[38:39], v[42:43], v[42:43], v[38:39]\n" \
"s_waitcnt vmcnt(0)\n" \
"v_and_b32_e32 v225, 7, v222\n" \
"v_lshl_add_u32 v225, v225, 2, s40\n" \
"v_and_b32_e32 v44, 0x820, %[vxs]\n" \
"v_cmp_ne_u32_e32 vcc, 0x820, v44\n" \
"s_mov_b32 s59, 0\n" \
"s_cbranch_vccnz TLcn_%=\n" \
"s_mov_b32 s59, 1\n" \
"buffer_load_dwordx4 a[128:131], %[vct], %[srdz], 0 offen offset:0 sc1\n" \
"buffer_load_dwordx4 a[132:135], %[rd0], %[srdz], 0 offen offset:0 sc1\n" \
"buffer_load_dwordx4 a[136:139], %[vct], %[srdz], s56 offen offset:0 sc1\n" \
"buffer_load_dwordx4 a[140:143], %[rd0], %[srdz], s56 offen offset:0 sc1\n" \
"buffer_load_dwordx4 a[144:147], %[vct], %[srdz], s57 offen offset:0 sc1\n" \
"buffer_load_dwordx4 a[148:151], %[rd0], %[srdz], s57 offen offset:0 sc1\n" \
"buffer_load_dwordx4 a[152:155], %[vct], %[srdz], s58 offen offset:0 sc1\n" \
"buffer_load_dwordx4 a[156:159], %[rd0], %[srdz], s58 offen offset:0 sc1\n" \
"TLcn_%=:\n" \
"v_mfma_f32_16x16x32_bf16 v[208:211], v[230:233], a[180:183], 0\n" \
"v_mfma_f32_16x16x32_bf16 v[212:215], v[230:233], a[188:191], 0\n" \
"v_mfma_f32_16x16x32_bf16 v[208:211], v[234:237], a[184:187], v[208:211]\n" \
"v_mfma_f32_16x16x32_bf16 v[212:215], v[234:237], a[192:195], v[212:215]\n" \
"v_lshlrev_b32_e32 v80, 16, v88\n" \
"v_and_b32_e32 v81, s43, v88\n" \
"v_lshlrev_b32_e32 v82, 16, v89\n" \
"v_and_b32_e32 v83, s43, v89\n" \
"v_lshlrev_b32_e32 v84, 16, v90\n" \
"v_and_b32_e32 v85, s43, v90\n" \
"v_lshlrev_b32_e32 v86, 16, v91\n" \
"v_pk_fma_f32 v[48:49], v[208:209], v[148:149], v[48:49]\n" \
"v_pk_fma_f32 v[50:51], v[210:211], v[150:151], v[50:51]\n" \
"v_pk_fma_f32 v[56:57], v[212:213], v[156:157], v[56:57]\n" \
"v_pk_fma_f32 v[58:59], v[214:215], v[158:159], v[58:59]\n" \
"v_pk_add_f32 v[14:15], v[14:15], v[48:49]\n" \
"v_pk_fma_f32 v[36:37], v[48:49], v[180:181], v[36:37]\n" \
"v_pk_fma_f32 v[38:39], v[48:49], v[48:49], v[38:39]\n" \
"v_pk_add_f32 v[14:15], v[14:15], v[50:51]\n" \
"v_pk_fma_f32 v[36:37], v[50:51], v[182:183], v[36:37]\n" \
"v_pk_fma_f32 v[38:39], v[50:51], v[50:51], v[38:39]\n" \
"v_pk_add_f32 v[14:15], v[14:15], v[56:57]\n" \
"v_pk_fma_f32 v[36:37], v[56:57], v[188:189], v[36:37]\n" \
"v_pk_fma_f32 v[38:39], v[56:57], v[56:57], v[38:39]\n" \
"v_pk_add_f32 v[14:15], v[14:15], v[58:59]\n" \
"v_pk_fma_f32 v[36:37], v[58:59], v[190:191], v[36:37]\n" \
"v_pk_fma_f32 v[38:39], v[58:59], v[58:59], v[38:39]\n" \
"v_pk_add_f32 v[4:5], v[4:5], v[14:15]\n" \
"v_pk_add_f32 v[6:7], v[6:7], v[36:37]\n" \
"v_pk_add_f32 v[12:13], v[12:13], v[38:39]\n" \
"v_add_f32_e32 v4, v4, v5\n" \
"v_add_f32_e32 v6, v6, v7\n" \
"v_add_f32_e32 v12, v12, v13\n" \
"v_and_b32_e32 v87, s43, v91\n" \
"v_mov_b32_e32 v208, v12\n" \
"v_permlane32_swap_b32_e32 v4, v6\n" \
"v_lshlrev_b32_e32 v88, 16, v92\n" \
"v_permlane32_swap_b32_e32 v12, v208\n" \
"v_add_f32_e32 v4, v4, v6\n" \
"v_add_f32_e32 v12, v12, v208\n" \
"v_and_b32_e32 v89, s43, v92\n" \
"v_lshlrev_b32_e32 v90, 16, v93\n" \
"v_permlane16_swap_b32_e32 v4, v12\n" \
"v_add_f32_e32 v4, v4, v12\n" \
"v_and_b32_e32 v91, s43, v93\n" \
"v_lshlrev_b32_e32 v92, 16, v94\n" \
"v_add_f32_dpp v4, v4, v4 quad_perm:[1,0,3,2] row_mask:0xf bank_mask:0xf\n" \
"v_and_b32_e32 v93, s43, v94\n" \
"v_lshlrev_b32_e32 v94, 16, v95\n" \
"v_add_f32_dpp v4, v4, v4 quad_perm:[2,3,0,1] row_mask:0xf bank_mask:0xf\n" \
"v_and_b32_e32 v95, s43, v95\n" \
"s_nop 0\n" \
"v_add_f32_dpp v4, v4, v4 row_ror:8 row_mask:0xf bank_mask:0xf\n" \
"v_mov_b32_e32 v12, v4\n" \
"s_nop 0\n" \
"s_nop 0\n" \
"v_permlane16_swap_b32_e32 v4, v12\n" \
"v_mov_b32_e32 v6, v4\n" \
"v_mul_f32_e32 v12, 0x3a800000, v12\n" \
"s_nop 0\n" \
"v_permlane32_swap_b32_e32 v4, v6\n" \
"v_mul_f32_e32 v218, 0x3a800000, v4\n" \
"v_mul_f32_e32 v208, v218, v218\n" \
"v_sub_f32_e32 v12, v12, v208\n" \
"v_cmp_nle_f32_e32 vcc, v208, v12\n" \
"v_add_f32_e32 v12, 0x3727c5ac, v12\n" \
"v_rsq_f32_e32 v220, v12\n" \
"v_fma_f32 v210, -v218, v223, v6\n" \
"s_mov_b64 exec, 17\n" \
"v_mul_f32_e32 v210, v210, v220\n" \
"v_max_f32_e32 v210, 0xff800000, v210\n" \
"v_ashrrev_i32_e32 v208, 31, v210\n" \
"v_or_b32_e32 v208, 0x80000000, v208\n" \
"v_xor_b32_e32 v210, v210, v208\n" \
"v_and_b32_e32 v210, 0xfffff800, v210\n" \
"v_or_b32_e32 v210, v210, %[ta0]\n" \
"ds_write_b32 v216, v210 offset:14400\n" \
"s_mov_b64 exec, -1\n" \
"s_cbranch_vccnz TLrare_%=\n" \
"TLrok_%=:\n" \
"s_movk_i32 s52, 0x1000\n" \
"s_waitcnt lgkmcnt(0)\n" \
"s_barrier\n" \
"ds_read_b32 v209, v225 offset:14400\n" \
"v_mov_b32_e32 v227, 1\n" \
"v_mov_b32_e32 v191, 0\n" \
"v_mov_b32_e32 v190, 0\n" \
"v_mov_b32_e32 v225, -1\n" \
"s_mov_b32 s41, 1\n" \
"s_cmp_lg_u32 %[wbytes], 0\n" \
"s_cselect_b32 s42, -8, 0\n" \
"s_waitcnt lgkmcnt(0)\n" \
"v_min_u32_dpp v208, v209, v209 quad_perm:[1,0,3,2] row_mask:0xf bank_mask:0xf\n" \
"v_max_u32_dpp v209, v209, v209 quad_perm:[1,0,3,2] row_mask:0xf bank_mask:0xf\n" \
"s_nop 1\n" \
"v_min_u32_dpp v210, v209, v209 quad_perm:[2,3,0,1] row_mask:0xf bank_mask:0xf\n" \
"v_max_u32_dpp v211, v208, v208 quad_perm:[2,3,0,1] row_mask:0xf bank_mask:0xf\n" \
"v_max_u32_dpp v209, v209, v209 quad_perm:[2,3,0,1] row_mask:0xf bank_mask:0xf\n" \
"v_max_u32_e32 v208, v210, v211\n" \
"s_nop 0\n" \
"v_min_u32_dpp v210, v209, v209 row_half_mirror row_mask:0xf bank_mask:0xf\n" \
"v_max_u32_dpp v211, v208, v208 row_half_mirror row_mask:0xf bank_mask:0xf\n" \
"v_max_u32_dpp v209, v209, v209 row_half_mirror row_mask:0xf bank_mask:0xf\n" \
"v_max_u32_e32 v208, v210, v211\n" \
"s_mov_b64 exec, 3\n" \
"buffer_atomic_or_x2 v[208:209], v229, %[srdz], s48 offen sc1\n" \
"s_mov_b64 exec, -1\n" \
"s_waitcnt vmcnt(1)\n" \
"TLcs_%=:\n" \
"v_mfma_f32_16x16x32_bf16 v[128:131], v[230:233], a[128:131], 0\n" \
"v_mfma_f32_16x16x32_bf16 v[132:135], v[230:233], a[136:139], 0\n" \
"v_mfma_f32_16x16x32_bf16 v[128:131], v[234:237], a[132:135], v[128:131]\n" \
"v_mfma_f32_16x16x32_bf16 v[132:135], v[234:237], a[140:143], v[132:135]\n" \
"v_mfma_f32_16x16x32_bf16 v[136:139], v[230:233], a[144:147], 0\n" \
"v_mfma_f32_16x16x32_bf16 v[140:143], v[230:233], a[152:155], 0\n" \
"v_mfma_f32_16x16x32_bf16 v[136:139], v[234:237], a[148:151], v[136:139]\n" \
"v_mfma_f32_16x16x32_bf16 v[140:143], v[234:237], a[156:159], v[140:143]\n" \
"s_nop 2\n" \
"v_pk_fma_f32 v[144:145], v[128:129], v[64:65], v[80:81]\n" \
"v_pk_fma_f32 v[146:147], v[130:131], v[66:67], v[82:83]\n" \
"v_pk_fma_f32 v[148:149], v[132:133], v[68:69], v[84:85]\n" \
"v_pk_fma_f32 v[150:151], v[134:135], v[70:71], v[86:87]\n" \
"v_pk_mul_f32 v[160:161], v[144:145], v[96:97]\n" \
"v_pk_mul_f32 v[162:163], v[146:147], v[98:99]\n" \
"v_pk_mul_f32 v[164:165], v[148:149], v[100:101]\n" \
"v_pk_mul_f32 v[166:167], v[150:151], v[102:103]\n" \
"v_cvt_pk_bf16_f32 v176, v160, v161\n" \
"v_cvt_pk_bf16_f32 v177, v162, v163\n" \
"v_cvt_pk_bf16_f32 v178, v164, v165\n" \
"v_cvt_pk_bf16_f32 v179, v166, v167\n" \
"ds_write_b128 %[oq0], v[176:179] offset:16384\n" \
"v_pk_fma_f32 v[152:153], v[136:137], v[72:73], v[88:89]\n" \
"v_pk_fma_f32 v[154:155], v[138:139], v[74:75], v[90:91]\n" \
"v_pk_fma_f32 v[156:157], v[140:141], v[76:77], v[92:93]\n" \
"v_pk_fma_f32 v[158:159], v[142:143], v[78:79], v[94:95]\n" \
"v_pk_mul_f32 v[168:169], v[152:153], v[104:105]\n" \
"v_pk_mul_f32 v[170:171], v[154:155], v[106:107]\n" \
"v_pk_mul_f32 v[172:173], v[156:157], v[108:109]\n" \
"v_pk_mul_f32 v[174:175], v[158:159], v[110:111]\n" \
"v_cvt_pk_bf16_f32 v180, v168, v169\n" \
"v_cvt_pk_bf16_f32 v181, v170, v171\n" \
"v_cvt_pk_bf16_f32 v182, v172, v173\n" \
"v_cvt_pk_bf16_f32 v183, v174, v175\n" \
"ds_write_b128 %[wo0], v[180:183] offset:16384\n" \
"ds_read_b128 v[4:7], v226 offset:0\n" \
"ds_read_b128 v[12:15], v226 offset:64\n" \
"ds_read_b128 v[20:23], v226 offset:128\n" \
"ds_read_b128 v[28:31], v226 offset:192\n" \
"ds_read_b128 v[36:39], v226 offset:256\n" \
"ds_read_b128 v[44:47], v226 offset:320\n" \
"ds_read_b128 v[52:55], v226 offset:384\n" \
"ds_read_b128 v[60:63], v226 offset:448\n" \
"v_cmp_u_f32_e32 vcc, v128, v132\n" \
"v_cmp_u_f32_e64 s[62:63], v136, v140\n" \
"s_or_b64 vcc, vcc, s[62:63]\n" \
"s_cbranch_scc1 TLcx_%=\n" \
"TLco_%=:\n" \
"v_pk_add_f32 v[184:185], v[144:145], v[146:147]\n" \
"v_pk_mul_f32 v[186:187], v[144:145], v[144:145]\n" \
"v_pk_mul_f32 v[188:189], v[144:145], v[112:113]\n" \
"v_pk_fma_f32 v[186:187], v[146:147], v[146:147], v[186:187]\n" \
"v_pk_fma_f32 v[188:189], v[146:147], v[114:115], v[188:189]\n" \
"v_pk_add_f32 v[184:185], v[184:185], v[148:149]\n" \
"v_pk_fma_f32 v[186:187], v[148:149], v[148:149], v[186:187]\n" \
"v_pk_fma_f32 v[188:189], v[148:149], v[116:117], v[188:189]\n" \
"v_pk_add_f32 v[184:185], v[184:185], v[150:151]\n" \
"v_pk_fma_f32 v[186:187], v[150:151], v[150:151], v[186:187]\n" \
"v_pk_fma_f32 v[188:189], v[150:151], v[118:119], v[188:189]\n" \
"v_pk_add_f32 v[184:185], v[184:185], v[152:153]\n" \
"v_pk_fma_f32 v[186:187], v[152:153], v[152:153], v[186:187]\n" \
"v_pk_fma_f32 v[188:189], v[152:153], v[120:121], v[188:189]\n" \
"v_pk_add_f32 v[184:185], v[184:185], v[154:155]\n" \
"v_pk_fma_f32 v[186:187], v[154:155], v[154:155], v[186:187]\n" \
"v_pk_fma_f32 v[188:189], v[154:155], v[122:123], v[188:189]\n" \
"v_pk_add_f32 v[184:185], v[184:185], v[156:157]\n" \
"v_pk_fma_f32 v[186:187], v[156:157], v[156:157], v[186:187]\n" \
"v_pk_fma_f32 v[188:189], v[156:157], v[124:125], v[188:189]\n" \
"v_pk_add_f32 v[184:185], v[184:185], v[158:159]\n" \
"v_pk_fma_f32 v[186:187], v[158:159], v[158:159], v[186:187]\n" \
"v_pk_fma_f32 v[188:189], v[158:159], v[126:127], v[188:189]\n" \
"v_add_f32_e32 v184, v184, v185\n" \
"v_add_f32_e32 v186, v186, v187\n" \
"v_add_f32_e32 v188, v188, v189\n" \
"s_waitcnt lgkmcnt(7)\n" \
"v_mfma_f32_16x16x32_bf16 v[208:211], a[64:67], v[4:7], 0\n" \
"v_mfma_f32_16x16x32_bf16 v[212:215], a[96:99], v[4:7], 0\n" \
"v_permlane16_swap_b32_e32 v184, v186\n" \
"v_mov_b32_e32 v189, v188\n" \
"s_waitcnt lgkmcnt(6)\n" \
"v_mfma_f32_16x16x32_bf16 v[208:211], a[68:71], v[12:15], v[208:211]\n" \
"v_mfma_f32_16x16x32_bf16 v[212:215], a[100:103], v[12:15], v[212:215]\n" \
"v_add_f32_e32 v184, v184, v186\n" \
"v_permlane16_swap_b32_e32 v188, v189\n" \
"s_waitcnt lgkmcnt(5)\n" \
"v_mfma_f32_16x16x32_bf16 v[208:211], a[72:75], v[20:23], v[208:211]\n" \
"v_mfma_f32_16x16x32_bf16 v[212:215], a[104:107], v[20:23], v[212:215]\n" \
"v_add_f32_e32 v188, v188, v189\n" \
"v_add_u32_e32 v176, 1632, %[kd0]\n" \
"s_waitcnt lgkmcnt(4)\n" \
"v_mfma_f32_16x16x32_bf16 v[208:211], a[76:79], v[28:31], v[208:211]\n" \
"v_mfma_f32_16x16x32_bf16 v[212:215], a[108:111], v[28:31], v[212:215]\n" \
"v_add_f32_dpp v184, v184, v184 quad_perm:[1,0,3,2] row_mask:0xf bank_mask:0xf\n" \
"v_add_f32_dpp v188, v188, v188 quad_perm:[1,0,3,2] row_mask:0xf bank_mask:0xf\n" \
"s_waitcnt lgkmcnt(3)\n" \
".if . - adaln_body != 65552\n" \
".error \"size model\"\n" \
".endif\n" \
"Lk15_%=:\n" \
"s_cbranch_execz Lk16_%=\n" \
"s_nop 0\n" \
"v_mfma_f32_16x16x32_bf16 v[208:211], a[80:83], v[36:39], v[208:211]\n" \
"v_mfma_f32_16x16x32_bf16 v[212:215], a[112:115], v[36:39], v[212:215]\n" \
"v_add_f32_dpp v184, v184, v184 quad_perm:[2,3,0,1] row_mask:0xf bank_mask:0xf\n" \
"v_add_f32_dpp v188, v188, v188 quad_perm:[2,3,0,1] row_mask:0xf bank_mask:0xf\n" \
"s_waitcnt lgkmcnt(2)\n" \
"v_mfma_f32_16x16x32_bf16 v[208:211], a[84:87], v[44:47], v[208:211]\n" \
"v_mfma_f32_16x16x32_bf16 v[212:215], a[116:119], v[44:47], v[212:215]\n" \
"v_add_f32_dpp v184, v184, v184 row_ror:8 row_mask:0xf bank_mask:0xf\n" \
"v_add_f32_dpp v188, v188, v188 row_ror:8 row_mask:0xf bank_mask:0xf\n" \
"s_waitcnt lgkmcnt(1)\n" \
"v_mfma_f32_16x16x32_bf16 v[208:211], a[88:91], v[52:55], v[208:211]\n" \
"v_mfma_f32_16x16x32_bf16 v[212:215], a[120:123], v[52:55], v[212:215]\n" \
"s_mov_b32 vcc_lo, 0xaaaaaaaa\n" \
"s_mov_b32 vcc_hi, 0xaaaaaaaa\n" \
"v_cndmask_b32_e32 v184, v184, v188, vcc\n" \
"s_waitcnt lgkmcnt(0)\n" \
"v_mfma_f32_16x16x32_bf16 v[208:211], a[92:95], v[60:63], v[208:211]\n" \
"v_mfma_f32_16x16x32_bf16 v[212:215], a[124:127], v[60:63], v[212:215]\n" \
"s_mov_b32 exec_lo, 0x330033\n" \
"s_mov_b32 exec_hi, 0x330033\n" \
"ds_write_b32 v228, v184 offset:0\n" \
"s_mov_b32 exec_lo, 0x3f003f\n" \
"s_mov_b32 exec_hi, 0x3f003f\n" \
"s_nop 1\n" \
"ds_write2_b32 %[kd0], v208, v209 offset0:0 offset1:4\n" \
"ds_write2_b32 %[kd0], v210, v211 offset0:8 offset1:12\n" \
"ds_write2_b32 v176, v212, v213 offset0:0 offset1:4\n" \
"ds_write2_b32 v176, v214, v215 offset0:8 offset1:12\n" \
"s_mov_b64 exec, -1\n" \
"s_waitcnt lgkmcnt(0)\n" \
"s_barrier\n" \
"ds_read_b128 v[4:7], %[vg0] offset:0\n" \
"ds_read_b128 v[12:15], %[vg0] offset:16\n" \
"ds_read_b128 v[20:23], %[vg0] offset:32\n" \
"ds_read_b128 v[28:31], v221 offset:0\n" \
"ds_read_b128 v[36:39], %[vx0] offset:1088\n" \
"ds_read_b128 v[44:47], %[vx0] offset:1360\n" \
"ds_read_b128 v[52:55], v221 offset:1632\n" \
"ds_read_b128 v[60:63], %[vx0] offset:2720\n" \
"ds_read_b128 v[128:131], %[vx0] offset:2992\n" \
"v_bfe_u32 v208, v222, 3, 1\n" \
"v_and_b32_e32 v209, 1, v222\n" \
"v_lshl_or_b32 v208, v208, 1, v209\n" \
"s_waitcnt lgkmcnt(6)\n" \
"v_pk_add_f32 v[4:5], v[4:5], v[6:7]\n" \
"v_pk_add_f32 v[12:13], v[12:13], v[14:15]\n" \
"v_pk_add_f32 v[20:21], v[20:21], v[22:23]\n" \
"v_add_f32_e32 v4, v4, v5\n" \
"v_add_f32_e32 v12, v12, v13\n" \
"v_add_f32_e32 v20, v20, v21\n" \
"v_mul_f32_e32 v4, 0x3a800000, v4\n" \
"v_mul_f32_e32 v12, 0x3a800000, v12\n" \
"v_mul_f32_e32 v209, v4, v4\n" \
"v_sub_f32_e32 v12, v12, v209\n" \
"s_waitcnt lgkmcnt(3)\n" \
"v_cmp_nle_f32_e64 s[50:51], v209, v12\n" \
"v_add_f32_e32 v12, 0x3727c5ac, v12\n" \
"v_pk_add_f32 v[28:29], v[28:29], v[30:31]\n" \
"v_rsq_f32_e32 v132, v12\n" \
"v_pk_add_f32 v[36:37], v[36:37], v[38:39]\n" \
"v_fma_f32 v20, -v4, v223, v20\n" \
"v_pk_add_f32 v[44:45], v[44:45], v[46:47]\n" \
"v_add_f32_e32 v28, v28, v29\n" \
"v_add_f32_e32 v36, v36, v37\n" \
"v_add_f32_e32 v44, v44, v45\n" \
"v_mul_f32_e32 v20, v20, v132\n" \
"s_waitcnt lgkmcnt(0)\n" \
"v_pk_add_f32 v[52:53], v[52:53], v[54:55]\n" \
"v_max_f32_e32 v210, 0xff800000, v20\n" \
"v_pk_add_f32 v[60:61], v[60:61], v[62:63]\n" \
"v_ashrrev_i32_e32 v209, 31, v210\n" \
"v_pk_add_f32 v[128:129], v[128:129], v[130:131]\n" \
"v_or_b32_e32 v209, 0x80000000, v209\n" \
"v_add_f32_e32 v52, v52, v53\n" \
"v_xor_b32_e32 v210, v210, v209\n" \
"v_add_f32_e32 v60, v60, v61\n" \
"v_and_b32_e32 v210, 0xfffff800, v210\n" \
"v_add_f32_e32 v128, v128, v129\n" \
"v_and_or_b32 v210, v210, v225, v208\n" \
"v_fma_f32 v28, -v4, v36, v28\n" \
"v_fma_f32 v52, -v4, v60, v52\n" \
"v_fma_f32 v28, v28, v132, v44\n" \
"v_min_u32_dpp v211, v210, v210 quad_perm:[1,0,3,2] row_mask:0xf bank_mask:0xf\n" \
"v_fma_f32 v52, v52, v132, v128\n" \
"v_max_u32_dpp v210, v210, v210 quad_perm:[1,0,3,2] row_mask:0xf bank_mask:0xf\n" \
"v_cvt_pk_bf16_f32 v28, 0, v28\n" \
"v_cvt_pk_bf16_f32 v52, 0, v52\n" \
"v_min_u32_dpp v212, v210, v210 row_ror:8 row_mask:0xf bank_mask:0xf\n" \
"v_mul_f32_e32 v214, 0xbfb8aa3b, v28\n" \
"v_max_u32_dpp v213, v211, v211 row_ror:8 row_mask:0xf bank_mask:0xf\n" \
"v_exp_f32_e32 v214, v214\n" \
"v_max_u32_dpp v210, v210, v210 row_ror:8 row_mask:0xf bank_mask:0xf\n" \
"v_add_f32_e32 v214, 1.0, v214\n" \
"v_max_u32_e32 v211, v212, v213\n" \
"v_rcp_f32_e32 v214, v214\n" \
"s_nop 0\n" \
"v_mul_f32_e32 v28, v28, v214\n" \
"v_cvt_pk_bf16_f32 v28, 0, v28\n" \
"v_mul_f32_e32 v28, v28, v52\n" \
"v_cvt_pk_bf16_f32 v28, 0, v28\n" \
"v_cndmask_b32_e32 v133, v210, v211, vcc\n" \
"v_readfirstlane_b32 s60, v210\n" \
"v_readfirstlane_b32 s61, v211\n" \
"v_xor_b32_e32 v212, v133, v208\n" \
"v_and_b32_e32 v213, 1, v212\n" \
"v_and_b32_e32 v212, 2, v212\n" \
"v_cmp_ne_u32_e64 s[54:55], 0, v213\n" \
"v_cmp_ne_u32_e64 s[56:57], 0, v212\n" \
"s_mov_b32 s62, 0x07060302\n" \
"v_mov_b32_dpp v213, v28 quad_perm:[1,0,3,2] row_mask:0xf bank_mask:0xf\n" \
"v_cndmask_b32_e64 v28, v28, v213, s[54:55]\n" \
"s_nop 1\n" \
"v_mov_b32_dpp v214, v28 row_ror:8 row_mask:0xf bank_mask:0xf\n" \
"v_cndmask_b32_e64 v28, v28, v214, s[56:57]\n" \
"s_mov_b32 vcc_lo, 0xcccccccc\n" \
"s_mov_b32 vcc_hi, 0xcccccccc\n" \
"v_mov_b32_dpp v213, v28 quad_perm:[2,3,0,1] row_mask:0xf bank_mask:0xf\n" \
"s_mov_b32 s58, 0x30003\n" \
"s_mov_b32 s59, 0xc000c\n" \
"v_perm_b32 v214, v213, v28, s62\n" \
"v_perm_b32 v215, v28, v213, s62\n" \
"v_cndmask_b32_e32 v214, v214, v215, vcc\n" \
"s_mov_b32 vcc_lo, 0xf0f0f0f0\n" \
"s_mov_b32 vcc_hi, 0xf0f0f0f0\n" \
"v_mov_b32_dpp v213, v214 row_ror:4 row_mask:0xf bank_mask:0xf\n" \
"v_cndmask_b32_e32 v164, v214, v213, vcc\n" \
"v_cndmask_b32_e32 v165, v213, v214, vcc\n" \
"v_mov_b32_e32 v166, v164\n" \
"v_mov_b32_e32 v167, v165\n" \
"s_nop 0\n" \
"v_permlane32_swap_b32_e32 v164, v166\n" \
"v_permlane32_swap_b32_e32 v165, v167\n" \
"s_nop 0\n" \
"v_permlane16_swap_b32_e32 v164, v166\n" \
"v_permlane16_swap_b32_e32 v165, v167\n" \
"v_cndmask_b32_e64 v176, 0, v164, s[58:59]\n" \
"v_cndmask_b32_e64 v177, 0, v165, s[58:59]\n" \
"v_cndmask_b32_e64 v178, 0, v166, s[58:59]\n" \
"v_cndmask_b32_e64 v179, 0, v167, s[58:59]\n" \
"s_lshl_b64 s[58:59], s[58:59], 4\n" \
"v_cndmask_b32_e64 v180, 0, v164, s[58:59]\n" \
"v_cndmask_b32_e64 v181, 0, v165, s[58:59]\n" \
"v_cndmask_b32_e64 v182, 0, v166, s[58:59]\n" \
"v_cndmask_b32_e64 v183, 0, v167, s[58:59]\n" \
"s_lshl_b64 s[58:59], s[58:59], 4\n" \
"v_mfma_f32_16x16x32_bf16 v[168:171], v[176:179], a[0:3], 0\n" \
"v_mfma_f32_16x16x32_bf16 v[172:175], v[176:179], a[16:19], 0\n" \
"v_cndmask_b32_e64 v184, 0, v164, s[58:59]\n" \
"v_cndmask_b32_e64 v185, 0, v165, s[58:59]\n" \
"v_cndmask_b32_e64 v186, 0, v166, s[58:59]\n" \
"v_cndmask_b32_e64 v187, 0, v167, s[58:59]\n" \
"s_lshl_b64 s[58:59], s[58:59], 4\n" \
"v_mfma_f32_16x16x32_bf16 v[168:171], v[180:183], a[4:7], v[168:171]\n" \
"v_mfma_f32_16x16x32_bf16 v[172:175], v[180:183], a[20:23], v[172:175]\n" \
"v_cndmask_b32_e64 v136, 0, v164, s[58:59]\n" \
"v_cndmask_b32_e64 v137, 0, v165, s[58:59]\n" \
"v_cndmask_b32_e64 v138, 0, v166, s[58:59]\n" \
"v_cndmask_b32_e64 v139, 0, v167, s[58:59]\n" \
"v_mfma_f32_16x16x32_bf16 v[168:171], v[184:187], a[8:11], v[168:171]\n" \
"v_mfma_f32_16x16x32_bf16 v[172:175], v[184:187], a[24:27], v[172:175]\n" \
"v_mfma_f32_16x16x32_bf16 v[168:171], v[136:139], a[12:15], v[168:171]\n" \
"v_mfma_f32_16x16x32_bf16 v[172:175], v[136:139], a[28:31], v[172:175]\n" \
"ds_read_b128 v[64:67], %[kb0] offset:0\n" \
"ds_read_b128 v[68:71], %[kb0] offset:1024\n" \
"ds_read_b128 v[72:75], %[vvb] offset:0\n" \
"ds_read_b128 v[76:79], %[vvb] offset:1024\n" \
"ds_read_b128 v[80:83], %[kb0] offset:4096\n" \
"ds_read_b128 v[84:87], %[kb0] offset:5120\n" \
"ds_read_b128 v[88:91], %[vvb] offset:4096\n" \
"ds_read_b128 v[92:95], %[vvb] offset:5120\n" \
"v_and_or_b32 v168, v168, s42, v227\n" \
"v_and_or_b32 v169, v169, s42, v227\n" \
"v_and_or_b32 v170, v170, s42, v227\n" \
"v_and_or_b32 v171, v171, s42, v227\n" \
"buffer_store_dwordx4 v[168:171], v217, %[srdz], s69 offen offset:0 sc1\n" \
"v_and_or_b32 v172, v172, s42, v227\n" \
"v_and_or_b32 v173, v173, s42, v227\n" \
"v_and_or_b32 v174, v174, s42, v227\n" \
"v_and_or_b32 v175, v175, s42, v227\n" \
"buffer_store_dwordx4 v[172:175], v217, %[srdz], s69 offen offset:1024 sc1\n" \
"s_movk_i32 s62, -128\n" \
"s_sub_u32 s63, 65536, s40\n" \
"v_mul_i32_i24_e64 v208, v219, s62\n" \
"v_add_u32_e32 v208, s63, v208\n" \
"v_lshl_add_u32 v209, %[oq0], 1, v208\n" \
"v_lshl_add_u32 v210, %[wo0], 1, v208\n" \
"ds_write_b128 v209, v[144:147] offset:0\n" \
"ds_write_b128 v209, v[148:151] offset:16\n" \
"ds_write_b128 v210, v[152:155] offset:0\n" \
"ds_write_b128 v210, v[156:159] offset:16\n" \
"v_mov_b32_dpp v211, v4 quad_perm:[1,0,3,2] row_mask:0xf bank_mask:0xf\n" \
"v_mov_b32_dpp v213, v132 quad_perm:[1,0,3,2] row_mask:0xf bank_mask:0xf\n" \
"v_mov_b32_dpp v215, v20 quad_perm:[1,0,3,2] row_mask:0xf bank_mask:0xf\n" \
"v_cndmask_b32_e64 v4, v4, v211, s[54:55]\n" \
"v_cndmask_b32_e64 v132, v132, v213, s[54:55]\n" \
"v_cndmask_b32_e64 v20, v20, v215, s[54:55]\n" \
"v_mov_b32_dpp v212, v4 row_ror:8 row_mask:0xf bank_mask:0xf\n" \
"v_mov_b32_dpp v214, v132 row_ror:8 row_mask:0xf bank_mask:0xf\n" \
"v_mov_b32_dpp v208, v20 row_ror:8 row_mask:0xf bank_mask:0xf\n" \
"v_cndmask_b32_e64 v4, v4, v212, s[56:57]\n" \
"v_cndmask_b32_e64 v132, v132, v214, s[56:57]\n" \
"v_cndmask_b32_e64 v20, v20, v208, s[56:57]\n" \
"v_mov_b32_e32 v126, v132\n" \
"v_and_b32_e32 v125, 3, v133\n" \
"s_bitcmp1_b32 s60, 0\n" \
"s_cselect_b32 s56, s45, s44\n" \
"s_cselect_b32 s57, s47, s46\n" \
"s_bitcmp1_b32 s60, 1\n" \
"s_cselect_b32 s60, s57, s56\n" \
"s_bitcmp1_b32 s61, 0\n" \
"s_cselect_b32 s56, s45, s44\n" \
"s_cselect_b32 s57, s47, s46\n" \
"s_bitcmp1_b32 s61, 1\n" \
"s_cselect_b32 s61, s57, s56\n" \
"s_cmp_lg_u64 s[50:51], 0\n" \
"s_cselect_b32 s53, 1, 0\n" \
"s_mov_b32 s51, 0x10000\n" \
"s_cmp_lg_u32 s41, 1\n" \
"s_cbranch_scc1 TLls_%=\n" \
"v_mul_f32_e64 v208, -v218, v220\n" \
"v_pk_fma_f32 v[128:129], v[0:1], v[220:221], v[208:209] op_sel_hi:[1,0,0]\n" \
"v_pk_fma_f32 v[130:131], v[2:3], v[220:221], v[208:209] op_sel_hi:[1,0,0]\n" \
"v_pk_fma_f32 v[132:133], v[8:9], v[220:221], v[208:209] op_sel_hi:[1,0,0]\n" \
"v_pk_fma_f32 v[134:135], v[10:11], v[220:221], v[208:209] op_sel_hi:[1,0,0]\n" \
"v_pk_fma_f32 v[136:137], v[16:17], v[220:221], v[208:209] op_sel_hi:[1,0,0]\n" \
"v_pk_fma_f32 v[138:139], v[18:19], v[220:221], v[208:209] op_sel_hi:[1,0,0]\n" \
"v_pk_fma_f32 v[140:141], v[24:25], v[220:221], v[208:209] op_sel_hi:[1,0,0]\n" \
"v_pk_fma_f32 v[142:143], v[26:27], v[220:221], v[208:209] op_sel_hi:[1,0,0]\n" \
"v_pk_fma_f32 v[144:145], v[32:33], v[220:221], v[208:209] op_sel_hi:[1,0,0]\n" \
"v_pk_fma_f32 v[146:147], v[34:35], v[220:221], v[208:209] op_sel_hi:[1,0,0]\n" \
"v_pk_fma_f32 v[148:149], v[40:41], v[220:221], v[208:209] op_sel_hi:[1,0,0]\n" \
"v_pk_fma_f32 v[150:151], v[42:43], v[220:221], v[208:209] op_sel_hi:[1,0,0]\n" \
"v_pk_fma_f32 v[152:153], v[48:49], v[220:221], v[208:209] op_sel_hi:[1,0,0]\n" \
"v_pk_fma_f32 v[154:155], v[50:51], v[220:221], v[208:209] op_sel_hi:[1,0,0]\n" \
"v_pk_fma_f32 v[156:157], v[56:57], v[220:221], v[208:209] op_sel_hi:[1,0,0]\n" \
"v_pk_fma_f32 v[158:159], v[58:59], v[220:221], v[208:209] op_sel_hi:[1,0,0]\n" \
"v_sub_u32_e32 v216, 0x7ff, %[ta0]\n" \
"v_cmp_eq_u32_e64 s[56:57], s60, v216\n" \
"v_cmp_eq_u32_e64 s[58:59], s61, v216\n" \
"s_cmp_eq_u32 %[wid], 3\n" \
"s_cselect_b32 s49, s49, 0x80000000\n" \
"v_lshrrev_b32_e32 v216, 2, v224\n" \
"s_waitcnt lgkmcnt(0)\n" \
"v_lshlrev_b32_e32 v160, 16, v192\n" \
"v_and_b32_e32 v161, s43, v192\n" \
"v_lshlrev_b32_e32 v176, 16, v64\n" \
"v_and_b32_e32 v177, s43, v64\n" \
"v_lshlrev_b32_e32 v162, 16, v193\n" \
"v_and_b32_e32 v163, s43, v193\n" \
"v_lshlrev_b32_e32 v178, 16, v65\n" \
"v_and_b32_e32 v179, s43, v65\n" \
"v_lshlrev_b32_e32 v164, 16, v196\n" \
"v_and_b32_e32 v165, s43, v196\n" \
"v_lshlrev_b32_e32 v180, 16, v68\n" \
"v_and_b32_e32 v181, s43, v68\n" \
"v_lshlrev_b32_e32 v166, 16, v197\n" \
"v_and_b32_e32 v167, s43, v197\n" \
"v_lshlrev_b32_e32 v182, 16, v69\n" \
"v_and_b32_e32 v183, s43, v69\n" \
"v_pk_fma_f32 v[128:129], v[128:129], v[160:161], v[176:177]\n" \
"v_pk_fma_f32 v[130:131], v[130:131], v[162:163], v[178:179]\n" \
"v_pk_fma_f32 v[132:133], v[132:133], v[164:165], v[180:181]\n" \
"v_pk_fma_f32 v[134:135], v[134:135], v[166:167], v[182:183]\n" \
"v_lshlrev_b32_e32 v168, 16, v80\n" \
"v_and_b32_e32 v169, s43, v80\n" \
"v_lshlrev_b32_e32 v170, 16, v81\n" \
"v_and_b32_e32 v171, s43, v81\n" \
"v_lshlrev_b32_e32 v172, 16, v84\n" \
"v_and_b32_e32 v173, s43, v84\n" \
"v_lshlrev_b32_e32 v174, 16, v85\n" \
"v_and_b32_e32 v175, s43, v85\n" \
"v_pk_fma_f32 v[128:129], v[128:129], v[168:169], v[0:1]\n" \
"v_pk_fma_f32 v[130:131], v[130:131], v[170:171], v[2:3]\n" \
"v_pk_fma_f32 v[132:133], v[132:133], v[172:173], v[8:9]\n" \
"v_pk_fma_f32 v[134:135], v[134:135], v[174:175], v[10:11]\n" \
"v_cvt_pk_bf16_f32 v128, v128, v129\n" \
"v_cvt_pk_bf16_f32 v130, v130, v131\n" \
"v_cvt_pk_bf16_f32 v132, v132, v133\n" \
"v_cvt_pk_bf16_f32 v134, v134, v135\n" \
"v_lshlrev_b32_e32 v160, 16, v194\n" \
"v_and_b32_e32 v161, s43, v194\n" \
"v_lshlrev_b32_e32 v176, 16, v66\n" \
"v_and_b32_e32 v177, s43, v66\n" \
"v_lshlrev_b32_e32 v162, 16, v195\n" \
"v_and_b32_e32 v163, s43, v195\n" \
"v_lshlrev_b32_e32 v178, 16, v67\n" \
"v_and_b32_e32 v179, s43, v67\n" \
"v_lshlrev_b32_e32 v164, 16, v198\n" \
"v_and_b32_e32 v165, s43, v198\n" \
"v_lshlrev_b32_e32 v180, 16, v70\n" \
"v_and_b32_e32 v181, s43, v70\n" \
"v_lshlrev_b32_e32 v166, 16, v199\n" \
"v_and_b32_e32 v167, s43, v199\n" \
"v_lshlrev_b32_e32 v182, 16, v71\n" \
"v_and_b32_e32 v183, s43, v71\n" \
"v_pk_fma_f32 v[136:137], v[136:137], v[160:161], v[176:177]\n" \
"v_pk_fma_f32 v[138:139], v[138:139], v[162:163], v[178:179]\n" \
"v_pk_fma_f32 v[140:141], v[140:141], v[164:165], v[180:181]\n" \
"v_pk_fma_f32 v[142:143], v[142:143], v[166:167], v[182:183]\n" \
"v_lshlrev_b32_e32 v168, 16, v82\n" \
"v_and_b32_e32 v169, s43, v82\n" \
"v_lshlrev_b32_e32 v170, 16, v83\n" \
"v_and_b32_e32 v171, s43, v83\n" \
"v_lshlrev_b32_e32 v172, 16, v86\n" \
"v_and_b32_e32 v173, s43, v86\n" \
"v_lshlrev_b32_e32 v174, 16, v87\n" \
"v_and_b32_e32 v175, s43, v87\n" \
"v_pk_fma_f32 v[136:137], v[136:137], v[168:169], v[16:17]\n" \
"v_pk_fma_f32 v[138:139], v[138:139], v[170:171], v[18:19]\n" \
"v_pk_fma_f32 v[140:141], v[140:141], v[172:173], v[24:25]\n" \
"v_pk_fma_f32 v[142:143], v[142:143], v[174:175], v[26:27]\n" \
"v_cvt_pk_bf16_f32 v136, v136, v137\n" \
"v_cvt_pk_bf16_f32 v138, v138, v139\n" \
"v_cvt_pk_bf16_f32 v140, v140, v141\n" \
"v_cvt_pk_bf16_f32 v142, v142, v143\n" \
"buffer_load_dwordx4 v[112:115], v216, %[srdz], s49 offen offset:0 sc1\n" \
"buffer_load_dwordx4 v[116:119], v216, %[srdz], s49 offen offset:16 sc1\n" \
"v_lshlrev_b32_e32 v160, 16, v200\n" \
"v_and_b32_e32 v161, s43, v200\n" \
"v_lshlrev_b32_e32 v176, 16, v72\n" \
"v_and_b32_e32 v177, s43, v72\n" \
"v_lshlrev_b32_e32 v162, 16, v201\n" \
"v_and_b32_e32 v163, s43, v201\n" \
"v_lshlrev_b32_e32 v178, 16, v73\n" \
"v_and_b32_e32 v179, s43, v73\n" \
"v_lshlrev_b32_e32 v164, 16, v204\n" \
"v_and_b32_e32 v165, s43, v204\n" \
"v_lshlrev_b32_e32 v180, 16, v76\n" \
"v_and_b32_e32 v181, s43, v76\n" \
"v_lshlrev_b32_e32 v166, 16, v205\n" \
"v_and_b32_e32 v167, s43, v205\n" \
"v_lshlrev_b32_e32 v182, 16, v77\n" \
"v_and_b32_e32 v183, s43, v77\n" \
"v_pk_fma_f32 v[144:145], v[144:145], v[160:161], v[176:177]\n" \
"v_pk_fma_f32 v[146:147], v[146:147], v[162:163], v[178:179]\n" \
"v_pk_fma_f32 v[148:149], v[148:149], v[164:165], v[180:181]\n" \
"v_pk_fma_f32 v[150:151], v[150:151], v[166:167], v[182:183]\n" \
"v_lshlrev_b32_e32 v168, 16, v88\n" \
"v_and_b32_e32 v169, s43, v88\n" \
"v_lshlrev_b32_e32 v170, 16, v89\n" \
"v_and_b32_e32 v171, s43, v89\n" \
"v_lshlrev_b32_e32 v172, 16, v92\n" \
"v_and_b32_e32 v173, s43, v92\n" \
"v_lshlrev_b32_e32 v174, 16, v93\n" \
"v_and_b32_e32 v175, s43, v93\n" \
"v_pk_fma_f32 v[144:145], v[144:145], v[168:169], v[32:33]\n" \
"v_pk_fma_f32 v[146:147], v[146:147], v[170:171], v[34:35]\n" \
"v_pk_fma_f32 v[148:149], v[148:149], v[172:173], v[40:41]\n" \
"v_pk_fma_f32 v[150:151], v[150:151], v[174:175], v[42:43]\n" \
"v_cvt_pk_bf16_f32 v144, v144, v145\n" \
"v_cvt_pk_bf16_f32 v146, v146, v147\n" \
"v_cvt_pk_bf16_f32 v148, v148, v149\n" \
"v_cvt_pk_bf16_f32 v150, v150, v151\n" \
"v_lshlrev_b32_e32 v160, 16, v202\n" \
"v_and_b32_e32 v161, s43, v202\n" \
"v_lshlrev_b32_e32 v176, 16, v74\n" \
"v_and_b32_e32 v177, s43, v74\n" \
"v_lshlrev_b32_e32 v162, 16, v203\n" \
"v_and_b32_e32 v163, s43, v203\n" \
"v_lshlrev_b32_e32 v178, 16, v75\n" \
"v_and_b32_e32 v179, s43, v75\n" \
"v_lshlrev_b32_e32 v164, 16, v206\n" \
"v_and_b32_e32 v165, s43, v206\n" \
"v_lshlrev_b32_e32 v180, 16, v78\n" \
"v_and_b32_e32 v181, s43, v78\n" \
"v_lshlrev_b32_e32 v166, 16, v207\n" \
"v_and_b32_e32 v167, s43, v207\n" \
"v_lshlrev_b32_e32 v182, 16, v79\n" \
"v_and_b32_e32 v183, s43, v79\n" \
"v_pk_fma_f32 v[152:153], v[152:153], v[160:161], v[176:177]\n" \
"v_pk_fma_f32 v[154:155], v[154:155], v[162:163], v[178:179]\n" \
"v_pk_fma_f32 v[156:157], v[156:157], v[164:165], v[180:181]\n" \
"v_pk_fma_f32 v[158:159], v[158:159], v[166:167], v[182:183]\n" \
"v_lshlrev_b32_e32 v168, 16, v90\n" \
"v_and_b32_e32 v169, s43, v90\n" \
"v_lshlrev_b32_e32 v170, 16, v91\n" \
"v_and_b32_e32 v171, s43, v91\n" \
"v_lshlrev_b32_e32 v172, 16, v94\n" \
"v_and_b32_e32 v173, s43, v94\n" \
"v_lshlrev_b32_e32 v174, 16, v95\n" \
"v_and_b32_e32 v175, s43, v95\n" \
"v_pk_fma_f32 v[152:153], v[152:153], v[168:169], v[48:49]\n" \
"v_pk_fma_f32 v[154:155], v[154:155], v[170:171], v[50:51]\n" \
"v_pk_fma_f32 v[156:157], v[156:157], v[172:173], v[56:57]\n" \
"v_pk_fma_f32 v[158:159], v[158:159], v[174:175], v[58:59]\n" \
"v_cvt_pk_bf16_f32 v152, v152, v153\n" \
"v_cvt_pk_bf16_f32 v154, v154, v155\n" \
"v_cvt_pk_bf16_f32 v156, v156, v157\n" \
"v_cvt_pk_bf16_f32 v158, v158, v159\n" \
"s_or_b64 %[lat], s[56:57], s[58:59]\n" \
"s_mov_b32 vcc_lo, 0x55555555\n" \
"s_mov_b32 vcc_hi, 0x55555555\n" \
"v_bfrev_b32_e32 v208, 1\n" \
"v_cndmask_b32_e64 v208, %[vd0], v208, %[lat]\n" \
"v_cndmask_b32_dpp v96, v136, v128, vcc quad_perm:[1,0,3,2] row_mask:0xf bank_mask:0xf\n" \
"v_cndmask_b32_dpp v97, v138, v130, vcc quad_perm:[1,0,3,2] row_mask:0xf bank_mask:0xf\n" \
"v_cndmask_b32_dpp v100, v140, v132, vcc quad_perm:[1,0,3,2] row_mask:0xf bank_mask:0xf\n" \
"v_cndmask_b32_dpp v101, v142, v134, vcc quad_perm:[1,0,3,2] row_mask:0xf bank_mask:0xf\n" \
"v_cndmask_b32_dpp v104, v152, v144, vcc quad_perm:[1,0,3,2] row_mask:0xf bank_mask:0xf\n" \
"v_cndmask_b32_dpp v105, v154, v146, vcc quad_perm:[1,0,3,2] row_mask:0xf bank_mask:0xf\n" \
"v_cndmask_b32_dpp v108, v156, v148, vcc quad_perm:[1,0,3,2] row_mask:0xf bank_mask:0xf\n" \
"v_cndmask_b32_dpp v109, v158, v150, vcc quad_perm:[1,0,3,2] row_mask:0xf bank_mask:0xf\n" \
"s_not_b64 vcc, vcc\n" \
"v_xor_b32_e32 v209, 0x80, v208\n" \
"v_cndmask_b32_dpp v98, v128, v136, vcc quad_perm:[1,0,3,2] row_mask:0xf bank_mask:0xf\n" \
"v_cndmask_b32_dpp v99, v130, v138, vcc quad_perm:[1,0,3,2] row_mask:0xf bank_mask:0xf\n" \
"v_cndmask_b32_dpp v102, v132, v140, vcc quad_perm:[1,0,3,2] row_mask:0xf bank_mask:0xf\n" \
"v_cndmask_b32_dpp v103, v134, v142, vcc quad_perm:[1,0,3,2] row_mask:0xf bank_mask:0xf\n" \
"v_cndmask_b32_dpp v106, v144, v152, vcc quad_perm:[1,0,3,2] row_mask:0xf bank_mask:0xf\n" \
"v_cndmask_b32_dpp v107, v146, v154, vcc quad_perm:[1,0,3,2] row_mask:0xf bank_mask:0xf\n" \
"v_cndmask_b32_dpp v110, v148, v156, vcc quad_perm:[1,0,3,2] row_mask:0xf bank_mask:0xf\n" \
"v_cndmask_b32_dpp v111, v150, v158, vcc quad_perm:[1,0,3,2] row_mask:0xf bank_mask:0xf\n" \
"buffer_store_dwordx4 v[96:99], v208, s[64:67], 0 offen offset:0 sc1\n" \
"buffer_store_dwordx4 v[100:103], v208, s[64:67], 0 offen offset:1024 sc1\n" \
"buffer_store_dwordx4 v[104:107], v209, s[64:67], 0 offen offset:0 sc1\n" \
"buffer_store_dwordx4 v[108:111], v209, s[64:67], 0 offen offset:1024 sc1\n" \
"TLls_%=:\n" \
"v_readlane_b32 s63, %[tlo], 28\n" \
"v_accvgpr_read_b32 v124, a32\n" \
"v_accvgpr_read_b32 v214, a33\n" \
"v_accvgpr_read_b32 v73, a34\n" \
"s_add_u32 s62, s40, 65536\n" \
"v_lshl_add_u32 v125, v125, 11, v214\n" \
"v_mov_b32_e32 v72, s40\n" \
"v_lshl_add_u32 v125, v125, 1, s62\n" \
"v_add_u32_e32 v214, s40, v214\n" \
"v_and_b32_e32 v74, 1, v222\n" \
"s_add_u32 s58, s63, 0x100000\n" \
"s_lshl_b32 s59, s41, 2\n" \
"s_sub_u32 s59, s59, %[wid]\n" \
"s_cmp_eq_u32 s59, 1\n" \
"s_cselect_b32 s59, 1, 0\n" \
"s_cbranch_scc0 TLkw_%=\n" \
"s_waitcnt vmcnt(4)\n" \
"TLk2_%=:\n" \
"v_min3_u32 v64, v112, v113, v114\n" \
"v_min3_u32 v65, v115, v116, v117\n" \
"v_min3_u32 v64, v64, v118, v119\n" \
"v_min_u32_e32 v64, v64, v65\n" \
"v_cmp_eq_u32_e32 vcc, 0, v64\n" \
"s_cbranch_vccnz TLk1_%=\n" \
"TLkw_%=:\n" \
"s_waitcnt lgkmcnt(0)\n" \
"s_barrier\n" \
"buffer_load_dwordx4 v[128:131], v73, %[srdz], s63 offen offset:0 sc1\n" \
"buffer_load_dwordx4 v[132:135], v73, %[srdz], s58 offen offset:0 sc1\n" \
"ds_read_b32 v120, v125 offset:0\n" \
"ds_read_u16 v121, v214 offset:2048\n" \
"ds_read_u16 v212, v214 offset:0\n" \
"ds_read_u16 v123, v214 offset:4096\n" \
"ds_read_b128 v[68:71], v72 offset:14384\n" \
"s_cmp_eq_u32 s59, 0\n" \
"s_cbranch_scc0 TLg1_%=\n" \
"s_waitcnt lgkmcnt(0)\n" \
"v_lshlrev_b32_e32 v121, 16, v121\n" \
"v_lshlrev_b32_e32 v212, 16, v212\n" \
"v_lshlrev_b32_e32 v123, 16, v123\n" \
"v_sub_f32_e32 v213, v120, v4\n" \
"v_mul_f32_e32 v213, v213, v126\n" \
"v_fma_f32 v127, v213, v121, v212\n" \
"v_add_f32_e32 v120, v120, v191\n" \
"v_add_f32_e32 v64, v68, v69\n" \
"v_add_f32_e32 v65, v70, v71\n" \
"v_add_f32_e32 v122, v64, v65\n" \
"v_add_f32_e32 v122, v122, v20\n" \
"v_cvt_pk_bf16_f32 v122, 0, v122\n" \
"v_mul_f32_e32 v122, 0xbfb8aa3b, v122\n" \
"v_exp_f32_e32 v122, v122\n" \
"s_nop 0\n" \
"v_add_f32_e32 v122, 1.0, v122\n" \
"v_rcp_f32_e32 v122, v122\n" \
"s_nop 0\n" \
"v_cvt_pk_bf16_f32 v122, 0, v122\n" \
"TLg2_%=:\n" \
"s_lshl_b32 s62, %[wid], 5\n" \
"v_lshrrev_b32_e32 v66, 2, v222\n" \
"s_add_u32 s62, s62, s40\n" \
"v_and_b32_e32 v66, 12, v66\n" \
"v_lshl_or_b32 v66, v74, 4, v66\n" \
"v_add_u32_e32 v66, s62, v66\n" \
"TLg3_%=:\n" \
"s_waitcnt vmcnt(0)\n" \
"v_bitop3_b32 v65, v132, v133, v134 bitop3:0x80\n" \
"v_bitop3_b32 v67, v128, v129, v130 bitop3:0x80\n" \
"v_bitop3_b32 v65, v65, v135, v131 bitop3:0x80\n" \
"v_pk_add_f32 v[128:129], v[128:129], v[132:133]\n" \
"v_bitop3_b32 v65, v65, v67, s41 bitop3:0x80\n" \
"v_pk_add_f32 v[130:131], v[130:131], v[134:135]\n" \
"v_cmp_eq_u32_e32 vcc, 0, v65\n" \
"s_nop 0\n" \
"v_permlane32_swap_b32_e32 v128, v130\n" \
"v_permlane32_swap_b32_e32 v129, v131\n" \
"v_add_f32_e32 v128, v128, v130\n" \
"v_add_f32_e32 v129, v129, v131\n" \
"s_cbranch_vccnz TLga_%=\n" \
"TLgb_%=:\n" \
"s_nop 0\n" \
"v_permlane16_swap_b32_e32 v128, v129\n" \
"v_add_f32_e32 v128, v128, v129\n" \
"s_nop 1\n" \
"v_add_f32_dpp v128, v128, v128 row_ror:8 row_mask:0xf bank_mask:0xf\n" \
"s_nop 1\n" \
"v_add_f32_dpp v128, v128, v128 row_ror:4 row_mask:0xf bank_mask:0xf\n" \
"s_nop 1\n" \
"v_add_f32_dpp v128, v128, v128 quad_perm:[2,3,0,1] row_mask:0xf bank_mask:0xf\n" \
"ds_write_b32 v66, v128 offset:29696\n" \
"v_and_b32_e32 v65, 7, v222\n" \
"v_mov_b32_e32 v74, s60\n" \
"v_lshl_add_u32 v65, v65, 2, s40\n" \
"v_mov_b32_e32 v75, s61\n" \
"s_mov_b32 vcc_lo, 0xaaaaaaaa\n" \
"s_min_u32 s56, s60, s61\n" \
"v_cndmask_b32_e32 v74, v74, v75, vcc\n" \
"s_max_u32 s57, s60, s61\n" \
"v_lshl_or_b32 v74, v74, 10, v124\n" \
"v_lshlrev_b32_e32 v74, 1, v74\n" \
"s_waitcnt lgkmcnt(0)\n" \
"s_barrier\n" \
"ds_read_b64 v[66:67], v72 offset:14432\n" \
"ds_read_b32 v68, v65 offset:29696\n" \
"ds_read_b32 v69, v65 offset:29728\n" \
"ds_read_b32 v70, v65 offset:29760\n" \
"ds_read_b32 v71, v65 offset:29792\n" \
"s_waitcnt lgkmcnt(4)\n" \
"v_readfirstlane_b32 %[k2], v66\n" \
"v_readfirstlane_b32 %[k1], v67\n" \
"s_waitcnt lgkmcnt(0)\n" \
"v_add_f32_e32 v68, v68, v69\n" \
"v_add_f32_e32 v70, v70, v71\n" \
"s_andn2_b32 %[k1], 0x7ff, %[k1]\n" \
"s_andn2_b32 %[k2], 0x7ff, %[k2]\n" \
"v_add_f32_e32 v68, v68, v70\n" \
"s_min_u32 s62, %[k1], %[k2]\n" \
"s_max_u32 s63, %[k1], %[k2]\n" \
"v_fma_f32 v68, v68, v122, v127\n" \
"s_cmp_lg_u64 s[62:63], s[56:57]\n" \
"s_cselect_b32 %[got], 2, 0\n" \
"v_fma_f32 v68, v68, v123, v120\n" \
"s_or_b32 %[got], %[got], s53\n" \
"s_cbranch_scc1 TLagain_%=\n" \
"TLfin_%=:\n" \
"s_lshr_b32 %[got], s41, 1\n" \
"s_cmp_lg_u32 %[wid], 0\n" \
"s_cselect_b32 s58, 0x80000000, 0\n" \
"v_cvt_pk_bf16_f32 v68, v68, v68\n" \
"s_mov_b64 exec, 0xff\n" \
"buffer_store_short v68, v74, s[64:67], s58 offen offset:0 sc1\n" \
"s_mov_b64 exec, -1\n" \
"s_branch Lend_%=\n" \
"TLpk_%=:\n" \
"v_readlane_b32 s62, %[tlo], 35\n" \
"v_lshrrev_b32_e32 v72, 2, v224\n" \
"s_mov_b32 s51, 0x10000\n" \
"s_nop 4\n" \
"TLpk1_%=:\n" \
"buffer_load_dwordx4 v[64:67], v72, %[srdz], s62 offen offset:0 sc1\n" \
"buffer_load_dwordx4 v[68:71], v72, %[srdz], s62 offen offset:16 sc1\n" \
"s_waitcnt vmcnt(0)\n" \
"v_min3_u32 v208, v64, v65, v66\n" \
"v_min3_u32 v209, v67, v68, v69\n" \
"v_min3_u32 v208, v208, v70, v71\n" \
"v_min_u32_e32 v208, v208, v209\n" \
"v_cmp_eq_u32_e32 vcc, 0, v208\n" \
"s_sub_u32 s51, s51, 1\n" \
"s_cbranch_scc1 TLpkb_%=\n" \
"s_cbranch_vccz TLpkb_%=\n" \
"s_sleep 8\n" \
"s_branch TLpk1_%=\n" \
"TLrare_%=:\n" \
"v_cmp_u_f32_e32 vcc, v14, v15\n" \
"s_cbranch_vccz TLvar_%=\n" \
"s_sub_u32 s52, s52, 1\n" \
"s_cbranch_scc1 TLvar_%=\n" \
"buffer_load_dwordx4 a[164:167], v217, %[srdz], s54 offen offset:0 sc1\n" \
"buffer_load_dwordx4 a[168:171], v217, %[srdz], s54 offen offset:1024 sc1\n" \
"buffer_load_dwordx4 a[172:175], v217, %[srdz], s54 offen offset:2048 sc1\n" \
"buffer_load_dwordx4 a[176:179], v217, %[srdz], s54 offen offset:3072 sc1\n" \
"buffer_load_dwordx4 a[180:183], v217, %[srdz], s55 offen offset:0 sc1\n" \
"buffer_load_dwordx4 a[184:187], v217, %[srdz], s55 offen offset:1024 sc1\n" \
"buffer_load_dwordx4 a[188:191], v217, %[srdz], s55 offen offset:2048 sc1\n" \
"buffer_load_dwordx4 a[192:195], v217, %[srdz], s55 offen offset:3072 sc1\n" \
"v_accvgpr_read_b32 v33, a248\n" \
"v_lshlrev_b32_e32 v32, 16, v33\n" \
"v_and_b32_e32 v33, s43, v33\n" \
"v_accvgpr_read_b32 v35, a249\n" \
"v_lshlrev_b32_e32 v34, 16, v35\n" \
"v_and_b32_e32 v35, s43, v35\n" \
"v_accvgpr_read_b32 v41, a252\n" \
"v_lshlrev_b32_e32 v40, 16, v41\n" \
"v_and_b32_e32 v41, s43, v41\n" \
"v_accvgpr_read_b32 v43, a253\n" \
"v_lshlrev_b32_e32 v42, 16, v43\n" \
"v_and_b32_e32 v43, s43, v43\n" \
"v_accvgpr_read_b32 v49, a250\n" \
"v_lshlrev_b32_e32 v48, 16, v49\n" \
"v_and_b32_e32 v49, s43, v49\n" \
"v_accvgpr_read_b32 v51, a251\n" \
"v_lshlrev_b32_e32 v50, 16, v51\n" \
"v_and_b32_e32 v51, s43, v51\n" \
"v_accvgpr_read_b32 v57, a254\n" \
"v_lshlrev_b32_e32 v56, 16, v57\n" \
"v_and_b32_e32 v57, s43, v57\n" \
"v_accvgpr_read_b32 v59, a255\n" \
"v_lshlrev_b32_e32 v58, 16, v59\n" \
"v_and_b32_e32 v59, s43, v59\n" \
"v_pk_mul_f32 v[6:7], v[0:1], v[160:161]\n" \
"v_pk_mul_f32 v[12:13], v[0:1], v[0:1]\n" \
"v_pk_add_f32 v[4:5], v[0:1], v[2:3]\n" \
"v_pk_fma_f32 v[6:7], v[2:3], v[162:163], v[6:7]\n" \
"v_pk_fma_f32 v[12:13], v[2:3], v[2:3], v[12:13]\n" \
"v_pk_add_f32 v[4:5], v[4:5], v[8:9]\n" \
"v_pk_fma_f32 v[6:7], v[8:9], v[168:169], v[6:7]\n" \
"v_pk_fma_f32 v[12:13], v[8:9], v[8:9], v[12:13]\n" \
"v_pk_add_f32 v[4:5], v[4:5], v[10:11]\n" \
"v_pk_fma_f32 v[6:7], v[10:11], v[170:171], v[6:7]\n" \
"v_pk_fma_f32 v[12:13], v[10:11], v[10:11], v[12:13]\n" \
"v_pk_add_f32 v[4:5], v[4:5], v[16:17]\n" \
"v_pk_fma_f32 v[6:7], v[16:17], v[164:165], v[6:7]\n" \
"v_pk_fma_f32 v[12:13], v[16:17], v[16:17], v[12:13]\n" \
"v_pk_add_f32 v[4:5], v[4:5], v[18:19]\n" \
"v_pk_fma_f32 v[6:7], v[18:19], v[166:167], v[6:7]\n" \
"v_pk_fma_f32 v[12:13], v[18:19], v[18:19], v[12:13]\n" \
"v_pk_add_f32 v[4:5], v[4:5], v[24:25]\n" \
"v_pk_fma_f32 v[6:7], v[24:25], v[172:173], v[6:7]\n" \
"v_pk_fma_f32 v[12:13], v[24:25], v[24:25], v[12:13]\n" \
"v_pk_add_f32 v[4:5], v[4:5], v[26:27]\n" \
"v_pk_fma_f32 v[6:7], v[26:27], v[174:175], v[6:7]\n" \
"v_pk_fma_f32 v[12:13], v[26:27], v[26:27], v[12:13]\n" \
"s_waitcnt vmcnt(0)\n" \
"s_nop 1\n" \
"v_mfma_f32_16x16x32_bf16 v[208:211], v[230:233], a[164:167], 0\n" \
"v_mfma_f32_16x16x32_bf16 v[212:215], v[230:233], a[172:175], 0\n" \
"v_mfma_f32_16x16x32_bf16 v[208:211], v[234:237], a[168:171], v[208:211]\n" \
"v_mfma_f32_16x16x32_bf16 v[212:215], v[234:237], a[176:179], v[212:215]\n" \
"s_nop 6\n" \
"v_pk_fma_f32 v[32:33], v[208:209], v[144:145], v[32:33]\n" \
"v_pk_fma_f32 v[34:35], v[210:211], v[146:147], v[34:35]\n" \
"v_pk_fma_f32 v[40:41], v[212:213], v[152:153], v[40:41]\n" \
"v_pk_fma_f32 v[42:43], v[214:215], v[154:155], v[42:43]\n" \
"v_pk_mul_f32 v[36:37], v[32:33], v[176:177]\n" \
"v_pk_mul_f32 v[38:39], v[32:33], v[32:33]\n" \
"v_pk_add_f32 v[14:15], v[32:33], v[34:35]\n" \
"v_pk_fma_f32 v[36:37], v[34:35], v[178:179], v[36:37]\n" \
"v_pk_fma_f32 v[38:39], v[34:35], v[34:35], v[38:39]\n" \
"v_pk_add_f32 v[14:15], v[14:15], v[40:41]\n" \
"v_pk_fma_f32 v[36:37], v[40:41], v[184:185], v[36:37]\n" \
"v_pk_fma_f32 v[38:39], v[40:41], v[40:41], v[38:39]\n" \
"v_pk_add_f32 v[14:15], v[14:15], v[42:43]\n" \
"v_pk_fma_f32 v[36:37], v[42:43], v[186:187], v[36:37]\n" \
"v_pk_fma_f32 v[38:39], v[42:43], v[42:43], v[38:39]\n" \
"s_nop 7\n" \
"v_mfma_f32_16x16x32_bf16 v[208:211], v[230:233], a[180:183], 0\n" \
"v_mfma_f32_16x16x32_bf16 v[212:215], v[230:233], a[188:191], 0\n" \
"v_mfma_f32_16x16x32_bf16 v[208:211], v[234:237], a[184:187], v[208:211]\n" \
"v_mfma_f32_16x16x32_bf16 v[212:215], v[234:237], a[192:195], v[212:215]\n" \
"s_nop 6\n" \
"v_pk_fma_f32 v[48:49], v[208:209], v[148:149], v[48:49]\n" \
"v_pk_fma_f32 v[50:51], v[210:211], v[150:151], v[50:51]\n" \
"v_pk_fma_f32 v[56:57], v[212:213], v[156:157], v[56:57]\n" \
"v_pk_fma_f32 v[58:59], v[214:215], v[158:159], v[58:59]\n" \
"v_pk_add_f32 v[14:15], v[14:15], v[48:49]\n" \
"v_pk_fma_f32 v[36:37], v[48:49], v[180:181], v[36:37]\n" \
"v_pk_fma_f32 v[38:39], v[48:49], v[48:49], v[38:39]\n" \
"v_pk_add_f32 v[14:15], v[14:15], v[50:51]\n" \
"v_pk_fma_f32 v[36:37], v[50:51], v[182:183], v[36:37]\n" \
"v_pk_fma_f32 v[38:39], v[50:51], v[50:51], v[38:39]\n" \
"v_pk_add_f32 v[14:15], v[14:15], v[56:57]\n" \
"v_pk_fma_f32 v[36:37], v[56:57], v[188:189], v[36:37]\n" \
"v_pk_fma_f32 v[38:39], v[56:57], v[56:57], v[38:39]\n" \
"v_pk_add_f32 v[14:15], v[14:15], v[58:59]\n" \
"v_pk_fma_f32 v[36:37], v[58:59], v[190:191], v[36:37]\n" \
"v_pk_fma_f32 v[38:39], v[58:59], v[58:59], v[38:39]\n" \
"v_pk_add_f32 v[4:5], v[4:5], v[14:15]\n" \
"v_pk_add_f32 v[6:7], v[6:7], v[36:37]\n" \
"v_pk_add_f32 v[12:13], v[12:13], v[38:39]\n" \
"v_add_f32_e32 v4, v4, v5\n" \
".if . - adaln_body != 69736\n" \
".error \"size model\"\n" \
".endif\n" \
"Lk16_%=:\n" \
"s_cbranch_execz adaln_kr\n" \
"s_nop 0\n" \
"v_add_f32_e32 v6, v6, v7\n" \
"v_add_f32_e32 v12, v12, v13\n" \
"v_mov_b32_e32 v208, v12\n" \
"v_permlane32_swap_b32_e32 v4, v6\n" \
"s_nop 0\n" \
"v_permlane32_swap_b32_e32 v12, v208\n" \
"v_add_f32_e32 v4, v4, v6\n" \
"v_add_f32_e32 v12, v12, v208\n" \
"s_nop 0\n" \
"s_nop 0\n" \
"v_permlane16_swap_b32_e32 v4, v12\n" \
"v_add_f32_e32 v4, v4, v12\n" \
"s_nop 1\n" \
"v_add_f32_dpp v4, v4, v4 quad_perm:[1,0,3,2] row_mask:0xf bank_mask:0xf\n" \
"s_nop 1\n" \
"v_add_f32_dpp v4, v4, v4 quad_perm:[2,3,0,1] row_mask:0xf bank_mask:0xf\n" \
"s_nop 1\n" \
"v_add_f32_dpp v4, v4, v4 row_ror:8 row_mask:0xf bank_mask:0xf\n" \
"v_mov_b32_e32 v12, v4\n" \
"s_nop 0\n" \
"s_nop 0\n" \
"v_permlane16_swap_b32_e32 v4, v12\n" \
"v_mov_b32_e32 v6, v4\n" \
"v_mul_f32_e32 v12, 0x3a800000, v12\n" \
"s_nop 0\n" \
"v_permlane32_swap_b32_e32 v4, v6\n" \
"v_mul_f32_e32 v218, 0x3a800000, v4\n" \
"v_mul_f32_e32 v208, v218, v218\n" \
"v_sub_f32_e32 v12, v12, v208\n" \
"v_cmp_nle_f32_e32 vcc, v208, v12\n" \
"v_add_f32_e32 v12, 0x3727c5ac, v12\n" \
"v_rsq_f32_e32 v220, v12\n" \
"v_fma_f32 v210, -v218, v223, v6\n" \
"s_mov_b64 exec, 17\n" \
"v_mul_f32_e32 v210, v210, v220\n" \
"v_max_f32_e32 v210, 0xff800000, v210\n" \
"v_ashrrev_i32_e32 v208, 31, v210\n" \
"v_or_b32_e32 v208, 0x80000000, v208\n" \
"v_xor_b32_e32 v210, v210, v208\n" \
"v_and_b32_e32 v210, 0xfffff800, v210\n" \
"v_or_b32_e32 v210, v210, %[ta0]\n" \
"ds_write_b32 v216, v210 offset:14400\n" \
"s_mov_b64 exec, -1\n" \
"s_cbranch_vccnz TLrare_%=\n" \
"s_branch TLrok_%=\n" \
"TLvar_%=:\n" \
"v_sub_f32_e32 v210, 0, v218\n" \
"v_pk_add_f32 v[212:213], v[0:1], v[210:211] op_sel_hi:[1,0]\n" \
"v_pk_mul_f32 v[214:215], v[212:213], v[212:213]\n" \
"v_pk_add_f32 v[212:213], v[2:3], v[210:211] op_sel_hi:[1,0]\n" \
"v_pk_fma_f32 v[214:215], v[212:213], v[212:213], v[214:215]\n" \
"v_pk_add_f32 v[212:213], v[8:9], v[210:211] op_sel_hi:[1,0]\n" \
"v_pk_fma_f32 v[214:215], v[212:213], v[212:213], v[214:215]\n" \
"v_pk_add_f32 v[212:213], v[10:11], v[210:211] op_sel_hi:[1,0]\n" \
"v_pk_fma_f32 v[214:215], v[212:213], v[212:213], v[214:215]\n" \
"v_pk_add_f32 v[212:213], v[16:17], v[210:211] op_sel_hi:[1,0]\n" \
"v_pk_fma_f32 v[214:215], v[212:213], v[212:213], v[214:215]\n" \
"v_pk_add_f32 v[212:213], v[18:19], v[210:211] op_sel_hi:[1,0]\n" \
"v_pk_fma_f32 v[214:215], v[212:213], v[212:213], v[214:215]\n" \
"v_pk_add_f32 v[212:213], v[24:25], v[210:211] op_sel_hi:[1,0]\n" \
"v_pk_fma_f32 v[214:215], v[212:213], v[212:213], v[214:215]\n" \
"v_pk_add_f32 v[212:213], v[26:27], v[210:211] op_sel_hi:[1,0]\n" \
"v_pk_fma_f32 v[214:215], v[212:213], v[212:213], v[214:215]\n" \
"v_pk_add_f32 v[212:213], v[32:33], v[210:211] op_sel_hi:[1,0]\n" \
"v_pk_fma_f32 v[214:215], v[212:213], v[212:213], v[214:215]\n" \
"v_pk_add_f32 v[212:213], v[34:35], v[210:211] op_sel_hi:[1,0]\n" \
"v_pk_fma_f32 v[214:215], v[212:213], v[212:213], v[214:215]\n" \
"v_pk_add_f32 v[212:213], v[40:41], v[210:211] op_sel_hi:[1,0]\n" \
"v_pk_fma_f32 v[214:215], v[212:213], v[212:213], v[214:215]\n" \
"v_pk_add_f32 v[212:213], v[42:43], v[210:211] op_sel_hi:[1,0]\n" \
"v_pk_fma_f32 v[214:215], v[212:213], v[212:213], v[214:215]\n" \
"v_pk_add_f32 v[212:213], v[48:49], v[210:211] op_sel_hi:[1,0]\n" \
"v_pk_fma_f32 v[214:215], v[212:213], v[212:213], v[214:215]\n" \
"v_pk_add_f32 v[212:213], v[50:51], v[210:211] op_sel_hi:[1,0]\n" \
"v_pk_fma_f32 v[214:215], v[212:213], v[212:213], v[214:215]\n" \
"v_pk_add_f32 v[212:213], v[56:57], v[210:211] op_sel_hi:[1,0]\n" \
"v_pk_fma_f32 v[214:215], v[212:213], v[212:213], v[214:215]\n" \
"v_pk_add_f32 v[212:213], v[58:59], v[210:211] op_sel_hi:[1,0]\n" \
"v_pk_fma_f32 v[214:215], v[212:213], v[212:213], v[214:215]\n" \
"v_add_f32_e32 v12, v214, v215\n" \
"s_nop 1\n" \
"v_add_f32_dpp v12, v12, v12 quad_perm:[1,0,3,2] row_mask:0xf bank_mask:0xf\n" \
"s_nop 1\n" \
"v_add_f32_dpp v12, v12, v12 quad_perm:[2,3,0,1] row_mask:0xf bank_mask:0xf\n" \
"s_nop 1\n" \
"v_add_f32_dpp v12, v12, v12 row_ror:8 row_mask:0xf bank_mask:0xf\n" \
"v_mov_b32_e32 v210, v12\n" \
"s_nop 0\n" \
"s_nop 0\n" \
"v_permlane16_swap_b32_e32 v12, v210\n" \
"v_add_f32_e32 v12, v12, v210\n" \
"v_mov_b32_e32 v210, v12\n" \
"s_nop 0\n" \
"s_nop 0\n" \
"v_permlane32_swap_b32_e32 v12, v210\n" \
"v_add_f32_e32 v12, v12, v210\n" \
"v_mul_f32_e32 v12, 0x3a800000, v12\n" \
"v_add_f32_e32 v12, 0x3727c5ac, v12\n" \
"v_rsq_f32_e32 v220, v12\n" \
"v_fma_f32 v210, -v218, v223, v6\n" \
"s_mov_b64 exec, 17\n" \
"v_mul_f32_e32 v210, v210, v220\n" \
"v_max_f32_e32 v210, 0xff800000, v210\n" \
"v_ashrrev_i32_e32 v208, 31, v210\n" \
"v_or_b32_e32 v208, 0x80000000, v208\n" \
"v_xor_b32_e32 v210, v210, v208\n" \
"v_and_b32_e32 v210, 0xfffff800, v210\n" \
"v_or_b32_e32 v210, v210, %[ta0]\n" \
"ds_write_b32 v216, v210 offset:14400\n" \
"s_mov_b64 exec, -1\n" \
"s_branch TLrok_%=\n" \
"TLcw_%=:\n" \
"s_waitcnt lgkmcnt(0)\n" \
"s_mov_b32 s63, 278528\n" \
"s_mov_b32 s51, 0x10000\n" \
"TLcw1_%=:\n" \
"buffer_load_dword %[vxs], %[vdn], %[srdz], s63 offen offset:0 sc1\n" \
"s_waitcnt vmcnt(0)\n" \
"v_and_b32_e32 v44, 0x820, %[vxs]\n" \
"v_cmp_ne_u32_e32 vcc, 0x820, v44\n" \
"s_sub_u32 s51, s51, 1\n" \
"s_cbranch_scc1 TLcw2_%=\n" \
"s_nop 3\n" \
"s_cbranch_vccz TLcw2_%=\n" \
"s_sleep 8\n" \
"s_branch TLcw1_%=\n" \
"TLcw2_%=:\n" \
"s_sleep 4\n" \
"s_mov_b32 s59, 1\n" \
"buffer_load_dwordx4 a[128:131], %[vct], %[srdz], 0 offen offset:0 sc1\n" \
"buffer_load_dwordx4 a[132:135], %[rd0], %[srdz], 0 offen offset:0 sc1\n" \
"buffer_load_dwordx4 a[136:139], %[vct], %[srdz], s56 offen offset:0 sc1\n" \
"buffer_load_dwordx4 a[140:143], %[rd0], %[srdz], s56 offen offset:0 sc1\n" \
"buffer_load_dwordx4 a[144:147], %[vct], %[srdz], s57 offen offset:0 sc1\n" \
"buffer_load_dwordx4 a[148:151], %[rd0], %[srdz], s57 offen offset:0 sc1\n" \
"buffer_load_dwordx4 a[152:155], %[vct], %[srdz], s58 offen offset:0 sc1\n" \
"buffer_load_dwordx4 a[156:159], %[rd0], %[srdz], s58 offen offset:0 sc1\n" \
"s_waitcnt vmcnt(0)\n" \
"s_branch TLcs_%=\n" \
"TLcx_%=:\n" \
"s_cmp_eq_u32 s59, 0\n" \
"s_cbranch_scc1 TLcw_%=\n" \
"s_sub_u32 s52, s52, 1\n" \
"s_cbranch_scc1 TLco_%=\n" \
"buffer_load_dwordx4 a[128:131], %[vct], %[srdz], 0 offen offset:0 sc1\n" \
"buffer_load_dwordx4 a[132:135], %[rd0], %[srdz], 0 offen offset:0 sc1\n" \
"buffer_load_dwordx4 a[136:139], %[vct], %[srdz], s56 offen offset:0 sc1\n" \
"buffer_load_dwordx4 a[140:143], %[rd0], %[srdz], s56 offen offset:0 sc1\n" \
"buffer_load_dwordx4 a[144:147], %[vct], %[srdz], s57 offen offset:0 sc1\n" \
"buffer_load_dwordx4 a[148:151], %[rd0], %[srdz], s57 offen offset:0 sc1\n" \
"buffer_load_dwordx4 a[152:155], %[vct], %[srdz], s58 offen offset:0 sc1\n" \
"buffer_load_dwordx4 a[156:159], %[rd0], %[srdz], s58 offen offset:0 sc1\n" \
"s_waitcnt vmcnt(0) lgkmcnt(0)\n" \
"s_branch TLcs_%=\n" \
"TLk1_%=:\n" \
"s_sub_u32 s51, s51, 1\n" \
"s_cbranch_scc1 TLkw_%=\n" \
"s_sleep 3\n" \
"buffer_load_dwordx4 v[112:115], v216, %[srdz], s49 offen offset:0 sc1\n" \
"buffer_load_dwordx4 v[116:119], v216, %[srdz], s49 offen offset:16 sc1\n" \
"s_waitcnt vmcnt(0)\n" \
"s_branch TLk2_%=\n" \
"TLg1_%=:\n" \
"s_waitcnt lgkmcnt(0)\n" \
"v_min_u32_e32 v64, v113, v115\n" \
"v_min_u32_e32 v65, v117, v119\n" \
"v_max_u32_e32 v113, v113, v115\n" \
"v_max_u32_e32 v117, v117, v119\n" \
"v_max3_u32 v112, v64, v112, v114\n" \
"v_max3_u32 v116, v65, v116, v118\n" \
"v_min_u32_e32 v64, v113, v117\n" \
"v_max_u32_e32 v113, v113, v117\n" \
"v_max3_u32 v112, v64, v112, v116\n" \
"s_nop 0\n" \
"v_min_u32_dpp v64, v113, v113 quad_perm:[1,0,3,2] row_mask:0xf bank_mask:0xf\n" \
"v_max_u32_dpp v65, v112, v112 quad_perm:[1,0,3,2] row_mask:0xf bank_mask:0xf\n" \
"v_max_u32_dpp v113, v113, v113 quad_perm:[1,0,3,2] row_mask:0xf bank_mask:0xf\n" \
"v_max_u32_e32 v112, v64, v65\n" \
"s_nop 0\n" \
"v_min_u32_dpp v64, v113, v113 quad_perm:[2,3,0,1] row_mask:0xf bank_mask:0xf\n" \
"v_max_u32_dpp v65, v112, v112 quad_perm:[2,3,0,1] row_mask:0xf bank_mask:0xf\n" \
"v_max_u32_dpp v113, v113, v113 quad_perm:[2,3,0,1] row_mask:0xf bank_mask:0xf\n" \
"v_max_u32_e32 v112, v64, v65\n" \
"s_nop 0\n" \
"v_min_u32_dpp v64, v113, v113 row_half_mirror row_mask:0xf bank_mask:0xf\n" \
"v_max_u32_dpp v65, v112, v112 row_half_mirror row_mask:0xf bank_mask:0xf\n" \
"v_max_u32_dpp v113, v113, v113 row_half_mirror row_mask:0xf bank_mask:0xf\n" \
"v_max_u32_e32 v112, v64, v65\n" \
"s_nop 0\n" \
"v_min_u32_dpp v64, v113, v113 row_mirror row_mask:0xf bank_mask:0xf\n" \
"v_max_u32_dpp v65, v112, v112 row_mirror row_mask:0xf bank_mask:0xf\n" \
"v_max_u32_dpp v113, v113, v113 row_mirror row_mask:0xf bank_mask:0xf\n" \
"v_max_u32_e32 v112, v64, v65\n" \
"v_mov_b32_e32 v115, v113\n" \
"v_mov_b32_e32 v114, v112\n" \
"s_nop 0\n" \
"v_permlane16_swap_b32_e32 v113, v115\n" \
"v_permlane16_swap_b32_e32 v112, v114\n" \
"v_min_u32_e32 v64, v113, v115\n" \
"v_max_u32_e32 v113, v113, v115\n" \
"v_max3_u32 v112, v64, v112, v114\n" \
"v_mov_b32_e32 v115, v113\n" \
"v_mov_b32_e32 v114, v112\n" \
"s_nop 0\n" \
"v_permlane32_swap_b32_e32 v113, v115\n" \
"v_permlane32_swap_b32_e32 v112, v114\n" \
"v_min_u32_e32 v64, v113, v115\n" \
"v_max_u32_e32 v113, v113, v115\n" \
"v_max3_u32 v112, v64, v112, v114\n" \
"s_mov_b64 exec, 1\n" \
"ds_write_b64 v72, v[112:113] offset:14432\n" \
"s_mov_b64 exec, -1\n" \
"s_branch TLg2_%=\n" \
"TLga_%=:\n" \
"s_sub_u32 s51, s51, 1\n" \
"s_cbranch_scc1 TLgb_%=\n" \
"buffer_load_dwordx4 v[128:131], v73, %[srdz], s63 offen offset:0 sc1\n" \
"buffer_load_dwordx4 v[132:135], v73, %[srdz], s58 offen offset:0 sc1\n" \
"s_branch TLg3_%=\n" \
"TLagain_%=:\n" \
"s_cmp_eq_u32 s41, 4\n" \
"s_cbranch_scc1 TLfin_%=\n" \
"s_lshl_b32 s41, s41, 1\n" \
"v_add_f32_e32 v191, v191, v4\n" \
"v_lshl_or_b32 v227, v227, 1, 1\n" \
"v_readlane_b32 s62, v191, 0\n" \
"v_readlane_b32 s63, v191, 1\n" \
"s_bitcmp1_b32 %[got], 1\n" \
"s_cselect_b32 s44, %[k1], s60\n" \
"s_cselect_b32 s45, %[k2], s61\n" \
"s_cselect_b32 s62, 0, s62\n" \
"s_cselect_b32 s63, 0, s63\n" \
"s_mov_b32 s46, s44\n" \
"s_mov_b32 s47, s45\n" \
"v_mov_b32_e32 v208, s62\n" \
"v_mov_b32_e32 v209, s63\n" \
"s_mov_b32 vcc_lo, 0xaaaaaaaa\n" \
"s_mov_b32 vcc_hi, 0xaaaaaaaa\n" \
"v_cndmask_b32_e32 v191, v208, v209, vcc\n" \
"s_mov_b32 vcc_lo, 0xf0f0f0f0\n" \
"s_mov_b32 vcc_hi, 0xf0f0f0f0\n" \
"v_cndmask_b32_e32 v190, v208, v209, vcc\n" \
"s_mov_b32 vcc_lo, 0xff00ff\n" \
"s_mov_b32 vcc_hi, 0xff00ff\n" \
"v_cndmask_b32_e64 v225, 0, -1, vcc\n" \
"v_sub_u32_e32 v216, 0x7ff, %[ta0]\n" \
"v_cmp_ne_u32_e64 s[56:57], s44, v216\n" \
"v_cmp_ne_u32_e64 s[58:59], s45, v216\n" \
"s_and_b64 s[56:57], s[56:57], s[58:59]\n" \
"s_and_b64 s[56:57], s[56:57], %[lat]\n" \
"s_mov_b64 %[lat], 0\n" \
"v_bfrev_b32_e32 v208, 1\n" \
"v_cndmask_b32_e64 v208, v208, %[vd0], s[56:57]\n" \
"v_xor_b32_e32 v209, 0x80, v208\n" \
"buffer_store_dwordx4 v[96:99], v208, s[64:67], 0 offen offset:0 sc1\n" \
"buffer_store_dwordx4 v[100:103], v208, s[64:67], 0 offen offset:1024 sc1\n" \
"buffer_store_dwordx4 v[104:107], v209, s[64:67], 0 offen offset:0 sc1\n" \
"buffer_store_dwordx4 v[108:111], v209, s[64:67], 0 offen offset:1024 sc1\n" \
"s_movk_i32 s56, 0x1000\n" \
"s_movk_i32 s57, 0x2000\n" \
"s_movk_i32 s58, 0x3000\n" \
"s_movk_i32 s52, 0x1000\n" \
"s_mov_b32 s59, 1\n" \
"s_nop 1\n" \
"v_and_b32_e32 v213, 4, v222\n" \
"v_mov_b32_e32 v208, s44\n" \
"v_cmp_ne_u32_e32 vcc, 0, v213\n" \
"v_mov_b32_e32 v214, s45\n" \
"v_mov_b32_e32 v209, s46\n" \
"v_mov_b32_e32 v215, s47\n" \
"v_cndmask_b32_e32 v208, v208, v214, vcc\n" \
"v_cndmask_b32_e32 v209, v209, v215, vcc\n" \
"v_cmp_lt_u32_e32 vcc, 31, v222\n" \
"v_and_b32_e32 v211, 0xf0, %[kb0]\n" \
"s_lshl_b32 s62, %[wid], 9\n" \
"v_cndmask_b32_e32 v210, v208, v209, vcc\n" \
"v_lshrrev_b32_e32 %[vdn], 6, v210\n" \
"v_lshlrev_b32_e32 %[vdn], 9, %[vdn]\n" \
"v_lshlrev_b32_e32 v213, 11, v210\n" \
"v_lshrrev_b32_e32 v214, 1, v210\n" \
"v_and_b32_e32 v214, 0x80, v214\n" \
"v_lshrrev_b32_e32 v215, 1, v211\n" \
"v_and_b32_e32 v215, 56, v215\n" \
"v_add3_u32 v213, v213, v214, v215\n" \
"v_and_b32_e32 v215, 0x80, v211\n" \
"v_lshlrev_b32_e32 v215, 1, v215\n" \
"v_add3_u32 v80, v213, v215, s62\n" \
"v_xor_b32_e32 v81, 0x80, v80\n" \
"s_mov_b32 s63, 278528\n" \
"buffer_load_dword %[vxs], %[vdn], %[srdz], s63 offen offset:0 sc1\n" \
"global_load_dwordx2 v[88:89], v80, %[px] offset:0\n" \
"global_load_dwordx2 v[90:91], v80, %[px] offset:64\n" \
"global_load_dwordx2 v[92:93], v81, %[px] offset:0\n" \
"global_load_dwordx2 v[94:95], v81, %[px] offset:64\n" \
"v_and_b32_e32 v213, 0x100, v210\n" \
"s_add_u32 s63, s62, s40\n" \
"v_add3_u32 v213, v213, v211, s63\n" \
"v_xor_b32_e32 v214, 0x100, v213\n" \
"ds_read_b128 v[104:107], v213 offset:2048\n" \
"ds_read_b128 v[108:111], v214 offset:2048\n" \
"ds_read_b128 v[64:67], v213 offset:6144\n" \
"ds_read_b128 v[68:71], v213 offset:8192\n" \
"ds_read_b128 v[72:75], v214 offset:6144\n" \
"ds_read_b128 v[76:79], v214 offset:8192\n" \
"ds_read_b128 v[112:115], v213 offset:10240\n" \
"ds_read_b128 v[116:119], v213 offset:12288\n" \
"ds_read_b128 v[120:123], v214 offset:10240\n" \
"ds_read_b128 v[124:127], v214 offset:12288\n" \
"s_movk_i32 s63, 2112\n" \
"v_mad_u32_u24 %[oq0], v219, s63, v213\n" \
"v_mad_u32_u24 %[wo0], v219, s63, v214\n" \
"s_lshl_b32 s62, %[wid], 10\n" \
"s_add_u32 s62, s62, 7340032\n" \
"v_and_b32_e32 v215, 0x3b0, v217\n" \
"v_add_u32_e32 v215, s62, v215\n" \
"s_mov_b32 s63, 38016\n" \
"v_lshrrev_b32_e32 %[vct], 1, v208\n" \
"v_and_b32_e32 v208, 1, v208\n" \
"v_mad_u32_u24 %[vct], %[vct], s63, v215\n" \
"v_lshl_add_u32 %[vct], v208, 6, %[vct]\n" \
"v_lshrrev_b32_e32 %[rd0], 1, v209\n" \
"v_and_b32_e32 v209, 1, v209\n" \
"v_mad_u32_u24 %[rd0], %[rd0], s63, v215\n" \
"v_lshl_add_u32 %[rd0], v209, 6, %[rd0]\n" \
"buffer_load_dwordx4 a[128:131], %[vct], %[srdz], 0 offen offset:0 sc1\n" \
"buffer_load_dwordx4 a[132:135], %[rd0], %[srdz], 0 offen offset:0 sc1\n" \
"buffer_load_dwordx4 a[136:139], %[vct], %[srdz], s56 offen offset:0 sc1\n" \
"buffer_load_dwordx4 a[140:143], %[rd0], %[srdz], s56 offen offset:0 sc1\n" \
"buffer_load_dwordx4 a[144:147], %[vct], %[srdz], s57 offen offset:0 sc1\n" \
"buffer_load_dwordx4 a[148:151], %[rd0], %[srdz], s57 offen offset:0 sc1\n" \
"buffer_load_dwordx4 a[152:155], %[vct], %[srdz], s58 offen offset:0 sc1\n" \
"buffer_load_dwordx4 a[156:159], %[rd0], %[srdz], s58 offen offset:0 sc1\n" \
"s_waitcnt vmcnt(0) lgkmcnt(0)\n" \
"v_lshlrev_b32_e32 v96, 16, v104\n" \
"v_and_b32_e32 v97, s43, v104\n" \
"v_lshlrev_b32_e32 v98, 16, v105\n" \
"v_and_b32_e32 v99, s43, v105\n" \
"v_lshlrev_b32_e32 v100, 16, v106\n" \
"v_and_b32_e32 v101, s43, v106\n" \
"v_lshlrev_b32_e32 v102, 16, v107\n" \
"v_and_b32_e32 v103, s43, v107\n" \
"v_lshlrev_b32_e32 v104, 16, v108\n" \
"v_and_b32_e32 v105, s43, v108\n" \
"v_lshlrev_b32_e32 v106, 16, v109\n" \
"v_and_b32_e32 v107, s43, v109\n" \
"v_lshlrev_b32_e32 v108, 16, v110\n" \
"v_and_b32_e32 v109, s43, v110\n" \
"v_lshlrev_b32_e32 v110, 16, v111\n" \
"v_and_b32_e32 v111, s43, v111\n" \
"v_lshlrev_b32_e32 v80, 16, v88\n" \
"v_and_b32_e32 v81, s43, v88\n" \
"v_lshlrev_b32_e32 v82, 16, v89\n" \
"v_and_b32_e32 v83, s43, v89\n" \
"v_lshlrev_b32_e32 v84, 16, v90\n" \
"v_and_b32_e32 v85, s43, v90\n" \
"v_lshlrev_b32_e32 v86, 16, v91\n" \
"v_and_b32_e32 v87, s43, v91\n" \
"v_lshlrev_b32_e32 v88, 16, v92\n" \
"v_and_b32_e32 v89, s43, v92\n" \
"v_lshlrev_b32_e32 v90, 16, v93\n" \
"v_and_b32_e32 v91, s43, v93\n" \
"v_lshlrev_b32_e32 v92, 16, v94\n" \
"v_and_b32_e32 v93, s43, v94\n" \
"v_lshlrev_b32_e32 v94, 16, v95\n" \
"v_and_b32_e32 v95, s43, v95\n" \
"v_sub_f32_e32 v80, v80, v190\n" \
"v_sub_f32_e32 v81, v81, v190\n" \
"v_sub_f32_e32 v82, v82, v190\n" \
"v_sub_f32_e32 v83, v83, v190\n" \
"v_sub_f32_e32 v84, v84, v190\n" \
"v_sub_f32_e32 v85, v85, v190\n" \
"v_sub_f32_e32 v86, v86, v190\n" \
"v_sub_f32_e32 v87, v87, v190\n" \
"v_sub_f32_e32 v88, v88, v190\n" \
"v_sub_f32_e32 v89, v89, v190\n" \
"v_sub_f32_e32 v90, v90, v190\n" \
"v_sub_f32_e32 v91, v91, v190\n" \
"v_sub_f32_e32 v92, v92, v190\n" \
"v_sub_f32_e32 v93, v93, v190\n" \
"v_sub_f32_e32 v94, v94, v190\n" \
"v_sub_f32_e32 v95, v95, v190\n" \
"s_branch TLcs_%=\n" \
"Lsp1_%=:\n" \
"s_movk_i32 s51, 0x1000\n" \
"s_cmp_gt_u32 s48, 0\n" \
"s_cselect_b32 s61, 0, 1\n" \
"s_nop 0\n" \
"s_mul_i32 s61, s61, 152064\n" \
"Lsq1_%=:\n" \
"buffer_load_dwordx4 a[64:67], %[oq0], %[srdz], s61 offen offset:0 sc1\n" \
"buffer_load_dwordx4 a[68:71], %[oq0], %[srdz], s61 offen offset:1024 sc1\n" \
"buffer_load_dwordx4 a[72:75], %[oq0], %[srdz], s61 offen offset:2048 sc1\n" \
"buffer_load_dwordx4 a[76:79], %[oq0], %[srdz], s61 offen offset:3072 sc1\n" \
"buffer_load_dwordx4 a[80:83], v208, %[srdz], s61 offen offset:0 sc1\n" \
"buffer_load_dwordx4 a[84:87], v208, %[srdz], s61 offen offset:1024 sc1\n" \
"buffer_load_dwordx4 a[88:91], v208, %[srdz], s61 offen offset:2048 sc1\n" \
"buffer_load_dwordx4 a[92:95], v208, %[srdz], s61 offen offset:3072 sc1\n" \
"buffer_load_dwordx4 a[96:99], v209, %[srdz], s61 offen offset:0 sc1\n" \
"buffer_load_dwordx4 a[100:103], v209, %[srdz], s61 offen offset:1024 sc1\n" \
"buffer_load_dwordx4 a[104:107], v209, %[srdz], s61 offen offset:2048 sc1\n" \
"buffer_load_dwordx4 a[108:111], v209, %[srdz], s61 offen offset:3072 sc1\n" \
"buffer_load_dwordx4 a[112:115], v210, %[srdz], s61 offen offset:0 sc1\n" \
"buffer_load_dwordx4 a[116:119], v210, %[srdz], s61 offen offset:1024 sc1\n" \
"buffer_load_dwordx4 a[120:123], v210, %[srdz], s61 offen offset:2048 sc1\n" \
"buffer_load_dwordx4 a[124:127], v210, %[srdz], s61 offen offset:3072 sc1\n" \
"s_waitcnt vmcnt(0)\n" \
"s_nop 0\n" \
"v_mfma_f32_16x16x32_bf16 v[128:131], a[236:239], a[64:67], v[192:195]\n" \
"v_mfma_f32_16x16x32_bf16 v[128:131], a[196:199], a[68:71], v[128:131]\n" \
"v_mfma_f32_16x16x32_bf16 v[128:131], a[200:203], a[72:75], v[128:131]\n" \
"v_mfma_f32_16x16x32_bf16 v[128:131], a[204:207], a[76:79], v[128:131]\n" \
"v_mfma_f32_16x16x32_bf16 v[132:135], a[236:239], a[80:83], v[196:199]\n" \
"v_mfma_f32_16x16x32_bf16 v[132:135], a[196:199], a[84:87], v[132:135]\n" \
"v_mfma_f32_16x16x32_bf16 v[132:135], a[200:203], a[88:91], v[132:135]\n" \
"v_mfma_f32_16x16x32_bf16 v[132:135], a[204:207], a[92:95], v[132:135]\n" \
"v_mfma_f32_16x16x32_bf16 v[136:139], a[236:239], a[96:99], v[200:203]\n" \
"v_mfma_f32_16x16x32_bf16 v[136:139], a[196:199], a[100:103], v[136:139]\n" \
"v_mfma_f32_16x16x32_bf16 v[136:139], a[200:203], a[104:107], v[136:139]\n" \
"v_mfma_f32_16x16x32_bf16 v[136:139], a[204:207], a[108:111], v[136:139]\n" \
"v_mfma_f32_16x16x32_bf16 v[140:143], a[236:239], a[112:115], v[204:207]\n" \
"v_mfma_f32_16x16x32_bf16 v[140:143], a[196:199], a[116:119], v[140:143]\n" \
"v_mfma_f32_16x16x32_bf16 v[140:143], a[200:203], a[120:123], v[140:143]\n" \
"v_mfma_f32_16x16x32_bf16 v[140:143], a[204:207], a[124:127], v[140:143]\n" \
"s_nop 7\n" \
"s_nop 0\n" \
"v_cmp_u_f32_e64 s[44:45], v128, v132\n" \
"v_cmp_u_f32_e64 s[56:57], v136, v140\n" \
"s_or_b64 s[56:57], s[56:57], s[44:45]\n" \
"s_cbranch_scc0 Lbk1_%=\n" \
"s_sub_u32 s51, s51, 1\n" \
"s_cbranch_scc0 Lsq1_%=\n" \
"s_branch Lbk1_%=\n" \
"Lsp2_%=:\n" \
"s_movk_i32 s51, 0x1000\n" \
"s_cmp_gt_u32 s48, 1\n" \
"s_cselect_b32 s61, 1, 2\n" \
"s_mul_i32 s61, s61, 152064\n" \
"Lsq2_%=:\n" \
"buffer_load_dwordx4 a[0:3], %[oq0], %[srdz], s61 offen offset:0 sc1\n" \
"buffer_load_dwordx4 a[4:7], %[oq0], %[srdz], s61 offen offset:1024 sc1\n" \
"buffer_load_dwordx4 a[8:11], %[oq0], %[srdz], s61 offen offset:2048 sc1\n" \
"buffer_load_dwordx4 a[12:15], %[oq0], %[srdz], s61 offen offset:3072 sc1\n" \
"buffer_load_dwordx4 a[16:19], v208, %[srdz], s61 offen offset:0 sc1\n" \
"buffer_load_dwordx4 a[20:23], v208, %[srdz], s61 offen offset:1024 sc1\n" \
"buffer_load_dwordx4 a[24:27], v208, %[srdz], s61 offen offset:2048 sc1\n" \
"buffer_load_dwordx4 a[28:31], v208, %[srdz], s61 offen offset:3072 sc1\n" \
"buffer_load_dwordx4 a[32:35], v209, %[srdz], s61 offen offset:0 sc1\n" \
"buffer_load_dwordx4 a[36:39], v209, %[srdz], s61 offen offset:1024 sc1\n" \
"buffer_load_dwordx4 a[40:43], v209, %[srdz], s61 offen offset:2048 sc1\n" \
"buffer_load_dwordx4 a[44:47], v209, %[srdz], s61 offen offset:3072 sc1\n" \
"buffer_load_dwordx4 a[48:51], v210, %[srdz], s61 offen offset:0 sc1\n" \
"buffer_load_dwordx4 a[52:55], v210, %[srdz], s61 offen offset:1024 sc1\n" \
"buffer_load_dwordx4 a[56:59], v210, %[srdz], s61 offen offset:2048 sc1\n" \
"buffer_load_dwordx4 a[60:63], v210, %[srdz], s61 offen offset:3072 sc1\n" \
"s_waitcnt vmcnt(0)\n" \
"s_nop 0\n" \
"v_mfma_f32_16x16x32_bf16 v[192:195], a[196:199], a[0:3], v[128:131]\n" \
"v_mfma_f32_16x16x32_bf16 v[192:195], a[200:203], a[4:7], v[192:195]\n" \
"v_mfma_f32_16x16x32_bf16 v[192:195], a[204:207], a[8:11], v[192:195]\n" \
"v_mfma_f32_16x16x32_bf16 v[192:195], a[232:235], a[12:15], v[192:195]\n" \
"v_mfma_f32_16x16x32_bf16 v[196:199], a[196:199], a[16:19], v[132:135]\n" \
"v_mfma_f32_16x16x32_bf16 v[196:199], a[200:203], a[20:23], v[196:199]\n" \
"v_mfma_f32_16x16x32_bf16 v[196:199], a[204:207], a[24:27], v[196:199]\n" \
"v_mfma_f32_16x16x32_bf16 v[196:199], a[232:235], a[28:31], v[196:199]\n" \
"v_mfma_f32_16x16x32_bf16 v[200:203], a[196:199], a[32:35], v[136:139]\n" \
"v_mfma_f32_16x16x32_bf16 v[200:203], a[200:203], a[36:39], v[200:203]\n" \
"v_mfma_f32_16x16x32_bf16 v[200:203], a[204:207], a[40:43], v[200:203]\n" \
"v_mfma_f32_16x16x32_bf16 v[200:203], a[232:235], a[44:47], v[200:203]\n" \
"v_mfma_f32_16x16x32_bf16 v[204:207], a[196:199], a[48:51], v[140:143]\n" \
"v_mfma_f32_16x16x32_bf16 v[204:207], a[200:203], a[52:55], v[204:207]\n" \
"v_mfma_f32_16x16x32_bf16 v[204:207], a[204:207], a[56:59], v[204:207]\n" \
"v_mfma_f32_16x16x32_bf16 v[204:207], a[232:235], a[60:63], v[204:207]\n" \
"s_nop 7\n" \
"s_nop 0\n" \
"v_cmp_u_f32_e64 s[44:45], v192, v196\n" \
"v_cmp_u_f32_e64 s[56:57], v200, v204\n" \
"s_or_b64 s[56:57], s[56:57], s[44:45]\n" \
"s_cbranch_scc0 Lbk2_%=\n" \
"s_sub_u32 s51, s51, 1\n" \
"s_cbranch_scc0 Lsq2_%=\n" \
"s_branch Lbk2_%=\n" \
"Lsp3_%=:\n" \
"s_movk_i32 s51, 0x1000\n" \
"s_cmp_gt_u32 s48, 2\n" \
"s_cselect_b32 s61, 2, 3\n" \
"s_mul_i32 s61, s61, 152064\n" \
"Lsq3_%=:\n" \
"buffer_load_dwordx4 a[128:131], %[oq0], %[srdz], s61 offen offset:0 sc1\n" \
"buffer_load_dwordx4 a[132:135], %[oq0], %[srdz], s61 offen offset:1024 sc1\n" \
"buffer_load_dwordx4 a[136:139], %[oq0], %[srdz], s61 offen offset:2048 sc1\n" \
"buffer_load_dwordx4 a[140:143], %[oq0], %[srdz], s61 offen offset:3072 sc1\n" \
"buffer_load_dwordx4 a[144:147], v208, %[srdz], s61 offen offset:0 sc1\n" \
"buffer_load_dwordx4 a[148:151], v208, %[srdz], s61 offen offset:1024 sc1\n" \
"buffer_load_dwordx4 a[152:155], v208, %[srdz], s61 offen offset:2048 sc1\n" \
"buffer_load_dwordx4 a[156:159], v208, %[srdz], s61 offen offset:3072 sc1\n" \
"buffer_load_dwordx4 a[160:163], v209, %[srdz], s61 offen offset:0 sc1\n" \
"buffer_load_dwordx4 a[164:167], v209, %[srdz], s61 offen offset:1024 sc1\n" \
"buffer_load_dwordx4 a[168:171], v209, %[srdz], s61 offen offset:2048 sc1\n" \
"buffer_load_dwordx4 a[172:175], v209, %[srdz], s61 offen offset:3072 sc1\n" \
"buffer_load_dwordx4 a[176:179], v210, %[srdz], s61 offen offset:0 sc1\n" \
"buffer_load_dwordx4 a[180:183], v210, %[srdz], s61 offen offset:1024 sc1\n" \
"buffer_load_dwordx4 a[184:187], v210, %[srdz], s61 offen offset:2048 sc1\n" \
"buffer_load_dwordx4 a[188:191], v210, %[srdz], s61 offen offset:3072 sc1\n" \
"s_waitcnt vmcnt(0)\n" \
"s_nop 0\n" \
"v_mfma_f32_16x16x32_bf16 v[128:131], a[196:199], a[128:131], v[192:195]\n" \
"v_mfma_f32_16x16x32_bf16 v[128:131], a[200:203], a[132:135], v[128:131]\n" \
"v_mfma_f32_16x16x32_bf16 v[128:131], a[204:207], a[136:139], v[128:131]\n" \
"v_mfma_f32_16x16x32_bf16 v[128:131], a[224:227], a[140:143], v[128:131]\n" \
"v_mfma_f32_16x16x32_bf16 v[132:135], a[196:199], a[144:147], v[196:199]\n" \
"v_mfma_f32_16x16x32_bf16 v[132:135], a[200:203], a[148:151], v[132:135]\n" \
"v_mfma_f32_16x16x32_bf16 v[132:135], a[204:207], a[152:155], v[132:135]\n" \
"v_mfma_f32_16x16x32_bf16 v[132:135], a[224:227], a[156:159], v[132:135]\n" \
"v_mfma_f32_16x16x32_bf16 v[136:139], a[196:199], a[160:163], v[200:203]\n" \
"v_mfma_f32_16x16x32_bf16 v[136:139], a[200:203], a[164:167], v[136:139]\n" \
"v_mfma_f32_16x16x32_bf16 v[136:139], a[204:207], a[168:171], v[136:139]\n" \
"v_mfma_f32_16x16x32_bf16 v[136:139], a[224:227], a[172:175], v[136:139]\n" \
"v_mfma_f32_16x16x32_bf16 v[140:143], a[196:199], a[176:179], v[204:207]\n" \
"v_mfma_f32_16x16x32_bf16 v[140:143], a[200:203], a[180:183], v[140:143]\n" \
"v_mfma_f32_16x16x32_bf16 v[140:143], a[204:207], a[184:187], v[140:143]\n" \
"v_mfma_f32_16x16x32_bf16 v[140:143], a[224:227], a[188:191], v[140:143]\n" \
"s_nop 7\n" \
"s_nop 0\n" \
"v_cmp_u_f32_e64 s[44:45], v128, v132\n" \
"v_cmp_u_f32_e64 s[56:57], v136, v140\n" \
"s_or_b64 s[56:57], s[56:57], s[44:45]\n" \
"s_cbranch_scc0 Lbk3_%=\n" \
"s_sub_u32 s51, s51, 1\n" \
"s_cbranch_scc0 Lsq3_%=\n" \
"s_branch Lbk3_%=\n" \
"Lsp4_%=:\n" \
"s_movk_i32 s51, 0x1000\n" \
"s_cmp_gt_u32 s48, 3\n" \
"s_cselect_b32 s61, 3, 4\n" \
"s_mul_i32 s61, s61, 152064\n" \
"Lsq4_%=:\n" \
"buffer_load_dwordx4 a[64:67], %[oq0], %[srdz], s61 offen offset:0 sc1\n" \
"buffer_load_dwordx4 a[68:71], %[oq0], %[srdz], s61 offen offset:1024 sc1\n" \
"buffer_load_dwordx4 a[72:75], %[oq0], %[srdz], s61 offen offset:2048 sc1\n" \
"buffer_load_dwordx4 a[76:79], %[oq0], %[srdz], s61 offen offset:3072 sc1\n" \
"buffer_load_dwordx4 a[80:83], v208, %[srdz], s61 offen offset:0 sc1\n" \
"buffer_load_dwordx4 a[84:87], v208, %[srdz], s61 offen offset:1024 sc1\n" \
"buffer_load_dwordx4 a[88:91], v208, %[srdz], s61 offen offset:2048 sc1\n" \
"buffer_load_dwordx4 a[92:95], v208, %[srdz], s61 offen offset:3072 sc1\n" \
"buffer_load_dwordx4 a[96:99], v209, %[srdz], s61 offen offset:0 sc1\n" \
"buffer_load_dwordx4 a[100:103], v209, %[srdz], s61 offen offset:1024 sc1\n" \
"buffer_load_dwordx4 a[104:107], v209, %[srdz], s61 offen offset:2048 sc1\n" \
"buffer_load_dwordx4 a[108:111], v209, %[srdz], s61 offen offset:3072 sc1\n" \
"buffer_load_dwordx4 a[112:115], v210, %[srdz], s61 offen offset:0 sc1\n" \
"buffer_load_dwordx4 a[116:119], v210, %[srdz], s61 offen offset:1024 sc1\n" \
"buffer_load_dwordx4 a[120:123], v210, %[srdz], s61 offen offset:2048 sc1\n" \
"buffer_load_dwordx4 a[124:127], v210, %[srdz], s61 offen offset:3072 sc1\n" \
"s_waitcnt vmcnt(0)\n" \
"s_nop 0\n" \
"v_mfma_f32_16x16x32_bf16 v[192:195], a[196:199], a[64:67], v[128:131]\n" \
"v_mfma_f32_16x16x32_bf16 v[192:195], a[200:203], a[68:71], v[192:195]\n" \
"v_mfma_f32_16x16x32_bf16 v[192:195], a[204:207], a[72:75], v[192:195]\n" \
"v_mfma_f32_16x16x32_bf16 v[192:195], a[220:223], a[76:79], v[192:195]\n" \
"v_mfma_f32_16x16x32_bf16 v[196:199], a[196:199], a[80:83], v[132:135]\n" \
"v_mfma_f32_16x16x32_bf16 v[196:199], a[200:203], a[84:87], v[196:199]\n" \
"v_mfma_f32_16x16x32_bf16 v[196:199], a[204:207], a[88:91], v[196:199]\n" \
"v_mfma_f32_16x16x32_bf16 v[196:199], a[220:223], a[92:95], v[196:199]\n" \
"v_mfma_f32_16x16x32_bf16 v[200:203], a[196:199], a[96:99], v[136:139]\n" \
"v_mfma_f32_16x16x32_bf16 v[200:203], a[200:203], a[100:103], v[200:203]\n" \
"v_mfma_f32_16x16x32_bf16 v[200:203], a[204:207], a[104:107], v[200:203]\n" \
"v_mfma_f32_16x16x32_bf16 v[200:203], a[220:223], a[108:111], v[200:203]\n" \
"v_mfma_f32_16x16x32_bf16 v[204:207], a[196:199], a[112:115], v[140:143]\n" \
"v_mfma_f32_16x16x32_bf16 v[204:207], a[200:203], a[116:119], v[204:207]\n" \
"v_mfma_f32_16x16x32_bf16 v[204:207], a[204:207], a[120:123], v[204:207]\n" \
"v_mfma_f32_16x16x32_bf16 v[204:207], a[220:223], a[124:127], v[204:207]\n" \
"s_nop 7\n" \
"s_nop 0\n" \
"v_cmp_u_f32_e64 s[44:45], v192, v196\n" \
"v_cmp_u_f32_e64 s[56:57], v200, v204\n" \
"s_or_b64 s[56:57], s[56:57], s[44:45]\n" \
"s_cbranch_scc0 Lbk4_%=\n" \
"s_sub_u32 s51, s51, 1\n" \
"s_cbranch_scc0 Lsq4_%=\n" \
"s_branch Lbk4_%=\n" \
"Lsp5_%=:\n" \
"s_movk_i32 s51, 0x1000\n" \
"s_cmp_gt_u32 s48, 4\n" \
"s_cselect_b32 s61, 4, 5\n" \
"s_mul_i32 s61, s61, 152064\n" \
"Lsq5_%=:\n" \
"buffer_load_dwordx4 v[0:3], %[oq0], %[srdz], s61 offen offset:0 sc1\n" \
"buffer_load_dwordx4 v[8:11], %[oq0], %[srdz], s61 offen offset:1024 sc1\n" \
"buffer_load_dwordx4 v[16:19], %[oq0], %[srdz], s61 offen offset:2048 sc1\n" \
"buffer_load_dwordx4 v[24:27], %[oq0], %[srdz], s61 offen offset:3072 sc1\n" \
"buffer_load_dwordx4 v[32:35], v208, %[srdz], s61 offen offset:0 sc1\n" \
"buffer_load_dwordx4 v[40:43], v208, %[srdz], s61 offen offset:1024 sc1\n" \
"buffer_load_dwordx4 v[48:51], v208, %[srdz], s61 offen offset:2048 sc1\n" \
"buffer_load_dwordx4 v[56:59], v208, %[srdz], s61 offen offset:3072 sc1\n" \
"buffer_load_dwordx4 v[64:67], v209, %[srdz], s61 offen offset:0 sc1\n" \
"buffer_load_dwordx4 v[72:75], v209, %[srdz], s61 offen offset:1024 sc1\n" \
"buffer_load_dwordx4 v[80:83], v209, %[srdz], s61 offen offset:2048 sc1\n" \
"buffer_load_dwordx4 v[88:91], v209, %[srdz], s61 offen offset:3072 sc1\n" \
"buffer_load_dwordx4 v[96:99], v210, %[srdz], s61 offen offset:0 sc1\n" \
"buffer_load_dwordx4 v[104:107], v210, %[srdz], s61 offen offset:1024 sc1\n" \
"buffer_load_dwordx4 v[112:115], v210, %[srdz], s61 offen offset:2048 sc1\n" \
"buffer_load_dwordx4 v[120:123], v210, %[srdz], s61 offen offset:3072 sc1\n" \
"s_waitcnt vmcnt(0)\n" \
"s_nop 0\n" \
"v_mfma_f32_16x16x32_bf16 v[128:131], a[232:235], v[0:3], v[192:195]\n" \
"v_mfma_f32_16x16x32_bf16 v[128:131], a[236:239], v[8:11], v[128:131]\n" \
"v_mfma_f32_16x16x32_bf16 v[128:131], a[208:211], v[16:19], v[128:131]\n" \
"v_mfma_f32_16x16x32_bf16 v[128:131], a[228:231], v[24:27], v[128:131]\n" \
"v_mfma_f32_16x16x32_bf16 v[132:135], a[232:235], v[32:35], v[196:199]\n" \
"v_mfma_f32_16x16x32_bf16 v[132:135], a[236:239], v[40:43], v[132:135]\n" \
"v_mfma_f32_16x16x32_bf16 v[132:135], a[208:211], v[48:51], v[132:135]\n" \
"v_mfma_f32_16x16x32_bf16 v[132:135], a[228:231], v[56:59], v[132:135]\n" \
"v_mfma_f32_16x16x32_bf16 v[136:139], a[232:235], v[64:67], v[200:203]\n" \
"v_mfma_f32_16x16x32_bf16 v[136:139], a[236:239], v[72:75], v[136:139]\n" \
"v_mfma_f32_16x16x32_bf16 v[136:139], a[208:211], v[80:83], v[136:139]\n" \
"v_mfma_f32_16x16x32_bf16 v[136:139], a[228:231], v[88:91], v[136:139]\n" \
"v_mfma_f32_16x16x32_bf16 v[140:143], a[232:235], v[96:99], v[204:207]\n" \
"v_mfma_f32_16x16x32_bf16 v[140:143], a[236:239], v[104:107], v[140:143]\n" \
"v_mfma_f32_16x16x32_bf16 v[140:143], a[208:211], v[112:115], v[140:143]\n" \
"v_mfma_f32_16x16x32_bf16 v[140:143], a[228:231], v[120:123], v[140:143]\n" \
"s_nop 7\n" \
"s_nop 0\n" \
"v_cmp_u_f32_e64 s[44:45], v128, v132\n" \
"v_cmp_u_f32_e64 s[56:57], v136, v140\n" \
"s_or_b64 s[56:57], s[56:57], s[44:45]\n" \
"s_cbranch_scc0 Lbk5_%=\n" \
"s_sub_u32 s51, s51, 1\n" \
"s_cbranch_scc0 Lsq5_%=\n" \
"s_branch Lbk5_%=\n" \
"Lsp6_%=:\n" \
"s_movk_i32 s51, 0x1000\n" \
"s_cmp_gt_u32 s48, 5\n" \
"s_cselect_b32 s61, 5, 6\n" \
"s_mul_i32 s61, s61, 152064\n" \
"Lsq6_%=:\n" \
"buffer_load_dwordx4 a[0:3], %[oq0], %[srdz], s61 offen offset:0 sc1\n" \
"buffer_load_dwordx4 a[4:7], %[oq0], %[srdz], s61 offen offset:1024 sc1\n" \
"buffer_load_dwordx4 a[8:11], %[oq0], %[srdz], s61 offen offset:2048 sc1\n" \
"buffer_load_dwordx4 a[12:15], %[oq0], %[srdz], s61 offen offset:3072 sc1\n" \
"buffer_load_dwordx4 a[16:19], v208, %[srdz], s61 offen offset:0 sc1\n" \
"buffer_load_dwordx4 a[20:23], v208, %[srdz], s61 offen offset:1024 sc1\n" \
"buffer_load_dwordx4 a[24:27], v208, %[srdz], s61 offen offset:2048 sc1\n" \
"buffer_load_dwordx4 a[28:31], v208, %[srdz], s61 offen offset:3072 sc1\n" \
"buffer_load_dwordx4 a[32:35], v209, %[srdz], s61 offen offset:0 sc1\n" \
"buffer_load_dwordx4 a[36:39], v209, %[srdz], s61 offen offset:1024 sc1\n" \
"buffer_load_dwordx4 a[40:43], v209, %[srdz], s61 offen offset:2048 sc1\n" \
"buffer_load_dwordx4 a[44:47], v209, %[srdz], s61 offen offset:3072 sc1\n" \
"buffer_load_dwordx4 a[48:51], v210, %[srdz], s61 offen offset:0 sc1\n" \
"buffer_load_dwordx4 a[52:55], v210, %[srdz], s61 offen offset:1024 sc1\n" \
"buffer_load_dwordx4 a[56:59], v210, %[srdz], s61 offen offset:2048 sc1\n" \
"buffer_load_dwordx4 a[60:63], v210, %[srdz], s61 offen offset:3072 sc1\n" \
"s_waitcnt vmcnt(0)\n" \
"s_nop 0\n" \
"v_mfma_f32_16x16x32_bf16 v[192:195], a[196:199], a[0:3], v[128:131]\n" \
"v_mfma_f32_16x16x32_bf16 v[192:195], a[200:203], a[4:7], v[192:195]\n" \
"v_mfma_f32_16x16x32_bf16 v[192:195], a[204:207], a[8:11], v[192:195]\n" \
"v_mfma_f32_16x16x32_bf16 v[192:195], a[220:223], a[12:15], v[192:195]\n" \
"v_mfma_f32_16x16x32_bf16 v[196:199], a[196:199], a[16:19], v[132:135]\n" \
"v_mfma_f32_16x16x32_bf16 v[196:199], a[200:203], a[20:23], v[196:199]\n" \
"v_mfma_f32_16x16x32_bf16 v[196:199], a[204:207], a[24:27], v[196:199]\n" \
"v_mfma_f32_16x16x32_bf16 v[196:199], a[220:223], a[28:31], v[196:199]\n" \
"v_mfma_f32_16x16x32_bf16 v[200:203], a[196:199], a[32:35], v[136:139]\n" \
"v_mfma_f32_16x16x32_bf16 v[200:203], a[200:203], a[36:39], v[200:203]\n" \
"v_mfma_f32_16x16x32_bf16 v[200:203], a[204:207], a[40:43], v[200:203]\n" \
"v_mfma_f32_16x16x32_bf16 v[200:203], a[220:223], a[44:47], v[200:203]\n" \
"v_mfma_f32_16x16x32_bf16 v[204:207], a[196:199], a[48:51], v[140:143]\n" \
"v_mfma_f32_16x16x32_bf16 v[204:207], a[200:203], a[52:55], v[204:207]\n" \
"v_mfma_f32_16x16x32_bf16 v[204:207], a[204:207], a[56:59], v[204:207]\n" \
"v_mfma_f32_16x16x32_bf16 v[204:207], a[220:223], a[60:63], v[204:207]\n" \
"s_nop 7\n" \
"s_nop 0\n" \
"v_cmp_u_f32_e64 s[44:45], v192, v196\n" \
"v_cmp_u_f32_e64 s[56:57], v200, v204\n" \
"s_or_b64 s[56:57], s[56:57], s[44:45]\n" \
"s_cbranch_scc0 Lbk6_%=\n" \
"s_sub_u32 s51, s51, 1\n" \
"s_cbranch_scc0 Lsq6_%=\n" \
"s_branch Lbk6_%=\n" \
"Lsp7_%=:\n" \
"s_movk_i32 s51, 0x1000\n" \
"s_cmp_gt_u32 s48, 6\n" \
"s_cselect_b32 s61, 6, 7\n" \
"s_mul_i32 s61, s61, 152064\n" \
"Lsq7_%=:\n" \
"buffer_load_dwordx4 a[128:131], %[oq0], %[srdz], s61 offen offset:0 sc1\n" \
"buffer_load_dwordx4 a[132:135], %[oq0], %[srdz], s61 offen offset:1024 sc1\n" \
"buffer_load_dwordx4 a[136:139], %[oq0], %[srdz], s61 offen offset:2048 sc1\n" \
"buffer_load_dwordx4 a[140:143], %[oq0], %[srdz], s61 offen offset:3072 sc1\n" \
"buffer_load_dwordx4 a[144:147], v208, %[srdz], s61 offen offset:0 sc1\n" \
"buffer_load_dwordx4 a[148:151], v208, %[srdz], s61 offen offset:1024 sc1\n" \
"buffer_load_dwordx4 a[152:155], v208, %[srdz], s61 offen offset:2048 sc1\n" \
"buffer_load_dwordx4 a[156:159], v208, %[srdz], s61 offen offset:3072 sc1\n" \
"buffer_load_dwordx4 a[160:163], v209, %[srdz], s61 offen offset:0 sc1\n" \
"buffer_load_dwordx4 a[164:167], v209, %[srdz], s61 offen offset:1024 sc1\n" \
"buffer_load_dwordx4 a[168:171], v209, %[srdz], s61 offen offset:2048 sc1\n" \
"buffer_load_dwordx4 a[172:175], v209, %[srdz], s61 offen offset:3072 sc1\n" \
"buffer_load_dwordx4 a[176:179], v210, %[srdz], s61 offen offset:0 sc1\n" \
"buffer_load_dwordx4 a[180:183], v210, %[srdz], s61 offen offset:1024 sc1\n" \
"buffer_load_dwordx4 a[184:187], v210, %[srdz], s61 offen offset:2048 sc1\n" \
"buffer_load_dwordx4 a[188:191], v210, %[srdz], s61 offen offset:3072 sc1\n" \
"s_waitcnt vmcnt(0)\n" \
"s_nop 0\n" \
"v_mfma_f32_16x16x32_bf16 v[128:131], a[208:211], a[128:131], v[192:195]\n" \
"v_mfma_f32_16x16x32_bf16 v[128:131], a[224:227], a[132:135], v[128:131]\n" \
"v_mfma_f32_16x16x32_bf16 v[128:131], a[228:231], a[136:139], v[128:131]\n" \
"v_mfma_f32_16x16x32_bf16 v[128:131], a[236:239], a[140:143], v[128:131]\n" \
"v_mfma_f32_16x16x32_bf16 v[132:135], a[208:211], a[144:147], v[196:199]\n" \
"v_mfma_f32_16x16x32_bf16 v[132:135], a[224:227], a[148:151], v[132:135]\n" \
"v_mfma_f32_16x16x32_bf16 v[132:135], a[228:231], a[152:155], v[132:135]\n" \
"v_mfma_f32_16x16x32_bf16 v[132:135], a[236:239], a[156:159], v[132:135]\n" \
"v_mfma_f32_16x16x32_bf16 v[136:139], a[208:211], a[160:163], v[200:203]\n" \
"v_mfma_f32_16x16x32_bf16 v[136:139], a[224:227], a[164:167], v[136:139]\n" \
"v_mfma_f32_16x16x32_bf16 v[136:139], a[228:231], a[168:171], v[136:139]\n" \
"v_mfma_f32_16x16x32_bf16 v[136:139], a[236:239], a[172:175], v[136:139]\n" \
"v_mfma_f32_16x16x32_bf16 v[140:143], a[208:211], a[176:179], v[204:207]\n" \
"v_mfma_f32_16x16x32_bf16 v[140:143], a[224:227], a[180:183], v[140:143]\n" \
"v_mfma_f32_16x16x32_bf16 v[140:143], a[228:231], a[184:187], v[140:143]\n" \
"v_mfma_f32_16x16x32_bf16 v[140:143], a[236:239], a[188:191], v[140:143]\n" \
"s_nop 7\n" \
"s_nop 0\n" \
"v_cmp_u_f32_e64 s[44:45], v128, v132\n" \
"v_cmp_u_f32_e64 s[56:57], v136, v140\n" \
"s_or_b64 s[56:57], s[56:57], s[44:45]\n" \
"s_cbranch_scc0 Lbk7_%=\n" \
"s_sub_u32 s51, s51, 1\n" \
"s_cbranch_scc0 Lsq7_%=\n" \
"s_branch Lbk7_%=\n" \
"Lspk_%=:\n" \
"s_nop 0\n" \
"s_mov_b32 s51, 0x10000\n" \
"Lsqk_%=:\n" \
"buffer_load_dword v229, v226, %[srdz], s50 offen sc1\n" \
"s_waitcnt vmcnt(0)\n" \
"s_nop 0\n" \
"v_cmp_eq_u32_e64 s[56:57], 0, v229\n" \
"s_cmp_lg_u64 s[56:57], 0\n" \
"s_cbranch_scc0 Lbkk_%=\n" \
"s_sub_u32 s51, s51, 1\n" \
"s_cbranch_scc0 Lsqk_%=\n" \
"s_branch Lbkk_%=\n" \
"Lspp_%=:\n" \
"s_nop 0\n" \
"s_mov_b32 s51, 0x10000\n" \
"Lsqp_%=:\n" \
"buffer_load_dword v232, v226, %[srdz], s54 offen sc1\n" \
"s_waitcnt vmcnt(0)\n" \
"v_cmp_eq_u32_e32 vcc, 0, v232\n" \
"s_cbranch_vccz Lbkp_%=\n" \
"s_sub_u32 s51, s51, 1\n" \
"s_cbranch_scc0 Lsqp_%=\n" \
"s_branch Lbkp_%=\n" \
"Lpars_%=:\n" \
"s_mov_b32 s51, 0x10000\n" \
"s_lshl_b32 s61, %[wid], 10\n" \
"v_lshrrev_b32_e32 v235, 3, v224\n" \
"v_add_u32_e64 v235, s61, v235\n" \
"Lpar1_%=:\n" \
"s_mov_b32 s61, 262144\n" \
"s_nop 0\n" \
"s_nop 0\n" \
"buffer_load_dwordx4 v[192:195], v235, %[srdz], s61 offen offset:0 sc1\n" \
"s_mov_b32 s61, 266240\n" \
"s_nop 0\n" \
"s_nop 0\n" \
"buffer_load_dwordx4 v[196:199], v235, %[srdz], s61 offen offset:0 sc1\n" \
"s_mov_b32 s61, 270336\n" \
"s_nop 0\n" \
"s_nop 0\n" \
"buffer_load_dwordx4 v[200:203], v235, %[srdz], s61 offen offset:0 sc1\n" \
"s_mov_b32 s61, 274432\n" \
"s_nop 0\n" \
"s_nop 0\n" \
"buffer_load_dwordx4 v[204:207], v235, %[srdz], s61 offen offset:0 sc1\n" \
"s_waitcnt vmcnt(0)\n" \
"s_nop 0\n" \
"v_and_or_b32 v236, v192, v193, 0\n" \
"v_bitop3_b32 v236, v236, v194, v195 bitop3:0x80\n" \
"v_bitop3_b32 v236, v236, v196, v197 bitop3:0x80\n" \
"v_bitop3_b32 v236, v236, v198, v199 bitop3:0x80\n" \
"v_bitop3_b32 v236, v236, v200, v201 bitop3:0x80\n" \
"v_bitop3_b32 v236, v236, v202, v203 bitop3:0x80\n" \
"v_bitop3_b32 v236, v236, v204, v205 bitop3:0x80\n" \
"v_bitop3_b32 v236, v236, v206, v207 bitop3:0x80\n" \
"v_and_b32_e64 v236, 1, v236\n" \
"v_cmp_eq_u32_e64 s[56:57], 0, v236\n" \
"s_cmp_lg_u64 s[56:57], 0\n" \
"s_cbranch_scc0 Lparb_%=\n" \
"s_sub_u32 s51, s51, 1\n" \
"s_cbranch_scc0 Lpar1_%=\n" \
"s_branch Lparb_%=\n" \
"Ls0_%=:\n" \
"s_movk_i32 s51, 0x1000\n" \
"s_waitcnt vmcnt(0)\n" \
"Ls01_%=:\n" \
"s_nop 0\n" \
"s_mov_b32 s40, 606208\n" \
"buffer_load_dwordx4 a[176:179], v237, %[srdz], s40 offen sc1\n" \
"s_mov_b32 s40, 610304\n" \
"buffer_load_dwordx4 a[180:183], v237, %[srdz], s40 offen sc1\n" \
"s_mov_b32 s40, 614400\n" \
"buffer_load_dwordx4 a[184:187], v237, %[srdz], s40 offen sc1\n" \
"s_mov_b32 s40, 618496\n" \
"buffer_load_dwordx4 a[188:191], v237, %[srdz], s40 offen sc1\n" \
"s_waitcnt vmcnt(0)\n" \
"s_nop 0\n" \
"v_mfma_f32_16x16x32_bf16 v[216:219], a[176:179], v[0:3], 0\n" \
"v_mfma_f32_16x16x32_bf16 v[216:219], a[180:183], v[8:11], v[216:219]\n" \
"v_mfma_f32_16x16x32_bf16 v[216:219], a[184:187], v[16:19], v[216:219]\n" \
"v_mfma_f32_16x16x32_bf16 v[216:219], a[188:191], v[24:27], v[216:219]\n" \
"s_nop 7\n" \
"s_nop 0\n" \
"v_mov_b32_dpp v231, v216 quad_perm:[0,1,2,3] row_mask:0x1 bank_mask:0xf\n" \
"v_mfma_f32_16x16x32_bf16 v[232:235], a[176:179], v[32:35], 0\n" \
"v_mfma_f32_16x16x32_bf16 v[232:235], a[180:183], v[40:43], v[232:235]\n" \
"v_mfma_f32_16x16x32_bf16 v[232:235], a[184:187], v[48:51], v[232:235]\n" \
"v_mfma_f32_16x16x32_bf16 v[232:235], a[188:191], v[56:59], v[232:235]\n" \
"s_nop 7\n" \
"s_nop 0\n" \
"v_mov_b32_dpp v231, v232 quad_perm:[0,1,2,3] row_mask:0x2 bank_mask:0xf\n" \
"v_mfma_f32_16x16x32_bf16 v[216:219], a[176:179], v[64:67], 0\n" \
"v_mfma_f32_16x16x32_bf16 v[216:219], a[180:183], v[72:75], v[216:219]\n" \
"v_mfma_f32_16x16x32_bf16 v[216:219], a[184:187], v[80:83], v[216:219]\n" \
"v_mfma_f32_16x16x32_bf16 v[216:219], a[188:191], v[88:91], v[216:219]\n" \
"s_nop 7\n" \
"s_nop 0\n" \
"v_mov_b32_dpp v231, v216 quad_perm:[0,1,2,3] row_mask:0x4 bank_mask:0xf\n" \
"v_mfma_f32_16x16x32_bf16 v[232:235], a[176:179], v[96:99], 0\n" \
"v_mfma_f32_16x16x32_bf16 v[232:235], a[180:183], v[104:107], v[232:235]\n" \
"v_mfma_f32_16x16x32_bf16 v[232:235], a[184:187], v[112:115], v[232:235]\n" \
"v_mfma_f32_16x16x32_bf16 v[232:235], a[188:191], v[120:123], v[232:235]\n" \
"s_nop 7\n" \
"s_nop 0\n" \
"v_mov_b32_dpp v231, v232 quad_perm:[0,1,2,3] row_mask:0x8 bank_mask:0xf\n" \
"v_cmp_u_f32_e64 s[42:43], v231, v231\n" \
"s_cmp_lg_u64 s[42:43], 0\n" \
"s_cbranch_scc0 Ls0b_%=\n" \
"s_sub_u32 s51, s51, 1\n" \
"s_cbranch_scc0 Ls01_%=\n" \
"s_branch Ls0b_%=\n" \
"Ls1_%=:\n" \
"s_nop 0\n" \
"s_mov_b32 s51, 0x10000\n" \
"Ls11_%=:\n" \
"buffer_load_dword v235, v234, %[srdz], s63 offen sc1\n" \
"s_waitcnt vmcnt(0)\n" \
"v_and_b32_e32 v230, 1, v235\n" \
"v_cmp_eq_u32_e64 s[68:69], 0, v230\n" \
"s_cmp_lg_u64 s[68:69], 0\n" \
"s_cbranch_scc0 Ls1b_%=\n" \
"s_sub_u32 s51, s51, 1\n" \
"s_cbranch_scc0 Ls11_%=\n" \
"s_branch Ls1b_%=\n" \
"Lus_%=:\n" \
"s_nop 0\n" \
"s_mov_b32 s51, 0x10000\n" \
"v_lshrrev_b32_e64 v235, 3, v224\n" \
"s_waitcnt vmcnt(0)\n" \
"Lus1_%=:\n" \
"s_lshl_b32 s62, %[wid], 10\n" \
"s_add_u32 s62, s62, 262144\n" \
"s_add_u32 s55, s62, 8192\n" \
"v_readlane_b32 s68, %[tlo], 20\n" \
"v_readlane_b32 s69, %[thi], 20\n" \
"s_lshl_b32 s63, %[wid], 9\n" \
"v_lshrrev_b32_e32 v225, 4, v224\n" \
"v_add_u32_e64 v225, s63, v225\n" \
"buffer_load_dwordx4 v[230:233], v235, %[srdz], s62 offen sc1\n" \
"s_nop 0\n" \
"s_nop 0\n" \
"global_load_dwordx2 v[226:227], v225, s[68:69]\n" \
"buffer_load_dwordx4 v[234:237], v235, %[srdz], s55 offen sc1\n" \
"s_waitcnt vmcnt(0)\n" \
"s_nop 0\n" \
"v_bitop3_b32 v225, v230, v231, v232 bitop3:0x80\n" \
"v_bitop3_b32 v225, v225, v233, v234 bitop3:0x80\n" \
"v_bitop3_b32 v225, v225, v235, v236 bitop3:0x80\n" \
"v_bitop3_b32 v225, v225, v237, 1 bitop3:0x80\n" \
"v_cmp_eq_u32_e32 vcc, 0, v225\n" \
"s_cbranch_vccz Lus2_%=\n" \
"v_lshrrev_b32_e32 v235, 3, v224\n" \
"s_sub_u32 s51, s51, 1\n" \
"s_cbranch_scc0 Lus1_%=\n" \
"Lus2_%=:\n" \
"s_branch Lub_%=\n" \
"Lwalk_%=:\n" \
"s_mov_b64 exec, 0\n" \
"s_cmp_eq_u32 %[wid], 1\n" \
"s_cbranch_scc1 Lk0_%=\n" \
"s_cmp_eq_u32 %[wid], 2\n" \
"s_cbranch_scc1 Lk6_%=\n" \
"s_branch Lk12_%=\n" \
"adaln_kr:\n" \
"s_mov_b64 exec, -1\n" \
"s_branch Lgo_%=\n" \
"Lslow_%=:\n" \
"s_mov_b32 s52, 1\n" \
"s_mov_b32 s48, 0\n" \
"s_mov_b32 s51, 32\n" \
"s_nop 0\n" \
"s_mov_b32 s62, 0x80000000\n" \
"s_mov_b32 s63, 0x80000000\n" \
"s_waitcnt vmcnt(0)\n" \
"s_nop 0\n" \
"v_readlane_b32 s64, %[tlo], 9\n" \
"v_readlane_b32 s65, %[thi], 9\n" \
"s_mov_b32 s66, %[wbytes]\n" \
"v_mov_b32_e32 v229, %[vg0]\n" \
"s_mov_b32 s44, %[m0t]\n" \
"s_mov_b32 s45, 0\n" \
"v_xor_b32_e64 v208, 64, %[kb0]\n" \
"v_xor_b32_e32 v209, 0x80, %[kb0]\n" \
"v_xor_b32_e32 v210, 0xc0, %[kb0]\n" \
"v_xor_b32_e64 v212, 64, %[kd0]\n" \
"v_xor_b32_e32 v213, 0x80, %[kd0]\n" \
"v_xor_b32_e32 v214, 0xc0, %[kd0]\n" \
"v_xor_b32_e64 v211, 32, %[vvb]\n" \
"v_add_u32_e32 v215, 0xfffffc80, %[vd0]\n" \
"s_mov_b32 s66, 0\n" \
"s_nop 0\n" \
"v_mov_b32_e32 v225, 0xff800000\n" \
"v_mov_b32_e32 v226, 0xff800000\n" \
"v_mov_b32_e32 v227, 0xff800000\n" \
"v_mov_b32_e32 v228, 0xff800000\n" \
"s_waitcnt vmcnt(0) lgkmcnt(0)\n" \
"s_barrier\n" \
"Lsl_%=:\n" \
"s_mov_b32 m0, s40\n" \
"s_nop 0\n" \
"buffer_load_dwordx4 %[kd0], %[srdk], s48 offen offset:0 lds\n" \
"buffer_load_dwordx4 v212, %[srdk], s48 offen offset:1024 lds\n" \
"buffer_load_dwordx4 v213, %[srdk], s48 offen offset:2048 lds\n" \
"buffer_load_dwordx4 v214, %[srdk], s48 offen offset:3072 lds\n" \
"s_waitcnt vmcnt(0)\n" \
"s_barrier\n" \
"ds_read_b128 a[64:67], %[kb0] offset:0\n" \
"ds_read_b128 a[68:71], v208 offset:0\n" \
"ds_read_b128 a[72:75], v209 offset:0\n" \
"ds_read_b128 a[76:79], v210 offset:0\n" \
"ds_read_b128 a[80:83], %[kb0] offset:4096\n" \
"ds_read_b128 a[84:87], v208 offset:4096\n" \
"ds_read_b128 a[88:91], v209 offset:4096\n" \
"ds_read_b128 a[92:95], v210 offset:4096\n" \
"ds_read_b128 a[96:99], %[kb0] offset:8192\n" \
"ds_read_b128 a[100:103], v208 offset:8192\n" \
"ds_read_b128 a[104:107], v209 offset:8192\n" \
"ds_read_b128 a[108:111], v210 offset:8192\n" \
"ds_read_b128 a[112:115], %[kb0] offset:12288\n" \
"ds_read_b128 a[116:119], v208 offset:12288\n" \
"ds_read_b128 a[120:123], v209 offset:12288\n" \
"ds_read_b128 a[124:127], v210 offset:12288\n" \
"s_waitcnt lgkmcnt(0)\n" \
"s_barrier\n" \
"v_mfma_f32_16x16x32_bf16 v[128:131], a[64:67], a[0:3], 0\n" \
"v_mfma_f32_16x16x32_bf16 v[128:131], a[68:71], a[4:7], v[128:131]\n" \
"v_mfma_f32_16x16x32_bf16 v[128:131], a[72:75], a[8:11], v[128:131]\n" \
"v_mfma_f32_16x16x32_bf16 v[128:131], a[76:79], a[12:15], v[128:131]\n" \
"v_mfma_f32_16x16x32_bf16 v[132:135], a[80:83], a[0:3], 0\n" \
"v_mfma_f32_16x16x32_bf16 v[132:135], a[84:87], a[4:7], v[132:135]\n" \
"v_mfma_f32_16x16x32_bf16 v[132:135], a[88:91], a[8:11], v[132:135]\n" \
"v_mfma_f32_16x16x32_bf16 v[132:135], a[92:95], a[12:15], v[132:135]\n" \
"v_mfma_f32_16x16x32_bf16 v[136:139], a[64:67], a[16:19], 0\n" \
"v_mfma_f32_16x16x32_bf16 v[136:139], a[68:71], a[20:23], v[136:139]\n" \
"v_mfma_f32_16x16x32_bf16 v[136:139], a[72:75], a[24:27], v[136:139]\n" \
"v_mfma_f32_16x16x32_bf16 v[136:139], a[76:79], a[28:31], v[136:139]\n" \
"v_mfma_f32_16x16x32_bf16 v[140:143], a[80:83], a[16:19], 0\n" \
"v_mfma_f32_16x16x32_bf16 v[140:143], a[84:87], a[20:23], v[140:143]\n" \
"v_mfma_f32_16x16x32_bf16 v[140:143], a[88:91], a[24:27], v[140:143]\n" \
"v_mfma_f32_16x16x32_bf16 v[140:143], a[92:95], a[28:31], v[140:143]\n" \
"v_mfma_f32_16x16x32_bf16 v[144:147], a[64:67], a[32:35], 0\n" \
"v_mfma_f32_16x16x32_bf16 v[144:147], a[68:71], a[36:39], v[144:147]\n" \
"v_mfma_f32_16x16x32_bf16 v[144:147], a[72:75], a[40:43], v[144:147]\n" \
"v_mfma_f32_16x16x32_bf16 v[144:147], a[76:79], a[44:47], v[144:147]\n" \
"v_mfma_f32_16x16x32_bf16 v[148:151], a[80:83], a[32:35], 0\n" \
"v_mfma_f32_16x16x32_bf16 v[148:151], a[84:87], a[36:39], v[148:151]\n" \
"v_mfma_f32_16x16x32_bf16 v[148:151], a[88:91], a[40:43], v[148:151]\n" \
"v_mfma_f32_16x16x32_bf16 v[148:151], a[92:95], a[44:47], v[148:151]\n" \
"v_mfma_f32_16x16x32_bf16 v[152:155], a[64:67], a[48:51], 0\n" \
"v_mfma_f32_16x16x32_bf16 v[152:155], a[68:71], a[52:55], v[152:155]\n" \
"v_mfma_f32_16x16x32_bf16 v[152:155], a[72:75], a[56:59], v[152:155]\n" \
"v_mfma_f32_16x16x32_bf16 v[152:155], a[76:79], a[60:63], v[152:155]\n" \
"v_mfma_f32_16x16x32_bf16 v[156:159], a[80:83], a[48:51], 0\n" \
"v_mfma_f32_16x16x32_bf16 v[156:159], a[84:87], a[52:55], v[156:159]\n" \
"v_mfma_f32_16x16x32_bf16 v[156:159], a[88:91], a[56:59], v[156:159]\n" \
"v_mfma_f32_16x16x32_bf16 v[156:159], a[92:95], a[60:63], v[156:159]\n" \
"s_nop 7\n" \
"s_nop 0\n" \
"v_max3_f32 v230, v128, v129, v130\n" \
"v_max3_f32 v231, v131, v132, v133\n" \
"v_max3_f32 v230, v230, v134, v135\n" \
"v_max3_f32 v225, v230, v231, v225\n" \
"v_max3_f32 v230, v136, v137, v138\n" \
"v_max3_f32 v231, v139, v140, v141\n" \
"v_max3_f32 v230, v230, v142, v143\n" \
"v_max3_f32 v226, v230, v231, v226\n" \
"v_max3_f32 v230, v144, v145, v146\n" \
"v_max3_f32 v231, v147, v148, v149\n" \
"v_max3_f32 v230, v230, v150, v151\n" \
"v_max3_f32 v227, v230, v231, v227\n" \
"v_max3_f32 v230, v152, v153, v154\n" \
"v_max3_f32 v231, v155, v156, v157\n" \
"v_max3_f32 v230, v230, v158, v159\n" \
"v_max3_f32 v228, v230, v231, v228\n" \
"s_nop 3\n" \
"s_nop 0\n" \
"v_mfma_f32_16x16x32_bf16 v[128:131], a[96:99], a[0:3], 0\n" \
"v_mfma_f32_16x16x32_bf16 v[128:131], a[100:103], a[4:7], v[128:131]\n" \
"v_mfma_f32_16x16x32_bf16 v[128:131], a[104:107], a[8:11], v[128:131]\n" \
"v_mfma_f32_16x16x32_bf16 v[128:131], a[108:111], a[12:15], v[128:131]\n" \
"v_mfma_f32_16x16x32_bf16 v[132:135], a[112:115], a[0:3], 0\n" \
"v_mfma_f32_16x16x32_bf16 v[132:135], a[116:119], a[4:7], v[132:135]\n" \
"v_mfma_f32_16x16x32_bf16 v[132:135], a[120:123], a[8:11], v[132:135]\n" \
"v_mfma_f32_16x16x32_bf16 v[132:135], a[124:127], a[12:15], v[132:135]\n" \
"v_mfma_f32_16x16x32_bf16 v[136:139], a[96:99], a[16:19], 0\n" \
"v_mfma_f32_16x16x32_bf16 v[136:139], a[100:103], a[20:23], v[136:139]\n" \
"v_mfma_f32_16x16x32_bf16 v[136:139], a[104:107], a[24:27], v[136:139]\n" \
"v_mfma_f32_16x16x32_bf16 v[136:139], a[108:111], a[28:31], v[136:139]\n" \
"v_mfma_f32_16x16x32_bf16 v[140:143], a[112:115], a[16:19], 0\n" \
"v_mfma_f32_16x16x32_bf16 v[140:143], a[116:119], a[20:23], v[140:143]\n" \
"v_mfma_f32_16x16x32_bf16 v[140:143], a[120:123], a[24:27], v[140:143]\n" \
"v_mfma_f32_16x16x32_bf16 v[140:143], a[124:127], a[28:31], v[140:143]\n" \
"v_mfma_f32_16x16x32_bf16 v[144:147], a[96:99], a[32:35], 0\n" \
"v_mfma_f32_16x16x32_bf16 v[144:147], a[100:103], a[36:39], v[144:147]\n" \
"v_mfma_f32_16x16x32_bf16 v[144:147], a[104:107], a[40:43], v[144:147]\n" \
"v_mfma_f32_16x16x32_bf16 v[144:147], a[108:111], a[44:47], v[144:147]\n" \
"v_mfma_f32_16x16x32_bf16 v[148:151], a[112:115], a[32:35], 0\n" \
"v_mfma_f32_16x16x32_bf16 v[148:151], a[116:119], a[36:39], v[148:151]\n" \
"v_mfma_f32_16x16x32_bf16 v[148:151], a[120:123], a[40:43], v[148:151]\n" \
"v_mfma_f32_16x16x32_bf16 v[148:151], a[124:127], a[44:47], v[148:151]\n" \
"v_mfma_f32_16x16x32_bf16 v[152:155], a[96:99], a[48:51], 0\n" \
"v_mfma_f32_16x16x32_bf16 v[152:155], a[100:103], a[52:55], v[152:155]\n" \
"v_mfma_f32_16x16x32_bf16 v[152:155], a[104:107], a[56:59], v[152:155]\n" \
"v_mfma_f32_16x16x32_bf16 v[152:155], a[108:111], a[60:63], v[152:155]\n" \
"v_mfma_f32_16x16x32_bf16 v[156:159], a[112:115], a[48:51], 0\n" \
"v_mfma_f32_16x16x32_bf16 v[156:159], a[116:119], a[52:55], v[156:159]\n" \
"v_mfma_f32_16x16x32_bf16 v[156:159], a[120:123], a[56:59], v[156:159]\n" \
"v_mfma_f32_16x16x32_bf16 v[156:159], a[124:127], a[60:63], v[156:159]\n" \
"s_nop 7\n" \
"s_nop 0\n" \
"v_max3_f32 v230, v128, v129, v130\n" \
"v_max3_f32 v231, v131, v132, v133\n" \
"v_max3_f32 v230, v230, v134, v135\n" \
"v_max3_f32 v225, v230, v231, v225\n" \
"v_max3_f32 v230, v136, v137, v138\n" \
"v_max3_f32 v231, v139, v140, v141\n" \
"v_max3_f32 v230, v230, v142, v143\n" \
"v_max3_f32 v226, v230, v231, v226\n" \
"v_max3_f32 v230, v144, v145, v146\n" \
"v_max3_f32 v231, v147, v148, v149\n" \
"v_max3_f32 v230, v230, v150, v151\n" \
"v_max3_f32 v227, v230, v231, v227\n" \
"v_max3_f32 v230, v152, v153, v154\n" \
"v_max3_f32 v231, v155, v156, v157\n" \
"v_max3_f32 v230, v230, v158, v159\n" \
"v_max3_f32 v228, v230, v231, v228\n" \
"s_nop 3\n" \
"s_add_u32 s48, s48, s54\n" \
"s_sub_u32 s51, s51, 1\n" \
"s_cmp_lg_u32 s51, 0\n" \
"s_cbranch_scc1 Lsl_%=\n" \
"s_nop 0\n" \
"v_mov_b32_e32 v232, v225\n" \
"v_mov_b32_e32 v233, v226\n" \
"v_mov_b32_e32 v234, v227\n" \
"v_mov_b32_e32 v235, v228\n" \
"s_nop 0\n" \
"v_permlane16_swap_b32_e32 v225, v232\n" \
"v_permlane16_swap_b32_e32 v226, v233\n" \
"v_permlane16_swap_b32_e32 v227, v234\n" \
"v_permlane16_swap_b32_e32 v228, v235\n" \
"v_max_f32_e32 v225, v225, v232\n" \
"v_max_f32_e32 v226, v226, v233\n" \
"v_max_f32_e32 v227, v227, v234\n" \
"v_max_f32_e32 v228, v228, v235\n" \
"v_mov_b32_e32 v232, v225\n" \
"v_mov_b32_e32 v233, v226\n" \
"v_mov_b32_e32 v234, v227\n" \
"v_mov_b32_e32 v235, v228\n" \
"s_nop 0\n" \
"v_permlane32_swap_b32_e32 v225, v232\n" \
"v_permlane32_swap_b32_e32 v226, v233\n" \
"v_permlane32_swap_b32_e32 v227, v234\n" \
"v_permlane32_swap_b32_e32 v228, v235\n" \
"v_max_f32_e32 v225, v225, v232\n" \
"v_max_f32_e32 v226, v226, v233\n" \
"v_max_f32_e32 v227, v227, v234\n" \
"v_max_f32_e32 v228, v228, v235\n" \
"s_cmp_lg_u32 s69, 0\n" \
"s_cbranch_scc1 Lnewm_%=\n" \
"v_sub_f32_e32 v225, 0, v160\n" \
"v_sub_f32_e32 v226, 0, v164\n" \
"v_sub_f32_e64 v227, 0, v168\n" \
"v_sub_f32_e32 v228, 0, v172\n" \
"Lnewm_%=:\n" \
"s_mov_b32 s48, 0\n" \
"s_mov_b32 m0, s40\n" \
"s_nop 0\n" \
"buffer_load_dwordx4 %[kd0], %[srdk], s48 offen offset:0 lds\n" \
"buffer_load_dwordx4 v212, %[srdk], s48 offen offset:1024 lds\n" \
"buffer_load_dwordx4 v213, %[srdk], s48 offen offset:2048 lds\n" \
"buffer_load_dwordx4 v214, %[srdk], s48 offen offset:3072 lds\n" \
"s_mov_b32 s48, 16384\n" \
"s_mov_b32 m0, s41\n" \
"s_nop 0\n" \
"buffer_load_dwordx4 %[kd0], %[srdk], s48 offen offset:0 lds\n" \
"buffer_load_dwordx4 v212, %[srdk], s48 offen offset:1024 lds\n" \
"buffer_load_dwordx4 v213, %[srdk], s48 offen offset:2048 lds\n" \
"buffer_load_dwordx4 v214, %[srdk], s48 offen offset:3072 lds\n" \
"s_add_u32 m0, %[m0b], 65536\n" \
"s_nop 0\n" \
"s_nop 0\n" \
"buffer_load_dwordx4 %[vd0], %[srdv], s48 offen offset:0 lds\n" \
"buffer_load_dwordx4 v215, %[srdv], s48 offen offset:1024 lds\n" \
"buffer_load_dwordx4 %[vd0], %[srdv], s48 offen offset:2048 lds\n" \
"buffer_load_dwordx4 v215, %[srdv], s48 offen offset:3072 lds\n" \
"s_mov_b32 s48, 32768\n" \
"s_mov_b32 m0, s42\n" \
"s_nop 0\n" \
"buffer_load_dwordx4 %[kd0], %[srdk], s48 offen offset:0 lds\n" \
"buffer_load_dwordx4 v212, %[srdk], s48 offen offset:1024 lds\n" \
"buffer_load_dwordx4 v213, %[srdk], s48 offen offset:2048 lds\n" \
"buffer_load_dwordx4 v214, %[srdk], s48 offen offset:3072 lds\n" \
"s_add_u32 m0, %[m0b], 81920\n" \
"s_nop 0\n" \
"s_nop 0\n" \
"buffer_load_dwordx4 %[vd0], %[srdv], s48 offen offset:0 lds\n" \
"buffer_load_dwordx4 v215, %[srdv], s48 offen offset:1024 lds\n" \
"buffer_load_dwordx4 %[vd0], %[srdv], s48 offen offset:2048 lds\n" \
"buffer_load_dwordx4 v215, %[srdv], s48 offen offset:3072 lds\n" \
"s_mov_b32 s48, 49152\n" \
"s_mov_b32 m0, s43\n" \
"s_nop 0\n" \
"buffer_load_dwordx4 %[kd0], %[srdk], s48 offen offset:0 lds\n" \
"buffer_load_dwordx4 v212, %[srdk], s48 offen offset:1024 lds\n" \
"buffer_load_dwordx4 v213, %[srdk], s48 offen offset:2048 lds\n" \
"buffer_load_dwordx4 v214, %[srdk], s48 offen offset:3072 lds\n" \
"s_add_u32 m0, %[m0b], 98304\n" \
"s_nop 0\n" \
"s_nop 0\n" \
"buffer_load_dwordx4 %[vd0], %[srdv], s48 offen offset:0 lds\n" \
"buffer_load_dwordx4 v215, %[srdv], s48 offen offset:1024 lds\n" \
"buffer_load_dwordx4 %[vd0], %[srdv], s48 offen offset:2048 lds\n" \
"buffer_load_dwordx4 v215, %[srdv], s48 offen offset:3072 lds\n" \
"s_waitcnt vmcnt(0)\n" \
"s_barrier\n" \
"ds_read_b128 a[64:67], %[kb0] offset:0\n" \
"ds_read_b128 a[68:71], v208 offset:0\n" \
"ds_read_b128 a[72:75], v209 offset:0\n" \
"ds_read_b128 a[76:79], v210 offset:0\n" \
"ds_read_b128 a[80:83], %[kb0] offset:4096\n" \
"ds_read_b128 a[84:87], v208 offset:4096\n" \
"ds_read_b128 a[88:91], v209 offset:4096\n" \
"ds_read_b128 a[92:95], v210 offset:4096\n" \
"s_mov_b32 s48, 65536\n" \
"s_mov_b32 s60, 0\n" \
"s_waitcnt lgkmcnt(0)\n" \
"v_mfma_f32_16x16x32_bf16 v[128:131], a[64:67], a[0:3], 0\n" \
"ds_read_b128 a[96:99], %[kb0] offset:8192\n" \
"v_mfma_f32_16x16x32_bf16 v[128:131], a[68:71], a[4:7], v[128:131]\n" \
"ds_read_b128 a[100:103], v208 offset:8192\n" \
"v_mfma_f32_16x16x32_bf16 v[128:131], a[72:75], a[8:11], v[128:131]\n" \
"ds_read_b128 a[104:107], v209 offset:8192\n" \
"v_mfma_f32_16x16x32_bf16 v[128:131], a[76:79], a[12:15], v[128:131]\n" \
"ds_read_b128 a[108:111], v210 offset:8192\n" \
"v_mfma_f32_16x16x32_bf16 v[132:135], a[80:83], a[0:3], 0\n" \
"ds_read_b128 a[112:115], %[kb0] offset:12288\n" \
"v_mfma_f32_16x16x32_bf16 v[132:135], a[84:87], a[4:7], v[132:135]\n" \
"ds_read_b128 a[116:119], v208 offset:12288\n" \
"v_mfma_f32_16x16x32_bf16 v[132:135], a[88:91], a[8:11], v[132:135]\n" \
"ds_read_b128 a[120:123], v209 offset:12288\n" \
"v_mfma_f32_16x16x32_bf16 v[132:135], a[92:95], a[12:15], v[132:135]\n" \
"ds_read_b128 a[124:127], v210 offset:12288\n" \
"v_mfma_f32_16x16x32_bf16 v[136:139], a[64:67], a[16:19], 0\n" \
"ds_read_b64_tr_b16 a[128:129], %[vvb] offset:0\n" \
"v_mfma_f32_16x16x32_bf16 v[136:139], a[68:71], a[20:23], v[136:139]\n" \
"ds_read_b64_tr_b16 a[130:131], %[vvb] offset:4096\n" \
"v_mfma_f32_16x16x32_bf16 v[136:139], a[72:75], a[24:27], v[136:139]\n" \
"ds_read_b64_tr_b16 a[132:133], v211 offset:0\n" \
"v_mfma_f32_16x16x32_bf16 v[136:139], a[76:79], a[28:31], v[136:139]\n" \
"ds_read_b64_tr_b16 a[134:135], v211 offset:4096\n" \
"v_mfma_f32_16x16x32_bf16 v[140:143], a[80:83], a[16:19], 0\n" \
"ds_read_b64_tr_b16 a[136:137], %[vvb] offset:512\n" \
"v_mfma_f32_16x16x32_bf16 v[140:143], a[84:87], a[20:23], v[140:143]\n" \
"ds_read_b64_tr_b16 a[138:139], %[vvb] offset:4608\n" \
"v_mfma_f32_16x16x32_bf16 v[140:143], a[88:91], a[24:27], v[140:143]\n" \
"ds_read_b64_tr_b16 a[140:141], v211 offset:512\n" \
"v_mfma_f32_16x16x32_bf16 v[140:143], a[92:95], a[28:31], v[140:143]\n" \
"ds_read_b64_tr_b16 a[142:143], v211 offset:4608\n" \
"v_mfma_f32_16x16x32_bf16 v[144:147], a[64:67], a[32:35], 0\n" \
"ds_read_b64_tr_b16 a[144:145], %[vvb] offset:1024\n" \
"v_mfma_f32_16x16x32_bf16 v[144:147], a[68:71], a[36:39], v[144:147]\n" \
"ds_read_b64_tr_b16 a[146:147], %[vvb] offset:5120\n" \
"v_mfma_f32_16x16x32_bf16 v[144:147], a[72:75], a[40:43], v[144:147]\n" \
"ds_read_b64_tr_b16 a[148:149], v211 offset:1024\n" \
"v_mfma_f32_16x16x32_bf16 v[144:147], a[76:79], a[44:47], v[144:147]\n" \
"ds_read_b64_tr_b16 a[150:151], v211 offset:5120\n" \
"v_mfma_f32_16x16x32_bf16 v[148:151], a[80:83], a[32:35], 0\n" \
"ds_read_b64_tr_b16 a[152:153], %[vvb] offset:1536\n" \
"v_mfma_f32_16x16x32_bf16 v[148:151], a[84:87], a[36:39], v[148:151]\n" \
"ds_read_b64_tr_b16 a[154:155], %[vvb] offset:5632\n" \
"v_mfma_f32_16x16x32_bf16 v[148:151], a[88:91], a[40:43], v[148:151]\n" \
"ds_read_b64_tr_b16 a[156:157], v211 offset:1536\n" \
"v_mfma_f32_16x16x32_bf16 v[148:151], a[92:95], a[44:47], v[148:151]\n" \
"ds_read_b64_tr_b16 a[158:159], v211 offset:5632\n" \
"v_mfma_f32_16x16x32_bf16 v[152:155], a[64:67], a[48:51], 0\n" \
"v_mfma_f32_16x16x32_bf16 v[152:155], a[68:71], a[52:55], v[152:155]\n" \
"v_mfma_f32_16x16x32_bf16 v[152:155], a[72:75], a[56:59], v[152:155]\n" \
"v_mfma_f32_16x16x32_bf16 v[152:155], a[76:79], a[60:63], v[152:155]\n" \
"v_mfma_f32_16x16x32_bf16 v[156:159], a[80:83], a[48:51], 0\n" \
"v_mfma_f32_16x16x32_bf16 v[156:159], a[84:87], a[52:55], v[156:159]\n" \
"v_mfma_f32_16x16x32_bf16 v[156:159], a[88:91], a[56:59], v[156:159]\n" \
"v_mfma_f32_16x16x32_bf16 v[156:159], a[92:95], a[60:63], v[156:159]\n" \
"s_branch Lhavem_%=\n" \
"s_nop 0\n" \
"Lend_%=:\n" \
"s_waitcnt lgkmcnt(0)\n" \
"s_nop 0\n" \
""

#define SRD_WORD3 0x00020000
DI u32x4 ld16_sc1(const void* p) { u32x4 v; asm volatile("global_load_dwordx4 %0, %1, off sc1" : "=v"(v) : "v"(p) : "memory"); return v; }
DI unsigned ord_key(float r, unsigned row) {
  const unsigned b = __float_as_uint(r), o = (b & 0x80000000u) ? ~b : (b | 0x80000000u);
  return max(o & 0xfffff800u, 0x800u) | (2047u - row);
}
DI void top2_merge(unsigned& a1, unsigned& a2, unsigned b1, unsigned b2) {
  const unsigned n1 = max(a1, b1), n2 = max(min(a1, b1), max(a2, b2)); a1 = n1; a2 = n2;
}
template <int CTRL> DI void top2_dpp(unsigned& k1, unsigned& k2) {
  const unsigned p1 = __builtin_amdgcn_update_dpp(0u, k1, CTRL, 0xf, 0xf, true), p2 = __builtin_amdgcn_update_dpp(0u, k2, CTRL, 0xf, 0xf, true); top2_merge(k1, k2, p1, p2);
}
DI float row_sum(float v) {
  v = dpp_add<DPP_XOR1>(v); v = dpp_add<DPP_XOR2>(v); v = dpp_add<DPP_ROR8>(v);
  v = swap16_add(v, v); return swap32_add(v, v);
}
DI unsigned h2_pair(float x0, float x1, float m, float rden, unsigned c1w, unsigned sbw) { return ln_pair(x0, x1, m, rden, lo16(c1w), hi16(c1w), lo16(sbw), hi16(sbw)); }
#define PAGE(n, nx) asm volatile("s_branch adaln_pe" #n "\n.p2align 12\nadaln_pg" #n ":\n\ts_cbranch_execz " nx "\nadaln_pe" #n ":")
constexpr int MOE_WGS = DFF / 16;
constexpr unsigned RETRY_CAP = 4096;
#define ASLEEP 2
#define KLOOK 4
#define YSPLIT 8
#define FSLEEP 14
#define KSLEEP 3
#define YEARLY 8
#define FREST 0
#define KL0 1u
#define KLS 2u
#define KST 2u
#define KCHK 8
#define KPAUSE 2
#define KEY_SEL b

#define WBYTES (moe ? DM * DFF * 2 : 0)
namespace fa {
constexpr unsigned LDS_BYTES = 163840;
constexpr float QSCALE = 0.12751743082f;
struct Args { const u16 *x, *wmod, *wq, *kc, *vc, *wo, *wg, *wu, *wd, *c, *wr; char* ws; u16* out;
};
__global__ __launch_bounds__(256, 1) void fattn_k(const Args p) {
  extern __shared__ __attribute__((aligned(16))) char lds[];
  const unsigned wid = __builtin_amdgcn_readfirstlane(threadIdx.x >> 6);
  const unsigned b = blockIdx.x, idx = b >> 3, kg = b & 3u, m = idx & 7u, hd = 8u * kg + m, y = (b & 4u) + (idx >> 3);
  const unsigned ldsb = (unsigned)(size_t)lds;
  const unsigned m0b = ldsb + 4096u * wid, m0t = ldsb + 131072u + 8192u * wid;
  const __amdgpu_buffer_rsrc_t srdz = __builtin_amdgcn_make_buffer_rsrc((void*)p.ws, (short)0, (int)WS_END, SRD_WORD3);
  const bool moe = b < MOE_WGS;
  const unsigned sl = 4u * ((b & 7u) * 7u + (idx >> 2)) + (idx & 3u), yp2 = (y & 1u) * 2u;
  const unsigned sin = (unsigned)WS_XIN + ((y * 32u + hd) * 4u + wid) * BOX;
  u32x2 xq[8]; unsigned fl, fl2, got, got2; unsigned long long tag;
  unsigned k1, k2; unsigned long long lat;
  const unsigned sxf = (unsigned)WS_XFL + (y * 4u + wid) * 512u, sxl = (unsigned)WS_XFL + (y * 4u + kg) * 512u;
#define FQ(i)
#define TQ(i)
  {
    const unsigned tid = threadIdx.x, lane = tid & 63u, l16 = lane & 15u, g = lane >> 4, hi = lane >> 5, r8 = (lane >> 2) & 7u;
    unsigned kb0 = ldsb + 256u * l16 + 16u * (g ^ l16);
    unsigned vvb = ldsb + 65536u + 2048u * hi + 288u * (g & 1u) + 64u * ((lane >> 2) & 3u) + 8u * (lane & 3u);
    unsigned vwb = ldsb + 65536u + 2048u * g + 64u * ((lane >> 2) & 3u) + 16u * (lane & 3u);
    unsigned kd0 = 4096u * wid + 256u * g + 16u * (l16 ^ g);
    unsigned vd0 = 4096u * wid + 256u * r8 + 64u * hi + 16u * ((lane & 3u) ^ (2u * (r8 >> 2))) + 1024u;
    unsigned wd0 = 8192u * (16u * wid + r8) + 64u * hi + 16u * (lane & 3u) + 4096u;
    unsigned hq0 = (unsigned)WS_H + (16u * y + 4u * wid) * 32768u + 16u * lane;
    unsigned oq0 = (unsigned)WS_XIN + ((y * 32u + kg * 8u) * 4u + wid) * BOX + 20480u + 16u * lane;
    const unsigned flag = ldsb + 49152u + 4u * wid;
    const __amdgpu_buffer_rsrc_t srdw = __builtin_amdgcn_make_buffer_rsrc((void*)((const char*)p.wq + 256u * hd - 4096), (short)0, (int)(DM * DQ * 2 - 256u * hd + 4096u), SRD_WORD3);
    const __amdgpu_buffer_rsrc_t srdk = __builtin_amdgcn_make_buffer_rsrc((void*)((const char*)p.kc + (size_t)hd * (SEQ * 256)), (short)0, SEQ * 256, SRD_WORD3);
    const __amdgpu_buffer_rsrc_t srdv = __builtin_amdgcn_make_buffer_rsrc((void*)((const char*)p.vc + (size_t)hd * (SEQ * 256) - 17408), (short)0, SEQ * 256 + 17408, SRD_WORD3);
    unsigned long long pct; unsigned clen;
    asm volatile("s_getpc_b64 %0\n\ts_mov_b32 %1, adaln_text_end@rel32@lo+4" : "=s"(pct), "=s"(clen));
    unsigned vct = min(((idx * 4u + wid) * 6u + min(lane, 5u)) * 128u, (clen - 4u) & ~3u);
#define XNT " nt"
#define WNT " nt"
      const unsigned gr = 128u * (b & 7u) + 4u * (tid >> 3), gc = 64u * (b >> 3) + 8u * (tid & 7u), gran0 = b;
      u32x4 w[4], xs[4]; u32x2 ep, cv;
#define XV(i) ({ u32x4 v_; asm volatile("v_accvgpr_read_b32 %0, a[%4]\n\tv_accvgpr_read_b32 %1, a[%4 + 1]\n\tv_accvgpr_read_b32 %2, a[%4 + 2]\n\tv_accvgpr_read_b32 %3, a[%4 + 3]" : "=v"(v_[0]), "=v"(v_[1]), "=v"(v_[2]), "=v"(v_[3]) : "n"(200 + 4 * (i))); v_; })
#define CPOS 1
      #pragma unroll
      for (int j = 0; j < 5; ++j) {
        if (j == CPOS) asm volatile("global_load_dwordx2 %0, %1, %2" : "=v"(cv) : "v"(gr * 2u), "s"(p.c) : "memory");
        if (j < 4) asm volatile("global_load_dwordx4 %0, %1, %2" WNT : "=v"(w[j]) : "v"(((gr + j) * NMOD + gc) * 2u), "s"(p.wmod) : "memory");
      }
      #pragma unroll
      for (int q = 0; q < 4; ++q) asm volatile("global_load_dwordx4 %0, %1, %2 offset:%3" XNT : "=v"(xs[q]) : "v"((8u * (y * 32u + hd) + 2u * wid) * 2048u + 16u * lane), "s"(p.x), "n"(1024 * q) : "memory");
      asm volatile("global_load_dwordx2 %0, %1, %2 sc1" : "=v"(ep) : "v"(0u), "s"(p.ws + WS_EP) : "memory");
      {
        const unsigned zo = b * Z_SHARE + tid * 16u - (unsigned)(WS_U - WS_Z);
        u32x4 zv;
        #pragma unroll
        for (int e = 0; e < 4; ++e) { const unsigned h = (zo + 4u * e + 1u) * 2654435761u; zv[e] = zo >= 16384u ? 0u : NORM && (zo & 2048u) ? 0x3f803f80u | ((h ^ (h >> 15)) * 0x2c1b3c6du & 0x80008000u) : ~0u; }
        asm volatile("buffer_store_dwordx4 %0, %1, %2, 0 offen sc1\n\ts_nop 1" :: "v"(zv), "v"(tid < Z_SHARE / 16u ? (unsigned)WS_Z + b * Z_SHARE + tid * 16u : 0x80000000u), "s"(srdz) : "memory");
      }
      FQ(6);
      asm volatile("global_load_dword a255, %0, %1" :: "v"(vct), "s"(pct) : "memory");
#define PRET 0
      asm volatile("s_mov_b64 exec, 0\n\ts_cmp_eq_u32 %0, 1\n\ts_cbranch_scc1 adaln_pg5\n\ts_cmp_eq_u32 %0, 2\n\ts_cbranch_scc1 adaln_pg6\n\ts_cmp_eq_u32 %0, 3\n\ts_cbranch_scc1 adaln_pg7\nadaln_fr:\n\ts_mov_b64 exec, -1" :: "s"(wid) : "scc");
      FQ(7);
      float* red = (float*)(lds + 131072 + 4096);
      auto silu = [](float v) { return rbf(v * __builtin_amdgcn_rcpf(1.f + __builtin_amdgcn_exp2f(v * -1.44269504089f))); };
      asm volatile("s_waitcnt vmcnt(%1)" : "+v"(cv) : "n"(11 - CPOS) : "memory");
      unsigned sp[2] = {cvtpk(silu(lo16(cv[0])), silu(hi16(cv[0]))), cvtpk(silu(lo16(cv[1])), silu(hi16(cv[1])))};
      unsigned gsa = lane < 4u ? (unsigned)WS_RST + ((8u * (y * 32u + hd) + 2u * wid) * 2u + lane) * 8u : 0x80000000u;
      unsigned gma = wid ? 0x80000000u : (unsigned)WS_MOD + (gran0 * 64u + lane) * 8u, gra = ldsb + 131072u + 4096u + lane * 4u;
      asm volatile("s_waitcnt vmcnt(7)" : "+v"(sp[0]), "+v"(sp[1]), "+v"(gsa), "+v"(gma), "+v"(gra), "+v"(w[0]), "+v"(w[1]), "+v"(w[2]), "+v"(w[3]) :: "memory");
      FQ(4);
      const unsigned rho = 0;
      unsigned tq0 = (idx * 8u + wid * 2u + ((lane >> 3) & 1u)) * 8192u + (b & 4u) * 256u + (lane & 7u) * 128u + 4096u - 256u * m;
#define WQT 0
#define KVT2 0
#define WQT2 4
#define TNT ""
      asm volatile("buffer_load_dword a255, %0, %1, 0 offen" TNT :: "v"(tq0), "s"(srdw) : "memory");
      {
        float acc[8];
        #pragma unroll
        for (int e = 0; e < 8; ++e) acc[e] = 0.f;
        #pragma unroll
        for (int j = 0; j < 2; ++j) {
          #pragma unroll
          for (int e = 0; e < 8; ++e) acc[e] = __builtin_amdgcn_fdot2_f32_bf16(__builtin_bit_cast(bf16x2, __builtin_amdgcn_perm(w[2 * j + 1][e >> 1], w[2 * j][e >> 1], (e & 1) ? 0x07060302u : 0x05040100u)), __builtin_bit_cast(bf16x2, sp[j]), acc[e], false);
        }
        float t[4], u[2];
        #pragma unroll
        for (int e = 0; e < 4; ++e) t[e] = swap32_add(acc[e], acc[e + 4]);
        #pragma unroll
        for (int e = 0; e < 2; ++e) u[e] = dpp_add<DPP_ROR8>(swap16_add(t[e], t[e + 2]));
        if ((lane & 8u) == 0) *(f32x2*)&red[wid * 64u + 8u * (tid & 7u) + 2u * g] = f32x2{u[0], u[1]};
      }
      unsigned gst, vx0r, vx1r;
      auto rowstats = [&]() __attribute__((always_inline)) {
      float stv4;
      {
        float t4[4] = {0.f, 0.f, 0.f, 0.f};
        #pragma unroll
        for (int e = 0; e < 16; ++e) {
          const unsigned xw = xs[e >> 2][e & 3]; const bf16x2 v = __builtin_bit_cast(bf16x2, xw);
          t4[2 * (e >> 3)] = __builtin_amdgcn_fdot2_f32_bf16(v, __builtin_bit_cast(bf16x2, 0x3f803f80u), t4[2 * (e >> 3)], false);
          t4[2 * (e >> 3) + 1] = __builtin_amdgcn_fdot2_f32_bf16(v, v, t4[2 * (e >> 3) + 1], false);
        }
        float u = swap16_add(swap32_add(t4[0], t4[2]), swap32_add(t4[1], t4[3]));
        u = dpp_add<DPP_XOR1>(u); u = dpp_add<DPP_XOR2>(u); u = dpp_add<DPP_ROR4>(u); u = dpp_add<DPP_ROR8>(u);
        const f32x2 mean = f32x2{__uint_as_float(__builtin_amdgcn_readlane(__float_as_uint(u), 0)), __uint_as_float(__builtin_amdgcn_readlane(__float_as_uint(u), 32))} * (1.f / DM);
        f32x2 v2 = f32x2{__uint_as_float(__builtin_amdgcn_readlane(__float_as_uint(u), 16)), __uint_as_float(__builtin_amdgcn_readlane(__float_as_uint(u), 48))} * (1.f / DM) - mean * mean;
        if (__builtin_expect(!(mean[0] * mean[0] <= v2[0] && mean[1] * mean[1] <= v2[1]), 0)) {
          f32x2 sq = {0.f, 0.f};
          #pragma unroll
          for (int e = 0; e < 8; ++e) {
            const f32x2 d0 = f32x2{lo16(xs[e >> 2][e & 3]), lo16(xs[2 + (e >> 2)][e & 3])} - mean, d1 = f32x2{hi16(xs[e >> 2][e & 3]), hi16(xs[2 + (e >> 2)][e & 3])} - mean;
            sq = __builtin_elementwise_fma(d1, d1, __builtin_elementwise_fma(d0, d0, sq));
          }
          v2 = f32x2{wave_sum(sq[0]), wave_sum(sq[1])} * (1.f / DM);
        }
        asm("s_nop 1\n\tv_readfirstlane_b32 %0, %2\n\tv_readfirstlane_b32 %1, %3" : "=&s"(vx0r), "=&s"(vx1r) : "v"(v2[0] + 1e-5f), "v"(v2[1] + 1e-5f));
        const float rs_ = __builtin_amdgcn_rcpf(rbf(__builtin_amdgcn_sqrtf(rbf(rbf((lane & 2u) ? v2[1] : v2[0]) + 1e-5f))));
        stv4 = rs_ * ((lane & 1u) ? 1.f : -rbf((lane & 2u) ? mean[1] : mean[0]));
      }
      gst = (__float_as_uint(stv4) + 0x80u) & 0xffffff00u; };
#define STP 0
      if (STP == 0) { asm volatile("s_waitcnt vmcnt(4)" : "+v"(xs[0]), "+v"(xs[1]), "+v"(xs[2]), "+v"(xs[3]) :: "memory"); rowstats(); asm volatile("" : "+v"(gst)); }
      asm volatile("s_waitcnt vmcnt(%5) lgkmcnt(0)\n\ts_barrier" : "+v"(xs[0]), "+v"(xs[1]), "+v"(xs[2]), "+v"(xs[3]), "+v"(ep) : "n"(PRET ? 3 : 2) : "memory");

      tag = (((unsigned long long)__builtin_amdgcn_readfirstlane(ep[1]) << 32) | __builtin_amdgcn_readfirstlane(ep[0])) + 1ull;
      const unsigned tl = (unsigned)tag & 0xffu, th = (unsigned)(tag >> 8);
      auto gran = [&](float v) { return ((unsigned long long)th << 32) | (((__float_as_uint(v) + 0x80u) & 0xffffff00u) | tl); };
      {
        float r0, r1, r2, r3;
        if (STP == 0) asm volatile("buffer_store_dwordx2 %0, %1, %2, 0 offen sc1" :: "v"(u32x2{gst | tl, th}), "v"(gsa), "s"(srdz) : "memory");
        asm volatile("ds_read_b32 %0, %4\n\tds_read_b32 %1, %4 offset:256\n\tds_read_b32 %2, %4 offset:512\n\tds_read_b32 %3, %4 offset:768\n\ts_waitcnt lgkmcnt(0)"
                     : "=&v"(r0), "=&v"(r1), "=&v"(r2), "=&v"(r3) : "v"(gra) : "memory");
        asm volatile("buffer_store_dwordx2 %0, %1, %2, 0 offen sc1\n\ts_nop 0" :: "v"(u32x2{((__float_as_uint((r0 + r1) + (r2 + r3)) + 0x80u) & 0xffffff00u) | tl, th}), "v"(gma), "s"(srdz) : "memory");
        if (STP == 1) { rowstats(); asm volatile("buffer_store_dwordx2 %0, %1, %2, 0 offen sc1\n\ts_nop 0" :: "v"(u32x2{gst | tl, th}), "v"(gsa), "s"(srdz) : "memory"); }
      }
      FQ(5);
#define SWQ 0
#define SWQ0 0x400000
      const unsigned rb0 = 16u * y + 4u * (hd >> 3), ch = 16u * (hd & 7u) + (tid >> 4);
      asm volatile("global_load_dwordx2 a[216:217], %0, %1" :: "v"(tid * 8u), "s"(p.c) : "memory");
      #pragma unroll
      for (int i = 0; i < 4; ++i) asm volatile("global_load_dwordx4 a[%2:%2 + 3], %0, %1" :: "v"(((rb0 + i) * 16u + l16) * 2048u + ch * 16u), "s"(p.x), "n"(200 + 4 * i) : "memory");
      PAGE(5, "adaln_fr");
      unsigned lane2 = lane; asm volatile("" : "+v"(lane2));
#define lane lane2
    unsigned wo0 = (32u * wid + r8) * 2048u + m * 256u + 64u * hi + 16u * ((lane & 3u) ^ (2u * (r8 >> 2)));
    const __amdgpu_buffer_rsrc_t srdt = __builtin_amdgcn_make_buffer_rsrc((void*)((const char*)p.wo + (size_t)kg * (DM * DM * 2)), (short)0, DM * DM * 2, SRD_WORD3);
    const __amdgpu_buffer_rsrc_t srdp = __builtin_amdgcn_make_buffer_rsrc((void*)((const char*)(y < 4u ? p.kc : p.vc) + (size_t)hd * (SEQ * 256) + (y & 3u) * 4096u), (short)0, (int)(KVPAR && (b & 1u) ? 0u : (KVPAR ? 0x400000u : 0u) + SEQ * 256 - (y & 3u) * 4096u), SRD_WORD3);
    unsigned vg0 = (256u * wid + (lane >> 1)) * (DFF * 2u) + sl * 32u + 16u * (lane & 1u);
    unsigned vdn = (16u * sl + hi) * 2048u + 512u * wid + 16u * ((lane & 31u) ^ (2u * hi));
    const unsigned mc0 = 2u * DM + 64u * ((idx >> 2) * 8u + (b & 7u)) + 16u * (idx & 3u);
    const unsigned md0 = (256u * wid + (lane >> 1)) * (NMOD * 2u) + mc0 * 2u + 16u * (lane & 1u);
    unsigned sd0 = ldsb + 131072u + 8192u + 512u * wid + 16u * g;
    fl = md0;
    unsigned rd0 = ldsb + 131072u + 7168u + 8192u * ((lane >> 2) & 3u) + 256u * wid + 4u * (lane & 3u);
    unsigned ta0 = m0t + 256u * g + 32u * ((lane >> 2) & 3u) + 8u * (lane & 3u);
    const unsigned c8 = (b * 4u + wid) * 8192u, cd = moe ? sl * 32768u + wid * 8192u : 0u, cw = (idx * 4u + wid) * 16384u;
    const unsigned cm = moe ? ((sl & 3u) * 4u + wid) * 64u * (DFF * 2u) + (sl >> 2) * 128u : 0u;
    unsigned vxs = BOX * (l16 >> 1) + 64u * (l16 & 1u) + 16u * g;
    const unsigned sxs = (unsigned)WS_XIN + (y * 128u + wid * 32u) * BOX + (m >> 1) * 1024u + kg * 256u + (m & 1u) * 128u;
    unsigned vx0 = ((256u * y + 8u * hd + 2u * wid + ((lane >> 2) & 1u)) * DM + 256u * hi + 128u * (l16 >> 3) + 16u * (g & 1u) + 4u * (lane & 3u) + 32u * yp2) * 2u;
    unsigned tlo = 0, thi = 0;
#define tab(ln, lo, hi2) asm("v_writelane_b32 %0, %2, " #ln "\n\tv_writelane_b32 %1, %3, " #ln : "+v"(tlo), "+v"(thi) : "s"(lo), "s"(hi2))
#define tabp(ln, q, off) do { const unsigned long long a_ = (size_t)(q) + (off); tab(ln, (unsigned)a_, (unsigned)(a_ >> 32)); } while (0)
#define TORD 0
#define TAB_X(n)  tabp(n, p.x, ((y * 32u + hd) * 2u + (wid & 1u)) * 8192u)
#define TAB_U(n)  tabp(n, p.wu, moe ? c8 : 0u)
#define TAB_O(n)  tabp(n, p.wo, c8)
#define TAB_D(n)  tabp(n, p.wd, cd)
    TAB_X(1); TAB_U(2); TAB_O(3); TAB_D(4);
    tabp(5, p.wo, kg * 2097152u + cw); tabp(6, p.wo, kg * 2097152u + cw + 8192u); tabp(7, p.wg, cm);
    tabp(17, p.wmod, 0u); tab(18, NP_FILL == 8 ? sin + 20480u : (unsigned)WS_XIN + ((y * 32u + kg * 8u + (m >> 1)) * 4u + wid) * BOX + 20480u + (m & 1u) * 8192u, (unsigned)WS_OFL + y * 16384u + kg * 128u);
    tab(19, (unsigned)WS_OFL + (y * 32u + hd) * 512u + wid * 128u, (unsigned)WS_OFL + (y * 32u + kg * 8u) * 512u + wid * 128u);
    tabp(8, p.wg, 0u); tabp(9, p.wu, 0u); tabp(10, p.wd, 0u);
    tab(11, m, m * 4u * BOX); tab(12, (y & 1u) * 128u, 128u - (y & 1u) * 128u);
    tab(13, sxl, (unsigned)WS_PFL + (y * 32u + hd) * 512u + kg * 128u); tab(14, sxs, sxf);
    tab(15, (unsigned)WS_MODT + (mc0 - 2u * DM + 4u * wid) * 4u, (mc0 >> 10) == 4u ? 0x3f800000u : 0u);
    tab(16, (unsigned)WS_PFL + y * 16384u + (hd * 4u + wid) * 4u, (unsigned)WS_PFL + (y * 32u + hd) * 512u + wid * 128u);
    const char* psig = p.ws + WS_CNT + y * 8192u + hd * 8u;
    const char* pcnt = p.ws + WS_CNT + y * 8192u + hd * 256u;
    tabp(20, p.wr, 0u); tab(21, (unsigned)WS_PART + b * 8192u + wid * 2048u, 0u);
    tabp(25, p.out, 0u); tab(26, 256u * y + 8u * hd + 2u * wid, (y & 1u) * 256u); tab(27, (unsigned)WS_KEYS + b * 8u + wid * 4096u, (unsigned)WS_KEYS + (b & 7u) * 2048u);
    tab(28, (unsigned)WS_PART + ((b & 7u) * 32u + idx) * 32u + wid * 262144u, ((b & 7u) * 32u + idx) * 8u);
    tab(31, (128u * m + 32u * (y >> 1) + 8u * (y & 1u)) * 2048u + 512u * wid, (unsigned)WS_U + (y >> 1) * 4096u + hd * 64u + (y & 1u) * 32u); tab(32, hd * 64u, 0u);
    tab(33, (unsigned)WS_PX + (y * 32u + hd) * 1024u + wid * 256u, (unsigned)WS_PX + y * 32768u + (8u * hd + 2u * wid) * 4u); tab(34, (256u * y + 64u * wid) * 2048u + 64u * hd, (unsigned)WS_PK + b * 8u + wid * 4096u);
    tab(35, (unsigned)WS_PK + (b & 7u) * 2048u, 256u * y + 8u * hd + 2u * wid);
      asm volatile("" : "+v"(tlo), "+v"(thi), "+v"(wo0), "+v"(vg0), "+v"(vdn), "+v"(ta0), "+v"(vxs), "+v"(vx0), "+v"(rd0), "+v"(fl), "+v"(sd0), "+v"(kb0), "+v"(vvb), "+v"(vwb), "+v"(kd0), "+v"(vd0), "+v"(wd0), "+v"(hq0), "+v"(oq0));
#undef lane
      float* par = (float*)(lds + 131072);
      u32x2 mp[8]; u32x4 rs;
      auto look = [&]() __attribute__((always_inline)) {
        #pragma unroll
        for (int k = 0; k < 8; ++k) asm volatile("global_load_dwordx2 %0, %1, %2 offset:%3 sc1" : "=v"(mp[k]) : "v"(((wid >> 1) * 16u + 2u * (hd & 7u) + (wid & 1u)) * 4096u + lane * 8u), "s"(p.ws + WS_MOD), "n"(512 * k) : "memory");
        asm volatile("global_load_dwordx4 %0, %1, %2 sc1" : "=v"(rs) : "v"(((rb0 + wid) * 16u + l16) * 16u), "s"(p.ws + WS_RST) : "memory");
      };
#define LOOKED "+v"(mp[0]), "+v"(mp[1]), "+v"(mp[2]), "+v"(mp[3]), "+v"(mp[4]), "+v"(mp[5]), "+v"(mp[6]), "+v"(mp[7]), "+v"(rs)
      look();
      #pragma unroll
      for (int q = 0; q < 14; ++q)
        asm volatile("s_mov_b32 m0, %0\n\ts_nop 0\n\tbuffer_load_dwordx4 %1, %2, %3 offen lds" :: "s"(m0b + 65536u + 16384u * (q >> 2) + 1024u * (q & 3)), "v"(wd0), "s"(srdw),
                     "s"(0x80000u * ((2u * rho + (q >> 2)) & 15u) + 65536u * ((q >> 1) & 1) + 128u * (q & 1)) : "m0", "memory");
#define KVT 0
#define KV0 0
      #pragma unroll
      for (int j = 0; j < KVT; ++j) asm volatile("buffer_load_dword a255, %0, %1, %2 offen" :: "v"(lane < 32u ? lane * 128u : 0x80000000u), "s"(srdp), "s"((KV0 + wid + 4u * j) * 16384u) : "memory");
      asm volatile("s_waitcnt vmcnt(%0)" :: "n"(27 + KVT) : "memory");
      { const u32x4 cs = XV(4); *(u32x2*)(lds + 131072 + 8192 + tid * 8u) = u32x2{cvtpk(silu(lo16(cs[0])), silu(hi16(cs[0]))), cvtpk(silu(lo16(cs[1])), silu(hi16(cs[1])))}; }
      asm volatile("s_waitcnt vmcnt(%9)" : LOOKED : "n"(14 + KVT) : "memory");
      for (unsigned spins = 0;; ++spins) {
        unsigned bl = (rs[0] ^ tl) | (rs[2] ^ tl), bh = (rs[1] ^ th) | (rs[3] ^ th);
        #pragma unroll
        for (int k = 0; k < 8; ++k) { bl |= mp[k][0] ^ tl; bh |= mp[k][1] ^ th; }
        if (__all(((bl & 0xffu) | bh) == 0u) || spins > SPIN_CAP) break;
        __builtin_amdgcn_s_sleep(ASLEEP);
        look();
        asm volatile("s_waitcnt vmcnt(0)" : LOOKED :: "memory");
      }
      {
        float v = 0.f;
        #pragma unroll
        for (int k = 0; k < 8; ++k) v += __uint_as_float(mp[k][0] & 0xffffff00u);
        v = rbf(v); par[tid] = (tid < 128u ? v : rbf(1.f + v)) * QSCALE;
        if (lane < 16u) *(u32x2*)(par + 256u + 8u * lane + 2u * wid) = u32x2{rs[0] & 0xffffff00u, rs[2] & 0xffffff00u};
      }
      PAGE(6, "adaln_fr");
      asm volatile("s_waitcnt lgkmcnt(0)\n\ts_barrier" ::: "memory");
      asm volatile("buffer_load_dword a255, %0, %1, %2 offen" TNT :: "v"(tq0), "s"(srdw), "s"(0x200000u) : "memory");
      FQ(0);
      {
        const float* pc = par + 8u * (tid >> 4);
        const f32x4 a0 = *(const f32x4*)pc, a1 = *(const f32x4*)(pc + 4), c0 = *(const f32x4*)(pc + 128), c1 = *(const f32x4*)(pc + 132);
        const f32x4 t0 = *(const f32x4*)(par + 256u + 8u * l16), t1 = *(const f32x4*)(par + 260u + 8u * l16);
        #pragma unroll
        for (int i = 0; i < 4; ++i) {
          const float mn = i < 2 ? t0[2 * i] : t1[2 * i - 4], rd = i < 2 ? t0[2 * i + 1] : t1[2 * i - 3];
          u32x4 o = XV(i);
          #pragma unroll
          for (int e = 0; e < 4; ++e) {
            const f32x2 sc = e < 2 ? f32x2{c0[2 * e], c0[2 * e + 1]} : f32x2{c1[2 * e - 4], c1[2 * e - 3]}, sh = e < 2 ? f32x2{a0[2 * e], a0[2 * e + 1]} : f32x2{a1[2 * e - 4], a1[2 * e - 3]};
            o[e] = cvtpk2(__builtin_elementwise_fma(__builtin_elementwise_fma(unpk(o[e]), f32x2{rd, rd}, f32x2{mn, mn}), sc, sh));
          }
          st16_wt(p.ws + WS_H + (rb0 + i) * 32768u + ch * 256u + 16u * l16, o);
        }
      }
      FQ(1);
    asm volatile(FA_BODY
      : FA_OUTS
      : [m0b] "s"(m0b), [flag] "s"(flag), [srdz] "s"(srdz), [srdw] "s"(srdw), [srdk] "s"(srdk), [srdv] "s"(srdv), [m0t] "s"(m0t), [srdt] "s"(srdt), [srdp] "s"(srdp),
        [tag] "s"(tag), [pcnt] "s"(pcnt), [psig] "s"(psig), [wid] "s"(wid), [px] "s"(p.x), [pct] "s"(pct), [sin] "s"(sin), [tlo] "v"(tlo), [thi] "v"(thi), [wbytes] "s"(WBYTES)
      : FA_CLOBBERS);
  }
  if (b == 255u && wid == 0u) __hip_atomic_store((gu64*)(p.ws + WS_EP), tag, RLX_AGENT);
  asm volatile("adaln_text_end:");
}
}

extern "C" void kernel_launch(void* const* d_in, const int* in_sizes, int n_in, void* d_out, int out_size, void* d_ws, size_t ws_size, hipStream_t stream) {
  (void)in_sizes; (void)n_in; (void)out_size;
  if (ws_size < WS_END) return;
  const u16* x    = (const u16*)d_in[0];
  const u16* kc   = (const u16*)d_in[1];
  const u16* vc   = (const u16*)d_in[2];
  const u16* wq   = (const u16*)d_in[3];
  const u16* wo   = (const u16*)d_in[4];
  const u16* wr   = (const u16*)d_in[6];
  const u16* wg   = (const u16*)d_in[7];
  const u16* wu   = (const u16*)d_in[8];
  const u16* wd   = (const u16*)d_in[9];
  const u16* c    = (const u16*)d_in[10];
  const u16* wmod = (const u16*)d_in[11];
  u16* out = (u16*)d_out;
  char* ws = (char*)d_ws;
  fa::Args fr;
  fr.x = x; fr.wmod = wmod; fr.wq = wq; fr.kc = kc; fr.vc = vc; fr.wo = wo; fr.wg = wg; fr.wu = wu; fr.wd = wd; fr.ws = ws; fr.out = out; fr.c = c; fr.wr = wr;
  hipFuncSetAttribute((const void*)fa::fattn_k, hipFuncAttributeMaxDynamicSharedMemorySize, (int)fa::LDS_BYTES);
  hipLaunchKernelGGL(fa::fattn_k, dim3(256), dim3(256), fa::LDS_BYTES, stream, fr);
}
```

```cpp
#include <hip/hip_runtime.h>
#include <hip/hip_bf16.h>
#include <utility>
#include <cstdint>

#pragma clang fp contract(off)
typedef float    f32x16 __attribute__((ext_vector_type(16)));
typedef float    f32x2  __attribute__((ext_vector_type(2)));
typedef float    f32x4  __attribute__((ext_vector_type(4)));
typedef __bf16   bf16x8 __attribute__((ext_vector_type(8)));
typedef __bf16   bf16x4 __attribute__((ext_vector_type(4)));
typedef __bf16   bf16x2 __attribute__((ext_vector_type(2)));
typedef unsigned u32x4  __attribute__((ext_vector_type(4)));
typedef unsigned u32x2  __attribute__((ext_vector_type(2)));
typedef short    s16x4  __attribute__((ext_vector_type(4)));
typedef unsigned short u16;
template <int... Is> using iseq = std::integer_sequence<int, Is...>;
template <int N> using make_iseq = std::make_integer_sequence<int, N>;
#define DI __device__ __forceinline__
#define TAILB
#define PACE2
#define SELB

constexpr int SEQ = 2048, DM = 1024, NHEAD = 32, DHEAD = 128, DQ = NHEAD * DHEAD, DFF = 3584, NMOD = 6 * DM;
constexpr size_t WS_EP    = 0;
constexpr size_t WS_RST   = 32768;
constexpr size_t WS_CNT   = 65536;
constexpr size_t WS_MOD   = 131072;
constexpr size_t WS_Z     = 262144;
constexpr size_t WS_MODT  = WS_Z;
constexpr size_t WS_XFL   = WS_Z + 16384;
#define KEY_REP 8u
constexpr size_t WS_KEYS  = WS_Z + 32768;
constexpr unsigned Z_SHARE = 2560;
static_assert(KEY_REP <= 8 && Z_SHARE * 256 == 655360, "zeroed area");
constexpr size_t WS_MD    = WS_Z + 49152;
constexpr size_t WS_PFL   = WS_Z + 65536;
constexpr size_t WS_OFL   = WS_Z + 196608;
constexpr size_t WS_PK    = WS_Z + 327680;
constexpr size_t WS_U     = WS_Z + 344064;
constexpr size_t WS_PX    = WS_Z + 393216;
constexpr size_t WS_H     = 1048576;
constexpr size_t WS_PART  = WS_H  + (size_t)SEQ * DM * 2;
constexpr size_t WS_XIN   = WS_PART + (size_t)256 * 2 * DM * 4;
#define BOXV 38016
constexpr unsigned BOX    = BOXV;
constexpr size_t WS_END   = WS_XIN + (size_t)BOX * (SEQ / 2);

DI float bf2f(u16 b) { return __uint_as_float((unsigned)b << 16); }
DI float rbf(float x) { return (float)(__bf16)x; }
DI u16   f2bf(float x) { return __builtin_bit_cast(u16, (__bf16)x); }
DI float lo16(unsigned w) { return __uint_as_float(w << 16); }
DI float hi16(unsigned w) { return __uint_as_float(w & 0xffff0000u); }
DI unsigned cvtpk(float lo, float hi) {
  f32x2 v = {lo, hi}; bf16x2 b = __builtin_convertvector(v, bf16x2); return __builtin_bit_cast(unsigned, b);
}
template <int CTRL> DI float dpp_add(float v) { return v + __uint_as_float(__builtin_amdgcn_update_dpp(0u, __float_as_uint(v), CTRL, 0xf, 0xf, true)); }
DI float swap32_add(float a, float b) {
  auto r = __builtin_amdgcn_permlane32_swap(__float_as_uint(a), __float_as_uint(b), false, false); return __uint_as_float(r[0]) + __uint_as_float(r[1]);
}
DI float swap16_add(float a, float b) {
  auto r = __builtin_amdgcn_permlane16_swap(__float_as_uint(a), __float_as_uint(b), false, false); return __uint_as_float(r[0]) + __uint_as_float(r[1]);
}
constexpr int DPP_XOR1 = 0xB1, DPP_XOR2 = 0x4E, DPP_ROR4 = 0x124, DPP_ROR8 = 0x128;
DI float wave_sum(float v) {
  v = dpp_add<DPP_XOR1>(v); v = dpp_add<DPP_XOR2>(v); v = dpp_add<DPP_ROR4>(v); v = dpp_add<DPP_ROR8>(v);
  v = swap16_add(v, v); return swap32_add(v, v);
}

DI void st16_wt(void* p, u32x4 v) { asm volatile("global_store_dwordx4 %0, %1, off sc1\n\ts_nop 1" :: "v"(p), "v"(v) : "memory"); }

DI f32x2 unpk(unsigned w) { return f32x2{lo16(w), hi16(w)}; }
DI unsigned cvtpk2(f32x2 v) { return __builtin_bit_cast(unsigned, __builtin_convertvector(v, bf16x2)); }
DI f32x2 rb2(f32x2 v) { return f32x2{__uint_as_float(cvtpk(0.f, v[0])), __uint_as_float(cvtpk(0.f, v[1]))}; }
DI unsigned ln_pair(float x0, float x1, float m, float rden, float c0, float c1, float s0, float s1) {
  const unsigned d = cvtpk2(f32x2{x0, x1} - m), l = cvtpk2(unpk(d) * rden), t = cvtpk2(unpk(l) * f32x2{c0, c1});
  return cvtpk2(unpk(t) + f32x2{s0, s1});
}
#define GAS __attribute__((address_space(1)))
#define LAS __attribute__((address_space(3)))
typedef GAS unsigned long long gu64;
typedef short v4i16_t __attribute__((ext_vector_type(4)));
DI bf16x4 lds_tr(unsigned a) { return __builtin_bit_cast(bf16x4, __builtin_amdgcn_ds_read_tr16_b64_v4i16((LAS v4i16_t*)a)); }
#define RLX_AGENT __ATOMIC_RELAXED, __HIP_MEMORY_SCOPE_AGENT
constexpr unsigned SPIN_CAP = 1u << 16;

static_assert(BOX == 38016, "box size");
#define NP_FILL 8
#define GOT_WAIT "8"
#define NORM 2
#define UNITP 1
#define INS 0
#define XP2 0
#define FA_CLOBBERS "v0", "v1", "v2", "v3", "v4", "v5", "v6", "v7", "v8", "v9", "v10", "v11", "v12", "v13", "v14", "v15", "v16", "v17", "v18", "v19", "v20", "v21", "v22", "v23", "v24", "v25", "v26", "v27", "v28", "v29", "v30", "v31", "v32", "v33", "v34", "v35", "v36", "v37", "v38", "v39", "v40", "v41", "v42", "v43", "v44", "v45", "v46", "v47", "v48", "v49", "v50", "v51", "v52", "v53", "v54", "v55", "v56", "v57", "v58", "v59", "v60", "v61", "v62", "v63", "v64", "v65", "v66", "v67", "v68", "v69", "v70", "v71", "v72", "v73", "v74", "v75", "v76", "v77", "v78", "v79", "v80", "v81", "v82", "v83", "v84", "v85", "v86", "v87", "v88", "v89", "v90", "v91", "v92", "v93", "v94", "v95", "v96", "v97", "v98", "v99", "v100", "v101", "v102", "v103", "v104", "v105", "v106", "v107", "v108", "v109", "v110", "v111", "v112", "v113", "v114", "v115", "v116", "v117", "v118", "v119", "v120", "v121", "v122", "v123", "v124", "v125", "v126", "v127", "v128", "v129", "v130", "v131", "v132", "v133", "v134", "v135", "v136", "v137", "v138", "v139", "v140", "v141", "v142", "v143", "v144", "v145", "v146", "v147", "v148", "v149", "v150", "v151", "v152", "v153", "v154", "v155", "v156", "v157", "v158", "v159", "v160", "v161", "v162", "v163", "v164", "v165", "v166", "v167", "v168", "v169", "v170", "v171", "v172", "v173", "v174", "v175", "v176", "v177", "v178", "v179", "v180", "v181", "v182", "v183", "v184", "v185", "v186", "v187", "v188", "v189", "v190", "v191", "v192", "v193", "v194", "v195", "v196", "v197", "v198", "v199", "v200", "v201", "v202", "v203", "v204", "v205", "v206", "v207", "v208", "v209", "v210", "v211", "v212", "v213", "v214", "v215", "v216", "v217", "v218", "v219", "v220", "v221", "v222", "v223", "v224", "v230", "v231", "v232", "v233", "v234", "v235", "v237", "a0", "a1", "a2", "a3", "a4", "a5", "a6", "a7", "a8", "a9", "a10", "a11", "a12", "a13", "a14", "a15", "a16", "a17", "a18", "a19", "a20", "a21", "a22", "a23", "a24", "a25", "a26", "a27", "a28", "a29", "a30", "a31", "a32", "a33", "a34", "a35", "a36", "a37", "a38", "a39", "a40", "a41", "a42", "a43", "a44", "a45", "a46", "a47", "a48", "a49", "a50", "a51", "a52", "a53", "a54", "a55", "a56", "a57", "a58", "a59", "a60", "a61", "a62", "a63", "a64", "a65", "a66", "a67", "a68", "a69", "a70", "a71", "a72", "a73", "a74", "a75", "a76", "a77", "a78", "a79", "a80", "a81", "a82", "a83", "a84", "a85", "a86", "a87", "a88", "a89", "a90", "a91", "a92", "a93", "a94", "a95", "a96", "a97", "a98", "a99", "a100", "a101", "a102", "a103", "a104", "a105", "a106", "a107", "a108", "a109", "a110", "a111", "a112", "a113", "a114", "a115", "a116", "a117", "a118", "a119", "a120", "a121", "a122", "a123", "a124", "a125", "a126", "a127", "a128", "a129", "a130", "a131", "a132", "a133", "a134", "a135", "a136", "a137", "a138", "a139", "a140", "a141", "a142", "a143", "a144", "a145", "a146", "a147", "a148", "a149", "a150", "a151", "a152", "a153", "a154", "a155", "a156", "a157", "a158", "a159", "a160", "a161", "a162", "a163", "a164", "a165", "a166", "a167", "a168", "a169", "a170", "a171", "a172", "a173", "a174", "a175", "a176", "a177", "a178", "a179", "a180", "a181", "a182", "a183", "a184", "a185", "a186", "a187", "a188", "a189", "a190", "a191", "a192", "a193", "a194", "a195", "a196", "a197", "a198", "a199", "a200", "a201", "a202", "a203", "a204", "a205", "a206", "a207", "a208", "a209", "a210", "a211", "a212", "a213", "a214", "a215", "a216", "a217", "a218", "a219", "a220", "a221", "a222", "a223", "a224", "a225", "a226", "a227", "a228", "a229", "a230", "a231", "a232", "a233", "a234", "a235", "a236", "a237", "a238", "a239", "a240", "a241", "a242", "a243", "a244", "a245", "a246", "a247", "a248", "a249", "a250", "a251", "a252", "a253", "a254", "a255", "s40", "s41", "s42", "s43", "s44", "s45", "s46", "s47", "s48", "s49", "s50", "s51", "s52", "s53", "s54", "s55", "s56", "s57", "s58", "s59", "s60", "s61", "s62", "s63", "s64", "s65", "s66", "s67", "s68", "s69", "vcc", "m0", "scc", "memory"
#define EARLY2 0
#define SHW 1
#define QROT 0
#define SKV 0
#define KVPAR 0
#define QT2 0
#define QSTAG 0
#define ATAIL 1
static_assert(WS_MODT == 262144 && WS_XFL == 278528 && WS_U == 606208 && WS_XIN == 7340032, "workspace layout");
#define FA_OUTS [got] "=&s"(got), [k1] "=&s"(k1), [k2] "=&s"(k2), [lat] "=&s"(lat), "+{v229}"(fl), "+{v225}"(vwb), "+{v226}"(wd0), "+{v227}"(hq0), "+{v228}"(sd0), "+{v236}"(tq0), [kb0] "+v"(kb0), [vvb] "+v"(vvb), [kd0] "+v"(kd0), [vd0] "+v"(vd0), [oq0] "+v"(oq0), [rd0] "+v"(rd0), [wo0] "+v"(wo0), [vg0] "+v"(vg0), [vx0] "+v"(vx0), [vdn] "+v"(vdn), [ta0] "+v"(ta0), [vxs] "+v"(vxs), [vct] "+v"(vct)
#define FA_BODY \
"s_branch adaln_body\n" \
".p2align 12\n" \
"adaln_pg7:\n" \
"adaln_body:\n" \
"s_cbranch_execz adaln_fr\n" \
"s_nop 0\n" \
"s_mov_b32 s55, 0x80000\n" \
"v_add_u32_e32 v219, 0x8000, v227\n" \
"v_add_u32_e32 v220, 0x10000, v227\n" \
"v_add_u32_e32 v221, 0x18000, v227\n" \
"s_mov_b32 s50, 0\n" \
"s_movk_i32 s53, 0x1000\n" \
"s_mov_b32 s52, 0\n" \
"s_nop 0\n" \
"s_mov_b32 s49, 1572864\n" \
"v_add_u32_e32 v216, 0xfffffc80, v226\n" \
"v_add_u32_e32 v217, 0xf800, v226\n" \
"v_add_u32_e32 v218, 0xf480, v226\n" \
"s_add_u32 s40, %[m0b], 0\n" \
"s_nop 0\n" \
"s_add_u32 s44, %[m0b], 65536\n" \
"s_add_u32 s41, %[m0b], 16384\n" \
"s_add_u32 s45, %[m0b], 81920\n" \
"s_add_u32 s42, %[m0b], 32768\n" \
"s_add_u32 s46, %[m0b], 98304\n" \
"s_add_u32 s43, %[m0b], 49152\n" \
"s_add_u32 s47, %[m0b], 114688\n" \
"v_xor_b32_e64 v208, 64, %[kb0]\n" \
"v_xor_b32_e32 v209, 0x80, %[kb0]\n" \
"v_xor_b32_e32 v210, 0xc0, %[kb0]\n" \
"v_xor_b32_e64 v212, 64, %[kd0]\n" \
"v_xor_b32_e32 v213, 0x80, %[kd0]\n" \
"v_xor_b32_e32 v214, 0xc0, %[kd0]\n" \
"v_xor_b32_e64 v211, 32, %[vvb]\n" \
"v_add_u32_e32 v215, 0xfffffc80, %[vd0]\n" \
"v_readlane_b32 s64, %[tlo], 17\n" \
"v_readlane_b32 s65, %[thi], 17\n" \
"s_mov_b32 s66, 12582912\n" \
"s_mov_b32 s67, 0x20000\n" \
"v_mov_b64_e32 v[0:1], 0\n" \
"v_mov_b64_e32 v[2:3], 0\n" \
"v_mov_b64_e32 v[4:5], 0\n" \
"v_mov_b64_e32 v[6:7], 0\n" \
"v_mov_b64_e32 v[8:9], 0\n" \
"v_mov_b64_e32 v[10:11], 0\n" \
"v_mov_b64_e32 v[12:13], 0\n" \
"v_mov_b64_e32 v[14:15], 0\n" \
"v_mov_b64_e32 v[16:17], 0\n" \
"v_mov_b64_e32 v[18:19], 0\n" \
"v_mov_b64_e32 v[20:21], 0\n" \
"v_mov_b64_e32 v[22:23], 0\n" \
"v_mov_b64_e32 v[24:25], 0\n" \
"v_mov_b64_e32 v[26:27], 0\n" \
"v_mov_b64_e32 v[28:29], 0\n" \
"v_mov_b64_e32 v[30:31], 0\n" \
"v_mov_b64_e32 v[32:33], 0\n" \
"v_mov_b64_e32 v[34:35], 0\n" \
"v_mov_b64_e32 v[36:37], 0\n" \
"v_mov_b64_e32 v[38:39], 0\n" \
"v_mov_b64_e32 v[40:41], 0\n" \
"v_mov_b64_e32 v[42:43], 0\n" \
"v_mov_b64_e32 v[44:45], 0\n" \
"v_mov_b64_e32 v[46:47], 0\n" \
"v_mov_b64_e32 v[48:49], 0\n" \
"v_mov_b64_e32 v[50:51], 0\n" \
"v_mov_b64_e32 v[52:53], 0\n" \
"v_mov_b64_e32 v[54:55], 0\n" \
"v_mov_b64_e32 v[56:57], 0\n" \
"v_mov_b64_e32 v[58:59], 0\n" \
"v_mov_b64_e32 v[60:61], 0\n" \
"v_mov_b64_e32 v[62:63], 0\n" \
"v_mov_b64_e32 v[64:65], 0\n" \
"v_mov_b64_e32 v[66:67], 0\n" \
"v_mov_b64_e32 v[68:69], 0\n" \
"v_mov_b64_e32 v[70:71], 0\n" \
"v_mov_b64_e32 v[72:73], 0\n" \
"v_mov_b64_e32 v[74:75], 0\n" \
"v_mov_b64_e32 v[76:77], 0\n" \
"v_mov_b64_e32 v[78:79], 0\n" \
"v_mov_b64_e32 v[80:81], 0\n" \
"v_mov_b64_e32 v[82:83], 0\n" \
"v_mov_b64_e32 v[84:85], 0\n" \
"v_mov_b64_e32 v[86:87], 0\n" \
"v_mov_b64_e32 v[88:89], 0\n" \
"v_mov_b64_e32 v[90:91], 0\n" \
"v_mov_b64_e32 v[92:93], 0\n" \
"v_mov_b64_e32 v[94:95], 0\n" \
"v_mov_b64_e32 v[96:97], 0\n" \
"v_mov_b64_e32 v[98:99], 0\n" \
"v_mov_b64_e32 v[100:101], 0\n" \
"v_mov_b64_e32 v[102:103], 0\n" \
"v_mov_b64_e32 v[104:105], 0\n" \
"v_mov_b64_e32 v[106:107], 0\n" \
"v_mov_b64_e32 v[108:109], 0\n" \
"v_mov_b64_e32 v[110:111], 0\n" \
"v_mov_b64_e32 v[112:113], 0\n" \
"v_mov_b64_e32 v[114:115], 0\n" \
"v_mov_b64_e32 v[116:117], 0\n" \
"v_mov_b64_e32 v[118:119], 0\n" \
"v_mov_b64_e32 v[120:121], 0\n" \
"v_mov_b64_e32 v[122:123], 0\n" \
"v_mov_b64_e32 v[124:125], 0\n" \
"v_mov_b64_e32 v[126:127], 0\n" \
"s_mov_b32 s51, 0x10000\n" \
"v_mbcnt_lo_u32_b32 v232, -1, 0\n" \
"v_mov_b64_e32 v[234:235], %[tag]\n" \
"v_lshlrev_b32_e32 v233, 3, v232\n" \
"v_lshlrev_b32_e32 v232, 8, v232\n" \
"s_waitcnt vmcnt(0)\n" \
"s_barrier\n" \
"s_cmp_lg_u32 %[wid], 0\n" \
"s_cbranch_scc1 Lwalk_%=\n" \
"s_mov_b32 exec_hi, 0\n" \
"global_store_dwordx2 v232, v[234:235], %[psig] sc1\n" \
"s_mov_b32 exec_hi, -1\n" \
"Lgo_%=:\n" \
"s_nop 0\n" \
"ds_read_b64_tr_b16 v[128:129], v225 offset:0\n" \
"ds_read_b64_tr_b16 v[130:131], v225 offset:256\n" \
"ds_read_b64_tr_b16 v[132:133], v225 offset:8192\n" \
"ds_read_b64_tr_b16 v[134:135], v225 offset:8448\n" \
"ds_read_b64_tr_b16 v[136:137], v225 offset:8\n" \
"ds_read_b64_tr_b16 v[138:139], v225 offset:264\n" \
"ds_read_b64_tr_b16 v[140:141], v225 offset:8200\n" \
"ds_read_b64_tr_b16 v[142:143], v225 offset:8456\n" \
"ds_read_b64_tr_b16 v[144:145], v225 offset:512\n" \
"ds_read_b64_tr_b16 v[146:147], v225 offset:768\n" \
"ds_read_b64_tr_b16 v[148:149], v225 offset:8704\n" \
"ds_read_b64_tr_b16 v[150:151], v225 offset:8960\n" \
"ds_read_b64_tr_b16 v[152:153], v225 offset:520\n" \
"ds_read_b64_tr_b16 v[154:155], v225 offset:776\n" \
"ds_read_b64_tr_b16 v[156:157], v225 offset:8712\n" \
"ds_read_b64_tr_b16 v[158:159], v225 offset:8968\n" \
"s_mov_b32 s60, 4194304\n" \
"v_and_b32_e32 v233, 56, v233\n" \
"s_lshl_b32 s61, %[wid], 6\n" \
"v_add_u32_e64 v233, s61, v233\n" \
"Lpoll_%=:\n" \
"global_load_dwordx2 v[230:231], v233, %[pcnt] sc1\n" \
"s_waitcnt vmcnt(0)\n" \
"v_cmp_ne_u64_e32 vcc, v[230:231], v[234:235]\n" \
"s_sub_u32 s51, s51, 1\n" \
"s_cbranch_scc1 Lpd_%=\n" \
"s_nop 3\n" \
"s_cbranch_vccnz Lpoll_%=\n" \
"Lpd_%=:\n" \
"buffer_load_dwordx4 a[128:131], v227, %[srdz], s50 offen offset:0 sc1\n" \
"buffer_load_dwordx4 a[132:135], v219, %[srdz], s50 offen offset:0 sc1\n" \
"buffer_load_dwordx4 a[136:139], v220, %[srdz], s50 offen offset:0 sc1\n" \
"buffer_load_dwordx4 a[140:143], v221, %[srdz], s50 offen offset:0 sc1\n" \
"buffer_load_dwordx4 a[144:147], v227, %[srdz], s50 offen offset:1024 sc1\n" \
"buffer_load_dwordx4 a[148:151], v219, %[srdz], s50 offen offset:1024 sc1\n" \
"buffer_load_dwordx4 a[152:155], v220, %[srdz], s50 offen offset:1024 sc1\n" \
"buffer_load_dwordx4 a[156:159], v221, %[srdz], s50 offen offset:1024 sc1\n" \
"buffer_load_dwordx4 a[160:163], v227, %[srdz], s50 offen offset:2048 sc1\n" \
"buffer_load_dwordx4 a[164:167], v219, %[srdz], s50 offen offset:2048 sc1\n" \
"buffer_load_dwordx4 a[168:171], v220, %[srdz], s50 offen offset:2048 sc1\n" \
"buffer_load_dwordx4 a[172:175], v221, %[srdz], s50 offen offset:2048 sc1\n" \
"buffer_load_dwordx4 a[176:179], v227, %[srdz], s50 offen offset:3072 sc1\n" \
"buffer_load_dwordx4 a[180:183], v219, %[srdz], s50 offen offset:3072 sc1\n" \
"buffer_load_dwordx4 a[184:187], v220, %[srdz], s50 offen offset:3072 sc1\n" \
"buffer_load_dwordx4 a[188:191], v221, %[srdz], s50 offen offset:3072 sc1\n" \
"s_add_u32 s50, s50, s53\n" \
"s_nop 0\n" \
"buffer_load_dwordx4 a[192:195], v227, %[srdz], s50 offen offset:0 sc1\n" \
"buffer_load_dwordx4 a[196:199], v219, %[srdz], s50 offen offset:0 sc1\n" \
"buffer_load_dwordx4 a[200:203], v220, %[srdz], s50 offen offset:0 sc1\n" \
"buffer_load_dwordx4 a[204:207], v221, %[srdz], s50 offen offset:0 sc1\n" \
"buffer_load_dwordx4 a[208:211], v227, %[srdz], s50 offen offset:1024 sc1\n" \
"buffer_load_dwordx4 a[212:215], v219, %[srdz], s50 offen offset:1024 sc1\n" \
"buffer_load_dwordx4 a[216:219], v220, %[srdz], s50 offen offset:1024 sc1\n" \
"buffer_load_dwordx4 a[220:223], v221, %[srdz], s50 offen offset:1024 sc1\n" \
"s_mov_b32 s51, 3\n" \
"s_waitcnt vmcnt(16) lgkmcnt(0)\n" \
"Lq_%=:\n" \
"v_mfma_f32_16x16x32_bf16 v[0:3], v[128:131], a[128:131], v[0:3]\n" \
"ds_read_b64_tr_b16 v[160:161], v225 offset:1024\n" \
"v_mfma_f32_16x16x32_bf16 v[0:3], v[132:135], a[144:147], v[0:3]\n" \
"ds_read_b64_tr_b16 v[162:163], v225 offset:1280\n" \
"v_mfma_f32_16x16x32_bf16 v[32:35], v[128:131], a[132:135], v[32:35]\n" \
"s_mov_b32 m0, s47\n" \
"s_nop 0\n" \
"buffer_load_dwordx4 v217, %[srdw], s49 offen offset:2048 lds\n" \
"v_mfma_f32_16x16x32_bf16 v[32:35], v[132:135], a[148:151], v[32:35]\n" \
"ds_read_b64_tr_b16 v[164:165], v225 offset:9216\n" \
"v_mfma_f32_16x16x32_bf16 v[64:67], v[128:131], a[136:139], v[64:67]\n" \
"ds_read_b64_tr_b16 v[166:167], v225 offset:9472\n" \
"v_mfma_f32_16x16x32_bf16 v[64:67], v[132:135], a[152:155], v[64:67]\n" \
"v_mfma_f32_16x16x32_bf16 v[96:99], v[128:131], a[140:143], v[96:99]\n" \
"ds_read_b64_tr_b16 v[168:169], v225 offset:1032\n" \
"v_mfma_f32_16x16x32_bf16 v[96:99], v[132:135], a[156:159], v[96:99]\n" \
"ds_read_b64_tr_b16 v[170:171], v225 offset:1288\n" \
"v_mfma_f32_16x16x32_bf16 v[4:7], v[136:139], a[128:131], v[4:7]\n" \
"buffer_load_dwordx4 v218, %[srdw], s49 offen offset:3072 lds\n" \
"v_mfma_f32_16x16x32_bf16 v[4:7], v[140:143], a[144:147], v[4:7]\n" \
"ds_read_b64_tr_b16 v[172:173], v225 offset:9224\n" \
"v_mfma_f32_16x16x32_bf16 v[36:39], v[136:139], a[132:135], v[36:39]\n" \
"ds_read_b64_tr_b16 v[174:175], v225 offset:9480\n" \
"v_mfma_f32_16x16x32_bf16 v[36:39], v[140:143], a[148:151], v[36:39]\n" \
"v_mfma_f32_16x16x32_bf16 v[68:71], v[136:139], a[136:139], v[68:71]\n" \
"ds_read_b64_tr_b16 v[176:177], v225 offset:1536\n" \
"v_mfma_f32_16x16x32_bf16 v[68:71], v[140:143], a[152:155], v[68:71]\n" \
"ds_read_b64_tr_b16 v[178:179], v225 offset:1792\n" \
"v_mfma_f32_16x16x32_bf16 v[100:103], v[136:139], a[140:143], v[100:103]\n" \
"buffer_load_dwordx4 a[224:227], v227, %[srdz], s50 offen offset:2048 sc1\n" \
"v_mfma_f32_16x16x32_bf16 v[100:103], v[140:143], a[156:159], v[100:103]\n" \
"ds_read_b64_tr_b16 v[180:181], v225 offset:9728\n" \
"v_mfma_f32_16x16x32_bf16 v[8:11], v[144:147], a[128:131], v[8:11]\n" \
"ds_read_b64_tr_b16 v[182:183], v225 offset:9984\n" \
"v_mfma_f32_16x16x32_bf16 v[8:11], v[148:151], a[144:147], v[8:11]\n" \
"v_mfma_f32_16x16x32_bf16 v[40:43], v[144:147], a[132:135], v[40:43]\n" \
"ds_read_b64_tr_b16 v[184:185], v225 offset:1544\n" \
"v_mfma_f32_16x16x32_bf16 v[40:43], v[148:151], a[148:151], v[40:43]\n" \
"ds_read_b64_tr_b16 v[186:187], v225 offset:1800\n" \
"v_mfma_f32_16x16x32_bf16 v[72:75], v[144:147], a[136:139], v[72:75]\n" \
"buffer_load_dwordx4 a[228:231], v219, %[srdz], s50 offen offset:2048 sc1\n" \
"v_mfma_f32_16x16x32_bf16 v[72:75], v[148:151], a[152:155], v[72:75]\n" \
"ds_read_b64_tr_b16 v[188:189], v225 offset:9736\n" \
"v_mfma_f32_16x16x32_bf16 v[104:107], v[144:147], a[140:143], v[104:107]\n" \
"ds_read_b64_tr_b16 v[190:191], v225 offset:9992\n" \
"v_mfma_f32_16x16x32_bf16 v[104:107], v[148:151], a[156:159], v[104:107]\n" \
"v_mfma_f32_16x16x32_bf16 v[12:15], v[152:155], a[128:131], v[12:15]\n" \
"buffer_load_dwordx4 a[232:235], v220, %[srdz], s50 offen offset:2048 sc1\n" \
"v_mfma_f32_16x16x32_bf16 v[12:15], v[156:159], a[144:147], v[12:15]\n" \
"v_mfma_f32_16x16x32_bf16 v[44:47], v[152:155], a[132:135], v[44:47]\n" \
"v_mfma_f32_16x16x32_bf16 v[44:47], v[156:159], a[148:151], v[44:47]\n" \
"v_mfma_f32_16x16x32_bf16 v[76:79], v[152:155], a[136:139], v[76:79]\n" \
"buffer_load_dwordx4 a[236:239], v221, %[srdz], s50 offen offset:2048 sc1\n" \
"v_mfma_f32_16x16x32_bf16 v[76:79], v[156:159], a[152:155], v[76:79]\n" \
"v_mfma_f32_16x16x32_bf16 v[108:111], v[152:155], a[140:143], v[108:111]\n" \
"v_mfma_f32_16x16x32_bf16 v[108:111], v[156:159], a[156:159], v[108:111]\n" \
"s_waitcnt vmcnt(29) lgkmcnt(0)\n" \
"s_barrier\n" \
"v_mfma_f32_16x16x32_bf16 v[16:19], v[160:163], a[128:131], v[16:19]\n" \
"ds_read_b64_tr_b16 v[128:129], v225 offset:16384\n" \
"v_mfma_f32_16x16x32_bf16 v[16:19], v[164:167], a[144:147], v[16:19]\n" \
"ds_read_b64_tr_b16 v[130:131], v225 offset:16640\n" \
"v_mfma_f32_16x16x32_bf16 v[48:51], v[160:163], a[132:135], v[48:51]\n" \
"s_add_u32 s49, s49, s55\n" \
"s_mov_b32 m0, s44\n" \
"s_nop 0\n" \
"buffer_load_dwordx4 v226, %[srdw], s49 offen offset:0 lds\n" \
"v_mfma_f32_16x16x32_bf16 v[48:51], v[164:167], a[148:151], v[48:51]\n" \
"ds_read_b64_tr_b16 v[132:133], v225 offset:24576\n" \
"v_mfma_f32_16x16x32_bf16 v[80:83], v[160:163], a[136:139], v[80:83]\n" \
"ds_read_b64_tr_b16 v[134:135], v225 offset:24832\n" \
"v_mfma_f32_16x16x32_bf16 v[80:83], v[164:167], a[152:155], v[80:83]\n" \
"v_mfma_f32_16x16x32_bf16 v[112:115], v[160:163], a[140:143], v[112:115]\n" \
"ds_read_b64_tr_b16 v[136:137], v225 offset:16392\n" \
"v_mfma_f32_16x16x32_bf16 v[112:115], v[164:167], a[156:159], v[112:115]\n" \
"ds_read_b64_tr_b16 v[138:139], v225 offset:16648\n" \
"v_mfma_f32_16x16x32_bf16 v[20:23], v[168:171], a[128:131], v[20:23]\n" \
"buffer_load_dwordx4 v216, %[srdw], s49 offen offset:1024 lds\n" \
"v_mfma_f32_16x16x32_bf16 v[20:23], v[172:175], a[144:147], v[20:23]\n" \
"ds_read_b64_tr_b16 v[140:141], v225 offset:24584\n" \
"v_mfma_f32_16x16x32_bf16 v[52:55], v[168:171], a[132:135], v[52:55]\n" \
"ds_read_b64_tr_b16 v[142:143], v225 offset:24840\n" \
"v_mfma_f32_16x16x32_bf16 v[52:55], v[172:175], a[148:151], v[52:55]\n" \
"v_mfma_f32_16x16x32_bf16 v[84:87], v[168:171], a[136:139], v[84:87]\n" \
"ds_read_b64_tr_b16 v[144:145], v225 offset:16896\n" \
"v_mfma_f32_16x16x32_bf16 v[84:87], v[172:175], a[152:155], v[84:87]\n" \
"ds_read_b64_tr_b16 v[146:147], v225 offset:17152\n" \
"v_mfma_f32_16x16x32_bf16 v[116:119], v[168:171], a[140:143], v[116:119]\n" \
"buffer_load_dwordx4 a[240:243], v227, %[srdz], s50 offen offset:3072 sc1\n" \
"v_mfma_f32_16x16x32_bf16 v[116:119], v[172:175], a[156:159], v[116:119]\n" \
"ds_read_b64_tr_b16 v[148:149], v225 offset:25088\n" \
"v_mfma_f32_16x16x32_bf16 v[24:27], v[176:179], a[128:131], v[24:27]\n" \
"ds_read_b64_tr_b16 v[150:151], v225 offset:25344\n" \
"v_mfma_f32_16x16x32_bf16 v[24:27], v[180:183], a[144:147], v[24:27]\n" \
"v_mfma_f32_16x16x32_bf16 v[56:59], v[176:179], a[132:135], v[56:59]\n" \
"ds_read_b64_tr_b16 v[152:153], v225 offset:16904\n" \
"v_mfma_f32_16x16x32_bf16 v[56:59], v[180:183], a[148:151], v[56:59]\n" \
"ds_read_b64_tr_b16 v[154:155], v225 offset:17160\n" \
"v_mfma_f32_16x16x32_bf16 v[88:91], v[176:179], a[136:139], v[88:91]\n" \
"buffer_load_dwordx4 a[244:247], v219, %[srdz], s50 offen offset:3072 sc1\n" \
"v_mfma_f32_16x16x32_bf16 v[88:91], v[180:183], a[152:155], v[88:91]\n" \
"ds_read_b64_tr_b16 v[156:157], v225 offset:25096\n" \
"v_mfma_f32_16x16x32_bf16 v[120:123], v[176:179], a[140:143], v[120:123]\n" \
"ds_read_b64_tr_b16 v[158:159], v225 offset:25352\n" \
"v_mfma_f32_16x16x32_bf16 v[120:123], v[180:183], a[156:159], v[120:123]\n" \
"v_mfma_f32_16x16x32_bf16 v[28:31], v[184:187], a[128:131], v[28:31]\n" \
"buffer_load_dwordx4 a[248:251], v220, %[srdz], s50 offen offset:3072 sc1\n" \
"v_mfma_f32_16x16x32_bf16 v[28:31], v[188:191], a[144:147], v[28:31]\n" \
"v_mfma_f32_16x16x32_bf16 v[60:63], v[184:187], a[132:135], v[60:63]\n" \
"v_mfma_f32_16x16x32_bf16 v[60:63], v[188:191], a[148:151], v[60:63]\n" \
"v_mfma_f32_16x16x32_bf16 v[92:95], v[184:187], a[136:139], v[92:95]\n" \
"buffer_load_dwordx4 a[252:255], v221, %[srdz], s50 offen offset:3072 sc1\n" \
"v_mfma_f32_16x16x32_bf16 v[92:95], v[188:191], a[152:155], v[92:95]\n" \
"v_mfma_f32_16x16x32_bf16 v[124:127], v[184:187], a[140:143], v[124:127]\n" \
"v_mfma_f32_16x16x32_bf16 v[124:127], v[188:191], a[156:159], v[124:127]\n" \
"s_waitcnt vmcnt(20) lgkmcnt(0)\n" \
"v_mfma_f32_16x16x32_bf16 v[0:3], v[128:131], a[160:163], v[0:3]\n" \
"ds_read_b64_tr_b16 v[160:161], v225 offset:17408\n" \
"v_mfma_f32_16x16x32_bf16 v[0:3], v[132:135], a[176:179], v[0:3]\n" \
"ds_read_b64_tr_b16 v[162:163], v225 offset:17664\n" \
"v_mfma_f32_16x16x32_bf16 v[32:35], v[128:131], a[164:167], v[32:35]\n" \
"s_mov_b32 m0, s44\n" \
"s_nop 0\n" \
"buffer_load_dwordx4 v217, %[srdw], s49 offen offset:2048 lds\n" \
"v_mfma_f32_16x16x32_bf16 v[32:35], v[132:135], a[180:183], v[32:35]\n" \
"ds_read_b64_tr_b16 v[164:165], v225 offset:25600\n" \
"v_mfma_f32_16x16x32_bf16 v[64:67], v[128:131], a[168:171], v[64:67]\n" \
"ds_read_b64_tr_b16 v[166:167], v225 offset:25856\n" \
"v_mfma_f32_16x16x32_bf16 v[64:67], v[132:135], a[184:187], v[64:67]\n" \
"v_mfma_f32_16x16x32_bf16 v[96:99], v[128:131], a[172:175], v[96:99]\n" \
"ds_read_b64_tr_b16 v[168:169], v225 offset:17416\n" \
"v_mfma_f32_16x16x32_bf16 v[96:99], v[132:135], a[188:191], v[96:99]\n" \
"ds_read_b64_tr_b16 v[170:171], v225 offset:17672\n" \
"v_mfma_f32_16x16x32_bf16 v[4:7], v[136:139], a[160:163], v[4:7]\n" \
"buffer_load_dwordx4 v218, %[srdw], s49 offen offset:3072 lds\n" \
"v_mfma_f32_16x16x32_bf16 v[4:7], v[140:143], a[176:179], v[4:7]\n" \
"ds_read_b64_tr_b16 v[172:173], v225 offset:25608\n" \
"v_mfma_f32_16x16x32_bf16 v[36:39], v[136:139], a[164:167], v[36:39]\n" \
"ds_read_b64_tr_b16 v[174:175], v225 offset:25864\n" \
"v_mfma_f32_16x16x32_bf16 v[36:39], v[140:143], a[180:183], v[36:39]\n" \
"v_mfma_f32_16x16x32_bf16 v[68:71], v[136:139], a[168:171], v[68:71]\n" \
"ds_read_b64_tr_b16 v[176:177], v225 offset:17920\n" \
"v_mfma_f32_16x16x32_bf16 v[68:71], v[140:143], a[184:187], v[68:71]\n" \
"ds_read_b64_tr_b16 v[178:179], v225 offset:18176\n" \
"v_mfma_f32_16x16x32_bf16 v[100:103], v[136:139], a[172:175], v[100:103]\n" \
"s_add_u32 s50, s50, s53\n" \
"buffer_load_dwordx4 a[128:131], v227, %[srdz], s50 offen offset:0 sc1\n" \
"v_mfma_f32_16x16x32_bf16 v[100:103], v[140:143], a[188:191], v[100:103]\n" \
"ds_read_b64_tr_b16 v[180:181], v225 offset:26112\n" \
"v_mfma_f32_16x16x32_bf16 v[8:11], v[144:147], a[160:163], v[8:11]\n" \
"ds_read_b64_tr_b16 v[182:183], v225 offset:26368\n" \
"v_mfma_f32_16x16x32_bf16 v[8:11], v[148:151], a[176:179], v[8:11]\n" \
"v_mfma_f32_16x16x32_bf16 v[40:43], v[144:147], a[164:167], v[40:43]\n" \
"ds_read_b64_tr_b16 v[184:185], v225 offset:17928\n" \
"v_mfma_f32_16x16x32_bf16 v[40:43], v[148:151], a[180:183], v[40:43]\n" \
"ds_read_b64_tr_b16 v[186:187], v225 offset:18184\n" \
"v_mfma_f32_16x16x32_bf16 v[72:75], v[144:147], a[168:171], v[72:75]\n" \
"buffer_load_dwordx4 a[132:135], v219, %[srdz], s50 offen offset:0 sc1\n" \
"v_mfma_f32_16x16x32_bf16 v[72:75], v[148:151], a[184:187], v[72:75]\n" \
"ds_read_b64_tr_b16 v[188:189], v225 offset:26120\n" \
"v_mfma_f32_16x16x32_bf16 v[104:107], v[144:147], a[172:175], v[104:107]\n" \
"ds_read_b64_tr_b16 v[190:191], v225 offset:26376\n" \
"v_mfma_f32_16x16x32_bf16 v[104:107], v[148:151], a[188:191], v[104:107]\n" \
"v_mfma_f32_16x16x32_bf16 v[12:15], v[152:155], a[160:163], v[12:15]\n" \
"buffer_load_dwordx4 a[136:139], v220, %[srdz], s50 offen offset:0 sc1\n" \
"v_mfma_f32_16x16x32_bf16 v[12:15], v[156:159], a[176:179], v[12:15]\n" \
"v_mfma_f32_16x16x32_bf16 v[44:47], v[152:155], a[164:167], v[44:47]\n" \
"v_mfma_f32_16x16x32_bf16 v[44:47], v[156:159], a[180:183], v[44:47]\n" \
"v_mfma_f32_16x16x32_bf16 v[76:79], v[152:155], a[168:171], v[76:79]\n" \
"buffer_load_dwordx4 a[140:143], v221, %[srdz], s50 offen offset:0 sc1\n" \
"v_mfma_f32_16x16x32_bf16 v[76:79], v[156:159], a[184:187], v[76:79]\n" \
"v_mfma_f32_16x16x32_bf16 v[108:111], v[152:155], a[172:175], v[108:111]\n" \
"v_mfma_f32_16x16x32_bf16 v[108:111], v[156:159], a[188:191], v[108:111]\n" \
"s_waitcnt vmcnt(29) lgkmcnt(0)\n" \
"s_barrier\n" \
"v_mfma_f32_16x16x32_bf16 v[16:19], v[160:163], a[160:163], v[16:19]\n" \
"ds_read_b64_tr_b16 v[128:129], v225 offset:32768\n" \
"v_mfma_f32_16x16x32_bf16 v[16:19], v[164:167], a[176:179], v[16:19]\n" \
"ds_read_b64_tr_b16 v[130:131], v225 offset:33024\n" \
"v_mfma_f32_16x16x32_bf16 v[48:51], v[160:163], a[164:167], v[48:51]\n" \
"s_add_u32 s49, s49, s55\n" \
"s_mov_b32 m0, s45\n" \
"s_nop 0\n" \
"buffer_load_dwordx4 v226, %[srdw], s49 offen offset:0 lds\n" \
"v_mfma_f32_16x16x32_bf16 v[48:51], v[164:167], a[180:183], v[48:51]\n" \
"ds_read_b64_tr_b16 v[132:133], v225 offset:40960\n" \
"v_mfma_f32_16x16x32_bf16 v[80:83], v[160:163], a[168:171], v[80:83]\n" \
"ds_read_b64_tr_b16 v[134:135], v225 offset:41216\n" \
"v_mfma_f32_16x16x32_bf16 v[80:83], v[164:167], a[184:187], v[80:83]\n" \
"v_mfma_f32_16x16x32_bf16 v[112:115], v[160:163], a[172:175], v[112:115]\n" \
"ds_read_b64_tr_b16 v[136:137], v225 offset:32776\n" \
"v_mfma_f32_16x16x32_bf16 v[112:115], v[164:167], a[188:191], v[112:115]\n" \
"ds_read_b64_tr_b16 v[138:139], v225 offset:33032\n" \
"v_mfma_f32_16x16x32_bf16 v[20:23], v[168:171], a[160:163], v[20:23]\n" \
"buffer_load_dwordx4 v216, %[srdw], s49 offen offset:1024 lds\n" \
"v_mfma_f32_16x16x32_bf16 v[20:23], v[172:175], a[176:179], v[20:23]\n" \
"ds_read_b64_tr_b16 v[140:141], v225 offset:40968\n" \
"v_mfma_f32_16x16x32_bf16 v[52:55], v[168:171], a[164:167], v[52:55]\n" \
"ds_read_b64_tr_b16 v[142:143], v225 offset:41224\n" \
"v_mfma_f32_16x16x32_bf16 v[52:55], v[172:175], a[180:183], v[52:55]\n" \
"v_mfma_f32_16x16x32_bf16 v[84:87], v[168:171], a[168:171], v[84:87]\n" \
"ds_read_b64_tr_b16 v[144:145], v225 offset:33280\n" \
"v_mfma_f32_16x16x32_bf16 v[84:87], v[172:175], a[184:187], v[84:87]\n" \
"ds_read_b64_tr_b16 v[146:147], v225 offset:33536\n" \
"v_mfma_f32_16x16x32_bf16 v[116:119], v[168:171], a[172:175], v[116:119]\n" \
"buffer_load_dwordx4 a[144:147], v227, %[srdz], s50 offen offset:1024 sc1\n" \
"v_mfma_f32_16x16x32_bf16 v[116:119], v[172:175], a[188:191], v[116:119]\n" \
"ds_read_b64_tr_b16 v[148:149], v225 offset:41472\n" \
"v_mfma_f32_16x16x32_bf16 v[24:27], v[176:179], a[160:163], v[24:27]\n" \
"ds_read_b64_tr_b16 v[150:151], v225 offset:41728\n" \
"v_mfma_f32_16x16x32_bf16 v[24:27], v[180:183], a[176:179], v[24:27]\n" \
"v_mfma_f32_16x16x32_bf16 v[56:59], v[176:179], a[164:167], v[56:59]\n" \
"ds_read_b64_tr_b16 v[152:153], v225 offset:33288\n" \
"v_mfma_f32_16x16x32_bf16 v[56:59], v[180:183], a[180:183], v[56:59]\n" \
"ds_read_b64_tr_b16 v[154:155], v225 offset:33544\n" \
"v_mfma_f32_16x16x32_bf16 v[88:91], v[176:179], a[168:171], v[88:91]\n" \
"buffer_load_dwordx4 a[148:151], v219, %[srdz], s50 offen offset:1024 sc1\n" \
"v_mfma_f32_16x16x32_bf16 v[88:91], v[180:183], a[184:187], v[88:91]\n" \
"ds_read_b64_tr_b16 v[156:157], v225 offset:41480\n" \
"v_mfma_f32_16x16x32_bf16 v[120:123], v[176:179], a[172:175], v[120:123]\n" \
"ds_read_b64_tr_b16 v[158:159], v225 offset:41736\n" \
"v_mfma_f32_16x16x32_bf16 v[120:123], v[180:183], a[188:191], v[120:123]\n" \
"v_mfma_f32_16x16x32_bf16 v[28:31], v[184:187], a[160:163], v[28:31]\n" \
"buffer_load_dwordx4 a[152:155], v220, %[srdz], s50 offen offset:1024 sc1\n" \
"v_mfma_f32_16x16x32_bf16 v[28:31], v[188:191], a[176:179], v[28:31]\n" \
"v_mfma_f32_16x16x32_bf16 v[60:63], v[184:187], a[164:167], v[60:63]\n" \
"v_mfma_f32_16x16x32_bf16 v[60:63], v[188:191], a[180:183], v[60:63]\n" \
"v_mfma_f32_16x16x32_bf16 v[92:95], v[184:187], a[168:171], v[92:95]\n" \
"buffer_load_dwordx4 a[156:159], v221, %[srdz], s50 offen offset:1024 sc1\n" \
"v_mfma_f32_16x16x32_bf16 v[92:95], v[188:191], a[184:187], v[92:95]\n" \
"v_mfma_f32_16x16x32_bf16 v[124:127], v[184:187], a[172:175], v[124:127]\n" \
"v_mfma_f32_16x16x32_bf16 v[124:127], v[188:191], a[188:191], v[124:127]\n" \
"s_waitcnt vmcnt(24) lgkmcnt(0)\n" \
"v_mfma_f32_16x16x32_bf16 v[0:3], v[128:131], a[192:195], v[0:3]\n" \
"ds_read_b64_tr_b16 v[160:161], v225 offset:33792\n" \
"v_mfma_f32_16x16x32_bf16 v[0:3], v[132:135], a[208:211], v[0:3]\n" \
"ds_read_b64_tr_b16 v[162:163], v225 offset:34048\n" \
"v_mfma_f32_16x16x32_bf16 v[32:35], v[128:131], a[196:199], v[32:35]\n" \
"s_mov_b32 m0, s45\n" \
"s_nop 0\n" \
"buffer_load_dwordx4 v217, %[srdw], s49 offen offset:2048 lds\n" \
"v_mfma_f32_16x16x32_bf16 v[32:35], v[132:135], a[212:215], v[32:35]\n" \
"ds_read_b64_tr_b16 v[164:165], v225 offset:41984\n" \
"v_mfma_f32_16x16x32_bf16 v[64:67], v[128:131], a[200:203], v[64:67]\n" \
"ds_read_b64_tr_b16 v[166:167], v225 offset:42240\n" \
"v_mfma_f32_16x16x32_bf16 v[64:67], v[132:135], a[216:219], v[64:67]\n" \
"v_mfma_f32_16x16x32_bf16 v[96:99], v[128:131], a[204:207], v[96:99]\n" \
"ds_read_b64_tr_b16 v[168:169], v225 offset:33800\n" \
"v_mfma_f32_16x16x32_bf16 v[96:99], v[132:135], a[220:223], v[96:99]\n" \
"ds_read_b64_tr_b16 v[170:171], v225 offset:34056\n" \
"v_mfma_f32_16x16x32_bf16 v[4:7], v[136:139], a[192:195], v[4:7]\n" \
"buffer_load_dwordx4 v218, %[srdw], s49 offen offset:3072 lds\n" \
"v_mfma_f32_16x16x32_bf16 v[4:7], v[140:143], a[208:211], v[4:7]\n" \
"ds_read_b64_tr_b16 v[172:173], v225 offset:41992\n" \
"v_mfma_f32_16x16x32_bf16 v[36:39], v[136:139], a[196:199], v[36:39]\n" \
"ds_read_b64_tr_b16 v[174:175], v225 offset:42248\n" \
"v_mfma_f32_16x16x32_bf16 v[36:39], v[140:143], a[212:215], v[36:39]\n" \
"v_mfma_f32_16x16x32_bf16 v[68:71], v[136:139], a[200:203], v[68:71]\n" \
"ds_read_b64_tr_b16 v[176:177], v225 offset:34304\n" \
"v_mfma_f32_16x16x32_bf16 v[68:71], v[140:143], a[216:219], v[68:71]\n" \
"ds_read_b64_tr_b16 v[178:179], v225 offset:34560\n" \
"v_mfma_f32_16x16x32_bf16 v[100:103], v[136:139], a[204:207], v[100:103]\n" \
"buffer_load_dwordx4 a[160:163], v227, %[srdz], s50 offen offset:2048 sc1\n" \
"v_mfma_f32_16x16x32_bf16 v[100:103], v[140:143], a[220:223], v[100:103]\n" \
"ds_read_b64_tr_b16 v[180:181], v225 offset:42496\n" \
"v_mfma_f32_16x16x32_bf16 v[8:11], v[144:147], a[192:195], v[8:11]\n" \
"ds_read_b64_tr_b16 v[182:183], v225 offset:42752\n" \
"v_mfma_f32_16x16x32_bf16 v[8:11], v[148:151], a[208:211], v[8:11]\n" \
"v_mfma_f32_16x16x32_bf16 v[40:43], v[144:147], a[196:199], v[40:43]\n" \
"ds_read_b64_tr_b16 v[184:185], v225 offset:34312\n" \
"v_mfma_f32_16x16x32_bf16 v[40:43], v[148:151], a[212:215], v[40:43]\n" \
"ds_read_b64_tr_b16 v[186:187], v225 offset:34568\n" \
"v_mfma_f32_16x16x32_bf16 v[72:75], v[144:147], a[200:203], v[72:75]\n" \
"buffer_load_dwordx4 a[164:167], v219, %[srdz], s50 offen offset:2048 sc1\n" \
"v_mfma_f32_16x16x32_bf16 v[72:75], v[148:151], a[216:219], v[72:75]\n" \
"ds_read_b64_tr_b16 v[188:189], v225 offset:42504\n" \
"v_mfma_f32_16x16x32_bf16 v[104:107], v[144:147], a[204:207], v[104:107]\n" \
"ds_read_b64_tr_b16 v[190:191], v225 offset:42760\n" \
"v_mfma_f32_16x16x32_bf16 v[104:107], v[148:151], a[220:223], v[104:107]\n" \
"v_mfma_f32_16x16x32_bf16 v[12:15], v[152:155], a[192:195], v[12:15]\n" \
"buffer_load_dwordx4 a[168:171], v220, %[srdz], s50 offen offset:2048 sc1\n" \
"v_mfma_f32_16x16x32_bf16 v[12:15], v[156:159], a[208:211], v[12:15]\n" \
"v_mfma_f32_16x16x32_bf16 v[44:47], v[152:155], a[196:199], v[44:47]\n" \
"v_mfma_f32_16x16x32_bf16 v[44:47], v[156:159], a[212:215], v[44:47]\n" \
"v_mfma_f32_16x16x32_bf16 v[76:79], v[152:155], a[200:203], v[76:79]\n" \
"buffer_load_dwordx4 a[172:175], v221, %[srdz], s50 offen offset:2048 sc1\n" \
"v_mfma_f32_16x16x32_bf16 v[76:79], v[156:159], a[216:219], v[76:79]\n" \
"v_mfma_f32_16x16x32_bf16 v[108:111], v[152:155], a[204:207], v[108:111]\n" \
"v_mfma_f32_16x16x32_bf16 v[108:111], v[156:159], a[220:223], v[108:111]\n" \
"s_waitcnt vmcnt(28) lgkmcnt(0)\n" \
"s_barrier\n" \
"v_mfma_f32_16x16x32_bf16 v[16:19], v[160:163], a[192:195], v[16:19]\n" \
"ds_read_b64_tr_b16 v[128:129], v225 offset:49152\n" \
"v_mfma_f32_16x16x32_bf16 v[16:19], v[164:167], a[208:211], v[16:19]\n" \
"ds_read_b64_tr_b16 v[130:131], v225 offset:49408\n" \
"v_mfma_f32_16x16x32_bf16 v[48:51], v[160:163], a[196:199], v[48:51]\n" \
"s_add_u32 s49, s49, s55\n" \
"s_mov_b32 m0, s46\n" \
"s_nop 0\n" \
"buffer_load_dwordx4 v226, %[srdw], s49 offen offset:0 lds\n" \
"v_mfma_f32_16x16x32_bf16 v[48:51], v[164:167], a[212:215], v[48:51]\n" \
"ds_read_b64_tr_b16 v[132:133], v225 offset:57344\n" \
"v_mfma_f32_16x16x32_bf16 v[80:83], v[160:163], a[200:203], v[80:83]\n" \
"ds_read_b64_tr_b16 v[134:135], v225 offset:57600\n" \
"v_mfma_f32_16x16x32_bf16 v[80:83], v[164:167], a[216:219], v[80:83]\n" \
"v_mfma_f32_16x16x32_bf16 v[112:115], v[160:163], a[204:207], v[112:115]\n" \
"ds_read_b64_tr_b16 v[136:137], v225 offset:49160\n" \
"v_mfma_f32_16x16x32_bf16 v[112:115], v[164:167], a[220:223], v[112:115]\n" \
"ds_read_b64_tr_b16 v[138:139], v225 offset:49416\n" \
"v_mfma_f32_16x16x32_bf16 v[20:23], v[168:171], a[192:195], v[20:23]\n" \
"buffer_load_dwordx4 v216, %[srdw], s49 offen offset:1024 lds\n" \
"v_mfma_f32_16x16x32_bf16 v[20:23], v[172:175], a[208:211], v[20:23]\n" \
"ds_read_b64_tr_b16 v[140:141], v225 offset:57352\n" \
"v_mfma_f32_16x16x32_bf16 v[52:55], v[168:171], a[196:199], v[52:55]\n" \
"ds_read_b64_tr_b16 v[142:143], v225 offset:57608\n" \
"v_mfma_f32_16x16x32_bf16 v[52:55], v[172:175], a[212:215], v[52:55]\n" \
"v_mfma_f32_16x16x32_bf16 v[84:87], v[168:171], a[200:203], v[84:87]\n" \
"ds_read_b64_tr_b16 v[144:145], v225 offset:49664\n" \
"v_mfma_f32_16x16x32_bf16 v[84:87], v[172:175], a[216:219], v[84:87]\n" \
"ds_read_b64_tr_b16 v[146:147], v225 offset:49920\n" \
"v_mfma_f32_16x16x32_bf16 v[116:119], v[168:171], a[204:207], v[116:119]\n" \
"buffer_load_dwordx4 a[176:179], v227, %[srdz], s50 offen offset:3072 sc1\n" \
"v_mfma_f32_16x16x32_bf16 v[116:119], v[172:175], a[220:223], v[116:119]\n" \
"ds_read_b64_tr_b16 v[148:149], v225 offset:57856\n" \
"v_mfma_f32_16x16x32_bf16 v[24:27], v[176:179], a[192:195], v[24:27]\n" \
"ds_read_b64_tr_b16 v[150:151], v225 offset:58112\n" \
"v_mfma_f32_16x16x32_bf16 v[24:27], v[180:183], a[208:211], v[24:27]\n" \
"v_mfma_f32_16x16x32_bf16 v[56:59], v[176:179], a[196:199], v[56:59]\n" \
"ds_read_b64_tr_b16 v[152:153], v225 offset:49672\n" \
"v_mfma_f32_16x16x32_bf16 v[56:59], v[180:183], a[212:215], v[56:59]\n" \
"ds_read_b64_tr_b16 v[154:155], v225 offset:49928\n" \
"v_mfma_f32_16x16x32_bf16 v[88:91], v[176:179], a[200:203], v[88:91]\n" \
"buffer_load_dwordx4 a[180:183], v219, %[srdz], s50 offen offset:3072 sc1\n" \
"v_mfma_f32_16x16x32_bf16 v[88:91], v[180:183], a[216:219], v[88:91]\n" \
"ds_read_b64_tr_b16 v[156:157], v225 offset:57864\n" \
"v_mfma_f32_16x16x32_bf16 v[120:123], v[176:179], a[204:207], v[120:123]\n" \
"ds_read_b64_tr_b16 v[158:159], v225 offset:58120\n" \
"v_mfma_f32_16x16x32_bf16 v[120:123], v[180:183], a[220:223], v[120:123]\n" \
"v_mfma_f32_16x16x32_bf16 v[28:31], v[184:187], a[192:195], v[28:31]\n" \
"buffer_load_dwordx4 a[184:187], v220, %[srdz], s50 offen offset:3072 sc1\n" \
"v_mfma_f32_16x16x32_bf16 v[28:31], v[188:191], a[208:211], v[28:31]\n" \
"v_mfma_f32_16x16x32_bf16 v[60:63], v[184:187], a[196:199], v[60:63]\n" \
"v_mfma_f32_16x16x32_bf16 v[60:63], v[188:191], a[212:215], v[60:63]\n" \
"v_mfma_f32_16x16x32_bf16 v[92:95], v[184:187], a[200:203], v[92:95]\n" \
"buffer_load_dwordx4 a[188:191], v221, %[srdz], s50 offen offset:3072 sc1\n" \
"v_mfma_f32_16x16x32_bf16 v[92:95], v[188:191], a[216:219], v[92:95]\n" \
"v_mfma_f32_16x16x32_bf16 v[124:127], v[184:187], a[204:207], v[124:127]\n" \
"v_mfma_f32_16x16x32_bf16 v[124:127], v[188:191], a[220:223], v[124:127]\n" \
"s_waitcnt vmcnt(24) lgkmcnt(0)\n" \
"v_mfma_f32_16x16x32_bf16 v[0:3], v[128:131], a[224:227], v[0:3]\n" \
"ds_read_b64_tr_b16 v[160:161], v225 offset:50176\n" \
"v_mfma_f32_16x16x32_bf16 v[0:3], v[132:135], a[240:243], v[0:3]\n" \
"ds_read_b64_tr_b16 v[162:163], v225 offset:50432\n" \
"v_mfma_f32_16x16x32_bf16 v[32:35], v[128:131], a[228:231], v[32:35]\n" \
"s_mov_b32 m0, s46\n" \
"s_nop 0\n" \
"buffer_load_dwordx4 v217, %[srdw], s49 offen offset:2048 lds\n" \
"v_mfma_f32_16x16x32_bf16 v[32:35], v[132:135], a[244:247], v[32:35]\n" \
"ds_read_b64_tr_b16 v[164:165], v225 offset:58368\n" \
"v_mfma_f32_16x16x32_bf16 v[64:67], v[128:131], a[232:235], v[64:67]\n" \
"ds_read_b64_tr_b16 v[166:167], v225 offset:58624\n" \
"v_mfma_f32_16x16x32_bf16 v[64:67], v[132:135], a[248:251], v[64:67]\n" \
"v_mfma_f32_16x16x32_bf16 v[96:99], v[128:131], a[236:239], v[96:99]\n" \
"ds_read_b64_tr_b16 v[168:169], v225 offset:50184\n" \
"v_mfma_f32_16x16x32_bf16 v[96:99], v[132:135], a[252:255], v[96:99]\n" \
"ds_read_b64_tr_b16 v[170:171], v225 offset:50440\n" \
"v_mfma_f32_16x16x32_bf16 v[4:7], v[136:139], a[224:227], v[4:7]\n" \
"buffer_load_dwordx4 v218, %[srdw], s49 offen offset:3072 lds\n" \
"v_mfma_f32_16x16x32_bf16 v[4:7], v[140:143], a[240:243], v[4:7]\n" \
"ds_read_b64_tr_b16 v[172:173], v225 offset:58376\n" \
"v_mfma_f32_16x16x32_bf16 v[36:39], v[136:139], a[228:231], v[36:39]\n" \
"ds_read_b64_tr_b16 v[174:175], v225 offset:58632\n" \
"v_mfma_f32_16x16x32_bf16 v[36:39], v[140:143], a[244:247], v[36:39]\n" \
"v_mfma_f32_16x16x32_bf16 v[68:71], v[136:139], a[232:235], v[68:71]\n" \
"ds_read_b64_tr_b16 v[176:177], v225 offset:50688\n" \
"v_mfma_f32_16x16x32_bf16 v[68:71], v[140:143], a[248:251], v[68:71]\n" \
"ds_read_b64_tr_b16 v[178:179], v225 offset:50944\n" \
"v_mfma_f32_16x16x32_bf16 v[100:103], v[136:139], a[236:239], v[100:103]\n" \
"s_add_u32 s50, s50, s53\n" \
"buffer_load_dwordx4 a[192:195], v227, %[srdz], s50 offen offset:0 sc1\n" \
"v_mfma_f32_16x16x32_bf16 v[100:103], v[140:143], a[252:255], v[100:103]\n" \
"ds_read_b64_tr_b16 v[180:181], v225 offset:58880\n" \
"v_mfma_f32_16x16x32_bf16 v[8:11], v[144:147], a[224:227], v[8:11]\n" \
"ds_read_b64_tr_b16 v[182:183], v225 offset:59136\n" \
"v_mfma_f32_16x16x32_bf16 v[8:11], v[148:151], a[240:243], v[8:11]\n" \
"v_mfma_f32_16x16x32_bf16 v[40:43], v[144:147], a[228:231], v[40:43]\n" \
"ds_read_b64_tr_b16 v[184:185], v225 offset:50696\n" \
"v_mfma_f32_16x16x32_bf16 v[40:43], v[148:151], a[244:247], v[40:43]\n" \
"ds_read_b64_tr_b16 v[186:187], v225 offset:50952\n" \
"v_mfma_f32_16x16x32_bf16 v[72:75], v[144:147], a[232:235], v[72:75]\n" \
"buffer_load_dwordx4 a[196:199], v219, %[srdz], s50 offen offset:0 sc1\n" \
"v_mfma_f32_16x16x32_bf16 v[72:75], v[148:151], a[248:251], v[72:75]\n" \
"ds_read_b64_tr_b16 v[188:189], v225 offset:58888\n" \
"v_mfma_f32_16x16x32_bf16 v[104:107], v[144:147], a[236:239], v[104:107]\n" \
"ds_read_b64_tr_b16 v[190:191], v225 offset:59144\n" \
"v_mfma_f32_16x16x32_bf16 v[104:107], v[148:151], a[252:255], v[104:107]\n" \
"v_mfma_f32_16x16x32_bf16 v[12:15], v[152:155], a[224:227], v[12:15]\n" \
"buffer_load_dwordx4 a[200:203], v220, %[srdz], s50 offen offset:0 sc1\n" \
"v_mfma_f32_16x16x32_bf16 v[12:15], v[156:159], a[240:243], v[12:15]\n" \
"v_mfma_f32_16x16x32_bf16 v[44:47], v[152:155], a[228:231], v[44:47]\n" \
"buffer_load_dword v237, v236, %[srdw], s60 offen\n" \
"s_add_u32 s60, s60, 0x200000\n" \
"v_mfma_f32_16x16x32_bf16 v[44:47], v[156:159], a[244:247], v[44:47]\n" \
"v_mfma_f32_16x16x32_bf16 v[76:79], v[152:155], a[232:235], v[76:79]\n" \
"buffer_load_dwordx4 a[204:207], v221, %[srdz], s50 offen offset:0 sc1\n" \
"v_mfma_f32_16x16x32_bf16 v[76:79], v[156:159], a[248:251], v[76:79]\n" \
".if . - adaln_body != 4096\n" \
".error \"size model\"\n" \
".endif\n" \
"Lk0_%=:\n" \
"s_cbranch_execz Lk1_%=\n" \
"s_nop 0\n" \
"v_mfma_f32_16x16x32_bf16 v[108:111], v[152:155], a[236:239], v[108:111]\n" \
"v_mfma_f32_16x16x32_bf16 v[108:111], v[156:159], a[252:255], v[108:111]\n" \
"s_waitcnt vmcnt(29) lgkmcnt(0)\n" \
"s_barrier\n" \
"v_mfma_f32_16x16x32_bf16 v[16:19], v[160:163], a[224:227], v[16:19]\n" \
"ds_read_b64_tr_b16 v[128:129], v225 offset:0\n" \
"v_mfma_f32_16x16x32_bf16 v[16:19], v[164:167], a[240:243], v[16:19]\n" \
"ds_read_b64_tr_b16 v[130:131], v225 offset:256\n" \
"v_mfma_f32_16x16x32_bf16 v[48:51], v[160:163], a[228:231], v[48:51]\n" \
"s_add_u32 s49, s49, s55\n" \
"s_mov_b32 m0, s47\n" \
"s_nop 0\n" \
"buffer_load_dwordx4 v226, %[srdw], s49 offen offset:0 lds\n" \
"v_mfma_f32_16x16x32_bf16 v[48:51], v[164:167], a[244:247], v[48:51]\n" \
"ds_read_b64_tr_b16 v[132:133], v225 offset:8192\n" \
"v_mfma_f32_16x16x32_bf16 v[80:83], v[160:163], a[232:235], v[80:83]\n" \
"ds_read_b64_tr_b16 v[134:135], v225 offset:8448\n" \
"v_mfma_f32_16x16x32_bf16 v[80:83], v[164:167], a[248:251], v[80:83]\n" \
"v_mfma_f32_16x16x32_bf16 v[112:115], v[160:163], a[236:239], v[112:115]\n" \
"ds_read_b64_tr_b16 v[136:137], v225 offset:8\n" \
"v_mfma_f32_16x16x32_bf16 v[112:115], v[164:167], a[252:255], v[112:115]\n" \
"ds_read_b64_tr_b16 v[138:139], v225 offset:264\n" \
"v_mfma_f32_16x16x32_bf16 v[20:23], v[168:171], a[224:227], v[20:23]\n" \
"buffer_load_dwordx4 v216, %[srdw], s49 offen offset:1024 lds\n" \
"v_mfma_f32_16x16x32_bf16 v[20:23], v[172:175], a[240:243], v[20:23]\n" \
"ds_read_b64_tr_b16 v[140:141], v225 offset:8200\n" \
"v_mfma_f32_16x16x32_bf16 v[52:55], v[168:171], a[228:231], v[52:55]\n" \
"ds_read_b64_tr_b16 v[142:143], v225 offset:8456\n" \
"v_mfma_f32_16x16x32_bf16 v[52:55], v[172:175], a[244:247], v[52:55]\n" \
"v_mfma_f32_16x16x32_bf16 v[84:87], v[168:171], a[232:235], v[84:87]\n" \
"ds_read_b64_tr_b16 v[144:145], v225 offset:512\n" \
"v_mfma_f32_16x16x32_bf16 v[84:87], v[172:175], a[248:251], v[84:87]\n" \
"ds_read_b64_tr_b16 v[146:147], v225 offset:768\n" \
"v_mfma_f32_16x16x32_bf16 v[116:119], v[168:171], a[236:239], v[116:119]\n" \
"buffer_load_dwordx4 a[208:211], v227, %[srdz], s50 offen offset:1024 sc1\n" \
"v_mfma_f32_16x16x32_bf16 v[116:119], v[172:175], a[252:255], v[116:119]\n" \
"ds_read_b64_tr_b16 v[148:149], v225 offset:8704\n" \
"v_mfma_f32_16x16x32_bf16 v[24:27], v[176:179], a[224:227], v[24:27]\n" \
"ds_read_b64_tr_b16 v[150:151], v225 offset:8960\n" \
"v_mfma_f32_16x16x32_bf16 v[24:27], v[180:183], a[240:243], v[24:27]\n" \
"v_mfma_f32_16x16x32_bf16 v[56:59], v[176:179], a[228:231], v[56:59]\n" \
"ds_read_b64_tr_b16 v[152:153], v225 offset:520\n" \
"v_mfma_f32_16x16x32_bf16 v[56:59], v[180:183], a[244:247], v[56:59]\n" \
"ds_read_b64_tr_b16 v[154:155], v225 offset:776\n" \
"v_mfma_f32_16x16x32_bf16 v[88:91], v[176:179], a[232:235], v[88:91]\n" \
"buffer_load_dwordx4 a[212:215], v219, %[srdz], s50 offen offset:1024 sc1\n" \
"v_mfma_f32_16x16x32_bf16 v[88:91], v[180:183], a[248:251], v[88:91]\n" \
"ds_read_b64_tr_b16 v[156:157], v225 offset:8712\n" \
"v_mfma_f32_16x16x32_bf16 v[120:123], v[176:179], a[236:239], v[120:123]\n" \
"ds_read_b64_tr_b16 v[158:159], v225 offset:8968\n" \
"v_mfma_f32_16x16x32_bf16 v[120:123], v[180:183], a[252:255], v[120:123]\n" \
"v_mfma_f32_16x16x32_bf16 v[28:31], v[184:187], a[224:227], v[28:31]\n" \
"buffer_load_dwordx4 a[216:219], v220, %[srdz], s50 offen offset:1024 sc1\n" \
"v_mfma_f32_16x16x32_bf16 v[28:31], v[188:191], a[240:243], v[28:31]\n" \
"v_mfma_f32_16x16x32_bf16 v[60:63], v[184:187], a[228:231], v[60:63]\n" \
"v_mfma_f32_16x16x32_bf16 v[60:63], v[188:191], a[244:247], v[60:63]\n" \
"v_mfma_f32_16x16x32_bf16 v[92:95], v[184:187], a[232:235], v[92:95]\n" \
"buffer_load_dwordx4 a[220:223], v221, %[srdz], s50 offen offset:1024 sc1\n" \
"v_mfma_f32_16x16x32_bf16 v[92:95], v[188:191], a[248:251], v[92:95]\n" \
"v_mfma_f32_16x16x32_bf16 v[124:127], v[184:187], a[236:239], v[124:127]\n" \
"v_mfma_f32_16x16x32_bf16 v[124:127], v[188:191], a[252:255], v[124:127]\n" \
"s_waitcnt vmcnt(25) lgkmcnt(0)\n" \
"s_sub_u32 s51, s51, 1\n" \
"s_cmp_lg_u32 s51, 0\n" \
"s_cbranch_scc1 Lq_%=\n" \
"s_nop 0\n" \
"v_mfma_f32_16x16x32_bf16 v[0:3], v[128:131], a[128:131], v[0:3]\n" \
"ds_read_b64_tr_b16 v[160:161], v225 offset:1024\n" \
"v_mfma_f32_16x16x32_bf16 v[0:3], v[132:135], a[144:147], v[0:3]\n" \
"ds_read_b64_tr_b16 v[162:163], v225 offset:1280\n" \
"v_mfma_f32_16x16x32_bf16 v[32:35], v[128:131], a[132:135], v[32:35]\n" \
"s_mov_b32 m0, s47\n" \
"s_nop 0\n" \
"buffer_load_dwordx4 v217, %[srdw], s49 offen offset:2048 lds\n" \
"v_mfma_f32_16x16x32_bf16 v[32:35], v[132:135], a[148:151], v[32:35]\n" \
"ds_read_b64_tr_b16 v[164:165], v225 offset:9216\n" \
"v_mfma_f32_16x16x32_bf16 v[64:67], v[128:131], a[136:139], v[64:67]\n" \
"ds_read_b64_tr_b16 v[166:167], v225 offset:9472\n" \
"v_mfma_f32_16x16x32_bf16 v[64:67], v[132:135], a[152:155], v[64:67]\n" \
"v_mfma_f32_16x16x32_bf16 v[96:99], v[128:131], a[140:143], v[96:99]\n" \
"ds_read_b64_tr_b16 v[168:169], v225 offset:1032\n" \
"v_mfma_f32_16x16x32_bf16 v[96:99], v[132:135], a[156:159], v[96:99]\n" \
"ds_read_b64_tr_b16 v[170:171], v225 offset:1288\n" \
"v_mfma_f32_16x16x32_bf16 v[4:7], v[136:139], a[128:131], v[4:7]\n" \
"buffer_load_dwordx4 v218, %[srdw], s49 offen offset:3072 lds\n" \
"v_mfma_f32_16x16x32_bf16 v[4:7], v[140:143], a[144:147], v[4:7]\n" \
"ds_read_b64_tr_b16 v[172:173], v225 offset:9224\n" \
"v_mfma_f32_16x16x32_bf16 v[36:39], v[136:139], a[132:135], v[36:39]\n" \
"ds_read_b64_tr_b16 v[174:175], v225 offset:9480\n" \
"v_mfma_f32_16x16x32_bf16 v[36:39], v[140:143], a[148:151], v[36:39]\n" \
"v_mfma_f32_16x16x32_bf16 v[68:71], v[136:139], a[136:139], v[68:71]\n" \
"ds_read_b64_tr_b16 v[176:177], v225 offset:1536\n" \
"v_mfma_f32_16x16x32_bf16 v[68:71], v[140:143], a[152:155], v[68:71]\n" \
"ds_read_b64_tr_b16 v[178:179], v225 offset:1792\n" \
"v_mfma_f32_16x16x32_bf16 v[100:103], v[136:139], a[140:143], v[100:103]\n" \
"buffer_load_dwordx4 a[224:227], v227, %[srdz], s50 offen offset:2048 sc1\n" \
"v_mfma_f32_16x16x32_bf16 v[100:103], v[140:143], a[156:159], v[100:103]\n" \
"ds_read_b64_tr_b16 v[180:181], v225 offset:9728\n" \
"v_mfma_f32_16x16x32_bf16 v[8:11], v[144:147], a[128:131], v[8:11]\n" \
"ds_read_b64_tr_b16 v[182:183], v225 offset:9984\n" \
"v_mfma_f32_16x16x32_bf16 v[8:11], v[148:151], a[144:147], v[8:11]\n" \
"v_mfma_f32_16x16x32_bf16 v[40:43], v[144:147], a[132:135], v[40:43]\n" \
"ds_read_b64_tr_b16 v[184:185], v225 offset:1544\n" \
"v_mfma_f32_16x16x32_bf16 v[40:43], v[148:151], a[148:151], v[40:43]\n" \
"ds_read_b64_tr_b16 v[186:187], v225 offset:1800\n" \
"v_mfma_f32_16x16x32_bf16 v[72:75], v[144:147], a[136:139], v[72:75]\n" \
"buffer_load_dwordx4 a[228:231], v219, %[srdz], s50 offen offset:2048 sc1\n" \
"v_mfma_f32_16x16x32_bf16 v[72:75], v[148:151], a[152:155], v[72:75]\n" \
"ds_read_b64_tr_b16 v[188:189], v225 offset:9736\n" \
"v_mfma_f32_16x16x32_bf16 v[104:107], v[144:147], a[140:143], v[104:107]\n" \
"ds_read_b64_tr_b16 v[190:191], v225 offset:9992\n" \
"v_mfma_f32_16x16x32_bf16 v[104:107], v[148:151], a[156:159], v[104:107]\n" \
"v_mfma_f32_16x16x32_bf16 v[12:15], v[152:155], a[128:131], v[12:15]\n" \
"buffer_load_dwordx4 a[232:235], v220, %[srdz], s50 offen offset:2048 sc1\n" \
"v_mfma_f32_16x16x32_bf16 v[12:15], v[156:159], a[144:147], v[12:15]\n" \
"v_mfma_f32_16x16x32_bf16 v[44:47], v[152:155], a[132:135], v[44:47]\n" \
"v_mfma_f32_16x16x32_bf16 v[44:47], v[156:159], a[148:151], v[44:47]\n" \
"v_mfma_f32_16x16x32_bf16 v[76:79], v[152:155], a[136:139], v[76:79]\n" \
"buffer_load_dwordx4 a[236:239], v221, %[srdz], s50 offen offset:2048 sc1\n" \
"v_mfma_f32_16x16x32_bf16 v[76:79], v[156:159], a[152:155], v[76:79]\n" \
"v_mfma_f32_16x16x32_bf16 v[108:111], v[152:155], a[140:143], v[108:111]\n" \
"v_mfma_f32_16x16x32_bf16 v[108:111], v[156:159], a[156:159], v[108:111]\n" \
"s_waitcnt vmcnt(29) lgkmcnt(0)\n" \
"s_barrier\n" \
"v_mfma_f32_16x16x32_bf16 v[16:19], v[160:163], a[128:131], v[16:19]\n" \
"ds_read_b64_tr_b16 v[128:129], v225 offset:16384\n" \
"v_mfma_f32_16x16x32_bf16 v[16:19], v[164:167], a[144:147], v[16:19]\n" \
"ds_read_b64_tr_b16 v[130:131], v225 offset:16640\n" \
"v_mfma_f32_16x16x32_bf16 v[48:51], v[160:163], a[132:135], v[48:51]\n" \
"s_mov_b32 s48, 16384\n" \
"s_mov_b32 m0, s44\n" \
"s_nop 0\n" \
"buffer_load_dwordx4 %[vd0], %[srdv], s48 offen offset:0 lds\n" \
"v_mfma_f32_16x16x32_bf16 v[48:51], v[164:167], a[148:151], v[48:51]\n" \
"ds_read_b64_tr_b16 v[132:133], v225 offset:24576\n" \
"v_mfma_f32_16x16x32_bf16 v[80:83], v[160:163], a[136:139], v[80:83]\n" \
"ds_read_b64_tr_b16 v[134:135], v225 offset:24832\n" \
"v_mfma_f32_16x16x32_bf16 v[80:83], v[164:167], a[152:155], v[80:83]\n" \
"v_mfma_f32_16x16x32_bf16 v[112:115], v[160:163], a[140:143], v[112:115]\n" \
"ds_read_b64_tr_b16 v[136:137], v225 offset:16392\n" \
"v_mfma_f32_16x16x32_bf16 v[112:115], v[164:167], a[156:159], v[112:115]\n" \
"ds_read_b64_tr_b16 v[138:139], v225 offset:16648\n" \
"v_mfma_f32_16x16x32_bf16 v[20:23], v[168:171], a[128:131], v[20:23]\n" \
"buffer_load_dwordx4 v215, %[srdv], s48 offen offset:1024 lds\n" \
"v_mfma_f32_16x16x32_bf16 v[20:23], v[172:175], a[144:147], v[20:23]\n" \
"ds_read_b64_tr_b16 v[140:141], v225 offset:24584\n" \
"v_mfma_f32_16x16x32_bf16 v[52:55], v[168:171], a[132:135], v[52:55]\n" \
"ds_read_b64_tr_b16 v[142:143], v225 offset:24840\n" \
"v_mfma_f32_16x16x32_bf16 v[52:55], v[172:175], a[148:151], v[52:55]\n" \
"v_mfma_f32_16x16x32_bf16 v[84:87], v[168:171], a[136:139], v[84:87]\n" \
"ds_read_b64_tr_b16 v[144:145], v225 offset:16896\n" \
"v_mfma_f32_16x16x32_bf16 v[84:87], v[172:175], a[152:155], v[84:87]\n" \
"ds_read_b64_tr_b16 v[146:147], v225 offset:17152\n" \
"v_mfma_f32_16x16x32_bf16 v[116:119], v[168:171], a[140:143], v[116:119]\n" \
"buffer_load_dwordx4 a[240:243], v227, %[srdz], s50 offen offset:3072 sc1\n" \
"v_mfma_f32_16x16x32_bf16 v[116:119], v[172:175], a[156:159], v[116:119]\n" \
"ds_read_b64_tr_b16 v[148:149], v225 offset:25088\n" \
"v_mfma_f32_16x16x32_bf16 v[24:27], v[176:179], a[128:131], v[24:27]\n" \
"ds_read_b64_tr_b16 v[150:151], v225 offset:25344\n" \
"v_mfma_f32_16x16x32_bf16 v[24:27], v[180:183], a[144:147], v[24:27]\n" \
"v_mfma_f32_16x16x32_bf16 v[56:59], v[176:179], a[132:135], v[56:59]\n" \
"ds_read_b64_tr_b16 v[152:153], v225 offset:16904\n" \
"v_mfma_f32_16x16x32_bf16 v[56:59], v[180:183], a[148:151], v[56:59]\n" \
"ds_read_b64_tr_b16 v[154:155], v225 offset:17160\n" \
"v_mfma_f32_16x16x32_bf16 v[88:91], v[176:179], a[136:139], v[88:91]\n" \
"buffer_load_dwordx4 a[244:247], v219, %[srdz], s50 offen offset:3072 sc1\n" \
"v_mfma_f32_16x16x32_bf16 v[88:91], v[180:183], a[152:155], v[88:91]\n" \
"ds_read_b64_tr_b16 v[156:157], v225 offset:25096\n" \
"v_mfma_f32_16x16x32_bf16 v[120:123], v[176:179], a[140:143], v[120:123]\n" \
"ds_read_b64_tr_b16 v[158:159], v225 offset:25352\n" \
"v_mfma_f32_16x16x32_bf16 v[120:123], v[180:183], a[156:159], v[120:123]\n" \
"v_mfma_f32_16x16x32_bf16 v[28:31], v[184:187], a[128:131], v[28:31]\n" \
"buffer_load_dwordx4 a[248:251], v220, %[srdz], s50 offen offset:3072 sc1\n" \
"v_mfma_f32_16x16x32_bf16 v[28:31], v[188:191], a[144:147], v[28:31]\n" \
"v_mfma_f32_16x16x32_bf16 v[60:63], v[184:187], a[132:135], v[60:63]\n" \
"v_mfma_f32_16x16x32_bf16 v[60:63], v[188:191], a[148:151], v[60:63]\n" \
"v_mfma_f32_16x16x32_bf16 v[92:95], v[184:187], a[136:139], v[92:95]\n" \
"buffer_load_dwordx4 a[252:255], v221, %[srdz], s50 offen offset:3072 sc1\n" \
"v_mfma_f32_16x16x32_bf16 v[92:95], v[188:191], a[152:155], v[92:95]\n" \
"v_mfma_f32_16x16x32_bf16 v[124:127], v[184:187], a[140:143], v[124:127]\n" \
"v_mfma_f32_16x16x32_bf16 v[124:127], v[188:191], a[156:159], v[124:127]\n" \
"s_waitcnt vmcnt(25) lgkmcnt(0)\n" \
"v_mfma_f32_16x16x32_bf16 v[0:3], v[128:131], a[160:163], v[0:3]\n" \
"ds_read_b64_tr_b16 v[160:161], v225 offset:17408\n" \
"v_mfma_f32_16x16x32_bf16 v[0:3], v[132:135], a[176:179], v[0:3]\n" \
"ds_read_b64_tr_b16 v[162:163], v225 offset:17664\n" \
"v_mfma_f32_16x16x32_bf16 v[32:35], v[128:131], a[164:167], v[32:35]\n" \
"s_mov_b32 s48, 16384\n" \
"s_mov_b32 m0, s44\n" \
"s_nop 0\n" \
"buffer_load_dwordx4 %[vd0], %[srdv], s48 offen offset:2048 lds\n" \
"v_mfma_f32_16x16x32_bf16 v[32:35], v[132:135], a[180:183], v[32:35]\n" \
"ds_read_b64_tr_b16 v[164:165], v225 offset:25600\n" \
"v_mfma_f32_16x16x32_bf16 v[64:67], v[128:131], a[168:171], v[64:67]\n" \
"ds_read_b64_tr_b16 v[166:167], v225 offset:25856\n" \
"v_mfma_f32_16x16x32_bf16 v[64:67], v[132:135], a[184:187], v[64:67]\n" \
"v_mfma_f32_16x16x32_bf16 v[96:99], v[128:131], a[172:175], v[96:99]\n" \
"ds_read_b64_tr_b16 v[168:169], v225 offset:17416\n" \
"v_mfma_f32_16x16x32_bf16 v[96:99], v[132:135], a[188:191], v[96:99]\n" \
"ds_read_b64_tr_b16 v[170:171], v225 offset:17672\n" \
"v_mfma_f32_16x16x32_bf16 v[4:7], v[136:139], a[160:163], v[4:7]\n" \
"buffer_load_dwordx4 v215, %[srdv], s48 offen offset:3072 lds\n" \
"v_mfma_f32_16x16x32_bf16 v[4:7], v[140:143], a[176:179], v[4:7]\n" \
"ds_read_b64_tr_b16 v[172:173], v225 offset:25608\n" \
"v_mfma_f32_16x16x32_bf16 v[36:39], v[136:139], a[164:167], v[36:39]\n" \
"ds_read_b64_tr_b16 v[174:175], v225 offset:25864\n" \
"v_mfma_f32_16x16x32_bf16 v[36:39], v[140:143], a[180:183], v[36:39]\n" \
"v_mfma_f32_16x16x32_bf16 v[68:71], v[136:139], a[168:171], v[68:71]\n" \
"ds_read_b64_tr_b16 v[176:177], v225 offset:17920\n" \
"v_mfma_f32_16x16x32_bf16 v[68:71], v[140:143], a[184:187], v[68:71]\n" \
"ds_read_b64_tr_b16 v[178:179], v225 offset:18176\n" \
"v_mfma_f32_16x16x32_bf16 v[100:103], v[136:139], a[172:175], v[100:103]\n" \
"s_mov_b32 s48, 0\n" \
"s_mov_b32 m0, s40\n" \
"s_nop 0\n" \
"buffer_load_dwordx4 %[kd0], %[srdk], s48 offen offset:0 lds\n" \
"v_mfma_f32_16x16x32_bf16 v[100:103], v[140:143], a[188:191], v[100:103]\n" \
"ds_read_b64_tr_b16 v[180:181], v225 offset:26112\n" \
"v_mfma_f32_16x16x32_bf16 v[8:11], v[144:147], a[160:163], v[8:11]\n" \
"ds_read_b64_tr_b16 v[182:183], v225 offset:26368\n" \
"v_mfma_f32_16x16x32_bf16 v[8:11], v[148:151], a[176:179], v[8:11]\n" \
"v_mfma_f32_16x16x32_bf16 v[40:43], v[144:147], a[164:167], v[40:43]\n" \
"ds_read_b64_tr_b16 v[184:185], v225 offset:17928\n" \
"v_mfma_f32_16x16x32_bf16 v[40:43], v[148:151], a[180:183], v[40:43]\n" \
"ds_read_b64_tr_b16 v[186:187], v225 offset:18184\n" \
"v_mfma_f32_16x16x32_bf16 v[72:75], v[144:147], a[168:171], v[72:75]\n" \
"buffer_load_dwordx4 v212, %[srdk], s48 offen offset:1024 lds\n" \
"v_mfma_f32_16x16x32_bf16 v[72:75], v[148:151], a[184:187], v[72:75]\n" \
"ds_read_b64_tr_b16 v[188:189], v225 offset:26120\n" \
"v_mfma_f32_16x16x32_bf16 v[104:107], v[144:147], a[172:175], v[104:107]\n" \
"ds_read_b64_tr_b16 v[190:191], v225 offset:26376\n" \
"v_mfma_f32_16x16x32_bf16 v[104:107], v[148:151], a[188:191], v[104:107]\n" \
"v_mfma_f32_16x16x32_bf16 v[12:15], v[152:155], a[160:163], v[12:15]\n" \
"buffer_load_dwordx4 v213, %[srdk], s48 offen offset:2048 lds\n" \
"v_mfma_f32_16x16x32_bf16 v[12:15], v[156:159], a[176:179], v[12:15]\n" \
"v_mfma_f32_16x16x32_bf16 v[44:47], v[152:155], a[164:167], v[44:47]\n" \
"v_mfma_f32_16x16x32_bf16 v[44:47], v[156:159], a[180:183], v[44:47]\n" \
"v_mfma_f32_16x16x32_bf16 v[76:79], v[152:155], a[168:171], v[76:79]\n" \
"buffer_load_dwordx4 v214, %[srdk], s48 offen offset:3072 lds\n" \
"v_mfma_f32_16x16x32_bf16 v[76:79], v[156:159], a[184:187], v[76:79]\n" \
"v_mfma_f32_16x16x32_bf16 v[108:111], v[152:155], a[172:175], v[108:111]\n" \
"v_mfma_f32_16x16x32_bf16 v[108:111], v[156:159], a[188:191], v[108:111]\n" \
"s_waitcnt vmcnt(29) lgkmcnt(0)\n" \
"s_barrier\n" \
"v_mfma_f32_16x16x32_bf16 v[16:19], v[160:163], a[160:163], v[16:19]\n" \
"ds_read_b64_tr_b16 v[128:129], v225 offset:32768\n" \
"v_mfma_f32_16x16x32_bf16 v[16:19], v[164:167], a[176:179], v[16:19]\n" \
"ds_read_b64_tr_b16 v[130:131], v225 offset:33024\n" \
"v_mfma_f32_16x16x32_bf16 v[48:51], v[160:163], a[164:167], v[48:51]\n" \
"s_mov_b32 s48, 32768\n" \
"s_mov_b32 m0, s45\n" \
"s_nop 0\n" \
"buffer_load_dwordx4 %[vd0], %[srdv], s48 offen offset:0 lds\n" \
"v_mfma_f32_16x16x32_bf16 v[48:51], v[164:167], a[180:183], v[48:51]\n" \
"ds_read_b64_tr_b16 v[132:133], v225 offset:40960\n" \
"v_mfma_f32_16x16x32_bf16 v[80:83], v[160:163], a[168:171], v[80:83]\n" \
"ds_read_b64_tr_b16 v[134:135], v225 offset:41216\n" \
"v_mfma_f32_16x16x32_bf16 v[80:83], v[164:167], a[184:187], v[80:83]\n" \
"v_mfma_f32_16x16x32_bf16 v[112:115], v[160:163], a[172:175], v[112:115]\n" \
"ds_read_b64_tr_b16 v[136:137], v225 offset:32776\n" \
"v_mfma_f32_16x16x32_bf16 v[112:115], v[164:167], a[188:191], v[112:115]\n" \
"ds_read_b64_tr_b16 v[138:139], v225 offset:33032\n" \
"v_mfma_f32_16x16x32_bf16 v[20:23], v[168:171], a[160:163], v[20:23]\n" \
"buffer_load_dwordx4 v215, %[srdv], s48 offen offset:1024 lds\n" \
"v_mfma_f32_16x16x32_bf16 v[20:23], v[172:175], a[176:179], v[20:23]\n" \
"ds_read_b64_tr_b16 v[140:141], v225 offset:40968\n" \
"v_mfma_f32_16x16x32_bf16 v[52:55], v[168:171], a[164:167], v[52:55]\n" \
"ds_read_b64_tr_b16 v[142:143], v225 offset:41224\n" \
"v_mfma_f32_16x16x32_bf16 v[52:55], v[172:175], a[180:183], v[52:55]\n" \
"v_mfma_f32_16x16x32_bf16 v[84:87], v[168:171], a[168:171], v[84:87]\n" \
"ds_read_b64_tr_b16 v[144:145], v225 offset:33280\n" \
"v_mfma_f32_16x16x32_bf16 v[84:87], v[172:175], a[184:187], v[84:87]\n" \
"ds_read_b64_tr_b16 v[146:147], v225 offset:33536\n" \
"v_mfma_f32_16x16x32_bf16 v[116:119], v[168:171], a[172:175], v[116:119]\n" \
"s_mov_b32 s48, 16384\n" \
"s_mov_b32 m0, s41\n" \
"s_nop 0\n" \
"buffer_load_dwordx4 %[kd0], %[srdk], s48 offen offset:0 lds\n" \
"v_mfma_f32_16x16x32_bf16 v[116:119], v[172:175], a[188:191], v[116:119]\n" \
"ds_read_b64_tr_b16 v[148:149], v225 offset:41472\n" \
"v_mfma_f32_16x16x32_bf16 v[24:27], v[176:179], a[160:163], v[24:27]\n" \
"ds_read_b64_tr_b16 v[150:151], v225 offset:41728\n" \
"v_mfma_f32_16x16x32_bf16 v[24:27], v[180:183], a[176:179], v[24:27]\n" \
"v_mfma_f32_16x16x32_bf16 v[56:59], v[176:179], a[164:167], v[56:59]\n" \
"ds_read_b64_tr_b16 v[152:153], v225 offset:33288\n" \
"v_mfma_f32_16x16x32_bf16 v[56:59], v[180:183], a[180:183], v[56:59]\n" \
"ds_read_b64_tr_b16 v[154:155], v225 offset:33544\n" \
"v_mfma_f32_16x16x32_bf16 v[88:91], v[176:179], a[168:171], v[88:91]\n" \
"buffer_load_dwordx4 v212, %[srdk], s48 offen offset:1024 lds\n" \
"v_mfma_f32_16x16x32_bf16 v[88:91], v[180:183], a[184:187], v[88:91]\n" \
"ds_read_b64_tr_b16 v[156:157], v225 offset:41480\n" \
"v_mfma_f32_16x16x32_bf16 v[120:123], v[176:179], a[172:175], v[120:123]\n" \
"ds_read_b64_tr_b16 v[158:159], v225 offset:41736\n" \
"v_mfma_f32_16x16x32_bf16 v[120:123], v[180:183], a[188:191], v[120:123]\n" \
"v_mfma_f32_16x16x32_bf16 v[28:31], v[184:187], a[160:163], v[28:31]\n" \
"buffer_load_dwordx4 v213, %[srdk], s48 offen offset:2048 lds\n" \
"v_mfma_f32_16x16x32_bf16 v[28:31], v[188:191], a[176:179], v[28:31]\n" \
"v_mfma_f32_16x16x32_bf16 v[60:63], v[184:187], a[164:167], v[60:63]\n" \
"v_mfma_f32_16x16x32_bf16 v[60:63], v[188:191], a[180:183], v[60:63]\n" \
"v_mfma_f32_16x16x32_bf16 v[92:95], v[184:187], a[168:171], v[92:95]\n" \
"buffer_load_dwordx4 v214, %[srdk], s48 offen offset:3072 lds\n" \
"v_mfma_f32_16x16x32_bf16 v[92:95], v[188:191], a[184:187], v[92:95]\n" \
"v_mfma_f32_16x16x32_bf16 v[124:127], v[184:187], a[172:175], v[124:127]\n" \
"v_mfma_f32_16x16x32_bf16 v[124:127], v[188:191], a[188:191], v[124:127]\n" \
"s_waitcnt vmcnt(24) lgkmcnt(0)\n" \
"v_mfma_f32_16x16x32_bf16 v[0:3], v[128:131], a[192:195], v[0:3]\n" \
"ds_read_b64_tr_b16 v[160:161], v225 offset:33792\n" \
"v_mfma_f32_16x16x32_bf16 v[0:3], v[132:135], a[208:211], v[0:3]\n" \
"ds_read_b64_tr_b16 v[162:163], v225 offset:34048\n" \
"v_mfma_f32_16x16x32_bf16 v[32:35], v[128:131], a[196:199], v[32:35]\n" \
"s_mov_b32 s48, 32768\n" \
"s_mov_b32 m0, s45\n" \
"s_nop 0\n" \
"buffer_load_dwordx4 %[vd0], %[srdv], s48 offen offset:2048 lds\n" \
"v_mfma_f32_16x16x32_bf16 v[32:35], v[132:135], a[212:215], v[32:35]\n" \
"ds_read_b64_tr_b16 v[164:165], v225 offset:41984\n" \
"v_mfma_f32_16x16x32_bf16 v[64:67], v[128:131], a[200:203], v[64:67]\n" \
"ds_read_b64_tr_b16 v[166:167], v225 offset:42240\n" \
"v_mfma_f32_16x16x32_bf16 v[64:67], v[132:135], a[216:219], v[64:67]\n" \
"v_mfma_f32_16x16x32_bf16 v[96:99], v[128:131], a[204:207], v[96:99]\n" \
"ds_read_b64_tr_b16 v[168:169], v225 offset:33800\n" \
"v_mfma_f32_16x16x32_bf16 v[96:99], v[132:135], a[220:223], v[96:99]\n" \
"ds_read_b64_tr_b16 v[170:171], v225 offset:34056\n" \
"v_mfma_f32_16x16x32_bf16 v[4:7], v[136:139], a[192:195], v[4:7]\n" \
"buffer_load_dwordx4 v215, %[srdv], s48 offen offset:3072 lds\n" \
"v_mfma_f32_16x16x32_bf16 v[4:7], v[140:143], a[208:211], v[4:7]\n" \
"ds_read_b64_tr_b16 v[172:173], v225 offset:41992\n" \
"v_mfma_f32_16x16x32_bf16 v[36:39], v[136:139], a[196:199], v[36:39]\n" \
"ds_read_b64_tr_b16 v[174:175], v225 offset:42248\n" \
"v_mfma_f32_16x16x32_bf16 v[36:39], v[140:143], a[212:215], v[36:39]\n" \
"v_mfma_f32_16x16x32_bf16 v[68:71], v[136:139], a[200:203], v[68:71]\n" \
"ds_read_b64_tr_b16 v[176:177], v225 offset:34304\n" \
"v_mfma_f32_16x16x32_bf16 v[68:71], v[140:143], a[216:219], v[68:71]\n" \
"ds_read_b64_tr_b16 v[178:179], v225 offset:34560\n" \
"v_mfma_f32_16x16x32_bf16 v[100:103], v[136:139], a[204:207], v[100:103]\n" \
"s_mov_b32 s48, 32768\n" \
"s_mov_b32 m0, s42\n" \
"s_nop 0\n" \
"buffer_load_dwordx4 %[kd0], %[srdk], s48 offen offset:0 lds\n" \
"v_mfma_f32_16x16x32_bf16 v[100:103], v[140:143], a[220:223], v[100:103]\n" \
"ds_read_b64_tr_b16 v[180:181], v225 offset:42496\n" \
"v_mfma_f32_16x16x32_bf16 v[8:11], v[144:147], a[192:195], v[8:11]\n" \
"ds_read_b64_tr_b16 v[182:183], v225 offset:42752\n" \
"v_mfma_f32_16x16x32_bf16 v[8:11], v[148:151], a[208:211], v[8:11]\n" \
"v_mfma_f32_16x16x32_bf16 v[40:43], v[144:147], a[196:199], v[40:43]\n" \
"ds_read_b64_tr_b16 v[184:185], v225 offset:34312\n" \
"v_mfma_f32_16x16x32_bf16 v[40:43], v[148:151], a[212:215], v[40:43]\n" \
"ds_read_b64_tr_b16 v[186:187], v225 offset:34568\n" \
"v_mfma_f32_16x16x32_bf16 v[72:75], v[144:147], a[200:203], v[72:75]\n" \
"buffer_load_dwordx4 v212, %[srdk], s48 offen offset:1024 lds\n" \
"v_mfma_f32_16x16x32_bf16 v[72:75], v[148:151], a[216:219], v[72:75]\n" \
"ds_read_b64_tr_b16 v[188:189], v225 offset:42504\n" \
"v_mfma_f32_16x16x32_bf16 v[104:107], v[144:147], a[204:207], v[104:107]\n" \
"ds_read_b64_tr_b16 v[190:191], v225 offset:42760\n" \
"v_mfma_f32_16x16x32_bf16 v[104:107], v[148:151], a[220:223], v[104:107]\n" \
"v_mfma_f32_16x16x32_bf16 v[12:15], v[152:155], a[192:195], v[12:15]\n" \
"buffer_load_dwordx4 v213, %[srdk], s48 offen offset:2048 lds\n" \
"v_mfma_f32_16x16x32_bf16 v[12:15], v[156:159], a[208:211], v[12:15]\n" \
"v_mfma_f32_16x16x32_bf16 v[44:47], v[152:155], a[196:199], v[44:47]\n" \
"v_mfma_f32_16x16x32_bf16 v[44:47], v[156:159], a[212:215], v[44:47]\n" \
"v_mfma_f32_16x16x32_bf16 v[76:79], v[152:155], a[200:203], v[76:79]\n" \
"buffer_load_dwordx4 v214, %[srdk], s48 offen offset:3072 lds\n" \
"v_mfma_f32_16x16x32_bf16 v[76:79], v[156:159], a[216:219], v[76:79]\n" \
"v_mfma_f32_16x16x32_bf16 v[108:111], v[152:155], a[204:207], v[108:111]\n" \
"v_mfma_f32_16x16x32_bf16 v[108:111], v[156:159], a[220:223], v[108:111]\n" \
"s_waitcnt vmcnt(28) lgkmcnt(0)\n" \
"s_barrier\n" \
"v_mfma_f32_16x16x32_bf16 v[16:19], v[160:163], a[192:195], v[16:19]\n" \
"ds_read_b64_tr_b16 v[128:129], v225 offset:49152\n" \
"s_mov_b32 s54, 0x4000\n" \
"v_mfma_f32_16x16x32_bf16 v[16:19], v[164:167], a[208:211], v[16:19]\n" \
"ds_read_b64_tr_b16 v[130:131], v225 offset:49408\n" \
"v_mfma_f32_16x16x32_bf16 v[48:51], v[160:163], a[196:199], v[48:51]\n" \
"s_mov_b32 s48, 49152\n" \
"s_mov_b32 m0, s46\n" \
"s_nop 0\n" \
"buffer_load_dwordx4 %[vd0], %[srdv], s48 offen offset:0 lds\n" \
"v_mbcnt_lo_u32_b32 v231, -1, 0\n" \
"v_mfma_f32_16x16x32_bf16 v[48:51], v[164:167], a[212:215], v[48:51]\n" \
"ds_read_b64_tr_b16 v[132:133], v225 offset:57344\n" \
"v_mfma_f32_16x16x32_bf16 v[80:83], v[160:163], a[200:203], v[80:83]\n" \
"ds_read_b64_tr_b16 v[134:135], v225 offset:57600\n" \
"v_mfma_f32_16x16x32_bf16 v[80:83], v[164:167], a[216:219], v[80:83]\n" \
"v_mbcnt_hi_u32_b32 v231, -1, v231\n" \
"v_mfma_f32_16x16x32_bf16 v[112:115], v[160:163], a[204:207], v[112:115]\n" \
"ds_read_b64_tr_b16 v[136:137], v225 offset:49160\n" \
"v_mfma_f32_16x16x32_bf16 v[112:115], v[164:167], a[220:223], v[112:115]\n" \
"ds_read_b64_tr_b16 v[138:139], v225 offset:49416\n" \
"v_mfma_f32_16x16x32_bf16 v[20:23], v[168:171], a[192:195], v[20:23]\n" \
"buffer_load_dwordx4 v215, %[srdv], s48 offen offset:1024 lds\n" \
"v_lshrrev_b32_e32 v232, 4, v231\n" \
"v_mfma_f32_16x16x32_bf16 v[20:23], v[172:175], a[208:211], v[20:23]\n" \
"ds_read_b64_tr_b16 v[140:141], v225 offset:57352\n" \
"v_mfma_f32_16x16x32_bf16 v[52:55], v[168:171], a[196:199], v[52:55]\n" \
"ds_read_b64_tr_b16 v[142:143], v225 offset:57608\n" \
"v_xor_b32_e64 v232, v232, v231\n" \
"v_mfma_f32_16x16x32_bf16 v[52:55], v[172:175], a[212:215], v[52:55]\n" \
"v_mfma_f32_16x16x32_bf16 v[84:87], v[168:171], a[200:203], v[84:87]\n" \
"ds_read_b64_tr_b16 v[144:145], v225 offset:49664\n" \
"v_mfma_f32_16x16x32_bf16 v[84:87], v[172:175], a[216:219], v[84:87]\n" \
"ds_read_b64_tr_b16 v[146:147], v225 offset:49920\n" \
"v_bfe_i32 v232, v232, 1, 1\n" \
"v_mfma_f32_16x16x32_bf16 v[116:119], v[168:171], a[204:207], v[116:119]\n" \
"s_mov_b32 s48, 49152\n" \
"s_mov_b32 m0, s43\n" \
"s_nop 0\n" \
"buffer_load_dwordx4 %[kd0], %[srdk], s48 offen offset:0 lds\n" \
"v_mfma_f32_16x16x32_bf16 v[116:119], v[172:175], a[220:223], v[116:119]\n" \
"ds_read_b64_tr_b16 v[148:149], v225 offset:57856\n" \
"v_mfma_f32_16x16x32_bf16 v[24:27], v[176:179], a[192:195], v[24:27]\n" \
"ds_read_b64_tr_b16 v[150:151], v225 offset:58112\n" \
"v_not_b32_e64 v232, v232\n" \
"v_mfma_f32_16x16x32_bf16 v[24:27], v[180:183], a[208:211], v[24:27]\n" \
"v_mfma_f32_16x16x32_bf16 v[56:59], v[176:179], a[196:199], v[56:59]\n" \
"ds_read_b64_tr_b16 v[152:153], v225 offset:49672\n" \
"v_mfma_f32_16x16x32_bf16 v[56:59], v[180:183], a[212:215], v[56:59]\n" \
"ds_read_b64_tr_b16 v[154:155], v225 offset:49928\n" \
"v_and_b32_e32 v230, 0x3f803f80, v232\n" \
"v_mfma_f32_16x16x32_bf16 v[88:91], v[176:179], a[200:203], v[88:91]\n" \
"buffer_load_dwordx4 v212, %[srdk], s48 offen offset:1024 lds\n" \
"v_mfma_f32_16x16x32_bf16 v[88:91], v[180:183], a[216:219], v[88:91]\n" \
"ds_read_b64_tr_b16 v[156:157], v225 offset:57864\n" \
"v_readlane_b32 s62, %[tlo], 18\n" \
"v_mfma_f32_16x16x32_bf16 v[120:123], v[176:179], a[204:207], v[120:123]\n" \
"ds_read_b64_tr_b16 v[158:159], v225 offset:58120\n" \
"v_mfma_f32_16x16x32_bf16 v[120:123], v[180:183], a[220:223], v[120:123]\n" \
"v_mfma_f32_16x16x32_bf16 v[28:31], v[184:187], a[192:195], v[28:31]\n" \
"buffer_load_dwordx4 v213, %[srdk], s48 offen offset:2048 lds\n" \
"v_readlane_b32 s61, %[tlo], 21\n" \
"v_mfma_f32_16x16x32_bf16 v[28:31], v[188:191], a[208:211], v[28:31]\n" \
"v_mfma_f32_16x16x32_bf16 v[60:63], v[184:187], a[196:199], v[60:63]\n" \
"v_mfma_f32_16x16x32_bf16 v[60:63], v[188:191], a[212:215], v[60:63]\n" \
"v_lshlrev_b32_e64 v224, 7, v231\n" \
"v_mfma_f32_16x16x32_bf16 v[92:95], v[184:187], a[200:203], v[92:95]\n" \
"buffer_load_dwordx4 v214, %[srdk], s48 offen offset:3072 lds\n" \
"v_mfma_f32_16x16x32_bf16 v[92:95], v[188:191], a[216:219], v[92:95]\n" \
"v_mfma_f32_16x16x32_bf16 v[124:127], v[184:187], a[204:207], v[124:127]\n" \
"v_mfma_f32_16x16x32_bf16 v[124:127], v[188:191], a[220:223], v[124:127]\n" \
"s_waitcnt vmcnt(24) lgkmcnt(0)\n" \
"v_mfma_f32_16x16x32_bf16 v[0:3], v[128:131], a[224:227], v[0:3]\n" \
"ds_read_b64_tr_b16 v[160:161], v225 offset:50176\n" \
"v_accvgpr_write_b32 a192, v230\n" \
"v_mfma_f32_16x16x32_bf16 v[0:3], v[132:135], a[240:243], v[0:3]\n" \
"ds_read_b64_tr_b16 v[162:163], v225 offset:50432\n" \
"v_mfma_f32_16x16x32_bf16 v[32:35], v[128:131], a[228:231], v[32:35]\n" \
"s_mov_b32 s48, 49152\n" \
"s_mov_b32 m0, s46\n" \
"s_nop 0\n" \
"buffer_load_dwordx4 %[vd0], %[srdv], s48 offen offset:2048 lds\n" \
"v_accvgpr_write_b32 a193, v230\n" \
"v_mfma_f32_16x16x32_bf16 v[32:35], v[132:135], a[244:247], v[32:35]\n" \
"ds_read_b64_tr_b16 v[164:165], v225 offset:58368\n" \
"v_mfma_f32_16x16x32_bf16 v[64:67], v[128:131], a[232:235], v[64:67]\n" \
"ds_read_b64_tr_b16 v[166:167], v225 offset:58624\n" \
"v_accvgpr_write_b32 a194, v230\n" \
"v_mfma_f32_16x16x32_bf16 v[64:67], v[132:135], a[248:251], v[64:67]\n" \
"v_mfma_f32_16x16x32_bf16 v[96:99], v[128:131], a[236:239], v[96:99]\n" \
"ds_read_b64_tr_b16 v[168:169], v225 offset:50184\n" \
"v_accvgpr_write_b32 a195, v230\n" \
"v_mfma_f32_16x16x32_bf16 v[96:99], v[132:135], a[252:255], v[96:99]\n" \
"ds_read_b64_tr_b16 v[170:171], v225 offset:50440\n" \
"v_mfma_f32_16x16x32_bf16 v[4:7], v[136:139], a[224:227], v[4:7]\n" \
"buffer_load_dwordx4 v215, %[srdv], s48 offen offset:3072 lds\n" \
"v_accvgpr_write_b32 a196, -1\n" \
"v_mfma_f32_16x16x32_bf16 v[4:7], v[140:143], a[240:243], v[4:7]\n" \
"ds_read_b64_tr_b16 v[172:173], v225 offset:58376\n" \
"v_mfma_f32_16x16x32_bf16 v[36:39], v[136:139], a[228:231], v[36:39]\n" \
"ds_read_b64_tr_b16 v[174:175], v225 offset:58632\n" \
"v_accvgpr_write_b32 a197, -1\n" \
"v_mfma_f32_16x16x32_bf16 v[36:39], v[140:143], a[244:247], v[36:39]\n" \
"v_mfma_f32_16x16x32_bf16 v[68:71], v[136:139], a[232:235], v[68:71]\n" \
"ds_read_b64_tr_b16 v[176:177], v225 offset:50688\n" \
"v_accvgpr_write_b32 a198, -1\n" \
"v_mfma_f32_16x16x32_bf16 v[68:71], v[140:143], a[248:251], v[68:71]\n" \
"ds_read_b64_tr_b16 v[178:179], v225 offset:50944\n" \
"v_mfma_f32_16x16x32_bf16 v[100:103], v[136:139], a[236:239], v[100:103]\n" \
"v_accvgpr_write_b32 a199, -1\n" \
"v_mfma_f32_16x16x32_bf16 v[100:103], v[140:143], a[252:255], v[100:103]\n" \
"ds_read_b64_tr_b16 v[180:181], v225 offset:58880\n" \
"v_mfma_f32_16x16x32_bf16 v[8:11], v[144:147], a[224:227], v[8:11]\n" \
"ds_read_b64_tr_b16 v[182:183], v225 offset:59136\n" \
"v_lshrrev_b32_e32 v231, 3, v224\n" \
"v_mfma_f32_16x16x32_bf16 v[8:11], v[148:151], a[240:243], v[8:11]\n" \
"v_mfma_f32_16x16x32_bf16 v[40:43], v[144:147], a[228:231], v[40:43]\n" \
"ds_read_b64_tr_b16 v[184:185], v225 offset:50696\n" \
"v_mov_b32_e64 v232, 0\n" \
"v_mfma_f32_16x16x32_bf16 v[40:43], v[148:151], a[244:247], v[40:43]\n" \
"ds_read_b64_tr_b16 v[186:187], v225 offset:50952\n" \
"v_mfma_f32_16x16x32_bf16 v[72:75], v[144:147], a[232:235], v[72:75]\n" \
"v_mov_b32_e64 v233, 0\n" \
"v_mfma_f32_16x16x32_bf16 v[72:75], v[148:151], a[248:251], v[72:75]\n" \
"ds_read_b64_tr_b16 v[188:189], v225 offset:58888\n" \
"v_mfma_f32_16x16x32_bf16 v[104:107], v[144:147], a[236:239], v[104:107]\n" \
"ds_read_b64_tr_b16 v[190:191], v225 offset:59144\n" \
"v_mov_b32_e64 v234, 0\n" \
"v_mfma_f32_16x16x32_bf16 v[104:107], v[148:151], a[252:255], v[104:107]\n" \
"v_mfma_f32_16x16x32_bf16 v[12:15], v[152:155], a[224:227], v[12:15]\n" \
"v_mov_b32_e64 v235, 0\n" \
"v_mfma_f32_16x16x32_bf16 v[12:15], v[156:159], a[240:243], v[12:15]\n" \
"v_mfma_f32_16x16x32_bf16 v[44:47], v[152:155], a[228:231], v[44:47]\n" \
"buffer_store_dwordx4 v[232:235], v231, %[srdz], s61 offen offset:0 sc1\n" \
"v_mfma_f32_16x16x32_bf16 v[44:47], v[156:159], a[244:247], v[44:47]\n" \
"v_mfma_f32_16x16x32_bf16 v[76:79], v[152:155], a[232:235], v[76:79]\n" \
"buffer_store_dwordx4 v[232:235], v231, %[srdz], s61 offen offset:1024 sc1\n" \
"v_mfma_f32_16x16x32_bf16 v[76:79], v[156:159], a[248:251], v[76:79]\n" \
"v_mfma_f32_16x16x32_bf16 v[108:111], v[152:155], a[236:239], v[108:111]\n" \
"v_mfma_f32_16x16x32_bf16 v[108:111], v[156:159], a[252:255], v[108:111]\n" \
"s_waitcnt vmcnt(22) lgkmcnt(0)\n" \
"s_barrier\n" \
"v_mfma_f32_16x16x32_bf16 v[16:19], v[160:163], a[224:227], v[16:19]\n" \
"ds_read_b128 a[64:67], %[kb0] offset:0\n" \
"v_mfma_f32_16x16x32_bf16 v[16:19], v[164:167], a[240:243], v[16:19]\n" \
"v_mfma_f32_16x16x32_bf16 v[20:23], v[168:171], a[224:227], v[20:23]\n" \
"v_cvt_pk_bf16_f32 v230, v0, v1\n" \
"v_cvt_pk_bf16_f32 v231, v2, v3\n" \
"v_cvt_pk_bf16_f32 v232, v4, v5\n" \
"ds_read_b128 a[68:71], v208 offset:0\n" \
"v_mfma_f32_16x16x32_bf16 v[20:23], v[172:175], a[240:243], v[20:23]\n" \
"v_cvt_pk_bf16_f32 v233, v6, v7\n" \
"v_accvgpr_write_b32 a0, v230\n" \
"v_accvgpr_write_b32 a1, v231\n" \
"v_mfma_f32_16x16x32_bf16 v[24:27], v[176:179], a[224:227], v[24:27]\n" \
"v_accvgpr_write_b32 a2, v232\n" \
"v_accvgpr_write_b32 a3, v233\n" \
"v_cvt_pk_bf16_f32 v234, v8, v9\n" \
"ds_read_b128 a[72:75], v209 offset:0\n" \
"v_mfma_f32_16x16x32_bf16 v[24:27], v[180:183], a[240:243], v[24:27]\n" \
"v_cvt_pk_bf16_f32 v235, v10, v11\n" \
"v_cvt_pk_bf16_f32 v236, v12, v13\n" \
"v_cvt_pk_bf16_f32 v237, v14, v15\n" \
"v_mfma_f32_16x16x32_bf16 v[28:31], v[184:187], a[224:227], v[28:31]\n" \
"v_accvgpr_write_b32 a4, v234\n" \
"v_accvgpr_write_b32 a5, v235\n" \
"v_accvgpr_write_b32 a6, v236\n" \
".if . - adaln_body != 8192\n" \
".error \"size model\"\n" \
".endif\n" \
"Lk1_%=:\n" \
"s_cbranch_execz Lk2_%=\n" \
"s_nop 0\n" \
"ds_read_b128 a[76:79], v210 offset:0\n" \
"v_mfma_f32_16x16x32_bf16 v[28:31], v[188:191], a[240:243], v[28:31]\n" \
"v_accvgpr_write_b32 a7, v237\n" \
"v_cvt_pk_bf16_f32 v230, v32, v33\n" \
"v_cvt_pk_bf16_f32 v231, v34, v35\n" \
"v_mfma_f32_16x16x32_bf16 v[48:51], v[160:163], a[228:231], v[48:51]\n" \
"v_cvt_pk_bf16_f32 v232, v36, v37\n" \
"v_cvt_pk_bf16_f32 v233, v38, v39\n" \
"v_accvgpr_write_b32 a16, v230\n" \
"ds_read_b128 a[80:83], %[kb0] offset:4096\n" \
"v_mfma_f32_16x16x32_bf16 v[48:51], v[164:167], a[244:247], v[48:51]\n" \
"v_accvgpr_write_b32 a17, v231\n" \
"v_accvgpr_write_b32 a18, v232\n" \
"v_accvgpr_write_b32 a19, v233\n" \
"v_mfma_f32_16x16x32_bf16 v[52:55], v[168:171], a[228:231], v[52:55]\n" \
"v_cvt_pk_bf16_f32 v234, v40, v41\n" \
"v_cvt_pk_bf16_f32 v235, v42, v43\n" \
"v_cvt_pk_bf16_f32 v236, v44, v45\n" \
"ds_read_b128 a[84:87], v208 offset:4096\n" \
"v_mfma_f32_16x16x32_bf16 v[52:55], v[172:175], a[244:247], v[52:55]\n" \
"v_cvt_pk_bf16_f32 v237, v46, v47\n" \
"v_accvgpr_write_b32 a20, v234\n" \
"v_accvgpr_write_b32 a21, v235\n" \
"v_mfma_f32_16x16x32_bf16 v[56:59], v[176:179], a[228:231], v[56:59]\n" \
"v_accvgpr_write_b32 a22, v236\n" \
"v_accvgpr_write_b32 a23, v237\n" \
"v_cvt_pk_bf16_f32 v230, v16, v17\n" \
"ds_read_b128 a[88:91], v209 offset:4096\n" \
"v_mfma_f32_16x16x32_bf16 v[56:59], v[180:183], a[244:247], v[56:59]\n" \
"v_cvt_pk_bf16_f32 v231, v18, v19\n" \
"v_cvt_pk_bf16_f32 v232, v20, v21\n" \
"v_cvt_pk_bf16_f32 v233, v22, v23\n" \
"v_mfma_f32_16x16x32_bf16 v[60:63], v[184:187], a[228:231], v[60:63]\n" \
"v_accvgpr_write_b32 a8, v230\n" \
"v_accvgpr_write_b32 a9, v231\n" \
"v_accvgpr_write_b32 a10, v232\n" \
"ds_read_b128 a[92:95], v210 offset:4096\n" \
"v_mfma_f32_16x16x32_bf16 v[60:63], v[188:191], a[244:247], v[60:63]\n" \
"v_accvgpr_write_b32 a11, v233\n" \
"v_cvt_pk_bf16_f32 v234, v24, v25\n" \
"v_cvt_pk_bf16_f32 v235, v26, v27\n" \
"v_mfma_f32_16x16x32_bf16 v[80:83], v[160:163], a[232:235], v[80:83]\n" \
"v_cvt_pk_bf16_f32 v236, v28, v29\n" \
"v_cvt_pk_bf16_f32 v237, v30, v31\n" \
"v_accvgpr_write_b32 a12, v234\n" \
"v_mfma_f32_16x16x32_bf16 v[80:83], v[164:167], a[248:251], v[80:83]\n" \
"v_accvgpr_write_b32 a13, v235\n" \
"v_accvgpr_write_b32 a14, v236\n" \
"v_accvgpr_write_b32 a15, v237\n" \
"v_mfma_f32_16x16x32_bf16 v[84:87], v[168:171], a[232:235], v[84:87]\n" \
"v_cvt_pk_bf16_f32 v230, v64, v65\n" \
"v_cvt_pk_bf16_f32 v231, v66, v67\n" \
"v_cvt_pk_bf16_f32 v232, v68, v69\n" \
"v_mfma_f32_16x16x32_bf16 v[84:87], v[172:175], a[248:251], v[84:87]\n" \
"v_cvt_pk_bf16_f32 v233, v70, v71\n" \
"v_accvgpr_write_b32 a32, v230\n" \
"v_accvgpr_write_b32 a33, v231\n" \
"v_mfma_f32_16x16x32_bf16 v[88:91], v[176:179], a[232:235], v[88:91]\n" \
"v_accvgpr_write_b32 a34, v232\n" \
"v_accvgpr_write_b32 a35, v233\n" \
"v_cvt_pk_bf16_f32 v234, v72, v73\n" \
"v_mfma_f32_16x16x32_bf16 v[88:91], v[180:183], a[248:251], v[88:91]\n" \
"v_cvt_pk_bf16_f32 v235, v74, v75\n" \
"v_cvt_pk_bf16_f32 v236, v76, v77\n" \
"v_cvt_pk_bf16_f32 v237, v78, v79\n" \
"v_mfma_f32_16x16x32_bf16 v[92:95], v[184:187], a[232:235], v[92:95]\n" \
"v_accvgpr_write_b32 a36, v234\n" \
"v_accvgpr_write_b32 a37, v235\n" \
"v_accvgpr_write_b32 a38, v236\n" \
"v_mfma_f32_16x16x32_bf16 v[92:95], v[188:191], a[248:251], v[92:95]\n" \
"v_accvgpr_write_b32 a39, v237\n" \
"v_cvt_pk_bf16_f32 v230, v48, v49\n" \
"v_cvt_pk_bf16_f32 v231, v50, v51\n" \
"v_mfma_f32_16x16x32_bf16 v[112:115], v[160:163], a[236:239], v[112:115]\n" \
"v_cvt_pk_bf16_f32 v232, v52, v53\n" \
"v_cvt_pk_bf16_f32 v233, v54, v55\n" \
"v_accvgpr_write_b32 a24, v230\n" \
"v_mfma_f32_16x16x32_bf16 v[112:115], v[164:167], a[252:255], v[112:115]\n" \
"v_accvgpr_write_b32 a25, v231\n" \
"v_accvgpr_write_b32 a26, v232\n" \
"v_accvgpr_write_b32 a27, v233\n" \
"v_mfma_f32_16x16x32_bf16 v[116:119], v[168:171], a[236:239], v[116:119]\n" \
"v_cvt_pk_bf16_f32 v234, v56, v57\n" \
"v_cvt_pk_bf16_f32 v235, v58, v59\n" \
"v_cvt_pk_bf16_f32 v236, v60, v61\n" \
"v_mfma_f32_16x16x32_bf16 v[116:119], v[172:175], a[252:255], v[116:119]\n" \
"v_cvt_pk_bf16_f32 v237, v62, v63\n" \
"v_accvgpr_write_b32 a28, v234\n" \
"v_accvgpr_write_b32 a29, v235\n" \
"v_mfma_f32_16x16x32_bf16 v[120:123], v[176:179], a[236:239], v[120:123]\n" \
"v_accvgpr_write_b32 a30, v236\n" \
"v_accvgpr_write_b32 a31, v237\n" \
"v_cvt_pk_bf16_f32 v230, v96, v97\n" \
"v_mfma_f32_16x16x32_bf16 v[120:123], v[180:183], a[252:255], v[120:123]\n" \
"v_cvt_pk_bf16_f32 v231, v98, v99\n" \
"v_cvt_pk_bf16_f32 v232, v100, v101\n" \
"v_cvt_pk_bf16_f32 v233, v102, v103\n" \
"v_mfma_f32_16x16x32_bf16 v[124:127], v[184:187], a[236:239], v[124:127]\n" \
"v_accvgpr_write_b32 a48, v230\n" \
"v_accvgpr_write_b32 a49, v231\n" \
"v_accvgpr_write_b32 a50, v232\n" \
"v_mfma_f32_16x16x32_bf16 v[124:127], v[188:191], a[252:255], v[124:127]\n" \
"v_accvgpr_write_b32 a51, v233\n" \
"s_waitcnt vmcnt(63) lgkmcnt(0)\n" \
"s_nop 0\n" \
"s_mov_b32 s48, 65536\n" \
"s_mov_b32 s60, 0\n" \
"s_waitcnt lgkmcnt(0)\n" \
"v_mfma_f32_16x16x32_bf16 v[128:131], a[64:67], a[0:3], 0\n" \
"ds_read_b128 a[96:99], %[kb0] offset:8192\n" \
"v_cvt_pk_bf16_f32 v234, v80, v81\n" \
"v_cvt_pk_bf16_f32 v235, v82, v83\n" \
"v_mfma_f32_16x16x32_bf16 v[128:131], a[68:71], a[4:7], v[128:131]\n" \
"ds_read_b128 a[100:103], v208 offset:8192\n" \
"v_cvt_pk_bf16_f32 v236, v84, v85\n" \
"v_cvt_pk_bf16_f32 v237, v86, v87\n" \
"v_mfma_f32_16x16x32_bf16 v[128:131], a[72:75], a[8:11], v[128:131]\n" \
"ds_read_b128 a[104:107], v209 offset:8192\n" \
"v_accvgpr_write_b32 a40, v234\n" \
"v_accvgpr_write_b32 a41, v235\n" \
"v_mfma_f32_16x16x32_bf16 v[128:131], a[76:79], a[12:15], v[128:131]\n" \
"ds_read_b128 a[108:111], v210 offset:8192\n" \
"v_accvgpr_write_b32 a42, v236\n" \
"v_accvgpr_write_b32 a43, v237\n" \
"v_mfma_f32_16x16x32_bf16 v[132:135], a[80:83], a[0:3], 0\n" \
"ds_read_b128 a[112:115], %[kb0] offset:12288\n" \
"v_cvt_pk_bf16_f32 v230, v88, v89\n" \
"v_cvt_pk_bf16_f32 v231, v90, v91\n" \
"v_mfma_f32_16x16x32_bf16 v[132:135], a[84:87], a[4:7], v[132:135]\n" \
"ds_read_b128 a[116:119], v208 offset:12288\n" \
"v_cvt_pk_bf16_f32 v232, v92, v93\n" \
"v_cvt_pk_bf16_f32 v233, v94, v95\n" \
"v_mfma_f32_16x16x32_bf16 v[132:135], a[88:91], a[8:11], v[132:135]\n" \
"ds_read_b128 a[120:123], v209 offset:12288\n" \
"v_accvgpr_write_b32 a44, v230\n" \
"v_accvgpr_write_b32 a45, v231\n" \
"v_mfma_f32_16x16x32_bf16 v[132:135], a[92:95], a[12:15], v[132:135]\n" \
"ds_read_b128 a[124:127], v210 offset:12288\n" \
"v_accvgpr_write_b32 a46, v232\n" \
"v_accvgpr_write_b32 a47, v233\n" \
"v_mfma_f32_16x16x32_bf16 v[136:139], a[64:67], a[16:19], 0\n" \
"ds_read_b64_tr_b16 a[128:129], %[vvb] offset:0\n" \
"v_cvt_pk_bf16_f32 v234, v104, v105\n" \
"v_cvt_pk_bf16_f32 v235, v106, v107\n" \
"v_mfma_f32_16x16x32_bf16 v[136:139], a[68:71], a[20:23], v[136:139]\n" \
"ds_read_b64_tr_b16 a[130:131], %[vvb] offset:4096\n" \
"v_cvt_pk_bf16_f32 v236, v108, v109\n" \
"v_cvt_pk_bf16_f32 v237, v110, v111\n" \
"v_mfma_f32_16x16x32_bf16 v[136:139], a[72:75], a[24:27], v[136:139]\n" \
"ds_read_b64_tr_b16 a[132:133], v211 offset:0\n" \
"v_accvgpr_write_b32 a52, v234\n" \
"v_accvgpr_write_b32 a53, v235\n" \
"v_mfma_f32_16x16x32_bf16 v[136:139], a[76:79], a[28:31], v[136:139]\n" \
"ds_read_b64_tr_b16 a[134:135], v211 offset:4096\n" \
"v_accvgpr_write_b32 a54, v236\n" \
"v_accvgpr_write_b32 a55, v237\n" \
"v_mfma_f32_16x16x32_bf16 v[140:143], a[80:83], a[16:19], 0\n" \
"ds_read_b64_tr_b16 a[136:137], %[vvb] offset:512\n" \
"v_cvt_pk_bf16_f32 v230, v112, v113\n" \
"v_cvt_pk_bf16_f32 v231, v114, v115\n" \
"v_mfma_f32_16x16x32_bf16 v[140:143], a[84:87], a[20:23], v[140:143]\n" \
"ds_read_b64_tr_b16 a[138:139], %[vvb] offset:4608\n" \
"v_cvt_pk_bf16_f32 v232, v116, v117\n" \
"v_cvt_pk_bf16_f32 v233, v118, v119\n" \
"v_mfma_f32_16x16x32_bf16 v[140:143], a[88:91], a[24:27], v[140:143]\n" \
"ds_read_b64_tr_b16 a[140:141], v211 offset:512\n" \
"v_accvgpr_write_b32 a56, v230\n" \
"v_accvgpr_write_b32 a57, v231\n" \
"v_mfma_f32_16x16x32_bf16 v[140:143], a[92:95], a[28:31], v[140:143]\n" \
"ds_read_b64_tr_b16 a[142:143], v211 offset:4608\n" \
"v_accvgpr_write_b32 a58, v232\n" \
"v_accvgpr_write_b32 a59, v233\n" \
"v_mfma_f32_16x16x32_bf16 v[144:147], a[64:67], a[32:35], 0\n" \
"ds_read_b64_tr_b16 a[144:145], %[vvb] offset:1024\n" \
"v_cvt_pk_bf16_f32 v234, v120, v121\n" \
"v_cvt_pk_bf16_f32 v235, v122, v123\n" \
"v_mfma_f32_16x16x32_bf16 v[144:147], a[68:71], a[36:39], v[144:147]\n" \
"ds_read_b64_tr_b16 a[146:147], %[vvb] offset:5120\n" \
"v_cvt_pk_bf16_f32 v236, v124, v125\n" \
"v_cvt_pk_bf16_f32 v237, v126, v127\n" \
"v_mfma_f32_16x16x32_bf16 v[144:147], a[72:75], a[40:43], v[144:147]\n" \
"ds_read_b64_tr_b16 a[148:149], v211 offset:1024\n" \
"v_accvgpr_write_b32 a60, v234\n" \
"v_accvgpr_write_b32 a61, v235\n" \
"v_mfma_f32_16x16x32_bf16 v[144:147], a[76:79], a[44:47], v[144:147]\n" \
"ds_read_b64_tr_b16 a[150:151], v211 offset:5120\n" \
"v_accvgpr_write_b32 a62, v236\n" \
"v_accvgpr_write_b32 a63, v237\n" \
"v_mfma_f32_16x16x32_bf16 v[148:151], a[80:83], a[32:35], 0\n" \
"ds_read_b64_tr_b16 a[152:153], %[vvb] offset:1536\n" \
"v_max3_f32 v192, v128, v129, v130\n" \
"v_max3_f32 v193, v131, v132, v133\n" \
"v_mfma_f32_16x16x32_bf16 v[148:151], a[84:87], a[36:39], v[148:151]\n" \
"ds_read_b64_tr_b16 a[154:155], %[vvb] offset:5632\n" \
"v_max3_f32 v192, v192, v134, v135\n" \
"v_max_f32_e64 v225, v192, v193\n" \
"v_mfma_f32_16x16x32_bf16 v[148:151], a[88:91], a[40:43], v[148:151]\n" \
"ds_read_b64_tr_b16 a[156:157], v211 offset:1536\n" \
"v_max3_f32 v192, v136, v137, v138\n" \
"v_max3_f32 v193, v139, v140, v141\n" \
"v_mfma_f32_16x16x32_bf16 v[148:151], a[92:95], a[44:47], v[148:151]\n" \
"ds_read_b64_tr_b16 a[158:159], v211 offset:5632\n" \
"v_max3_f32 v192, v192, v142, v143\n" \
"v_max_f32_e64 v226, v192, v193\n" \
"v_mfma_f32_16x16x32_bf16 v[152:155], a[64:67], a[48:51], 0\n" \
"ds_read_b128 a[200:203], v228 offset:0\n" \
"s_mov_b32 s44, %[m0t]\n" \
"s_mov_b32 s45, 0\n" \
"v_mfma_f32_16x16x32_bf16 v[152:155], a[68:71], a[52:55], v[152:155]\n" \
"ds_read_b128 a[204:207], v228 offset:64\n" \
"v_mfma_f32_16x16x32_bf16 v[152:155], a[72:75], a[56:59], v[152:155]\n" \
"ds_read_b128 a[208:211], v228 offset:128\n" \
"v_mfma_f32_16x16x32_bf16 v[152:155], a[76:79], a[60:63], v[152:155]\n" \
"ds_read_b128 a[212:215], v228 offset:192\n" \
"v_mfma_f32_16x16x32_bf16 v[156:159], a[80:83], a[48:51], 0\n" \
"ds_read_b128 a[216:219], v228 offset:256\n" \
"v_mfma_f32_16x16x32_bf16 v[156:159], a[84:87], a[52:55], v[156:159]\n" \
"ds_read_b128 a[220:223], v228 offset:320\n" \
"v_mfma_f32_16x16x32_bf16 v[156:159], a[88:91], a[56:59], v[156:159]\n" \
"ds_read_b128 a[224:227], v228 offset:384\n" \
"v_max3_f32 v192, v144, v145, v146\n" \
"v_max3_f32 v193, v147, v148, v149\n" \
"v_mfma_f32_16x16x32_bf16 v[156:159], a[92:95], a[60:63], v[156:159]\n" \
"ds_read_b128 a[228:231], v228 offset:448\n" \
"v_max3_f32 v192, v192, v150, v151\n" \
"v_max_f32_e64 v227, v192, v193\n" \
"s_nop 3\n" \
"s_nop 0\n" \
"v_max3_f32 v230, v152, v153, v154\n" \
"v_max3_f32 v231, v155, v156, v157\n" \
"v_max3_f32 v230, v230, v158, v159\n" \
"v_max_f32_e32 v228, v230, v231\n" \
"v_mov_b32_e32 v232, v225\n" \
"v_mov_b32_e32 v233, v226\n" \
"v_mov_b32_e32 v234, v227\n" \
"v_mov_b32_e32 v235, v228\n" \
"s_nop 0\n" \
"v_permlane16_swap_b32_e32 v225, v232\n" \
"v_permlane16_swap_b32_e32 v226, v233\n" \
"v_permlane16_swap_b32_e32 v227, v234\n" \
"v_permlane16_swap_b32_e32 v228, v235\n" \
"v_max_f32_e32 v225, v225, v232\n" \
"v_max_f32_e32 v226, v226, v233\n" \
"v_max_f32_e32 v227, v227, v234\n" \
"v_max_f32_e32 v228, v228, v235\n" \
"v_mov_b32_e32 v232, v225\n" \
"v_mov_b32_e32 v233, v226\n" \
"v_mov_b32_e32 v234, v227\n" \
"v_mov_b32_e32 v235, v228\n" \
"s_nop 0\n" \
"v_permlane32_swap_b32_e32 v225, v232\n" \
"v_permlane32_swap_b32_e32 v226, v233\n" \
"v_permlane32_swap_b32_e32 v227, v234\n" \
"v_permlane32_swap_b32_e32 v228, v235\n" \
"v_max_f32_e32 v225, v225, v232\n" \
"v_max_f32_e32 v226, v226, v233\n" \
"v_max_f32_e32 v227, v227, v234\n" \
"v_max_f32_e64 v228, v228, v235\n" \
"Lhavem_%=:\n" \
"s_nop 3\n" \
"v_mov_b64_e32 v[176:177], 0\n" \
"v_mov_b64_e32 v[178:179], 0\n" \
"v_mov_b64_e32 v[180:181], 0\n" \
"v_mov_b64_e32 v[182:183], 0\n" \
"v_sub_f32_e32 v160, 0, v225\n" \
"v_sub_f32_e32 v161, 0, v225\n" \
"v_sub_f32_e32 v162, 0, v225\n" \
"v_sub_f32_e32 v163, 0, v225\n" \
"v_sub_f32_e32 v164, 0, v226\n" \
"v_sub_f32_e32 v165, 0, v226\n" \
"v_sub_f32_e32 v166, 0, v226\n" \
"v_sub_f32_e32 v167, 0, v226\n" \
"v_sub_f32_e32 v168, 0, v227\n" \
"v_sub_f32_e32 v169, 0, v227\n" \
"v_sub_f32_e32 v170, 0, v227\n" \
"v_sub_f32_e32 v171, 0, v227\n" \
"v_sub_f32_e32 v172, 0, v228\n" \
"v_sub_f32_e32 v173, 0, v228\n" \
"v_sub_f32_e32 v174, 0, v228\n" \
"v_sub_f32_e32 v175, 0, v228\n" \
"v_sub_f32_e32 v128, v128, v225\n" \
"v_sub_f32_e32 v129, v129, v225\n" \
"v_sub_f32_e32 v130, v130, v225\n" \
"v_sub_f32_e32 v131, v131, v225\n" \
"v_sub_f32_e32 v132, v132, v225\n" \
"v_sub_f32_e32 v133, v133, v225\n" \
"v_sub_f32_e32 v134, v134, v225\n" \
"v_sub_f32_e32 v135, v135, v225\n" \
"v_sub_f32_e32 v136, v136, v226\n" \
"v_sub_f32_e32 v137, v137, v226\n" \
"v_sub_f32_e32 v138, v138, v226\n" \
"v_sub_f32_e32 v139, v139, v226\n" \
"v_sub_f32_e32 v140, v140, v226\n" \
"v_sub_f32_e32 v141, v141, v226\n" \
"v_sub_f32_e32 v142, v142, v226\n" \
"v_sub_f32_e32 v143, v143, v226\n" \
"v_sub_f32_e32 v144, v144, v227\n" \
"v_sub_f32_e32 v145, v145, v227\n" \
"v_sub_f32_e32 v146, v146, v227\n" \
"v_sub_f32_e32 v147, v147, v227\n" \
"v_sub_f32_e32 v148, v148, v227\n" \
"v_sub_f32_e32 v149, v149, v227\n" \
"v_sub_f32_e32 v150, v150, v227\n" \
"v_sub_f32_e32 v151, v151, v227\n" \
"v_sub_f32_e32 v152, v152, v228\n" \
"v_sub_f32_e32 v153, v153, v228\n" \
"v_sub_f32_e32 v154, v154, v228\n" \
"v_sub_f32_e32 v155, v155, v228\n" \
"v_sub_f32_e32 v156, v156, v228\n" \
"v_sub_f32_e32 v157, v157, v228\n" \
"v_sub_f32_e32 v158, v158, v228\n" \
"v_sub_f32_e32 v159, v159, v228\n" \
"v_exp_f32_e32 v128, v128\n" \
"v_exp_f32_e32 v129, v129\n" \
"v_exp_f32_e32 v130, v130\n" \
"v_exp_f32_e32 v131, v131\n" \
"v_exp_f32_e32 v132, v132\n" \
"v_exp_f32_e32 v133, v133\n" \
"v_exp_f32_e32 v134, v134\n" \
"v_exp_f32_e32 v135, v135\n" \
"v_exp_f32_e32 v136, v136\n" \
"v_exp_f32_e32 v137, v137\n" \
"v_exp_f32_e32 v138, v138\n" \
"v_exp_f32_e32 v139, v139\n" \
"v_exp_f32_e32 v140, v140\n" \
"v_exp_f32_e32 v141, v141\n" \
"v_exp_f32_e32 v142, v142\n" \
"v_exp_f32_e32 v143, v143\n" \
"v_exp_f32_e32 v144, v144\n" \
"v_exp_f32_e32 v145, v145\n" \
"v_exp_f32_e32 v146, v146\n" \
"v_exp_f32_e32 v147, v147\n" \
"v_exp_f32_e32 v148, v148\n" \
"v_exp_f32_e32 v149, v149\n" \
"v_exp_f32_e32 v150, v150\n" \
"v_exp_f32_e32 v151, v151\n" \
"v_exp_f32_e32 v152, v152\n" \
"v_exp_f32_e32 v153, v153\n" \
"v_exp_f32_e32 v154, v154\n" \
"v_exp_f32_e64 v155, v155\n" \
"v_bfe_i32 v228, v224, 7, 1\n" \
"v_and_b32_e32 v228, 0xaaaa, v228\n" \
"v_or_b32_e32 v228, 0x4444, v228\n" \
"v_cvt_pk_bf16_f32 v184, v128, v129\n" \
"v_cvt_pk_bf16_f32 v185, v130, v131\n" \
"v_cvt_pk_bf16_f32 v186, v132, v133\n" \
"v_cvt_pk_bf16_f32 v187, v134, v135\n" \
"v_cvt_pk_bf16_f32 v188, v136, v137\n" \
"v_cvt_pk_bf16_f32 v189, v138, v139\n" \
"v_cvt_pk_bf16_f32 v190, v140, v141\n" \
"v_cvt_pk_bf16_f32 v191, v142, v143\n" \
"v_cvt_pk_bf16_f32 v216, v144, v145\n" \
"v_cvt_pk_bf16_f32 v217, v146, v147\n" \
"v_cvt_pk_bf16_f32 v218, v148, v149\n" \
"v_cvt_pk_bf16_f32 v219, v150, v151\n" \
"v_lshrrev_b32_e64 v235, 3, v224\n" \
"s_waitcnt vmcnt(2) lgkmcnt(0)\n" \
"s_barrier\n" \
"v_mfma_f32_16x16x32_bf16 v[128:131], a[96:99], a[0:3], v[160:163]\n" \
"v_exp_f32_e64 v156, v156\n" \
"v_mfma_f32_16x16x32_bf16 v[128:131], a[100:103], a[4:7], v[128:131]\n" \
"s_add_u32 m0, %[m0b], 114688\n" \
"v_mfma_f32_16x16x32_bf16 v[128:131], a[104:107], a[8:11], v[128:131]\n" \
"v_exp_f32_e64 v157, v157\n" \
"v_mfma_f32_16x16x32_bf16 v[128:131], a[108:111], a[12:15], v[128:131]\n" \
"v_cvt_pk_bf16_f32 v220, v152, v153\n" \
"v_cvt_pk_bf16_f32 v221, v154, v155\n" \
"v_mfma_f32_16x16x32_bf16 v[132:135], a[112:115], a[0:3], v[160:163]\n" \
"v_exp_f32_e64 v158, v158\n" \
"v_mfma_f32_16x16x32_bf16 v[132:135], a[116:119], a[4:7], v[132:135]\n" \
"v_mfma_f32_16x16x32_bf16 v[132:135], a[120:123], a[8:11], v[132:135]\n" \
"buffer_load_dwordx4 %[vd0], %[srdv], s48 offen offset:0 lds\n" \
"v_mfma_f32_16x16x32_bf16 v[132:135], a[124:127], a[12:15], v[132:135]\n" \
"v_exp_f32_e64 v159, v159\n" \
"v_mfma_f32_16x16x32_bf16 v[0:3], a[128:131], v[184:187], 0\n" \
"ds_read_b128 a[64:67], %[kb0] offset:16384\n" \
"v_mfma_f32_16x16x32_bf16 v[32:35], a[128:131], v[188:191], 0\n" \
"v_exp_f32_e64 v128, v128\n" \
"v_mfma_f32_16x16x32_bf16 v[4:7], a[132:135], v[184:187], 0\n" \
"v_cvt_pk_bf16_f32 v222, v156, v157\n" \
"v_cvt_pk_bf16_f32 v223, v158, v159\n" \
"v_mfma_f32_16x16x32_bf16 v[36:39], a[132:135], v[188:191], 0\n" \
"v_exp_f32_e64 v129, v129\n" \
"v_mfma_f32_16x16x32_bf16 v[8:11], a[136:139], v[184:187], 0\n" \
"ds_read_b128 a[68:71], v208 offset:16384\n" \
"v_mfma_f32_16x16x32_bf16 v[40:43], a[136:139], v[188:191], 0\n" \
"v_exp_f32_e64 v130, v130\n" \
"v_mfma_f32_16x16x32_bf16 v[12:15], a[140:143], v[184:187], 0\n" \
"ds_read_b128 a[72:75], v209 offset:16384\n" \
"v_mfma_f32_16x16x32_bf16 v[44:47], a[140:143], v[188:191], 0\n" \
"v_exp_f32_e64 v131, v131\n" \
"v_smfmac_f32_16x16x64_bf16 v[176:179], a[192:195], v[184:191], v228\n" \
"v_mfma_f32_16x16x32_bf16 v[136:139], a[96:99], a[16:19], v[164:167]\n" \
"v_exp_f32_e64 v132, v132\n" \
"v_mfma_f32_16x16x32_bf16 v[136:139], a[100:103], a[20:23], v[136:139]\n" \
"ds_read_b128 a[76:79], v210 offset:16384\n" \
"v_mfma_f32_16x16x32_bf16 v[136:139], a[104:107], a[24:27], v[136:139]\n" \
"v_exp_f32_e64 v133, v133\n" \
"v_mfma_f32_16x16x32_bf16 v[136:139], a[108:111], a[28:31], v[136:139]\n" \
"v_cvt_pk_bf16_f32 v192, v128, v129\n" \
"v_cvt_pk_bf16_f32 v193, v130, v131\n" \
"v_mfma_f32_16x16x32_bf16 v[140:143], a[112:115], a[16:19], v[164:167]\n" \
"v_exp_f32_e64 v134, v134\n" \
"v_mfma_f32_16x16x32_bf16 v[140:143], a[116:119], a[20:23], v[140:143]\n" \
"ds_read_b128 a[80:83], %[kb0] offset:20480\n" \
"v_mfma_f32_16x16x32_bf16 v[140:143], a[120:123], a[24:27], v[140:143]\n" \
"buffer_load_dwordx4 v215, %[srdv], s48 offen offset:1024 lds\n" \
"v_mfma_f32_16x16x32_bf16 v[140:143], a[124:127], a[28:31], v[140:143]\n" \
"v_exp_f32_e64 v135, v135\n" \
"v_mfma_f32_16x16x32_bf16 v[16:19], a[144:147], v[184:187], 0\n" \
"ds_read_b128 a[84:87], v208 offset:20480\n" \
"v_mfma_f32_16x16x32_bf16 v[48:51], a[144:147], v[188:191], 0\n" \
"v_exp_f32_e64 v136, v136\n" \
"v_mfma_f32_16x16x32_bf16 v[20:23], a[148:151], v[184:187], 0\n" \
"v_cvt_pk_bf16_f32 v194, v132, v133\n" \
"v_cvt_pk_bf16_f32 v195, v134, v135\n" \
"v_mfma_f32_16x16x32_bf16 v[52:55], a[148:151], v[188:191], 0\n" \
"v_exp_f32_e64 v137, v137\n" \
"v_mfma_f32_16x16x32_bf16 v[24:27], a[152:155], v[184:187], 0\n" \
"v_exp_f32_e64 v138, v138\n" \
"v_mfma_f32_16x16x32_bf16 v[56:59], a[152:155], v[188:191], 0\n" \
"ds_read_b128 a[88:91], v209 offset:20480\n" \
"v_mfma_f32_16x16x32_bf16 v[28:31], a[156:159], v[184:187], 0\n" \
"v_exp_f32_e64 v139, v139\n" \
"v_mfma_f32_16x16x32_bf16 v[60:63], a[156:159], v[188:191], 0\n" \
"v_mfma_f32_16x16x32_bf16 v[144:147], a[96:99], a[32:35], v[168:171]\n" \
"v_exp_f32_e64 v140, v140\n" \
"v_mfma_f32_16x16x32_bf16 v[144:147], a[100:103], a[36:39], v[144:147]\n" \
"ds_read_b128 a[92:95], v210 offset:20480\n" \
"v_mfma_f32_16x16x32_bf16 v[144:147], a[104:107], a[40:43], v[144:147]\n" \
"v_exp_f32_e64 v141, v141\n" \
"v_mfma_f32_16x16x32_bf16 v[144:147], a[108:111], a[44:47], v[144:147]\n" \
"v_cvt_pk_bf16_f32 v196, v136, v137\n" \
"v_cvt_pk_bf16_f32 v197, v138, v139\n" \
"v_mfma_f32_16x16x32_bf16 v[148:151], a[112:115], a[32:35], v[168:171]\n" \
"v_exp_f32_e64 v142, v142\n" \
"v_mfma_f32_16x16x32_bf16 v[148:151], a[116:119], a[36:39], v[148:151]\n" \
"v_mfma_f32_16x16x32_bf16 v[148:151], a[120:123], a[40:43], v[148:151]\n" \
"buffer_load_dwordx4 %[vd0], %[srdv], s48 offen offset:2048 lds\n" \
"v_mfma_f32_16x16x32_bf16 v[148:151], a[124:127], a[44:47], v[148:151]\n" \
"v_exp_f32_e64 v143, v143\n" \
"v_mfma_f32_16x16x32_bf16 v[64:67], a[128:131], v[216:219], 0\n" \
"ds_read_b64_tr_b16 a[160:161], %[vvb] offset:8192\n" \
"v_mfma_f32_16x16x32_bf16 v[96:99], a[128:131], v[220:223], 0\n" \
"v_exp_f32_e64 v144, v144\n" \
"v_mfma_f32_16x16x32_bf16 v[68:71], a[132:135], v[216:219], 0\n" \
"v_cvt_pk_bf16_f32 v198, v140, v141\n" \
"v_cvt_pk_bf16_f32 v199, v142, v143\n" \
"v_mfma_f32_16x16x32_bf16 v[100:103], a[132:135], v[220:223], 0\n" \
"v_exp_f32_e64 v145, v145\n" \
"v_mfma_f32_16x16x32_bf16 v[72:75], a[136:139], v[216:219], 0\n" \
"ds_read_b64_tr_b16 a[162:163], %[vvb] offset:12288\n" \
"ds_read_b64_tr_b16 a[164:165], v211 offset:8192\n" \
"v_mfma_f32_16x16x32_bf16 v[104:107], a[136:139], v[220:223], 0\n" \
"v_exp_f32_e64 v146, v146\n" \
"v_mfma_f32_16x16x32_bf16 v[76:79], a[140:143], v[216:219], 0\n" \
"ds_read_b64_tr_b16 a[166:167], v211 offset:12288\n" \
"v_mfma_f32_16x16x32_bf16 v[108:111], a[140:143], v[220:223], 0\n" \
"v_exp_f32_e64 v147, v147\n" \
"v_smfmac_f32_16x16x64_bf16 v[180:183], a[192:195], v[216:223], v228\n" \
"ds_read_b64_tr_b16 a[168:169], %[vvb] offset:8704\n" \
"v_mfma_f32_16x16x32_bf16 v[152:155], a[96:99], a[48:51], v[172:175]\n" \
"v_exp_f32_e64 v148, v148\n" \
"v_mfma_f32_16x16x32_bf16 v[152:155], a[100:103], a[52:55], v[152:155]\n" \
"ds_read_b64_tr_b16 a[170:171], %[vvb] offset:12800\n" \
"v_mfma_f32_16x16x32_bf16 v[152:155], a[104:107], a[56:59], v[152:155]\n" \
"v_exp_f32_e64 v149, v149\n" \
"v_mfma_f32_16x16x32_bf16 v[152:155], a[108:111], a[60:63], v[152:155]\n" \
"v_cvt_pk_bf16_f32 v200, v144, v145\n" \
"v_cvt_pk_bf16_f32 v201, v146, v147\n" \
"v_mfma_f32_16x16x32_bf16 v[156:159], a[112:115], a[48:51], v[172:175]\n" \
"v_exp_f32_e64 v150, v150\n" \
"v_mfma_f32_16x16x32_bf16 v[156:159], a[116:119], a[52:55], v[156:159]\n" \
"ds_read_b64_tr_b16 a[172:173], v211 offset:8704\n" \
"v_mfma_f32_16x16x32_bf16 v[156:159], a[120:123], a[56:59], v[156:159]\n" \
"buffer_load_dwordx4 v215, %[srdv], s48 offen offset:3072 lds\n" \
"v_mfma_f32_16x16x32_bf16 v[156:159], a[124:127], a[60:63], v[156:159]\n" \
"v_exp_f32_e64 v151, v151\n" \
"v_mfma_f32_16x16x32_bf16 v[80:83], a[144:147], v[216:219], 0\n" \
"ds_read_b64_tr_b16 a[174:175], v211 offset:12800\n" \
"ds_read_b64_tr_b16 a[176:177], %[vvb] offset:9216\n" \
"v_mfma_f32_16x16x32_bf16 v[112:115], a[144:147], v[220:223], 0\n" \
"v_exp_f32_e64 v152, v152\n" \
"v_mfma_f32_16x16x32_bf16 v[84:87], a[148:151], v[216:219], 0\n" \
"v_cvt_pk_bf16_f32 v202, v148, v149\n" \
"v_cvt_pk_bf16_f32 v203, v150, v151\n" \
"v_mfma_f32_16x16x32_bf16 v[116:119], a[148:151], v[220:223], 0\n" \
"v_exp_f32_e64 v153, v153\n" \
"v_mfma_f32_16x16x32_bf16 v[88:91], a[152:155], v[216:219], 0\n" \
"v_exp_f32_e64 v154, v154\n" \
"v_mfma_f32_16x16x32_bf16 v[120:123], a[152:155], v[220:223], 0\n" \
"ds_read_b64_tr_b16 a[178:179], %[vvb] offset:13312\n" \
"v_mfma_f32_16x16x32_bf16 v[92:95], a[156:159], v[216:219], 0\n" \
"v_exp_f32_e64 v155, v155\n" \
"v_mov_b64_e32 v[216:217], 0\n" \
"v_mov_b64_e32 v[218:219], 0\n" \
"v_mfma_f32_16x16x32_bf16 v[124:127], a[156:159], v[220:223], 0\n" \
"ds_read_b64_tr_b16 a[180:181], v211 offset:9216\n" \
"s_waitcnt lgkmcnt(13)\n" \
"s_nop 0\n" \
"LmA_%=:\n" \
"v_mfma_f32_16x16x32_bf16 v[128:131], a[64:67], a[0:3], v[160:163]\n" \
"v_exp_f32_e64 v156, v156\n" \
"v_mfma_f32_16x16x32_bf16 v[128:131], a[68:71], a[4:7], v[128:131]\n" \
"ds_read_b64_tr_b16 a[182:183], v211 offset:13312\n" \
"s_add_u32 m0, %[m0b], 0\n" \
"v_mfma_f32_16x16x32_bf16 v[128:131], a[72:75], a[8:11], v[128:131]\n" \
"v_exp_f32_e32 v157, v157\n" \
"v_mfma_f32_16x16x32_bf16 v[128:131], a[76:79], a[12:15], v[128:131]\n" \
"v_cvt_pk_bf16_f32 v204, v152, v153\n" \
"v_cvt_pk_bf16_f32 v205, v154, v155\n" \
"v_mfma_f32_16x16x32_bf16 v[132:135], a[80:83], a[0:3], v[160:163]\n" \
"v_exp_f32_e64 v158, v158\n" \
"v_mfma_f32_16x16x32_bf16 v[132:135], a[84:87], a[4:7], v[132:135]\n" \
"ds_read_b64_tr_b16 a[184:185], %[vvb] offset:9728\n" \
"s_waitcnt lgkmcnt(5)\n" \
"v_mfma_f32_16x16x32_bf16 v[132:135], a[88:91], a[8:11], v[132:135]\n" \
"buffer_load_dwordx4 %[kd0], %[srdk], s48 offen offset:0 lds\n" \
"v_mfma_f32_16x16x32_bf16 v[132:135], a[92:95], a[12:15], v[132:135]\n" \
"v_exp_f32_e32 v159, v159\n" \
"v_mfma_f32_16x16x32_bf16 v[64:67], a[128:131], v[216:219], v[64:67]\n" \
"ds_read_b64_tr_b16 a[186:187], %[vvb] offset:13824\n" \
"ds_read_b64_tr_b16 a[188:189], v211 offset:9728\n" \
"v_mfma_f32_16x16x32_bf16 v[64:67], a[160:163], v[200:203], v[64:67]\n" \
"v_exp_f32_e64 v128, v128\n" \
"v_mfma_f32_16x16x32_bf16 v[68:71], a[132:135], v[216:219], v[68:71]\n" \
"v_cvt_pk_bf16_f32 v206, v156, v157\n" \
"v_cvt_pk_bf16_f32 v207, v158, v159\n" \
"v_mfma_f32_16x16x32_bf16 v[68:71], a[164:167], v[200:203], v[68:71]\n" \
"v_exp_f32_e64 v129, v129\n" \
"v_mfma_f32_16x16x32_bf16 v[72:75], a[136:139], v[216:219], v[72:75]\n" \
"ds_read_b64_tr_b16 a[190:191], v211 offset:13824\n" \
"ds_read_b64_tr_b16 a[128:129], %[vvb] offset:16384\n" \
"s_add_u32 s60, s60, 1\n" \
"s_nop 0\n" \
"v_readlane_b32 s58, %[tlo], s60\n" \
"v_mfma_f32_16x16x32_bf16 v[72:75], a[168:171], v[200:203], v[72:75]\n" \
"v_exp_f32_e64 v130, v130\n" \
"v_mfma_f32_16x16x32_bf16 v[76:79], a[140:143], v[216:219], v[76:79]\n" \
"ds_read_b64_tr_b16 a[130:131], %[vvb] offset:20480\n" \
"ds_read_b64_tr_b16 a[132:133], v211 offset:16384\n" \
"v_readlane_b32 s59, %[thi], s60\n" \
"v_mfma_f32_16x16x32_bf16 v[76:79], a[172:175], v[200:203], v[76:79]\n" \
"v_exp_f32_e64 v131, v131\n" \
"v_smfmac_f32_16x16x64_bf16 v[176:179], a[192:195], v[192:199], v228\n" \
"ds_read_b64_tr_b16 a[134:135], v211 offset:20480\n" \
"ds_read_b64_tr_b16 a[136:137], %[vvb] offset:16896\n" \
"v_mfma_f32_16x16x32_bf16 v[136:139], a[64:67], a[16:19], v[164:167]\n" \
"v_exp_f32_e64 v132, v132\n" \
"v_mfma_f32_16x16x32_bf16 v[136:139], a[68:71], a[20:23], v[136:139]\n" \
"ds_read_b64_tr_b16 a[138:139], %[vvb] offset:20992\n" \
"ds_read_b64_tr_b16 a[140:141], v211 offset:16896\n" \
"v_mfma_f32_16x16x32_bf16 v[136:139], a[72:75], a[24:27], v[136:139]\n" \
"v_exp_f32_e64 v133, v133\n" \
"v_mfma_f32_16x16x32_bf16 v[136:139], a[76:79], a[28:31], v[136:139]\n" \
"v_cvt_pk_bf16_f32 v184, v128, v129\n" \
"v_cvt_pk_bf16_f32 v185, v130, v131\n" \
"v_mfma_f32_16x16x32_bf16 v[140:143], a[80:83], a[16:19], v[164:167]\n" \
"v_exp_f32_e64 v134, v134\n" \
"v_mfma_f32_16x16x32_bf16 v[140:143], a[84:87], a[20:23], v[140:143]\n" \
"ds_read_b64_tr_b16 a[142:143], v211 offset:20992\n" \
"s_waitcnt lgkmcnt(8)\n" \
"v_mfma_f32_16x16x32_bf16 v[140:143], a[88:91], a[24:27], v[140:143]\n" \
"buffer_load_dwordx4 v212, %[srdk], s48 offen offset:1024 lds\n" \
"v_mfma_f32_16x16x32_bf16 v[140:143], a[92:95], a[28:31], v[140:143]\n" \
"v_exp_f32_e32 v135, v135\n" \
"v_mfma_f32_16x16x32_bf16 v[80:83], a[144:147], v[216:219], v[80:83]\n" \
"ds_read_b64_tr_b16 a[144:145], %[vvb] offset:17408\n" \
"ds_read_b64_tr_b16 a[146:147], %[vvb] offset:21504\n" \
"v_mfma_f32_16x16x32_bf16 v[80:83], a[176:179], v[200:203], v[80:83]\n" \
"v_exp_f32_e64 v136, v136\n" \
"v_mfma_f32_16x16x32_bf16 v[84:87], a[148:151], v[216:219], v[84:87]\n" \
"v_cvt_pk_bf16_f32 v186, v132, v133\n" \
"v_cvt_pk_bf16_f32 v187, v134, v135\n" \
"v_mfma_f32_16x16x32_bf16 v[84:87], a[180:183], v[200:203], v[84:87]\n" \
"v_exp_f32_e64 v137, v137\n" \
"v_mfma_f32_16x16x32_bf16 v[88:91], a[152:155], v[216:219], v[88:91]\n" \
".if . - adaln_body != 12288\n" \
".error \"size model\"\n" \
".endif\n" \
"Lk2_%=:\n" \
"s_cbranch_execz Lk3_%=\n" \
"s_nop 0\n" \
"v_exp_f32_e64 v138, v138\n" \
"v_mfma_f32_16x16x32_bf16 v[88:91], a[184:187], v[200:203], v[88:91]\n" \
"ds_read_b64_tr_b16 a[148:149], v211 offset:17408\n" \
"ds_read_b64_tr_b16 a[150:151], v211 offset:21504\n" \
"v_mfma_f32_16x16x32_bf16 v[92:95], a[156:159], v[216:219], v[92:95]\n" \
"v_exp_f32_e64 v139, v139\n" \
"v_mfma_f32_16x16x32_bf16 v[92:95], a[188:191], v[200:203], v[92:95]\n" \
"ds_read_b64_tr_b16 a[152:153], %[vvb] offset:17920\n" \
"v_mfma_f32_16x16x32_bf16 v[144:147], a[64:67], a[32:35], v[168:171]\n" \
"v_exp_f32_e64 v140, v140\n" \
"v_mfma_f32_16x16x32_bf16 v[144:147], a[68:71], a[36:39], v[144:147]\n" \
"ds_read_b64_tr_b16 a[154:155], %[vvb] offset:22016\n" \
"ds_read_b64_tr_b16 a[156:157], v211 offset:17920\n" \
"v_mfma_f32_16x16x32_bf16 v[144:147], a[72:75], a[40:43], v[144:147]\n" \
"v_exp_f32_e64 v141, v141\n" \
"v_mfma_f32_16x16x32_bf16 v[144:147], a[76:79], a[44:47], v[144:147]\n" \
"v_cvt_pk_bf16_f32 v188, v136, v137\n" \
"v_cvt_pk_bf16_f32 v189, v138, v139\n" \
"v_mfma_f32_16x16x32_bf16 v[148:151], a[80:83], a[32:35], v[168:171]\n" \
"v_exp_f32_e64 v142, v142\n" \
"v_mfma_f32_16x16x32_bf16 v[148:151], a[84:87], a[36:39], v[148:151]\n" \
"ds_read_b64_tr_b16 a[158:159], v211 offset:22016\n" \
"s_waitcnt lgkmcnt(8)\n" \
"v_mfma_f32_16x16x32_bf16 v[148:151], a[88:91], a[40:43], v[148:151]\n" \
"buffer_load_dwordx4 v213, %[srdk], s48 offen offset:2048 lds\n" \
"v_mfma_f32_16x16x32_bf16 v[148:151], a[92:95], a[44:47], v[148:151]\n" \
"v_exp_f32_e32 v143, v143\n" \
"v_mfma_f32_16x16x32_bf16 v[0:3], a[160:163], v[192:195], v[0:3]\n" \
"ds_read_b128 a[96:99], %[kb0] offset:24576\n" \
"v_mfma_f32_16x16x32_bf16 v[0:3], a[128:131], v[184:187], v[0:3]\n" \
"v_exp_f32_e64 v144, v144\n" \
"v_mfma_f32_16x16x32_bf16 v[4:7], a[164:167], v[192:195], v[4:7]\n" \
"v_cvt_pk_bf16_f32 v190, v140, v141\n" \
"v_cvt_pk_bf16_f32 v191, v142, v143\n" \
"v_mfma_f32_16x16x32_bf16 v[4:7], a[132:135], v[184:187], v[4:7]\n" \
"v_exp_f32_e64 v145, v145\n" \
"v_mfma_f32_16x16x32_bf16 v[8:11], a[168:171], v[192:195], v[8:11]\n" \
"ds_read_b128 a[100:103], v208 offset:24576\n" \
"v_mfma_f32_16x16x32_bf16 v[8:11], a[136:139], v[184:187], v[8:11]\n" \
"v_exp_f32_e64 v146, v146\n" \
"v_mfma_f32_16x16x32_bf16 v[12:15], a[172:175], v[192:195], v[12:15]\n" \
"ds_read_b128 a[104:107], v209 offset:24576\n" \
"v_mfma_f32_16x16x32_bf16 v[12:15], a[140:143], v[184:187], v[12:15]\n" \
"v_exp_f32_e64 v147, v147\n" \
"v_smfmac_f32_16x16x64_bf16 v[180:183], a[192:195], v[200:207], v228\n" \
"ds_read_b128 a[108:111], v210 offset:24576\n" \
"v_mfma_f32_16x16x32_bf16 v[152:155], a[64:67], a[48:51], v[172:175]\n" \
"v_exp_f32_e64 v148, v148\n" \
"v_mfma_f32_16x16x32_bf16 v[152:155], a[68:71], a[52:55], v[152:155]\n" \
"ds_read_b128 a[112:115], %[kb0] offset:28672\n" \
"v_mfma_f32_16x16x32_bf16 v[152:155], a[72:75], a[56:59], v[152:155]\n" \
"v_exp_f32_e64 v149, v149\n" \
"v_mfma_f32_16x16x32_bf16 v[152:155], a[76:79], a[60:63], v[152:155]\n" \
"v_cvt_pk_bf16_f32 v216, v144, v145\n" \
"v_cvt_pk_bf16_f32 v217, v146, v147\n" \
"v_mfma_f32_16x16x32_bf16 v[156:159], a[80:83], a[48:51], v[172:175]\n" \
"v_exp_f32_e64 v150, v150\n" \
"v_mfma_f32_16x16x32_bf16 v[156:159], a[84:87], a[52:55], v[156:159]\n" \
"ds_read_b128 a[116:119], v208 offset:28672\n" \
"s_waitcnt lgkmcnt(6)\n" \
"v_mfma_f32_16x16x32_bf16 v[156:159], a[88:91], a[56:59], v[156:159]\n" \
"buffer_load_dwordx4 v214, %[srdk], s48 offen offset:3072 lds\n" \
"v_mfma_f32_16x16x32_bf16 v[156:159], a[92:95], a[60:63], v[156:159]\n" \
"v_exp_f32_e32 v151, v151\n" \
"v_mfma_f32_16x16x32_bf16 v[16:19], a[176:179], v[192:195], v[16:19]\n" \
"ds_read_b128 a[120:123], v209 offset:28672\n" \
"v_mfma_f32_16x16x32_bf16 v[16:19], a[144:147], v[184:187], v[16:19]\n" \
"v_exp_f32_e64 v152, v152\n" \
"v_mfma_f32_16x16x32_bf16 v[20:23], a[180:183], v[192:195], v[20:23]\n" \
"v_cvt_pk_bf16_f32 v218, v148, v149\n" \
"v_cvt_pk_bf16_f32 v219, v150, v151\n" \
"v_mfma_f32_16x16x32_bf16 v[20:23], a[148:151], v[184:187], v[20:23]\n" \
"v_exp_f32_e64 v153, v153\n" \
"v_mfma_f32_16x16x32_bf16 v[24:27], a[184:187], v[192:195], v[24:27]\n" \
"v_exp_f32_e64 v154, v154\n" \
"v_mfma_f32_16x16x32_bf16 v[24:27], a[152:155], v[184:187], v[24:27]\n" \
"ds_read_b128 a[124:127], v210 offset:28672\n" \
"s_add_u32 s48, s48, 0x4000\n" \
"s_mov_b32 m0, s44\n" \
"s_addk_i32 s44, 0x400\n" \
"buffer_load_dwordx4 v229, s[64:67], s45 offen offset:0 nt lds\n" \
"s_add_u32 s45, s45, 393216\n" \
"v_mfma_f32_16x16x32_bf16 v[28:31], a[188:191], v[192:195], v[28:31]\n" \
"v_exp_f32_e64 v155, v155\n" \
"v_mfma_f32_16x16x32_bf16 v[28:31], a[156:159], v[184:187], v[28:31]\n" \
"s_waitcnt vmcnt(14) lgkmcnt(2)\n" \
"s_barrier\n" \
"v_mfma_f32_16x16x32_bf16 v[128:131], a[96:99], a[0:3], v[160:163]\n" \
"v_exp_f32_e64 v156, v156\n" \
"v_mfma_f32_16x16x32_bf16 v[128:131], a[100:103], a[4:7], v[128:131]\n" \
"s_add_u32 m0, %[m0b], 65536\n" \
"v_mfma_f32_16x16x32_bf16 v[128:131], a[104:107], a[8:11], v[128:131]\n" \
"v_exp_f32_e64 v157, v157\n" \
"v_mfma_f32_16x16x32_bf16 v[128:131], a[108:111], a[12:15], v[128:131]\n" \
"v_cvt_pk_bf16_f32 v220, v152, v153\n" \
"v_cvt_pk_bf16_f32 v221, v154, v155\n" \
"v_mfma_f32_16x16x32_bf16 v[132:135], a[112:115], a[0:3], v[160:163]\n" \
"v_exp_f32_e64 v158, v158\n" \
"v_mfma_f32_16x16x32_bf16 v[132:135], a[116:119], a[4:7], v[132:135]\n" \
"s_waitcnt lgkmcnt(0)\n" \
"v_mfma_f32_16x16x32_bf16 v[132:135], a[120:123], a[8:11], v[132:135]\n" \
"buffer_load_dwordx4 %[vd0], %[srdv], s48 offen offset:0 lds\n" \
"v_mfma_f32_16x16x32_bf16 v[132:135], a[124:127], a[12:15], v[132:135]\n" \
"v_exp_f32_e32 v159, v159\n" \
"v_mfma_f32_16x16x32_bf16 v[32:35], a[160:163], v[196:199], v[32:35]\n" \
"ds_read_b128 a[64:67], %[kb0] offset:32768\n" \
"v_mfma_f32_16x16x32_bf16 v[32:35], a[128:131], v[188:191], v[32:35]\n" \
"v_exp_f32_e64 v128, v128\n" \
"v_mfma_f32_16x16x32_bf16 v[36:39], a[164:167], v[196:199], v[36:39]\n" \
"v_cvt_pk_bf16_f32 v222, v156, v157\n" \
"v_cvt_pk_bf16_f32 v223, v158, v159\n" \
"v_mfma_f32_16x16x32_bf16 v[36:39], a[132:135], v[188:191], v[36:39]\n" \
"v_exp_f32_e64 v129, v129\n" \
"v_mfma_f32_16x16x32_bf16 v[40:43], a[168:171], v[196:199], v[40:43]\n" \
"ds_read_b128 a[68:71], v208 offset:32768\n" \
"v_mfma_f32_16x16x32_bf16 v[40:43], a[136:139], v[188:191], v[40:43]\n" \
"v_exp_f32_e64 v130, v130\n" \
"v_mfma_f32_16x16x32_bf16 v[44:47], a[172:175], v[196:199], v[44:47]\n" \
"ds_read_b128 a[72:75], v209 offset:32768\n" \
"v_mfma_f32_16x16x32_bf16 v[44:47], a[140:143], v[188:191], v[44:47]\n" \
"v_exp_f32_e64 v131, v131\n" \
"v_smfmac_f32_16x16x64_bf16 v[176:179], a[192:195], v[184:191], v228\n" \
"v_mfma_f32_16x16x32_bf16 v[136:139], a[96:99], a[16:19], v[164:167]\n" \
"v_exp_f32_e64 v132, v132\n" \
"v_mfma_f32_16x16x32_bf16 v[136:139], a[100:103], a[20:23], v[136:139]\n" \
"ds_read_b128 a[76:79], v210 offset:32768\n" \
"v_mfma_f32_16x16x32_bf16 v[136:139], a[104:107], a[24:27], v[136:139]\n" \
"v_exp_f32_e64 v133, v133\n" \
"v_mfma_f32_16x16x32_bf16 v[136:139], a[108:111], a[28:31], v[136:139]\n" \
"v_cvt_pk_bf16_f32 v192, v128, v129\n" \
"v_cvt_pk_bf16_f32 v193, v130, v131\n" \
"v_mfma_f32_16x16x32_bf16 v[140:143], a[112:115], a[16:19], v[164:167]\n" \
"v_exp_f32_e64 v134, v134\n" \
"v_mfma_f32_16x16x32_bf16 v[140:143], a[116:119], a[20:23], v[140:143]\n" \
"ds_read_b128 a[80:83], %[kb0] offset:36864\n" \
"v_mfma_f32_16x16x32_bf16 v[140:143], a[120:123], a[24:27], v[140:143]\n" \
"buffer_load_dwordx4 v215, %[srdv], s48 offen offset:1024 lds\n" \
"v_mfma_f32_16x16x32_bf16 v[140:143], a[124:127], a[28:31], v[140:143]\n" \
"v_exp_f32_e64 v135, v135\n" \
"v_mfma_f32_16x16x32_bf16 v[48:51], a[176:179], v[196:199], v[48:51]\n" \
"ds_read_b128 a[84:87], v208 offset:36864\n" \
"v_mfma_f32_16x16x32_bf16 v[48:51], a[144:147], v[188:191], v[48:51]\n" \
"v_exp_f32_e64 v136, v136\n" \
"v_mfma_f32_16x16x32_bf16 v[52:55], a[180:183], v[196:199], v[52:55]\n" \
"v_cvt_pk_bf16_f32 v194, v132, v133\n" \
"v_cvt_pk_bf16_f32 v195, v134, v135\n" \
"v_mfma_f32_16x16x32_bf16 v[52:55], a[148:151], v[188:191], v[52:55]\n" \
"v_exp_f32_e64 v137, v137\n" \
"v_mfma_f32_16x16x32_bf16 v[56:59], a[184:187], v[196:199], v[56:59]\n" \
"v_exp_f32_e64 v138, v138\n" \
"v_mfma_f32_16x16x32_bf16 v[56:59], a[152:155], v[188:191], v[56:59]\n" \
"ds_read_b128 a[88:91], v209 offset:36864\n" \
"v_mfma_f32_16x16x32_bf16 v[60:63], a[188:191], v[196:199], v[60:63]\n" \
"v_exp_f32_e64 v139, v139\n" \
"v_mfma_f32_16x16x32_bf16 v[60:63], a[156:159], v[188:191], v[60:63]\n" \
"s_cmp_lg_u32 %[wid], 0\n" \
"s_cbranch_scc1 LtA3_%=\n" \
"global_load_dword v237, v224, s[58:59]\n" \
"s_add_u32 s61, s48, 49152\n" \
"s_mov_b32 exec_hi, 0\n" \
"s_nop 0\n" \
"buffer_load_dword v237, v224, %[srdp], s61 offen\n" \
"s_mov_b32 exec_hi, -1\n" \
"s_nop 0\n" \
"LtA3_%=:\n" \
"v_mfma_f32_16x16x32_bf16 v[144:147], a[96:99], a[32:35], v[168:171]\n" \
"v_exp_f32_e64 v140, v140\n" \
"v_mfma_f32_16x16x32_bf16 v[144:147], a[100:103], a[36:39], v[144:147]\n" \
"ds_read_b128 a[92:95], v210 offset:36864\n" \
"v_mfma_f32_16x16x32_bf16 v[144:147], a[104:107], a[40:43], v[144:147]\n" \
"v_exp_f32_e64 v141, v141\n" \
"v_mfma_f32_16x16x32_bf16 v[144:147], a[108:111], a[44:47], v[144:147]\n" \
"v_cvt_pk_bf16_f32 v196, v136, v137\n" \
"v_cvt_pk_bf16_f32 v197, v138, v139\n" \
"v_mfma_f32_16x16x32_bf16 v[148:151], a[112:115], a[32:35], v[168:171]\n" \
"v_exp_f32_e64 v142, v142\n" \
"v_mfma_f32_16x16x32_bf16 v[148:151], a[116:119], a[36:39], v[148:151]\n" \
"v_mfma_f32_16x16x32_bf16 v[148:151], a[120:123], a[40:43], v[148:151]\n" \
"buffer_load_dwordx4 %[vd0], %[srdv], s48 offen offset:2048 lds\n" \
"v_mfma_f32_16x16x32_bf16 v[148:151], a[124:127], a[44:47], v[148:151]\n" \
"v_exp_f32_e64 v143, v143\n" \
"v_mfma_f32_16x16x32_bf16 v[96:99], a[160:163], v[204:207], v[96:99]\n" \
"ds_read_b64_tr_b16 a[160:161], %[vvb] offset:24576\n" \
"v_mfma_f32_16x16x32_bf16 v[96:99], a[128:131], v[220:223], v[96:99]\n" \
"v_exp_f32_e64 v144, v144\n" \
"v_mfma_f32_16x16x32_bf16 v[100:103], a[164:167], v[204:207], v[100:103]\n" \
"v_cvt_pk_bf16_f32 v198, v140, v141\n" \
"v_cvt_pk_bf16_f32 v199, v142, v143\n" \
"v_mfma_f32_16x16x32_bf16 v[100:103], a[132:135], v[220:223], v[100:103]\n" \
"v_exp_f32_e64 v145, v145\n" \
"v_mfma_f32_16x16x32_bf16 v[104:107], a[168:171], v[204:207], v[104:107]\n" \
"ds_read_b64_tr_b16 a[162:163], %[vvb] offset:28672\n" \
"ds_read_b64_tr_b16 a[164:165], v211 offset:24576\n" \
"v_mfma_f32_16x16x32_bf16 v[104:107], a[136:139], v[220:223], v[104:107]\n" \
"v_exp_f32_e64 v146, v146\n" \
"v_mfma_f32_16x16x32_bf16 v[108:111], a[172:175], v[204:207], v[108:111]\n" \
"ds_read_b64_tr_b16 a[166:167], v211 offset:28672\n" \
"v_mfma_f32_16x16x32_bf16 v[108:111], a[140:143], v[220:223], v[108:111]\n" \
"v_exp_f32_e64 v147, v147\n" \
"v_smfmac_f32_16x16x64_bf16 v[180:183], a[192:195], v[216:223], v228\n" \
"ds_read_b64_tr_b16 a[168:169], %[vvb] offset:25088\n" \
"v_mfma_f32_16x16x32_bf16 v[152:155], a[96:99], a[48:51], v[172:175]\n" \
"v_exp_f32_e64 v148, v148\n" \
"v_mfma_f32_16x16x32_bf16 v[152:155], a[100:103], a[52:55], v[152:155]\n" \
"ds_read_b64_tr_b16 a[170:171], %[vvb] offset:29184\n" \
"v_mfma_f32_16x16x32_bf16 v[152:155], a[104:107], a[56:59], v[152:155]\n" \
"v_exp_f32_e64 v149, v149\n" \
"v_mfma_f32_16x16x32_bf16 v[152:155], a[108:111], a[60:63], v[152:155]\n" \
"v_cvt_pk_bf16_f32 v200, v144, v145\n" \
"v_cvt_pk_bf16_f32 v201, v146, v147\n" \
"v_mfma_f32_16x16x32_bf16 v[156:159], a[112:115], a[48:51], v[172:175]\n" \
"v_exp_f32_e64 v150, v150\n" \
"v_mfma_f32_16x16x32_bf16 v[156:159], a[116:119], a[52:55], v[156:159]\n" \
"ds_read_b64_tr_b16 a[172:173], v211 offset:25088\n" \
"v_mfma_f32_16x16x32_bf16 v[156:159], a[120:123], a[56:59], v[156:159]\n" \
"buffer_load_dwordx4 v215, %[srdv], s48 offen offset:3072 lds\n" \
"v_mfma_f32_16x16x32_bf16 v[156:159], a[124:127], a[60:63], v[156:159]\n" \
"v_exp_f32_e64 v151, v151\n" \
"v_mfma_f32_16x16x32_bf16 v[112:115], a[176:179], v[204:207], v[112:115]\n" \
"ds_read_b64_tr_b16 a[174:175], v211 offset:29184\n" \
"ds_read_b64_tr_b16 a[176:177], %[vvb] offset:25600\n" \
"v_mfma_f32_16x16x32_bf16 v[112:115], a[144:147], v[220:223], v[112:115]\n" \
"v_exp_f32_e64 v152, v152\n" \
"v_mfma_f32_16x16x32_bf16 v[116:119], a[180:183], v[204:207], v[116:119]\n" \
"v_cvt_pk_bf16_f32 v202, v148, v149\n" \
"v_cvt_pk_bf16_f32 v203, v150, v151\n" \
"v_mfma_f32_16x16x32_bf16 v[116:119], a[148:151], v[220:223], v[116:119]\n" \
"v_exp_f32_e64 v153, v153\n" \
"v_mfma_f32_16x16x32_bf16 v[120:123], a[184:187], v[204:207], v[120:123]\n" \
"v_exp_f32_e64 v154, v154\n" \
"v_mfma_f32_16x16x32_bf16 v[120:123], a[152:155], v[220:223], v[120:123]\n" \
"ds_read_b64_tr_b16 a[178:179], %[vvb] offset:29696\n" \
"v_mfma_f32_16x16x32_bf16 v[124:127], a[188:191], v[204:207], v[124:127]\n" \
"v_exp_f32_e64 v155, v155\n" \
"v_mfma_f32_16x16x32_bf16 v[124:127], a[156:159], v[220:223], v[124:127]\n" \
"ds_read_b64_tr_b16 a[180:181], v211 offset:25600\n" \
"s_waitcnt lgkmcnt(13)\n" \
"v_mfma_f32_16x16x32_bf16 v[128:131], a[64:67], a[0:3], v[160:163]\n" \
"v_exp_f32_e32 v156, v156\n" \
"v_mfma_f32_16x16x32_bf16 v[128:131], a[68:71], a[4:7], v[128:131]\n" \
"ds_read_b64_tr_b16 a[182:183], v211 offset:29696\n" \
"s_add_u32 m0, %[m0b], 16384\n" \
"v_mfma_f32_16x16x32_bf16 v[128:131], a[72:75], a[8:11], v[128:131]\n" \
"v_exp_f32_e64 v157, v157\n" \
"v_mfma_f32_16x16x32_bf16 v[128:131], a[76:79], a[12:15], v[128:131]\n" \
"v_cvt_pk_bf16_f32 v204, v152, v153\n" \
"v_cvt_pk_bf16_f32 v205, v154, v155\n" \
"v_mfma_f32_16x16x32_bf16 v[132:135], a[80:83], a[0:3], v[160:163]\n" \
"v_exp_f32_e64 v158, v158\n" \
"v_mfma_f32_16x16x32_bf16 v[132:135], a[84:87], a[4:7], v[132:135]\n" \
"ds_read_b64_tr_b16 a[184:185], %[vvb] offset:26112\n" \
"s_waitcnt lgkmcnt(5)\n" \
"v_mfma_f32_16x16x32_bf16 v[132:135], a[88:91], a[8:11], v[132:135]\n" \
"buffer_load_dwordx4 %[kd0], %[srdk], s48 offen offset:0 lds\n" \
"v_mfma_f32_16x16x32_bf16 v[132:135], a[92:95], a[12:15], v[132:135]\n" \
"v_exp_f32_e32 v159, v159\n" \
"v_mfma_f32_16x16x32_bf16 v[64:67], a[128:131], v[216:219], v[64:67]\n" \
"ds_read_b64_tr_b16 a[186:187], %[vvb] offset:30208\n" \
"ds_read_b64_tr_b16 a[188:189], v211 offset:26112\n" \
"v_mfma_f32_16x16x32_bf16 v[64:67], a[160:163], v[200:203], v[64:67]\n" \
"v_exp_f32_e64 v128, v128\n" \
"v_mfma_f32_16x16x32_bf16 v[68:71], a[132:135], v[216:219], v[68:71]\n" \
"v_cvt_pk_bf16_f32 v206, v156, v157\n" \
"v_cvt_pk_bf16_f32 v207, v158, v159\n" \
"v_mfma_f32_16x16x32_bf16 v[68:71], a[164:167], v[200:203], v[68:71]\n" \
"v_exp_f32_e64 v129, v129\n" \
"v_mfma_f32_16x16x32_bf16 v[72:75], a[136:139], v[216:219], v[72:75]\n" \
"ds_read_b64_tr_b16 a[190:191], v211 offset:30208\n" \
"ds_read_b64_tr_b16 a[128:129], %[vvb] offset:32768\n" \
"v_mfma_f32_16x16x32_bf16 v[72:75], a[168:171], v[200:203], v[72:75]\n" \
"v_exp_f32_e64 v130, v130\n" \
"v_mfma_f32_16x16x32_bf16 v[76:79], a[140:143], v[216:219], v[76:79]\n" \
"ds_read_b64_tr_b16 a[130:131], %[vvb] offset:36864\n" \
"ds_read_b64_tr_b16 a[132:133], v211 offset:32768\n" \
"v_mfma_f32_16x16x32_bf16 v[76:79], a[172:175], v[200:203], v[76:79]\n" \
"v_exp_f32_e64 v131, v131\n" \
"v_smfmac_f32_16x16x64_bf16 v[176:179], a[192:195], v[192:199], v228\n" \
"ds_read_b64_tr_b16 a[134:135], v211 offset:36864\n" \
"ds_read_b64_tr_b16 a[136:137], %[vvb] offset:33280\n" \
"v_mfma_f32_16x16x32_bf16 v[136:139], a[64:67], a[16:19], v[164:167]\n" \
"v_exp_f32_e64 v132, v132\n" \
"v_mfma_f32_16x16x32_bf16 v[136:139], a[68:71], a[20:23], v[136:139]\n" \
"ds_read_b64_tr_b16 a[138:139], %[vvb] offset:37376\n" \
"ds_read_b64_tr_b16 a[140:141], v211 offset:33280\n" \
"v_mfma_f32_16x16x32_bf16 v[136:139], a[72:75], a[24:27], v[136:139]\n" \
"v_exp_f32_e64 v133, v133\n" \
"v_mfma_f32_16x16x32_bf16 v[136:139], a[76:79], a[28:31], v[136:139]\n" \
"v_cvt_pk_bf16_f32 v184, v128, v129\n" \
"v_cvt_pk_bf16_f32 v185, v130, v131\n" \
"v_mfma_f32_16x16x32_bf16 v[140:143], a[80:83], a[16:19], v[164:167]\n" \
"v_exp_f32_e64 v134, v134\n" \
"v_mfma_f32_16x16x32_bf16 v[140:143], a[84:87], a[20:23], v[140:143]\n" \
"ds_read_b64_tr_b16 a[142:143], v211 offset:37376\n" \
"s_waitcnt lgkmcnt(8)\n" \
"v_mfma_f32_16x16x32_bf16 v[140:143], a[88:91], a[24:27], v[140:143]\n" \
"buffer_load_dwordx4 v212, %[srdk], s48 offen offset:1024 lds\n" \
"v_mfma_f32_16x16x32_bf16 v[140:143], a[92:95], a[28:31], v[140:143]\n" \
"v_exp_f32_e32 v135, v135\n" \
"v_mfma_f32_16x16x32_bf16 v[80:83], a[144:147], v[216:219], v[80:83]\n" \
"ds_read_b64_tr_b16 a[144:145], %[vvb] offset:33792\n" \
"ds_read_b64_tr_b16 a[146:147], %[vvb] offset:37888\n" \
"v_mfma_f32_16x16x32_bf16 v[80:83], a[176:179], v[200:203], v[80:83]\n" \
"v_exp_f32_e64 v136, v136\n" \
"v_mfma_f32_16x16x32_bf16 v[84:87], a[148:151], v[216:219], v[84:87]\n" \
"v_cvt_pk_bf16_f32 v186, v132, v133\n" \
"v_cvt_pk_bf16_f32 v187, v134, v135\n" \
"v_mfma_f32_16x16x32_bf16 v[84:87], a[180:183], v[200:203], v[84:87]\n" \
"v_exp_f32_e64 v137, v137\n" \
"v_mfma_f32_16x16x32_bf16 v[88:91], a[152:155], v[216:219], v[88:91]\n" \
"v_exp_f32_e64 v138, v138\n" \
"v_mfma_f32_16x16x32_bf16 v[88:91], a[184:187], v[200:203], v[88:91]\n" \
"ds_read_b64_tr_b16 a[148:149], v211 offset:33792\n" \
"ds_read_b64_tr_b16 a[150:151], v211 offset:37888\n" \
"v_mfma_f32_16x16x32_bf16 v[92:95], a[156:159], v[216:219], v[92:95]\n" \
"v_exp_f32_e64 v139, v139\n" \
"v_mfma_f32_16x16x32_bf16 v[92:95], a[188:191], v[200:203], v[92:95]\n" \
"ds_read_b64_tr_b16 a[152:153], %[vvb] offset:34304\n" \
"buffer_store_dwordx4 a[196:199], v235, %[srdz], s62 offen offset:0 sc1\n" \
"v_mfma_f32_16x16x32_bf16 v[144:147], a[64:67], a[32:35], v[168:171]\n" \
"v_exp_f32_e64 v140, v140\n" \
"v_mfma_f32_16x16x32_bf16 v[144:147], a[68:71], a[36:39], v[144:147]\n" \
"ds_read_b64_tr_b16 a[154:155], %[vvb] offset:38400\n" \
"ds_read_b64_tr_b16 a[156:157], v211 offset:34304\n" \
"v_mfma_f32_16x16x32_bf16 v[144:147], a[72:75], a[40:43], v[144:147]\n" \
"v_exp_f32_e64 v141, v141\n" \
"v_mfma_f32_16x16x32_bf16 v[144:147], a[76:79], a[44:47], v[144:147]\n" \
"v_cvt_pk_bf16_f32 v188, v136, v137\n" \
"v_cvt_pk_bf16_f32 v189, v138, v139\n" \
"v_mfma_f32_16x16x32_bf16 v[148:151], a[80:83], a[32:35], v[168:171]\n" \
"v_exp_f32_e64 v142, v142\n" \
"v_mfma_f32_16x16x32_bf16 v[148:151], a[84:87], a[36:39], v[148:151]\n" \
"ds_read_b64_tr_b16 a[158:159], v211 offset:38400\n" \
"s_waitcnt lgkmcnt(8)\n" \
"v_mfma_f32_16x16x32_bf16 v[148:151], a[88:91], a[40:43], v[148:151]\n" \
"buffer_load_dwordx4 v213, %[srdk], s48 offen offset:2048 lds\n" \
"v_mfma_f32_16x16x32_bf16 v[148:151], a[92:95], a[44:47], v[148:151]\n" \
"v_exp_f32_e32 v143, v143\n" \
"v_mfma_f32_16x16x32_bf16 v[0:3], a[160:163], v[192:195], v[0:3]\n" \
"ds_read_b128 a[96:99], %[kb0] offset:40960\n" \
"v_mfma_f32_16x16x32_bf16 v[0:3], a[128:131], v[184:187], v[0:3]\n" \
"v_exp_f32_e64 v144, v144\n" \
"v_mfma_f32_16x16x32_bf16 v[4:7], a[164:167], v[192:195], v[4:7]\n" \
"v_cvt_pk_bf16_f32 v190, v140, v141\n" \
"v_cvt_pk_bf16_f32 v191, v142, v143\n" \
"v_mfma_f32_16x16x32_bf16 v[4:7], a[132:135], v[184:187], v[4:7]\n" \
"v_exp_f32_e64 v145, v145\n" \
"v_mfma_f32_16x16x32_bf16 v[8:11], a[168:171], v[192:195], v[8:11]\n" \
"ds_read_b128 a[100:103], v208 offset:40960\n" \
"v_mfma_f32_16x16x32_bf16 v[8:11], a[136:139], v[184:187], v[8:11]\n" \
"v_exp_f32_e64 v146, v146\n" \
"v_mfma_f32_16x16x32_bf16 v[12:15], a[172:175], v[192:195], v[12:15]\n" \
"ds_read_b128 a[104:107], v209 offset:40960\n" \
"v_mfma_f32_16x16x32_bf16 v[12:15], a[140:143], v[184:187], v[12:15]\n" \
"v_exp_f32_e64 v147, v147\n" \
"v_smfmac_f32_16x16x64_bf16 v[180:183], a[192:195], v[200:207], v228\n" \
"ds_read_b128 a[108:111], v210 offset:40960\n" \
"v_mfma_f32_16x16x32_bf16 v[152:155], a[64:67], a[48:51], v[172:175]\n" \
"v_exp_f32_e64 v148, v148\n" \
"v_mfma_f32_16x16x32_bf16 v[152:155], a[68:71], a[52:55], v[152:155]\n" \
"ds_read_b128 a[112:115], %[kb0] offset:45056\n" \
"v_mfma_f32_16x16x32_bf16 v[152:155], a[72:75], a[56:59], v[152:155]\n" \
"v_exp_f32_e64 v149, v149\n" \
"v_mfma_f32_16x16x32_bf16 v[152:155], a[76:79], a[60:63], v[152:155]\n" \
"v_cvt_pk_bf16_f32 v216, v144, v145\n" \
"v_cvt_pk_bf16_f32 v217, v146, v147\n" \
"v_mfma_f32_16x16x32_bf16 v[156:159], a[80:83], a[48:51], v[172:175]\n" \
"v_exp_f32_e64 v150, v150\n" \
"v_mfma_f32_16x16x32_bf16 v[156:159], a[84:87], a[52:55], v[156:159]\n" \
"ds_read_b128 a[116:119], v208 offset:45056\n" \
"s_waitcnt lgkmcnt(6)\n" \
"v_mfma_f32_16x16x32_bf16 v[156:159], a[88:91], a[56:59], v[156:159]\n" \
"buffer_load_dwordx4 v214, %[srdk], s48 offen offset:3072 lds\n" \
"v_mfma_f32_16x16x32_bf16 v[156:159], a[92:95], a[60:63], v[156:159]\n" \
"v_exp_f32_e32 v151, v151\n" \
"v_mfma_f32_16x16x32_bf16 v[16:19], a[176:179], v[192:195], v[16:19]\n" \
"ds_read_b128 a[120:123], v209 offset:45056\n" \
"v_mfma_f32_16x16x32_bf16 v[16:19], a[144:147], v[184:187], v[16:19]\n" \
"v_exp_f32_e64 v152, v152\n" \
"v_mfma_f32_16x16x32_bf16 v[20:23], a[180:183], v[192:195], v[20:23]\n" \
"v_cvt_pk_bf16_f32 v218, v148, v149\n" \
"v_cvt_pk_bf16_f32 v219, v150, v151\n" \
"v_mfma_f32_16x16x32_bf16 v[20:23], a[148:151], v[184:187], v[20:23]\n" \
"v_exp_f32_e64 v153, v153\n" \
"v_mfma_f32_16x16x32_bf16 v[24:27], a[184:187], v[192:195], v[24:27]\n" \
"v_exp_f32_e64 v154, v154\n" \
"v_mfma_f32_16x16x32_bf16 v[24:27], a[152:155], v[184:187], v[24:27]\n" \
"ds_read_b128 a[124:127], v210 offset:45056\n" \
"s_add_u32 s48, s48, 0x4000\n" \
"v_mfma_f32_16x16x32_bf16 v[28:31], a[188:191], v[192:195], v[28:31]\n" \
"v_exp_f32_e64 v155, v155\n" \
"v_mfma_f32_16x16x32_bf16 v[28:31], a[156:159], v[184:187], v[28:31]\n" \
"s_waitcnt vmcnt(14) lgkmcnt(2)\n" \
"s_barrier\n" \
"v_mfma_f32_16x16x32_bf16 v[128:131], a[96:99], a[0:3], v[160:163]\n" \
"v_exp_f32_e64 v156, v156\n" \
"v_mfma_f32_16x16x32_bf16 v[128:131], a[100:103], a[4:7], v[128:131]\n" \
"s_add_u32 m0, %[m0b], 81920\n" \
"v_mfma_f32_16x16x32_bf16 v[128:131], a[104:107], a[8:11], v[128:131]\n" \
"v_exp_f32_e64 v157, v157\n" \
"v_mfma_f32_16x16x32_bf16 v[128:131], a[108:111], a[12:15], v[128:131]\n" \
"v_cvt_pk_bf16_f32 v220, v152, v153\n" \
"v_cvt_pk_bf16_f32 v221, v154, v155\n" \
"v_mfma_f32_16x16x32_bf16 v[132:135], a[112:115], a[0:3], v[160:163]\n" \
"v_exp_f32_e64 v158, v158\n" \
"v_mfma_f32_16x16x32_bf16 v[132:135], a[116:119], a[4:7], v[132:135]\n" \
"s_waitcnt lgkmcnt(0)\n" \
"v_mfma_f32_16x16x32_bf16 v[132:135], a[120:123], a[8:11], v[132:135]\n" \
"buffer_load_dwordx4 %[vd0], %[srdv], s48 offen offset:0 lds\n" \
"v_mfma_f32_16x16x32_bf16 v[132:135], a[124:127], a[12:15], v[132:135]\n" \
"v_exp_f32_e32 v159, v159\n" \
"v_mfma_f32_16x16x32_bf16 v[32:35], a[160:163], v[196:199], v[32:35]\n" \
"ds_read_b128 a[64:67], %[kb0] offset:49152\n" \
"v_mfma_f32_16x16x32_bf16 v[32:35], a[128:131], v[188:191], v[32:35]\n" \
"v_exp_f32_e64 v128, v128\n" \
"v_mfma_f32_16x16x32_bf16 v[36:39], a[164:167], v[196:199], v[36:39]\n" \
"v_cvt_pk_bf16_f32 v222, v156, v157\n" \
"v_cvt_pk_bf16_f32 v223, v158, v159\n" \
"v_mfma_f32_16x16x32_bf16 v[36:39], a[132:135], v[188:191], v[36:39]\n" \
"v_exp_f32_e64 v129, v129\n" \
"v_mfma_f32_16x16x32_bf16 v[40:43], a[168:171], v[196:199], v[40:43]\n" \
"ds_read_b128 a[68:71], v208 offset:49152\n" \
"v_mfma_f32_16x16x32_bf16 v[40:43], a[136:139], v[188:191], v[40:43]\n" \
"v_exp_f32_e64 v130, v130\n" \
"v_mfma_f32_16x16x32_bf16 v[44:47], a[172:175], v[196:199], v[44:47]\n" \
"ds_read_b128 a[72:75], v209 offset:49152\n" \
"v_mfma_f32_16x16x32_bf16 v[44:47], a[140:143], v[188:191], v[44:47]\n" \
"v_exp_f32_e64 v131, v131\n" \
"v_smfmac_f32_16x16x64_bf16 v[176:179], a[192:195], v[184:191], v228\n" \
"v_mfma_f32_16x16x32_bf16 v[136:139], a[96:99], a[16:19], v[164:167]\n" \
"v_exp_f32_e64 v132, v132\n" \
"v_mfma_f32_16x16x32_bf16 v[136:139], a[100:103], a[20:23], v[136:139]\n" \
"ds_read_b128 a[76:79], v210 offset:49152\n" \
"v_mfma_f32_16x16x32_bf16 v[136:139], a[104:107], a[24:27], v[136:139]\n" \
"v_exp_f32_e64 v133, v133\n" \
"v_mfma_f32_16x16x32_bf16 v[136:139], a[108:111], a[28:31], v[136:139]\n" \
"v_cvt_pk_bf16_f32 v192, v128, v129\n" \
"v_cvt_pk_bf16_f32 v193, v130, v131\n" \
"v_mfma_f32_16x16x32_bf16 v[140:143], a[112:115], a[16:19], v[164:167]\n" \
"v_exp_f32_e64 v134, v134\n" \
"v_mfma_f32_16x16x32_bf16 v[140:143], a[116:119], a[20:23], v[140:143]\n" \
"ds_read_b128 a[80:83], %[kb0] offset:53248\n" \
"v_mfma_f32_16x16x32_bf16 v[140:143], a[120:123], a[24:27], v[140:143]\n" \
"buffer_load_dwordx4 v215, %[srdv], s48 offen offset:1024 lds\n" \
"v_mfma_f32_16x16x32_bf16 v[140:143], a[124:127], a[28:31], v[140:143]\n" \
"v_exp_f32_e64 v135, v135\n" \
"v_mfma_f32_16x16x32_bf16 v[48:51], a[176:179], v[196:199], v[48:51]\n" \
"ds_read_b128 a[84:87], v208 offset:53248\n" \
"v_mfma_f32_16x16x32_bf16 v[48:51], a[144:147], v[188:191], v[48:51]\n" \
"v_exp_f32_e64 v136, v136\n" \
"v_mfma_f32_16x16x32_bf16 v[52:55], a[180:183], v[196:199], v[52:55]\n" \
"v_cvt_pk_bf16_f32 v194, v132, v133\n" \
"v_cvt_pk_bf16_f32 v195, v134, v135\n" \
"v_mfma_f32_16x16x32_bf16 v[52:55], a[148:151], v[188:191], v[52:55]\n" \
"v_exp_f32_e64 v137, v137\n" \
"v_mfma_f32_16x16x32_bf16 v[56:59], a[184:187], v[196:199], v[56:59]\n" \
"v_exp_f32_e64 v138, v138\n" \
"v_mfma_f32_16x16x32_bf16 v[56:59], a[152:155], v[188:191], v[56:59]\n" \
"ds_read_b128 a[88:91], v209 offset:53248\n" \
"v_mfma_f32_16x16x32_bf16 v[60:63], a[188:191], v[196:199], v[60:63]\n" \
"v_exp_f32_e64 v139, v139\n" \
"v_mfma_f32_16x16x32_bf16 v[60:63], a[156:159], v[188:191], v[60:63]\n" \
"s_cmp_lg_u32 %[wid], 1\n" \
"s_cbranch_scc1 LtA5_%=\n" \
"global_load_dword v237, v224, s[58:59]\n" \
"s_add_u32 s61, s48, 49152\n" \
"s_mov_b32 exec_hi, 0\n" \
"s_nop 0\n" \
"buffer_load_dword v237, v224, %[srdp], s61 offen\n" \
"s_mov_b32 exec_hi, -1\n" \
"s_nop 0\n" \
"LtA5_%=:\n" \
"v_mfma_f32_16x16x32_bf16 v[144:147], a[96:99], a[32:35], v[168:171]\n" \
"v_exp_f32_e64 v140, v140\n" \
"v_mfma_f32_16x16x32_bf16 v[144:147], a[100:103], a[36:39], v[144:147]\n" \
"ds_read_b128 a[92:95], v210 offset:53248\n" \
"v_mfma_f32_16x16x32_bf16 v[144:147], a[104:107], a[40:43], v[144:147]\n" \
"v_exp_f32_e64 v141, v141\n" \
"v_mfma_f32_16x16x32_bf16 v[144:147], a[108:111], a[44:47], v[144:147]\n" \
"v_cvt_pk_bf16_f32 v196, v136, v137\n" \
"v_cvt_pk_bf16_f32 v197, v138, v139\n" \
"v_mfma_f32_16x16x32_bf16 v[148:151], a[112:115], a[32:35], v[168:171]\n" \
"v_exp_f32_e64 v142, v142\n" \
"v_mfma_f32_16x16x32_bf16 v[148:151], a[116:119], a[36:39], v[148:151]\n" \
"v_mfma_f32_16x16x32_bf16 v[148:151], a[120:123], a[40:43], v[148:151]\n" \
"buffer_load_dwordx4 %[vd0], %[srdv], s48 offen offset:2048 lds\n" \
"v_mfma_f32_16x16x32_bf16 v[148:151], a[124:127], a[44:47], v[148:151]\n" \
"v_exp_f32_e64 v143, v143\n" \
"v_mfma_f32_16x16x32_bf16 v[96:99], a[160:163], v[204:207], v[96:99]\n" \
"ds_read_b64_tr_b16 a[160:161], %[vvb] offset:40960\n" \
"v_mfma_f32_16x16x32_bf16 v[96:99], a[128:131], v[220:223], v[96:99]\n" \
"v_exp_f32_e64 v144, v144\n" \
"v_mfma_f32_16x16x32_bf16 v[100:103], a[164:167], v[204:207], v[100:103]\n" \
"v_cvt_pk_bf16_f32 v198, v140, v141\n" \
"v_cvt_pk_bf16_f32 v199, v142, v143\n" \
"v_mfma_f32_16x16x32_bf16 v[100:103], a[132:135], v[220:223], v[100:103]\n" \
"v_exp_f32_e64 v145, v145\n" \
"v_mfma_f32_16x16x32_bf16 v[104:107], a[168:171], v[204:207], v[104:107]\n" \
"ds_read_b64_tr_b16 a[162:163], %[vvb] offset:45056\n" \
"ds_read_b64_tr_b16 a[164:165], v211 offset:40960\n" \
"v_mfma_f32_16x16x32_bf16 v[104:107], a[136:139], v[220:223], v[104:107]\n" \
"v_exp_f32_e64 v146, v146\n" \
"v_mfma_f32_16x16x32_bf16 v[108:111], a[172:175], v[204:207], v[108:111]\n" \
"ds_read_b64_tr_b16 a[166:167], v211 offset:45056\n" \
"v_mfma_f32_16x16x32_bf16 v[108:111], a[140:143], v[220:223], v[108:111]\n" \
"v_exp_f32_e64 v147, v147\n" \
"v_smfmac_f32_16x16x64_bf16 v[180:183], a[192:195], v[216:223], v228\n" \
"ds_read_b64_tr_b16 a[168:169], %[vvb] offset:41472\n" \
"v_mfma_f32_16x16x32_bf16 v[152:155], a[96:99], a[48:51], v[172:175]\n" \
"v_exp_f32_e64 v148, v148\n" \
"v_mfma_f32_16x16x32_bf16 v[152:155], a[100:103], a[52:55], v[152:155]\n" \
"ds_read_b64_tr_b16 a[170:171], %[vvb] offset:45568\n" \
"v_mfma_f32_16x16x32_bf16 v[152:155], a[104:107], a[56:59], v[152:155]\n" \
"v_exp_f32_e64 v149, v149\n" \
"v_mfma_f32_16x16x32_bf16 v[152:155], a[108:111], a[60:63], v[152:155]\n" \
"v_cvt_pk_bf16_f32 v200, v144, v145\n" \
"v_cvt_pk_bf16_f32 v201, v146, v147\n" \
"v_mfma_f32_16x16x32_bf16 v[156:159], a[112:115], a[48:51], v[172:175]\n" \
"v_exp_f32_e64 v150, v150\n" \
"v_mfma_f32_16x16x32_bf16 v[156:159], a[116:119], a[52:55], v[156:159]\n" \
"ds_read_b64_tr_b16 a[172:173], v211 offset:41472\n" \
"v_mfma_f32_16x16x32_bf16 v[156:159], a[120:123], a[56:59], v[156:159]\n" \
"buffer_load_dwordx4 v215, %[srdv], s48 offen offset:3072 lds\n" \
"v_mfma_f32_16x16x32_bf16 v[156:159], a[124:127], a[60:63], v[156:159]\n" \
"v_exp_f32_e64 v151, v151\n" \
"v_mfma_f32_16x16x32_bf16 v[112:115], a[176:179], v[204:207], v[112:115]\n" \
"ds_read_b64_tr_b16 a[174:175], v211 offset:45568\n" \
"ds_read_b64_tr_b16 a[176:177], %[vvb] offset:41984\n" \
"v_mfma_f32_16x16x32_bf16 v[112:115], a[144:147], v[220:223], v[112:115]\n" \
"v_exp_f32_e64 v152, v152\n" \
"v_mfma_f32_16x16x32_bf16 v[116:119], a[180:183], v[204:207], v[116:119]\n" \
"v_cvt_pk_bf16_f32 v202, v148, v149\n" \
".if . - adaln_body != 16384\n" \
".error \"size model\"\n" \
".endif\n" \
"Lk3_%=:\n" \
"s_cbranch_execz Lk4_%=\n" \
"s_nop 0\n" \
"v_cvt_pk_bf16_f32 v203, v150, v151\n" \
"v_mfma_f32_16x16x32_bf16 v[116:119], a[148:151], v[220:223], v[116:119]\n" \
"v_exp_f32_e64 v153, v153\n" \
"v_mfma_f32_16x16x32_bf16 v[120:123], a[184:187], v[204:207], v[120:123]\n" \
"v_exp_f32_e64 v154, v154\n" \
"v_mfma_f32_16x16x32_bf16 v[120:123], a[152:155], v[220:223], v[120:123]\n" \
"ds_read_b64_tr_b16 a[178:179], %[vvb] offset:46080\n" \
"v_mfma_f32_16x16x32_bf16 v[124:127], a[188:191], v[204:207], v[124:127]\n" \
"v_exp_f32_e64 v155, v155\n" \
"v_mfma_f32_16x16x32_bf16 v[124:127], a[156:159], v[220:223], v[124:127]\n" \
"ds_read_b64_tr_b16 a[180:181], v211 offset:41984\n" \
"s_waitcnt lgkmcnt(13)\n" \
"v_mfma_f32_16x16x32_bf16 v[128:131], a[64:67], a[0:3], v[160:163]\n" \
"v_exp_f32_e32 v156, v156\n" \
"v_mfma_f32_16x16x32_bf16 v[128:131], a[68:71], a[4:7], v[128:131]\n" \
"ds_read_b64_tr_b16 a[182:183], v211 offset:46080\n" \
"s_add_u32 m0, %[m0b], 32768\n" \
"v_mfma_f32_16x16x32_bf16 v[128:131], a[72:75], a[8:11], v[128:131]\n" \
"v_exp_f32_e64 v157, v157\n" \
"v_mfma_f32_16x16x32_bf16 v[128:131], a[76:79], a[12:15], v[128:131]\n" \
"v_cvt_pk_bf16_f32 v204, v152, v153\n" \
"v_cvt_pk_bf16_f32 v205, v154, v155\n" \
"v_mfma_f32_16x16x32_bf16 v[132:135], a[80:83], a[0:3], v[160:163]\n" \
"v_exp_f32_e64 v158, v158\n" \
"v_mfma_f32_16x16x32_bf16 v[132:135], a[84:87], a[4:7], v[132:135]\n" \
"ds_read_b64_tr_b16 a[184:185], %[vvb] offset:42496\n" \
"s_waitcnt lgkmcnt(5)\n" \
"v_mfma_f32_16x16x32_bf16 v[132:135], a[88:91], a[8:11], v[132:135]\n" \
"buffer_load_dwordx4 %[kd0], %[srdk], s48 offen offset:0 lds\n" \
"v_mfma_f32_16x16x32_bf16 v[132:135], a[92:95], a[12:15], v[132:135]\n" \
"v_exp_f32_e32 v159, v159\n" \
"v_mfma_f32_16x16x32_bf16 v[64:67], a[128:131], v[216:219], v[64:67]\n" \
"ds_read_b64_tr_b16 a[186:187], %[vvb] offset:46592\n" \
"ds_read_b64_tr_b16 a[188:189], v211 offset:42496\n" \
"v_mfma_f32_16x16x32_bf16 v[64:67], a[160:163], v[200:203], v[64:67]\n" \
"v_exp_f32_e64 v128, v128\n" \
"v_mfma_f32_16x16x32_bf16 v[68:71], a[132:135], v[216:219], v[68:71]\n" \
"v_cvt_pk_bf16_f32 v206, v156, v157\n" \
"v_cvt_pk_bf16_f32 v207, v158, v159\n" \
"v_mfma_f32_16x16x32_bf16 v[68:71], a[164:167], v[200:203], v[68:71]\n" \
"v_exp_f32_e64 v129, v129\n" \
"v_mfma_f32_16x16x32_bf16 v[72:75], a[136:139], v[216:219], v[72:75]\n" \
"ds_read_b64_tr_b16 a[190:191], v211 offset:46592\n" \
"ds_read_b64_tr_b16 a[128:129], %[vvb] offset:49152\n" \
"v_mfma_f32_16x16x32_bf16 v[72:75], a[168:171], v[200:203], v[72:75]\n" \
"v_exp_f32_e64 v130, v130\n" \
"v_mfma_f32_16x16x32_bf16 v[76:79], a[140:143], v[216:219], v[76:79]\n" \
"ds_read_b64_tr_b16 a[130:131], %[vvb] offset:53248\n" \
"ds_read_b64_tr_b16 a[132:133], v211 offset:49152\n" \
"v_mfma_f32_16x16x32_bf16 v[76:79], a[172:175], v[200:203], v[76:79]\n" \
"v_exp_f32_e64 v131, v131\n" \
"v_smfmac_f32_16x16x64_bf16 v[176:179], a[192:195], v[192:199], v228\n" \
"ds_read_b64_tr_b16 a[134:135], v211 offset:53248\n" \
"ds_read_b64_tr_b16 a[136:137], %[vvb] offset:49664\n" \
"v_mfma_f32_16x16x32_bf16 v[136:139], a[64:67], a[16:19], v[164:167]\n" \
"v_exp_f32_e64 v132, v132\n" \
"v_mfma_f32_16x16x32_bf16 v[136:139], a[68:71], a[20:23], v[136:139]\n" \
"ds_read_b64_tr_b16 a[138:139], %[vvb] offset:53760\n" \
"ds_read_b64_tr_b16 a[140:141], v211 offset:49664\n" \
"v_mfma_f32_16x16x32_bf16 v[136:139], a[72:75], a[24:27], v[136:139]\n" \
"v_exp_f32_e64 v133, v133\n" \
"v_mfma_f32_16x16x32_bf16 v[136:139], a[76:79], a[28:31], v[136:139]\n" \
"v_cvt_pk_bf16_f32 v184, v128, v129\n" \
"v_cvt_pk_bf16_f32 v185, v130, v131\n" \
"v_mfma_f32_16x16x32_bf16 v[140:143], a[80:83], a[16:19], v[164:167]\n" \
"v_exp_f32_e64 v134, v134\n" \
"v_mfma_f32_16x16x32_bf16 v[140:143], a[84:87], a[20:23], v[140:143]\n" \
"ds_read_b64_tr_b16 a[142:143], v211 offset:53760\n" \
"s_waitcnt lgkmcnt(8)\n" \
"v_mfma_f32_16x16x32_bf16 v[140:143], a[88:91], a[24:27], v[140:143]\n" \
"buffer_load_dwordx4 v212, %[srdk], s48 offen offset:1024 lds\n" \
"v_mfma_f32_16x16x32_bf16 v[140:143], a[92:95], a[28:31], v[140:143]\n" \
"v_exp_f32_e32 v135, v135\n" \
"v_mfma_f32_16x16x32_bf16 v[80:83], a[144:147], v[216:219], v[80:83]\n" \
"ds_read_b64_tr_b16 a[144:145], %[vvb] offset:50176\n" \
"ds_read_b64_tr_b16 a[146:147], %[vvb] offset:54272\n" \
"v_mfma_f32_16x16x32_bf16 v[80:83], a[176:179], v[200:203], v[80:83]\n" \
"v_exp_f32_e64 v136, v136\n" \
"v_mfma_f32_16x16x32_bf16 v[84:87], a[148:151], v[216:219], v[84:87]\n" \
"v_cvt_pk_bf16_f32 v186, v132, v133\n" \
"v_cvt_pk_bf16_f32 v187, v134, v135\n" \
"v_mfma_f32_16x16x32_bf16 v[84:87], a[180:183], v[200:203], v[84:87]\n" \
"v_exp_f32_e64 v137, v137\n" \
"v_mfma_f32_16x16x32_bf16 v[88:91], a[152:155], v[216:219], v[88:91]\n" \
"v_exp_f32_e64 v138, v138\n" \
"v_mfma_f32_16x16x32_bf16 v[88:91], a[184:187], v[200:203], v[88:91]\n" \
"ds_read_b64_tr_b16 a[148:149], v211 offset:50176\n" \
"ds_read_b64_tr_b16 a[150:151], v211 offset:54272\n" \
"v_mfma_f32_16x16x32_bf16 v[92:95], a[156:159], v[216:219], v[92:95]\n" \
"v_exp_f32_e64 v139, v139\n" \
"v_mfma_f32_16x16x32_bf16 v[92:95], a[188:191], v[200:203], v[92:95]\n" \
"ds_read_b64_tr_b16 a[152:153], %[vvb] offset:50688\n" \
"global_load_dword v237, %[vct], %[pct]\n" \
"v_mfma_f32_16x16x32_bf16 v[144:147], a[64:67], a[32:35], v[168:171]\n" \
"v_exp_f32_e64 v140, v140\n" \
"v_mfma_f32_16x16x32_bf16 v[144:147], a[68:71], a[36:39], v[144:147]\n" \
"ds_read_b64_tr_b16 a[154:155], %[vvb] offset:54784\n" \
"ds_read_b64_tr_b16 a[156:157], v211 offset:50688\n" \
"v_mfma_f32_16x16x32_bf16 v[144:147], a[72:75], a[40:43], v[144:147]\n" \
"v_exp_f32_e64 v141, v141\n" \
"v_mfma_f32_16x16x32_bf16 v[144:147], a[76:79], a[44:47], v[144:147]\n" \
"v_cvt_pk_bf16_f32 v188, v136, v137\n" \
"v_cvt_pk_bf16_f32 v189, v138, v139\n" \
"v_mfma_f32_16x16x32_bf16 v[148:151], a[80:83], a[32:35], v[168:171]\n" \
"v_exp_f32_e64 v142, v142\n" \
"v_mfma_f32_16x16x32_bf16 v[148:151], a[84:87], a[36:39], v[148:151]\n" \
"ds_read_b64_tr_b16 a[158:159], v211 offset:54784\n" \
"s_waitcnt lgkmcnt(8)\n" \
"v_mfma_f32_16x16x32_bf16 v[148:151], a[88:91], a[40:43], v[148:151]\n" \
"buffer_load_dwordx4 v213, %[srdk], s48 offen offset:2048 lds\n" \
"v_mfma_f32_16x16x32_bf16 v[148:151], a[92:95], a[44:47], v[148:151]\n" \
"v_exp_f32_e32 v143, v143\n" \
"v_mfma_f32_16x16x32_bf16 v[0:3], a[160:163], v[192:195], v[0:3]\n" \
"ds_read_b128 a[96:99], %[kb0] offset:57344\n" \
"v_mfma_f32_16x16x32_bf16 v[0:3], a[128:131], v[184:187], v[0:3]\n" \
"v_exp_f32_e64 v144, v144\n" \
"v_mfma_f32_16x16x32_bf16 v[4:7], a[164:167], v[192:195], v[4:7]\n" \
"v_cvt_pk_bf16_f32 v190, v140, v141\n" \
"v_cvt_pk_bf16_f32 v191, v142, v143\n" \
"v_mfma_f32_16x16x32_bf16 v[4:7], a[132:135], v[184:187], v[4:7]\n" \
"v_exp_f32_e64 v145, v145\n" \
"v_mfma_f32_16x16x32_bf16 v[8:11], a[168:171], v[192:195], v[8:11]\n" \
"ds_read_b128 a[100:103], v208 offset:57344\n" \
"v_mfma_f32_16x16x32_bf16 v[8:11], a[136:139], v[184:187], v[8:11]\n" \
"v_exp_f32_e64 v146, v146\n" \
"v_mfma_f32_16x16x32_bf16 v[12:15], a[172:175], v[192:195], v[12:15]\n" \
"ds_read_b128 a[104:107], v209 offset:57344\n" \
"v_mfma_f32_16x16x32_bf16 v[12:15], a[140:143], v[184:187], v[12:15]\n" \
"v_exp_f32_e64 v147, v147\n" \
"v_smfmac_f32_16x16x64_bf16 v[180:183], a[192:195], v[200:207], v228\n" \
"ds_read_b128 a[108:111], v210 offset:57344\n" \
"v_mfma_f32_16x16x32_bf16 v[152:155], a[64:67], a[48:51], v[172:175]\n" \
"v_exp_f32_e64 v148, v148\n" \
"v_mfma_f32_16x16x32_bf16 v[152:155], a[68:71], a[52:55], v[152:155]\n" \
"ds_read_b128 a[112:115], %[kb0] offset:61440\n" \
"v_mfma_f32_16x16x32_bf16 v[152:155], a[72:75], a[56:59], v[152:155]\n" \
"v_exp_f32_e64 v149, v149\n" \
"v_mfma_f32_16x16x32_bf16 v[152:155], a[76:79], a[60:63], v[152:155]\n" \
"v_cvt_pk_bf16_f32 v216, v144, v145\n" \
"v_cvt_pk_bf16_f32 v217, v146, v147\n" \
"v_mfma_f32_16x16x32_bf16 v[156:159], a[80:83], a[48:51], v[172:175]\n" \
"v_exp_f32_e64 v150, v150\n" \
"v_mfma_f32_16x16x32_bf16 v[156:159], a[84:87], a[52:55], v[156:159]\n" \
"ds_read_b128 a[116:119], v208 offset:61440\n" \
"s_waitcnt lgkmcnt(6)\n" \
"v_mfma_f32_16x16x32_bf16 v[156:159], a[88:91], a[56:59], v[156:159]\n" \
"buffer_load_dwordx4 v214, %[srdk], s48 offen offset:3072 lds\n" \
"v_mfma_f32_16x16x32_bf16 v[156:159], a[92:95], a[60:63], v[156:159]\n" \
"v_exp_f32_e32 v151, v151\n" \
"v_mfma_f32_16x16x32_bf16 v[16:19], a[176:179], v[192:195], v[16:19]\n" \
"ds_read_b128 a[120:123], v209 offset:61440\n" \
"v_mfma_f32_16x16x32_bf16 v[16:19], a[144:147], v[184:187], v[16:19]\n" \
"v_exp_f32_e64 v152, v152\n" \
"v_mfma_f32_16x16x32_bf16 v[20:23], a[180:183], v[192:195], v[20:23]\n" \
"v_cvt_pk_bf16_f32 v218, v148, v149\n" \
"v_cvt_pk_bf16_f32 v219, v150, v151\n" \
"v_mfma_f32_16x16x32_bf16 v[20:23], a[148:151], v[184:187], v[20:23]\n" \
"v_exp_f32_e64 v153, v153\n" \
"v_mfma_f32_16x16x32_bf16 v[24:27], a[184:187], v[192:195], v[24:27]\n" \
"v_exp_f32_e64 v154, v154\n" \
"v_mfma_f32_16x16x32_bf16 v[24:27], a[152:155], v[184:187], v[24:27]\n" \
"ds_read_b128 a[124:127], v210 offset:61440\n" \
"s_add_u32 s48, s48, 0x4000\n" \
"s_mov_b32 m0, s44\n" \
"s_addk_i32 s44, 0x400\n" \
"buffer_load_dwordx4 v229, s[64:67], s45 offen offset:0 nt lds\n" \
"s_add_u32 s45, s45, 393216\n" \
"v_mfma_f32_16x16x32_bf16 v[28:31], a[188:191], v[192:195], v[28:31]\n" \
"v_exp_f32_e64 v155, v155\n" \
"v_mfma_f32_16x16x32_bf16 v[28:31], a[156:159], v[184:187], v[28:31]\n" \
"s_waitcnt vmcnt(15) lgkmcnt(2)\n" \
"s_barrier\n" \
"v_mfma_f32_16x16x32_bf16 v[128:131], a[96:99], a[0:3], v[160:163]\n" \
"v_exp_f32_e64 v156, v156\n" \
"v_mfma_f32_16x16x32_bf16 v[128:131], a[100:103], a[4:7], v[128:131]\n" \
"s_add_u32 m0, %[m0b], 98304\n" \
"v_mfma_f32_16x16x32_bf16 v[128:131], a[104:107], a[8:11], v[128:131]\n" \
"v_exp_f32_e64 v157, v157\n" \
"v_mfma_f32_16x16x32_bf16 v[128:131], a[108:111], a[12:15], v[128:131]\n" \
"v_cvt_pk_bf16_f32 v220, v152, v153\n" \
"v_cvt_pk_bf16_f32 v221, v154, v155\n" \
"v_mfma_f32_16x16x32_bf16 v[132:135], a[112:115], a[0:3], v[160:163]\n" \
"v_exp_f32_e64 v158, v158\n" \
"v_mfma_f32_16x16x32_bf16 v[132:135], a[116:119], a[4:7], v[132:135]\n" \
"s_waitcnt lgkmcnt(0)\n" \
"v_mfma_f32_16x16x32_bf16 v[132:135], a[120:123], a[8:11], v[132:135]\n" \
"buffer_load_dwordx4 %[vd0], %[srdv], s48 offen offset:0 lds\n" \
"v_mfma_f32_16x16x32_bf16 v[132:135], a[124:127], a[12:15], v[132:135]\n" \
"v_exp_f32_e32 v159, v159\n" \
"v_mfma_f32_16x16x32_bf16 v[32:35], a[160:163], v[196:199], v[32:35]\n" \
"ds_read_b128 a[64:67], %[kb0] offset:0\n" \
"v_mfma_f32_16x16x32_bf16 v[32:35], a[128:131], v[188:191], v[32:35]\n" \
"v_exp_f32_e64 v128, v128\n" \
"v_mfma_f32_16x16x32_bf16 v[36:39], a[164:167], v[196:199], v[36:39]\n" \
"v_cvt_pk_bf16_f32 v222, v156, v157\n" \
"v_cvt_pk_bf16_f32 v223, v158, v159\n" \
"v_mfma_f32_16x16x32_bf16 v[36:39], a[132:135], v[188:191], v[36:39]\n" \
"v_exp_f32_e64 v129, v129\n" \
"v_mfma_f32_16x16x32_bf16 v[40:43], a[168:171], v[196:199], v[40:43]\n" \
"ds_read_b128 a[68:71], v208 offset:0\n" \
"v_mfma_f32_16x16x32_bf16 v[40:43], a[136:139], v[188:191], v[40:43]\n" \
"v_exp_f32_e64 v130, v130\n" \
"v_mfma_f32_16x16x32_bf16 v[44:47], a[172:175], v[196:199], v[44:47]\n" \
"ds_read_b128 a[72:75], v209 offset:0\n" \
"v_mfma_f32_16x16x32_bf16 v[44:47], a[140:143], v[188:191], v[44:47]\n" \
"v_exp_f32_e64 v131, v131\n" \
"v_smfmac_f32_16x16x64_bf16 v[176:179], a[192:195], v[184:191], v228\n" \
"v_mfma_f32_16x16x32_bf16 v[136:139], a[96:99], a[16:19], v[164:167]\n" \
"v_exp_f32_e64 v132, v132\n" \
"v_mfma_f32_16x16x32_bf16 v[136:139], a[100:103], a[20:23], v[136:139]\n" \
"ds_read_b128 a[76:79], v210 offset:0\n" \
"v_mfma_f32_16x16x32_bf16 v[136:139], a[104:107], a[24:27], v[136:139]\n" \
"v_exp_f32_e64 v133, v133\n" \
"v_mfma_f32_16x16x32_bf16 v[136:139], a[108:111], a[28:31], v[136:139]\n" \
"v_cvt_pk_bf16_f32 v192, v128, v129\n" \
"v_cvt_pk_bf16_f32 v193, v130, v131\n" \
"v_mfma_f32_16x16x32_bf16 v[140:143], a[112:115], a[16:19], v[164:167]\n" \
"v_exp_f32_e64 v134, v134\n" \
"v_mfma_f32_16x16x32_bf16 v[140:143], a[116:119], a[20:23], v[140:143]\n" \
"ds_read_b128 a[80:83], %[kb0] offset:4096\n" \
"v_mfma_f32_16x16x32_bf16 v[140:143], a[120:123], a[24:27], v[140:143]\n" \
"buffer_load_dwordx4 v215, %[srdv], s48 offen offset:1024 lds\n" \
"v_mfma_f32_16x16x32_bf16 v[140:143], a[124:127], a[28:31], v[140:143]\n" \
"v_exp_f32_e64 v135, v135\n" \
"v_mfma_f32_16x16x32_bf16 v[48:51], a[176:179], v[196:199], v[48:51]\n" \
"ds_read_b128 a[84:87], v208 offset:4096\n" \
"v_mfma_f32_16x16x32_bf16 v[48:51], a[144:147], v[188:191], v[48:51]\n" \
"v_exp_f32_e64 v136, v136\n" \
"v_mfma_f32_16x16x32_bf16 v[52:55], a[180:183], v[196:199], v[52:55]\n" \
"v_cvt_pk_bf16_f32 v194, v132, v133\n" \
"v_cvt_pk_bf16_f32 v195, v134, v135\n" \
"v_mfma_f32_16x16x32_bf16 v[52:55], a[148:151], v[188:191], v[52:55]\n" \
"v_exp_f32_e64 v137, v137\n" \
"v_mfma_f32_16x16x32_bf16 v[56:59], a[184:187], v[196:199], v[56:59]\n" \
"v_exp_f32_e64 v138, v138\n" \
"v_mfma_f32_16x16x32_bf16 v[56:59], a[152:155], v[188:191], v[56:59]\n" \
"ds_read_b128 a[88:91], v209 offset:4096\n" \
"v_mfma_f32_16x16x32_bf16 v[60:63], a[188:191], v[196:199], v[60:63]\n" \
"v_exp_f32_e64 v139, v139\n" \
"v_mfma_f32_16x16x32_bf16 v[60:63], a[156:159], v[188:191], v[60:63]\n" \
"s_cmp_lg_u32 %[wid], 2\n" \
"s_cbranch_scc1 LtA7_%=\n" \
"global_load_dword v237, v224, s[58:59]\n" \
"s_add_u32 s61, s48, 49152\n" \
"s_mov_b32 exec_hi, 0\n" \
"s_nop 0\n" \
"buffer_load_dword v237, v224, %[srdp], s61 offen\n" \
"s_mov_b32 exec_hi, -1\n" \
"s_nop 0\n" \
"LtA7_%=:\n" \
"v_mfma_f32_16x16x32_bf16 v[144:147], a[96:99], a[32:35], v[168:171]\n" \
"v_exp_f32_e64 v140, v140\n" \
"v_mfma_f32_16x16x32_bf16 v[144:147], a[100:103], a[36:39], v[144:147]\n" \
"ds_read_b128 a[92:95], v210 offset:4096\n" \
"v_mfma_f32_16x16x32_bf16 v[144:147], a[104:107], a[40:43], v[144:147]\n" \
"v_exp_f32_e64 v141, v141\n" \
"v_mfma_f32_16x16x32_bf16 v[144:147], a[108:111], a[44:47], v[144:147]\n" \
"v_cvt_pk_bf16_f32 v196, v136, v137\n" \
"v_cvt_pk_bf16_f32 v197, v138, v139\n" \
"v_mfma_f32_16x16x32_bf16 v[148:151], a[112:115], a[32:35], v[168:171]\n" \
"v_exp_f32_e64 v142, v142\n" \
"v_mfma_f32_16x16x32_bf16 v[148:151], a[116:119], a[36:39], v[148:151]\n" \
"v_mfma_f32_16x16x32_bf16 v[148:151], a[120:123], a[40:43], v[148:151]\n" \
"buffer_load_dwordx4 %[vd0], %[srdv], s48 offen offset:2048 lds\n" \
"v_mfma_f32_16x16x32_bf16 v[148:151], a[124:127], a[44:47], v[148:151]\n" \
"v_exp_f32_e64 v143, v143\n" \
"v_mfma_f32_16x16x32_bf16 v[96:99], a[160:163], v[204:207], v[96:99]\n" \
"ds_read_b64_tr_b16 a[160:161], %[vvb] offset:57344\n" \
"v_mfma_f32_16x16x32_bf16 v[96:99], a[128:131], v[220:223], v[96:99]\n" \
"v_exp_f32_e64 v144, v144\n" \
"v_mfma_f32_16x16x32_bf16 v[100:103], a[164:167], v[204:207], v[100:103]\n" \
"v_cvt_pk_bf16_f32 v198, v140, v141\n" \
"v_cvt_pk_bf16_f32 v199, v142, v143\n" \
"v_mfma_f32_16x16x32_bf16 v[100:103], a[132:135], v[220:223], v[100:103]\n" \
"v_exp_f32_e64 v145, v145\n" \
"v_mfma_f32_16x16x32_bf16 v[104:107], a[168:171], v[204:207], v[104:107]\n" \
"ds_read_b64_tr_b16 a[162:163], %[vvb] offset:61440\n" \
"ds_read_b64_tr_b16 a[164:165], v211 offset:57344\n" \
"v_mfma_f32_16x16x32_bf16 v[104:107], a[136:139], v[220:223], v[104:107]\n" \
"v_exp_f32_e64 v146, v146\n" \
"v_mfma_f32_16x16x32_bf16 v[108:111], a[172:175], v[204:207], v[108:111]\n" \
"ds_read_b64_tr_b16 a[166:167], v211 offset:61440\n" \
"v_mfma_f32_16x16x32_bf16 v[108:111], a[140:143], v[220:223], v[108:111]\n" \
"v_exp_f32_e64 v147, v147\n" \
"v_smfmac_f32_16x16x64_bf16 v[180:183], a[192:195], v[216:223], v228\n" \
"ds_read_b64_tr_b16 a[168:169], %[vvb] offset:57856\n" \
"v_mfma_f32_16x16x32_bf16 v[152:155], a[96:99], a[48:51], v[172:175]\n" \
"v_exp_f32_e64 v148, v148\n" \
"v_mfma_f32_16x16x32_bf16 v[152:155], a[100:103], a[52:55], v[152:155]\n" \
"ds_read_b64_tr_b16 a[170:171], %[vvb] offset:61952\n" \
"v_mfma_f32_16x16x32_bf16 v[152:155], a[104:107], a[56:59], v[152:155]\n" \
"v_exp_f32_e64 v149, v149\n" \
"v_mfma_f32_16x16x32_bf16 v[152:155], a[108:111], a[60:63], v[152:155]\n" \
"v_cvt_pk_bf16_f32 v200, v144, v145\n" \
"v_cvt_pk_bf16_f32 v201, v146, v147\n" \
"v_mfma_f32_16x16x32_bf16 v[156:159], a[112:115], a[48:51], v[172:175]\n" \
"v_exp_f32_e64 v150, v150\n" \
"v_mfma_f32_16x16x32_bf16 v[156:159], a[116:119], a[52:55], v[156:159]\n" \
"ds_read_b64_tr_b16 a[172:173], v211 offset:57856\n" \
"v_mfma_f32_16x16x32_bf16 v[156:159], a[120:123], a[56:59], v[156:159]\n" \
"buffer_load_dwordx4 v215, %[srdv], s48 offen offset:3072 lds\n" \
"v_mfma_f32_16x16x32_bf16 v[156:159], a[124:127], a[60:63], v[156:159]\n" \
"v_exp_f32_e64 v151, v151\n" \
"v_mfma_f32_16x16x32_bf16 v[112:115], a[176:179], v[204:207], v[112:115]\n" \
"ds_read_b64_tr_b16 a[174:175], v211 offset:61952\n" \
"ds_read_b64_tr_b16 a[176:177], %[vvb] offset:58368\n" \
"v_mfma_f32_16x16x32_bf16 v[112:115], a[144:147], v[220:223], v[112:115]\n" \
"v_exp_f32_e64 v152, v152\n" \
"v_mfma_f32_16x16x32_bf16 v[116:119], a[180:183], v[204:207], v[116:119]\n" \
"v_cvt_pk_bf16_f32 v202, v148, v149\n" \
"v_cvt_pk_bf16_f32 v203, v150, v151\n" \
"v_mfma_f32_16x16x32_bf16 v[116:119], a[148:151], v[220:223], v[116:119]\n" \
"v_exp_f32_e64 v153, v153\n" \
"v_mfma_f32_16x16x32_bf16 v[120:123], a[184:187], v[204:207], v[120:123]\n" \
"v_exp_f32_e64 v154, v154\n" \
"v_mfma_f32_16x16x32_bf16 v[120:123], a[152:155], v[220:223], v[120:123]\n" \
"ds_read_b64_tr_b16 a[178:179], %[vvb] offset:62464\n" \
"v_mfma_f32_16x16x32_bf16 v[124:127], a[188:191], v[204:207], v[124:127]\n" \
"v_exp_f32_e64 v155, v155\n" \
"v_mfma_f32_16x16x32_bf16 v[124:127], a[156:159], v[220:223], v[124:127]\n" \
"ds_read_b64_tr_b16 a[180:181], v211 offset:58368\n" \
"s_waitcnt lgkmcnt(13)\n" \
"v_mfma_f32_16x16x32_bf16 v[128:131], a[64:67], a[0:3], v[160:163]\n" \
"v_exp_f32_e32 v156, v156\n" \
"v_mfma_f32_16x16x32_bf16 v[128:131], a[68:71], a[4:7], v[128:131]\n" \
"ds_read_b64_tr_b16 a[182:183], v211 offset:62464\n" \
"s_add_u32 m0, %[m0b], 49152\n" \
"v_mfma_f32_16x16x32_bf16 v[128:131], a[72:75], a[8:11], v[128:131]\n" \
"v_exp_f32_e64 v157, v157\n" \
"v_mfma_f32_16x16x32_bf16 v[128:131], a[76:79], a[12:15], v[128:131]\n" \
"v_cvt_pk_bf16_f32 v204, v152, v153\n" \
"v_cvt_pk_bf16_f32 v205, v154, v155\n" \
"v_mfma_f32_16x16x32_bf16 v[132:135], a[80:83], a[0:3], v[160:163]\n" \
"v_exp_f32_e64 v158, v158\n" \
"v_mfma_f32_16x16x32_bf16 v[132:135], a[84:87], a[4:7], v[132:135]\n" \
"ds_read_b64_tr_b16 a[184:185], %[vvb] offset:58880\n" \
"s_waitcnt lgkmcnt(5)\n" \
"v_mfma_f32_16x16x32_bf16 v[132:135], a[88:91], a[8:11], v[132:135]\n" \
"buffer_load_dwordx4 %[kd0], %[srdk], s48 offen offset:0 lds\n" \
"v_mfma_f32_16x16x32_bf16 v[132:135], a[92:95], a[12:15], v[132:135]\n" \
"v_exp_f32_e32 v159, v159\n" \
"v_mfma_f32_16x16x32_bf16 v[64:67], a[128:131], v[216:219], v[64:67]\n" \
"ds_read_b64_tr_b16 a[186:187], %[vvb] offset:62976\n" \
"ds_read_b64_tr_b16 a[188:189], v211 offset:58880\n" \
"v_mfma_f32_16x16x32_bf16 v[64:67], a[160:163], v[200:203], v[64:67]\n" \
"v_exp_f32_e64 v128, v128\n" \
"v_mfma_f32_16x16x32_bf16 v[68:71], a[132:135], v[216:219], v[68:71]\n" \
"v_cvt_pk_bf16_f32 v206, v156, v157\n" \
"v_cvt_pk_bf16_f32 v207, v158, v159\n" \
"v_mfma_f32_16x16x32_bf16 v[68:71], a[164:167], v[200:203], v[68:71]\n" \
"v_exp_f32_e64 v129, v129\n" \
"v_mfma_f32_16x16x32_bf16 v[72:75], a[136:139], v[216:219], v[72:75]\n" \
"ds_read_b64_tr_b16 a[190:191], v211 offset:62976\n" \
"ds_read_b64_tr_b16 a[128:129], %[vvb] offset:0\n" \
"v_mfma_f32_16x16x32_bf16 v[72:75], a[168:171], v[200:203], v[72:75]\n" \
"v_exp_f32_e64 v130, v130\n" \
"v_mfma_f32_16x16x32_bf16 v[76:79], a[140:143], v[216:219], v[76:79]\n" \
"ds_read_b64_tr_b16 a[130:131], %[vvb] offset:4096\n" \
"ds_read_b64_tr_b16 a[132:133], v211 offset:0\n" \
"v_mfma_f32_16x16x32_bf16 v[76:79], a[172:175], v[200:203], v[76:79]\n" \
"v_exp_f32_e64 v131, v131\n" \
"v_smfmac_f32_16x16x64_bf16 v[176:179], a[192:195], v[192:199], v228\n" \
"ds_read_b64_tr_b16 a[134:135], v211 offset:4096\n" \
"ds_read_b64_tr_b16 a[136:137], %[vvb] offset:512\n" \
"v_mfma_f32_16x16x32_bf16 v[136:139], a[64:67], a[16:19], v[164:167]\n" \
"v_exp_f32_e64 v132, v132\n" \
"v_mfma_f32_16x16x32_bf16 v[136:139], a[68:71], a[20:23], v[136:139]\n" \
"ds_read_b64_tr_b16 a[138:139], %[vvb] offset:4608\n" \
"ds_read_b64_tr_b16 a[140:141], v211 offset:512\n" \
"v_mfma_f32_16x16x32_bf16 v[136:139], a[72:75], a[24:27], v[136:139]\n" \
"v_exp_f32_e64 v133, v133\n" \
"v_mfma_f32_16x16x32_bf16 v[136:139], a[76:79], a[28:31], v[136:139]\n" \
"v_cvt_pk_bf16_f32 v184, v128, v129\n" \
"v_cvt_pk_bf16_f32 v185, v130, v131\n" \
"v_mfma_f32_16x16x32_bf16 v[140:143], a[80:83], a[16:19], v[164:167]\n" \
"v_exp_f32_e64 v134, v134\n" \
"v_mfma_f32_16x16x32_bf16 v[140:143], a[84:87], a[20:23], v[140:143]\n" \
"ds_read_b64_tr_b16 a[142:143], v211 offset:4608\n" \
"s_waitcnt lgkmcnt(8)\n" \
"v_mfma_f32_16x16x32_bf16 v[140:143], a[88:91], a[24:27], v[140:143]\n" \
"buffer_load_dwordx4 v212, %[srdk], s48 offen offset:1024 lds\n" \
"v_mfma_f32_16x16x32_bf16 v[140:143], a[92:95], a[28:31], v[140:143]\n" \
"v_exp_f32_e32 v135, v135\n" \
"v_mfma_f32_16x16x32_bf16 v[80:83], a[144:147], v[216:219], v[80:83]\n" \
"ds_read_b64_tr_b16 a[144:145], %[vvb] offset:1024\n" \
"ds_read_b64_tr_b16 a[146:147], %[vvb] offset:5120\n" \
"v_mfma_f32_16x16x32_bf16 v[80:83], a[176:179], v[200:203], v[80:83]\n" \
"v_exp_f32_e64 v136, v136\n" \
"v_mfma_f32_16x16x32_bf16 v[84:87], a[148:151], v[216:219], v[84:87]\n" \
"v_cvt_pk_bf16_f32 v186, v132, v133\n" \
"v_cvt_pk_bf16_f32 v187, v134, v135\n" \
"v_mfma_f32_16x16x32_bf16 v[84:87], a[180:183], v[200:203], v[84:87]\n" \
"v_exp_f32_e64 v137, v137\n" \
"v_mfma_f32_16x16x32_bf16 v[88:91], a[152:155], v[216:219], v[88:91]\n" \
"v_exp_f32_e64 v138, v138\n" \
"v_mfma_f32_16x16x32_bf16 v[88:91], a[184:187], v[200:203], v[88:91]\n" \
"ds_read_b64_tr_b16 a[148:149], v211 offset:1024\n" \
"ds_read_b64_tr_b16 a[150:151], v211 offset:5120\n" \
"v_mfma_f32_16x16x32_bf16 v[92:95], a[156:159], v[216:219], v[92:95]\n" \
"v_exp_f32_e64 v139, v139\n" \
"v_mfma_f32_16x16x32_bf16 v[92:95], a[188:191], v[200:203], v[92:95]\n" \
"ds_read_b64_tr_b16 a[152:153], %[vvb] offset:1536\n" \
"buffer_store_dwordx4 a[196:199], v235, %[srdz], s62 offen offset:1024 sc1\n" \
"s_add_u32 s62, s62, 2048\n" \
"v_mfma_f32_16x16x32_bf16 v[144:147], a[64:67], a[32:35], v[168:171]\n" \
"v_exp_f32_e64 v140, v140\n" \
"v_mfma_f32_16x16x32_bf16 v[144:147], a[68:71], a[36:39], v[144:147]\n" \
"ds_read_b64_tr_b16 a[154:155], %[vvb] offset:5632\n" \
"ds_read_b64_tr_b16 a[156:157], v211 offset:1536\n" \
"v_mfma_f32_16x16x32_bf16 v[144:147], a[72:75], a[40:43], v[144:147]\n" \
"v_exp_f32_e64 v141, v141\n" \
"v_mfma_f32_16x16x32_bf16 v[144:147], a[76:79], a[44:47], v[144:147]\n" \
"v_cvt_pk_bf16_f32 v188, v136, v137\n" \
"v_cvt_pk_bf16_f32 v189, v138, v139\n" \
"v_mfma_f32_16x16x32_bf16 v[148:151], a[80:83], a[32:35], v[168:171]\n" \
"v_exp_f32_e64 v142, v142\n" \
"v_mfma_f32_16x16x32_bf16 v[148:151], a[84:87], a[36:39], v[148:151]\n" \
"ds_read_b64_tr_b16 a[158:159], v211 offset:5632\n" \
"s_waitcnt lgkmcnt(8)\n" \
"v_mfma_f32_16x16x32_bf16 v[148:151], a[88:91], a[40:43], v[148:151]\n" \
"buffer_load_dwordx4 v213, %[srdk], s48 offen offset:2048 lds\n" \
"v_mfma_f32_16x16x32_bf16 v[148:151], a[92:95], a[44:47], v[148:151]\n" \
"v_exp_f32_e32 v143, v143\n" \
"v_mfma_f32_16x16x32_bf16 v[0:3], a[160:163], v[192:195], v[0:3]\n" \
"ds_read_b128 a[96:99], %[kb0] offset:8192\n" \
"v_mfma_f32_16x16x32_bf16 v[0:3], a[128:131], v[184:187], v[0:3]\n" \
"v_exp_f32_e64 v144, v144\n" \
"v_mfma_f32_16x16x32_bf16 v[4:7], a[164:167], v[192:195], v[4:7]\n" \
"v_cvt_pk_bf16_f32 v190, v140, v141\n" \
"v_cvt_pk_bf16_f32 v191, v142, v143\n" \
"v_mfma_f32_16x16x32_bf16 v[4:7], a[132:135], v[184:187], v[4:7]\n" \
"v_exp_f32_e64 v145, v145\n" \
"v_mfma_f32_16x16x32_bf16 v[8:11], a[168:171], v[192:195], v[8:11]\n" \
"ds_read_b128 a[100:103], v208 offset:8192\n" \
"v_mfma_f32_16x16x32_bf16 v[8:11], a[136:139], v[184:187], v[8:11]\n" \
"v_exp_f32_e64 v146, v146\n" \
"v_mfma_f32_16x16x32_bf16 v[12:15], a[172:175], v[192:195], v[12:15]\n" \
"ds_read_b128 a[104:107], v209 offset:8192\n" \
"v_mfma_f32_16x16x32_bf16 v[12:15], a[140:143], v[184:187], v[12:15]\n" \
"v_exp_f32_e64 v147, v147\n" \
"v_smfmac_f32_16x16x64_bf16 v[180:183], a[192:195], v[200:207], v228\n" \
"ds_read_b128 a[108:111], v210 offset:8192\n" \
"v_mfma_f32_16x16x32_bf16 v[152:155], a[64:67], a[48:51], v[172:175]\n" \
"v_exp_f32_e64 v148, v148\n" \
"v_mfma_f32_16x16x32_bf16 v[152:155], a[68:71], a[52:55], v[152:155]\n" \
"ds_read_b128 a[112:115], %[kb0] offset:12288\n" \
"v_mfma_f32_16x16x32_bf16 v[152:155], a[72:75], a[56:59], v[152:155]\n" \
"v_exp_f32_e64 v149, v149\n" \
"v_mfma_f32_16x16x32_bf16 v[152:155], a[76:79], a[60:63], v[152:155]\n" \
"v_cvt_pk_bf16_f32 v216, v144, v145\n" \
"v_cvt_pk_bf16_f32 v217, v146, v147\n" \
"v_mfma_f32_16x16x32_bf16 v[156:159], a[80:83], a[48:51], v[172:175]\n" \
"v_exp_f32_e64 v150, v150\n" \
"v_mfma_f32_16x16x32_bf16 v[156:159], a[84:87], a[52:55], v[156:159]\n" \
"ds_read_b128 a[116:119], v208 offset:12288\n" \
"s_waitcnt lgkmcnt(6)\n" \
"v_mfma_f32_16x16x32_bf16 v[156:159], a[88:91], a[56:59], v[156:159]\n" \
"buffer_load_dwordx4 v214, %[srdk], s48 offen offset:3072 lds\n" \
"v_mfma_f32_16x16x32_bf16 v[156:159], a[92:95], a[60:63], v[156:159]\n" \
"v_exp_f32_e32 v151, v151\n" \
"v_mfma_f32_16x16x32_bf16 v[16:19], a[176:179], v[192:195], v[16:19]\n" \
"ds_read_b128 a[120:123], v209 offset:12288\n" \
"v_mfma_f32_16x16x32_bf16 v[16:19], a[144:147], v[184:187], v[16:19]\n" \
"v_exp_f32_e64 v152, v152\n" \
"v_mfma_f32_16x16x32_bf16 v[20:23], a[180:183], v[192:195], v[20:23]\n" \
"v_cvt_pk_bf16_f32 v218, v148, v149\n" \
"v_cvt_pk_bf16_f32 v219, v150, v151\n" \
"v_mfma_f32_16x16x32_bf16 v[20:23], a[148:151], v[184:187], v[20:23]\n" \
"v_exp_f32_e64 v153, v153\n" \
"v_mfma_f32_16x16x32_bf16 v[24:27], a[184:187], v[192:195], v[24:27]\n" \
"v_exp_f32_e64 v154, v154\n" \
"v_mfma_f32_16x16x32_bf16 v[24:27], a[152:155], v[184:187], v[24:27]\n" \
"ds_read_b128 a[124:127], v210 offset:12288\n" \
"s_add_u32 s48, s48, 0x4000\n" \
"v_mfma_f32_16x16x32_bf16 v[28:31], a[188:191], v[192:195], v[28:31]\n" \
"v_exp_f32_e64 v155, v155\n" \
"v_mfma_f32_16x16x32_bf16 v[28:31], a[156:159], v[184:187], v[28:31]\n" \
"s_waitcnt vmcnt(15) lgkmcnt(2)\n" \
"s_barrier\n" \
"v_mfma_f32_16x16x32_bf16 v[128:131], a[96:99], a[0:3], v[160:163]\n" \
"v_exp_f32_e64 v156, v156\n" \
"v_mfma_f32_16x16x32_bf16 v[128:131], a[100:103], a[4:7], v[128:131]\n" \
"s_add_u32 m0, %[m0b], 114688\n" \
"v_mfma_f32_16x16x32_bf16 v[128:131], a[104:107], a[8:11], v[128:131]\n" \
"v_exp_f32_e64 v157, v157\n" \
"v_mfma_f32_16x16x32_bf16 v[128:131], a[108:111], a[12:15], v[128:131]\n" \
"v_cvt_pk_bf16_f32 v220, v152, v153\n" \
"v_cvt_pk_bf16_f32 v221, v154, v155\n" \
"v_mfma_f32_16x16x32_bf16 v[132:135], a[112:115], a[0:3], v[160:163]\n" \
"v_exp_f32_e64 v158, v158\n" \
"v_mfma_f32_16x16x32_bf16 v[132:135], a[116:119], a[4:7], v[132:135]\n" \
"s_waitcnt lgkmcnt(0)\n" \
"v_mfma_f32_16x16x32_bf16 v[132:135], a[120:123], a[8:11], v[132:135]\n" \
"buffer_load_dwordx4 %[vd0], %[srdv], s48 offen offset:0 lds\n" \
"v_mfma_f32_16x16x32_bf16 v[132:135], a[124:127], a[12:15], v[132:135]\n" \
"v_exp_f32_e32 v159, v159\n" \
"v_mfma_f32_16x16x32_bf16 v[32:35], a[160:163], v[196:199], v[32:35]\n" \
"ds_read_b128 a[64:67], %[kb0] offset:16384\n" \
"v_mfma_f32_16x16x32_bf16 v[32:35], a[128:131], v[188:191], v[32:35]\n" \
"v_exp_f32_e64 v128, v128\n" \
"v_mfma_f32_16x16x32_bf16 v[36:39], a[164:167], v[196:199], v[36:39]\n" \
"v_cvt_pk_bf16_f32 v222, v156, v157\n" \
"v_cvt_pk_bf16_f32 v223, v158, v159\n" \
"v_mfma_f32_16x16x32_bf16 v[36:39], a[132:135], v[188:191], v[36:39]\n" \
"v_exp_f32_e64 v129, v129\n" \
"v_mfma_f32_16x16x32_bf16 v[40:43], a[168:171], v[196:199], v[40:43]\n" \
"ds_read_b128 a[68:71], v208 offset:16384\n" \
"v_mfma_f32_16x16x32_bf16 v[40:43], a[136:139], v[188:191], v[40:43]\n" \
"v_exp_f32_e64 v130, v130\n" \
"v_mfma_f32_16x16x32_bf16 v[44:47], a[172:175], v[196:199], v[44:47]\n" \
"ds_read_b128 a[72:75], v209 offset:16384\n" \
"v_mfma_f32_16x16x32_bf16 v[44:47], a[140:143], v[188:191], v[44:47]\n" \
"v_exp_f32_e64 v131, v131\n" \
"v_smfmac_f32_16x16x64_bf16 v[176:179], a[192:195], v[184:191], v228\n" \
"v_mfma_f32_16x16x32_bf16 v[136:139], a[96:99], a[16:19], v[164:167]\n" \
"v_exp_f32_e64 v132, v132\n" \
"v_mfma_f32_16x16x32_bf16 v[136:139], a[100:103], a[20:23], v[136:139]\n" \
"ds_read_b128 a[76:79], v210 offset:16384\n" \
"v_mfma_f32_16x16x32_bf16 v[136:139], a[104:107], a[24:27], v[136:139]\n" \
"v_exp_f32_e64 v133, v133\n" \
"v_mfma_f32_16x16x32_bf16 v[136:139], a[108:111], a[28:31], v[136:139]\n" \
"v_cvt_pk_bf16_f32 v192, v128, v129\n" \
"v_cvt_pk_bf16_f32 v193, v130, v131\n" \
"v_mfma_f32_16x16x32_bf16 v[140:143], a[112:115], a[16:19], v[164:167]\n" \
"v_exp_f32_e64 v134, v134\n" \
"v_mfma_f32_16x16x32_bf16 v[140:143], a[116:119], a[20:23], v[140:143]\n" \
"ds_read_b128 a[80:83], %[kb0] offset:20480\n" \
"v_mfma_f32_16x16x32_bf16 v[140:143], a[120:123], a[24:27], v[140:143]\n" \
"buffer_load_dwordx4 v215, %[srdv], s48 offen offset:1024 lds\n" \
"v_mfma_f32_16x16x32_bf16 v[140:143], a[124:127], a[28:31], v[140:143]\n" \
".if . - adaln_body != 20480\n" \
".error \"size model\"\n" \
".endif\n" \
"Lk4_%=:\n" \
"s_cbranch_execz Lk5_%=\n" \
"s_nop 0\n" \
"v_exp_f32_e64 v135, v135\n" \
"v_mfma_f32_16x16x32_bf16 v[48:51], a[176:179], v[196:199], v[48:51]\n" \
"ds_read_b128 a[84:87], v208 offset:20480\n" \
"v_mfma_f32_16x16x32_bf16 v[48:51], a[144:147], v[188:191], v[48:51]\n" \
"v_exp_f32_e64 v136, v136\n" \
"v_mfma_f32_16x16x32_bf16 v[52:55], a[180:183], v[196:199], v[52:55]\n" \
"v_cvt_pk_bf16_f32 v194, v132, v133\n" \
"v_cvt_pk_bf16_f32 v195, v134, v135\n" \
"v_mfma_f32_16x16x32_bf16 v[52:55], a[148:151], v[188:191], v[52:55]\n" \
"v_exp_f32_e64 v137, v137\n" \
"v_mfma_f32_16x16x32_bf16 v[56:59], a[184:187], v[196:199], v[56:59]\n" \
"v_exp_f32_e64 v138, v138\n" \
"v_mfma_f32_16x16x32_bf16 v[56:59], a[152:155], v[188:191], v[56:59]\n" \
"ds_read_b128 a[88:91], v209 offset:20480\n" \
"v_mfma_f32_16x16x32_bf16 v[60:63], a[188:191], v[196:199], v[60:63]\n" \
"v_exp_f32_e64 v139, v139\n" \
"v_mfma_f32_16x16x32_bf16 v[60:63], a[156:159], v[188:191], v[60:63]\n" \
"s_cmp_lg_u32 %[wid], 3\n" \
"s_cbranch_scc1 LtA1_%=\n" \
"global_load_dword v237, v224, s[58:59]\n" \
"s_add_u32 s61, s48, 49152\n" \
"s_mov_b32 exec_hi, 0\n" \
"s_nop 0\n" \
"buffer_load_dword v237, v224, %[srdp], s61 offen\n" \
"s_mov_b32 exec_hi, -1\n" \
"s_nop 0\n" \
"LtA1_%=:\n" \
"v_mfma_f32_16x16x32_bf16 v[144:147], a[96:99], a[32:35], v[168:171]\n" \
"v_exp_f32_e64 v140, v140\n" \
"v_mfma_f32_16x16x32_bf16 v[144:147], a[100:103], a[36:39], v[144:147]\n" \
"ds_read_b128 a[92:95], v210 offset:20480\n" \
"v_mfma_f32_16x16x32_bf16 v[144:147], a[104:107], a[40:43], v[144:147]\n" \
"v_exp_f32_e64 v141, v141\n" \
"v_mfma_f32_16x16x32_bf16 v[144:147], a[108:111], a[44:47], v[144:147]\n" \
"v_cvt_pk_bf16_f32 v196, v136, v137\n" \
"v_cvt_pk_bf16_f32 v197, v138, v139\n" \
"v_mfma_f32_16x16x32_bf16 v[148:151], a[112:115], a[32:35], v[168:171]\n" \
"v_exp_f32_e64 v142, v142\n" \
"v_mfma_f32_16x16x32_bf16 v[148:151], a[116:119], a[36:39], v[148:151]\n" \
"v_mfma_f32_16x16x32_bf16 v[148:151], a[120:123], a[40:43], v[148:151]\n" \
"buffer_load_dwordx4 %[vd0], %[srdv], s48 offen offset:2048 lds\n" \
"v_mfma_f32_16x16x32_bf16 v[148:151], a[124:127], a[44:47], v[148:151]\n" \
"v_exp_f32_e64 v143, v143\n" \
"v_mfma_f32_16x16x32_bf16 v[96:99], a[160:163], v[204:207], v[96:99]\n" \
"ds_read_b64_tr_b16 a[160:161], %[vvb] offset:8192\n" \
"v_mfma_f32_16x16x32_bf16 v[96:99], a[128:131], v[220:223], v[96:99]\n" \
"v_exp_f32_e64 v144, v144\n" \
"v_mfma_f32_16x16x32_bf16 v[100:103], a[164:167], v[204:207], v[100:103]\n" \
"v_cvt_pk_bf16_f32 v198, v140, v141\n" \
"v_cvt_pk_bf16_f32 v199, v142, v143\n" \
"v_mfma_f32_16x16x32_bf16 v[100:103], a[132:135], v[220:223], v[100:103]\n" \
"v_exp_f32_e64 v145, v145\n" \
"v_mfma_f32_16x16x32_bf16 v[104:107], a[168:171], v[204:207], v[104:107]\n" \
"ds_read_b64_tr_b16 a[162:163], %[vvb] offset:12288\n" \
"ds_read_b64_tr_b16 a[164:165], v211 offset:8192\n" \
"v_mfma_f32_16x16x32_bf16 v[104:107], a[136:139], v[220:223], v[104:107]\n" \
"v_exp_f32_e64 v146, v146\n" \
"v_mfma_f32_16x16x32_bf16 v[108:111], a[172:175], v[204:207], v[108:111]\n" \
"ds_read_b64_tr_b16 a[166:167], v211 offset:12288\n" \
"v_mfma_f32_16x16x32_bf16 v[108:111], a[140:143], v[220:223], v[108:111]\n" \
"v_exp_f32_e64 v147, v147\n" \
"v_smfmac_f32_16x16x64_bf16 v[180:183], a[192:195], v[216:223], v228\n" \
"ds_read_b64_tr_b16 a[168:169], %[vvb] offset:8704\n" \
"v_mfma_f32_16x16x32_bf16 v[152:155], a[96:99], a[48:51], v[172:175]\n" \
"v_exp_f32_e64 v148, v148\n" \
"v_mfma_f32_16x16x32_bf16 v[152:155], a[100:103], a[52:55], v[152:155]\n" \
"ds_read_b64_tr_b16 a[170:171], %[vvb] offset:12800\n" \
"v_mfma_f32_16x16x32_bf16 v[152:155], a[104:107], a[56:59], v[152:155]\n" \
"v_exp_f32_e64 v149, v149\n" \
"v_mfma_f32_16x16x32_bf16 v[152:155], a[108:111], a[60:63], v[152:155]\n" \
"v_cvt_pk_bf16_f32 v200, v144, v145\n" \
"v_cvt_pk_bf16_f32 v201, v146, v147\n" \
"v_mfma_f32_16x16x32_bf16 v[156:159], a[112:115], a[48:51], v[172:175]\n" \
"v_exp_f32_e64 v150, v150\n" \
"v_mfma_f32_16x16x32_bf16 v[156:159], a[116:119], a[52:55], v[156:159]\n" \
"ds_read_b64_tr_b16 a[172:173], v211 offset:8704\n" \
"v_mfma_f32_16x16x32_bf16 v[156:159], a[120:123], a[56:59], v[156:159]\n" \
"buffer_load_dwordx4 v215, %[srdv], s48 offen offset:3072 lds\n" \
"v_mfma_f32_16x16x32_bf16 v[156:159], a[124:127], a[60:63], v[156:159]\n" \
"v_exp_f32_e64 v151, v151\n" \
"v_mfma_f32_16x16x32_bf16 v[112:115], a[176:179], v[204:207], v[112:115]\n" \
"ds_read_b64_tr_b16 a[174:175], v211 offset:12800\n" \
"ds_read_b64_tr_b16 a[176:177], %[vvb] offset:9216\n" \
"v_mfma_f32_16x16x32_bf16 v[112:115], a[144:147], v[220:223], v[112:115]\n" \
"v_exp_f32_e64 v152, v152\n" \
"v_mfma_f32_16x16x32_bf16 v[116:119], a[180:183], v[204:207], v[116:119]\n" \
"v_cvt_pk_bf16_f32 v202, v148, v149\n" \
"v_cvt_pk_bf16_f32 v203, v150, v151\n" \
"v_mfma_f32_16x16x32_bf16 v[116:119], a[148:151], v[220:223], v[116:119]\n" \
"v_exp_f32_e64 v153, v153\n" \
"v_mfma_f32_16x16x32_bf16 v[120:123], a[184:187], v[204:207], v[120:123]\n" \
"v_exp_f32_e64 v154, v154\n" \
"v_mfma_f32_16x16x32_bf16 v[120:123], a[152:155], v[220:223], v[120:123]\n" \
"ds_read_b64_tr_b16 a[178:179], %[vvb] offset:13312\n" \
"v_mfma_f32_16x16x32_bf16 v[124:127], a[188:191], v[204:207], v[124:127]\n" \
"v_exp_f32_e64 v155, v155\n" \
"v_mfma_f32_16x16x32_bf16 v[124:127], a[156:159], v[220:223], v[124:127]\n" \
"ds_read_b64_tr_b16 a[180:181], v211 offset:9216\n" \
"s_waitcnt lgkmcnt(13)\n" \
"s_cmp_lg_u32 s60, 4\n" \
"s_cbranch_scc1 LmA_%=\n" \
"s_nop 0\n" \
"v_readlane_b32 s64, %[tlo], 9\n" \
"v_readlane_b32 s65, %[thi], 9\n" \
"s_mov_b32 s66, %[wbytes]\n" \
"v_mov_b32_e32 v229, %[vg0]\n" \
"s_mov_b32 s44, %[m0t]\n" \
"s_mov_b32 s45, 0\n" \
"s_cmp_lg_u32 s52, 0\n" \
"s_cbranch_scc1 LmB_%=\n" \
"s_add_u32 s62, %[sin], 8192\n" \
"s_add_u32 s63, %[sin], 28672\n" \
"s_waitcnt vmcnt(13)\n" \
"s_nop 0\n" \
"ds_read_b64_tr_b16 a[232:233], %[ta0] offset:0\n" \
"ds_read_b64_tr_b16 a[234:235], %[ta0] offset:128\n" \
"ds_read_b64_tr_b16 a[236:237], %[ta0] offset:1024\n" \
"ds_read_b64_tr_b16 a[238:239], %[ta0] offset:1152\n" \
"ds_read_b64_tr_b16 a[240:241], %[ta0] offset:2048\n" \
"ds_read_b64_tr_b16 a[242:243], %[ta0] offset:2176\n" \
"ds_read_b64_tr_b16 a[244:245], %[ta0] offset:3072\n" \
"ds_read_b64_tr_b16 a[246:247], %[ta0] offset:3200\n" \
"ds_read_b64_tr_b16 a[248:249], %[ta0] offset:4096\n" \
"ds_read_b64_tr_b16 a[250:251], %[ta0] offset:4224\n" \
"ds_read_b64_tr_b16 a[252:253], %[ta0] offset:5120\n" \
"ds_read_b64_tr_b16 a[254:255], %[ta0] offset:5248\n" \
"v_add_u32_e32 v234, %[m0t], v235\n" \
"s_waitcnt lgkmcnt(8)\n" \
"v_mfma_f32_16x16x32_bf16 v[230:233], a[232:235], a[200:203], 0\n" \
"v_mfma_f32_16x16x32_bf16 v[230:233], a[236:239], a[204:207], v[230:233]\n" \
"ds_read_b64_tr_b16 a[232:233], %[ta0] offset:6144\n" \
"ds_read_b64_tr_b16 a[234:235], %[ta0] offset:6272\n" \
"ds_read_b64_tr_b16 a[236:237], %[ta0] offset:7168\n" \
"ds_read_b64_tr_b16 a[238:239], %[ta0] offset:7296\n" \
"s_waitcnt lgkmcnt(4)\n" \
"s_nop 0\n" \
"v_mfma_f32_16x16x32_bf16 v[230:233], a[240:243], a[208:211], v[230:233]\n" \
"v_mfma_f32_16x16x32_bf16 v[230:233], a[244:247], a[212:215], v[230:233]\n" \
"v_mfma_f32_16x16x32_bf16 v[230:233], a[248:251], a[216:219], v[230:233]\n" \
"v_mfma_f32_16x16x32_bf16 v[230:233], a[252:255], a[220:223], v[230:233]\n" \
"s_waitcnt lgkmcnt(0)\n" \
"s_nop 0\n" \
"v_mfma_f32_16x16x32_bf16 v[230:233], a[232:235], a[224:227], v[230:233]\n" \
"v_mfma_f32_16x16x32_bf16 v[230:233], a[236:239], a[228:231], v[230:233]\n" \
"s_nop 7\n" \
"s_nop 0\n" \
"ds_write_b128 v234, v[230:233] offset:7168\n" \
"LmB_%=:\n" \
"v_mfma_f32_16x16x32_bf16 v[128:131], a[64:67], a[0:3], v[160:163]\n" \
"v_exp_f32_e64 v156, v156\n" \
"v_mfma_f32_16x16x32_bf16 v[128:131], a[68:71], a[4:7], v[128:131]\n" \
"ds_read_b64_tr_b16 a[182:183], v211 offset:13312\n" \
"s_add_u32 m0, %[m0b], 0\n" \
"v_mfma_f32_16x16x32_bf16 v[128:131], a[72:75], a[8:11], v[128:131]\n" \
"v_exp_f32_e32 v157, v157\n" \
"v_mfma_f32_16x16x32_bf16 v[128:131], a[76:79], a[12:15], v[128:131]\n" \
"v_cvt_pk_bf16_f32 v204, v152, v153\n" \
"v_cvt_pk_bf16_f32 v205, v154, v155\n" \
"v_mfma_f32_16x16x32_bf16 v[132:135], a[80:83], a[0:3], v[160:163]\n" \
"v_exp_f32_e64 v158, v158\n" \
"v_mfma_f32_16x16x32_bf16 v[132:135], a[84:87], a[4:7], v[132:135]\n" \
"ds_read_b64_tr_b16 a[184:185], %[vvb] offset:9728\n" \
"s_waitcnt lgkmcnt(5)\n" \
"v_mfma_f32_16x16x32_bf16 v[132:135], a[88:91], a[8:11], v[132:135]\n" \
"buffer_load_dwordx4 %[kd0], %[srdk], s48 offen offset:0 lds\n" \
"v_mfma_f32_16x16x32_bf16 v[132:135], a[92:95], a[12:15], v[132:135]\n" \
"v_exp_f32_e32 v159, v159\n" \
"v_mfma_f32_16x16x32_bf16 v[64:67], a[128:131], v[216:219], v[64:67]\n" \
"ds_read_b64_tr_b16 a[186:187], %[vvb] offset:13824\n" \
"ds_read_b64_tr_b16 a[188:189], v211 offset:9728\n" \
"v_mfma_f32_16x16x32_bf16 v[64:67], a[160:163], v[200:203], v[64:67]\n" \
"v_exp_f32_e64 v128, v128\n" \
"v_mfma_f32_16x16x32_bf16 v[68:71], a[132:135], v[216:219], v[68:71]\n" \
"v_cvt_pk_bf16_f32 v206, v156, v157\n" \
"v_cvt_pk_bf16_f32 v207, v158, v159\n" \
"v_mfma_f32_16x16x32_bf16 v[68:71], a[164:167], v[200:203], v[68:71]\n" \
"v_exp_f32_e64 v129, v129\n" \
"v_mfma_f32_16x16x32_bf16 v[72:75], a[136:139], v[216:219], v[72:75]\n" \
"ds_read_b64_tr_b16 a[190:191], v211 offset:13824\n" \
"ds_read_b64_tr_b16 a[128:129], %[vvb] offset:16384\n" \
"s_add_u32 s60, s60, 1\n" \
"s_nop 0\n" \
"v_readlane_b32 s58, %[tlo], s60\n" \
"v_mfma_f32_16x16x32_bf16 v[72:75], a[168:171], v[200:203], v[72:75]\n" \
"v_exp_f32_e64 v130, v130\n" \
"v_mfma_f32_16x16x32_bf16 v[76:79], a[140:143], v[216:219], v[76:79]\n" \
"ds_read_b64_tr_b16 a[130:131], %[vvb] offset:20480\n" \
"ds_read_b64_tr_b16 a[132:133], v211 offset:16384\n" \
"v_readlane_b32 s59, %[thi], s60\n" \
"v_mfma_f32_16x16x32_bf16 v[76:79], a[172:175], v[200:203], v[76:79]\n" \
"v_exp_f32_e64 v131, v131\n" \
"v_smfmac_f32_16x16x64_bf16 v[176:179], a[192:195], v[192:199], v228\n" \
"ds_read_b64_tr_b16 a[134:135], v211 offset:20480\n" \
"ds_read_b64_tr_b16 a[136:137], %[vvb] offset:16896\n" \
"s_cmp_eq_u32 s60, 7\n" \
"s_cselect_b32 s61, 56, 1\n" \
"v_mul_u32_u24_e64 v236, s61, v224\n" \
"v_mfma_f32_16x16x32_bf16 v[136:139], a[64:67], a[16:19], v[164:167]\n" \
"v_exp_f32_e64 v132, v132\n" \
"v_mfma_f32_16x16x32_bf16 v[136:139], a[68:71], a[20:23], v[136:139]\n" \
"ds_read_b64_tr_b16 a[138:139], %[vvb] offset:20992\n" \
"ds_read_b64_tr_b16 a[140:141], v211 offset:16896\n" \
"v_mfma_f32_16x16x32_bf16 v[136:139], a[72:75], a[24:27], v[136:139]\n" \
"v_exp_f32_e64 v133, v133\n" \
"v_mfma_f32_16x16x32_bf16 v[136:139], a[76:79], a[28:31], v[136:139]\n" \
"v_cvt_pk_bf16_f32 v184, v128, v129\n" \
"v_cvt_pk_bf16_f32 v185, v130, v131\n" \
"v_mfma_f32_16x16x32_bf16 v[140:143], a[80:83], a[16:19], v[164:167]\n" \
"v_exp_f32_e64 v134, v134\n" \
"v_mfma_f32_16x16x32_bf16 v[140:143], a[84:87], a[20:23], v[140:143]\n" \
"ds_read_b64_tr_b16 a[142:143], v211 offset:20992\n" \
"s_waitcnt lgkmcnt(8)\n" \
"v_mfma_f32_16x16x32_bf16 v[140:143], a[88:91], a[24:27], v[140:143]\n" \
"buffer_load_dwordx4 v212, %[srdk], s48 offen offset:1024 lds\n" \
"v_mfma_f32_16x16x32_bf16 v[140:143], a[92:95], a[28:31], v[140:143]\n" \
"v_exp_f32_e32 v135, v135\n" \
"v_mfma_f32_16x16x32_bf16 v[80:83], a[144:147], v[216:219], v[80:83]\n" \
"ds_read_b64_tr_b16 a[144:145], %[vvb] offset:17408\n" \
"ds_read_b64_tr_b16 a[146:147], %[vvb] offset:21504\n" \
"v_mfma_f32_16x16x32_bf16 v[80:83], a[176:179], v[200:203], v[80:83]\n" \
"v_exp_f32_e64 v136, v136\n" \
"v_mfma_f32_16x16x32_bf16 v[84:87], a[148:151], v[216:219], v[84:87]\n" \
"v_cvt_pk_bf16_f32 v186, v132, v133\n" \
"v_cvt_pk_bf16_f32 v187, v134, v135\n" \
"v_mfma_f32_16x16x32_bf16 v[84:87], a[180:183], v[200:203], v[84:87]\n" \
"v_exp_f32_e64 v137, v137\n" \
"v_mfma_f32_16x16x32_bf16 v[88:91], a[152:155], v[216:219], v[88:91]\n" \
"v_exp_f32_e64 v138, v138\n" \
"v_mfma_f32_16x16x32_bf16 v[88:91], a[184:187], v[200:203], v[88:91]\n" \
"ds_read_b64_tr_b16 a[148:149], v211 offset:17408\n" \
"ds_read_b64_tr_b16 a[150:151], v211 offset:21504\n" \
"v_mfma_f32_16x16x32_bf16 v[92:95], a[156:159], v[216:219], v[92:95]\n" \
"v_exp_f32_e64 v139, v139\n" \
"v_mfma_f32_16x16x32_bf16 v[92:95], a[188:191], v[200:203], v[92:95]\n" \
"ds_read_b64_tr_b16 a[152:153], %[vvb] offset:17920\n" \
"buffer_store_dwordx4 a[196:199], v235, %[srdz], s63 offen offset:0 sc1\n" \
"v_mfma_f32_16x16x32_bf16 v[144:147], a[64:67], a[32:35], v[168:171]\n" \
"v_exp_f32_e64 v140, v140\n" \
"v_mfma_f32_16x16x32_bf16 v[144:147], a[68:71], a[36:39], v[144:147]\n" \
"ds_read_b64_tr_b16 a[154:155], %[vvb] offset:22016\n" \
"ds_read_b64_tr_b16 a[156:157], v211 offset:17920\n" \
"v_mfma_f32_16x16x32_bf16 v[144:147], a[72:75], a[40:43], v[144:147]\n" \
"v_exp_f32_e64 v141, v141\n" \
"v_mfma_f32_16x16x32_bf16 v[144:147], a[76:79], a[44:47], v[144:147]\n" \
"v_cvt_pk_bf16_f32 v188, v136, v137\n" \
"v_cvt_pk_bf16_f32 v189, v138, v139\n" \
"v_mfma_f32_16x16x32_bf16 v[148:151], a[80:83], a[32:35], v[168:171]\n" \
"v_exp_f32_e64 v142, v142\n" \
"v_mfma_f32_16x16x32_bf16 v[148:151], a[84:87], a[36:39], v[148:151]\n" \
"ds_read_b64_tr_b16 a[158:159], v211 offset:22016\n" \
"s_waitcnt lgkmcnt(8)\n" \
"v_mfma_f32_16x16x32_bf16 v[148:151], a[88:91], a[40:43], v[148:151]\n" \
"buffer_load_dwordx4 v213, %[srdk], s48 offen offset:2048 lds\n" \
"v_mfma_f32_16x16x32_bf16 v[148:151], a[92:95], a[44:47], v[148:151]\n" \
"v_exp_f32_e32 v143, v143\n" \
"v_mfma_f32_16x16x32_bf16 v[0:3], a[160:163], v[192:195], v[0:3]\n" \
"ds_read_b128 a[96:99], %[kb0] offset:24576\n" \
"v_mfma_f32_16x16x32_bf16 v[0:3], a[128:131], v[184:187], v[0:3]\n" \
"v_exp_f32_e64 v144, v144\n" \
"v_mfma_f32_16x16x32_bf16 v[4:7], a[164:167], v[192:195], v[4:7]\n" \
"v_cvt_pk_bf16_f32 v190, v140, v141\n" \
"v_cvt_pk_bf16_f32 v191, v142, v143\n" \
"v_mfma_f32_16x16x32_bf16 v[4:7], a[132:135], v[184:187], v[4:7]\n" \
"v_exp_f32_e64 v145, v145\n" \
"v_mfma_f32_16x16x32_bf16 v[8:11], a[168:171], v[192:195], v[8:11]\n" \
"ds_read_b128 a[100:103], v208 offset:24576\n" \
"v_mfma_f32_16x16x32_bf16 v[8:11], a[136:139], v[184:187], v[8:11]\n" \
"v_exp_f32_e64 v146, v146\n" \
"v_mfma_f32_16x16x32_bf16 v[12:15], a[172:175], v[192:195], v[12:15]\n" \
"ds_read_b128 a[104:107], v209 offset:24576\n" \
"v_mfma_f32_16x16x32_bf16 v[12:15], a[140:143], v[184:187], v[12:15]\n" \
"v_exp_f32_e64 v147, v147\n" \
"v_smfmac_f32_16x16x64_bf16 v[180:183], a[192:195], v[200:207], v228\n" \
"ds_read_b128 a[108:111], v210 offset:24576\n" \
"v_mfma_f32_16x16x32_bf16 v[152:155], a[64:67], a[48:51], v[172:175]\n" \
"v_exp_f32_e64 v148, v148\n" \
"v_mfma_f32_16x16x32_bf16 v[152:155], a[68:71], a[52:55], v[152:155]\n" \
"ds_read_b128 a[112:115], %[kb0] offset:28672\n" \
"v_mfma_f32_16x16x32_bf16 v[152:155], a[72:75], a[56:59], v[152:155]\n" \
"v_exp_f32_e64 v149, v149\n" \
"v_mfma_f32_16x16x32_bf16 v[152:155], a[76:79], a[60:63], v[152:155]\n" \
"v_cvt_pk_bf16_f32 v216, v144, v145\n" \
"v_cvt_pk_bf16_f32 v217, v146, v147\n" \
"v_mfma_f32_16x16x32_bf16 v[156:159], a[80:83], a[48:51], v[172:175]\n" \
"v_exp_f32_e64 v150, v150\n" \
"v_mfma_f32_16x16x32_bf16 v[156:159], a[84:87], a[52:55], v[156:159]\n" \
"ds_read_b128 a[116:119], v208 offset:28672\n" \
"s_waitcnt lgkmcnt(6)\n" \
"v_mfma_f32_16x16x32_bf16 v[156:159], a[88:91], a[56:59], v[156:159]\n" \
"buffer_load_dwordx4 v214, %[srdk], s48 offen offset:3072 lds\n" \
"v_mfma_f32_16x16x32_bf16 v[156:159], a[92:95], a[60:63], v[156:159]\n" \
"v_exp_f32_e32 v151, v151\n" \
"v_mfma_f32_16x16x32_bf16 v[16:19], a[176:179], v[192:195], v[16:19]\n" \
"ds_read_b128 a[120:123], v209 offset:28672\n" \
"v_mfma_f32_16x16x32_bf16 v[16:19], a[144:147], v[184:187], v[16:19]\n" \
"v_exp_f32_e64 v152, v152\n" \
"v_mfma_f32_16x16x32_bf16 v[20:23], a[180:183], v[192:195], v[20:23]\n" \
"v_cvt_pk_bf16_f32 v218, v148, v149\n" \
"v_cvt_pk_bf16_f32 v219, v150, v151\n" \
"v_mfma_f32_16x16x32_bf16 v[20:23], a[148:151], v[184:187], v[20:23]\n" \
"v_exp_f32_e64 v153, v153\n" \
"v_mfma_f32_16x16x32_bf16 v[24:27], a[184:187], v[192:195], v[24:27]\n" \
"v_exp_f32_e64 v154, v154\n" \
"v_mfma_f32_16x16x32_bf16 v[24:27], a[152:155], v[184:187], v[24:27]\n" \
"ds_read_b128 a[124:127], v210 offset:28672\n" \
"s_add_u32 s48, s48, 0x4000\n" \
"s_mov_b32 m0, s44\n" \
"s_addk_i32 s44, 0x400\n" \
"buffer_load_dwordx4 v229, s[64:67], s45 offen offset:0 lds\n" \
"s_add_u32 s45, s45, 229376\n" \
"v_mfma_f32_16x16x32_bf16 v[28:31], a[188:191], v[192:195], v[28:31]\n" \
"v_exp_f32_e64 v155, v155\n" \
"v_mfma_f32_16x16x32_bf16 v[28:31], a[156:159], v[184:187], v[28:31]\n" \
"s_waitcnt vmcnt(15) lgkmcnt(2)\n" \
"s_barrier\n" \
"v_mfma_f32_16x16x32_bf16 v[128:131], a[96:99], a[0:3], v[160:163]\n" \
"v_exp_f32_e64 v156, v156\n" \
"v_mfma_f32_16x16x32_bf16 v[128:131], a[100:103], a[4:7], v[128:131]\n" \
"s_add_u32 m0, %[m0b], 65536\n" \
"v_mfma_f32_16x16x32_bf16 v[128:131], a[104:107], a[8:11], v[128:131]\n" \
"v_exp_f32_e64 v157, v157\n" \
"v_mfma_f32_16x16x32_bf16 v[128:131], a[108:111], a[12:15], v[128:131]\n" \
"v_cvt_pk_bf16_f32 v220, v152, v153\n" \
"v_cvt_pk_bf16_f32 v221, v154, v155\n" \
"v_mfma_f32_16x16x32_bf16 v[132:135], a[112:115], a[0:3], v[160:163]\n" \
"v_exp_f32_e64 v158, v158\n" \
"v_mfma_f32_16x16x32_bf16 v[132:135], a[116:119], a[4:7], v[132:135]\n" \
"s_cmp_lg_u32 s60, 5\n" \
"s_cbranch_scc1 Lsjb_%=\n" \
"ds_read_b32 v230, %[rd0]\n" \
"v_readlane_b32 s55, %[thi], 15\n" \
"v_lshrrev_b32_e32 v234, 5, v224\n" \
"s_waitcnt lgkmcnt(0)\n" \
"v_add_f32_dpp v230, v230, v230 row_ror:4 row_mask:0xf bank_mask:0xf\n" \
"s_nop 1\n" \
"v_add_f32_dpp v230, v230, v230 row_ror:8 row_mask:0xf bank_mask:0xf\n" \
"v_cvt_pk_bf16_f32 v230, v230, v230\n" \
"v_lshlrev_b32_e32 v230, 16, v230\n" \
"v_add_f32_e64 v230, s55, v230\n" \
"v_cvt_pk_bf16_f32 v230, v230, v230\n" \
"v_readlane_b32 s55, %[tlo], 15\n" \
"v_lshl_or_b32 v230, v230, 16, 1\n" \
"s_cmp_lg_u32 s52, 0\n" \
"s_cselect_b32 s55, 0x80000000, s55\n" \
"s_mov_b64 exec, 15\n" \
"buffer_store_dword v230, v234, %[srdz], s55 offen sc1\n" \
"s_mov_b64 exec, -1\n" \
"Lsjb_%=:\n" \
"v_mfma_f32_16x16x32_bf16 v[132:135], a[120:123], a[8:11], v[132:135]\n" \
"buffer_load_dwordx4 %[vd0], %[srdv], s48 offen offset:0 lds\n" \
"v_mfma_f32_16x16x32_bf16 v[132:135], a[124:127], a[12:15], v[132:135]\n" \
"v_exp_f32_e32 v159, v159\n" \
"v_mfma_f32_16x16x32_bf16 v[32:35], a[160:163], v[196:199], v[32:35]\n" \
"ds_read_b128 a[64:67], %[kb0] offset:32768\n" \
"v_mfma_f32_16x16x32_bf16 v[32:35], a[128:131], v[188:191], v[32:35]\n" \
"v_exp_f32_e64 v128, v128\n" \
"v_mfma_f32_16x16x32_bf16 v[36:39], a[164:167], v[196:199], v[36:39]\n" \
"v_cvt_pk_bf16_f32 v222, v156, v157\n" \
"v_cvt_pk_bf16_f32 v223, v158, v159\n" \
"v_mfma_f32_16x16x32_bf16 v[36:39], a[132:135], v[188:191], v[36:39]\n" \
"v_exp_f32_e64 v129, v129\n" \
"v_mfma_f32_16x16x32_bf16 v[40:43], a[168:171], v[196:199], v[40:43]\n" \
"ds_read_b128 a[68:71], v208 offset:32768\n" \
"v_mfma_f32_16x16x32_bf16 v[40:43], a[136:139], v[188:191], v[40:43]\n" \
"v_exp_f32_e64 v130, v130\n" \
"v_mfma_f32_16x16x32_bf16 v[44:47], a[172:175], v[196:199], v[44:47]\n" \
"ds_read_b128 a[72:75], v209 offset:32768\n" \
"v_mfma_f32_16x16x32_bf16 v[44:47], a[140:143], v[188:191], v[44:47]\n" \
"v_exp_f32_e64 v131, v131\n" \
"v_smfmac_f32_16x16x64_bf16 v[176:179], a[192:195], v[184:191], v228\n" \
"v_mfma_f32_16x16x32_bf16 v[136:139], a[96:99], a[16:19], v[164:167]\n" \
"v_exp_f32_e64 v132, v132\n" \
"v_mfma_f32_16x16x32_bf16 v[136:139], a[100:103], a[20:23], v[136:139]\n" \
"ds_read_b128 a[76:79], v210 offset:32768\n" \
"v_mfma_f32_16x16x32_bf16 v[136:139], a[104:107], a[24:27], v[136:139]\n" \
"v_exp_f32_e64 v133, v133\n" \
"v_mfma_f32_16x16x32_bf16 v[136:139], a[108:111], a[28:31], v[136:139]\n" \
"v_cvt_pk_bf16_f32 v192, v128, v129\n" \
"v_cvt_pk_bf16_f32 v193, v130, v131\n" \
"v_mfma_f32_16x16x32_bf16 v[140:143], a[112:115], a[16:19], v[164:167]\n" \
"v_exp_f32_e64 v134, v134\n" \
"v_mfma_f32_16x16x32_bf16 v[140:143], a[116:119], a[20:23], v[140:143]\n" \
"ds_read_b128 a[80:83], %[kb0] offset:36864\n" \
"v_mfma_f32_16x16x32_bf16 v[140:143], a[120:123], a[24:27], v[140:143]\n" \
"buffer_load_dwordx4 v215, %[srdv], s48 offen offset:1024 lds\n" \
"v_mfma_f32_16x16x32_bf16 v[140:143], a[124:127], a[28:31], v[140:143]\n" \
"v_exp_f32_e64 v135, v135\n" \
"v_mfma_f32_16x16x32_bf16 v[48:51], a[176:179], v[196:199], v[48:51]\n" \
"ds_read_b128 a[84:87], v208 offset:36864\n" \
"v_mfma_f32_16x16x32_bf16 v[48:51], a[144:147], v[188:191], v[48:51]\n" \
"v_exp_f32_e64 v136, v136\n" \
"v_mfma_f32_16x16x32_bf16 v[52:55], a[180:183], v[196:199], v[52:55]\n" \
"v_cvt_pk_bf16_f32 v194, v132, v133\n" \
"v_cvt_pk_bf16_f32 v195, v134, v135\n" \
"v_mfma_f32_16x16x32_bf16 v[52:55], a[148:151], v[188:191], v[52:55]\n" \
"v_exp_f32_e64 v137, v137\n" \
"v_mfma_f32_16x16x32_bf16 v[56:59], a[184:187], v[196:199], v[56:59]\n" \
"v_exp_f32_e64 v138, v138\n" \
"v_mfma_f32_16x16x32_bf16 v[56:59], a[152:155], v[188:191], v[56:59]\n" \
"ds_read_b128 a[88:91], v209 offset:36864\n" \
"v_mfma_f32_16x16x32_bf16 v[60:63], a[188:191], v[196:199], v[60:63]\n" \
"v_exp_f32_e64 v139, v139\n" \
"v_mfma_f32_16x16x32_bf16 v[60:63], a[156:159], v[188:191], v[60:63]\n" \
"s_cmp_lg_u32 %[wid], 0\n" \
"s_cbranch_scc1 LtB3_%=\n" \
"global_load_dword v237, v236, s[58:59]\n" \
"s_add_u32 s61, s48, 49152\n" \
"s_mov_b32 exec_hi, 0\n" \
"s_nop 0\n" \
"buffer_load_dword v237, v224, %[srdp], s61 offen\n" \
"s_mov_b32 exec_hi, -1\n" \
"s_nop 0\n" \
"LtB3_%=:\n" \
"v_mfma_f32_16x16x32_bf16 v[144:147], a[96:99], a[32:35], v[168:171]\n" \
"v_exp_f32_e64 v140, v140\n" \
"v_mfma_f32_16x16x32_bf16 v[144:147], a[100:103], a[36:39], v[144:147]\n" \
"ds_read_b128 a[92:95], v210 offset:36864\n" \
"v_mfma_f32_16x16x32_bf16 v[144:147], a[104:107], a[40:43], v[144:147]\n" \
"v_exp_f32_e64 v141, v141\n" \
"v_mfma_f32_16x16x32_bf16 v[144:147], a[108:111], a[44:47], v[144:147]\n" \
"v_cvt_pk_bf16_f32 v196, v136, v137\n" \
"v_cvt_pk_bf16_f32 v197, v138, v139\n" \
"v_mfma_f32_16x16x32_bf16 v[148:151], a[112:115], a[32:35], v[168:171]\n" \
"v_exp_f32_e64 v142, v142\n" \
"v_mfma_f32_16x16x32_bf16 v[148:151], a[116:119], a[36:39], v[148:151]\n" \
"v_mfma_f32_16x16x32_bf16 v[148:151], a[120:123], a[40:43], v[148:151]\n" \
"buffer_load_dwordx4 %[vd0], %[srdv], s48 offen offset:2048 lds\n" \
"v_mfma_f32_16x16x32_bf16 v[148:151], a[124:127], a[44:47], v[148:151]\n" \
"v_exp_f32_e64 v143, v143\n" \
"v_mfma_f32_16x16x32_bf16 v[96:99], a[160:163], v[204:207], v[96:99]\n" \
"ds_read_b64_tr_b16 a[160:161], %[vvb] offset:24576\n" \
"v_mfma_f32_16x16x32_bf16 v[96:99], a[128:131], v[220:223], v[96:99]\n" \
"v_exp_f32_e64 v144, v144\n" \
"v_mfma_f32_16x16x32_bf16 v[100:103], a[164:167], v[204:207], v[100:103]\n" \
"v_cvt_pk_bf16_f32 v198, v140, v141\n" \
"v_cvt_pk_bf16_f32 v199, v142, v143\n" \
"v_mfma_f32_16x16x32_bf16 v[100:103], a[132:135], v[220:223], v[100:103]\n" \
"v_exp_f32_e64 v145, v145\n" \
"v_mfma_f32_16x16x32_bf16 v[104:107], a[168:171], v[204:207], v[104:107]\n" \
"ds_read_b64_tr_b16 a[162:163], %[vvb] offset:28672\n" \
"ds_read_b64_tr_b16 a[164:165], v211 offset:24576\n" \
"v_mfma_f32_16x16x32_bf16 v[104:107], a[136:139], v[220:223], v[104:107]\n" \
"v_exp_f32_e64 v146, v146\n" \
"v_mfma_f32_16x16x32_bf16 v[108:111], a[172:175], v[204:207], v[108:111]\n" \
"ds_read_b64_tr_b16 a[166:167], v211 offset:28672\n" \
"v_mfma_f32_16x16x32_bf16 v[108:111], a[140:143], v[220:223], v[108:111]\n" \
"v_exp_f32_e64 v147, v147\n" \
"v_smfmac_f32_16x16x64_bf16 v[180:183], a[192:195], v[216:223], v228\n" \
"ds_read_b64_tr_b16 a[168:169], %[vvb] offset:25088\n" \
"buffer_store_dwordx4 a[196:199], v235, %[srdz], s62 offen offset:0 sc1\n" \
"v_mfma_f32_16x16x32_bf16 v[152:155], a[96:99], a[48:51], v[172:175]\n" \
"v_exp_f32_e64 v148, v148\n" \
"v_mfma_f32_16x16x32_bf16 v[152:155], a[100:103], a[52:55], v[152:155]\n" \
"ds_read_b64_tr_b16 a[170:171], %[vvb] offset:29184\n" \
"v_mfma_f32_16x16x32_bf16 v[152:155], a[104:107], a[56:59], v[152:155]\n" \
"v_exp_f32_e64 v149, v149\n" \
"v_mfma_f32_16x16x32_bf16 v[152:155], a[108:111], a[60:63], v[152:155]\n" \
"v_cvt_pk_bf16_f32 v200, v144, v145\n" \
"v_cvt_pk_bf16_f32 v201, v146, v147\n" \
"v_mfma_f32_16x16x32_bf16 v[156:159], a[112:115], a[48:51], v[172:175]\n" \
"v_exp_f32_e64 v150, v150\n" \
"v_mfma_f32_16x16x32_bf16 v[156:159], a[116:119], a[52:55], v[156:159]\n" \
"ds_read_b64_tr_b16 a[172:173], v211 offset:25088\n" \
"v_mfma_f32_16x16x32_bf16 v[156:159], a[120:123], a[56:59], v[156:159]\n" \
"buffer_load_dwordx4 v215, %[srdv], s48 offen offset:3072 lds\n" \
"v_mfma_f32_16x16x32_bf16 v[156:159], a[124:127], a[60:63], v[156:159]\n" \
"v_exp_f32_e64 v151, v151\n" \
"v_mfma_f32_16x16x32_bf16 v[112:115], a[176:179], v[204:207], v[112:115]\n" \
"ds_read_b64_tr_b16 a[174:175], v211 offset:29184\n" \
"ds_read_b64_tr_b16 a[176:177], %[vvb] offset:25600\n" \
"v_mfma_f32_16x16x32_bf16 v[112:115], a[144:147], v[220:223], v[112:115]\n" \
"v_exp_f32_e64 v152, v152\n" \
"v_mfma_f32_16x16x32_bf16 v[116:119], a[180:183], v[204:207], v[116:119]\n" \
"v_cvt_pk_bf16_f32 v202, v148, v149\n" \
"v_cvt_pk_bf16_f32 v203, v150, v151\n" \
"v_mfma_f32_16x16x32_bf16 v[116:119], a[148:151], v[220:223], v[116:119]\n" \
"v_exp_f32_e64 v153, v153\n" \
"v_mfma_f32_16x16x32_bf16 v[120:123], a[184:187], v[204:207], v[120:123]\n" \
"v_exp_f32_e64 v154, v154\n" \
"v_mfma_f32_16x16x32_bf16 v[120:123], a[152:155], v[220:223], v[120:123]\n" \
"ds_read_b64_tr_b16 a[178:179], %[vvb] offset:29696\n" \
"v_mfma_f32_16x16x32_bf16 v[124:127], a[188:191], v[204:207], v[124:127]\n" \
"v_exp_f32_e64 v155, v155\n" \
"v_mfma_f32_16x16x32_bf16 v[124:127], a[156:159], v[220:223], v[124:127]\n" \
"ds_read_b64_tr_b16 a[180:181], v211 offset:25600\n" \
"s_waitcnt lgkmcnt(13)\n" \
"v_mfma_f32_16x16x32_bf16 v[128:131], a[64:67], a[0:3], v[160:163]\n" \
"v_exp_f32_e32 v156, v156\n" \
"v_mfma_f32_16x16x32_bf16 v[128:131], a[68:71], a[4:7], v[128:131]\n" \
"ds_read_b64_tr_b16 a[182:183], v211 offset:29696\n" \
"s_add_u32 m0, %[m0b], 16384\n" \
"v_mfma_f32_16x16x32_bf16 v[128:131], a[72:75], a[8:11], v[128:131]\n" \
"v_exp_f32_e64 v157, v157\n" \
"v_mfma_f32_16x16x32_bf16 v[128:131], a[76:79], a[12:15], v[128:131]\n" \
"v_cvt_pk_bf16_f32 v204, v152, v153\n" \
"v_cvt_pk_bf16_f32 v205, v154, v155\n" \
"v_mfma_f32_16x16x32_bf16 v[132:135], a[80:83], a[0:3], v[160:163]\n" \
"v_exp_f32_e64 v158, v158\n" \
"v_mfma_f32_16x16x32_bf16 v[132:135], a[84:87], a[4:7], v[132:135]\n" \
"ds_read_b64_tr_b16 a[184:185], %[vvb] offset:26112\n" \
"s_waitcnt lgkmcnt(5)\n" \
"v_mfma_f32_16x16x32_bf16 v[132:135], a[88:91], a[8:11], v[132:135]\n" \
"buffer_load_dwordx4 %[kd0], %[srdk], s48 offen offset:0 lds\n" \
"v_mfma_f32_16x16x32_bf16 v[132:135], a[92:95], a[12:15], v[132:135]\n" \
"v_exp_f32_e32 v159, v159\n" \
"v_mfma_f32_16x16x32_bf16 v[64:67], a[128:131], v[216:219], v[64:67]\n" \
"ds_read_b64_tr_b16 a[186:187], %[vvb] offset:30208\n" \
"ds_read_b64_tr_b16 a[188:189], v211 offset:26112\n" \
"v_mfma_f32_16x16x32_bf16 v[64:67], a[160:163], v[200:203], v[64:67]\n" \
"v_exp_f32_e64 v128, v128\n" \
"v_mfma_f32_16x16x32_bf16 v[68:71], a[132:135], v[216:219], v[68:71]\n" \
"v_cvt_pk_bf16_f32 v206, v156, v157\n" \
"v_cvt_pk_bf16_f32 v207, v158, v159\n" \
"v_mfma_f32_16x16x32_bf16 v[68:71], a[164:167], v[200:203], v[68:71]\n" \
"v_exp_f32_e64 v129, v129\n" \
"v_mfma_f32_16x16x32_bf16 v[72:75], a[136:139], v[216:219], v[72:75]\n" \
"ds_read_b64_tr_b16 a[190:191], v211 offset:30208\n" \
"ds_read_b64_tr_b16 a[128:129], %[vvb] offset:32768\n" \
"v_mfma_f32_16x16x32_bf16 v[72:75], a[168:171], v[200:203], v[72:75]\n" \
"v_exp_f32_e64 v130, v130\n" \
"v_mfma_f32_16x16x32_bf16 v[76:79], a[140:143], v[216:219], v[76:79]\n" \
"ds_read_b64_tr_b16 a[130:131], %[vvb] offset:36864\n" \
"ds_read_b64_tr_b16 a[132:133], v211 offset:32768\n" \
"v_mfma_f32_16x16x32_bf16 v[76:79], a[172:175], v[200:203], v[76:79]\n" \
"v_exp_f32_e64 v131, v131\n" \
"v_smfmac_f32_16x16x64_bf16 v[176:179], a[192:195], v[192:199], v228\n" \
"ds_read_b64_tr_b16 a[134:135], v211 offset:36864\n" \
"ds_read_b64_tr_b16 a[136:137], %[vvb] offset:33280\n" \
"v_mfma_f32_16x16x32_bf16 v[136:139], a[64:67], a[16:19], v[164:167]\n" \
"v_exp_f32_e64 v132, v132\n" \
"v_mfma_f32_16x16x32_bf16 v[136:139], a[68:71], a[20:23], v[136:139]\n" \
"ds_read_b64_tr_b16 a[138:139], %[vvb] offset:37376\n" \
"ds_read_b64_tr_b16 a[140:141], v211 offset:33280\n" \
"v_mfma_f32_16x16x32_bf16 v[136:139], a[72:75], a[24:27], v[136:139]\n" \
"v_exp_f32_e64 v133, v133\n" \
"v_mfma_f32_16x16x32_bf16 v[136:139], a[76:79], a[28:31], v[136:139]\n" \
"v_cvt_pk_bf16_f32 v184, v128, v129\n" \
"v_cvt_pk_bf16_f32 v185, v130, v131\n" \
"v_mfma_f32_16x16x32_bf16 v[140:143], a[80:83], a[16:19], v[164:167]\n" \
"v_exp_f32_e64 v134, v134\n" \
"v_mfma_f32_16x16x32_bf16 v[140:143], a[84:87], a[20:23], v[140:143]\n" \
"ds_read_b64_tr_b16 a[142:143], v211 offset:37376\n" \
"s_waitcnt lgkmcnt(8)\n" \
"v_mfma_f32_16x16x32_bf16 v[140:143], a[88:91], a[24:27], v[140:143]\n" \
"buffer_load_dwordx4 v212, %[srdk], s48 offen offset:1024 lds\n" \
"v_mfma_f32_16x16x32_bf16 v[140:143], a[92:95], a[28:31], v[140:143]\n" \
"v_exp_f32_e32 v135, v135\n" \
"v_mfma_f32_16x16x32_bf16 v[80:83], a[144:147], v[216:219], v[80:83]\n" \
".if . - adaln_body != 24576\n" \
".error \"size model\"\n" \
".endif\n" \
"Lk5_%=:\n" \
"s_cbranch_execz adaln_kr\n" \
"s_nop 0\n" \
"ds_read_b64_tr_b16 a[144:145], %[vvb] offset:33792\n" \
"ds_read_b64_tr_b16 a[146:147], %[vvb] offset:37888\n" \
"v_mfma_f32_16x16x32_bf16 v[80:83], a[176:179], v[200:203], v[80:83]\n" \
"v_exp_f32_e64 v136, v136\n" \
"v_mfma_f32_16x16x32_bf16 v[84:87], a[148:151], v[216:219], v[84:87]\n" \
"v_cvt_pk_bf16_f32 v186, v132, v133\n" \
"v_cvt_pk_bf16_f32 v187, v134, v135\n" \
"v_mfma_f32_16x16x32_bf16 v[84:87], a[180:183], v[200:203], v[84:87]\n" \
"v_exp_f32_e64 v137, v137\n" \
"v_mfma_f32_16x16x32_bf16 v[88:91], a[152:155], v[216:219], v[88:91]\n" \
"v_exp_f32_e64 v138, v138\n" \
"v_mfma_f32_16x16x32_bf16 v[88:91], a[184:187], v[200:203], v[88:91]\n" \
"ds_read_b64_tr_b16 a[148:149], v211 offset:33792\n" \
"ds_read_b64_tr_b16 a[150:151], v211 offset:37888\n" \
"v_mfma_f32_16x16x32_bf16 v[92:95], a[156:159], v[216:219], v[92:95]\n" \
"v_exp_f32_e64 v139, v139\n" \
"v_mfma_f32_16x16x32_bf16 v[92:95], a[188:191], v[200:203], v[92:95]\n" \
"ds_read_b64_tr_b16 a[152:153], %[vvb] offset:34304\n" \
"buffer_store_dwordx4 a[196:199], v235, %[srdz], s63 offen offset:1024 sc1\n" \
"v_mfma_f32_16x16x32_bf16 v[144:147], a[64:67], a[32:35], v[168:171]\n" \
"v_exp_f32_e64 v140, v140\n" \
"v_mfma_f32_16x16x32_bf16 v[144:147], a[68:71], a[36:39], v[144:147]\n" \
"ds_read_b64_tr_b16 a[154:155], %[vvb] offset:38400\n" \
"ds_read_b64_tr_b16 a[156:157], v211 offset:34304\n" \
"v_mfma_f32_16x16x32_bf16 v[144:147], a[72:75], a[40:43], v[144:147]\n" \
"v_exp_f32_e64 v141, v141\n" \
"v_mfma_f32_16x16x32_bf16 v[144:147], a[76:79], a[44:47], v[144:147]\n" \
"v_cvt_pk_bf16_f32 v188, v136, v137\n" \
"v_cvt_pk_bf16_f32 v189, v138, v139\n" \
"v_mfma_f32_16x16x32_bf16 v[148:151], a[80:83], a[32:35], v[168:171]\n" \
"v_exp_f32_e64 v142, v142\n" \
"v_mfma_f32_16x16x32_bf16 v[148:151], a[84:87], a[36:39], v[148:151]\n" \
"ds_read_b64_tr_b16 a[158:159], v211 offset:38400\n" \
"s_waitcnt lgkmcnt(8)\n" \
"v_mfma_f32_16x16x32_bf16 v[148:151], a[88:91], a[40:43], v[148:151]\n" \
"buffer_load_dwordx4 v213, %[srdk], s48 offen offset:2048 lds\n" \
"v_mfma_f32_16x16x32_bf16 v[148:151], a[92:95], a[44:47], v[148:151]\n" \
"v_exp_f32_e32 v143, v143\n" \
"v_mfma_f32_16x16x32_bf16 v[0:3], a[160:163], v[192:195], v[0:3]\n" \
"ds_read_b128 a[96:99], %[kb0] offset:40960\n" \
"v_mfma_f32_16x16x32_bf16 v[0:3], a[128:131], v[184:187], v[0:3]\n" \
"v_exp_f32_e64 v144, v144\n" \
"v_mfma_f32_16x16x32_bf16 v[4:7], a[164:167], v[192:195], v[4:7]\n" \
"v_cvt_pk_bf16_f32 v190, v140, v141\n" \
"v_cvt_pk_bf16_f32 v191, v142, v143\n" \
"v_mfma_f32_16x16x32_bf16 v[4:7], a[132:135], v[184:187], v[4:7]\n" \
"v_exp_f32_e64 v145, v145\n" \
"v_mfma_f32_16x16x32_bf16 v[8:11], a[168:171], v[192:195], v[8:11]\n" \
"ds_read_b128 a[100:103], v208 offset:40960\n" \
"v_mfma_f32_16x16x32_bf16 v[8:11], a[136:139], v[184:187], v[8:11]\n" \
"v_exp_f32_e64 v146, v146\n" \
"v_mfma_f32_16x16x32_bf16 v[12:15], a[172:175], v[192:195], v[12:15]\n" \
"ds_read_b128 a[104:107], v209 offset:40960\n" \
"v_mfma_f32_16x16x32_bf16 v[12:15], a[140:143], v[184:187], v[12:15]\n" \
"v_exp_f32_e64 v147, v147\n" \
"v_smfmac_f32_16x16x64_bf16 v[180:183], a[192:195], v[200:207], v228\n" \
"ds_read_b128 a[108:111], v210 offset:40960\n" \
"v_mfma_f32_16x16x32_bf16 v[152:155], a[64:67], a[48:51], v[172:175]\n" \
"v_exp_f32_e64 v148, v148\n" \
"v_mfma_f32_16x16x32_bf16 v[152:155], a[68:71], a[52:55], v[152:155]\n" \
"ds_read_b128 a[112:115], %[kb0] offset:45056\n" \
"v_mfma_f32_16x16x32_bf16 v[152:155], a[72:75], a[56:59], v[152:155]\n" \
"v_exp_f32_e64 v149, v149\n" \
"v_mfma_f32_16x16x32_bf16 v[152:155], a[76:79], a[60:63], v[152:155]\n" \
"v_cvt_pk_bf16_f32 v216, v144, v145\n" \
"v_cvt_pk_bf16_f32 v217, v146, v147\n" \
"v_mfma_f32_16x16x32_bf16 v[156:159], a[80:83], a[48:51], v[172:175]\n" \
"v_exp_f32_e64 v150, v150\n" \
"v_mfma_f32_16x16x32_bf16 v[156:159], a[84:87], a[52:55], v[156:159]\n" \
"ds_read_b128 a[116:119], v208 offset:45056\n" \
"s_waitcnt lgkmcnt(6)\n" \
"v_mfma_f32_16x16x32_bf16 v[156:159], a[88:91], a[56:59], v[156:159]\n" \
"buffer_load_dwordx4 v214, %[srdk], s48 offen offset:3072 lds\n" \
"v_mfma_f32_16x16x32_bf16 v[156:159], a[92:95], a[60:63], v[156:159]\n" \
"v_exp_f32_e32 v151, v151\n" \
"v_mfma_f32_16x16x32_bf16 v[16:19], a[176:179], v[192:195], v[16:19]\n" \
"ds_read_b128 a[120:123], v209 offset:45056\n" \
"v_mfma_f32_16x16x32_bf16 v[16:19], a[144:147], v[184:187], v[16:19]\n" \
"v_exp_f32_e64 v152, v152\n" \
"v_mfma_f32_16x16x32_bf16 v[20:23], a[180:183], v[192:195], v[20:23]\n" \
"v_cvt_pk_bf16_f32 v218, v148, v149\n" \
"v_cvt_pk_bf16_f32 v219, v150, v151\n" \
"v_mfma_f32_16x16x32_bf16 v[20:23], a[148:151], v[184:187], v[20:23]\n" \
"v_exp_f32_e64 v153, v153\n" \
"v_mfma_f32_16x16x32_bf16 v[24:27], a[184:187], v[192:195], v[24:27]\n" \
"v_exp_f32_e64 v154, v154\n" \
"v_mfma_f32_16x16x32_bf16 v[24:27], a[152:155], v[184:187], v[24:27]\n" \
"ds_read_b128 a[124:127], v210 offset:45056\n" \
"s_add_u32 s48, s48, 0x4000\n" \
"v_mfma_f32_16x16x32_bf16 v[28:31], a[188:191], v[192:195], v[28:31]\n" \
"v_exp_f32_e64 v155, v155\n" \
"v_mfma_f32_16x16x32_bf16 v[28:31], a[156:159], v[184:187], v[28:31]\n" \
"s_waitcnt vmcnt(16) lgkmcnt(2)\n" \
"s_barrier\n" \
"v_mfma_f32_16x16x32_bf16 v[128:131], a[96:99], a[0:3], v[160:163]\n" \
"v_exp_f32_e64 v156, v156\n" \
"v_mfma_f32_16x16x32_bf16 v[128:131], a[100:103], a[4:7], v[128:131]\n" \
"s_add_u32 m0, %[m0b], 81920\n" \
"v_mfma_f32_16x16x32_bf16 v[128:131], a[104:107], a[8:11], v[128:131]\n" \
"v_exp_f32_e64 v157, v157\n" \
"v_mfma_f32_16x16x32_bf16 v[128:131], a[108:111], a[12:15], v[128:131]\n" \
"v_cvt_pk_bf16_f32 v220, v152, v153\n" \
"v_cvt_pk_bf16_f32 v221, v154, v155\n" \
"v_mfma_f32_16x16x32_bf16 v[132:135], a[112:115], a[0:3], v[160:163]\n" \
"v_exp_f32_e64 v158, v158\n" \
"v_mfma_f32_16x16x32_bf16 v[132:135], a[116:119], a[4:7], v[132:135]\n" \
"s_waitcnt lgkmcnt(0)\n" \
"v_mfma_f32_16x16x32_bf16 v[132:135], a[120:123], a[8:11], v[132:135]\n" \
"buffer_load_dwordx4 %[vd0], %[srdv], s48 offen offset:0 lds\n" \
"v_mfma_f32_16x16x32_bf16 v[132:135], a[124:127], a[12:15], v[132:135]\n" \
"v_exp_f32_e32 v159, v159\n" \
"v_mfma_f32_16x16x32_bf16 v[32:35], a[160:163], v[196:199], v[32:35]\n" \
"ds_read_b128 a[64:67], %[kb0] offset:49152\n" \
"v_mfma_f32_16x16x32_bf16 v[32:35], a[128:131], v[188:191], v[32:35]\n" \
"v_exp_f32_e64 v128, v128\n" \
"v_mfma_f32_16x16x32_bf16 v[36:39], a[164:167], v[196:199], v[36:39]\n" \
"v_cvt_pk_bf16_f32 v222, v156, v157\n" \
"v_cvt_pk_bf16_f32 v223, v158, v159\n" \
"v_mfma_f32_16x16x32_bf16 v[36:39], a[132:135], v[188:191], v[36:39]\n" \
"v_exp_f32_e64 v129, v129\n" \
"v_mfma_f32_16x16x32_bf16 v[40:43], a[168:171], v[196:199], v[40:43]\n" \
"ds_read_b128 a[68:71], v208 offset:49152\n" \
"v_mfma_f32_16x16x32_bf16 v[40:43], a[136:139], v[188:191], v[40:43]\n" \
"v_exp_f32_e64 v130, v130\n" \
"v_mfma_f32_16x16x32_bf16 v[44:47], a[172:175], v[196:199], v[44:47]\n" \
"ds_read_b128 a[72:75], v209 offset:49152\n" \
"v_mfma_f32_16x16x32_bf16 v[44:47], a[140:143], v[188:191], v[44:47]\n" \
"v_exp_f32_e64 v131, v131\n" \
"v_smfmac_f32_16x16x64_bf16 v[176:179], a[192:195], v[184:191], v228\n" \
"v_mfma_f32_16x16x32_bf16 v[136:139], a[96:99], a[16:19], v[164:167]\n" \
"v_exp_f32_e64 v132, v132\n" \
"v_mfma_f32_16x16x32_bf16 v[136:139], a[100:103], a[20:23], v[136:139]\n" \
"ds_read_b128 a[76:79], v210 offset:49152\n" \
"v_mfma_f32_16x16x32_bf16 v[136:139], a[104:107], a[24:27], v[136:139]\n" \
"v_exp_f32_e64 v133, v133\n" \
"v_mfma_f32_16x16x32_bf16 v[136:139], a[108:111], a[28:31], v[136:139]\n" \
"v_cvt_pk_bf16_f32 v192, v128, v129\n" \
"v_cvt_pk_bf16_f32 v193, v130, v131\n" \
"v_mfma_f32_16x16x32_bf16 v[140:143], a[112:115], a[16:19], v[164:167]\n" \
"v_exp_f32_e64 v134, v134\n" \
"v_mfma_f32_16x16x32_bf16 v[140:143], a[116:119], a[20:23], v[140:143]\n" \
"ds_read_b128 a[80:83], %[kb0] offset:53248\n" \
"v_mfma_f32_16x16x32_bf16 v[140:143], a[120:123], a[24:27], v[140:143]\n" \
"buffer_load_dwordx4 v215, %[srdv], s48 offen offset:1024 lds\n" \
"v_mfma_f32_16x16x32_bf16 v[140:143], a[124:127], a[28:31], v[140:143]\n" \
"v_exp_f32_e64 v135, v135\n" \
"v_mfma_f32_16x16x32_bf16 v[48:51], a[176:179], v[196:199], v[48:51]\n" \
"ds_read_b128 a[84:87], v208 offset:53248\n" \
"v_mfma_f32_16x16x32_bf16 v[48:51], a[144:147], v[188:191], v[48:51]\n" \
"v_exp_f32_e64 v136, v136\n" \
"v_mfma_f32_16x16x32_bf16 v[52:55], a[180:183], v[196:199], v[52:55]\n" \
"v_cvt_pk_bf16_f32 v194, v132, v133\n" \
"v_cvt_pk_bf16_f32 v195, v134, v135\n" \
"v_mfma_f32_16x16x32_bf16 v[52:55], a[148:151], v[188:191], v[52:55]\n" \
"v_exp_f32_e64 v137, v137\n" \
"v_mfma_f32_16x16x32_bf16 v[56:59], a[184:187], v[196:199], v[56:59]\n" \
"v_exp_f32_e64 v138, v138\n" \
"v_mfma_f32_16x16x32_bf16 v[56:59], a[152:155], v[188:191], v[56:59]\n" \
"ds_read_b128 a[88:91], v209 offset:53248\n" \
"v_mfma_f32_16x16x32_bf16 v[60:63], a[188:191], v[196:199], v[60:63]\n" \
"v_exp_f32_e64 v139, v139\n" \
"v_mfma_f32_16x16x32_bf16 v[60:63], a[156:159], v[188:191], v[60:63]\n" \
"s_cmp_lg_u32 %[wid], 1\n" \
"s_cbranch_scc1 LtB5_%=\n" \
"global_load_dword v237, v236, s[58:59]\n" \
"s_add_u32 s61, s48, 49152\n" \
"s_mov_b32 exec_hi, 0\n" \
"s_nop 0\n" \
"buffer_load_dword v237, v224, %[srdp], s61 offen\n" \
"s_mov_b32 exec_hi, -1\n" \
"s_nop 0\n" \
"LtB5_%=:\n" \
"v_mfma_f32_16x16x32_bf16 v[144:147], a[96:99], a[32:35], v[168:171]\n" \
"v_exp_f32_e64 v140, v140\n" \
"v_mfma_f32_16x16x32_bf16 v[144:147], a[100:103], a[36:39], v[144:147]\n" \
"ds_read_b128 a[92:95], v210 offset:53248\n" \
"v_mfma_f32_16x16x32_bf16 v[144:147], a[104:107], a[40:43], v[144:147]\n" \
"v_exp_f32_e64 v141, v141\n" \
"v_mfma_f32_16x16x32_bf16 v[144:147], a[108:111], a[44:47], v[144:147]\n" \
"v_cvt_pk_bf16_f32 v196, v136, v137\n" \
"v_cvt_pk_bf16_f32 v197, v138, v139\n" \
"v_mfma_f32_16x16x32_bf16 v[148:151], a[112:115], a[32:35], v[168:171]\n" \
"v_exp_f32_e64 v142, v142\n" \
"v_mfma_f32_16x16x32_bf16 v[148:151], a[116:119], a[36:39], v[148:151]\n" \
"v_mfma_f32_16x16x32_bf16 v[148:151], a[120:123], a[40:43], v[148:151]\n" \
"buffer_load_dwordx4 %[vd0], %[srdv], s48 offen offset:2048 lds\n" \
"v_mfma_f32_16x16x32_bf16 v[148:151], a[124:127], a[44:47], v[148:151]\n" \
"v_exp_f32_e64 v143, v143\n" \
"v_mfma_f32_16x16x32_bf16 v[96:99], a[160:163], v[204:207], v[96:99]\n" \
"ds_read_b64_tr_b16 a[160:161], %[vvb] offset:40960\n" \
"v_mfma_f32_16x16x32_bf16 v[96:99], a[128:131], v[220:223], v[96:99]\n" \
"v_exp_f32_e64 v144, v144\n" \
"v_mfma_f32_16x16x32_bf16 v[100:103], a[164:167], v[204:207], v[100:103]\n" \
"v_cvt_pk_bf16_f32 v198, v140, v141\n" \
"v_cvt_pk_bf16_f32 v199, v142, v143\n" \
"v_mfma_f32_16x16x32_bf16 v[100:103], a[132:135], v[220:223], v[100:103]\n" \
"v_exp_f32_e64 v145, v145\n" \
"v_mfma_f32_16x16x32_bf16 v[104:107], a[168:171], v[204:207], v[104:107]\n" \
"ds_read_b64_tr_b16 a[162:163], %[vvb] offset:45056\n" \
"ds_read_b64_tr_b16 a[164:165], v211 offset:40960\n" \
"v_mfma_f32_16x16x32_bf16 v[104:107], a[136:139], v[220:223], v[104:107]\n" \
"v_exp_f32_e64 v146, v146\n" \
"v_mfma_f32_16x16x32_bf16 v[108:111], a[172:175], v[204:207], v[108:111]\n" \
"ds_read_b64_tr_b16 a[166:167], v211 offset:45056\n" \
"v_mfma_f32_16x16x32_bf16 v[108:111], a[140:143], v[220:223], v[108:111]\n" \
"v_exp_f32_e64 v147, v147\n" \
"v_smfmac_f32_16x16x64_bf16 v[180:183], a[192:195], v[216:223], v228\n" \
"ds_read_b64_tr_b16 a[168:169], %[vvb] offset:41472\n" \
"v_mfma_f32_16x16x32_bf16 v[152:155], a[96:99], a[48:51], v[172:175]\n" \
"v_exp_f32_e64 v148, v148\n" \
"v_mfma_f32_16x16x32_bf16 v[152:155], a[100:103], a[52:55], v[152:155]\n" \
"ds_read_b64_tr_b16 a[170:171], %[vvb] offset:45568\n" \
"v_mfma_f32_16x16x32_bf16 v[152:155], a[104:107], a[56:59], v[152:155]\n" \
"v_exp_f32_e64 v149, v149\n" \
"v_mfma_f32_16x16x32_bf16 v[152:155], a[108:111], a[60:63], v[152:155]\n" \
"v_cvt_pk_bf16_f32 v200, v144, v145\n" \
"v_cvt_pk_bf16_f32 v201, v146, v147\n" \
"v_mfma_f32_16x16x32_bf16 v[156:159], a[112:115], a[48:51], v[172:175]\n" \
"v_exp_f32_e64 v150, v150\n" \
"v_mfma_f32_16x16x32_bf16 v[156:159], a[116:119], a[52:55], v[156:159]\n" \
"ds_read_b64_tr_b16 a[172:173], v211 offset:41472\n" \
"v_mfma_f32_16x16x32_bf16 v[156:159], a[120:123], a[56:59], v[156:159]\n" \
"buffer_load_dwordx4 v215, %[srdv], s48 offen offset:3072 lds\n" \
"v_mfma_f32_16x16x32_bf16 v[156:159], a[124:127], a[60:63], v[156:159]\n" \
"v_exp_f32_e64 v151, v151\n" \
"v_mfma_f32_16x16x32_bf16 v[112:115], a[176:179], v[204:207], v[112:115]\n" \
"ds_read_b64_tr_b16 a[174:175], v211 offset:45568\n" \
"ds_read_b64_tr_b16 a[176:177], %[vvb] offset:41984\n" \
"v_mfma_f32_16x16x32_bf16 v[112:115], a[144:147], v[220:223], v[112:115]\n" \
"v_exp_f32_e64 v152, v152\n" \
"v_mfma_f32_16x16x32_bf16 v[116:119], a[180:183], v[204:207], v[116:119]\n" \
"v_cvt_pk_bf16_f32 v202, v148, v149\n" \
"v_cvt_pk_bf16_f32 v203, v150, v151\n" \
"v_mfma_f32_16x16x32_bf16 v[116:119], a[148:151], v[220:223], v[116:119]\n" \
"v_exp_f32_e64 v153, v153\n" \
"v_mfma_f32_16x16x32_bf16 v[120:123], a[184:187], v[204:207], v[120:123]\n" \
"v_exp_f32_e64 v154, v154\n" \
"v_mfma_f32_16x16x32_bf16 v[120:123], a[152:155], v[220:223], v[120:123]\n" \
"ds_read_b64_tr_b16 a[178:179], %[vvb] offset:46080\n" \
"v_mfma_f32_16x16x32_bf16 v[124:127], a[188:191], v[204:207], v[124:127]\n" \
"v_exp_f32_e64 v155, v155\n" \
"v_mfma_f32_16x16x32_bf16 v[124:127], a[156:159], v[220:223], v[124:127]\n" \
"ds_read_b64_tr_b16 a[180:181], v211 offset:41984\n" \
"s_waitcnt lgkmcnt(13)\n" \
"v_mfma_f32_16x16x32_bf16 v[128:131], a[64:67], a[0:3], v[160:163]\n" \
"v_exp_f32_e32 v156, v156\n" \
"v_mfma_f32_16x16x32_bf16 v[128:131], a[68:71], a[4:7], v[128:131]\n" \
"ds_read_b64_tr_b16 a[182:183], v211 offset:46080\n" \
"s_add_u32 m0, %[m0b], 32768\n" \
"v_mfma_f32_16x16x32_bf16 v[128:131], a[72:75], a[8:11], v[128:131]\n" \
"v_exp_f32_e64 v157, v157\n" \
"v_mfma_f32_16x16x32_bf16 v[128:131], a[76:79], a[12:15], v[128:131]\n" \
"v_cvt_pk_bf16_f32 v204, v152, v153\n" \
"v_cvt_pk_bf16_f32 v205, v154, v155\n" \
"v_mfma_f32_16x16x32_bf16 v[132:135], a[80:83], a[0:3], v[160:163]\n" \
"v_exp_f32_e64 v158, v158\n" \
"v_mfma_f32_16x16x32_bf16 v[132:135], a[84:87], a[4:7], v[132:135]\n" \
"ds_read_b64_tr_b16 a[184:185], %[vvb] offset:42496\n" \
"s_waitcnt lgkmcnt(5)\n" \
"v_mfma_f32_16x16x32_bf16 v[132:135], a[88:91], a[8:11], v[132:135]\n" \
"buffer_load_dwordx4 %[kd0], %[srdk], s48 offen offset:0 lds\n" \
"v_mfma_f32_16x16x32_bf16 v[132:135], a[92:95], a[12:15], v[132:135]\n" \
"v_exp_f32_e32 v159, v159\n" \
"v_mfma_f32_16x16x32_bf16 v[64:67], a[128:131], v[216:219], v[64:67]\n" \
"ds_read_b64_tr_b16 a[186:187], %[vvb] offset:46592\n" \
"ds_read_b64_tr_b16 a[188:189], v211 offset:42496\n" \
"v_mfma_f32_16x16x32_bf16 v[64:67], a[160:163], v[200:203], v[64:67]\n" \
"v_exp_f32_e64 v128, v128\n" \
"v_mfma_f32_16x16x32_bf16 v[68:71], a[132:135], v[216:219], v[68:71]\n" \
"v_cvt_pk_bf16_f32 v206, v156, v157\n" \
"v_cvt_pk_bf16_f32 v207, v158, v159\n" \
"v_mfma_f32_16x16x32_bf16 v[68:71], a[164:167], v[200:203], v[68:71]\n" \
"v_exp_f32_e64 v129, v129\n" \
"v_mfma_f32_16x16x32_bf16 v[72:75], a[136:139], v[216:219], v[72:75]\n" \
"ds_read_b64_tr_b16 a[190:191], v211 offset:46592\n" \
"ds_read_b64_tr_b16 a[128:129], %[vvb] offset:49152\n" \
"v_mfma_f32_16x16x32_bf16 v[72:75], a[168:171], v[200:203], v[72:75]\n" \
"v_exp_f32_e64 v130, v130\n" \
"v_mfma_f32_16x16x32_bf16 v[76:79], a[140:143], v[216:219], v[76:79]\n" \
"ds_read_b64_tr_b16 a[130:131], %[vvb] offset:53248\n" \
"ds_read_b64_tr_b16 a[132:133], v211 offset:49152\n" \
"v_mfma_f32_16x16x32_bf16 v[76:79], a[172:175], v[200:203], v[76:79]\n" \
"v_exp_f32_e64 v131, v131\n" \
"v_smfmac_f32_16x16x64_bf16 v[176:179], a[192:195], v[192:199], v228\n" \
"ds_read_b64_tr_b16 a[134:135], v211 offset:53248\n" \
"ds_read_b64_tr_b16 a[136:137], %[vvb] offset:49664\n" \
"buffer_store_dwordx4 a[196:199], v235, %[srdz], s62 offen offset:1024 sc1\n" \
"v_mfma_f32_16x16x32_bf16 v[136:139], a[64:67], a[16:19], v[164:167]\n" \
"v_exp_f32_e64 v132, v132\n" \
"v_mfma_f32_16x16x32_bf16 v[136:139], a[68:71], a[20:23], v[136:139]\n" \
"ds_read_b64_tr_b16 a[138:139], %[vvb] offset:53760\n" \
"ds_read_b64_tr_b16 a[140:141], v211 offset:49664\n" \
"v_mfma_f32_16x16x32_bf16 v[136:139], a[72:75], a[24:27], v[136:139]\n" \
"v_exp_f32_e64 v133, v133\n" \
"v_mfma_f32_16x16x32_bf16 v[136:139], a[76:79], a[28:31], v[136:139]\n" \
"v_cvt_pk_bf16_f32 v184, v128, v129\n" \
"v_cvt_pk_bf16_f32 v185, v130, v131\n" \
"v_mfma_f32_16x16x32_bf16 v[140:143], a[80:83], a[16:19], v[164:167]\n" \
"v_exp_f32_e64 v134, v134\n" \
"v_mfma_f32_16x16x32_bf16 v[140:143], a[84:87], a[20:23], v[140:143]\n" \
"ds_read_b64_tr_b16 a[142:143], v211 offset:53760\n" \
"s_waitcnt lgkmcnt(8)\n" \
"v_mfma_f32_16x16x32_bf16 v[140:143], a[88:91], a[24:27], v[140:143]\n" \
"buffer_load_dwordx4 v212, %[srdk], s48 offen offset:1024 lds\n" \
"v_mfma_f32_16x16x32_bf16 v[140:143], a[92:95], a[28:31], v[140:143]\n" \
"v_exp_f32_e32 v135, v135\n" \
"v_mfma_f32_16x16x32_bf16 v[80:83], a[144:147], v[216:219], v[80:83]\n" \
"ds_read_b64_tr_b16 a[144:145], %[vvb] offset:50176\n" \
"ds_read_b64_tr_b16 a[146:147], %[vvb] offset:54272\n" \
"v_mfma_f32_16x16x32_bf16 v[80:83], a[176:179], v[200:203], v[80:83]\n" \
"v_exp_f32_e64 v136, v136\n" \
"v_mfma_f32_16x16x32_bf16 v[84:87], a[148:151], v[216:219], v[84:87]\n" \
"v_cvt_pk_bf16_f32 v186, v132, v133\n" \
"v_cvt_pk_bf16_f32 v187, v134, v135\n" \
"v_mfma_f32_16x16x32_bf16 v[84:87], a[180:183], v[200:203], v[84:87]\n" \
"v_exp_f32_e64 v137, v137\n" \
"v_mfma_f32_16x16x32_bf16 v[88:91], a[152:155], v[216:219], v[88:91]\n" \
"v_exp_f32_e64 v138, v138\n" \
"v_mfma_f32_16x16x32_bf16 v[88:91], a[184:187], v[200:203], v[88:91]\n" \
"ds_read_b64_tr_b16 a[148:149], v211 offset:50176\n" \
"ds_read_b64_tr_b16 a[150:151], v211 offset:54272\n" \
"v_mfma_f32_16x16x32_bf16 v[92:95], a[156:159], v[216:219], v[92:95]\n" \
"v_exp_f32_e64 v139, v139\n" \
"v_mfma_f32_16x16x32_bf16 v[92:95], a[188:191], v[200:203], v[92:95]\n" \
"ds_read_b64_tr_b16 a[152:153], %[vvb] offset:50688\n" \
"global_load_dword v237, %[vct], %[pct]\n" \
"v_mfma_f32_16x16x32_bf16 v[144:147], a[64:67], a[32:35], v[168:171]\n" \
"v_exp_f32_e64 v140, v140\n" \
"v_mfma_f32_16x16x32_bf16 v[144:147], a[68:71], a[36:39], v[144:147]\n" \
"ds_read_b64_tr_b16 a[154:155], %[vvb] offset:54784\n" \
"ds_read_b64_tr_b16 a[156:157], v211 offset:50688\n" \
"v_mfma_f32_16x16x32_bf16 v[144:147], a[72:75], a[40:43], v[144:147]\n" \
"v_exp_f32_e64 v141, v141\n" \
"v_mfma_f32_16x16x32_bf16 v[144:147], a[76:79], a[44:47], v[144:147]\n" \
"v_cvt_pk_bf16_f32 v188, v136, v137\n" \
"v_cvt_pk_bf16_f32 v189, v138, v139\n" \
"v_mfma_f32_16x16x32_bf16 v[148:151], a[80:83], a[32:35], v[168:171]\n" \
"v_exp_f32_e64 v142, v142\n" \
"v_mfma_f32_16x16x32_bf16 v[148:151], a[84:87], a[36:39], v[148:151]\n" \
"ds_read_b64_tr_b16 a[158:159], v211 offset:54784\n" \
"s_waitcnt lgkmcnt(8)\n" \
"v_mfma_f32_16x16x32_bf16 v[148:151], a[88:91], a[40:43], v[148:151]\n" \
"buffer_load_dwordx4 v213, %[srdk], s48 offen offset:2048 lds\n" \
"v_mfma_f32_16x16x32_bf16 v[148:151], a[92:95], a[44:47], v[148:151]\n" \
"v_exp_f32_e32 v143, v143\n" \
"v_mfma_f32_16x16x32_bf16 v[0:3], a[160:163], v[192:195], v[0:3]\n" \
"ds_read_b128 a[96:99], %[kb0] offset:57344\n" \
"v_mfma_f32_16x16x32_bf16 v[0:3], a[128:131], v[184:187], v[0:3]\n" \
"v_exp_f32_e64 v144, v144\n" \
"v_mfma_f32_16x16x32_bf16 v[4:7], a[164:167], v[192:195], v[4:7]\n" \
"v_cvt_pk_bf16_f32 v190, v140, v141\n" \
"v_cvt_pk_bf16_f32 v191, v142, v143\n" \
"v_mfma_f32_16x16x32_bf16 v[4:7], a[132:135], v[184:187], v[4:7]\n" \
"v_exp_f32_e64 v145, v145\n" \
"v_mfma_f32_16x16x32_bf16 v[8:11], a[168:171], v[192:195], v[8:11]\n" \
"ds_read_b128 a[100:103], v208 offset:57344\n" \
"v_mfma_f32_16x16x32_bf16 v[8:11], a[136:139], v[184:187], v[8:11]\n" \
"v_exp_f32_e64 v146, v146\n" \
"v_mfma_f32_16x16x32_bf16 v[12:15], a[172:175], v[192:195], v[12:15]\n" \
"ds_read_b128 a[104:107], v209 offset:57344\n" \
"v_mfma_f32_16x16x32_bf16 v[12:15], a[140:143], v[184:187], v[12:15]\n" \
"v_exp_f32_e64 v147, v147\n" \
"v_smfmac_f32_16x16x64_bf16 v[180:183], a[192:195], v[200:207], v228\n" \
"ds_read_b128 a[108:111], v210 offset:57344\n" \
"v_mfma_f32_16x16x32_bf16 v[152:155], a[64:67], a[48:51], v[172:175]\n" \
"v_exp_f32_e64 v148, v148\n" \
"v_mfma_f32_16x16x32_bf16 v[152:155], a[68:71], a[52:55], v[152:155]\n" \
"ds_read_b128 a[112:115], %[kb0] offset:61440\n" \
"v_mfma_f32_16x16x32_bf16 v[152:155], a[72:75], a[56:59], v[152:155]\n" \
"v_exp_f32_e64 v149, v149\n" \
"v_mfma_f32_16x16x32_bf16 v[152:155], a[76:79], a[60:63], v[152:155]\n" \
"v_cvt_pk_bf16_f32 v216, v144, v145\n" \
"v_cvt_pk_bf16_f32 v217, v146, v147\n" \
"v_mfma_f32_16x16x32_bf16 v[156:159], a[80:83], a[48:51], v[172:175]\n" \
"v_exp_f32_e64 v150, v150\n" \
"v_mfma_f32_16x16x32_bf16 v[156:159], a[84:87], a[52:55], v[156:159]\n" \
"ds_read_b128 a[116:119], v208 offset:61440\n" \
"s_waitcnt lgkmcnt(6)\n" \
"v_mfma_f32_16x16x32_bf16 v[156:159], a[88:91], a[56:59], v[156:159]\n" \
"buffer_load_dwordx4 v214, %[srdk], s48 offen offset:3072 lds\n" \
"v_mfma_f32_16x16x32_bf16 v[156:159], a[92:95], a[60:63], v[156:159]\n" \
"v_exp_f32_e32 v151, v151\n" \
"v_mfma_f32_16x16x32_bf16 v[16:19], a[176:179], v[192:195], v[16:19]\n" \
"ds_read_b128 a[120:123], v209 offset:61440\n" \
"v_mfma_f32_16x16x32_bf16 v[16:19], a[144:147], v[184:187], v[16:19]\n" \
"v_exp_f32_e64 v152, v152\n" \
"v_mfma_f32_16x16x32_bf16 v[20:23], a[180:183], v[192:195], v[20:23]\n" \
"v_cvt_pk_bf16_f32 v218, v148, v149\n" \
"v_cvt_pk_bf16_f32 v219, v150, v151\n" \
"v_mfma_f32_16x16x32_bf16 v[20:23], a[148:151], v[184:187], v[20:23]\n" \
"v_exp_f32_e64 v153, v153\n" \
"v_mfma_f32_16x16x32_bf16 v[24:27], a[184:187], v[192:195], v[24:27]\n" \
"v_exp_f32_e64 v154, v154\n" \
"v_mfma_f32_16x16x32_bf16 v[24:27], a[152:155], v[184:187], v[24:27]\n" \
"ds_read_b128 a[124:127], v210 offset:61440\n" \
"s_add_u32 s48, s48, 0x4000\n" \
"s_mov_b32 m0, s44\n" \
"s_addk_i32 s44, 0x400\n" \
"buffer_load_dwordx4 v229, s[64:67], s45 offen offset:0 lds\n" \
"s_add_u32 s45, s45, 229376\n" \
"v_mfma_f32_16x16x32_bf16 v[28:31], a[188:191], v[192:195], v[28:31]\n" \
"v_exp_f32_e64 v155, v155\n" \
"v_mfma_f32_16x16x32_bf16 v[28:31], a[156:159], v[184:187], v[28:31]\n" \
"s_waitcnt vmcnt(16) lgkmcnt(2)\n" \
"s_barrier\n" \
"v_mfma_f32_16x16x32_bf16 v[128:131], a[96:99], a[0:3], v[160:163]\n" \
"v_exp_f32_e64 v156, v156\n" \
"v_mfma_f32_16x16x32_bf16 v[128:131], a[100:103], a[4:7], v[128:131]\n" \
"s_add_u32 m0, %[m0b], 98304\n" \
"v_mfma_f32_16x16x32_bf16 v[128:131], a[104:107], a[8:11], v[128:131]\n" \
"v_exp_f32_e64 v157, v157\n" \
"v_mfma_f32_16x16x32_bf16 v[128:131], a[108:111], a[12:15], v[128:131]\n" \
"v_cvt_pk_bf16_f32 v220, v152, v153\n" \
"v_cvt_pk_bf16_f32 v221, v154, v155\n" \
"v_mfma_f32_16x16x32_bf16 v[132:135], a[112:115], a[0:3], v[160:163]\n" \
"v_exp_f32_e64 v158, v158\n" \
"v_mfma_f32_16x16x32_bf16 v[132:135], a[116:119], a[4:7], v[132:135]\n" \
"s_waitcnt lgkmcnt(0)\n" \
"v_mfma_f32_16x16x32_bf16 v[132:135], a[120:123], a[8:11], v[132:135]\n" \
"buffer_load_dwordx4 %[vd0], %[srdv], s48 offen offset:0 lds\n" \
"v_mfma_f32_16x16x32_bf16 v[132:135], a[124:127], a[12:15], v[132:135]\n" \
"v_exp_f32_e32 v159, v159\n" \
"v_mfma_f32_16x16x32_bf16 v[32:35], a[160:163], v[196:199], v[32:35]\n" \
"ds_read_b128 a[64:67], %[kb0] offset:0\n" \
"v_mfma_f32_16x16x32_bf16 v[32:35], a[128:131], v[188:191], v[32:35]\n" \
"v_exp_f32_e64 v128, v128\n" \
"v_mfma_f32_16x16x32_bf16 v[36:39], a[164:167], v[196:199], v[36:39]\n" \
"v_cvt_pk_bf16_f32 v222, v156, v157\n" \
"v_cvt_pk_bf16_f32 v223, v158, v159\n" \
"v_mfma_f32_16x16x32_bf16 v[36:39], a[132:135], v[188:191], v[36:39]\n" \
"v_exp_f32_e64 v129, v129\n" \
"v_mfma_f32_16x16x32_bf16 v[40:43], a[168:171], v[196:199], v[40:43]\n" \
"ds_read_b128 a[68:71], v208 offset:0\n" \
"v_mfma_f32_16x16x32_bf16 v[40:43], a[136:139], v[188:191], v[40:43]\n" \
"v_exp_f32_e64 v130, v130\n" \
"v_mfma_f32_16x16x32_bf16 v[44:47], a[172:175], v[196:199], v[44:47]\n" \
"ds_read_b128 a[72:75], v209 offset:0\n" \
"v_mfma_f32_16x16x32_bf16 v[44:47], a[140:143], v[188:191], v[44:47]\n" \
"v_exp_f32_e64 v131, v131\n" \
"v_smfmac_f32_16x16x64_bf16 v[176:179], a[192:195], v[184:191], v228\n" \
"buffer_store_dwordx4 a[196:199], v235, %[srdz], s63 offen offset:2048 sc1\n" \
"s_add_u32 s63, s63, 3072\n" \
"v_mfma_f32_16x16x32_bf16 v[136:139], a[96:99], a[16:19], v[164:167]\n" \
"v_exp_f32_e64 v132, v132\n" \
"v_mfma_f32_16x16x32_bf16 v[136:139], a[100:103], a[20:23], v[136:139]\n" \
"ds_read_b128 a[76:79], v210 offset:0\n" \
"v_mfma_f32_16x16x32_bf16 v[136:139], a[104:107], a[24:27], v[136:139]\n" \
"v_exp_f32_e64 v133, v133\n" \
"v_mfma_f32_16x16x32_bf16 v[136:139], a[108:111], a[28:31], v[136:139]\n" \
"v_cvt_pk_bf16_f32 v192, v128, v129\n" \
"v_cvt_pk_bf16_f32 v193, v130, v131\n" \
"v_mfma_f32_16x16x32_bf16 v[140:143], a[112:115], a[16:19], v[164:167]\n" \
"v_exp_f32_e64 v134, v134\n" \
"v_mfma_f32_16x16x32_bf16 v[140:143], a[116:119], a[20:23], v[140:143]\n" \
"ds_read_b128 a[80:83], %[kb0] offset:4096\n" \
"v_mfma_f32_16x16x32_bf16 v[140:143], a[120:123], a[24:27], v[140:143]\n" \
"buffer_load_dwordx4 v215, %[srdv], s48 offen offset:1024 lds\n" \
"v_mfma_f32_16x16x32_bf16 v[140:143], a[124:127], a[28:31], v[140:143]\n" \
"v_exp_f32_e64 v135, v135\n" \
"v_mfma_f32_16x16x32_bf16 v[48:51], a[176:179], v[196:199], v[48:51]\n" \
"ds_read_b128 a[84:87], v208 offset:4096\n" \
"v_mfma_f32_16x16x32_bf16 v[48:51], a[144:147], v[188:191], v[48:51]\n" \
"v_exp_f32_e64 v136, v136\n" \
"v_mfma_f32_16x16x32_bf16 v[52:55], a[180:183], v[196:199], v[52:55]\n" \
"v_cvt_pk_bf16_f32 v194, v132, v133\n" \
"v_cvt_pk_bf16_f32 v195, v134, v135\n" \
"v_mfma_f32_16x16x32_bf16 v[52:55], a[148:151], v[188:191], v[52:55]\n" \
"v_exp_f32_e64 v137, v137\n" \
"v_mfma_f32_16x16x32_bf16 v[56:59], a[184:187], v[196:199], v[56:59]\n" \
"v_exp_f32_e64 v138, v138\n" \
"v_mfma_f32_16x16x32_bf16 v[56:59], a[152:155], v[188:191], v[56:59]\n" \
"ds_read_b128 a[88:91], v209 offset:4096\n" \
"v_mfma_f32_16x16x32_bf16 v[60:63], a[188:191], v[196:199], v[60:63]\n" \
"v_exp_f32_e64 v139, v139\n" \
"v_mfma_f32_16x16x32_bf16 v[60:63], a[156:159], v[188:191], v[60:63]\n" \
"s_cmp_lg_u32 %[wid], 2\n" \
"s_cbranch_scc1 LtB7_%=\n" \
"global_load_dword v237, v236, s[58:59]\n" \
"s_add_u32 s61, s48, 49152\n" \
"s_mov_b32 exec_hi, 0\n" \
"s_nop 0\n" \
"buffer_load_dword v237, v224, %[srdp], s61 offen\n" \
"s_mov_b32 exec_hi, -1\n" \
"s_nop 0\n" \
"LtB7_%=:\n" \
"v_mfma_f32_16x16x32_bf16 v[144:147], a[96:99], a[32:35], v[168:171]\n" \
"v_exp_f32_e64 v140, v140\n" \
"v_mfma_f32_16x16x32_bf16 v[144:147], a[100:103], a[36:39], v[144:147]\n" \
"ds_read_b128 a[92:95], v210 offset:4096\n" \
"v_mfma_f32_16x16x32_bf16 v[144:147], a[104:107], a[40:43], v[144:147]\n" \
"v_exp_f32_e64 v141, v141\n" \
"v_mfma_f32_16x16x32_bf16 v[144:147], a[108:111], a[44:47], v[144:147]\n" \
"v_cvt_pk_bf16_f32 v196, v136, v137\n" \
"v_cvt_pk_bf16_f32 v197, v138, v139\n" \
"v_mfma_f32_16x16x32_bf16 v[148:151], a[112:115], a[32:35], v[168:171]\n" \
"v_exp_f32_e64 v142, v142\n" \
"v_mfma_f32_16x16x32_bf16 v[148:151], a[116:119], a[36:39], v[148:151]\n" \
"v_mfma_f32_16x16x32_bf16 v[148:151], a[120:123], a[40:43], v[148:151]\n" \
"buffer_load_dwordx4 %[vd0], %[srdv], s48 offen offset:2048 lds\n" \
"v_mfma_f32_16x16x32_bf16 v[148:151], a[124:127], a[44:47], v[148:151]\n" \
"v_exp_f32_e64 v143, v143\n" \
"v_mfma_f32_16x16x32_bf16 v[96:99], a[160:163], v[204:207], v[96:99]\n" \
"ds_read_b64_tr_b16 a[160:161], %[vvb] offset:57344\n" \
"v_mfma_f32_16x16x32_bf16 v[96:99], a[128:131], v[220:223], v[96:99]\n" \
"v_exp_f32_e64 v144, v144\n" \
"v_mfma_f32_16x16x32_bf16 v[100:103], a[164:167], v[204:207], v[100:103]\n" \
"v_cvt_pk_bf16_f32 v198, v140, v141\n" \
"v_cvt_pk_bf16_f32 v199, v142, v143\n" \
"v_mfma_f32_16x16x32_bf16 v[100:103], a[132:135], v[220:223], v[100:103]\n" \
"v_exp_f32_e64 v145, v145\n" \
"v_mfma_f32_16x16x32_bf16 v[104:107], a[168:171], v[204:207], v[104:107]\n" \
"ds_read_b64_tr_b16 a[162:163], %[vvb] offset:61440\n" \
"ds_read_b64_tr_b16 a[164:165], v211 offset:57344\n" \
"v_mfma_f32_16x16x32_bf16 v[104:107], a[136:139], v[220:223], v[104:107]\n" \
"v_exp_f32_e64 v146, v146\n" \
"v_mfma_f32_16x16x32_bf16 v[108:111], a[172:175], v[204:207], v[108:111]\n" \
"ds_read_b64_tr_b16 a[166:167], v211 offset:61440\n" \
"v_mfma_f32_16x16x32_bf16 v[108:111], a[140:143], v[220:223], v[108:111]\n" \
"v_exp_f32_e64 v147, v147\n" \
"v_smfmac_f32_16x16x64_bf16 v[180:183], a[192:195], v[216:223], v228\n" \
"ds_read_b64_tr_b16 a[168:169], %[vvb] offset:57856\n" \
"v_mfma_f32_16x16x32_bf16 v[152:155], a[96:99], a[48:51], v[172:175]\n" \
"v_exp_f32_e64 v148, v148\n" \
"v_mfma_f32_16x16x32_bf16 v[152:155], a[100:103], a[52:55], v[152:155]\n" \
"ds_read_b64_tr_b16 a[170:171], %[vvb] offset:61952\n" \
"v_mfma_f32_16x16x32_bf16 v[152:155], a[104:107], a[56:59], v[152:155]\n" \
"v_exp_f32_e64 v149, v149\n" \
"v_mfma_f32_16x16x32_bf16 v[152:155], a[108:111], a[60:63], v[152:155]\n" \
"v_cvt_pk_bf16_f32 v200, v144, v145\n" \
"v_cvt_pk_bf16_f32 v201, v146, v147\n" \
"v_mfma_f32_16x16x32_bf16 v[156:159], a[112:115], a[48:51], v[172:175]\n" \
".if . - adaln_body != 28672\n" \
".error \"size model\"\n" \
".endif\n" \
"Lk6_%=:\n" \
"s_cbranch_execz Lk7_%=\n" \
"s_nop 0\n" \
"v_exp_f32_e64 v150, v150\n" \
"v_mfma_f32_16x16x32_bf16 v[156:159], a[116:119], a[52:55], v[156:159]\n" \
"ds_read_b64_tr_b16 a[172:173], v211 offset:57856\n" \
"v_mfma_f32_16x16x32_bf16 v[156:159], a[120:123], a[56:59], v[156:159]\n" \
"buffer_load_dwordx4 v215, %[srdv], s48 offen offset:3072 lds\n" \
"v_mfma_f32_16x16x32_bf16 v[156:159], a[124:127], a[60:63], v[156:159]\n" \
"v_exp_f32_e64 v151, v151\n" \
"v_mfma_f32_16x16x32_bf16 v[112:115], a[176:179], v[204:207], v[112:115]\n" \
"ds_read_b64_tr_b16 a[174:175], v211 offset:61952\n" \
"ds_read_b64_tr_b16 a[176:177], %[vvb] offset:58368\n" \
"v_mfma_f32_16x16x32_bf16 v[112:115], a[144:147], v[220:223], v[112:115]\n" \
"v_exp_f32_e64 v152, v152\n" \
"v_mfma_f32_16x16x32_bf16 v[116:119], a[180:183], v[204:207], v[116:119]\n" \
"v_cvt_pk_bf16_f32 v202, v148, v149\n" \
"v_cvt_pk_bf16_f32 v203, v150, v151\n" \
"v_mfma_f32_16x16x32_bf16 v[116:119], a[148:151], v[220:223], v[116:119]\n" \
"v_exp_f32_e64 v153, v153\n" \
"v_mfma_f32_16x16x32_bf16 v[120:123], a[184:187], v[204:207], v[120:123]\n" \
"v_exp_f32_e64 v154, v154\n" \
"v_mfma_f32_16x16x32_bf16 v[120:123], a[152:155], v[220:223], v[120:123]\n" \
"ds_read_b64_tr_b16 a[178:179], %[vvb] offset:62464\n" \
"v_mfma_f32_16x16x32_bf16 v[124:127], a[188:191], v[204:207], v[124:127]\n" \
"v_exp_f32_e64 v155, v155\n" \
"v_mfma_f32_16x16x32_bf16 v[124:127], a[156:159], v[220:223], v[124:127]\n" \
"ds_read_b64_tr_b16 a[180:181], v211 offset:58368\n" \
"s_waitcnt lgkmcnt(13)\n" \
"v_mfma_f32_16x16x32_bf16 v[128:131], a[64:67], a[0:3], v[160:163]\n" \
"v_exp_f32_e32 v156, v156\n" \
"v_mfma_f32_16x16x32_bf16 v[128:131], a[68:71], a[4:7], v[128:131]\n" \
"ds_read_b64_tr_b16 a[182:183], v211 offset:62464\n" \
"s_add_u32 m0, %[m0b], 49152\n" \
"v_mfma_f32_16x16x32_bf16 v[128:131], a[72:75], a[8:11], v[128:131]\n" \
"v_exp_f32_e64 v157, v157\n" \
"v_mfma_f32_16x16x32_bf16 v[128:131], a[76:79], a[12:15], v[128:131]\n" \
"v_cvt_pk_bf16_f32 v204, v152, v153\n" \
"v_cvt_pk_bf16_f32 v205, v154, v155\n" \
"v_mfma_f32_16x16x32_bf16 v[132:135], a[80:83], a[0:3], v[160:163]\n" \
"v_exp_f32_e64 v158, v158\n" \
"v_mfma_f32_16x16x32_bf16 v[132:135], a[84:87], a[4:7], v[132:135]\n" \
"ds_read_b64_tr_b16 a[184:185], %[vvb] offset:58880\n" \
"s_waitcnt lgkmcnt(5)\n" \
"v_mfma_f32_16x16x32_bf16 v[132:135], a[88:91], a[8:11], v[132:135]\n" \
"buffer_load_dwordx4 %[kd0], %[srdk], s48 offen offset:0 lds\n" \
"v_mfma_f32_16x16x32_bf16 v[132:135], a[92:95], a[12:15], v[132:135]\n" \
"v_exp_f32_e32 v159, v159\n" \
"v_mfma_f32_16x16x32_bf16 v[64:67], a[128:131], v[216:219], v[64:67]\n" \
"ds_read_b64_tr_b16 a[186:187], %[vvb] offset:62976\n" \
"ds_read_b64_tr_b16 a[188:189], v211 offset:58880\n" \
"v_mfma_f32_16x16x32_bf16 v[64:67], a[160:163], v[200:203], v[64:67]\n" \
"v_exp_f32_e64 v128, v128\n" \
"v_mfma_f32_16x16x32_bf16 v[68:71], a[132:135], v[216:219], v[68:71]\n" \
"v_cvt_pk_bf16_f32 v206, v156, v157\n" \
"v_cvt_pk_bf16_f32 v207, v158, v159\n" \
"v_mfma_f32_16x16x32_bf16 v[68:71], a[164:167], v[200:203], v[68:71]\n" \
"v_exp_f32_e64 v129, v129\n" \
"v_mfma_f32_16x16x32_bf16 v[72:75], a[136:139], v[216:219], v[72:75]\n" \
"ds_read_b64_tr_b16 a[190:191], v211 offset:62976\n" \
"ds_read_b64_tr_b16 a[128:129], %[vvb] offset:0\n" \
"v_mfma_f32_16x16x32_bf16 v[72:75], a[168:171], v[200:203], v[72:75]\n" \
"v_exp_f32_e64 v130, v130\n" \
"v_mfma_f32_16x16x32_bf16 v[76:79], a[140:143], v[216:219], v[76:79]\n" \
"ds_read_b64_tr_b16 a[130:131], %[vvb] offset:4096\n" \
"ds_read_b64_tr_b16 a[132:133], v211 offset:0\n" \
"v_mfma_f32_16x16x32_bf16 v[76:79], a[172:175], v[200:203], v[76:79]\n" \
"v_exp_f32_e64 v131, v131\n" \
"v_smfmac_f32_16x16x64_bf16 v[176:179], a[192:195], v[192:199], v228\n" \
"ds_read_b64_tr_b16 a[134:135], v211 offset:4096\n" \
"ds_read_b64_tr_b16 a[136:137], %[vvb] offset:512\n" \
"v_mfma_f32_16x16x32_bf16 v[136:139], a[64:67], a[16:19], v[164:167]\n" \
"v_exp_f32_e64 v132, v132\n" \
"v_mfma_f32_16x16x32_bf16 v[136:139], a[68:71], a[20:23], v[136:139]\n" \
"ds_read_b64_tr_b16 a[138:139], %[vvb] offset:4608\n" \
"ds_read_b64_tr_b16 a[140:141], v211 offset:512\n" \
"v_mfma_f32_16x16x32_bf16 v[136:139], a[72:75], a[24:27], v[136:139]\n" \
"v_exp_f32_e64 v133, v133\n" \
"v_mfma_f32_16x16x32_bf16 v[136:139], a[76:79], a[28:31], v[136:139]\n" \
"v_cvt_pk_bf16_f32 v184, v128, v129\n" \
"v_cvt_pk_bf16_f32 v185, v130, v131\n" \
"v_mfma_f32_16x16x32_bf16 v[140:143], a[80:83], a[16:19], v[164:167]\n" \
"v_exp_f32_e64 v134, v134\n" \
"v_mfma_f32_16x16x32_bf16 v[140:143], a[84:87], a[20:23], v[140:143]\n" \
"ds_read_b64_tr_b16 a[142:143], v211 offset:4608\n" \
"s_waitcnt lgkmcnt(8)\n" \
"v_mfma_f32_16x16x32_bf16 v[140:143], a[88:91], a[24:27], v[140:143]\n" \
"buffer_load_dwordx4 v212, %[srdk], s48 offen offset:1024 lds\n" \
"v_mfma_f32_16x16x32_bf16 v[140:143], a[92:95], a[28:31], v[140:143]\n" \
"v_exp_f32_e32 v135, v135\n" \
"v_mfma_f32_16x16x32_bf16 v[80:83], a[144:147], v[216:219], v[80:83]\n" \
"ds_read_b64_tr_b16 a[144:145], %[vvb] offset:1024\n" \
"ds_read_b64_tr_b16 a[146:147], %[vvb] offset:5120\n" \
"v_mfma_f32_16x16x32_bf16 v[80:83], a[176:179], v[200:203], v[80:83]\n" \
"v_exp_f32_e64 v136, v136\n" \
"v_mfma_f32_16x16x32_bf16 v[84:87], a[148:151], v[216:219], v[84:87]\n" \
"v_cvt_pk_bf16_f32 v186, v132, v133\n" \
"v_cvt_pk_bf16_f32 v187, v134, v135\n" \
"v_mfma_f32_16x16x32_bf16 v[84:87], a[180:183], v[200:203], v[84:87]\n" \
"v_exp_f32_e64 v137, v137\n" \
"v_mfma_f32_16x16x32_bf16 v[88:91], a[152:155], v[216:219], v[88:91]\n" \
"v_exp_f32_e64 v138, v138\n" \
"v_mfma_f32_16x16x32_bf16 v[88:91], a[184:187], v[200:203], v[88:91]\n" \
"ds_read_b64_tr_b16 a[148:149], v211 offset:1024\n" \
"ds_read_b64_tr_b16 a[150:151], v211 offset:5120\n" \
"v_mfma_f32_16x16x32_bf16 v[92:95], a[156:159], v[216:219], v[92:95]\n" \
"v_exp_f32_e64 v139, v139\n" \
"v_mfma_f32_16x16x32_bf16 v[92:95], a[188:191], v[200:203], v[92:95]\n" \
"ds_read_b64_tr_b16 a[152:153], %[vvb] offset:1536\n" \
"buffer_store_dwordx4 a[196:199], v235, %[srdz], s62 offen offset:2048 sc1\n" \
"s_add_u32 s62, s62, 3072\n" \
"v_mfma_f32_16x16x32_bf16 v[144:147], a[64:67], a[32:35], v[168:171]\n" \
"v_exp_f32_e64 v140, v140\n" \
"v_mfma_f32_16x16x32_bf16 v[144:147], a[68:71], a[36:39], v[144:147]\n" \
"ds_read_b64_tr_b16 a[154:155], %[vvb] offset:5632\n" \
"ds_read_b64_tr_b16 a[156:157], v211 offset:1536\n" \
"v_mfma_f32_16x16x32_bf16 v[144:147], a[72:75], a[40:43], v[144:147]\n" \
"v_exp_f32_e64 v141, v141\n" \
"v_mfma_f32_16x16x32_bf16 v[144:147], a[76:79], a[44:47], v[144:147]\n" \
"v_cvt_pk_bf16_f32 v188, v136, v137\n" \
"v_cvt_pk_bf16_f32 v189, v138, v139\n" \
"v_mfma_f32_16x16x32_bf16 v[148:151], a[80:83], a[32:35], v[168:171]\n" \
"v_exp_f32_e64 v142, v142\n" \
"v_mfma_f32_16x16x32_bf16 v[148:151], a[84:87], a[36:39], v[148:151]\n" \
"ds_read_b64_tr_b16 a[158:159], v211 offset:5632\n" \
"s_waitcnt lgkmcnt(8)\n" \
"v_mfma_f32_16x16x32_bf16 v[148:151], a[88:91], a[40:43], v[148:151]\n" \
"buffer_load_dwordx4 v213, %[srdk], s48 offen offset:2048 lds\n" \
"v_mfma_f32_16x16x32_bf16 v[148:151], a[92:95], a[44:47], v[148:151]\n" \
"v_exp_f32_e32 v143, v143\n" \
"v_mfma_f32_16x16x32_bf16 v[0:3], a[160:163], v[192:195], v[0:3]\n" \
"ds_read_b128 a[96:99], %[kb0] offset:8192\n" \
"v_mfma_f32_16x16x32_bf16 v[0:3], a[128:131], v[184:187], v[0:3]\n" \
"v_exp_f32_e64 v144, v144\n" \
"v_mfma_f32_16x16x32_bf16 v[4:7], a[164:167], v[192:195], v[4:7]\n" \
"v_cvt_pk_bf16_f32 v190, v140, v141\n" \
"v_cvt_pk_bf16_f32 v191, v142, v143\n" \
"v_mfma_f32_16x16x32_bf16 v[4:7], a[132:135], v[184:187], v[4:7]\n" \
"v_exp_f32_e64 v145, v145\n" \
"v_mfma_f32_16x16x32_bf16 v[8:11], a[168:171], v[192:195], v[8:11]\n" \
"ds_read_b128 a[100:103], v208 offset:8192\n" \
"v_mfma_f32_16x16x32_bf16 v[8:11], a[136:139], v[184:187], v[8:11]\n" \
"v_exp_f32_e64 v146, v146\n" \
"v_mfma_f32_16x16x32_bf16 v[12:15], a[172:175], v[192:195], v[12:15]\n" \
"ds_read_b128 a[104:107], v209 offset:8192\n" \
"v_mfma_f32_16x16x32_bf16 v[12:15], a[140:143], v[184:187], v[12:15]\n" \
"v_exp_f32_e64 v147, v147\n" \
"v_smfmac_f32_16x16x64_bf16 v[180:183], a[192:195], v[200:207], v228\n" \
"ds_read_b128 a[108:111], v210 offset:8192\n" \
"v_mfma_f32_16x16x32_bf16 v[152:155], a[64:67], a[48:51], v[172:175]\n" \
"v_exp_f32_e64 v148, v148\n" \
"v_mfma_f32_16x16x32_bf16 v[152:155], a[68:71], a[52:55], v[152:155]\n" \
"ds_read_b128 a[112:115], %[kb0] offset:12288\n" \
"v_mfma_f32_16x16x32_bf16 v[152:155], a[72:75], a[56:59], v[152:155]\n" \
"v_exp_f32_e64 v149, v149\n" \
"v_mfma_f32_16x16x32_bf16 v[152:155], a[76:79], a[60:63], v[152:155]\n" \
"v_cvt_pk_bf16_f32 v216, v144, v145\n" \
"v_cvt_pk_bf16_f32 v217, v146, v147\n" \
"v_mfma_f32_16x16x32_bf16 v[156:159], a[80:83], a[48:51], v[172:175]\n" \
"v_exp_f32_e64 v150, v150\n" \
"v_mfma_f32_16x16x32_bf16 v[156:159], a[84:87], a[52:55], v[156:159]\n" \
"ds_read_b128 a[116:119], v208 offset:12288\n" \
"s_waitcnt lgkmcnt(6)\n" \
"v_mfma_f32_16x16x32_bf16 v[156:159], a[88:91], a[56:59], v[156:159]\n" \
"buffer_load_dwordx4 v214, %[srdk], s48 offen offset:3072 lds\n" \
"v_mfma_f32_16x16x32_bf16 v[156:159], a[92:95], a[60:63], v[156:159]\n" \
"v_exp_f32_e32 v151, v151\n" \
"v_mfma_f32_16x16x32_bf16 v[16:19], a[176:179], v[192:195], v[16:19]\n" \
"ds_read_b128 a[120:123], v209 offset:12288\n" \
"v_mfma_f32_16x16x32_bf16 v[16:19], a[144:147], v[184:187], v[16:19]\n" \
"v_exp_f32_e64 v152, v152\n" \
"v_mfma_f32_16x16x32_bf16 v[20:23], a[180:183], v[192:195], v[20:23]\n" \
"v_cvt_pk_bf16_f32 v218, v148, v149\n" \
"v_cvt_pk_bf16_f32 v219, v150, v151\n" \
"v_mfma_f32_16x16x32_bf16 v[20:23], a[148:151], v[184:187], v[20:23]\n" \
"v_exp_f32_e64 v153, v153\n" \
"v_mfma_f32_16x16x32_bf16 v[24:27], a[184:187], v[192:195], v[24:27]\n" \
"v_exp_f32_e64 v154, v154\n" \
"v_mfma_f32_16x16x32_bf16 v[24:27], a[152:155], v[184:187], v[24:27]\n" \
"ds_read_b128 a[124:127], v210 offset:12288\n" \
"s_add_u32 s48, s48, 0x4000\n" \
"v_mfma_f32_16x16x32_bf16 v[28:31], a[188:191], v[192:195], v[28:31]\n" \
"v_exp_f32_e64 v155, v155\n" \
"v_mfma_f32_16x16x32_bf16 v[28:31], a[156:159], v[184:187], v[28:31]\n" \
"s_waitcnt vmcnt(17) lgkmcnt(2)\n" \
"s_barrier\n" \
"v_mfma_f32_16x16x32_bf16 v[128:131], a[96:99], a[0:3], v[160:163]\n" \
"v_exp_f32_e64 v156, v156\n" \
"v_mfma_f32_16x16x32_bf16 v[128:131], a[100:103], a[4:7], v[128:131]\n" \
"s_add_u32 m0, %[m0b], 114688\n" \
"v_mfma_f32_16x16x32_bf16 v[128:131], a[104:107], a[8:11], v[128:131]\n" \
"v_exp_f32_e64 v157, v157\n" \
"v_mfma_f32_16x16x32_bf16 v[128:131], a[108:111], a[12:15], v[128:131]\n" \
"v_cvt_pk_bf16_f32 v220, v152, v153\n" \
"v_cvt_pk_bf16_f32 v221, v154, v155\n" \
"v_mfma_f32_16x16x32_bf16 v[132:135], a[112:115], a[0:3], v[160:163]\n" \
"v_exp_f32_e64 v158, v158\n" \
"v_mfma_f32_16x16x32_bf16 v[132:135], a[116:119], a[4:7], v[132:135]\n" \
"s_waitcnt lgkmcnt(0)\n" \
"v_mfma_f32_16x16x32_bf16 v[132:135], a[120:123], a[8:11], v[132:135]\n" \
"buffer_load_dwordx4 %[vd0], %[srdv], s48 offen offset:0 lds\n" \
"v_mfma_f32_16x16x32_bf16 v[132:135], a[124:127], a[12:15], v[132:135]\n" \
"v_exp_f32_e32 v159, v159\n" \
"v_mfma_f32_16x16x32_bf16 v[32:35], a[160:163], v[196:199], v[32:35]\n" \
"ds_read_b128 a[64:67], %[kb0] offset:16384\n" \
"v_mfma_f32_16x16x32_bf16 v[32:35], a[128:131], v[188:191], v[32:35]\n" \
"v_exp_f32_e64 v128, v128\n" \
"v_mfma_f32_16x16x32_bf16 v[36:39], a[164:167], v[196:199], v[36:39]\n" \
"v_cvt_pk_bf16_f32 v222, v156, v157\n" \
"v_cvt_pk_bf16_f32 v223, v158, v159\n" \
"v_mfma_f32_16x16x32_bf16 v[36:39], a[132:135], v[188:191], v[36:39]\n" \
"v_exp_f32_e64 v129, v129\n" \
"v_mfma_f32_16x16x32_bf16 v[40:43], a[168:171], v[196:199], v[40:43]\n" \
"ds_read_b128 a[68:71], v208 offset:16384\n" \
"v_mfma_f32_16x16x32_bf16 v[40:43], a[136:139], v[188:191], v[40:43]\n" \
"v_exp_f32_e64 v130, v130\n" \
"v_mfma_f32_16x16x32_bf16 v[44:47], a[172:175], v[196:199], v[44:47]\n" \
"ds_read_b128 a[72:75], v209 offset:16384\n" \
"v_mfma_f32_16x16x32_bf16 v[44:47], a[140:143], v[188:191], v[44:47]\n" \
"v_exp_f32_e64 v131, v131\n" \
"v_smfmac_f32_16x16x64_bf16 v[176:179], a[192:195], v[184:191], v228\n" \
"v_mfma_f32_16x16x32_bf16 v[136:139], a[96:99], a[16:19], v[164:167]\n" \
"v_exp_f32_e64 v132, v132\n" \
"v_mfma_f32_16x16x32_bf16 v[136:139], a[100:103], a[20:23], v[136:139]\n" \
"ds_read_b128 a[76:79], v210 offset:16384\n" \
"v_mfma_f32_16x16x32_bf16 v[136:139], a[104:107], a[24:27], v[136:139]\n" \
"v_exp_f32_e64 v133, v133\n" \
"v_mfma_f32_16x16x32_bf16 v[136:139], a[108:111], a[28:31], v[136:139]\n" \
"v_cvt_pk_bf16_f32 v192, v128, v129\n" \
"v_cvt_pk_bf16_f32 v193, v130, v131\n" \
"v_mfma_f32_16x16x32_bf16 v[140:143], a[112:115], a[16:19], v[164:167]\n" \
"v_exp_f32_e64 v134, v134\n" \
"v_mfma_f32_16x16x32_bf16 v[140:143], a[116:119], a[20:23], v[140:143]\n" \
"ds_read_b128 a[80:83], %[kb0] offset:20480\n" \
"v_mfma_f32_16x16x32_bf16 v[140:143], a[120:123], a[24:27], v[140:143]\n" \
"buffer_load_dwordx4 v215, %[srdv], s48 offen offset:1024 lds\n" \
"v_mfma_f32_16x16x32_bf16 v[140:143], a[124:127], a[28:31], v[140:143]\n" \
"v_exp_f32_e64 v135, v135\n" \
"v_mfma_f32_16x16x32_bf16 v[48:51], a[176:179], v[196:199], v[48:51]\n" \
"ds_read_b128 a[84:87], v208 offset:20480\n" \
"v_mfma_f32_16x16x32_bf16 v[48:51], a[144:147], v[188:191], v[48:51]\n" \
"v_exp_f32_e64 v136, v136\n" \
"v_mfma_f32_16x16x32_bf16 v[52:55], a[180:183], v[196:199], v[52:55]\n" \
"v_cvt_pk_bf16_f32 v194, v132, v133\n" \
"v_cvt_pk_bf16_f32 v195, v134, v135\n" \
"v_mfma_f32_16x16x32_bf16 v[52:55], a[148:151], v[188:191], v[52:55]\n" \
"v_exp_f32_e64 v137, v137\n" \
"v_mfma_f32_16x16x32_bf16 v[56:59], a[184:187], v[196:199], v[56:59]\n" \
"v_exp_f32_e64 v138, v138\n" \
"v_mfma_f32_16x16x32_bf16 v[56:59], a[152:155], v[188:191], v[56:59]\n" \
"ds_read_b128 a[88:91], v209 offset:20480\n" \
"v_mfma_f32_16x16x32_bf16 v[60:63], a[188:191], v[196:199], v[60:63]\n" \
"v_exp_f32_e64 v139, v139\n" \
"v_mfma_f32_16x16x32_bf16 v[60:63], a[156:159], v[188:191], v[60:63]\n" \
"s_cmp_lg_u32 %[wid], 3\n" \
"s_cbranch_scc1 LtB1_%=\n" \
"global_load_dword v237, v236, s[58:59]\n" \
"s_add_u32 s61, s48, 49152\n" \
"s_mov_b32 exec_hi, 0\n" \
"s_nop 0\n" \
"buffer_load_dword v237, v224, %[srdp], s61 offen\n" \
"s_mov_b32 exec_hi, -1\n" \
"s_nop 0\n" \
"LtB1_%=:\n" \
"v_mfma_f32_16x16x32_bf16 v[144:147], a[96:99], a[32:35], v[168:171]\n" \
"v_exp_f32_e64 v140, v140\n" \
"v_mfma_f32_16x16x32_bf16 v[144:147], a[100:103], a[36:39], v[144:147]\n" \
"ds_read_b128 a[92:95], v210 offset:20480\n" \
"v_mfma_f32_16x16x32_bf16 v[144:147], a[104:107], a[40:43], v[144:147]\n" \
"v_exp_f32_e64 v141, v141\n" \
"v_mfma_f32_16x16x32_bf16 v[144:147], a[108:111], a[44:47], v[144:147]\n" \
"v_cvt_pk_bf16_f32 v196, v136, v137\n" \
"v_cvt_pk_bf16_f32 v197, v138, v139\n" \
"v_mfma_f32_16x16x32_bf16 v[148:151], a[112:115], a[32:35], v[168:171]\n" \
"v_exp_f32_e64 v142, v142\n" \
"v_mfma_f32_16x16x32_bf16 v[148:151], a[116:119], a[36:39], v[148:151]\n" \
"v_mfma_f32_16x16x32_bf16 v[148:151], a[120:123], a[40:43], v[148:151]\n" \
"buffer_load_dwordx4 %[vd0], %[srdv], s48 offen offset:2048 lds\n" \
"v_mfma_f32_16x16x32_bf16 v[148:151], a[124:127], a[44:47], v[148:151]\n" \
"v_exp_f32_e64 v143, v143\n" \
"v_mfma_f32_16x16x32_bf16 v[96:99], a[160:163], v[204:207], v[96:99]\n" \
"ds_read_b64_tr_b16 a[160:161], %[vvb] offset:8192\n" \
"v_mfma_f32_16x16x32_bf16 v[96:99], a[128:131], v[220:223], v[96:99]\n" \
"v_exp_f32_e64 v144, v144\n" \
"v_mfma_f32_16x16x32_bf16 v[100:103], a[164:167], v[204:207], v[100:103]\n" \
"v_cvt_pk_bf16_f32 v198, v140, v141\n" \
"v_cvt_pk_bf16_f32 v199, v142, v143\n" \
"v_mfma_f32_16x16x32_bf16 v[100:103], a[132:135], v[220:223], v[100:103]\n" \
"v_exp_f32_e64 v145, v145\n" \
"v_mfma_f32_16x16x32_bf16 v[104:107], a[168:171], v[204:207], v[104:107]\n" \
"ds_read_b64_tr_b16 a[162:163], %[vvb] offset:12288\n" \
"ds_read_b64_tr_b16 a[164:165], v211 offset:8192\n" \
"v_mfma_f32_16x16x32_bf16 v[104:107], a[136:139], v[220:223], v[104:107]\n" \
"v_exp_f32_e64 v146, v146\n" \
"v_mfma_f32_16x16x32_bf16 v[108:111], a[172:175], v[204:207], v[108:111]\n" \
"ds_read_b64_tr_b16 a[166:167], v211 offset:12288\n" \
"v_mfma_f32_16x16x32_bf16 v[108:111], a[140:143], v[220:223], v[108:111]\n" \
"v_exp_f32_e64 v147, v147\n" \
"v_smfmac_f32_16x16x64_bf16 v[180:183], a[192:195], v[216:223], v228\n" \
"ds_read_b64_tr_b16 a[168:169], %[vvb] offset:8704\n" \
"v_mfma_f32_16x16x32_bf16 v[152:155], a[96:99], a[48:51], v[172:175]\n" \
"v_exp_f32_e64 v148, v148\n" \
"v_mfma_f32_16x16x32_bf16 v[152:155], a[100:103], a[52:55], v[152:155]\n" \
"ds_read_b64_tr_b16 a[170:171], %[vvb] offset:12800\n" \
"v_mfma_f32_16x16x32_bf16 v[152:155], a[104:107], a[56:59], v[152:155]\n" \
"v_exp_f32_e64 v149, v149\n" \
"v_mfma_f32_16x16x32_bf16 v[152:155], a[108:111], a[60:63], v[152:155]\n" \
"v_cvt_pk_bf16_f32 v200, v144, v145\n" \
"v_cvt_pk_bf16_f32 v201, v146, v147\n" \
"v_mfma_f32_16x16x32_bf16 v[156:159], a[112:115], a[48:51], v[172:175]\n" \
"v_exp_f32_e64 v150, v150\n" \
"v_mfma_f32_16x16x32_bf16 v[156:159], a[116:119], a[52:55], v[156:159]\n" \
"ds_read_b64_tr_b16 a[172:173], v211 offset:8704\n" \
"v_mfma_f32_16x16x32_bf16 v[156:159], a[120:123], a[56:59], v[156:159]\n" \
"buffer_load_dwordx4 v215, %[srdv], s48 offen offset:3072 lds\n" \
"v_mfma_f32_16x16x32_bf16 v[156:159], a[124:127], a[60:63], v[156:159]\n" \
"v_exp_f32_e64 v151, v151\n" \
"v_mfma_f32_16x16x32_bf16 v[112:115], a[176:179], v[204:207], v[112:115]\n" \
"ds_read_b64_tr_b16 a[174:175], v211 offset:12800\n" \
"ds_read_b64_tr_b16 a[176:177], %[vvb] offset:9216\n" \
"v_mfma_f32_16x16x32_bf16 v[112:115], a[144:147], v[220:223], v[112:115]\n" \
"v_exp_f32_e64 v152, v152\n" \
"v_mfma_f32_16x16x32_bf16 v[116:119], a[180:183], v[204:207], v[116:119]\n" \
"v_cvt_pk_bf16_f32 v202, v148, v149\n" \
"v_cvt_pk_bf16_f32 v203, v150, v151\n" \
"v_mfma_f32_16x16x32_bf16 v[116:119], a[148:151], v[220:223], v[116:119]\n" \
"v_exp_f32_e64 v153, v153\n" \
"v_mfma_f32_16x16x32_bf16 v[120:123], a[184:187], v[204:207], v[120:123]\n" \
"v_exp_f32_e64 v154, v154\n" \
"v_mfma_f32_16x16x32_bf16 v[120:123], a[152:155], v[220:223], v[120:123]\n" \
"ds_read_b64_tr_b16 a[178:179], %[vvb] offset:13312\n" \
"v_mfma_f32_16x16x32_bf16 v[124:127], a[188:191], v[204:207], v[124:127]\n" \
"v_exp_f32_e64 v155, v155\n" \
"v_mfma_f32_16x16x32_bf16 v[124:127], a[156:159], v[220:223], v[124:127]\n" \
"ds_read_b64_tr_b16 a[180:181], v211 offset:9216\n" \
"s_waitcnt lgkmcnt(13)\n" \
"s_cmp_lg_u32 s60, 7\n" \
"s_cbranch_scc1 LmB_%=\n" \
"s_nop 0\n" \
"v_readlane_b32 s48, %[tlo], 11\n" \
"v_readlane_b32 s58, %[tlo], 12\n" \
"v_mfma_f32_16x16x32_bf16 v[128:131], a[64:67], a[0:3], v[160:163]\n" \
"v_exp_f32_e32 v156, v156\n" \
"v_lshlrev_b32_e32 v229, 2, v224\n" \
"v_mfma_f32_16x16x32_bf16 v[128:131], a[68:71], a[4:7], v[128:131]\n" \
"ds_read_b64_tr_b16 a[182:183], v211 offset:13312\n" \
"s_add_u32 m0, %[m0b], 0\n" \
"s_mov_b32 s49, s48\n" \
"v_mfma_f32_16x16x32_bf16 v[128:131], a[72:75], a[8:11], v[128:131]\n" \
"v_exp_f32_e64 v157, v157\n" \
"v_and_b32_e32 v229, 0x1800, v229\n" \
"v_mfma_f32_16x16x32_bf16 v[128:131], a[76:79], a[12:15], v[128:131]\n" \
"v_cvt_pk_bf16_f32 v204, v152, v153\n" \
"v_cvt_pk_bf16_f32 v205, v154, v155\n" \
"v_bfe_u32 %[ta0], v224, 11, 1\n" \
"v_mfma_f32_16x16x32_bf16 v[132:135], a[80:83], a[0:3], v[160:163]\n" \
"v_exp_f32_e64 v158, v158\n" \
"v_lshl_or_b32 v229, %[ta0], 15, v229\n" \
"v_lshrrev_b32_e64 %[ta0], 12, v224\n" \
"v_lshl_or_b32 v229, %[ta0], 13, v229\n" \
"v_mfma_f32_16x16x32_bf16 v[132:135], a[84:87], a[4:7], v[132:135]\n" \
"ds_read_b64_tr_b16 a[184:185], %[vvb] offset:9728\n" \
"s_lshl_b32 s49, s49, 18\n" \
"s_or_b32 s49, s49, s58\n" \
"v_and_or_b32 v229, v235, 48, v229\n" \
"s_waitcnt lgkmcnt(5)\n" \
"v_mfma_f32_16x16x32_bf16 v[132:135], a[88:91], a[8:11], v[132:135]\n" \
"buffer_load_dwordx4 %[wo0], %[srdt], s49 offen offset:0 lds\n" \
"v_mfma_f32_16x16x32_bf16 v[132:135], a[92:95], a[12:15], v[132:135]\n" \
"v_exp_f32_e32 v159, v159\n" \
"v_readlane_b32 s51, %[tlo], 31\n" \
"v_mfma_f32_16x16x32_bf16 v[64:67], a[128:131], v[216:219], v[64:67]\n" \
"ds_read_b64_tr_b16 a[186:187], %[vvb] offset:13824\n" \
"ds_read_b64_tr_b16 a[188:189], v211 offset:9728\n" \
"v_readlane_b32 s60, %[thi], 11\n" \
"v_mfma_f32_16x16x32_bf16 v[64:67], a[160:163], v[200:203], v[64:67]\n" \
"v_exp_f32_e32 v128, v128\n" \
"s_lshl_b32 s62, %[wid], 10\n" \
"v_mfma_f32_16x16x32_bf16 v[68:71], a[132:135], v[216:219], v[68:71]\n" \
"v_cvt_pk_bf16_f32 v206, v156, v157\n" \
"v_cvt_pk_bf16_f32 v207, v158, v159\n" \
"s_add_u32 s62, s62, 262144\n" \
"v_mfma_f32_16x16x32_bf16 v[68:71], a[164:167], v[200:203], v[68:71]\n" \
"v_exp_f32_e64 v129, v129\n" \
"s_add_u32 s55, s62, 8192\n" \
"v_mfma_f32_16x16x32_bf16 v[72:75], a[136:139], v[216:219], v[72:75]\n" \
"ds_read_b64_tr_b16 a[190:191], v211 offset:13824\n" \
"ds_read_b64_tr_b16 a[128:129], %[vvb] offset:16384\n" \
"v_readlane_b32 s59, %[thi], 12\n" \
"v_mfma_f32_16x16x32_bf16 v[72:75], a[168:171], v[200:203], v[72:75]\n" \
"v_exp_f32_e64 v130, v130\n" \
"v_readlane_b32 s68, %[tlo], 20\n" \
"v_mfma_f32_16x16x32_bf16 v[76:79], a[140:143], v[216:219], v[76:79]\n" \
"ds_read_b64_tr_b16 a[130:131], %[vvb] offset:20480\n" \
"ds_read_b64_tr_b16 a[132:133], v211 offset:16384\n" \
"v_readlane_b32 s53, %[tlo], 13\n" \
"v_mfma_f32_16x16x32_bf16 v[76:79], a[172:175], v[200:203], v[76:79]\n" \
"v_exp_f32_e64 v131, v131\n" \
"v_readlane_b32 s69, %[thi], 20\n" \
"s_lshl_b32 s63, %[wid], 9\n" \
"v_smfmac_f32_16x16x64_bf16 v[176:179], a[192:195], v[192:199], v228\n" \
"ds_read_b64_tr_b16 a[134:135], v211 offset:20480\n" \
"ds_read_b64_tr_b16 a[136:137], %[vvb] offset:16896\n" \
"v_readlane_b32 s50, %[thi], 13\n" \
"v_mfma_f32_16x16x32_bf16 v[136:139], a[64:67], a[16:19], v[164:167]\n" \
"v_exp_f32_e32 v132, v132\n" \
"v_lshrrev_b32_e32 v225, 4, v224\n" \
"v_add_u32_e32 v225, s63, v225\n" \
"v_mfma_f32_16x16x32_bf16 v[136:139], a[68:71], a[20:23], v[136:139]\n" \
"ds_read_b64_tr_b16 a[138:139], %[vvb] offset:20992\n" \
"ds_read_b64_tr_b16 a[140:141], v211 offset:16896\n" \
"v_readlane_b32 s46, %[tlo], 14\n" \
"v_add_u32_e32 v212, 0x3c00, %[wo0]\n" \
"v_mfma_f32_16x16x32_bf16 v[136:139], a[72:75], a[24:27], v[136:139]\n" \
"v_exp_f32_e64 v133, v133\n" \
"buffer_load_dwordx4 v[230:233], v235, %[srdz], s62 offen sc1\n" \
"v_mfma_f32_16x16x32_bf16 v[136:139], a[76:79], a[28:31], v[136:139]\n" \
"v_cvt_pk_bf16_f32 v184, v128, v129\n" \
"v_cvt_pk_bf16_f32 v185, v130, v131\n" \
"global_load_dwordx2 v[226:227], v225, s[68:69]\n" \
"v_mfma_f32_16x16x32_bf16 v[140:143], a[80:83], a[16:19], v[164:167]\n" \
"v_exp_f32_e64 v134, v134\n" \
"buffer_load_dwordx4 v[234:237], v235, %[srdz], s55 offen sc1\n" \
"v_mfma_f32_16x16x32_bf16 v[140:143], a[84:87], a[20:23], v[140:143]\n" \
"ds_read_b64_tr_b16 a[142:143], v211 offset:20992\n" \
"v_readlane_b32 s47, %[thi], 14\n" \
"v_add_u32_e32 v213, 0x7800, %[wo0]\n" \
"s_waitcnt lgkmcnt(8)\n" \
"v_mfma_f32_16x16x32_bf16 v[140:143], a[88:91], a[24:27], v[140:143]\n" \
"buffer_load_dwordx4 v212, %[srdt], s49 offen offset:1024 lds\n" \
"v_mfma_f32_16x16x32_bf16 v[140:143], a[92:95], a[28:31], v[140:143]\n" \
"v_exp_f32_e32 v135, v135\n" \
"v_mfma_f32_16x16x32_bf16 v[80:83], a[144:147], v[216:219], v[80:83]\n" \
"ds_read_b64_tr_b16 a[144:145], %[vvb] offset:17408\n" \
"ds_read_b64_tr_b16 a[146:147], %[vvb] offset:21504\n" \
"v_mfma_f32_16x16x32_bf16 v[80:83], a[176:179], v[200:203], v[80:83]\n" \
"v_exp_f32_e64 v136, v136\n" \
"v_mfma_f32_16x16x32_bf16 v[84:87], a[148:151], v[216:219], v[84:87]\n" \
"v_cvt_pk_bf16_f32 v186, v132, v133\n" \
"v_cvt_pk_bf16_f32 v187, v134, v135\n" \
"v_mfma_f32_16x16x32_bf16 v[84:87], a[180:183], v[200:203], v[84:87]\n" \
"v_exp_f32_e64 v137, v137\n" \
"v_mfma_f32_16x16x32_bf16 v[88:91], a[152:155], v[216:219], v[88:91]\n" \
"v_exp_f32_e64 v138, v138\n" \
"v_mfma_f32_16x16x32_bf16 v[88:91], a[184:187], v[200:203], v[88:91]\n" \
"ds_read_b64_tr_b16 a[148:149], v211 offset:17408\n" \
"ds_read_b64_tr_b16 a[150:151], v211 offset:21504\n" \
"v_mfma_f32_16x16x32_bf16 v[92:95], a[156:159], v[216:219], v[92:95]\n" \
"v_exp_f32_e64 v139, v139\n" \
"v_mfma_f32_16x16x32_bf16 v[92:95], a[188:191], v[200:203], v[92:95]\n" \
"ds_read_b64_tr_b16 a[152:153], %[vvb] offset:17920\n" \
"buffer_load_dwordx4 a[212:215], v229, %[srdt], s51 offen offset:0\n" \
"v_mfma_f32_16x16x32_bf16 v[144:147], a[64:67], a[32:35], v[168:171]\n" \
"v_exp_f32_e64 v140, v140\n" \
"v_mfma_f32_16x16x32_bf16 v[144:147], a[68:71], a[36:39], v[144:147]\n" \
"ds_read_b64_tr_b16 a[154:155], %[vvb] offset:22016\n" \
"ds_read_b64_tr_b16 a[156:157], v211 offset:17920\n" \
"v_mfma_f32_16x16x32_bf16 v[144:147], a[72:75], a[40:43], v[144:147]\n" \
"v_exp_f32_e64 v141, v141\n" \
"v_mfma_f32_16x16x32_bf16 v[144:147], a[76:79], a[44:47], v[144:147]\n" \
"v_cvt_pk_bf16_f32 v188, v136, v137\n" \
"v_cvt_pk_bf16_f32 v189, v138, v139\n" \
"v_mfma_f32_16x16x32_bf16 v[148:151], a[80:83], a[32:35], v[168:171]\n" \
"v_exp_f32_e64 v142, v142\n" \
"v_mfma_f32_16x16x32_bf16 v[148:151], a[84:87], a[36:39], v[148:151]\n" \
"ds_read_b64_tr_b16 a[158:159], v211 offset:22016\n" \
"s_waitcnt lgkmcnt(8)\n" \
"v_mfma_f32_16x16x32_bf16 v[148:151], a[88:91], a[40:43], v[148:151]\n" \
"buffer_load_dwordx4 v213, %[srdt], s49 offen offset:2048 lds\n" \
"v_mfma_f32_16x16x32_bf16 v[148:151], a[92:95], a[44:47], v[148:151]\n" \
"v_exp_f32_e32 v143, v143\n" \
"v_mfma_f32_16x16x32_bf16 v[0:3], a[160:163], v[192:195], v[0:3]\n" \
"ds_read_b128 a[96:99], %[kb0] offset:24576\n" \
"buffer_load_dwordx4 a[216:219], v229, %[srdt], s51 offen offset:64\n" \
"v_mfma_f32_16x16x32_bf16 v[0:3], a[128:131], v[184:187], v[0:3]\n" \
"v_exp_f32_e64 v144, v144\n" \
"v_mfma_f32_16x16x32_bf16 v[4:7], a[164:167], v[192:195], v[4:7]\n" \
"v_cvt_pk_bf16_f32 v190, v140, v141\n" \
"v_cvt_pk_bf16_f32 v191, v142, v143\n" \
"v_mfma_f32_16x16x32_bf16 v[4:7], a[132:135], v[184:187], v[4:7]\n" \
"v_exp_f32_e64 v145, v145\n" \
"v_mfma_f32_16x16x32_bf16 v[8:11], a[168:171], v[192:195], v[8:11]\n" \
"ds_read_b128 a[100:103], v208 offset:24576\n" \
"buffer_load_dwordx4 a[220:223], v229, %[srdt], s51 offen offset:128\n" \
"v_mfma_f32_16x16x32_bf16 v[8:11], a[136:139], v[184:187], v[8:11]\n" \
"v_exp_f32_e64 v146, v146\n" \
"v_mfma_f32_16x16x32_bf16 v[12:15], a[172:175], v[192:195], v[12:15]\n" \
"ds_read_b128 a[104:107], v209 offset:24576\n" \
"v_add_u32_e32 v214, 0xb400, %[wo0]\n" \
"v_mfma_f32_16x16x32_bf16 v[12:15], a[140:143], v[184:187], v[12:15]\n" \
"v_exp_f32_e64 v147, v147\n" \
"v_smfmac_f32_16x16x64_bf16 v[180:183], a[192:195], v[200:207], v228\n" \
"ds_read_b128 a[108:111], v210 offset:24576\n" \
"buffer_load_dwordx4 a[224:227], v229, %[srdt], s51 offen offset:192\n" \
"v_mfma_f32_16x16x32_bf16 v[152:155], a[64:67], a[48:51], v[172:175]\n" \
"v_exp_f32_e64 v148, v148\n" \
"v_mfma_f32_16x16x32_bf16 v[152:155], a[68:71], a[52:55], v[152:155]\n" \
"ds_read_b128 a[112:115], %[kb0] offset:28672\n" \
"buffer_load_dwordx4 a[228:231], v229, %[srdt], s51 offen offset:256\n" \
"v_mfma_f32_16x16x32_bf16 v[152:155], a[72:75], a[56:59], v[152:155]\n" \
"v_exp_f32_e64 v149, v149\n" \
"v_mfma_f32_16x16x32_bf16 v[152:155], a[76:79], a[60:63], v[152:155]\n" \
"v_cvt_pk_bf16_f32 v216, v144, v145\n" \
"v_cvt_pk_bf16_f32 v217, v146, v147\n" \
"v_mfma_f32_16x16x32_bf16 v[156:159], a[80:83], a[48:51], v[172:175]\n" \
"v_exp_f32_e64 v150, v150\n" \
"v_mfma_f32_16x16x32_bf16 v[156:159], a[84:87], a[52:55], v[156:159]\n" \
"ds_read_b128 a[116:119], v208 offset:28672\n" \
"s_waitcnt lgkmcnt(6)\n" \
"v_mfma_f32_16x16x32_bf16 v[156:159], a[88:91], a[56:59], v[156:159]\n" \
"buffer_load_dwordx4 v214, %[srdt], s49 offen offset:3072 lds\n" \
"v_mfma_f32_16x16x32_bf16 v[156:159], a[92:95], a[60:63], v[156:159]\n" \
"v_exp_f32_e32 v151, v151\n" \
"v_mfma_f32_16x16x32_bf16 v[16:19], a[176:179], v[192:195], v[16:19]\n" \
"ds_read_b128 a[120:123], v209 offset:28672\n" \
"buffer_load_dwordx4 a[232:235], v229, %[srdt], s51 offen offset:320\n" \
"v_mfma_f32_16x16x32_bf16 v[16:19], a[144:147], v[184:187], v[16:19]\n" \
"v_exp_f32_e64 v152, v152\n" \
"buffer_load_dwordx4 a[236:239], v229, %[srdt], s51 offen offset:384\n" \
"v_mfma_f32_16x16x32_bf16 v[20:23], a[180:183], v[192:195], v[20:23]\n" \
"v_cvt_pk_bf16_f32 v218, v148, v149\n" \
"v_cvt_pk_bf16_f32 v219, v150, v151\n" \
"buffer_load_dwordx4 a[196:199], v229, %[srdt], s51 offen offset:448\n" \
"v_mfma_f32_16x16x32_bf16 v[20:23], a[148:151], v[184:187], v[20:23]\n" \
"v_exp_f32_e64 v153, v153\n" \
"v_mfma_f32_16x16x32_bf16 v[24:27], a[184:187], v[192:195], v[24:27]\n" \
"v_exp_f32_e64 v154, v154\n" \
"v_mfma_f32_16x16x32_bf16 v[24:27], a[152:155], v[184:187], v[24:27]\n" \
"ds_read_b128 a[124:127], v210 offset:28672\n" \
"s_waitcnt vmcnt(11)\n" \
"v_bitop3_b32 v225, v230, v231, v232 bitop3:0x80\n" \
"v_mfma_f32_16x16x32_bf16 v[28:31], a[188:191], v[192:195], v[28:31]\n" \
"v_exp_f32_e32 v155, v155\n" \
"v_bitop3_b32 v225, v225, v233, v234 bitop3:0x80\n" \
"v_bitop3_b32 v225, v225, v235, v236 bitop3:0x80\n" \
"v_bitop3_b32 v225, v225, v237, 1 bitop3:0x80\n" \
"v_mfma_f32_16x16x32_bf16 v[28:31], a[156:159], v[184:187], v[28:31]\n" \
"s_waitcnt vmcnt(12) lgkmcnt(2)\n" \
"s_barrier\n" \
".if . - adaln_body != 32768\n" \
".error \"size model\"\n" \
".endif\n" \
"Lk7_%=:\n" \
"s_cbranch_execz Lk8_%=\n" \
"s_nop 0\n" \
"v_mfma_f32_16x16x32_bf16 v[128:131], a[96:99], a[0:3], v[160:163]\n" \
"v_exp_f32_e64 v156, v156\n" \
"v_readlane_b32 s64, %[tlo], 8\n" \
"v_readlane_b32 s65, %[thi], 8\n" \
"v_cmp_eq_u32_e32 vcc, 0, v225\n" \
"v_mfma_f32_16x16x32_bf16 v[128:131], a[100:103], a[4:7], v[128:131]\n" \
"s_cbranch_vccnz Lus_%=\n" \
"Lub_%=:\n" \
"v_mfma_f32_16x16x32_bf16 v[128:131], a[104:107], a[8:11], v[128:131]\n" \
"v_exp_f32_e32 v157, v157\n" \
"v_lshlrev_b32_e32 v225, 16, v226\n" \
"v_mfma_f32_16x16x32_bf16 v[128:131], a[108:111], a[12:15], v[128:131]\n" \
"v_cvt_pk_bf16_f32 v220, v152, v153\n" \
"v_cvt_pk_bf16_f32 v221, v154, v155\n" \
"v_and_b32_e32 v226, 0xffff0000, v226\n" \
"v_mfma_f32_16x16x32_bf16 v[132:135], a[112:115], a[0:3], v[160:163]\n" \
"v_exp_f32_e32 v158, v158\n" \
"v_mul_f32_e32 v234, v234, v225\n" \
"v_mul_f32_e64 v235, v235, v226\n" \
"v_mfma_f32_16x16x32_bf16 v[132:135], a[116:119], a[4:7], v[132:135]\n" \
"v_mul_f32_e32 v230, v230, v234\n" \
"s_waitcnt lgkmcnt(0)\n" \
"v_mfma_f32_16x16x32_bf16 v[132:135], a[120:123], a[8:11], v[132:135]\n" \
"s_add_u32 m0, %[m0b], 65536\n" \
"s_mov_b32 s61, 0\n" \
"buffer_load_dwordx4 %[vg0], s[64:67], s61 offen offset:0 lds\n" \
"v_mfma_f32_16x16x32_bf16 v[132:135], a[124:127], a[12:15], v[132:135]\n" \
"v_exp_f32_e32 v159, v159\n" \
"v_mul_f32_e64 v231, v231, v235\n" \
"v_mfma_f32_16x16x32_bf16 v[32:35], a[160:163], v[196:199], v[32:35]\n" \
"ds_read_b128 a[64:67], %[kb0] offset:32768\n" \
"v_mul_f32_e32 %[ta0], v234, v234\n" \
"v_fmac_f32_e32 %[ta0], v235, v235\n" \
"v_mfma_f32_16x16x32_bf16 v[32:35], a[128:131], v[188:191], v[32:35]\n" \
"v_exp_f32_e64 v128, v128\n" \
"v_cvt_pk_bf16_f32 v230, v230, v231\n" \
"v_mfma_f32_16x16x32_bf16 v[36:39], a[164:167], v[196:199], v[36:39]\n" \
"v_cvt_pk_bf16_f32 v222, v156, v157\n" \
"v_cvt_pk_bf16_f32 v223, v158, v159\n" \
"v_cvt_pk_bf16_f32 v234, v234, v235\n" \
"v_mfma_f32_16x16x32_bf16 v[36:39], a[132:135], v[188:191], v[36:39]\n" \
"v_exp_f32_e64 v129, v129\n" \
"v_mfma_f32_16x16x32_bf16 v[40:43], a[168:171], v[196:199], v[40:43]\n" \
"ds_read_b128 a[68:71], v208 offset:32768\n" \
"v_lshlrev_b32_e64 v225, 16, v227\n" \
"v_mfma_f32_16x16x32_bf16 v[40:43], a[136:139], v[188:191], v[40:43]\n" \
"v_exp_f32_e64 v130, v130\n" \
"v_mfma_f32_16x16x32_bf16 v[44:47], a[172:175], v[196:199], v[44:47]\n" \
"ds_read_b128 a[72:75], v209 offset:32768\n" \
"v_and_b32_e32 v227, 0xffff0000, v227\n" \
"v_mfma_f32_16x16x32_bf16 v[44:47], a[140:143], v[188:191], v[44:47]\n" \
"v_exp_f32_e64 v131, v131\n" \
"v_smfmac_f32_16x16x64_bf16 v[176:179], a[192:195], v[184:191], v228\n" \
"v_mul_f32_e32 v236, v236, v225\n" \
"v_mul_f32_e32 v237, v237, v227\n" \
"v_mfma_f32_16x16x32_bf16 v[136:139], a[96:99], a[16:19], v[164:167]\n" \
"v_exp_f32_e64 v132, v132\n" \
"v_mfma_f32_16x16x32_bf16 v[136:139], a[100:103], a[20:23], v[136:139]\n" \
"ds_read_b128 a[76:79], v210 offset:32768\n" \
"v_mul_f32_e32 v232, v232, v236\n" \
"v_mul_f32_e32 v233, v233, v237\n" \
"v_mfma_f32_16x16x32_bf16 v[136:139], a[104:107], a[24:27], v[136:139]\n" \
"v_exp_f32_e32 v133, v133\n" \
"v_fmac_f32_e32 %[ta0], v236, v236\n" \
"v_mfma_f32_16x16x32_bf16 v[136:139], a[108:111], a[28:31], v[136:139]\n" \
"v_cvt_pk_bf16_f32 v192, v128, v129\n" \
"v_cvt_pk_bf16_f32 v193, v130, v131\n" \
"v_fmac_f32_e32 %[ta0], v237, v237\n" \
"v_mfma_f32_16x16x32_bf16 v[140:143], a[112:115], a[16:19], v[164:167]\n" \
"v_exp_f32_e32 v134, v134\n" \
"v_cvt_pk_bf16_f32 v231, v232, v233\n" \
"v_mfma_f32_16x16x32_bf16 v[140:143], a[116:119], a[20:23], v[140:143]\n" \
"ds_read_b128 a[80:83], %[kb0] offset:36864\n" \
"v_cvt_pk_bf16_f32 v235, v236, v237\n" \
"v_mfma_f32_16x16x32_bf16 v[140:143], a[120:123], a[24:27], v[140:143]\n" \
"s_add_u32 m0, %[m0b], 66560\n" \
"s_mov_b32 s61, 229376\n" \
"buffer_load_dwordx4 %[vg0], s[64:67], s61 offen offset:0 lds\n" \
"v_mfma_f32_16x16x32_bf16 v[140:143], a[124:127], a[28:31], v[140:143]\n" \
"v_exp_f32_e64 v135, v135\n" \
"s_add_u32 s62, %[m0t], 6144\n" \
"v_mfma_f32_16x16x32_bf16 v[48:51], a[176:179], v[196:199], v[48:51]\n" \
"ds_read_b128 a[84:87], v208 offset:36864\n" \
"v_lshrrev_b32_e32 v232, 4, v224\n" \
"v_add_u32_e32 v232, s62, v232\n" \
"v_mfma_f32_16x16x32_bf16 v[48:51], a[144:147], v[188:191], v[48:51]\n" \
"v_exp_f32_e64 v136, v136\n" \
"ds_write_b64 v232, v[230:231] offset:0\n" \
"v_mfma_f32_16x16x32_bf16 v[52:55], a[180:183], v[196:199], v[52:55]\n" \
"v_cvt_pk_bf16_f32 v194, v132, v133\n" \
"v_cvt_pk_bf16_f32 v195, v134, v135\n" \
"v_mov_b64_e32 v[236:237], 0\n" \
"v_mfma_f32_16x16x32_bf16 v[52:55], a[148:151], v[188:191], v[52:55]\n" \
"v_exp_f32_e32 v137, v137\n" \
"ds_write_b64 v232, v[236:237] offset:1024\n" \
"v_lshrrev_b32_e64 v233, 3, v224\n" \
"v_mfma_f32_16x16x32_bf16 v[56:59], a[184:187], v[196:199], v[56:59]\n" \
"v_exp_f32_e64 v138, v138\n" \
"v_and_b32_e32 v232, 0x170, v233\n" \
"v_mfma_f32_16x16x32_bf16 v[56:59], a[152:155], v[188:191], v[56:59]\n" \
"ds_read_b128 a[88:91], v209 offset:36864\n" \
"v_lshrrev_b32_e64 v225, 7, v224\n" \
"v_and_or_b32 v232, v225, 8, v232\n" \
"v_mfma_f32_16x16x32_bf16 v[60:63], a[188:191], v[196:199], v[60:63]\n" \
"v_exp_f32_e64 v139, v139\n" \
"v_mfma_f32_16x16x32_bf16 v[60:63], a[156:159], v[188:191], v[60:63]\n" \
"v_lshrrev_b32_e64 v225, 12, v224\n" \
"v_lshl_or_b32 v232, v225, 7, v232\n" \
"v_mfma_f32_16x16x32_bf16 v[144:147], a[96:99], a[32:35], v[168:171]\n" \
"v_exp_f32_e64 v140, v140\n" \
"v_mfma_f32_16x16x32_bf16 v[144:147], a[100:103], a[36:39], v[144:147]\n" \
"ds_read_b128 a[92:95], v210 offset:36864\n" \
"v_add_u32_e64 v232, s62, v232\n" \
"ds_write_b64 v232, v[234:235] offset:512\n" \
"v_mfma_f32_16x16x32_bf16 v[144:147], a[104:107], a[40:43], v[144:147]\n" \
"v_exp_f32_e64 v141, v141\n" \
"s_mov_b32 s68, 0xf0000f\n" \
"v_mfma_f32_16x16x32_bf16 v[144:147], a[108:111], a[44:47], v[144:147]\n" \
"v_cvt_pk_bf16_f32 v196, v136, v137\n" \
"v_cvt_pk_bf16_f32 v197, v138, v139\n" \
"s_mov_b32 s69, 0xf0000f00\n" \
"v_mfma_f32_16x16x32_bf16 v[148:151], a[112:115], a[32:35], v[168:171]\n" \
"v_exp_f32_e64 v142, v142\n" \
"v_mov_b32_e32 v230, 0x400\n" \
"v_mfma_f32_16x16x32_bf16 v[148:151], a[116:119], a[36:39], v[148:151]\n" \
"v_cndmask_b32_e64 v230, v230, 0, s[68:69]\n" \
"v_and_or_b32 v230, v233, 48, v230\n" \
"v_add_u32_e64 v230, s62, v230\n" \
"v_mfma_f32_16x16x32_bf16 v[148:151], a[120:123], a[40:43], v[148:151]\n" \
"s_add_u32 m0, %[m0b], 67584\n" \
"s_mov_b32 s61, 458752\n" \
"buffer_load_dwordx4 %[vg0], s[64:67], s61 offen offset:0 lds\n" \
"v_mfma_f32_16x16x32_bf16 v[148:151], a[124:127], a[44:47], v[148:151]\n" \
"v_exp_f32_e64 v143, v143\n" \
"v_mfma_f32_16x16x32_bf16 v[96:99], a[160:163], v[204:207], v[96:99]\n" \
"ds_read_b64_tr_b16 a[160:161], %[vvb] offset:24576\n" \
"ds_read_b128 a[200:203], v230 offset:0\n" \
"ds_read_b128 a[204:207], v230 offset:64\n" \
"v_mfma_f32_16x16x32_bf16 v[96:99], a[128:131], v[220:223], v[96:99]\n" \
"v_exp_f32_e64 v144, v144\n" \
"ds_read_b128 a[208:211], v230 offset:128\n" \
"v_mfma_f32_16x16x32_bf16 v[100:103], a[164:167], v[204:207], v[100:103]\n" \
"v_cvt_pk_bf16_f32 v198, v140, v141\n" \
"v_cvt_pk_bf16_f32 v199, v142, v143\n" \
"v_mfma_f32_16x16x32_bf16 v[100:103], a[132:135], v[220:223], v[100:103]\n" \
"v_exp_f32_e64 v145, v145\n" \
"v_mfma_f32_16x16x32_bf16 v[104:107], a[168:171], v[204:207], v[104:107]\n" \
"ds_read_b64_tr_b16 a[162:163], %[vvb] offset:28672\n" \
"ds_read_b64_tr_b16 a[164:165], v211 offset:24576\n" \
"v_mfma_f32_16x16x32_bf16 v[104:107], a[136:139], v[220:223], v[104:107]\n" \
"v_exp_f32_e64 v146, v146\n" \
"v_mfma_f32_16x16x32_bf16 v[108:111], a[172:175], v[204:207], v[108:111]\n" \
"ds_read_b64_tr_b16 a[166:167], v211 offset:28672\n" \
"s_waitcnt vmcnt(12) lgkmcnt(5)\n" \
"v_mfma_f32_16x16x32_bf16 v[234:237], a[212:215], a[200:203], 0\n" \
"ds_read_b128 a[200:203], v230 offset:192\n" \
"v_mfma_f32_16x16x32_bf16 v[108:111], a[140:143], v[220:223], v[108:111]\n" \
"v_exp_f32_e32 v147, v147\n" \
"v_smfmac_f32_16x16x64_bf16 v[180:183], a[192:195], v[216:223], v228\n" \
"ds_read_b64_tr_b16 a[168:169], %[vvb] offset:25088\n" \
"v_mfma_f32_16x16x32_bf16 v[152:155], a[96:99], a[48:51], v[172:175]\n" \
"v_exp_f32_e32 v148, v148\n" \
"s_waitcnt vmcnt(10) lgkmcnt(6)\n" \
"v_mfma_f32_16x16x32_bf16 v[234:237], a[216:219], a[204:207], v[234:237]\n" \
"ds_read_b128 a[204:207], v230 offset:256\n" \
"v_mfma_f32_16x16x32_bf16 v[152:155], a[100:103], a[52:55], v[152:155]\n" \
"ds_read_b64_tr_b16 a[170:171], %[vvb] offset:29184\n" \
"v_mfma_f32_16x16x32_bf16 v[152:155], a[104:107], a[56:59], v[152:155]\n" \
"v_exp_f32_e64 v149, v149\n" \
"v_mfma_f32_16x16x32_bf16 v[152:155], a[108:111], a[60:63], v[152:155]\n" \
"v_cvt_pk_bf16_f32 v200, v144, v145\n" \
"v_cvt_pk_bf16_f32 v201, v146, v147\n" \
"s_waitcnt vmcnt(9) lgkmcnt(7)\n" \
"v_mfma_f32_16x16x32_bf16 v[234:237], a[220:223], a[208:211], v[234:237]\n" \
"ds_read_b128 a[208:211], v230 offset:320\n" \
"v_mfma_f32_16x16x32_bf16 v[156:159], a[112:115], a[48:51], v[172:175]\n" \
"v_exp_f32_e32 v150, v150\n" \
"v_mfma_f32_16x16x32_bf16 v[156:159], a[116:119], a[52:55], v[156:159]\n" \
"ds_read_b64_tr_b16 a[172:173], v211 offset:25088\n" \
"v_mfma_f32_16x16x32_bf16 v[156:159], a[120:123], a[56:59], v[156:159]\n" \
"s_add_u32 m0, %[m0b], 68608\n" \
"s_mov_b32 s61, 688128\n" \
"buffer_load_dwordx4 %[vg0], s[64:67], s61 offen offset:0 lds\n" \
"s_waitcnt vmcnt(9) lgkmcnt(5)\n" \
"v_mfma_f32_16x16x32_bf16 v[234:237], a[224:227], a[200:203], v[234:237]\n" \
"ds_read_b128 a[200:203], v230 offset:384\n" \
"v_mfma_f32_16x16x32_bf16 v[156:159], a[124:127], a[60:63], v[156:159]\n" \
"v_exp_f32_e32 v151, v151\n" \
"v_mfma_f32_16x16x32_bf16 v[112:115], a[176:179], v[204:207], v[112:115]\n" \
"ds_read_b64_tr_b16 a[174:175], v211 offset:29184\n" \
"ds_read_b64_tr_b16 a[176:177], %[vvb] offset:25600\n" \
"v_mfma_f32_16x16x32_bf16 v[112:115], a[144:147], v[220:223], v[112:115]\n" \
"v_exp_f32_e32 v152, v152\n" \
"s_waitcnt vmcnt(8) lgkmcnt(6)\n" \
"v_mfma_f32_16x16x32_bf16 v[234:237], a[228:231], a[204:207], v[234:237]\n" \
"ds_read_b128 a[204:207], v230 offset:448\n" \
"v_mfma_f32_16x16x32_bf16 v[116:119], a[180:183], v[204:207], v[116:119]\n" \
"v_cvt_pk_bf16_f32 v202, v148, v149\n" \
"v_cvt_pk_bf16_f32 v203, v150, v151\n" \
"v_mfma_f32_16x16x32_bf16 v[116:119], a[148:151], v[220:223], v[116:119]\n" \
"v_exp_f32_e64 v153, v153\n" \
"v_mfma_f32_16x16x32_bf16 v[120:123], a[184:187], v[204:207], v[120:123]\n" \
"v_exp_f32_e32 v154, v154\n" \
"s_waitcnt vmcnt(6) lgkmcnt(5)\n" \
"v_mfma_f32_16x16x32_bf16 v[234:237], a[232:235], a[208:211], v[234:237]\n" \
"v_mfma_f32_16x16x32_bf16 v[120:123], a[152:155], v[220:223], v[120:123]\n" \
"ds_read_b64_tr_b16 a[178:179], %[vvb] offset:29696\n" \
"v_mfma_f32_16x16x32_bf16 v[124:127], a[188:191], v[204:207], v[124:127]\n" \
"v_exp_f32_e64 v155, v155\n" \
"v_mfma_f32_16x16x32_bf16 v[124:127], a[156:159], v[220:223], v[124:127]\n" \
"ds_read_b64_tr_b16 a[180:181], v211 offset:25600\n" \
"s_waitcnt lgkmcnt(14)\n" \
"v_mfma_f32_16x16x32_bf16 v[128:131], a[64:67], a[0:3], v[160:163]\n" \
"v_exp_f32_e32 v156, v156\n" \
"s_waitcnt vmcnt(5) lgkmcnt(5)\n" \
"v_mfma_f32_16x16x32_bf16 v[234:237], a[236:239], a[200:203], v[234:237]\n" \
"v_mfma_f32_16x16x32_bf16 v[128:131], a[68:71], a[4:7], v[128:131]\n" \
"ds_read_b64_tr_b16 a[182:183], v211 offset:29696\n" \
"s_add_u32 m0, %[m0b], 16384\n" \
"s_mov_b32 s49, s48\n" \
"v_mbcnt_lo_u32_b32 v231, -1, 0\n" \
"v_mfma_f32_16x16x32_bf16 v[128:131], a[72:75], a[8:11], v[128:131]\n" \
"v_exp_f32_e64 v157, v157\n" \
"v_mfma_f32_16x16x32_bf16 v[128:131], a[76:79], a[12:15], v[128:131]\n" \
"v_cvt_pk_bf16_f32 v204, v152, v153\n" \
"v_cvt_pk_bf16_f32 v205, v154, v155\n" \
"s_waitcnt vmcnt(4) lgkmcnt(3)\n" \
"v_mfma_f32_16x16x32_bf16 v[234:237], a[196:199], a[204:207], v[234:237]\n" \
"v_mfma_f32_16x16x32_bf16 v[132:135], a[80:83], a[0:3], v[160:163]\n" \
"v_exp_f32_e32 v158, v158\n" \
"v_mfma_f32_16x16x32_bf16 v[132:135], a[84:87], a[4:7], v[132:135]\n" \
"ds_read_b64_tr_b16 a[184:185], %[vvb] offset:26112\n" \
"s_lshl_b32 s49, s49, 18\n" \
"s_or_b32 s49, s49, s59\n" \
"v_mbcnt_hi_u32_b32 v231, -1, v231\n" \
"s_waitcnt lgkmcnt(6)\n" \
"v_mfma_f32_16x16x32_bf16 v[132:135], a[88:91], a[8:11], v[132:135]\n" \
"buffer_load_dwordx4 %[wo0], %[srdt], s49 offen offset:0 lds\n" \
"v_mfma_f32_16x16x32_bf16 v[132:135], a[92:95], a[12:15], v[132:135]\n" \
"v_exp_f32_e32 v159, v159\n" \
"v_mfma_f32_16x16x32_bf16 v[64:67], a[128:131], v[216:219], v[64:67]\n" \
"ds_read_b64_tr_b16 a[186:187], %[vvb] offset:30208\n" \
"ds_read_b64_tr_b16 a[188:189], v211 offset:26112\n" \
"v_xor_b32_e32 v229, 0x80, %[vx0]\n" \
"v_and_b32_e64 v226, 31, v231\n" \
"v_mfma_f32_16x16x32_bf16 v[64:67], a[160:163], v[200:203], v[64:67]\n" \
"v_exp_f32_e64 v128, v128\n" \
"v_mfma_f32_16x16x32_bf16 v[68:71], a[132:135], v[216:219], v[68:71]\n" \
"v_cvt_pk_bf16_f32 v206, v156, v157\n" \
"v_cvt_pk_bf16_f32 v207, v158, v159\n" \
"v_mfma_f32_16x16x32_bf16 v[68:71], a[164:167], v[200:203], v[68:71]\n" \
"v_exp_f32_e64 v129, v129\n" \
"v_mfma_f32_16x16x32_bf16 v[72:75], a[136:139], v[216:219], v[72:75]\n" \
"ds_read_b64_tr_b16 a[190:191], v211 offset:30208\n" \
"ds_read_b64_tr_b16 a[128:129], %[vvb] offset:32768\n" \
"v_lshlrev_b32_e64 v226, 2, v226\n" \
"v_mfma_f32_16x16x32_bf16 v[72:75], a[168:171], v[200:203], v[72:75]\n" \
"v_exp_f32_e64 v130, v130\n" \
"s_mov_b32 s68, 0xaaaaaaaa\n" \
"v_mfma_f32_16x16x32_bf16 v[76:79], a[140:143], v[216:219], v[76:79]\n" \
"ds_read_b64_tr_b16 a[130:131], %[vvb] offset:36864\n" \
"ds_read_b64_tr_b16 a[132:133], v211 offset:32768\n" \
"v_cmp_gt_u32_e32 vcc, 1, v231\n" \
"v_mfma_f32_16x16x32_bf16 v[76:79], a[172:175], v[200:203], v[76:79]\n" \
"v_exp_f32_e32 v131, v131\n" \
"s_mov_b32 s69, 0xaaaaaaaa\n" \
"v_smfmac_f32_16x16x64_bf16 v[176:179], a[192:195], v[192:199], v228\n" \
"ds_read_b64_tr_b16 a[134:135], v211 offset:36864\n" \
"ds_read_b64_tr_b16 a[136:137], %[vvb] offset:33280\n" \
"global_load_dwordx2 a[240:241], %[vx0], %[px] offset:0\n" \
"v_mov_b32_e32 v225, 0x80000000\n" \
"v_mfma_f32_16x16x32_bf16 v[136:139], a[64:67], a[16:19], v[164:167]\n" \
"v_exp_f32_e64 v132, v132\n" \
"v_cndmask_b32_e64 v234, v234, v235, s[68:69]\n" \
"v_mfma_f32_16x16x32_bf16 v[136:139], a[68:71], a[20:23], v[136:139]\n" \
"ds_read_b64_tr_b16 a[138:139], %[vvb] offset:37376\n" \
"ds_read_b64_tr_b16 a[140:141], v211 offset:33280\n" \
"v_cndmask_b32_e64 v236, v236, v237, s[68:69]\n" \
"v_mfma_f32_16x16x32_bf16 v[136:139], a[72:75], a[24:27], v[136:139]\n" \
"v_exp_f32_e64 v133, v133\n" \
"s_mov_b32 s68, 0xcccccccc\n" \
"v_mfma_f32_16x16x32_bf16 v[136:139], a[76:79], a[28:31], v[136:139]\n" \
"v_cvt_pk_bf16_f32 v184, v128, v129\n" \
"v_cvt_pk_bf16_f32 v185, v130, v131\n" \
"s_mov_b32 s69, 0xcccccccc\n" \
"v_mfma_f32_16x16x32_bf16 v[140:143], a[80:83], a[16:19], v[164:167]\n" \
"v_exp_f32_e64 v134, v134\n" \
"v_cndmask_b32_e64 v234, v234, v236, s[68:69]\n" \
"v_mfma_f32_16x16x32_bf16 v[140:143], a[84:87], a[20:23], v[140:143]\n" \
"ds_read_b64_tr_b16 a[142:143], v211 offset:37376\n" \
"v_cndmask_b32_e32 v225, v225, v224, vcc\n" \
"s_waitcnt lgkmcnt(8)\n" \
"v_mfma_f32_16x16x32_bf16 v[140:143], a[88:91], a[24:27], v[140:143]\n" \
"buffer_load_dwordx4 v212, %[srdt], s49 offen offset:1024 lds\n" \
"v_mfma_f32_16x16x32_bf16 v[140:143], a[92:95], a[28:31], v[140:143]\n" \
"v_exp_f32_e32 v135, v135\n" \
"v_lshrrev_b32_e32 v235, 3, v224\n" \
"v_mfma_f32_16x16x32_bf16 v[80:83], a[144:147], v[216:219], v[80:83]\n" \
"ds_read_b64_tr_b16 a[144:145], %[vvb] offset:33792\n" \
"ds_read_b64_tr_b16 a[146:147], %[vvb] offset:37888\n" \
"v_and_b32_e32 v236, 0xc0, v235\n" \
"v_mfma_f32_16x16x32_bf16 v[80:83], a[176:179], v[200:203], v[80:83]\n" \
"v_exp_f32_e64 v136, v136\n" \
"v_add_f32_dpp v234, v234, v234 quad_perm:[1,0,3,2] row_mask:0xf bank_mask:0xf\n" \
"v_mfma_f32_16x16x32_bf16 v[84:87], a[148:151], v[216:219], v[84:87]\n" \
"v_cvt_pk_bf16_f32 v186, v132, v133\n" \
"v_cvt_pk_bf16_f32 v187, v134, v135\n" \
"v_and_or_b32 v236, v231, 48, v236\n" \
"v_mfma_f32_16x16x32_bf16 v[84:87], a[180:183], v[200:203], v[84:87]\n" \
"v_exp_f32_e64 v137, v137\n" \
"s_mul_i32 s62, %[wid], 8188\n" \
"v_mfma_f32_16x16x32_bf16 v[88:91], a[152:155], v[216:219], v[88:91]\n" \
"v_exp_f32_e32 v138, v138\n" \
"s_sub_u32 s62, %[m0t], s62\n" \
"v_mfma_f32_16x16x32_bf16 v[88:91], a[184:187], v[200:203], v[88:91]\n" \
"ds_read_b64_tr_b16 a[148:149], v211 offset:33792\n" \
"ds_read_b64_tr_b16 a[150:151], v211 offset:37888\n" \
"v_add_f32_dpp v234, v234, v234 quad_perm:[2,3,0,1] row_mask:0xf bank_mask:0xf\n" \
"v_mfma_f32_16x16x32_bf16 v[92:95], a[156:159], v[216:219], v[92:95]\n" \
"v_exp_f32_e32 v139, v139\n" \
"v_add_u32_e32 v236, s62, v236\n" \
"v_mfma_f32_16x16x32_bf16 v[92:95], a[188:191], v[200:203], v[92:95]\n" \
"ds_read_b64_tr_b16 a[152:153], %[vvb] offset:34304\n" \
"global_load_dwordx2 a[244:245], %[vx0], %[px] offset:1024\n" \
"v_mfma_f32_16x16x32_bf16 v[144:147], a[64:67], a[32:35], v[168:171]\n" \
"v_exp_f32_e64 v140, v140\n" \
"ds_write_b32 v236, v234 offset:7680\n" \
"v_mfma_f32_16x16x32_bf16 v[144:147], a[68:71], a[36:39], v[144:147]\n" \
"ds_read_b64_tr_b16 a[154:155], %[vvb] offset:38400\n" \
"ds_read_b64_tr_b16 a[156:157], v211 offset:34304\n" \
"s_sub_u32 s61, s48, 0\n" \
"s_cmp_lt_u32 s61, 4\n" \
"s_cselect_b64 vcc, -1, 0\n" \
"v_mfma_f32_16x16x32_bf16 v[144:147], a[72:75], a[40:43], v[144:147]\n" \
"v_exp_f32_e32 v141, v141\n" \
"v_add_f32_dpp %[ta0], %[ta0], %[ta0] quad_perm:[1,0,3,2] row_mask:0xf bank_mask:0xf\n" \
"v_mfma_f32_16x16x32_bf16 v[144:147], a[76:79], a[44:47], v[144:147]\n" \
"v_cvt_pk_bf16_f32 v188, v136, v137\n" \
"v_cvt_pk_bf16_f32 v189, v138, v139\n" \
"v_add_f32_dpp %[ta0], %[ta0], %[ta0] quad_perm:[2,3,0,1] row_mask:0xf bank_mask:0xf\n" \
"v_mfma_f32_16x16x32_bf16 v[148:151], a[80:83], a[32:35], v[168:171]\n" \
"v_exp_f32_e64 v142, v142\n" \
"v_add_f32_dpp %[ta0], %[ta0], %[ta0] row_ror:4 row_mask:0xf bank_mask:0xf\n" \
"v_mfma_f32_16x16x32_bf16 v[148:151], a[84:87], a[36:39], v[148:151]\n" \
"ds_read_b64_tr_b16 a[158:159], v211 offset:38400\n" \
"v_readlane_b32 s55, %[tlo], 16\n" \
"s_waitcnt lgkmcnt(9)\n" \
"v_mfma_f32_16x16x32_bf16 v[148:151], a[88:91], a[40:43], v[148:151]\n" \
"buffer_load_dwordx4 v213, %[srdt], s49 offen offset:2048 lds\n" \
"v_mfma_f32_16x16x32_bf16 v[148:151], a[92:95], a[44:47], v[148:151]\n" \
"v_exp_f32_e32 v143, v143\n" \
"v_add_f32_dpp %[ta0], %[ta0], %[ta0] row_ror:8 row_mask:0xf bank_mask:0xf\n" \
"v_mfma_f32_16x16x32_bf16 v[0:3], a[160:163], v[192:195], v[0:3]\n" \
"ds_read_b128 a[96:99], %[kb0] offset:40960\n" \
"v_cmp_gt_u32_e64 s[56:57], 32, v231\n" \
"v_mov_b32_e64 v237, %[ta0]\n" \
"v_mfma_f32_16x16x32_bf16 v[0:3], a[128:131], v[184:187], v[0:3]\n" \
"v_exp_f32_e32 v144, v144\n" \
"v_permlane16_swap_b32_e32 %[ta0], v237\n" \
"v_mfma_f32_16x16x32_bf16 v[4:7], a[164:167], v[192:195], v[4:7]\n" \
"v_cvt_pk_bf16_f32 v190, v140, v141\n" \
"v_cvt_pk_bf16_f32 v191, v142, v143\n" \
"v_add_f32_e64 %[ta0], %[ta0], v237\n" \
"v_mfma_f32_16x16x32_bf16 v[4:7], a[132:135], v[184:187], v[4:7]\n" \
"v_exp_f32_e32 v145, v145\n" \
"v_mov_b32_e32 v237, %[ta0]\n" \
"v_mfma_f32_16x16x32_bf16 v[8:11], a[168:171], v[192:195], v[8:11]\n" \
"ds_read_b128 a[100:103], v208 offset:40960\n" \
"v_lshlrev_b32_e32 v232, 2, v224\n" \
"v_permlane32_swap_b32_e32 %[ta0], v237\n" \
"v_mfma_f32_16x16x32_bf16 v[8:11], a[136:139], v[184:187], v[8:11]\n" \
"v_exp_f32_e32 v146, v146\n" \
"v_add_f32_e32 %[ta0], %[ta0], v237\n" \
"v_mfma_f32_16x16x32_bf16 v[12:15], a[172:175], v[192:195], v[12:15]\n" \
"ds_read_b128 a[104:107], v209 offset:40960\n" \
"v_mov_b32_e32 v233, 0x80000000\n" \
"v_mov_b32_e64 v237, s62\n" \
"v_mfma_f32_16x16x32_bf16 v[12:15], a[140:143], v[184:187], v[12:15]\n" \
"v_exp_f32_e64 v147, v147\n" \
"ds_write_b32 v237, %[ta0] offset:7936\n" \
"v_smfmac_f32_16x16x64_bf16 v[180:183], a[192:195], v[200:207], v228\n" \
"ds_read_b128 a[108:111], v210 offset:40960\n" \
"global_load_dwordx2 a[248:249], v229, %[px] offset:0\n" \
"v_mfma_f32_16x16x32_bf16 v[152:155], a[64:67], a[48:51], v[172:175]\n" \
"v_exp_f32_e32 v148, v148\n" \
"s_lshl_b32 s62, %[wid], 13\n" \
"v_mfma_f32_16x16x32_bf16 v[152:155], a[68:71], a[52:55], v[152:155]\n" \
"ds_read_b128 a[112:115], %[kb0] offset:45056\n" \
"v_cndmask_b32_e64 v232, v233, v232, s[56:57]\n" \
"s_sub_u32 s62, %[m0t], s62\n" \
"v_mfma_f32_16x16x32_bf16 v[152:155], a[72:75], a[56:59], v[152:155]\n" \
"v_exp_f32_e32 v149, v149\n" \
"v_mfma_f32_16x16x32_bf16 v[152:155], a[76:79], a[60:63], v[152:155]\n" \
"v_cvt_pk_bf16_f32 v216, v144, v145\n" \
"v_cvt_pk_bf16_f32 v217, v146, v147\n" \
"v_mfma_f32_16x16x32_bf16 v[156:159], a[80:83], a[48:51], v[172:175]\n" \
"v_exp_f32_e64 v150, v150\n" \
"v_mfma_f32_16x16x32_bf16 v[156:159], a[84:87], a[52:55], v[156:159]\n" \
"ds_read_b128 a[116:119], v208 offset:45056\n" \
"s_waitcnt lgkmcnt(7)\n" \
"v_mfma_f32_16x16x32_bf16 v[156:159], a[88:91], a[56:59], v[156:159]\n" \
"buffer_load_dwordx4 v214, %[srdt], s49 offen offset:3072 lds\n" \
"v_mfma_f32_16x16x32_bf16 v[156:159], a[92:95], a[60:63], v[156:159]\n" \
"v_exp_f32_e32 v151, v151\n" \
"v_mfma_f32_16x16x32_bf16 v[16:19], a[176:179], v[192:195], v[16:19]\n" \
"ds_read_b128 a[120:123], v209 offset:45056\n" \
"v_and_b32_e32 v230, 0xf0, v235\n" \
"v_add_u32_e64 v230, s62, v230\n" \
"v_mfma_f32_16x16x32_bf16 v[16:19], a[144:147], v[184:187], v[16:19]\n" \
"v_exp_f32_e64 v152, v152\n" \
"v_readlane_b32 s51, %[thi], 31\n" \
"v_mfma_f32_16x16x32_bf16 v[20:23], a[180:183], v[192:195], v[20:23]\n" \
"v_cvt_pk_bf16_f32 v218, v148, v149\n" \
"v_cvt_pk_bf16_f32 v219, v150, v151\n" \
"s_cmp_eq_u32 %[wid], 0\n" \
"s_cselect_b32 s51, s51, 0x80000000\n" \
"v_mfma_f32_16x16x32_bf16 v[20:23], a[148:151], v[184:187], v[20:23]\n" \
"v_exp_f32_e32 v153, v153\n" \
"v_mfma_f32_16x16x32_bf16 v[24:27], a[184:187], v[192:195], v[24:27]\n" \
"v_exp_f32_e64 v154, v154\n" \
"v_mfma_f32_16x16x32_bf16 v[24:27], a[152:155], v[184:187], v[24:27]\n" \
"ds_read_b128 a[124:127], v210 offset:45056\n" \
"buffer_store_dword v233, v232, %[srdz], s55 offen sc1\n" \
"global_load_dwordx2 a[252:253], v229, %[px] offset:1024\n" \
"v_mfma_f32_16x16x32_bf16 v[28:31], a[188:191], v[192:195], v[28:31]\n" \
"v_exp_f32_e64 v155, v155\n" \
"v_mfma_f32_16x16x32_bf16 v[28:31], a[156:159], v[184:187], v[28:31]\n" \
"s_waitcnt vmcnt(63) lgkmcnt(2)\n" \
"s_barrier\n" \
"v_mfma_f32_16x16x32_bf16 v[128:131], a[96:99], a[0:3], v[160:163]\n" \
"v_exp_f32_e64 v156, v156\n" \
"ds_read_b128 v[232:235], v230 offset:7680\n" \
"v_mfma_f32_16x16x32_bf16 v[128:131], a[100:103], a[4:7], v[128:131]\n" \
"v_lshrrev_b32_e64 v227, 5, v231\n" \
"v_mfma_f32_16x16x32_bf16 v[128:131], a[104:107], a[8:11], v[128:131]\n" \
"v_exp_f32_e32 v157, v157\n" \
"v_lshrrev_b32_e32 v230, 6, v224\n" \
"v_mfma_f32_16x16x32_bf16 v[128:131], a[108:111], a[12:15], v[128:131]\n" \
"v_cvt_pk_bf16_f32 v220, v152, v153\n" \
"v_cvt_pk_bf16_f32 v221, v154, v155\n" \
"v_mfma_f32_16x16x32_bf16 v[132:135], a[112:115], a[0:3], v[160:163]\n" \
"v_exp_f32_e64 v158, v158\n" \
"v_mfma_f32_16x16x32_bf16 v[132:135], a[116:119], a[4:7], v[132:135]\n" \
"v_lshlrev_b32_e32 v227, 10, v227\n" \
"s_waitcnt lgkmcnt(1)\n" \
"v_mfma_f32_16x16x32_bf16 v[132:135], a[120:123], a[8:11], v[132:135]\n" \
"s_add_u32 m0, %[m0b], 81920\n" \
"s_mov_b32 s61, 917504\n" \
"buffer_load_dwordx4 %[vg0], s[64:67], s61 offen offset:0 lds\n" \
"v_mfma_f32_16x16x32_bf16 v[132:135], a[124:127], a[12:15], v[132:135]\n" \
"v_exp_f32_e32 v159, v159\n" \
"s_waitcnt lgkmcnt(0)\n" \
"v_add_f32_e64 v232, v232, v233\n" \
"v_mfma_f32_16x16x32_bf16 v[32:35], a[160:163], v[196:199], v[32:35]\n" \
"ds_read_b128 a[64:67], %[kb0] offset:49152\n" \
"v_add_u32_e32 v227, 0x10000, v227\n" \
"v_add_f32_e64 v234, v234, v235\n" \
"v_mfma_f32_16x16x32_bf16 v[32:35], a[128:131], v[188:191], v[32:35]\n" \
"v_exp_f32_e32 v128, v128\n" \
"v_add_f32_e32 v232, v232, v234\n" \
"v_mfma_f32_16x16x32_bf16 v[36:39], a[164:167], v[196:199], v[36:39]\n" \
"v_cvt_pk_bf16_f32 v222, v156, v157\n" \
"v_cvt_pk_bf16_f32 v223, v158, v159\n" \
"v_mov_b32_dpp v233, v232 quad_perm:[1,0,3,2] row_mask:0xf bank_mask:0xf\n" \
"v_mfma_f32_16x16x32_bf16 v[36:39], a[132:135], v[188:191], v[36:39]\n" \
"v_exp_f32_e64 v129, v129\n" \
"v_cvt_pk_bf16_f32 v232, v232, v233\n" \
"v_mfma_f32_16x16x32_bf16 v[40:43], a[168:171], v[196:199], v[40:43]\n" \
"ds_read_b128 a[68:71], v208 offset:49152\n" \
"v_sub_u32_e64 v215, %[vvb], v227\n" \
"s_mov_b64 exec, 0x5555\n" \
"buffer_store_dword v232, v230, %[srdz], s51 offen sc1\n" \
"s_mov_b64 exec, -1\n" \
"v_mfma_f32_16x16x32_bf16 v[40:43], a[136:139], v[188:191], v[40:43]\n" \
"v_exp_f32_e32 v130, v130\n" \
"v_mov_b32_e64 v237, s62\n" \
"v_mfma_f32_16x16x32_bf16 v[44:47], a[172:175], v[196:199], v[44:47]\n" \
"ds_read_b128 a[72:75], v209 offset:49152\n" \
"v_xor_b32_e64 v227, 32, v215\n" \
"ds_read_b128 v[232:235], v237 offset:7936\n" \
"v_mfma_f32_16x16x32_bf16 v[44:47], a[140:143], v[188:191], v[44:47]\n" \
"v_exp_f32_e64 v131, v131\n" \
"v_smfmac_f32_16x16x64_bf16 v[176:179], a[192:195], v[184:191], v228\n" \
"global_load_dwordx2 a[242:243], %[vx0], %[px] offset:64\n" \
"v_mfma_f32_16x16x32_bf16 v[136:139], a[96:99], a[16:19], v[164:167]\n" \
"v_exp_f32_e64 v132, v132\n" \
"v_mfma_f32_16x16x32_bf16 v[136:139], a[100:103], a[20:23], v[136:139]\n" \
"ds_read_b128 a[76:79], v210 offset:49152\n" \
"v_mfma_f32_16x16x32_bf16 v[136:139], a[104:107], a[24:27], v[136:139]\n" \
"v_exp_f32_e32 v133, v133\n" \
"s_waitcnt lgkmcnt(1)\n" \
"v_add_f32_e64 v232, v232, v233\n" \
"v_mfma_f32_16x16x32_bf16 v[136:139], a[108:111], a[28:31], v[136:139]\n" \
"v_cvt_pk_bf16_f32 v192, v128, v129\n" \
"v_cvt_pk_bf16_f32 v193, v130, v131\n" \
"v_add_f32_e64 v234, v234, v235\n" \
"v_mfma_f32_16x16x32_bf16 v[140:143], a[112:115], a[16:19], v[164:167]\n" \
"v_exp_f32_e64 v134, v134\n" \
"v_mfma_f32_16x16x32_bf16 v[140:143], a[116:119], a[20:23], v[140:143]\n" \
"ds_read_b128 a[80:83], %[kb0] offset:53248\n" \
"v_mfma_f32_16x16x32_bf16 v[140:143], a[120:123], a[24:27], v[140:143]\n" \
"s_add_u32 m0, %[m0b], 82944\n" \
"s_mov_b32 s61, 1146880\n" \
"buffer_load_dwordx4 %[vg0], s[64:67], s61 offen offset:0 lds\n" \
"v_mfma_f32_16x16x32_bf16 v[140:143], a[124:127], a[28:31], v[140:143]\n" \
"v_exp_f32_e64 v135, v135\n" \
"v_mfma_f32_16x16x32_bf16 v[48:51], a[176:179], v[196:199], v[48:51]\n" \
"ds_read_b128 a[84:87], v208 offset:53248\n" \
"v_add_f32_e64 v232, v232, v234\n" \
"v_mul_f32_e32 v232, 0x43000000, v232\n" \
"v_mfma_f32_16x16x32_bf16 v[48:51], a[144:147], v[188:191], v[48:51]\n" \
"v_exp_f32_e32 v136, v136\n" \
"v_rcp_f32_e32 v232, v232\n" \
"v_mfma_f32_16x16x32_bf16 v[52:55], a[180:183], v[196:199], v[52:55]\n" \
"v_cvt_pk_bf16_f32 v194, v132, v133\n" \
"v_cvt_pk_bf16_f32 v195, v134, v135\n" \
"v_readfirstlane_b32 s63, v232\n" \
"v_mfma_f32_16x16x32_bf16 v[52:55], a[148:151], v[188:191], v[52:55]\n" \
"v_exp_f32_e32 v137, v137\n" \
"v_mfma_f32_16x16x32_bf16 v[56:59], a[184:187], v[196:199], v[56:59]\n" \
"v_exp_f32_e64 v138, v138\n" \
"v_mfma_f32_16x16x32_bf16 v[56:59], a[152:155], v[188:191], v[56:59]\n" \
"ds_read_b128 a[88:91], v209 offset:53248\n" \
"v_lshrrev_b32_e64 v236, 3, v224\n" \
"v_mfma_f32_16x16x32_bf16 v[60:63], a[188:191], v[196:199], v[60:63]\n" \
"v_exp_f32_e64 v139, v139\n" \
"v_mfma_f32_16x16x32_bf16 v[60:63], a[156:159], v[188:191], v[60:63]\n" \
"global_load_dwordx2 a[246:247], %[vx0], %[px] offset:1088\n" \
"v_mfma_f32_16x16x32_bf16 v[144:147], a[96:99], a[32:35], v[168:171]\n" \
"v_exp_f32_e64 v140, v140\n" \
"v_mfma_f32_16x16x32_bf16 v[144:147], a[100:103], a[36:39], v[144:147]\n" \
"ds_read_b128 a[92:95], v210 offset:53248\n" \
"v_and_b32_e32 v230, 0xb0, v236\n" \
"v_mfma_f32_16x16x32_bf16 v[144:147], a[104:107], a[40:43], v[144:147]\n" \
"v_exp_f32_e64 v141, v141\n" \
"v_mfma_f32_16x16x32_bf16 v[144:147], a[108:111], a[44:47], v[144:147]\n" \
"v_cvt_pk_bf16_f32 v196, v136, v137\n" \
"v_cvt_pk_bf16_f32 v197, v138, v139\n" \
"v_mfma_f32_16x16x32_bf16 v[148:151], a[112:115], a[32:35], v[168:171]\n" \
"v_exp_f32_e64 v142, v142\n" \
"v_mfma_f32_16x16x32_bf16 v[148:151], a[116:119], a[36:39], v[148:151]\n" \
"v_lshrrev_b32_e64 v236, 2, v236\n" \
"v_mfma_f32_16x16x32_bf16 v[148:151], a[120:123], a[40:43], v[148:151]\n" \
"s_add_u32 m0, %[m0b], 83968\n" \
"s_mov_b32 s61, 1376256\n" \
"buffer_load_dwordx4 %[vg0], s[64:67], s61 offen offset:0 lds\n" \
"v_mfma_f32_16x16x32_bf16 v[148:151], a[124:127], a[44:47], v[148:151]\n" \
"v_exp_f32_e64 v143, v143\n" \
"v_mfma_f32_16x16x32_bf16 v[96:99], a[160:163], v[204:207], v[96:99]\n" \
"ds_read_b64_tr_b16 a[160:161], %[vvb] offset:40960\n" \
"v_and_or_b32 v230, v236, 64, v230\n" \
"v_mfma_f32_16x16x32_bf16 v[96:99], a[128:131], v[220:223], v[96:99]\n" \
"v_exp_f32_e64 v144, v144\n" \
"v_mfma_f32_16x16x32_bf16 v[100:103], a[164:167], v[204:207], v[100:103]\n" \
"v_cvt_pk_bf16_f32 v198, v140, v141\n" \
"v_cvt_pk_bf16_f32 v199, v142, v143\n" \
"v_mfma_f32_16x16x32_bf16 v[100:103], a[132:135], v[220:223], v[100:103]\n" \
"v_exp_f32_e64 v145, v145\n" \
"v_mfma_f32_16x16x32_bf16 v[104:107], a[168:171], v[204:207], v[104:107]\n" \
"ds_read_b64_tr_b16 a[162:163], %[vvb] offset:45056\n" \
".if . - adaln_body != 36864\n" \
".error \"size model\"\n" \
".endif\n" \
"Lk8_%=:\n" \
"s_cbranch_execz Lk9_%=\n" \
"s_nop 0\n" \
"ds_read_b64_tr_b16 a[164:165], v211 offset:40960\n" \
"v_mfma_f32_16x16x32_bf16 v[104:107], a[136:139], v[220:223], v[104:107]\n" \
"v_exp_f32_e64 v146, v146\n" \
"v_mfma_f32_16x16x32_bf16 v[108:111], a[172:175], v[204:207], v[108:111]\n" \
"ds_read_b64_tr_b16 a[166:167], v211 offset:45056\n" \
"v_lshrrev_b32_e64 v236, 12, v224\n" \
"v_mfma_f32_16x16x32_bf16 v[108:111], a[140:143], v[220:223], v[108:111]\n" \
"v_exp_f32_e64 v147, v147\n" \
"v_smfmac_f32_16x16x64_bf16 v[180:183], a[192:195], v[216:223], v228\n" \
"ds_read_b64_tr_b16 a[168:169], %[vvb] offset:41472\n" \
"global_load_dwordx2 a[250:251], v229, %[px] offset:64\n" \
"v_mfma_f32_16x16x32_bf16 v[152:155], a[96:99], a[48:51], v[172:175]\n" \
"v_exp_f32_e64 v148, v148\n" \
"v_mfma_f32_16x16x32_bf16 v[152:155], a[100:103], a[52:55], v[152:155]\n" \
"ds_read_b64_tr_b16 a[170:171], %[vvb] offset:45568\n" \
"v_lshl_or_b32 v230, v236, 13, v230\n" \
"v_mfma_f32_16x16x32_bf16 v[152:155], a[104:107], a[56:59], v[152:155]\n" \
"v_exp_f32_e64 v149, v149\n" \
"v_readlane_b32 s51, %[thi], 26\n" \
"v_mfma_f32_16x16x32_bf16 v[152:155], a[108:111], a[60:63], v[152:155]\n" \
"v_cvt_pk_bf16_f32 v200, v144, v145\n" \
"v_cvt_pk_bf16_f32 v201, v146, v147\n" \
"v_add_u32_e64 v230, s62, v230\n" \
"v_mfma_f32_16x16x32_bf16 v[156:159], a[112:115], a[48:51], v[172:175]\n" \
"v_exp_f32_e32 v150, v150\n" \
"v_add_u32_e32 v230, s51, v230\n" \
"v_mfma_f32_16x16x32_bf16 v[156:159], a[116:119], a[52:55], v[156:159]\n" \
"ds_read_b64_tr_b16 a[172:173], v211 offset:41472\n" \
"v_xor_b32_e32 v236, 0x100, v230\n" \
"v_mfma_f32_16x16x32_bf16 v[156:159], a[120:123], a[56:59], v[156:159]\n" \
"s_add_u32 m0, %[m0b], 84992\n" \
"s_mov_b32 s61, 1605632\n" \
"buffer_load_dwordx4 %[vg0], s[64:67], s61 offen offset:0 lds\n" \
"v_mfma_f32_16x16x32_bf16 v[156:159], a[124:127], a[60:63], v[156:159]\n" \
"v_exp_f32_e64 v151, v151\n" \
"ds_read_b128 a[212:215], v230 offset:6656\n" \
"v_mfma_f32_16x16x32_bf16 v[112:115], a[176:179], v[204:207], v[112:115]\n" \
"ds_read_b64_tr_b16 a[174:175], v211 offset:45568\n" \
"ds_read_b64_tr_b16 a[176:177], %[vvb] offset:41984\n" \
"ds_read_b128 a[216:219], v230 offset:23040\n" \
"v_mfma_f32_16x16x32_bf16 v[112:115], a[144:147], v[220:223], v[112:115]\n" \
"v_exp_f32_e64 v152, v152\n" \
"ds_read_b128 a[220:223], v236 offset:6656\n" \
"v_mfma_f32_16x16x32_bf16 v[116:119], a[180:183], v[204:207], v[116:119]\n" \
"v_cvt_pk_bf16_f32 v202, v148, v149\n" \
"v_cvt_pk_bf16_f32 v203, v150, v151\n" \
"ds_read_b128 a[224:227], v236 offset:23040\n" \
"v_mfma_f32_16x16x32_bf16 v[116:119], a[148:151], v[220:223], v[116:119]\n" \
"v_exp_f32_e64 v153, v153\n" \
"v_mfma_f32_16x16x32_bf16 v[120:123], a[184:187], v[204:207], v[120:123]\n" \
"v_exp_f32_e64 v154, v154\n" \
"v_mfma_f32_16x16x32_bf16 v[120:123], a[152:155], v[220:223], v[120:123]\n" \
"ds_read_b64_tr_b16 a[178:179], %[vvb] offset:46080\n" \
"global_load_dwordx2 a[254:255], v229, %[px] offset:1088\n" \
"v_mfma_f32_16x16x32_bf16 v[124:127], a[188:191], v[204:207], v[124:127]\n" \
"v_exp_f32_e64 v155, v155\n" \
"v_mfma_f32_16x16x32_bf16 v[124:127], a[156:159], v[220:223], v[124:127]\n" \
"ds_read_b64_tr_b16 a[180:181], v211 offset:41984\n" \
"s_waitcnt lgkmcnt(14)\n" \
"v_mfma_f32_16x16x32_bf16 v[128:131], a[64:67], a[0:3], v[160:163]\n" \
"v_exp_f32_e32 v156, v156\n" \
"v_mfma_f32_16x16x32_bf16 v[128:131], a[68:71], a[4:7], v[128:131]\n" \
"ds_read_b64_tr_b16 a[182:183], v211 offset:46080\n" \
"s_add_u32 m0, %[m0b], 32768\n" \
"s_cmp_gt_u32 s48, 0\n" \
"v_mfma_f32_16x16x32_bf16 v[128:131], a[72:75], a[8:11], v[128:131]\n" \
"v_exp_f32_e32 v157, v157\n" \
"v_mfma_f32_16x16x32_bf16 v[128:131], a[76:79], a[12:15], v[128:131]\n" \
"v_cvt_pk_bf16_f32 v204, v152, v153\n" \
"v_cvt_pk_bf16_f32 v205, v154, v155\n" \
"v_mfma_f32_16x16x32_bf16 v[132:135], a[80:83], a[0:3], v[160:163]\n" \
"v_exp_f32_e64 v158, v158\n" \
"v_mfma_f32_16x16x32_bf16 v[132:135], a[84:87], a[4:7], v[132:135]\n" \
"ds_read_b64_tr_b16 a[184:185], %[vvb] offset:42496\n" \
"s_cselect_b32 s49, 0, 1\n" \
"s_lshl_b32 s49, s49, 18\n" \
"s_or_b32 s49, s49, s58\n" \
"s_waitcnt lgkmcnt(8)\n" \
"v_mfma_f32_16x16x32_bf16 v[132:135], a[88:91], a[8:11], v[132:135]\n" \
"buffer_load_dwordx4 %[wo0], %[srdt], s49 offen offset:0 lds\n" \
"v_mfma_f32_16x16x32_bf16 v[132:135], a[92:95], a[12:15], v[132:135]\n" \
"v_exp_f32_e64 v159, v159\n" \
"v_mfma_f32_16x16x32_bf16 v[64:67], a[128:131], v[216:219], v[64:67]\n" \
"ds_read_b64_tr_b16 a[186:187], %[vvb] offset:46592\n" \
"ds_read_b64_tr_b16 a[188:189], v211 offset:42496\n" \
"v_mfma_f32_16x16x32_bf16 v[64:67], a[160:163], v[200:203], v[64:67]\n" \
"v_exp_f32_e64 v128, v128\n" \
"v_mfma_f32_16x16x32_bf16 v[68:71], a[132:135], v[216:219], v[68:71]\n" \
"v_cvt_pk_bf16_f32 v206, v156, v157\n" \
"v_cvt_pk_bf16_f32 v207, v158, v159\n" \
"v_mfma_f32_16x16x32_bf16 v[68:71], a[164:167], v[200:203], v[68:71]\n" \
"v_exp_f32_e64 v129, v129\n" \
"v_mfma_f32_16x16x32_bf16 v[72:75], a[136:139], v[216:219], v[72:75]\n" \
"ds_read_b64_tr_b16 a[190:191], v211 offset:46592\n" \
"ds_read_b64_tr_b16 a[128:129], %[vvb] offset:49152\n" \
"v_mfma_f32_16x16x32_bf16 v[72:75], a[168:171], v[200:203], v[72:75]\n" \
"v_exp_f32_e64 v130, v130\n" \
"v_mfma_f32_16x16x32_bf16 v[76:79], a[140:143], v[216:219], v[76:79]\n" \
"ds_read_b64_tr_b16 a[130:131], %[vvb] offset:53248\n" \
"ds_read_b64_tr_b16 a[132:133], v211 offset:49152\n" \
"v_mfma_f32_16x16x32_bf16 v[76:79], a[172:175], v[200:203], v[76:79]\n" \
"v_exp_f32_e64 v131, v131\n" \
"v_smfmac_f32_16x16x64_bf16 v[176:179], a[192:195], v[192:199], v228\n" \
"ds_read_b64_tr_b16 a[134:135], v211 offset:53248\n" \
"ds_read_b64_tr_b16 a[136:137], %[vvb] offset:49664\n" \
"v_mfma_f32_16x16x32_bf16 v[136:139], a[64:67], a[16:19], v[164:167]\n" \
"v_exp_f32_e64 v132, v132\n" \
"v_mfma_f32_16x16x32_bf16 v[136:139], a[68:71], a[20:23], v[136:139]\n" \
"ds_read_b64_tr_b16 a[138:139], %[vvb] offset:53760\n" \
"ds_read_b64_tr_b16 a[140:141], v211 offset:49664\n" \
"v_mfma_f32_16x16x32_bf16 v[136:139], a[72:75], a[24:27], v[136:139]\n" \
"v_exp_f32_e64 v133, v133\n" \
"v_mfma_f32_16x16x32_bf16 v[136:139], a[76:79], a[28:31], v[136:139]\n" \
"v_cvt_pk_bf16_f32 v184, v128, v129\n" \
"v_cvt_pk_bf16_f32 v185, v130, v131\n" \
"v_mfma_f32_16x16x32_bf16 v[140:143], a[80:83], a[16:19], v[164:167]\n" \
"v_exp_f32_e64 v134, v134\n" \
"v_mfma_f32_16x16x32_bf16 v[140:143], a[84:87], a[20:23], v[140:143]\n" \
"ds_read_b64_tr_b16 a[142:143], v211 offset:53760\n" \
"s_waitcnt lgkmcnt(8)\n" \
"v_mfma_f32_16x16x32_bf16 v[140:143], a[88:91], a[24:27], v[140:143]\n" \
"buffer_load_dwordx4 v212, %[srdt], s49 offen offset:1024 lds\n" \
"v_mfma_f32_16x16x32_bf16 v[140:143], a[92:95], a[28:31], v[140:143]\n" \
"v_exp_f32_e32 v135, v135\n" \
"v_mfma_f32_16x16x32_bf16 v[80:83], a[144:147], v[216:219], v[80:83]\n" \
"ds_read_b64_tr_b16 a[144:145], %[vvb] offset:50176\n" \
"ds_read_b64_tr_b16 a[146:147], %[vvb] offset:54272\n" \
"v_mfma_f32_16x16x32_bf16 v[80:83], a[176:179], v[200:203], v[80:83]\n" \
"v_exp_f32_e64 v136, v136\n" \
"v_mfma_f32_16x16x32_bf16 v[84:87], a[148:151], v[216:219], v[84:87]\n" \
"v_cvt_pk_bf16_f32 v186, v132, v133\n" \
"v_cvt_pk_bf16_f32 v187, v134, v135\n" \
"v_mfma_f32_16x16x32_bf16 v[84:87], a[180:183], v[200:203], v[84:87]\n" \
"v_exp_f32_e64 v137, v137\n" \
"v_mfma_f32_16x16x32_bf16 v[88:91], a[152:155], v[216:219], v[88:91]\n" \
"v_exp_f32_e64 v138, v138\n" \
"v_mfma_f32_16x16x32_bf16 v[88:91], a[184:187], v[200:203], v[88:91]\n" \
"ds_read_b64_tr_b16 a[148:149], v211 offset:50176\n" \
"ds_read_b64_tr_b16 a[150:151], v211 offset:54272\n" \
"v_readlane_b32 s64, %[tlo], 9\n" \
"v_mfma_f32_16x16x32_bf16 v[92:95], a[156:159], v[216:219], v[92:95]\n" \
"v_exp_f32_e64 v139, v139\n" \
"v_mfma_f32_16x16x32_bf16 v[92:95], a[188:191], v[200:203], v[92:95]\n" \
"ds_read_b64_tr_b16 a[152:153], %[vvb] offset:50688\n" \
"v_readlane_b32 s65, %[thi], 9\n" \
"v_mfma_f32_16x16x32_bf16 v[144:147], a[64:67], a[32:35], v[168:171]\n" \
"v_exp_f32_e64 v140, v140\n" \
"v_mfma_f32_16x16x32_bf16 v[144:147], a[68:71], a[36:39], v[144:147]\n" \
"ds_read_b64_tr_b16 a[154:155], %[vvb] offset:54784\n" \
"ds_read_b64_tr_b16 a[156:157], v211 offset:50688\n" \
"v_mfma_f32_16x16x32_bf16 v[144:147], a[72:75], a[40:43], v[144:147]\n" \
"v_exp_f32_e64 v141, v141\n" \
"v_mfma_f32_16x16x32_bf16 v[144:147], a[76:79], a[44:47], v[144:147]\n" \
"v_cvt_pk_bf16_f32 v188, v136, v137\n" \
"v_cvt_pk_bf16_f32 v189, v138, v139\n" \
"v_mfma_f32_16x16x32_bf16 v[148:151], a[80:83], a[32:35], v[168:171]\n" \
"v_exp_f32_e64 v142, v142\n" \
"v_mfma_f32_16x16x32_bf16 v[148:151], a[84:87], a[36:39], v[148:151]\n" \
"ds_read_b64_tr_b16 a[158:159], v211 offset:54784\n" \
"s_waitcnt lgkmcnt(10)\n" \
"v_mfma_f32_16x16x32_bf16 v[148:151], a[88:91], a[40:43], v[148:151]\n" \
"buffer_load_dwordx4 v213, %[srdt], s49 offen offset:2048 lds\n" \
"v_mfma_f32_16x16x32_bf16 v[148:151], a[92:95], a[44:47], v[148:151]\n" \
"v_exp_f32_e32 v143, v143\n" \
"v_mfma_f32_16x16x32_bf16 v[0:3], a[160:163], v[192:195], v[0:3]\n" \
"ds_read_b128 a[96:99], %[kb0] offset:57344\n" \
"v_mfma_f32_16x16x32_bf16 v[0:3], a[128:131], v[184:187], v[0:3]\n" \
"v_exp_f32_e64 v144, v144\n" \
"v_mfma_f32_16x16x32_bf16 v[4:7], a[164:167], v[192:195], v[4:7]\n" \
"v_cvt_pk_bf16_f32 v190, v140, v141\n" \
"v_cvt_pk_bf16_f32 v191, v142, v143\n" \
"v_mfma_f32_16x16x32_bf16 v[4:7], a[132:135], v[184:187], v[4:7]\n" \
"v_exp_f32_e64 v145, v145\n" \
"v_mfma_f32_16x16x32_bf16 v[8:11], a[168:171], v[192:195], v[8:11]\n" \
"ds_read_b128 a[100:103], v208 offset:57344\n" \
"v_mfma_f32_16x16x32_bf16 v[8:11], a[136:139], v[184:187], v[8:11]\n" \
"v_exp_f32_e32 v146, v146\n" \
"s_waitcnt vmcnt(3) lgkmcnt(10)\n" \
"v_mfma_f32_16x16x32_bf16 v[232:235], a[240:243], a[212:215], 0\n" \
"v_mfma_f32_16x16x32_bf16 v[232:235], a[244:247], a[216:219], v[232:235]\n" \
"v_mfma_f32_16x16x32_bf16 v[232:235], a[248:251], a[220:223], v[232:235]\n" \
"v_mfma_f32_16x16x32_bf16 v[232:235], a[252:255], a[224:227], v[232:235]\n" \
"s_mov_b32 s68, 0xaaaaaaaa\n" \
"s_mov_b32 s69, 0xaaaaaaaa\n" \
"v_mfma_f32_16x16x32_bf16 v[12:15], a[172:175], v[192:195], v[12:15]\n" \
"ds_read_b128 a[104:107], v209 offset:57344\n" \
"v_mfma_f32_16x16x32_bf16 v[12:15], a[140:143], v[184:187], v[12:15]\n" \
"v_exp_f32_e64 v147, v147\n" \
"v_smfmac_f32_16x16x64_bf16 v[180:183], a[192:195], v[200:207], v228\n" \
"ds_read_b128 a[108:111], v210 offset:57344\n" \
"buffer_load_dword v229, v226, %[srdz], s50 offen sc1\n" \
"v_mfma_f32_16x16x32_bf16 v[152:155], a[64:67], a[48:51], v[172:175]\n" \
"v_exp_f32_e64 v148, v148\n" \
"v_mfma_f32_16x16x32_bf16 v[152:155], a[68:71], a[52:55], v[152:155]\n" \
"ds_read_b128 a[112:115], %[kb0] offset:61440\n" \
"v_cndmask_b32_e64 v232, v232, v233, s[68:69]\n" \
"v_mfma_f32_16x16x32_bf16 v[152:155], a[72:75], a[56:59], v[152:155]\n" \
"v_exp_f32_e64 v149, v149\n" \
"v_mfma_f32_16x16x32_bf16 v[152:155], a[76:79], a[60:63], v[152:155]\n" \
"v_cvt_pk_bf16_f32 v216, v144, v145\n" \
"v_cvt_pk_bf16_f32 v217, v146, v147\n" \
"v_mfma_f32_16x16x32_bf16 v[156:159], a[80:83], a[48:51], v[172:175]\n" \
"v_exp_f32_e64 v150, v150\n" \
"v_mfma_f32_16x16x32_bf16 v[156:159], a[84:87], a[52:55], v[156:159]\n" \
"ds_read_b128 a[116:119], v208 offset:61440\n" \
"s_waitcnt lgkmcnt(6)\n" \
"v_mfma_f32_16x16x32_bf16 v[156:159], a[88:91], a[56:59], v[156:159]\n" \
"buffer_load_dwordx4 v214, %[srdt], s49 offen offset:3072 lds\n" \
"v_mfma_f32_16x16x32_bf16 v[156:159], a[92:95], a[60:63], v[156:159]\n" \
"v_exp_f32_e32 v151, v151\n" \
"v_mfma_f32_16x16x32_bf16 v[16:19], a[176:179], v[192:195], v[16:19]\n" \
"ds_read_b128 a[120:123], v209 offset:61440\n" \
"v_cndmask_b32_e64 v234, v234, v235, s[68:69]\n" \
"v_mfma_f32_16x16x32_bf16 v[16:19], a[144:147], v[184:187], v[16:19]\n" \
"v_exp_f32_e64 v152, v152\n" \
"v_mfma_f32_16x16x32_bf16 v[20:23], a[180:183], v[192:195], v[20:23]\n" \
"v_cvt_pk_bf16_f32 v218, v148, v149\n" \
"v_cvt_pk_bf16_f32 v219, v150, v151\n" \
"v_mfma_f32_16x16x32_bf16 v[20:23], a[148:151], v[184:187], v[20:23]\n" \
"v_exp_f32_e64 v153, v153\n" \
"v_mfma_f32_16x16x32_bf16 v[24:27], a[184:187], v[192:195], v[24:27]\n" \
"v_exp_f32_e64 v154, v154\n" \
"v_mfma_f32_16x16x32_bf16 v[24:27], a[152:155], v[184:187], v[24:27]\n" \
"ds_read_b128 a[124:127], v210 offset:61440\n" \
"s_mov_b32 s68, 0xcccccccc\n" \
"v_mfma_f32_16x16x32_bf16 v[28:31], a[188:191], v[192:195], v[28:31]\n" \
"v_exp_f32_e64 v155, v155\n" \
"v_mfma_f32_16x16x32_bf16 v[28:31], a[156:159], v[184:187], v[28:31]\n" \
"s_waitcnt vmcnt(30) lgkmcnt(2)\n" \
"s_barrier\n" \
"v_mfma_f32_16x16x32_bf16 v[128:131], a[96:99], a[0:3], v[160:163]\n" \
"v_exp_f32_e64 v156, v156\n" \
"s_mov_b32 m0, s44\n" \
"s_addk_i32 s44, 0x400\n" \
"buffer_load_dwordx4 %[vg0], s[64:67], s45 offen offset:0 lds\n" \
"s_add_u32 s45, s45, 229376\n" \
"v_mfma_f32_16x16x32_bf16 v[128:131], a[100:103], a[4:7], v[128:131]\n" \
"s_add_u32 m0, %[m0b], 98304\n" \
"s_cmp_gt_u32 s48, 0\n" \
"v_mfma_f32_16x16x32_bf16 v[128:131], a[104:107], a[8:11], v[128:131]\n" \
"v_exp_f32_e32 v157, v157\n" \
"v_mfma_f32_16x16x32_bf16 v[128:131], a[108:111], a[12:15], v[128:131]\n" \
"v_cvt_pk_bf16_f32 v220, v152, v153\n" \
"v_cvt_pk_bf16_f32 v221, v154, v155\n" \
"v_mfma_f32_16x16x32_bf16 v[132:135], a[112:115], a[0:3], v[160:163]\n" \
"v_exp_f32_e64 v158, v158\n" \
"v_mfma_f32_16x16x32_bf16 v[132:135], a[116:119], a[4:7], v[132:135]\n" \
"s_cselect_b32 s49, 0, 1\n" \
"s_lshl_b32 s49, s49, 18\n" \
"s_or_b32 s49, s49, s59\n" \
"s_waitcnt lgkmcnt(0)\n" \
"v_mfma_f32_16x16x32_bf16 v[132:135], a[120:123], a[8:11], v[132:135]\n" \
"buffer_load_dwordx4 %[wo0], %[srdt], s49 offen offset:0 lds\n" \
"v_mfma_f32_16x16x32_bf16 v[132:135], a[124:127], a[12:15], v[132:135]\n" \
"v_exp_f32_e64 v159, v159\n" \
"v_mfma_f32_16x16x32_bf16 v[32:35], a[160:163], v[196:199], v[32:35]\n" \
"ds_read_b64_tr_b16 a[196:197], v215 offset:0\n" \
"s_mov_b32 s69, 0xcccccccc\n" \
"v_mfma_f32_16x16x32_bf16 v[32:35], a[128:131], v[188:191], v[32:35]\n" \
"v_exp_f32_e64 v128, v128\n" \
"v_mfma_f32_16x16x32_bf16 v[36:39], a[164:167], v[196:199], v[36:39]\n" \
"v_cvt_pk_bf16_f32 v222, v156, v157\n" \
"v_cvt_pk_bf16_f32 v223, v158, v159\n" \
"v_mfma_f32_16x16x32_bf16 v[36:39], a[132:135], v[188:191], v[36:39]\n" \
"v_exp_f32_e64 v129, v129\n" \
"v_mfma_f32_16x16x32_bf16 v[40:43], a[168:171], v[196:199], v[40:43]\n" \
"ds_read_b64_tr_b16 a[198:199], v215 offset:2048\n" \
"v_cndmask_b32_e64 v232, v232, v234, s[68:69]\n" \
"v_mfma_f32_16x16x32_bf16 v[40:43], a[136:139], v[188:191], v[40:43]\n" \
"v_exp_f32_e64 v130, v130\n" \
"v_mfma_f32_16x16x32_bf16 v[44:47], a[172:175], v[196:199], v[44:47]\n" \
"ds_read_b64_tr_b16 a[200:201], v215 offset:4096\n" \
"s_mov_b32 s68, 0xf0000f\n" \
"v_mfma_f32_16x16x32_bf16 v[44:47], a[140:143], v[188:191], v[44:47]\n" \
"v_exp_f32_e64 v131, v131\n" \
"v_smfmac_f32_16x16x64_bf16 v[176:179], a[192:195], v[184:191], v228\n" \
"s_mov_b32 s69, 0xf0000f00\n" \
"v_cndmask_b32_e64 %[rd0], 0, v232, s[68:69]\n" \
"v_mfma_f32_16x16x32_bf16 v[136:139], a[96:99], a[16:19], v[164:167]\n" \
"v_exp_f32_e64 v132, v132\n" \
"v_mfma_f32_16x16x32_bf16 v[136:139], a[100:103], a[20:23], v[136:139]\n" \
"ds_read_b64_tr_b16 a[202:203], v215 offset:6144\n" \
"v_readlane_b32 s62, %[tlo], 32\n" \
"v_mfma_f32_16x16x32_bf16 v[136:139], a[104:107], a[24:27], v[136:139]\n" \
"v_exp_f32_e64 v133, v133\n" \
"v_mfma_f32_16x16x32_bf16 v[136:139], a[108:111], a[28:31], v[136:139]\n" \
"v_cvt_pk_bf16_f32 v192, v128, v129\n" \
"v_cvt_pk_bf16_f32 v193, v130, v131\n" \
"v_mfma_f32_16x16x32_bf16 v[140:143], a[112:115], a[16:19], v[164:167]\n" \
"v_exp_f32_e64 v134, v134\n" \
"v_mfma_f32_16x16x32_bf16 v[140:143], a[116:119], a[20:23], v[140:143]\n" \
"ds_read_b64_tr_b16 a[204:205], v215 offset:8192\n" \
"v_mfma_f32_16x16x32_bf16 v[140:143], a[120:123], a[24:27], v[140:143]\n" \
"buffer_load_dwordx4 v212, %[srdt], s49 offen offset:1024 lds\n" \
"v_mfma_f32_16x16x32_bf16 v[140:143], a[124:127], a[28:31], v[140:143]\n" \
"v_exp_f32_e64 v135, v135\n" \
"v_mfma_f32_16x16x32_bf16 v[48:51], a[176:179], v[196:199], v[48:51]\n" \
"ds_read_b64_tr_b16 a[206:207], v215 offset:10240\n" \
"v_add_u32_e32 v208, 0x1000, %[oq0]\n" \
"v_mfma_f32_16x16x32_bf16 v[48:51], a[144:147], v[188:191], v[48:51]\n" \
"v_exp_f32_e64 v136, v136\n" \
"v_mfma_f32_16x16x32_bf16 v[52:55], a[180:183], v[196:199], v[52:55]\n" \
"v_cvt_pk_bf16_f32 v194, v132, v133\n" \
"v_cvt_pk_bf16_f32 v195, v134, v135\n" \
"v_mfma_f32_16x16x32_bf16 v[52:55], a[148:151], v[188:191], v[52:55]\n" \
"v_exp_f32_e64 v137, v137\n" \
"v_mfma_f32_16x16x32_bf16 v[56:59], a[184:187], v[196:199], v[56:59]\n" \
"v_exp_f32_e64 v138, v138\n" \
"v_mfma_f32_16x16x32_bf16 v[56:59], a[152:155], v[188:191], v[56:59]\n" \
"ds_read_b64_tr_b16 a[208:209], v215 offset:12288\n" \
"v_add_u32_e32 v209, 0x2000, %[oq0]\n" \
"v_mfma_f32_16x16x32_bf16 v[60:63], a[188:191], v[196:199], v[60:63]\n" \
"v_exp_f32_e64 v139, v139\n" \
"v_mfma_f32_16x16x32_bf16 v[60:63], a[156:159], v[188:191], v[60:63]\n" \
"v_add_u32_e32 v210, 0x3000, %[oq0]\n" \
"v_lshrrev_b32_e64 v237, 7, v224\n" \
"v_mfma_f32_16x16x32_bf16 v[144:147], a[96:99], a[32:35], v[168:171]\n" \
"v_exp_f32_e64 v140, v140\n" \
"v_mfma_f32_16x16x32_bf16 v[144:147], a[100:103], a[36:39], v[144:147]\n" \
"ds_read_b64_tr_b16 a[210:211], v215 offset:14336\n" \
"v_and_b32_e64 v237, 48, v237\n" \
"v_mfma_f32_16x16x32_bf16 v[144:147], a[104:107], a[40:43], v[144:147]\n" \
"v_exp_f32_e64 v141, v141\n" \
"v_mfma_f32_16x16x32_bf16 v[144:147], a[108:111], a[44:47], v[144:147]\n" \
"v_cvt_pk_bf16_f32 v196, v136, v137\n" \
"v_cvt_pk_bf16_f32 v197, v138, v139\n" \
"v_mfma_f32_16x16x32_bf16 v[148:151], a[112:115], a[32:35], v[168:171]\n" \
"v_exp_f32_e64 v142, v142\n" \
"v_mfma_f32_16x16x32_bf16 v[148:151], a[116:119], a[36:39], v[148:151]\n" \
"v_add_u32_e64 v237, s62, v237\n" \
"v_mfma_f32_16x16x32_bf16 v[148:151], a[120:123], a[40:43], v[148:151]\n" \
"buffer_load_dwordx4 v213, %[srdt], s49 offen offset:2048 lds\n" \
"v_mfma_f32_16x16x32_bf16 v[148:151], a[124:127], a[44:47], v[148:151]\n" \
"v_exp_f32_e64 v143, v143\n" \
"v_mfma_f32_16x16x32_bf16 v[96:99], a[160:163], v[204:207], v[96:99]\n" \
"ds_read_b64_tr_b16 a[160:161], %[vvb] offset:57344\n" \
"v_mfma_f32_16x16x32_bf16 v[96:99], a[128:131], v[220:223], v[96:99]\n" \
"v_exp_f32_e64 v144, v144\n" \
"v_mfma_f32_16x16x32_bf16 v[100:103], a[164:167], v[204:207], v[100:103]\n" \
"v_cvt_pk_bf16_f32 v198, v140, v141\n" \
"v_cvt_pk_bf16_f32 v199, v142, v143\n" \
"v_mfma_f32_16x16x32_bf16 v[100:103], a[132:135], v[220:223], v[100:103]\n" \
"v_exp_f32_e64 v145, v145\n" \
"v_mfma_f32_16x16x32_bf16 v[104:107], a[168:171], v[204:207], v[104:107]\n" \
"ds_read_b64_tr_b16 a[162:163], %[vvb] offset:61440\n" \
"ds_read_b64_tr_b16 a[164:165], v211 offset:57344\n" \
"v_mfma_f32_16x16x32_bf16 v[104:107], a[136:139], v[220:223], v[104:107]\n" \
"v_exp_f32_e64 v146, v146\n" \
"v_mfma_f32_16x16x32_bf16 v[108:111], a[172:175], v[204:207], v[108:111]\n" \
"ds_read_b64_tr_b16 a[166:167], v211 offset:61440\n" \
"v_mfma_f32_16x16x32_bf16 v[108:111], a[140:143], v[220:223], v[108:111]\n" \
"v_exp_f32_e64 v147, v147\n" \
"v_smfmac_f32_16x16x64_bf16 v[180:183], a[192:195], v[216:223], v228\n" \
"ds_read_b64_tr_b16 a[168:169], %[vvb] offset:57856\n" \
"v_mfma_f32_16x16x32_bf16 v[152:155], a[96:99], a[48:51], v[172:175]\n" \
"v_exp_f32_e64 v148, v148\n" \
"v_mfma_f32_16x16x32_bf16 v[152:155], a[100:103], a[52:55], v[152:155]\n" \
"ds_read_b64_tr_b16 a[170:171], %[vvb] offset:61952\n" \
"v_mfma_f32_16x16x32_bf16 v[152:155], a[104:107], a[56:59], v[152:155]\n" \
"v_exp_f32_e64 v149, v149\n" \
"v_mfma_f32_16x16x32_bf16 v[152:155], a[108:111], a[60:63], v[152:155]\n" \
"v_cvt_pk_bf16_f32 v200, v144, v145\n" \
"v_cvt_pk_bf16_f32 v201, v146, v147\n" \
"v_mfma_f32_16x16x32_bf16 v[156:159], a[112:115], a[48:51], v[172:175]\n" \
"v_exp_f32_e64 v150, v150\n" \
"v_mfma_f32_16x16x32_bf16 v[156:159], a[116:119], a[52:55], v[156:159]\n" \
"ds_read_b64_tr_b16 a[172:173], v211 offset:57856\n" \
"v_mfma_f32_16x16x32_bf16 v[156:159], a[120:123], a[56:59], v[156:159]\n" \
"buffer_load_dwordx4 v214, %[srdt], s49 offen offset:3072 lds\n" \
"v_mfma_f32_16x16x32_bf16 v[156:159], a[124:127], a[60:63], v[156:159]\n" \
"v_exp_f32_e64 v151, v151\n" \
"v_mfma_f32_16x16x32_bf16 v[112:115], a[176:179], v[204:207], v[112:115]\n" \
"ds_read_b64_tr_b16 a[174:175], v211 offset:61952\n" \
"ds_read_b64_tr_b16 a[176:177], %[vvb] offset:58368\n" \
"v_mfma_f32_16x16x32_bf16 v[112:115], a[144:147], v[220:223], v[112:115]\n" \
"v_exp_f32_e64 v152, v152\n" \
"s_mov_b32 m0, s44\n" \
"s_addk_i32 s44, 0x400\n" \
"buffer_load_dwordx4 %[vg0], s[64:67], s45 offen offset:0 lds\n" \
"s_add_u32 s45, s45, 229376\n" \
"v_mfma_f32_16x16x32_bf16 v[116:119], a[180:183], v[204:207], v[116:119]\n" \
"v_cvt_pk_bf16_f32 v202, v148, v149\n" \
"v_cvt_pk_bf16_f32 v203, v150, v151\n" \
"v_mfma_f32_16x16x32_bf16 v[116:119], a[148:151], v[220:223], v[116:119]\n" \
"v_exp_f32_e64 v153, v153\n" \
"v_mfma_f32_16x16x32_bf16 v[120:123], a[184:187], v[204:207], v[120:123]\n" \
"v_exp_f32_e64 v154, v154\n" \
"v_mfma_f32_16x16x32_bf16 v[120:123], a[152:155], v[220:223], v[120:123]\n" \
"ds_read_b64_tr_b16 a[178:179], %[vvb] offset:62464\n" \
"s_waitcnt vmcnt(7)\n" \
"v_cmp_eq_u32_e64 s[56:57], 0, v229\n" \
"s_cmp_lg_u64 s[56:57], 0\n" \
"s_cbranch_scc1 Lspk_%=\n" \
"Lbkk_%=:\n" \
"v_mfma_f32_16x16x32_bf16 v[124:127], a[188:191], v[204:207], v[124:127]\n" \
"v_exp_f32_e32 v155, v155\n" \
"v_mfma_f32_16x16x32_bf16 v[124:127], a[156:159], v[220:223], v[124:127]\n" \
"ds_read_b64_tr_b16 a[180:181], v211 offset:58368\n" \
"ds_read_b64_tr_b16 a[182:183], v211 offset:62464\n" \
"ds_read_b64_tr_b16 a[184:185], %[vvb] offset:58880\n" \
"ds_read_b64_tr_b16 a[186:187], %[vvb] offset:62976\n" \
"ds_read_b64_tr_b16 a[188:189], v211 offset:58880\n" \
"ds_read_b64_tr_b16 a[190:191], v211 offset:62976\n" \
"s_waitcnt lgkmcnt(8)\n" \
"s_nop 0\n" \
"v_smfmac_f32_16x16x64_bf16 v[176:179], a[192:195], v[192:199], v228\n" \
"v_exp_f32_e64 v156, v156\n" \
"ds_read_b64_tr_b16 a[212:213], v227 offset:0\n" \
"v_mfma_f32_16x16x32_bf16 v[0:3], a[160:163], v[192:195], v[0:3]\n" \
"v_exp_f32_e64 v157, v157\n" \
"ds_read_b64_tr_b16 a[214:215], v227 offset:2048\n" \
"v_mfma_f32_16x16x32_bf16 v[4:7], a[164:167], v[192:195], v[4:7]\n" \
"v_cvt_pk_bf16_f32 v204, v152, v153\n" \
"ds_read_b64_tr_b16 a[216:217], v227 offset:4096\n" \
"v_mfma_f32_16x16x32_bf16 v[8:11], a[168:171], v[192:195], v[8:11]\n" \
"v_cvt_pk_bf16_f32 v205, v154, v155\n" \
"ds_read_b64_tr_b16 a[218:219], v227 offset:6144\n" \
"v_mfma_f32_16x16x32_bf16 v[12:15], a[172:175], v[192:195], v[12:15]\n" \
"v_exp_f32_e64 v158, v158\n" \
"ds_read_b64_tr_b16 a[220:221], v227 offset:8192\n" \
"s_waitcnt lgkmcnt(5)\n" \
"s_nop 0\n" \
"v_mfma_f32_16x16x32_bf16 v[16:19], a[176:179], v[192:195], v[16:19]\n" \
"v_exp_f32_e64 v159, v159\n" \
"ds_read_b64_tr_b16 a[222:223], v227 offset:10240\n" \
"v_mfma_f32_16x16x32_bf16 v[20:23], a[180:183], v[192:195], v[20:23]\n" \
"v_cvt_pk_bf16_f32 v206, v156, v157\n" \
"ds_read_b64_tr_b16 a[224:225], v227 offset:12288\n" \
"v_mfma_f32_16x16x32_bf16 v[24:27], a[184:187], v[192:195], v[24:27]\n" \
"v_cvt_pk_bf16_f32 v207, v158, v159\n" \
"ds_read_b64_tr_b16 a[226:227], v227 offset:14336\n" \
"v_mfma_f32_16x16x32_bf16 v[28:31], a[188:191], v[192:195], v[28:31]\n" \
"v_add_f32_e64 v176, v176, v177\n" \
"v_smfmac_f32_16x16x64_bf16 v[180:183], a[192:195], v[200:207], v228\n" \
"v_add_f32_e32 v178, v178, v179\n" \
"v_add_f32_e32 v231, v176, v178\n" \
"v_rcp_f32_e32 v176, v176\n" \
"v_rcp_f32_e32 v178, v178\n" \
"v_mfma_f32_16x16x32_bf16 v[32:35], a[160:163], v[196:199], v[32:35]\n" \
"v_pk_mul_f32 v[0:1], v[0:1], v[176:177] op_sel_hi:[1,0]\n" \
"v_pk_mul_f32 v[2:3], v[2:3], v[176:177] op_sel_hi:[1,0]\n" \
"v_pk_mul_f32 v[4:5], v[4:5], v[176:177] op_sel_hi:[1,0]\n" \
"v_pk_mul_f32 v[6:7], v[6:7], v[176:177] op_sel_hi:[1,0]\n" \
"v_cvt_pk_bf16_f32 v0, v0, v1\n" \
"v_cvt_pk_bf16_f32 v1, v2, v3\n" \
"v_cvt_pk_bf16_f32 v2, v4, v5\n" \
"v_mfma_f32_16x16x32_bf16 v[36:39], a[164:167], v[196:199], v[36:39]\n" \
"v_cvt_pk_bf16_f32 v3, v6, v7\n" \
"s_mov_b32 s55, 0x4e800000\n" \
"v_mov_b32_e32 v236, %[flag]\n" \
"v_add_f32_e32 v180, v180, v181\n" \
"v_add_f32_e32 v182, v182, v183\n" \
"v_add_f32_e32 v231, v231, v180\n" \
"v_add_f32_e64 v231, v231, v182\n" \
"v_mfma_f32_16x16x32_bf16 v[40:43], a[168:171], v[196:199], v[40:43]\n" \
"v_rcp_f32_e64 v180, v180\n" \
"v_cmp_nle_f32_e64 s[56:57], v231, s55\n" \
"v_rcp_f32_e32 v182, v182\n" \
"s_nop 3\n" \
"s_cmp_lg_u64 s[56:57], 0\n" \
"s_cselect_b32 s55, 1, 0\n" \
"v_mov_b32_e64 v230, s55\n" \
"v_mfma_f32_16x16x32_bf16 v[44:47], a[172:175], v[196:199], v[44:47]\n" \
"s_mov_b32 s69, s55\n" \
"s_andn2_b32 s55, s55, s52\n" \
"s_cselect_b64 vcc, 0, vcc\n" \
"s_nop 0\n" \
"ds_write_b32 v236, v230\n" \
"v_and_b32_e32 v236, -16, v236\n" \
"s_cbranch_vccz Lg37_%=\n" \
"buffer_store_dwordx4 v[0:3], %[oq0], %[srdz], s60 offen offset:0 sc1\n" \
"Lg37_%=:\n" \
"v_pk_mul_f32 v[8:9], v[8:9], v[176:177] op_sel_hi:[1,0]\n" \
"v_mfma_f32_16x16x32_bf16 v[48:51], a[176:179], v[196:199], v[48:51]\n" \
"v_pk_mul_f32 v[10:11], v[10:11], v[176:177] op_sel_hi:[1,0]\n" \
"v_pk_mul_f32 v[12:13], v[12:13], v[176:177] op_sel_hi:[1,0]\n" \
"v_pk_mul_f32 v[14:15], v[14:15], v[176:177] op_sel_hi:[1,0]\n" \
"v_cvt_pk_bf16_f32 v8, v8, v9\n" \
"v_cvt_pk_bf16_f32 v9, v10, v11\n" \
"v_cvt_pk_bf16_f32 v10, v12, v13\n" \
"v_cvt_pk_bf16_f32 v11, v14, v15\n" \
"v_mfma_f32_16x16x32_bf16 v[52:55], a[180:183], v[196:199], v[52:55]\n" \
"s_cbranch_vccz Lg38_%=\n" \
"s_nop 0\n" \
"buffer_store_dwordx4 v[8:11], %[oq0], %[srdz], s60 offen offset:1024 sc1\n" \
"Lg38_%=:\n" \
"v_pk_mul_f32 v[16:17], v[16:17], v[176:177] op_sel_hi:[1,0]\n" \
"v_pk_mul_f32 v[18:19], v[18:19], v[176:177] op_sel_hi:[1,0]\n" \
"v_pk_mul_f32 v[20:21], v[20:21], v[176:177] op_sel_hi:[1,0]\n" \
"v_pk_mul_f32 v[22:23], v[22:23], v[176:177] op_sel_hi:[1,0]\n" \
"v_cvt_pk_bf16_f32 v16, v16, v17\n" \
"v_cvt_pk_bf16_f32 v17, v18, v19\n" \
"v_mfma_f32_16x16x32_bf16 v[56:59], a[184:187], v[196:199], v[56:59]\n" \
"v_cvt_pk_bf16_f32 v18, v20, v21\n" \
"v_cvt_pk_bf16_f32 v19, v22, v23\n" \
"s_cbranch_vccz Lg39_%=\n" \
"s_nop 0\n" \
"buffer_store_dwordx4 v[16:19], %[oq0], %[srdz], s60 offen offset:2048 sc1\n" \
"Lg39_%=:\n" \
"v_pk_mul_f32 v[24:25], v[24:25], v[176:177] op_sel_hi:[1,0]\n" \
"v_pk_mul_f32 v[26:27], v[26:27], v[176:177] op_sel_hi:[1,0]\n" \
"v_pk_mul_f32 v[28:29], v[28:29], v[176:177] op_sel_hi:[1,0]\n" \
"v_pk_mul_f32 v[30:31], v[30:31], v[176:177] op_sel_hi:[1,0]\n" \
"v_mfma_f32_16x16x32_bf16 v[60:63], a[188:191], v[196:199], v[60:63]\n" \
"v_cvt_pk_bf16_f32 v24, v24, v25\n" \
"v_cvt_pk_bf16_f32 v25, v26, v27\n" \
"v_cvt_pk_bf16_f32 v26, v28, v29\n" \
"v_cvt_pk_bf16_f32 v27, v30, v31\n" \
"s_cbranch_vccz Lg40_%=\n" \
"s_nop 0\n" \
"buffer_store_dwordx4 v[24:27], %[oq0], %[srdz], s60 offen offset:3072 sc1\n" \
"Lg40_%=:\n" \
"v_mfma_f32_16x16x32_bf16 v[64:67], a[128:131], v[216:219], v[64:67]\n" \
"v_pk_mul_f32 v[32:33], v[32:33], v[178:179] op_sel_hi:[1,0]\n" \
"v_pk_mul_f32 v[34:35], v[34:35], v[178:179] op_sel_hi:[1,0]\n" \
"v_pk_mul_f32 v[36:37], v[36:37], v[178:179] op_sel_hi:[1,0]\n" \
"v_mfma_f32_16x16x32_bf16 v[64:67], a[160:163], v[200:203], v[64:67]\n" \
"v_pk_mul_f32 v[38:39], v[38:39], v[178:179] op_sel_hi:[1,0]\n" \
"v_cvt_pk_bf16_f32 v32, v32, v33\n" \
"v_cvt_pk_bf16_f32 v33, v34, v35\n" \
"v_mfma_f32_16x16x32_bf16 v[68:71], a[132:135], v[216:219], v[68:71]\n" \
"v_cvt_pk_bf16_f32 v34, v36, v37\n" \
"v_cvt_pk_bf16_f32 v35, v38, v39\n" \
"s_cbranch_vccz Lg41_%=\n" \
"s_nop 0\n" \
"buffer_store_dwordx4 v[32:35], v208, %[srdz], s60 offen offset:0 sc1\n" \
"Lg41_%=:\n" \
"v_mfma_f32_16x16x32_bf16 v[68:71], a[164:167], v[200:203], v[68:71]\n" \
"v_pk_mul_f32 v[40:41], v[40:41], v[178:179] op_sel_hi:[1,0]\n" \
"v_pk_mul_f32 v[42:43], v[42:43], v[178:179] op_sel_hi:[1,0]\n" \
"v_pk_mul_f32 v[44:45], v[44:45], v[178:179] op_sel_hi:[1,0]\n" \
"v_mfma_f32_16x16x32_bf16 v[72:75], a[136:139], v[216:219], v[72:75]\n" \
"v_pk_mul_f32 v[46:47], v[46:47], v[178:179] op_sel_hi:[1,0]\n" \
"v_cvt_pk_bf16_f32 v40, v40, v41\n" \
"v_cvt_pk_bf16_f32 v41, v42, v43\n" \
"v_mfma_f32_16x16x32_bf16 v[72:75], a[168:171], v[200:203], v[72:75]\n" \
"v_cvt_pk_bf16_f32 v42, v44, v45\n" \
"v_cvt_pk_bf16_f32 v43, v46, v47\n" \
"s_cbranch_vccz Lg42_%=\n" \
"s_nop 0\n" \
"buffer_store_dwordx4 v[40:43], v208, %[srdz], s60 offen offset:1024 sc1\n" \
"Lg42_%=:\n" \
"v_mfma_f32_16x16x32_bf16 v[76:79], a[140:143], v[216:219], v[76:79]\n" \
"v_pk_mul_f32 v[48:49], v[48:49], v[178:179] op_sel_hi:[1,0]\n" \
"v_pk_mul_f32 v[50:51], v[50:51], v[178:179] op_sel_hi:[1,0]\n" \
"v_pk_mul_f32 v[52:53], v[52:53], v[178:179] op_sel_hi:[1,0]\n" \
"v_mfma_f32_16x16x32_bf16 v[76:79], a[172:175], v[200:203], v[76:79]\n" \
"v_pk_mul_f32 v[54:55], v[54:55], v[178:179] op_sel_hi:[1,0]\n" \
"v_cvt_pk_bf16_f32 v48, v48, v49\n" \
"v_cvt_pk_bf16_f32 v49, v50, v51\n" \
"v_mfma_f32_16x16x32_bf16 v[80:83], a[144:147], v[216:219], v[80:83]\n" \
".if . - adaln_body != 40960\n" \
".error \"size model\"\n" \
".endif\n" \
"Lk9_%=:\n" \
"s_cbranch_execz Lk10_%=\n" \
"s_nop 0\n" \
"v_cvt_pk_bf16_f32 v50, v52, v53\n" \
"v_cvt_pk_bf16_f32 v51, v54, v55\n" \
"s_cbranch_vccz Lg43_%=\n" \
"s_nop 0\n" \
"buffer_store_dwordx4 v[48:51], v208, %[srdz], s60 offen offset:2048 sc1\n" \
"Lg43_%=:\n" \
"v_mfma_f32_16x16x32_bf16 v[80:83], a[176:179], v[200:203], v[80:83]\n" \
"v_pk_mul_f32 v[56:57], v[56:57], v[178:179] op_sel_hi:[1,0]\n" \
"v_pk_mul_f32 v[58:59], v[58:59], v[178:179] op_sel_hi:[1,0]\n" \
"v_pk_mul_f32 v[60:61], v[60:61], v[178:179] op_sel_hi:[1,0]\n" \
"v_mfma_f32_16x16x32_bf16 v[84:87], a[148:151], v[216:219], v[84:87]\n" \
"v_pk_mul_f32 v[62:63], v[62:63], v[178:179] op_sel_hi:[1,0]\n" \
"v_cvt_pk_bf16_f32 v56, v56, v57\n" \
"v_cvt_pk_bf16_f32 v57, v58, v59\n" \
"v_mfma_f32_16x16x32_bf16 v[84:87], a[180:183], v[200:203], v[84:87]\n" \
"v_cvt_pk_bf16_f32 v58, v60, v61\n" \
"v_cvt_pk_bf16_f32 v59, v62, v63\n" \
"s_cbranch_vccz Lg44_%=\n" \
"s_nop 0\n" \
"buffer_store_dwordx4 v[56:59], v208, %[srdz], s60 offen offset:3072 sc1\n" \
"Lg44_%=:\n" \
"v_mfma_f32_16x16x32_bf16 v[88:91], a[152:155], v[216:219], v[88:91]\n" \
"v_mfma_f32_16x16x32_bf16 v[88:91], a[184:187], v[200:203], v[88:91]\n" \
"v_mfma_f32_16x16x32_bf16 v[92:95], a[156:159], v[216:219], v[92:95]\n" \
"v_mfma_f32_16x16x32_bf16 v[92:95], a[188:191], v[200:203], v[92:95]\n" \
"s_waitcnt vmcnt(14) lgkmcnt(0)\n" \
"s_bitcmp1_b32 0xf, s48\n" \
"s_cbranch_scc1 Lcw1_%=\n" \
"s_waitcnt vmcnt(6)\n" \
"Lcw1_%=:\n" \
"s_barrier\n" \
"s_nop 0\n" \
"ds_read_b128 v[232:235], v236\n" \
"v_mfma_f32_16x16x32_bf16 v[96:99], a[160:163], v[204:207], v[96:99]\n" \
"v_pk_mul_f32 v[64:65], v[64:65], v[180:181] op_sel_hi:[1,0]\n" \
"v_pk_mul_f32 v[66:67], v[66:67], v[180:181] op_sel_hi:[1,0]\n" \
"v_pk_mul_f32 v[68:69], v[68:69], v[180:181] op_sel_hi:[1,0]\n" \
"v_pk_mul_f32 v[70:71], v[70:71], v[180:181] op_sel_hi:[1,0]\n" \
"v_cvt_pk_bf16_f32 v64, v64, v65\n" \
"v_mfma_f32_16x16x32_bf16 v[100:103], a[164:167], v[204:207], v[100:103]\n" \
"v_cvt_pk_bf16_f32 v65, v66, v67\n" \
"v_cvt_pk_bf16_f32 v66, v68, v69\n" \
"v_cvt_pk_bf16_f32 v67, v70, v71\n" \
"s_cbranch_vccz Lg45_%=\n" \
"s_nop 0\n" \
"buffer_store_dwordx4 v[64:67], v209, %[srdz], s60 offen offset:0 sc1\n" \
"Lg45_%=:\n" \
"v_pk_mul_f32 v[72:73], v[72:73], v[180:181] op_sel_hi:[1,0]\n" \
"v_mfma_f32_16x16x32_bf16 v[104:107], a[168:171], v[204:207], v[104:107]\n" \
"v_pk_mul_f32 v[74:75], v[74:75], v[180:181] op_sel_hi:[1,0]\n" \
"v_pk_mul_f32 v[76:77], v[76:77], v[180:181] op_sel_hi:[1,0]\n" \
"v_pk_mul_f32 v[78:79], v[78:79], v[180:181] op_sel_hi:[1,0]\n" \
"v_cvt_pk_bf16_f32 v72, v72, v73\n" \
"v_cvt_pk_bf16_f32 v73, v74, v75\n" \
"v_mfma_f32_16x16x32_bf16 v[108:111], a[172:175], v[204:207], v[108:111]\n" \
"v_cvt_pk_bf16_f32 v74, v76, v77\n" \
"v_cvt_pk_bf16_f32 v75, v78, v79\n" \
"s_cbranch_vccz Lg46_%=\n" \
"s_nop 0\n" \
"buffer_store_dwordx4 v[72:75], v209, %[srdz], s60 offen offset:1024 sc1\n" \
"Lg46_%=:\n" \
"v_pk_mul_f32 v[80:81], v[80:81], v[180:181] op_sel_hi:[1,0]\n" \
"v_pk_mul_f32 v[82:83], v[82:83], v[180:181] op_sel_hi:[1,0]\n" \
"v_mfma_f32_16x16x32_bf16 v[112:115], a[176:179], v[204:207], v[112:115]\n" \
"v_pk_mul_f32 v[84:85], v[84:85], v[180:181] op_sel_hi:[1,0]\n" \
"v_pk_mul_f32 v[86:87], v[86:87], v[180:181] op_sel_hi:[1,0]\n" \
"v_cvt_pk_bf16_f32 v80, v80, v81\n" \
"v_cvt_pk_bf16_f32 v81, v82, v83\n" \
"v_cvt_pk_bf16_f32 v82, v84, v85\n" \
"v_mfma_f32_16x16x32_bf16 v[116:119], a[180:183], v[204:207], v[116:119]\n" \
"v_cvt_pk_bf16_f32 v83, v86, v87\n" \
"s_cbranch_vccz Lg47_%=\n" \
"s_nop 0\n" \
"buffer_store_dwordx4 v[80:83], v209, %[srdz], s60 offen offset:2048 sc1\n" \
"Lg47_%=:\n" \
"v_pk_mul_f32 v[88:89], v[88:89], v[180:181] op_sel_hi:[1,0]\n" \
"v_pk_mul_f32 v[90:91], v[90:91], v[180:181] op_sel_hi:[1,0]\n" \
"v_pk_mul_f32 v[92:93], v[92:93], v[180:181] op_sel_hi:[1,0]\n" \
"v_mfma_f32_16x16x32_bf16 v[120:123], a[184:187], v[204:207], v[120:123]\n" \
"v_pk_mul_f32 v[94:95], v[94:95], v[180:181] op_sel_hi:[1,0]\n" \
"v_cvt_pk_bf16_f32 v88, v88, v89\n" \
"v_cvt_pk_bf16_f32 v89, v90, v91\n" \
"v_cvt_pk_bf16_f32 v90, v92, v93\n" \
"v_cvt_pk_bf16_f32 v91, v94, v95\n" \
"v_mfma_f32_16x16x32_bf16 v[124:127], a[188:191], v[204:207], v[124:127]\n" \
"s_cbranch_vccz Lg48_%=\n" \
"s_nop 0\n" \
"buffer_store_dwordx4 v[88:91], v209, %[srdz], s60 offen offset:3072 sc1\n" \
"Lg48_%=:\n" \
"s_waitcnt lgkmcnt(0)\n" \
"s_nop 0\n" \
"v_or3_b32 v232, v232, v233, v234\n" \
"v_or_b32_e32 v232, v232, v235\n" \
"s_nop 0\n" \
"v_readfirstlane_b32 s55, v232\n" \
"s_nop 3\n" \
"s_andn2_b32 s55, s55, s52\n" \
"s_cbranch_scc1 Lslow_%=\n" \
"v_mfma_f32_16x16x32_bf16 v[192:195], a[196:199], v[0:3], 0\n" \
"v_mfma_f32_16x16x32_bf16 v[192:195], a[200:203], v[8:11], v[192:195]\n" \
"v_pk_mul_f32 v[96:97], v[96:97], v[182:183] op_sel_hi:[1,0]\n" \
"v_pk_mul_f32 v[98:99], v[98:99], v[182:183] op_sel_hi:[1,0]\n" \
"v_mfma_f32_16x16x32_bf16 v[192:195], a[204:207], v[16:19], v[192:195]\n" \
"v_pk_mul_f32 v[100:101], v[100:101], v[182:183] op_sel_hi:[1,0]\n" \
"v_pk_mul_f32 v[102:103], v[102:103], v[182:183] op_sel_hi:[1,0]\n" \
"s_add_u32 m0, %[m0b], 114688\n" \
"s_cmp_gt_u32 s48, 1\n" \
"s_cselect_b32 s49, 1, 2\n" \
"s_lshl_b32 s49, s49, 18\n" \
"s_or_b32 s49, s49, s58\n" \
"buffer_load_dwordx4 %[wo0], %[srdt], s49 offen offset:0 lds\n" \
"v_mfma_f32_16x16x32_bf16 v[192:195], a[208:211], v[24:27], v[192:195]\n" \
"v_cvt_pk_bf16_f32 v96, v96, v97\n" \
"v_cvt_pk_bf16_f32 v97, v98, v99\n" \
"v_mfma_f32_16x16x32_bf16 v[144:147], a[212:215], v[0:3], 0\n" \
"v_cvt_pk_bf16_f32 v98, v100, v101\n" \
"v_mfma_f32_16x16x32_bf16 v[144:147], a[216:219], v[8:11], v[144:147]\n" \
"v_cvt_pk_bf16_f32 v99, v102, v103\n" \
"s_cbranch_vccz Lg1_%=\n" \
"s_nop 0\n" \
"buffer_store_dwordx4 v[96:99], v210, %[srdz], s60 offen offset:0 sc1\n" \
"Lg1_%=:\n" \
"v_mfma_f32_16x16x32_bf16 v[144:147], a[220:223], v[16:19], v[144:147]\n" \
"ds_read_b64_tr_b16 a[228:229], v215 offset:512\n" \
"v_pk_mul_f32 v[104:105], v[104:105], v[182:183] op_sel_hi:[1,0]\n" \
"v_pk_mul_f32 v[106:107], v[106:107], v[182:183] op_sel_hi:[1,0]\n" \
"v_mfma_f32_16x16x32_bf16 v[144:147], a[224:227], v[24:27], v[144:147]\n" \
"ds_read_b64_tr_b16 a[230:231], v215 offset:2560\n" \
"v_pk_mul_f32 v[108:109], v[108:109], v[182:183] op_sel_hi:[1,0]\n" \
"v_mfma_f32_16x16x32_bf16 v[196:199], a[196:199], v[32:35], 0\n" \
"ds_read_b64_tr_b16 a[232:233], v215 offset:4608\n" \
"v_pk_mul_f32 v[110:111], v[110:111], v[182:183] op_sel_hi:[1,0]\n" \
"v_cvt_pk_bf16_f32 v104, v104, v105\n" \
"v_mfma_f32_16x16x32_bf16 v[196:199], a[200:203], v[40:43], v[196:199]\n" \
"ds_read_b64_tr_b16 a[234:235], v215 offset:6656\n" \
"v_cvt_pk_bf16_f32 v105, v106, v107\n" \
"v_cvt_pk_bf16_f32 v106, v108, v109\n" \
"v_mfma_f32_16x16x32_bf16 v[196:199], a[204:207], v[48:51], v[196:199]\n" \
"v_cvt_pk_bf16_f32 v107, v110, v111\n" \
"s_cbranch_vccz Lg2_%=\n" \
"s_nop 0\n" \
"buffer_store_dwordx4 v[104:107], v210, %[srdz], s60 offen offset:1024 sc1\n" \
"Lg2_%=:\n" \
"v_mfma_f32_16x16x32_bf16 v[196:199], a[208:211], v[56:59], v[196:199]\n" \
"v_pk_mul_f32 v[112:113], v[112:113], v[182:183] op_sel_hi:[1,0]\n" \
"v_mfma_f32_16x16x32_bf16 v[148:151], a[212:215], v[32:35], 0\n" \
"v_pk_mul_f32 v[114:115], v[114:115], v[182:183] op_sel_hi:[1,0]\n" \
"v_pk_mul_f32 v[116:117], v[116:117], v[182:183] op_sel_hi:[1,0]\n" \
"buffer_load_dwordx4 v212, %[srdt], s49 offen offset:1024 lds\n" \
"v_mfma_f32_16x16x32_bf16 v[148:151], a[216:219], v[40:43], v[148:151]\n" \
"v_pk_mul_f32 v[118:119], v[118:119], v[182:183] op_sel_hi:[1,0]\n" \
"v_cvt_pk_bf16_f32 v112, v112, v113\n" \
"v_mfma_f32_16x16x32_bf16 v[148:151], a[220:223], v[48:51], v[148:151]\n" \
"ds_read_b64_tr_b16 a[236:237], v215 offset:8704\n" \
"v_cvt_pk_bf16_f32 v113, v114, v115\n" \
"v_mfma_f32_16x16x32_bf16 v[148:151], a[224:227], v[56:59], v[148:151]\n" \
"v_cvt_pk_bf16_f32 v114, v116, v117\n" \
"v_cvt_pk_bf16_f32 v115, v118, v119\n" \
"v_mfma_f32_16x16x32_bf16 v[200:203], a[196:199], v[64:67], 0\n" \
"ds_read_b64_tr_b16 a[238:239], v215 offset:10752\n" \
"s_cbranch_vccz Lg3_%=\n" \
"s_nop 0\n" \
"buffer_store_dwordx4 v[112:115], v210, %[srdz], s60 offen offset:2048 sc1\n" \
"Lg3_%=:\n" \
"v_pk_mul_f32 v[120:121], v[120:121], v[182:183] op_sel_hi:[1,0]\n" \
"v_mfma_f32_16x16x32_bf16 v[200:203], a[200:203], v[72:75], v[200:203]\n" \
"v_pk_mul_f32 v[122:123], v[122:123], v[182:183] op_sel_hi:[1,0]\n" \
"v_pk_mul_f32 v[124:125], v[124:125], v[182:183] op_sel_hi:[1,0]\n" \
"v_mfma_f32_16x16x32_bf16 v[200:203], a[204:207], v[80:83], v[200:203]\n" \
"v_pk_mul_f32 v[126:127], v[126:127], v[182:183] op_sel_hi:[1,0]\n" \
"v_mfma_f32_16x16x32_bf16 v[200:203], a[208:211], v[88:91], v[200:203]\n" \
"v_cvt_pk_bf16_f32 v120, v120, v121\n" \
"v_cvt_pk_bf16_f32 v121, v122, v123\n" \
"v_mfma_f32_16x16x32_bf16 v[152:155], a[212:215], v[64:67], 0\n" \
"v_cvt_pk_bf16_f32 v122, v124, v125\n" \
"v_cvt_pk_bf16_f32 v123, v126, v127\n" \
"v_mfma_f32_16x16x32_bf16 v[152:155], a[216:219], v[72:75], v[152:155]\n" \
"s_cbranch_vccz Lg4_%=\n" \
"s_nop 0\n" \
"buffer_store_dwordx4 v[120:123], v210, %[srdz], s60 offen offset:3072 sc1\n" \
"Lg4_%=:\n" \
"v_mfma_f32_16x16x32_bf16 v[152:155], a[220:223], v[80:83], v[152:155]\n" \
"v_add_u32_e32 v220, 0x10000, v215\n" \
"v_mfma_f32_16x16x32_bf16 v[152:155], a[224:227], v[88:91], v[152:155]\n" \
"v_readlane_b32 s62, %[tlo], 19\n" \
"buffer_load_dwordx4 v213, %[srdt], s49 offen offset:2048 lds\n" \
"v_mfma_f32_16x16x32_bf16 v[204:207], a[196:199], v[96:99], 0\n" \
"v_add_u32_e32 v221, 0x10000, v227\n" \
"v_mfma_f32_16x16x32_bf16 v[204:207], a[200:203], v[104:107], v[204:207]\n" \
"ds_read_b64_tr_b16 a[196:197], v215 offset:12800\n" \
"v_mfma_f32_16x16x32_bf16 v[204:207], a[204:207], v[112:115], v[204:207]\n" \
"ds_read_b64_tr_b16 a[198:199], v215 offset:14848\n" \
"v_mfma_f32_16x16x32_bf16 v[204:207], a[208:211], v[120:123], v[204:207]\n" \
"ds_read_b64_tr_b16 a[200:201], v227 offset:512\n" \
"v_mfma_f32_16x16x32_bf16 v[156:159], a[212:215], v[96:99], 0\n" \
"ds_read_b64_tr_b16 a[202:203], v227 offset:2560\n" \
"v_mfma_f32_16x16x32_bf16 v[156:159], a[216:219], v[104:107], v[156:159]\n" \
"ds_read_b64_tr_b16 a[204:205], v227 offset:4608\n" \
"v_mfma_f32_16x16x32_bf16 v[156:159], a[220:223], v[112:115], v[156:159]\n" \
"ds_read_b64_tr_b16 a[206:207], v227 offset:6656\n" \
"v_mfma_f32_16x16x32_bf16 v[156:159], a[224:227], v[120:123], v[156:159]\n" \
"s_waitcnt vmcnt(63) lgkmcnt(4)\n" \
"s_nop 0\n" \
"v_mfma_f32_16x16x32_bf16 v[160:163], a[228:231], v[0:3], 0\n" \
"ds_read_b64_tr_b16 a[208:209], v227 offset:8704\n" \
"v_mfma_f32_16x16x32_bf16 v[160:163], a[232:235], v[8:11], v[160:163]\n" \
"ds_read_b64_tr_b16 a[210:211], v227 offset:10752\n" \
"s_mov_b32 s40, 606208\n" \
"buffer_load_dwordx4 a[176:179], v237, %[srdz], s40 offen sc1\n" \
"v_mfma_f32_16x16x32_bf16 v[164:167], a[228:231], v[32:35], 0\n" \
"ds_read_b64_tr_b16 a[212:213], v227 offset:12800\n" \
"v_mfma_f32_16x16x32_bf16 v[164:167], a[232:235], v[40:43], v[164:167]\n" \
"ds_read_b64_tr_b16 a[214:215], v227 offset:14848\n" \
"buffer_load_dwordx4 v214, %[srdt], s49 offen offset:3072 lds\n" \
"v_mfma_f32_16x16x32_bf16 v[168:171], a[228:231], v[64:67], 0\n" \
"v_mfma_f32_16x16x32_bf16 v[168:171], a[232:235], v[72:75], v[168:171]\n" \
"v_mfma_f32_16x16x32_bf16 v[172:175], a[228:231], v[96:99], 0\n" \
"ds_read_b64_tr_b16 a[216:217], v215 offset:16384\n" \
"v_mfma_f32_16x16x32_bf16 v[172:175], a[232:235], v[104:107], v[172:175]\n" \
"ds_read_b64_tr_b16 a[218:219], v215 offset:18432\n" \
"v_mfma_f32_16x16x32_bf16 v[160:163], a[236:239], v[16:19], v[160:163]\n" \
"ds_read_b64_tr_b16 a[220:221], v215 offset:20480\n" \
"v_mfma_f32_16x16x32_bf16 v[160:163], a[196:199], v[24:27], v[160:163]\n" \
"ds_read_b64_tr_b16 a[222:223], v215 offset:22528\n" \
"s_mov_b32 s40, 610304\n" \
"buffer_load_dwordx4 a[180:183], v237, %[srdz], s40 offen sc1\n" \
"v_mfma_f32_16x16x32_bf16 v[164:167], a[236:239], v[48:51], v[164:167]\n" \
"v_mfma_f32_16x16x32_bf16 v[164:167], a[196:199], v[56:59], v[164:167]\n" \
"v_mfma_f32_16x16x32_bf16 v[168:171], a[236:239], v[80:83], v[168:171]\n" \
"s_waitcnt vmcnt(63) lgkmcnt(0)\n" \
"s_barrier\n" \
"v_mfma_f32_16x16x32_bf16 v[168:171], a[196:199], v[88:91], v[168:171]\n" \
"v_mfma_f32_16x16x32_bf16 v[172:175], a[236:239], v[112:115], v[172:175]\n" \
"ds_read_b64_tr_b16 a[224:225], v215 offset:24576\n" \
"v_mfma_f32_16x16x32_bf16 v[172:175], a[196:199], v[120:123], v[172:175]\n" \
"s_waitcnt vmcnt(63) lgkmcnt(5)\n" \
"s_nop 0\n" \
"v_mfma_f32_16x16x32_bf16 v[176:179], a[200:203], v[0:3], 0\n" \
"ds_read_b64_tr_b16 a[226:227], v215 offset:26624\n" \
"v_mfma_f32_16x16x32_bf16 v[176:179], a[204:207], v[8:11], v[176:179]\n" \
"ds_read_b64_tr_b16 a[228:229], v215 offset:28672\n" \
"s_mov_b32 s40, 614400\n" \
"buffer_load_dwordx4 a[184:187], v237, %[srdz], s40 offen sc1\n" \
"v_mfma_f32_16x16x32_bf16 v[180:183], a[200:203], v[32:35], 0\n" \
"ds_read_b64_tr_b16 a[230:231], v215 offset:30720\n" \
"v_mfma_f32_16x16x32_bf16 v[180:183], a[204:207], v[40:43], v[180:183]\n" \
"s_add_u32 m0, %[m0b], 49152\n" \
"s_cmp_gt_u32 s48, 1\n" \
"s_cselect_b32 s49, 1, 2\n" \
"s_lshl_b32 s49, s49, 18\n" \
"s_or_b32 s49, s49, s59\n" \
"buffer_load_dwordx4 %[wo0], %[srdt], s49 offen offset:0 lds\n" \
"v_mfma_f32_16x16x32_bf16 v[184:187], a[200:203], v[64:67], 0\n" \
"v_mfma_f32_16x16x32_bf16 v[184:187], a[204:207], v[72:75], v[184:187]\n" \
"buffer_load_dwordx4 v212, %[srdt], s49 offen offset:1024 lds\n" \
"v_mfma_f32_16x16x32_bf16 v[188:191], a[200:203], v[96:99], 0\n" \
"ds_read_b64_tr_b16 a[232:233], v227 offset:16384\n" \
"v_mfma_f32_16x16x32_bf16 v[188:191], a[204:207], v[104:107], v[188:191]\n" \
"ds_read_b64_tr_b16 a[234:235], v227 offset:18432\n" \
"v_mfma_f32_16x16x32_bf16 v[176:179], a[208:211], v[16:19], v[176:179]\n" \
"ds_read_b64_tr_b16 a[236:237], v227 offset:20480\n" \
"v_mfma_f32_16x16x32_bf16 v[176:179], a[212:215], v[24:27], v[176:179]\n" \
"ds_read_b64_tr_b16 a[238:239], v227 offset:22528\n" \
"s_mov_b32 s40, 618496\n" \
"buffer_load_dwordx4 a[188:191], v237, %[srdz], s40 offen sc1\n" \
"v_mfma_f32_16x16x32_bf16 v[180:183], a[208:211], v[48:51], v[180:183]\n" \
"v_mfma_f32_16x16x32_bf16 v[180:183], a[212:215], v[56:59], v[180:183]\n" \
"v_mfma_f32_16x16x32_bf16 v[184:187], a[208:211], v[80:83], v[184:187]\n" \
"v_mfma_f32_16x16x32_bf16 v[184:187], a[212:215], v[88:91], v[184:187]\n" \
"buffer_load_dwordx4 v213, %[srdt], s49 offen offset:2048 lds\n" \
"v_mfma_f32_16x16x32_bf16 v[188:191], a[208:211], v[112:115], v[188:191]\n" \
"ds_read_b64_tr_b16 a[196:197], v227 offset:24576\n" \
"v_mfma_f32_16x16x32_bf16 v[188:191], a[212:215], v[120:123], v[188:191]\n" \
"s_waitcnt vmcnt(63) lgkmcnt(5)\n" \
"s_nop 0\n" \
"v_mfma_f32_16x16x32_bf16 v[4:7], a[216:219], v[0:3], 0\n" \
"ds_read_b64_tr_b16 a[198:199], v227 offset:26624\n" \
"v_mfma_f32_16x16x32_bf16 v[4:7], a[220:223], v[8:11], v[4:7]\n" \
"ds_read_b64_tr_b16 a[200:201], v227 offset:28672\n" \
"v_mfma_f32_16x16x32_bf16 v[36:39], a[216:219], v[32:35], 0\n" \
"ds_read_b64_tr_b16 a[202:203], v227 offset:30720\n" \
"v_mfma_f32_16x16x32_bf16 v[36:39], a[220:223], v[40:43], v[36:39]\n" \
"v_mfma_f32_16x16x32_bf16 v[68:71], a[216:219], v[64:67], 0\n" \
"buffer_load_dwordx4 v214, %[srdt], s49 offen offset:3072 lds\n" \
"v_mfma_f32_16x16x32_bf16 v[68:71], a[220:223], v[72:75], v[68:71]\n" \
"v_mfma_f32_16x16x32_bf16 v[100:103], a[216:219], v[96:99], 0\n" \
"ds_read_b64_tr_b16 a[204:205], v215 offset:16896\n" \
"v_mfma_f32_16x16x32_bf16 v[100:103], a[220:223], v[104:107], v[100:103]\n" \
"ds_read_b64_tr_b16 a[206:207], v215 offset:18944\n" \
"v_mfma_f32_16x16x32_bf16 v[4:7], a[224:227], v[16:19], v[4:7]\n" \
"ds_read_b64_tr_b16 a[208:209], v215 offset:20992\n" \
"v_mfma_f32_16x16x32_bf16 v[4:7], a[228:231], v[24:27], v[4:7]\n" \
"ds_read_b64_tr_b16 a[210:211], v215 offset:23040\n" \
"v_mfma_f32_16x16x32_bf16 v[36:39], a[224:227], v[48:51], v[36:39]\n" \
"v_mfma_f32_16x16x32_bf16 v[36:39], a[228:231], v[56:59], v[36:39]\n" \
"v_mfma_f32_16x16x32_bf16 v[68:71], a[224:227], v[80:83], v[68:71]\n" \
"v_mfma_f32_16x16x32_bf16 v[68:71], a[228:231], v[88:91], v[68:71]\n" \
"v_mfma_f32_16x16x32_bf16 v[100:103], a[224:227], v[112:115], v[100:103]\n" \
"ds_read_b64_tr_b16 a[212:213], v215 offset:25088\n" \
"v_mfma_f32_16x16x32_bf16 v[100:103], a[228:231], v[120:123], v[100:103]\n" \
"s_waitcnt vmcnt(63) lgkmcnt(5)\n" \
"s_nop 0\n" \
"v_mfma_f32_16x16x32_bf16 v[12:15], a[232:235], v[0:3], 0\n" \
"ds_read_b64_tr_b16 a[214:215], v215 offset:27136\n" \
"s_cmp_gt_u32 s48, 0\n" \
"s_cselect_b32 s50, 0, 1\n" \
"s_mul_i32 s50, s50, 152064\n" \
"buffer_load_dwordx4 a[64:67], %[oq0], %[srdz], s50 offen offset:0 sc1\n" \
"v_mov_b32_e32 v230, 0\n" \
"v_mov_b32_e32 v236, 0\n" \
"v_mfma_f32_16x16x32_bf16 v[12:15], a[236:239], v[8:11], v[12:15]\n" \
"ds_read_b64_tr_b16 a[216:217], v215 offset:29184\n" \
"v_mov_b32_e32 v222, 0\n" \
"v_mov_b32_e32 v223, 0\n" \
"v_mfma_f32_16x16x32_bf16 v[44:47], a[232:235], v[32:35], 0\n" \
"ds_read_b64_tr_b16 a[218:219], v215 offset:31232\n" \
"buffer_load_dwordx4 a[68:71], %[oq0], %[srdz], s50 offen offset:1024 sc1\n" \
"v_dot2c_f32_bf16_e32 v230, v0, v0\n" \
"v_dot2c_f32_bf16_e32 v236, v32, v32\n" \
"v_mfma_f32_16x16x32_bf16 v[44:47], a[236:239], v[40:43], v[44:47]\n" \
"v_dot2c_f32_bf16_e32 v222, v64, v64\n" \
"v_dot2c_f32_bf16_e32 v223, v96, v96\n" \
"v_mfma_f32_16x16x32_bf16 v[76:79], a[232:235], v[64:67], 0\n" \
"buffer_load_dwordx4 a[80:83], v208, %[srdz], s50 offen offset:0 sc1\n" \
"v_dot2c_f32_bf16_e32 v230, v1, v1\n" \
"v_dot2c_f32_bf16_e32 v236, v33, v33\n" \
"v_mfma_f32_16x16x32_bf16 v[76:79], a[236:239], v[72:75], v[76:79]\n" \
"v_dot2c_f32_bf16_e32 v222, v65, v65\n" \
"v_dot2c_f32_bf16_e32 v223, v97, v97\n" \
"v_mfma_f32_16x16x32_bf16 v[108:111], a[232:235], v[96:99], 0\n" \
"ds_read_b64_tr_b16 a[220:221], v227 offset:16896\n" \
"buffer_load_dwordx4 a[84:87], v208, %[srdz], s50 offen offset:1024 sc1\n" \
"v_dot2c_f32_bf16_e32 v230, v2, v2\n" \
"v_dot2c_f32_bf16_e32 v236, v34, v34\n" \
"v_mfma_f32_16x16x32_bf16 v[108:111], a[236:239], v[104:107], v[108:111]\n" \
"ds_read_b64_tr_b16 a[222:223], v227 offset:18944\n" \
"v_dot2c_f32_bf16_e32 v222, v66, v66\n" \
"v_dot2c_f32_bf16_e32 v223, v98, v98\n" \
"v_mfma_f32_16x16x32_bf16 v[12:15], a[196:199], v[16:19], v[12:15]\n" \
"ds_read_b64_tr_b16 a[224:225], v227 offset:25088\n" \
"buffer_load_dwordx4 a[96:99], v209, %[srdz], s50 offen offset:0 sc1\n" \
"v_dot2c_f32_bf16_e32 v230, v3, v3\n" \
"v_dot2c_f32_bf16_e32 v236, v35, v35\n" \
"v_mfma_f32_16x16x32_bf16 v[12:15], a[200:203], v[24:27], v[12:15]\n" \
"ds_read_b64_tr_b16 a[226:227], v227 offset:27136\n" \
"v_dot2c_f32_bf16_e32 v222, v67, v67\n" \
"v_dot2c_f32_bf16_e32 v223, v99, v99\n" \
"v_mfma_f32_16x16x32_bf16 v[44:47], a[196:199], v[48:51], v[44:47]\n" \
"ds_read_b64_tr_b16 a[228:229], v227 offset:20992\n" \
"buffer_load_dwordx4 a[100:103], v209, %[srdz], s50 offen offset:1024 sc1\n" \
"v_dot2c_f32_bf16_e32 v230, v8, v8\n" \
"v_dot2c_f32_bf16_e32 v236, v40, v40\n" \
"v_mfma_f32_16x16x32_bf16 v[44:47], a[200:203], v[56:59], v[44:47]\n" \
"ds_read_b64_tr_b16 a[230:231], v227 offset:23040\n" \
"v_dot2c_f32_bf16_e32 v222, v72, v72\n" \
"v_dot2c_f32_bf16_e32 v223, v104, v104\n" \
"v_mfma_f32_16x16x32_bf16 v[76:79], a[196:199], v[80:83], v[76:79]\n" \
"ds_read_b64_tr_b16 a[232:233], v227 offset:29184\n" \
"buffer_load_dwordx4 a[112:115], v210, %[srdz], s50 offen offset:0 sc1\n" \
"v_dot2c_f32_bf16_e32 v230, v9, v9\n" \
"v_dot2c_f32_bf16_e32 v236, v41, v41\n" \
"v_mfma_f32_16x16x32_bf16 v[76:79], a[200:203], v[88:91], v[76:79]\n" \
"ds_read_b64_tr_b16 a[234:235], v227 offset:31232\n" \
"v_dot2c_f32_bf16_e32 v222, v73, v73\n" \
"v_dot2c_f32_bf16_e32 v223, v105, v105\n" \
"v_mfma_f32_16x16x32_bf16 v[108:111], a[196:199], v[112:115], v[108:111]\n" \
"ds_read_b64_tr_b16 a[236:237], v215 offset:32768\n" \
"buffer_load_dwordx4 a[116:119], v210, %[srdz], s50 offen offset:1024 sc1\n" \
"v_dot2c_f32_bf16_e32 v230, v10, v10\n" \
"v_dot2c_f32_bf16_e32 v236, v42, v42\n" \
"v_mfma_f32_16x16x32_bf16 v[108:111], a[200:203], v[120:123], v[108:111]\n" \
"v_dot2c_f32_bf16_e32 v222, v74, v74\n" \
"v_dot2c_f32_bf16_e32 v223, v106, v106\n" \
"s_waitcnt vmcnt(63) lgkmcnt(13)\n" \
"s_nop 0\n" \
"v_mfma_f32_16x16x32_bf16 v[20:23], a[204:207], v[0:3], 0\n" \
"ds_read_b64_tr_b16 a[238:239], v215 offset:34816\n" \
"buffer_load_dwordx4 a[72:75], %[oq0], %[srdz], s50 offen offset:2048 sc1\n" \
"v_dot2c_f32_bf16_e32 v230, v11, v11\n" \
"v_dot2c_f32_bf16_e32 v236, v43, v43\n" \
"v_mfma_f32_16x16x32_bf16 v[20:23], a[208:211], v[8:11], v[20:23]\n" \
"ds_read_b64_tr_b16 a[196:197], v215 offset:36864\n" \
"v_dot2c_f32_bf16_e32 v222, v75, v75\n" \
"v_dot2c_f32_bf16_e32 v223, v107, v107\n" \
"v_mfma_f32_16x16x32_bf16 v[52:55], a[204:207], v[32:35], 0\n" \
"ds_read_b64_tr_b16 a[198:199], v215 offset:38912\n" \
"buffer_load_dwordx4 a[76:79], %[oq0], %[srdz], s50 offen offset:3072 sc1\n" \
"v_dot2c_f32_bf16_e32 v230, v16, v16\n" \
"v_dot2c_f32_bf16_e32 v236, v48, v48\n" \
"v_mfma_f32_16x16x32_bf16 v[52:55], a[208:211], v[40:43], v[52:55]\n" \
"v_dot2c_f32_bf16_e32 v222, v80, v80\n" \
"v_dot2c_f32_bf16_e32 v223, v112, v112\n" \
"v_mfma_f32_16x16x32_bf16 v[84:87], a[204:207], v[64:67], 0\n" \
"v_dot2c_f32_bf16_e32 v230, v17, v17\n" \
"v_dot2c_f32_bf16_e32 v236, v49, v49\n" \
"s_waitcnt vmcnt(63) lgkmcnt(4)\n" \
"s_barrier\n" \
"v_mfma_f32_16x16x32_bf16 v[84:87], a[208:211], v[72:75], v[84:87]\n" \
"buffer_load_dwordx4 a[88:91], v208, %[srdz], s50 offen offset:2048 sc1\n" \
"v_dot2c_f32_bf16_e32 v222, v81, v81\n" \
"v_dot2c_f32_bf16_e32 v223, v113, v113\n" \
"v_mfma_f32_16x16x32_bf16 v[116:119], a[204:207], v[96:99], 0\n" \
"ds_read_b64_tr_b16 a[200:201], v215 offset:40960\n" \
"v_dot2c_f32_bf16_e32 v230, v18, v18\n" \
"v_dot2c_f32_bf16_e32 v236, v50, v50\n" \
"v_mfma_f32_16x16x32_bf16 v[116:119], a[208:211], v[104:107], v[116:119]\n" \
"ds_read_b64_tr_b16 a[202:203], v215 offset:43008\n" \
"buffer_load_dwordx4 a[92:95], v208, %[srdz], s50 offen offset:3072 sc1\n" \
"v_dot2c_f32_bf16_e32 v222, v82, v82\n" \
"v_dot2c_f32_bf16_e32 v223, v114, v114\n" \
"v_mfma_f32_16x16x32_bf16 v[20:23], a[212:215], v[16:19], v[20:23]\n" \
"ds_read_b64_tr_b16 a[204:205], v215 offset:45056\n" \
"v_dot2c_f32_bf16_e32 v230, v19, v19\n" \
"v_dot2c_f32_bf16_e32 v236, v51, v51\n" \
"v_mfma_f32_16x16x32_bf16 v[20:23], a[216:219], v[24:27], v[20:23]\n" \
"ds_read_b64_tr_b16 a[206:207], v215 offset:47104\n" \
"buffer_load_dwordx4 a[104:107], v209, %[srdz], s50 offen offset:2048 sc1\n" \
"v_dot2c_f32_bf16_e32 v222, v83, v83\n" \
"v_dot2c_f32_bf16_e32 v223, v115, v115\n" \
"v_mfma_f32_16x16x32_bf16 v[52:55], a[212:215], v[48:51], v[52:55]\n" \
"v_dot2c_f32_bf16_e32 v230, v24, v24\n" \
"v_dot2c_f32_bf16_e32 v236, v56, v56\n" \
"s_waitcnt vmcnt(15)\n" \
"s_nop 0\n" \
"v_mfma_f32_16x16x32_bf16 v[232:235], a[176:179], a[176:179], 0\n" \
"v_mfma_f32_16x16x32_bf16 v[232:235], a[180:183], a[180:183], v[232:235]\n" \
"v_mfma_f32_16x16x32_bf16 v[232:235], a[184:187], a[184:187], v[232:235]\n" \
"v_mfma_f32_16x16x32_bf16 v[232:235], a[188:191], a[188:191], v[232:235]\n" \
"v_mfma_f32_16x16x32_bf16 v[52:55], a[216:219], v[56:59], v[52:55]\n" \
"buffer_load_dwordx4 a[108:111], v209, %[srdz], s50 offen offset:3072 sc1\n" \
"v_dot2c_f32_bf16_e32 v222, v88, v88\n" \
"v_dot2c_f32_bf16_e32 v223, v120, v120\n" \
"v_mfma_f32_16x16x32_bf16 v[84:87], a[212:215], v[80:83], v[84:87]\n" \
"v_dot2c_f32_bf16_e32 v230, v25, v25\n" \
"v_dot2c_f32_bf16_e32 v236, v57, v57\n" \
"v_mfma_f32_16x16x32_bf16 v[84:87], a[216:219], v[88:91], v[84:87]\n" \
"buffer_load_dwordx4 a[120:123], v210, %[srdz], s50 offen offset:2048 sc1\n" \
"v_dot2c_f32_bf16_e32 v222, v89, v89\n" \
"v_dot2c_f32_bf16_e32 v223, v121, v121\n" \
"v_mfma_f32_16x16x32_bf16 v[116:119], a[212:215], v[112:115], v[116:119]\n" \
"v_dot2c_f32_bf16_e32 v230, v26, v26\n" \
"v_dot2c_f32_bf16_e32 v236, v58, v58\n" \
"v_mfma_f32_16x16x32_bf16 v[216:219], a[176:179], v[0:3], 0\n" \
"v_mfma_f32_16x16x32_bf16 v[216:219], a[180:183], v[8:11], v[216:219]\n" \
"v_mfma_f32_16x16x32_bf16 v[216:219], a[184:187], v[16:19], v[216:219]\n" \
"v_mfma_f32_16x16x32_bf16 v[216:219], a[188:191], v[24:27], v[216:219]\n" \
"v_mfma_f32_16x16x32_bf16 v[116:119], a[216:219], v[120:123], v[116:119]\n" \
"v_dot2c_f32_bf16_e32 v222, v90, v90\n" \
"v_dot2c_f32_bf16_e32 v223, v122, v122\n" \
"s_waitcnt vmcnt(63) lgkmcnt(10)\n" \
"s_nop 0\n" \
"v_mfma_f32_16x16x32_bf16 v[28:31], a[220:223], v[0:3], 0\n" \
"buffer_load_dwordx4 a[124:127], v210, %[srdz], s50 offen offset:3072 sc1\n" \
"v_dot2c_f32_bf16_e32 v230, v27, v27\n" \
"v_dot2c_f32_bf16_e32 v236, v59, v59\n" \
"v_mfma_f32_16x16x32_bf16 v[28:31], a[228:231], v[8:11], v[28:31]\n" \
"s_sub_u32 s61, s48, 4\n" \
"s_cmp_lt_u32 s61, 2\n" \
"s_cselect_b64 vcc, -1, 0\n" \
"v_dot2c_f32_bf16_e32 v222, v91, v91\n" \
"v_dot2c_f32_bf16_e32 v223, v123, v123\n" \
"s_nop 0\n" \
"v_readlane_b32 s40, v232, 0\n" \
"v_mfma_f32_16x16x32_bf16 v[60:63], a[220:223], v[32:35], 0\n" \
"s_cbranch_vccz Lg5_%=\n" \
"s_nop 0\n" \
"buffer_store_dwordx4 v[0:3], %[oq0], %[srdz], s60 offen offset:0 sc1\n" \
"Lg5_%=:\n" \
"v_mfma_f32_16x16x32_bf16 v[232:235], a[176:179], v[32:35], 0\n" \
"v_mfma_f32_16x16x32_bf16 v[232:235], a[180:183], v[40:43], v[232:235]\n" \
"v_mfma_f32_16x16x32_bf16 v[232:235], a[184:187], v[48:51], v[232:235]\n" \
"v_mfma_f32_16x16x32_bf16 v[232:235], a[188:191], v[56:59], v[232:235]\n" \
"v_mfma_f32_16x16x32_bf16 v[60:63], a[228:231], v[40:43], v[60:63]\n" \
"v_mov_b32_dpp v231, v216 quad_perm:[0,1,2,3] row_mask:0x1 bank_mask:0xf\n" \
"v_mfma_f32_16x16x32_bf16 v[92:95], a[220:223], v[64:67], 0\n" \
"s_cmp_gt_u32 s48, 1\n" \
"s_cselect_b32 s50, 1, 2\n" \
"s_mul_i32 s50, s50, 152064\n" \
"buffer_load_dwordx4 a[0:3], %[oq0], %[srdz], s50 offen offset:0 sc1\n" \
"v_mfma_f32_16x16x32_bf16 v[92:95], a[228:231], v[72:75], v[92:95]\n" \
"v_mfma_f32_16x16x32_bf16 v[124:127], a[220:223], v[96:99], 0\n" \
"ds_read_b64_tr_b16 a[208:209], v227 offset:32768\n" \
"v_mfma_f32_16x16x32_bf16 v[216:219], a[176:179], v[64:67], 0\n" \
"v_mfma_f32_16x16x32_bf16 v[216:219], a[180:183], v[72:75], v[216:219]\n" \
"v_mfma_f32_16x16x32_bf16 v[216:219], a[184:187], v[80:83], v[216:219]\n" \
"v_mfma_f32_16x16x32_bf16 v[216:219], a[188:191], v[88:91], v[216:219]\n" \
"v_mfma_f32_16x16x32_bf16 v[124:127], a[228:231], v[104:107], v[124:127]\n" \
"ds_read_b64_tr_b16 a[210:211], v227 offset:34816\n" \
"v_mov_b32_dpp v231, v232 quad_perm:[0,1,2,3] row_mask:0x2 bank_mask:0xf\n" \
"s_waitcnt vmcnt(15) lgkmcnt(6)\n" \
"s_nop 0\n" \
"v_mfma_f32_16x16x32_bf16 v[128:131], a[236:239], a[64:67], v[192:195]\n" \
"ds_read_b64_tr_b16 a[212:213], v227 offset:36864\n" \
"v_mfma_f32_16x16x32_bf16 v[128:131], a[196:199], a[68:71], v[128:131]\n" \
"ds_read_b64_tr_b16 a[214:215], v227 offset:38912\n" \
"s_waitcnt vmcnt(13) lgkmcnt(15)\n" \
"s_nop 0\n" \
"v_mfma_f32_16x16x32_bf16 v[132:135], a[236:239], a[80:83], v[196:199]\n" \
"s_cbranch_vccz Lg6_%=\n" \
"s_nop 0\n" \
"buffer_store_dwordx4 v[8:11], %[oq0], %[srdz], s60 offen offset:1024 sc1\n" \
"Lg6_%=:\n" \
"v_mfma_f32_16x16x32_bf16 v[232:235], a[176:179], v[96:99], 0\n" \
"v_mfma_f32_16x16x32_bf16 v[232:235], a[180:183], v[104:107], v[232:235]\n" \
"v_mfma_f32_16x16x32_bf16 v[232:235], a[184:187], v[112:115], v[232:235]\n" \
"v_mfma_f32_16x16x32_bf16 v[232:235], a[188:191], v[120:123], v[232:235]\n" \
"v_mfma_f32_16x16x32_bf16 v[132:135], a[196:199], a[84:87], v[132:135]\n" \
"v_mov_b32_dpp v231, v216 quad_perm:[0,1,2,3] row_mask:0x4 bank_mask:0xf\n" \
"s_waitcnt vmcnt(13) lgkmcnt(15)\n" \
"s_bitcmp1_b32 0x30, s48\n" \
"s_cbranch_scc1 Lcw2_%=\n" \
"s_waitcnt vmcnt(11)\n" \
"Lcw2_%=:\n" \
"v_mfma_f32_16x16x32_bf16 v[136:139], a[236:239], a[96:99], v[200:203]\n" \
"buffer_load_dwordx4 a[4:7], %[oq0], %[srdz], s50 offen offset:1024 sc1\n" \
"v_mfma_f32_16x16x32_bf16 v[136:139], a[196:199], a[100:103], v[136:139]\n" \
"s_waitcnt vmcnt(12) lgkmcnt(15)\n" \
"s_bitcmp1_b32 0x30, s48\n" \
"s_cbranch_scc1 Lcw3_%=\n" \
"s_waitcnt vmcnt(10)\n" \
"Lcw3_%=:\n" \
"v_mfma_f32_16x16x32_bf16 v[140:143], a[236:239], a[112:115], v[204:207]\n" \
"ds_read_b64_tr_b16 a[216:217], v227 offset:40960\n" \
"v_mfma_f32_16x16x32_bf16 v[140:143], a[196:199], a[116:119], v[140:143]\n" \
"s_waitcnt vmcnt(10) lgkmcnt(5)\n" \
"s_bitcmp1_b32 0x30, s48\n" \
"s_cbranch_scc1 Lcw4_%=\n" \
"s_waitcnt vmcnt(8)\n" \
"Lcw4_%=:\n" \
"v_mfma_f32_16x16x32_bf16 v[128:131], a[200:203], a[72:75], v[128:131]\n" \
"ds_read_b64_tr_b16 a[218:219], v227 offset:43008\n" \
"v_mov_b32_dpp v231, v232 quad_perm:[0,1,2,3] row_mask:0x8 bank_mask:0xf\n" \
"buffer_load_dwordx4 a[8:11], %[oq0], %[srdz], s50 offen offset:2048 sc1\n" \
"v_mfma_f32_16x16x32_bf16 v[128:131], a[204:207], a[76:79], v[128:131]\n" \
"ds_read_b64_tr_b16 a[220:221], v227 offset:45056\n" \
"s_waitcnt vmcnt(9) lgkmcnt(15)\n" \
"s_bitcmp1_b32 0x30, s48\n" \
"s_cbranch_scc1 Lcw5_%=\n" \
"s_waitcnt vmcnt(7)\n" \
"Lcw5_%=:\n" \
"v_mfma_f32_16x16x32_bf16 v[132:135], a[200:203], a[88:91], v[132:135]\n" \
"ds_read_b64_tr_b16 a[222:223], v227 offset:47104\n" \
"v_cmp_u_f32_e64 s[42:43], v231, v231\n" \
"v_readlane_b32 s41, %[tlo], 33\n" \
"v_mfma_f32_16x16x32_bf16 v[132:135], a[204:207], a[92:95], v[132:135]\n" \
"s_cbranch_vccz Lg7_%=\n" \
"s_nop 0\n" \
"buffer_store_dwordx4 v[16:19], %[oq0], %[srdz], s60 offen offset:2048 sc1\n" \
"Lg7_%=:\n" \
"v_permlane32_swap_b32_e32 v230, v222\n" \
"v_permlane32_swap_b32_e32 v236, v223\n" \
"s_waitcnt vmcnt(6) lgkmcnt(14)\n" \
"s_bitcmp1_b32 0x30, s48\n" \
"s_cbranch_scc1 Lcw6_%=\n" \
"s_waitcnt vmcnt(3)\n" \
"Lcw6_%=:\n" \
"v_mfma_f32_16x16x32_bf16 v[136:139], a[200:203], a[104:107], v[136:139]\n" \
"v_add_f32_e32 v230, v230, v222\n" \
"v_add_f32_e32 v236, v236, v223\n" \
"v_mfma_f32_16x16x32_bf16 v[136:139], a[204:207], a[108:111], v[136:139]\n" \
"v_lshrrev_b32_e64 v219, 5, v224\n" \
"buffer_load_dwordx4 a[12:15], %[oq0], %[srdz], s50 offen offset:3072 sc1\n" \
"v_mfma_f32_16x16x32_bf16 v[140:143], a[200:203], a[120:123], v[140:143]\n" \
"ds_read_b64_tr_b16 a[228:229], v215 offset:33280\n" \
"v_permlane16_swap_b32_e32 v230, v236\n" \
"v_add_f32_e32 v230, v230, v236\n" \
"v_mfma_f32_16x16x32_bf16 v[140:143], a[204:207], a[124:127], v[140:143]\n" \
"ds_read_b64_tr_b16 a[230:231], v215 offset:35328\n" \
"v_mul_f32_e32 v230, s40, v230\n" \
"v_mul_f32_e32 v230, s63, v230\n" \
"v_mfma_f32_16x16x32_bf16 v[28:31], a[224:227], v[16:19], v[28:31]\n" \
"s_cmp_lg_u64 s[42:43], 0\n" \
"s_cbranch_scc1 Ls0_%=\n" \
"Ls0b_%=:\n" \
"v_cvt_pk_bf16_f32 v231, v230, v231\n" \
"buffer_load_dwordx4 a[16:19], v208, %[srdz], s50 offen offset:0 sc1\n" \
"v_mfma_f32_16x16x32_bf16 v[28:31], a[232:235], v[24:27], v[28:31]\n" \
"v_or_b32_e64 v231, 1, v231\n" \
"v_mfma_f32_16x16x32_bf16 v[60:63], a[224:227], v[48:51], v[60:63]\n" \
"v_mfma_f32_16x16x32_bf16 v[60:63], a[232:235], v[56:59], v[60:63]\n" \
"s_cbranch_vccz Lg8_%=\n" \
"s_nop 0\n" \
"buffer_store_dwordx4 v[24:27], %[oq0], %[srdz], s60 offen offset:3072 sc1\n" \
"Lg8_%=:\n" \
"v_mfma_f32_16x16x32_bf16 v[92:95], a[224:227], v[80:83], v[92:95]\n" \
"v_mfma_f32_16x16x32_bf16 v[92:95], a[232:235], v[88:91], v[92:95]\n" \
"v_cmp_u_f32_e64 s[44:45], v128, v132\n" \
"buffer_store_dword v231, v219, %[srdz], s41 offen sc1\n" \
"v_mfma_f32_16x16x32_bf16 v[124:127], a[224:227], v[112:115], v[124:127]\n" \
"v_mfma_f32_16x16x32_bf16 v[124:127], a[232:235], v[120:123], v[124:127]\n" \
"v_cmp_u_f32_e64 s[56:57], v136, v140\n" \
"s_or_b64 s[56:57], s[56:57], s[44:45]\n" \
"s_cbranch_scc1 Lsp1_%=\n" \
"Lbk1_%=:\n" \
"s_waitcnt vmcnt(63) lgkmcnt(2)\n" \
"s_nop 0\n" \
"v_mfma_f32_16x16x32_bf16 v[144:147], a[208:211], a[64:67], v[144:147]\n" \
"ds_read_b64_tr_b16 a[224:225], v215 offset:37376\n" \
"buffer_load_dwordx4 a[20:23], v208, %[srdz], s50 offen offset:1024 sc1\n" \
"v_mfma_f32_16x16x32_bf16 v[144:147], a[212:215], a[68:71], v[144:147]\n" \
"ds_read_b64_tr_b16 a[226:227], v215 offset:39424\n" \
"v_mfma_f32_16x16x32_bf16 v[148:151], a[208:211], a[80:83], v[148:151]\n" \
"ds_read_b64_tr_b16 a[196:197], v215 offset:41472\n" \
"v_mfma_f32_16x16x32_bf16 v[148:151], a[212:215], a[84:87], v[148:151]\n" \
"ds_read_b64_tr_b16 a[198:199], v215 offset:43520\n" \
"v_mfma_f32_16x16x32_bf16 v[152:155], a[208:211], a[96:99], v[152:155]\n" \
"ds_read_b64_tr_b16 a[200:201], v215 offset:45568\n" \
"s_cbranch_vccz Lg9_%=\n" \
"s_nop 0\n" \
"buffer_store_dwordx4 v[32:35], v208, %[srdz], s60 offen offset:0 sc1\n" \
"Lg9_%=:\n" \
"v_mfma_f32_16x16x32_bf16 v[152:155], a[212:215], a[100:103], v[152:155]\n" \
"ds_read_b64_tr_b16 a[202:203], v215 offset:47616\n" \
"v_mfma_f32_16x16x32_bf16 v[156:159], a[208:211], a[112:115], v[156:159]\n" \
"ds_read_b64_tr_b16 a[204:205], v227 offset:33280\n" \
"buffer_load_dwordx4 a[24:27], v208, %[srdz], s50 offen offset:2048 sc1\n" \
"v_mfma_f32_16x16x32_bf16 v[156:159], a[212:215], a[116:119], v[156:159]\n" \
"ds_read_b64_tr_b16 a[206:207], v227 offset:35328\n" \
"v_mfma_f32_16x16x32_bf16 v[144:147], a[216:219], a[72:75], v[144:147]\n" \
"ds_read_b64_tr_b16 a[232:233], v227 offset:37376\n" \
"v_mfma_f32_16x16x32_bf16 v[144:147], a[220:223], a[76:79], v[144:147]\n" \
"ds_read_b64_tr_b16 a[234:235], v227 offset:39424\n" \
"v_mfma_f32_16x16x32_bf16 v[148:151], a[216:219], a[88:91], v[148:151]\n" \
"buffer_load_dwordx4 a[28:31], v208, %[srdz], s50 offen offset:3072 sc1\n" \
"v_mfma_f32_16x16x32_bf16 v[148:151], a[220:223], a[92:95], v[148:151]\n" \
"v_mfma_f32_16x16x32_bf16 v[152:155], a[216:219], a[104:107], v[152:155]\n" \
".if . - adaln_body != 45056\n" \
".error \"size model\"\n" \
".endif\n" \
"Lk10_%=:\n" \
"s_cbranch_execz Lk11_%=\n" \
"s_nop 0\n" \
"s_cbranch_vccz Lg10_%=\n" \
"s_nop 0\n" \
"buffer_store_dwordx4 v[40:43], v208, %[srdz], s60 offen offset:1024 sc1\n" \
"Lg10_%=:\n" \
"v_mfma_f32_16x16x32_bf16 v[152:155], a[220:223], a[108:111], v[152:155]\n" \
"v_mfma_f32_16x16x32_bf16 v[156:159], a[216:219], a[120:123], v[156:159]\n" \
"ds_read_b64_tr_b16 a[236:237], v227 offset:41472\n" \
"buffer_load_dwordx4 a[32:35], v209, %[srdz], s50 offen offset:0 sc1\n" \
"v_mfma_f32_16x16x32_bf16 v[156:159], a[220:223], a[124:127], v[156:159]\n" \
"s_waitcnt vmcnt(63) lgkmcnt(5)\n" \
"s_nop 0\n" \
"v_mfma_f32_16x16x32_bf16 v[160:163], a[228:231], a[64:67], v[160:163]\n" \
"ds_read_b64_tr_b16 a[238:239], v227 offset:43520\n" \
"v_mfma_f32_16x16x32_bf16 v[160:163], a[224:227], a[68:71], v[160:163]\n" \
"ds_read_b64_tr_b16 a[208:209], v227 offset:45568\n" \
"v_mfma_f32_16x16x32_bf16 v[164:167], a[228:231], a[80:83], v[164:167]\n" \
"ds_read_b64_tr_b16 a[210:211], v227 offset:47616\n" \
"buffer_load_dwordx4 a[36:39], v209, %[srdz], s50 offen offset:1024 sc1\n" \
"v_mfma_f32_16x16x32_bf16 v[164:167], a[224:227], a[84:87], v[164:167]\n" \
"v_mfma_f32_16x16x32_bf16 v[168:171], a[228:231], a[96:99], v[168:171]\n" \
"v_mfma_f32_16x16x32_bf16 v[168:171], a[224:227], a[100:103], v[168:171]\n" \
"s_cbranch_vccz Lg11_%=\n" \
"s_nop 0\n" \
"buffer_store_dwordx4 v[48:51], v208, %[srdz], s60 offen offset:2048 sc1\n" \
"Lg11_%=:\n" \
"v_mfma_f32_16x16x32_bf16 v[172:175], a[228:231], a[112:115], v[172:175]\n" \
"v_mfma_f32_16x16x32_bf16 v[172:175], a[224:227], a[116:119], v[172:175]\n" \
"buffer_load_dwordx4 a[40:43], v209, %[srdz], s50 offen offset:2048 sc1\n" \
"v_mfma_f32_16x16x32_bf16 v[160:163], a[196:199], a[72:75], v[160:163]\n" \
"v_mfma_f32_16x16x32_bf16 v[160:163], a[200:203], a[76:79], v[160:163]\n" \
"buffer_load_dwordx4 a[44:47], v209, %[srdz], s50 offen offset:3072 sc1\n" \
"v_mfma_f32_16x16x32_bf16 v[164:167], a[196:199], a[88:91], v[164:167]\n" \
"v_mfma_f32_16x16x32_bf16 v[164:167], a[200:203], a[92:95], v[164:167]\n" \
"buffer_load_dwordx4 a[48:51], v210, %[srdz], s50 offen offset:0 sc1\n" \
"v_mfma_f32_16x16x32_bf16 v[168:171], a[196:199], a[104:107], v[168:171]\n" \
"s_waitcnt vmcnt(59) lgkmcnt(0)\n" \
"s_bitcmp1_b32 0xf, s48\n" \
"s_cbranch_scc1 Lcw7_%=\n" \
"s_waitcnt vmcnt(50)\n" \
"s_bitcmp1_b32 0x30, s48\n" \
"s_cbranch_scc1 Lcw7_%=\n" \
"s_waitcnt vmcnt(43)\n" \
"Lcw7_%=:\n" \
"s_barrier\n" \
"v_mfma_f32_16x16x32_bf16 v[168:171], a[200:203], a[108:111], v[168:171]\n" \
"ds_read_b64_tr_b16 a[212:213], v220 offset:32768\n" \
"s_cbranch_vccz Lg12_%=\n" \
"s_nop 0\n" \
"buffer_store_dwordx4 v[56:59], v208, %[srdz], s60 offen offset:3072 sc1\n" \
"Lg12_%=:\n" \
"v_mfma_f32_16x16x32_bf16 v[172:175], a[196:199], a[120:123], v[172:175]\n" \
"ds_read_b64_tr_b16 a[214:215], v220 offset:34816\n" \
"v_mfma_f32_16x16x32_bf16 v[172:175], a[200:203], a[124:127], v[172:175]\n" \
"s_waitcnt vmcnt(63) lgkmcnt(2)\n" \
"s_nop 0\n" \
"v_mfma_f32_16x16x32_bf16 v[176:179], a[204:207], a[64:67], v[176:179]\n" \
"ds_read_b64_tr_b16 a[216:217], v220 offset:36864\n" \
"buffer_load_dwordx4 a[52:55], v210, %[srdz], s50 offen offset:1024 sc1\n" \
"v_mfma_f32_16x16x32_bf16 v[176:179], a[232:235], a[68:71], v[176:179]\n" \
"ds_read_b64_tr_b16 a[218:219], v220 offset:38912\n" \
"v_mfma_f32_16x16x32_bf16 v[180:183], a[204:207], a[80:83], v[180:183]\n" \
"ds_read_b64_tr_b16 a[220:221], v220 offset:40960\n" \
"v_mfma_f32_16x16x32_bf16 v[180:183], a[232:235], a[84:87], v[180:183]\n" \
"ds_read_b64_tr_b16 a[222:223], v220 offset:43008\n" \
"v_mfma_f32_16x16x32_bf16 v[184:187], a[204:207], a[96:99], v[184:187]\n" \
"ds_read_b64_tr_b16 a[228:229], v220 offset:45056\n" \
"buffer_load_dwordx4 a[56:59], v210, %[srdz], s50 offen offset:2048 sc1\n" \
"v_mfma_f32_16x16x32_bf16 v[184:187], a[232:235], a[100:103], v[184:187]\n" \
"ds_read_b64_tr_b16 a[230:231], v220 offset:47104\n" \
"v_mfma_f32_16x16x32_bf16 v[188:191], a[204:207], a[112:115], v[188:191]\n" \
"ds_read_b64_tr_b16 a[224:225], v221 offset:32768\n" \
"s_cbranch_vccz Lg13_%=\n" \
"s_nop 0\n" \
"buffer_store_dwordx4 v[64:67], v209, %[srdz], s60 offen offset:0 sc1\n" \
"Lg13_%=:\n" \
"v_mfma_f32_16x16x32_bf16 v[188:191], a[232:235], a[116:119], v[188:191]\n" \
"ds_read_b64_tr_b16 a[226:227], v221 offset:34816\n" \
"v_mfma_f32_16x16x32_bf16 v[176:179], a[236:239], a[72:75], v[176:179]\n" \
"ds_read_b64_tr_b16 a[196:197], v221 offset:36864\n" \
"buffer_load_dwordx4 a[60:63], v210, %[srdz], s50 offen offset:3072 sc1\n" \
"v_mfma_f32_16x16x32_bf16 v[176:179], a[208:211], a[76:79], v[176:179]\n" \
"ds_read_b64_tr_b16 a[198:199], v221 offset:38912\n" \
"v_mfma_f32_16x16x32_bf16 v[180:183], a[236:239], a[88:91], v[180:183]\n" \
"s_cmp_gt_u32 s48, 2\n" \
"s_cselect_b32 s50, 2, 3\n" \
"s_mul_i32 s50, s50, 152064\n" \
"buffer_load_dwordx4 a[128:131], %[oq0], %[srdz], s50 offen offset:0 sc1\n" \
"v_mfma_f32_16x16x32_bf16 v[180:183], a[208:211], a[92:95], v[180:183]\n" \
"v_mfma_f32_16x16x32_bf16 v[184:187], a[236:239], a[104:107], v[184:187]\n" \
"v_mfma_f32_16x16x32_bf16 v[184:187], a[208:211], a[108:111], v[184:187]\n" \
"v_mfma_f32_16x16x32_bf16 v[188:191], a[236:239], a[120:123], v[188:191]\n" \
"ds_read_b64_tr_b16 a[200:201], v221 offset:40960\n" \
"s_cbranch_vccz Lg14_%=\n" \
"s_nop 0\n" \
"buffer_store_dwordx4 v[72:75], v209, %[srdz], s60 offen offset:1024 sc1\n" \
"Lg14_%=:\n" \
"v_mfma_f32_16x16x32_bf16 v[188:191], a[208:211], a[124:127], v[188:191]\n" \
"s_waitcnt vmcnt(63) lgkmcnt(5)\n" \
"s_nop 0\n" \
"v_mfma_f32_16x16x32_bf16 v[4:7], a[212:215], a[64:67], v[4:7]\n" \
"ds_read_b64_tr_b16 a[202:203], v221 offset:43008\n" \
"v_mfma_f32_16x16x32_bf16 v[4:7], a[216:219], a[68:71], v[4:7]\n" \
"ds_read_b64_tr_b16 a[204:205], v221 offset:45056\n" \
"s_add_u32 m0, %[m0b], 0\n" \
"s_cmp_gt_u32 s48, 2\n" \
"s_cselect_b32 s49, 2, 3\n" \
"s_lshl_b32 s49, s49, 18\n" \
"s_or_b32 s49, s49, s58\n" \
"s_nop 0\n" \
"buffer_load_dwordx4 %[wo0], %[srdt], s49 offen offset:0 lds\n" \
"v_mfma_f32_16x16x32_bf16 v[36:39], a[212:215], a[80:83], v[36:39]\n" \
"ds_read_b64_tr_b16 a[206:207], v221 offset:47104\n" \
"v_mfma_f32_16x16x32_bf16 v[36:39], a[216:219], a[84:87], v[36:39]\n" \
"buffer_load_dwordx4 a[132:135], %[oq0], %[srdz], s50 offen offset:1024 sc1\n" \
"v_mfma_f32_16x16x32_bf16 v[68:71], a[212:215], a[96:99], v[68:71]\n" \
"v_mfma_f32_16x16x32_bf16 v[68:71], a[216:219], a[100:103], v[68:71]\n" \
"v_mfma_f32_16x16x32_bf16 v[100:103], a[212:215], a[112:115], v[100:103]\n" \
"ds_read_b64_tr_b16 a[232:233], v220 offset:33280\n" \
"v_mfma_f32_16x16x32_bf16 v[100:103], a[216:219], a[116:119], v[100:103]\n" \
"ds_read_b64_tr_b16 a[234:235], v220 offset:35328\n" \
"s_cbranch_vccz Lg15_%=\n" \
"s_nop 0\n" \
"buffer_store_dwordx4 v[80:83], v209, %[srdz], s60 offen offset:2048 sc1\n" \
"Lg15_%=:\n" \
"v_mfma_f32_16x16x32_bf16 v[4:7], a[220:223], a[72:75], v[4:7]\n" \
"ds_read_b64_tr_b16 a[236:237], v220 offset:37376\n" \
"s_lshl_b32 s40, %[wid], 12\n" \
"s_nop 0\n" \
"s_mov_b32 s43, 0xffff0000\n" \
"v_mfma_f32_16x16x32_bf16 v[4:7], a[228:231], a[76:79], v[4:7]\n" \
"ds_read_b64_tr_b16 a[238:239], v220 offset:39424\n" \
"buffer_load_dwordx4 a[136:139], %[oq0], %[srdz], s50 offen offset:2048 sc1\n" \
"v_mfma_f32_16x16x32_bf16 v[36:39], a[220:223], a[88:91], v[36:39]\n" \
"v_lshrrev_b32_e32 v222, 7, v224\n" \
"s_sub_u32 s40, %[m0b], s40\n" \
"v_mfma_f32_16x16x32_bf16 v[36:39], a[228:231], a[92:95], v[36:39]\n" \
"v_readlane_b32 s41, %[tlo], 26\n" \
"v_mfma_f32_16x16x32_bf16 v[68:71], a[220:223], a[104:107], v[68:71]\n" \
"v_readlane_b32 s42, %[thi], 26\n" \
"v_mfma_f32_16x16x32_bf16 v[68:71], a[228:231], a[108:111], v[68:71]\n" \
"buffer_load_dwordx4 a[140:143], %[oq0], %[srdz], s50 offen offset:3072 sc1\n" \
"v_mfma_f32_16x16x32_bf16 v[100:103], a[220:223], a[120:123], v[100:103]\n" \
"ds_read_b64_tr_b16 a[208:209], v220 offset:41472\n" \
"v_lshrrev_b32_e64 v217, 3, v224\n" \
"v_mfma_f32_16x16x32_bf16 v[100:103], a[228:231], a[124:127], v[100:103]\n" \
"s_waitcnt vmcnt(63) lgkmcnt(5)\n" \
"s_nop 0\n" \
"v_mfma_f32_16x16x32_bf16 v[12:15], a[224:227], a[64:67], v[12:15]\n" \
"ds_read_b64_tr_b16 a[210:211], v220 offset:43520\n" \
"s_cbranch_vccz Lg16_%=\n" \
"s_nop 0\n" \
"buffer_store_dwordx4 v[88:91], v209, %[srdz], s60 offen offset:3072 sc1\n" \
"Lg16_%=:\n" \
"v_mfma_f32_16x16x32_bf16 v[12:15], a[196:199], a[68:71], v[12:15]\n" \
"ds_read_b64_tr_b16 a[212:213], v220 offset:45568\n" \
"v_lshrrev_b32_e64 v219, 5, v222\n" \
"v_mfma_f32_16x16x32_bf16 v[44:47], a[224:227], a[80:83], v[44:47]\n" \
"ds_read_b64_tr_b16 a[214:215], v220 offset:47616\n" \
"buffer_load_dwordx4 v212, %[srdt], s49 offen offset:1024 lds\n" \
"v_mfma_f32_16x16x32_bf16 v[44:47], a[196:199], a[84:87], v[44:47]\n" \
"v_bfe_u32 v235, v222, 2, 1\n" \
"v_mfma_f32_16x16x32_bf16 v[76:79], a[224:227], a[96:99], v[76:79]\n" \
"v_lshl_or_b32 v219, v219, 1, v235\n" \
"v_mfma_f32_16x16x32_bf16 v[76:79], a[196:199], a[100:103], v[76:79]\n" \
"buffer_load_dwordx4 a[144:147], v208, %[srdz], s50 offen offset:0 sc1\n" \
"v_mfma_f32_16x16x32_bf16 v[108:111], a[224:227], a[112:115], v[108:111]\n" \
"ds_read_b64_tr_b16 a[216:217], v221 offset:33280\n" \
"v_add_u32_e64 v235, s41, v235\n" \
"v_mfma_f32_16x16x32_bf16 v[108:111], a[196:199], a[116:119], v[108:111]\n" \
"ds_read_b64_tr_b16 a[218:219], v221 offset:35328\n" \
"v_sub_u32_e32 %[ta0], 0x7ff, v235\n" \
"v_mfma_f32_16x16x32_bf16 v[12:15], a[200:203], a[72:75], v[12:15]\n" \
"ds_read_b64_tr_b16 a[220:221], v221 offset:41472\n" \
"s_cbranch_vccz Lg17_%=\n" \
"s_nop 0\n" \
"buffer_store_dwordx4 v[96:99], v210, %[srdz], s60 offen offset:0 sc1\n" \
"Lg17_%=:\n" \
"v_mfma_f32_16x16x32_bf16 v[12:15], a[204:207], a[76:79], v[12:15]\n" \
"ds_read_b64_tr_b16 a[222:223], v221 offset:43520\n" \
"v_and_b32_e64 v234, 16, v222\n" \
"v_mfma_f32_16x16x32_bf16 v[44:47], a[200:203], a[88:91], v[44:47]\n" \
"ds_read_b64_tr_b16 a[228:229], v221 offset:37376\n" \
"v_and_b32_e64 v233, 1, v222\n" \
"v_mfma_f32_16x16x32_bf16 v[44:47], a[204:207], a[92:95], v[44:47]\n" \
"ds_read_b64_tr_b16 a[230:231], v221 offset:39424\n" \
"v_cmp_eq_u32_e64 s[68:69], 1, v233\n" \
"v_mfma_f32_16x16x32_bf16 v[76:79], a[200:203], a[104:107], v[76:79]\n" \
"ds_read_b64_tr_b16 a[224:225], v221 offset:45568\n" \
"s_lshl_b32 s52, %[wid], 2\n" \
"s_nop 0\n" \
"v_mfma_f32_16x16x32_bf16 v[76:79], a[204:207], a[108:111], v[76:79]\n" \
"ds_read_b64_tr_b16 a[226:227], v221 offset:47616\n" \
"v_cndmask_b32_e64 v234, v234, 32, s[68:69]\n" \
"v_mfma_f32_16x16x32_bf16 v[108:111], a[200:203], a[120:123], v[108:111]\n" \
"buffer_load_dwordx4 a[148:151], v208, %[srdz], s50 offen offset:1024 sc1\n" \
"v_mfma_f32_16x16x32_bf16 v[108:111], a[204:207], a[124:127], v[108:111]\n" \
"s_waitcnt vmcnt(63) lgkmcnt(12)\n" \
"s_nop 0\n" \
"v_mfma_f32_16x16x32_bf16 v[20:23], a[232:235], a[64:67], v[20:23]\n" \
"s_cbranch_vccz Lg18_%=\n" \
"s_nop 0\n" \
"buffer_store_dwordx4 v[104:107], v210, %[srdz], s60 offen offset:1024 sc1\n" \
"Lg18_%=:\n" \
"v_mfma_f32_16x16x32_bf16 v[20:23], a[236:239], a[68:71], v[20:23]\n" \
"s_add_u32 s52, s52, s40\n" \
"s_nop 0\n" \
"v_mfma_f32_16x16x32_bf16 v[52:55], a[232:235], a[80:83], v[52:55]\n" \
"buffer_load_dwordx4 v213, %[srdt], s49 offen offset:2048 lds\n" \
"v_mfma_f32_16x16x32_bf16 v[52:55], a[236:239], a[84:87], v[52:55]\n" \
"v_mad_u32_u24 v228, v219, 48, v234\n" \
"s_add_u32 s63, s52, 29184\n" \
"v_mfma_f32_16x16x32_bf16 v[84:87], a[232:235], a[96:99], v[84:87]\n" \
"s_waitcnt vmcnt(63) lgkmcnt(0)\n" \
"s_bitcmp1_b32 0x30, s48\n" \
"s_cbranch_scc1 Lcw8_%=\n" \
"s_waitcnt vmcnt(49)\n" \
"Lcw8_%=:\n" \
"s_barrier\n" \
"s_nop 0\n" \
"v_mfma_f32_16x16x32_bf16 v[84:87], a[236:239], a[100:103], v[84:87]\n" \
"ds_read_b64_tr_b16 a[196:197], v220 offset:49152\n" \
"v_add_u32_e64 v228, s63, v228\n" \
"v_mfma_f32_16x16x32_bf16 v[116:119], a[232:235], a[112:115], v[116:119]\n" \
"ds_read_b64_tr_b16 a[198:199], v220 offset:51200\n" \
"buffer_load_dwordx4 a[152:155], v208, %[srdz], s50 offen offset:2048 sc1\n" \
"v_mfma_f32_16x16x32_bf16 v[116:119], a[236:239], a[116:119], v[116:119]\n" \
"ds_read_b64_tr_b16 a[200:201], v220 offset:53248\n" \
"v_and_b32_e64 v216, 15, v222\n" \
"v_mfma_f32_16x16x32_bf16 v[20:23], a[208:211], a[72:75], v[20:23]\n" \
"ds_read_b64_tr_b16 a[202:203], v220 offset:55296\n" \
"v_lshrrev_b32_e64 v218, 4, v222\n" \
"v_mfma_f32_16x16x32_bf16 v[20:23], a[212:215], a[76:79], v[20:23]\n" \
"ds_read_b64_tr_b16 a[204:205], v220 offset:57344\n" \
"s_cbranch_vccz Lg19_%=\n" \
"s_nop 0\n" \
"buffer_store_dwordx4 v[112:115], v210, %[srdz], s60 offen offset:2048 sc1\n" \
"Lg19_%=:\n" \
"v_mfma_f32_16x16x32_bf16 v[52:55], a[208:211], a[88:91], v[52:55]\n" \
"ds_read_b64_tr_b16 a[206:207], v220 offset:59392\n" \
"v_lshrrev_b32_e64 v230, 3, v216\n" \
"v_mfma_f32_16x16x32_bf16 v[52:55], a[212:215], a[92:95], v[52:55]\n" \
"ds_read_b64_tr_b16 a[232:233], v220 offset:61440\n" \
"v_lshl_or_b32 v230, v230, 1, v233\n" \
"v_mfma_f32_16x16x32_bf16 v[84:87], a[208:211], a[104:107], v[84:87]\n" \
"ds_read_b64_tr_b16 a[234:235], v220 offset:63488\n" \
"s_add_u32 s63, s40, 29184\n" \
"v_mfma_f32_16x16x32_bf16 v[84:87], a[212:215], a[108:111], v[84:87]\n" \
"buffer_load_dwordx4 a[156:159], v208, %[srdz], s50 offen offset:3072 sc1\n" \
"v_mfma_f32_16x16x32_bf16 v[116:119], a[208:211], a[120:123], v[116:119]\n" \
"v_mad_u32_u24 %[vg0], v230, 48, s63\n" \
"s_add_u32 s63, s52, 25600\n" \
"v_mfma_f32_16x16x32_bf16 v[116:119], a[212:215], a[124:127], v[116:119]\n" \
"s_waitcnt vmcnt(20) lgkmcnt(4)\n" \
"s_bitcmp1_b32 0x30, s48\n" \
"s_cbranch_scc1 Lcw9_%=\n" \
"s_waitcnt vmcnt(13)\n" \
"Lcw9_%=:\n" \
"v_mfma_f32_16x16x32_bf16 v[28:31], a[216:219], a[64:67], v[28:31]\n" \
"v_lshlrev_b32_e64 v235, 6, v218\n" \
"v_mfma_f32_16x16x32_bf16 v[28:31], a[228:231], a[68:71], v[28:31]\n" \
"s_cbranch_vccz Lg20_%=\n" \
"s_nop 0\n" \
"buffer_store_dwordx4 v[120:123], v210, %[srdz], s60 offen offset:3072 sc1\n" \
"Lg20_%=:\n" \
"v_mfma_f32_16x16x32_bf16 v[60:63], a[216:219], a[80:83], v[60:63]\n" \
"v_add_u32_e32 v234, s63, v235\n" \
"s_movk_i32 s52, 272\n" \
"v_mfma_f32_16x16x32_bf16 v[60:63], a[228:231], a[84:87], v[60:63]\n" \
"buffer_load_dwordx4 v214, %[srdt], s49 offen offset:3072 lds\n" \
"v_mfma_f32_16x16x32_bf16 v[92:95], a[216:219], a[96:99], v[92:95]\n" \
"v_mad_u32_u24 %[kd0], v216, s52, v234\n" \
"v_mfma_f32_16x16x32_bf16 v[92:95], a[228:231], a[100:103], v[92:95]\n" \
"buffer_load_dwordx4 a[160:163], v209, %[srdz], s50 offen offset:0 sc1\n" \
"v_mfma_f32_16x16x32_bf16 v[124:127], a[216:219], a[112:115], v[124:127]\n" \
"ds_read_b64_tr_b16 a[236:237], v221 offset:49152\n" \
"v_bfe_u32 v234, v222, 1, 2\n" \
"v_mfma_f32_16x16x32_bf16 v[124:127], a[228:231], a[116:119], v[124:127]\n" \
"ds_read_b64_tr_b16 a[238:239], v221 offset:51200\n" \
"v_lshl_add_u32 %[vx0], v234, 4, v235\n" \
"v_mfma_f32_16x16x32_bf16 v[192:195], a[196:199], a[0:3], v[128:131]\n" \
"ds_read_b64_tr_b16 a[208:209], v221 offset:53248\n" \
"s_add_u32 s63, s40, 25600\n" \
"v_mfma_f32_16x16x32_bf16 v[192:195], a[200:203], a[4:7], v[192:195]\n" \
"ds_read_b64_tr_b16 a[210:211], v221 offset:55296\n" \
"v_add_u32_e64 %[vx0], s63, %[vx0]\n" \
"v_mfma_f32_16x16x32_bf16 v[196:199], a[196:199], a[16:19], v[132:135]\n" \
"buffer_load_dwordx4 a[164:167], v209, %[srdz], s50 offen offset:1024 sc1\n" \
"v_mfma_f32_16x16x32_bf16 v[196:199], a[200:203], a[20:23], v[196:199]\n" \
"s_movk_i32 s52, 2112\n" \
"s_nop 0\n" \
"v_mfma_f32_16x16x32_bf16 v[200:203], a[196:199], a[32:35], v[136:139]\n" \
"v_mul_u32_u24_e64 v211, s52, v216\n" \
"v_mfma_f32_16x16x32_bf16 v[200:203], a[200:203], a[36:39], v[200:203]\n" \
"v_add_u32_e32 v211, 16384, v211\n" \
"v_mfma_f32_16x16x32_bf16 v[204:207], a[196:199], a[48:51], v[140:143]\n" \
"ds_read_b64_tr_b16 a[212:213], v221 offset:57344\n" \
"s_sub_u32 s61, s48, 6\n" \
"s_cmp_lt_u32 s61, 2\n" \
"s_cselect_b64 vcc, -1, 0\n" \
"s_nop 0\n" \
"v_mov_b32_e32 v235, 2048\n" \
"v_mfma_f32_16x16x32_bf16 v[204:207], a[200:203], a[52:55], v[204:207]\n" \
"s_waitcnt vmcnt(21) lgkmcnt(5)\n" \
"s_bitcmp1_b32 0x30, s48\n" \
"s_cbranch_scc1 Lcw10_%=\n" \
"s_waitcnt vmcnt(14)\n" \
"Lcw10_%=:\n" \
"v_mfma_f32_16x16x32_bf16 v[192:195], a[204:207], a[8:11], v[192:195]\n" \
"ds_read_b64_tr_b16 a[214:215], v221 offset:59392\n" \
"s_cbranch_vccz Lg21_%=\n" \
"s_nop 0\n" \
"buffer_store_dwordx4 v[0:3], %[oq0], %[srdz], s60 offen offset:0 sc1\n" \
"Lg21_%=:\n" \
"v_mfma_f32_16x16x32_bf16 v[192:195], a[232:235], a[12:15], v[192:195]\n" \
"ds_read_b64_tr_b16 a[216:217], v221 offset:61440\n" \
"v_cmp_eq_u32_e64 s[68:69], 4, v216\n" \
"v_mfma_f32_16x16x32_bf16 v[196:199], a[204:207], a[24:27], v[196:199]\n" \
"ds_read_b64_tr_b16 a[218:219], v221 offset:63488\n" \
"s_lshl_b32 s52, %[wid], 9\n" \
"s_nop 0\n" \
"v_mfma_f32_16x16x32_bf16 v[196:199], a[232:235], a[28:31], v[196:199]\n" \
"buffer_load_dwordx4 a[168:171], v209, %[srdz], s50 offen offset:2048 sc1\n" \
"v_mfma_f32_16x16x32_bf16 v[200:203], a[204:207], a[40:43], v[200:203]\n" \
"v_cndmask_b32_e64 v235, 0, v235, s[68:69]\n" \
"v_mfma_f32_16x16x32_bf16 v[200:203], a[232:235], a[44:47], v[200:203]\n" \
"v_cmp_gt_u32_e64 s[68:69], 4, v216\n" \
"s_add_u32 s52, s52, s40\n" \
"s_nop 0\n" \
"v_mfma_f32_16x16x32_bf16 v[204:207], a[204:207], a[56:59], v[204:207]\n" \
"ds_read_b64_tr_b16 a[228:229], v220 offset:49664\n" \
"v_cndmask_b32_e64 v211, v235, v211, s[68:69]\n" \
"v_mfma_f32_16x16x32_bf16 v[204:207], a[232:235], a[60:63], v[204:207]\n" \
"ds_read_b64_tr_b16 a[230:231], v220 offset:51712\n" \
"v_lshlrev_b32_e64 v235, 4, v218\n" \
"v_mfma_f32_16x16x32_bf16 v[28:31], a[220:223], a[72:75], v[28:31]\n" \
"s_cbranch_vccz Lg22_%=\n" \
"s_nop 0\n" \
"buffer_store_dwordx4 v[8:11], %[oq0], %[srdz], s60 offen offset:1024 sc1\n" \
"Lg22_%=:\n" \
"v_mfma_f32_16x16x32_bf16 v[28:31], a[224:227], a[76:79], v[28:31]\n" \
"v_add3_u32 v211, v211, v235, s52\n" \
"v_mfma_f32_16x16x32_bf16 v[60:63], a[220:223], a[88:91], v[60:63]\n" \
"buffer_load_dwordx4 a[172:175], v209, %[srdz], s50 offen offset:3072 sc1\n" \
"v_mfma_f32_16x16x32_bf16 v[60:63], a[224:227], a[92:95], v[60:63]\n" \
"v_and_b32_e32 v216, 0x2b0, v217\n" \
"v_mfma_f32_16x16x32_bf16 v[92:95], a[220:223], a[104:107], v[92:95]\n" \
"v_lshrrev_b32_e64 v218, 2, v217\n" \
"v_mfma_f32_16x16x32_bf16 v[92:95], a[224:227], a[108:111], v[92:95]\n" \
"v_cmp_u_f32_e64 s[44:45], v192, v196\n" \
"v_and_or_b32 v216, v218, 64, v216\n" \
"s_add_u32 s52, s40, s42\n" \
"s_nop 0\n" \
"v_mfma_f32_16x16x32_bf16 v[124:127], a[220:223], a[120:123], v[124:127]\n" \
"s_add_u32 m0, %[m0b], 16384\n" \
"s_cmp_gt_u32 s48, 2\n" \
"s_cselect_b32 s49, 2, 3\n" \
"s_lshl_b32 s49, s49, 18\n" \
"s_or_b32 s49, s49, s59\n" \
"buffer_load_dwordx4 %[wo0], %[srdt], s49 offen offset:0 lds\n" \
"v_mfma_f32_16x16x32_bf16 v[124:127], a[224:227], a[124:127], v[124:127]\n" \
"v_cmp_u_f32_e64 s[56:57], v200, v204\n" \
"s_or_b64 s[56:57], s[56:57], s[44:45]\n" \
"s_cbranch_scc1 Lsp2_%=\n" \
"Lbk2_%=:\n" \
"s_waitcnt vmcnt(63) lgkmcnt(2)\n" \
"s_nop 0\n" \
"v_mfma_f32_16x16x32_bf16 v[144:147], a[236:239], a[0:3], v[144:147]\n" \
"ds_read_b64_tr_b16 a[220:221], v220 offset:53760\n" \
"v_add_u32_e64 %[kb0], s52, v216\n" \
"v_mfma_f32_16x16x32_bf16 v[144:147], a[208:211], a[4:7], v[144:147]\n" \
"ds_read_b64_tr_b16 a[222:223], v220 offset:55808\n" \
"s_cbranch_vccz Lg23_%=\n" \
"s_nop 0\n" \
"buffer_store_dwordx4 v[16:19], %[oq0], %[srdz], s60 offen offset:2048 sc1\n" \
"Lg23_%=:\n" \
"v_mfma_f32_16x16x32_bf16 v[148:151], a[236:239], a[16:19], v[148:151]\n" \
"ds_read_b64_tr_b16 a[196:197], v220 offset:57856\n" \
"v_xor_b32_e32 %[vvb], 0x100, %[kb0]\n" \
"v_mfma_f32_16x16x32_bf16 v[148:151], a[208:211], a[20:23], v[148:151]\n" \
"ds_read_b64_tr_b16 a[198:199], v220 offset:59904\n" \
"buffer_load_dwordx4 a[176:179], v210, %[srdz], s50 offen offset:0 sc1\n" \
"v_mfma_f32_16x16x32_bf16 v[152:155], a[236:239], a[32:35], v[152:155]\n" \
"ds_read_b64_tr_b16 a[200:201], v220 offset:61952\n" \
"v_and_b32_e32 v235, 0x200, v217\n" \
"v_mfma_f32_16x16x32_bf16 v[152:155], a[208:211], a[36:39], v[152:155]\n" \
"ds_read_b64_tr_b16 a[202:203], v220 offset:64000\n" \
"v_and_b32_e64 v234, 8, v222\n" \
"v_mfma_f32_16x16x32_bf16 v[156:159], a[236:239], a[48:51], v[156:159]\n" \
"ds_read_b64_tr_b16 a[204:205], v221 offset:49664\n" \
"v_lshl_or_b32 v235, v234, 5, v235\n" \
"v_mfma_f32_16x16x32_bf16 v[156:159], a[208:211], a[52:55], v[156:159]\n" \
"ds_read_b64_tr_b16 a[206:207], v221 offset:51712\n" \
"v_and_b32_e64 v234, 16, v222\n" \
"v_mfma_f32_16x16x32_bf16 v[144:147], a[212:215], a[8:11], v[144:147]\n" \
"ds_read_b64_tr_b16 a[224:225], v221 offset:53760\n" \
"v_lshl_or_b32 v235, v234, 1, v235\n" \
"v_mfma_f32_16x16x32_bf16 v[144:147], a[216:219], a[12:15], v[144:147]\n" \
"ds_read_b64_tr_b16 a[226:227], v221 offset:55808\n" \
"v_lshl_or_b32 v235, v233, 6, v235\n" \
"v_mfma_f32_16x16x32_bf16 v[148:151], a[212:215], a[24:27], v[148:151]\n" \
"s_cbranch_vccz Lg24_%=\n" \
"s_nop 0\n" \
"buffer_store_dwordx4 v[24:27], %[oq0], %[srdz], s60 offen offset:3072 sc1\n" \
"Lg24_%=:\n" \
"v_mfma_f32_16x16x32_bf16 v[148:151], a[216:219], a[28:31], v[148:151]\n" \
"v_and_b32_e64 v216, 2, v222\n" \
"v_mfma_f32_16x16x32_bf16 v[152:155], a[212:215], a[40:43], v[152:155]\n" \
"buffer_load_dwordx4 a[180:183], v210, %[srdz], s50 offen offset:1024 sc1\n" \
"v_mfma_f32_16x16x32_bf16 v[152:155], a[216:219], a[44:47], v[152:155]\n" \
"v_lshl_or_b32 v235, v216, 3, v235\n" \
"s_lshr_b32 s52, s42, 1\n" \
"s_nop 0\n" \
"v_mfma_f32_16x16x32_bf16 v[156:159], a[212:215], a[56:59], v[156:159]\n" \
"ds_read_b64_tr_b16 a[232:233], v221 offset:57856\n" \
"v_sub_u32_e32 v234, 0x7ff, %[ta0]\n" \
"v_mfma_f32_16x16x32_bf16 v[156:159], a[216:219], a[60:63], v[156:159]\n" \
"s_waitcnt vmcnt(63) lgkmcnt(5)\n" \
"s_nop 0\n" \
"v_mfma_f32_16x16x32_bf16 v[160:163], a[228:231], a[0:3], v[160:163]\n" \
"ds_read_b64_tr_b16 a[234:235], v221 offset:59904\n" \
"buffer_load_dwordx4 v212, %[srdt], s49 offen offset:1024 lds\n" \
"v_mfma_f32_16x16x32_bf16 v[160:163], a[220:223], a[4:7], v[160:163]\n" \
"ds_read_b64_tr_b16 a[236:237], v221 offset:61952\n" \
"v_lshlrev_b32_e64 v234, 11, v234\n" \
"v_mfma_f32_16x16x32_bf16 v[164:167], a[228:231], a[16:19], v[164:167]\n" \
"ds_read_b64_tr_b16 a[238:239], v221 offset:64000\n" \
"v_add3_u32 %[vd0], v234, v235, s52\n" \
"v_mfma_f32_16x16x32_bf16 v[164:167], a[220:223], a[20:23], v[164:167]\n" \
"s_cbranch_vccz Lg25_%=\n" \
"s_nop 0\n" \
"buffer_store_dwordx4 v[32:35], v208, %[srdz], s60 offen offset:0 sc1\n" \
"Lg25_%=:\n" \
"v_mfma_f32_16x16x32_bf16 v[168:171], a[228:231], a[32:35], v[168:171]\n" \
"v_mfma_f32_16x16x32_bf16 v[168:171], a[220:223], a[36:39], v[168:171]\n" \
"buffer_load_dwordx4 a[184:187], v210, %[srdz], s50 offen offset:2048 sc1\n" \
"v_mfma_f32_16x16x32_bf16 v[172:175], a[228:231], a[48:51], v[172:175]\n" \
"v_mfma_f32_16x16x32_bf16 v[172:175], a[220:223], a[52:55], v[172:175]\n" \
"buffer_load_dwordx4 a[188:191], v210, %[srdz], s50 offen offset:3072 sc1\n" \
"v_mfma_f32_16x16x32_bf16 v[160:163], a[196:199], a[8:11], v[160:163]\n" \
"v_mfma_f32_16x16x32_bf16 v[160:163], a[200:203], a[12:15], v[160:163]\n" \
"v_mfma_f32_16x16x32_bf16 v[164:167], a[196:199], a[24:27], v[164:167]\n" \
"v_mfma_f32_16x16x32_bf16 v[164:167], a[200:203], a[28:31], v[164:167]\n" \
"v_mfma_f32_16x16x32_bf16 v[168:171], a[196:199], a[40:43], v[168:171]\n" \
"s_waitcnt vmcnt(63) lgkmcnt(0)\n" \
"s_bitcmp1_b32 0x30, s48\n" \
"s_cbranch_scc1 Lcw11_%=\n" \
"s_waitcnt vmcnt(60)\n" \
"s_bitcmp1_b32 0xc0, s48\n" \
"s_cbranch_scc1 Lcw11_%=\n" \
"s_waitcnt vmcnt(55)\n" \
"Lcw11_%=:\n" \
"s_barrier\n" \
"s_nop 0\n" \
"v_mfma_f32_16x16x32_bf16 v[168:171], a[200:203], a[44:47], v[168:171]\n" \
"ds_read_b64_tr_b16 a[208:209], v215 offset:49152\n" \
"s_cbranch_vccz Lg26_%=\n" \
"s_nop 0\n" \
"buffer_store_dwordx4 v[40:43], v208, %[srdz], s60 offen offset:1024 sc1\n" \
"Lg26_%=:\n" \
"v_mfma_f32_16x16x32_bf16 v[172:175], a[196:199], a[56:59], v[172:175]\n" \
"ds_read_b64_tr_b16 a[210:211], v215 offset:51200\n" \
"v_mfma_f32_16x16x32_bf16 v[172:175], a[200:203], a[60:63], v[172:175]\n" \
"s_waitcnt vmcnt(63) lgkmcnt(2)\n" \
"s_nop 0\n" \
"v_mfma_f32_16x16x32_bf16 v[176:179], a[204:207], a[0:3], v[176:179]\n" \
"ds_read_b64_tr_b16 a[212:213], v215 offset:53248\n" \
"global_load_dword v237, %[vct], %[pct]\n" \
"v_mfma_f32_16x16x32_bf16 v[176:179], a[224:227], a[4:7], v[176:179]\n" \
"ds_read_b64_tr_b16 a[214:215], v215 offset:55296\n" \
"v_mfma_f32_16x16x32_bf16 v[180:183], a[204:207], a[16:19], v[180:183]\n" \
"ds_read_b64_tr_b16 a[216:217], v215 offset:57344\n" \
"buffer_load_dwordx4 v213, %[srdt], s49 offen offset:2048 lds\n" \
"v_mfma_f32_16x16x32_bf16 v[180:183], a[224:227], a[20:23], v[180:183]\n" \
"ds_read_b64_tr_b16 a[218:219], v215 offset:59392\n" \
"v_mfma_f32_16x16x32_bf16 v[184:187], a[204:207], a[32:35], v[184:187]\n" \
"ds_read_b64_tr_b16 a[228:229], v215 offset:61440\n" \
"v_mfma_f32_16x16x32_bf16 v[184:187], a[224:227], a[36:39], v[184:187]\n" \
"ds_read_b64_tr_b16 a[230:231], v215 offset:63488\n" \
"s_cbranch_vccz Lg27_%=\n" \
"s_nop 0\n" \
"buffer_store_dwordx4 v[48:51], v208, %[srdz], s60 offen offset:2048 sc1\n" \
"Lg27_%=:\n" \
"v_mfma_f32_16x16x32_bf16 v[188:191], a[204:207], a[48:51], v[188:191]\n" \
"ds_read_b64_tr_b16 a[220:221], v227 offset:49152\n" \
"v_mfma_f32_16x16x32_bf16 v[188:191], a[224:227], a[52:55], v[188:191]\n" \
"ds_read_b64_tr_b16 a[222:223], v227 offset:51200\n" \
"v_mfma_f32_16x16x32_bf16 v[176:179], a[232:235], a[8:11], v[176:179]\n" \
"ds_read_b64_tr_b16 a[196:197], v227 offset:53248\n" \
"v_mfma_f32_16x16x32_bf16 v[176:179], a[236:239], a[12:15], v[176:179]\n" \
"ds_read_b64_tr_b16 a[198:199], v227 offset:55296\n" \
"s_cmp_gt_u32 s48, 3\n" \
"s_cselect_b32 s50, 3, 4\n" \
"s_mul_i32 s50, s50, 152064\n" \
"buffer_load_dwordx4 a[64:67], %[oq0], %[srdz], s50 offen offset:0 sc1\n" \
"v_mfma_f32_16x16x32_bf16 v[180:183], a[232:235], a[24:27], v[180:183]\n" \
"v_mfma_f32_16x16x32_bf16 v[180:183], a[236:239], a[28:31], v[180:183]\n" \
"buffer_load_dwordx4 v214, %[srdt], s49 offen offset:3072 lds\n" \
"v_mfma_f32_16x16x32_bf16 v[184:187], a[232:235], a[40:43], v[184:187]\n" \
"v_mfma_f32_16x16x32_bf16 v[184:187], a[236:239], a[44:47], v[184:187]\n" \
"v_mfma_f32_16x16x32_bf16 v[188:191], a[232:235], a[56:59], v[188:191]\n" \
"ds_read_b64_tr_b16 a[200:201], v227 offset:57344\n" \
"s_cbranch_vccz Lg28_%=\n" \
"s_nop 0\n" \
"buffer_store_dwordx4 v[56:59], v208, %[srdz], s60 offen offset:3072 sc1\n" \
"Lg28_%=:\n" \
"v_mfma_f32_16x16x32_bf16 v[188:191], a[236:239], a[60:63], v[188:191]\n" \
"s_waitcnt vmcnt(63) lgkmcnt(5)\n" \
"s_nop 0\n" \
"v_mfma_f32_16x16x32_bf16 v[4:7], a[208:211], a[0:3], v[4:7]\n" \
"ds_read_b64_tr_b16 a[202:203], v227 offset:59392\n" \
"buffer_load_dwordx4 a[68:71], %[oq0], %[srdz], s50 offen offset:1024 sc1\n" \
"v_mfma_f32_16x16x32_bf16 v[4:7], a[212:215], a[4:7], v[4:7]\n" \
"ds_read_b64_tr_b16 a[204:205], v227 offset:61440\n" \
"v_mfma_f32_16x16x32_bf16 v[36:39], a[208:211], a[16:19], v[36:39]\n" \
"ds_read_b64_tr_b16 a[206:207], v227 offset:63488\n" \
"v_mfma_f32_16x16x32_bf16 v[36:39], a[212:215], a[20:23], v[36:39]\n" \
"v_mfma_f32_16x16x32_bf16 v[68:71], a[208:211], a[32:35], v[68:71]\n" \
"buffer_load_dwordx4 a[72:75], %[oq0], %[srdz], s50 offen offset:2048 sc1\n" \
"v_mfma_f32_16x16x32_bf16 v[68:71], a[212:215], a[36:39], v[68:71]\n" \
"v_mfma_f32_16x16x32_bf16 v[100:103], a[208:211], a[48:51], v[100:103]\n" \
"ds_read_b64_tr_b16 a[224:225], v215 offset:49664\n" \
"v_mfma_f32_16x16x32_bf16 v[100:103], a[212:215], a[52:55], v[100:103]\n" \
"ds_read_b64_tr_b16 a[226:227], v215 offset:51712\n" \
"s_cbranch_vccz Lg29_%=\n" \
"s_nop 0\n" \
"buffer_store_dwordx4 v[64:67], v209, %[srdz], s60 offen offset:0 sc1\n" \
"Lg29_%=:\n" \
"v_mfma_f32_16x16x32_bf16 v[4:7], a[216:219], a[8:11], v[4:7]\n" \
"ds_read_b64_tr_b16 a[232:233], v215 offset:53760\n" \
"v_mfma_f32_16x16x32_bf16 v[4:7], a[228:231], a[12:15], v[4:7]\n" \
"ds_read_b64_tr_b16 a[234:235], v215 offset:55808\n" \
"v_mfma_f32_16x16x32_bf16 v[36:39], a[216:219], a[24:27], v[36:39]\n" \
"v_mfma_f32_16x16x32_bf16 v[36:39], a[228:231], a[28:31], v[36:39]\n" \
"buffer_load_dwordx4 a[76:79], %[oq0], %[srdz], s50 offen offset:3072 sc1\n" \
"v_mfma_f32_16x16x32_bf16 v[68:71], a[216:219], a[40:43], v[68:71]\n" \
"v_mfma_f32_16x16x32_bf16 v[68:71], a[228:231], a[44:47], v[68:71]\n" \
"v_mfma_f32_16x16x32_bf16 v[100:103], a[216:219], a[56:59], v[100:103]\n" \
"ds_read_b64_tr_b16 a[236:237], v215 offset:57856\n" \
"v_mfma_f32_16x16x32_bf16 v[100:103], a[228:231], a[60:63], v[100:103]\n" \
"s_waitcnt vmcnt(63) lgkmcnt(5)\n" \
"s_nop 0\n" \
"v_mfma_f32_16x16x32_bf16 v[12:15], a[220:223], a[0:3], v[12:15]\n" \
"ds_read_b64_tr_b16 a[238:239], v215 offset:59904\n" \
"s_cbranch_vccz Lg30_%=\n" \
"s_nop 0\n" \
"buffer_store_dwordx4 v[72:75], v209, %[srdz], s60 offen offset:1024 sc1\n" \
"Lg30_%=:\n" \
"v_mfma_f32_16x16x32_bf16 v[12:15], a[196:199], a[4:7], v[12:15]\n" \
"ds_read_b64_tr_b16 a[208:209], v215 offset:61952\n" \
"v_mfma_f32_16x16x32_bf16 v[44:47], a[220:223], a[16:19], v[44:47]\n" \
"ds_read_b64_tr_b16 a[210:211], v215 offset:64000\n" \
"v_mfma_f32_16x16x32_bf16 v[44:47], a[196:199], a[20:23], v[44:47]\n" \
"buffer_load_dwordx4 a[80:83], v208, %[srdz], s50 offen offset:0 sc1\n" \
"v_mfma_f32_16x16x32_bf16 v[76:79], a[220:223], a[32:35], v[76:79]\n" \
"v_mfma_f32_16x16x32_bf16 v[76:79], a[196:199], a[36:39], v[76:79]\n" \
"s_add_u32 m0, %[m0b], 32768\n" \
"s_cmp_gt_u32 s48, 3\n" \
"s_cselect_b32 s49, 3, 4\n" \
"s_lshl_b32 s49, s49, 18\n" \
"s_or_b32 s49, s49, s58\n" \
"buffer_load_dwordx4 %[wo0], %[srdt], s49 offen offset:0 lds\n" \
"v_mfma_f32_16x16x32_bf16 v[108:111], a[220:223], a[48:51], v[108:111]\n" \
"ds_read_b64_tr_b16 a[212:213], v227 offset:49664\n" \
"v_mfma_f32_16x16x32_bf16 v[108:111], a[196:199], a[52:55], v[108:111]\n" \
"ds_read_b64_tr_b16 a[214:215], v227 offset:51712\n" \
"v_mfma_f32_16x16x32_bf16 v[12:15], a[200:203], a[8:11], v[12:15]\n" \
"ds_read_b64_tr_b16 a[216:217], v227 offset:57856\n" \
"v_mfma_f32_16x16x32_bf16 v[12:15], a[204:207], a[12:15], v[12:15]\n" \
"ds_read_b64_tr_b16 a[218:219], v227 offset:59904\n" \
"s_cbranch_vccz Lg31_%=\n" \
"s_nop 0\n" \
"buffer_store_dwordx4 v[80:83], v209, %[srdz], s60 offen offset:2048 sc1\n" \
"Lg31_%=:\n" \
"v_mfma_f32_16x16x32_bf16 v[44:47], a[200:203], a[24:27], v[44:47]\n" \
"ds_read_b64_tr_b16 a[228:229], v227 offset:53760\n" \
"v_mfma_f32_16x16x32_bf16 v[44:47], a[204:207], a[28:31], v[44:47]\n" \
"ds_read_b64_tr_b16 a[230:231], v227 offset:55808\n" \
"v_mfma_f32_16x16x32_bf16 v[76:79], a[200:203], a[40:43], v[76:79]\n" \
"ds_read_b64_tr_b16 a[220:221], v227 offset:61952\n" \
"buffer_load_dwordx4 a[84:87], v208, %[srdz], s50 offen offset:1024 sc1\n" \
"v_mfma_f32_16x16x32_bf16 v[76:79], a[204:207], a[44:47], v[76:79]\n" \
"ds_read_b64_tr_b16 a[222:223], v227 offset:64000\n" \
"v_mfma_f32_16x16x32_bf16 v[108:111], a[200:203], a[56:59], v[108:111]\n" \
"v_mfma_f32_16x16x32_bf16 v[108:111], a[204:207], a[60:63], v[108:111]\n" \
"s_waitcnt vmcnt(63) lgkmcnt(12)\n" \
"s_nop 0\n" \
"v_mfma_f32_16x16x32_bf16 v[20:23], a[224:227], a[0:3], v[20:23]\n" \
"v_mfma_f32_16x16x32_bf16 v[20:23], a[232:235], a[4:7], v[20:23]\n" \
"v_mfma_f32_16x16x32_bf16 v[52:55], a[224:227], a[16:19], v[52:55]\n" \
"s_cbranch_vccz Lg32_%=\n" \
"s_nop 0\n" \
"buffer_store_dwordx4 v[88:91], v209, %[srdz], s60 offen offset:3072 sc1\n" \
"Lg32_%=:\n" \
"v_mfma_f32_16x16x32_bf16 v[52:55], a[232:235], a[20:23], v[52:55]\n" \
"v_mfma_f32_16x16x32_bf16 v[84:87], a[224:227], a[32:35], v[84:87]\n" \
"s_waitcnt vmcnt(32) lgkmcnt(0)\n" \
"s_nop 0\n" \
"s_bitcmp1_b32 0xc0, s48\n" \
"s_cbranch_scc1 Lcw12_%=\n" \
"s_waitcnt vmcnt(20)\n" \
"Lcw12_%=:\n" \
"s_barrier\n" \
"s_nop 0\n" \
"v_mfma_f32_16x16x32_bf16 v[84:87], a[232:235], a[36:39], v[84:87]\n" \
"ds_read_b64_tr_b16 a[196:197], v215 offset:0\n" \
"buffer_load_dwordx4 a[88:91], v208, %[srdz], s50 offen offset:2048 sc1\n" \
"v_mfma_f32_16x16x32_bf16 v[116:119], a[224:227], a[48:51], v[116:119]\n" \
"ds_read_b64_tr_b16 a[198:199], v215 offset:2048\n" \
"v_mfma_f32_16x16x32_bf16 v[116:119], a[232:235], a[52:55], v[116:119]\n" \
"ds_read_b64_tr_b16 a[200:201], v215 offset:4096\n" \
"buffer_load_dwordx4 v212, %[srdt], s49 offen offset:1024 lds\n" \
"v_mfma_f32_16x16x32_bf16 v[20:23], a[236:239], a[8:11], v[20:23]\n" \
"ds_read_b64_tr_b16 a[202:203], v215 offset:6144\n" \
"v_mfma_f32_16x16x32_bf16 v[20:23], a[208:211], a[12:15], v[20:23]\n" \
"ds_read_b64_tr_b16 a[204:205], v215 offset:8192\n" \
".if . - adaln_body != 49152\n" \
".error \"size model\"\n" \
".endif\n" \
"Lk11_%=:\n" \
"s_cbranch_execz adaln_kr\n" \
"s_nop 0\n" \
"v_mfma_f32_16x16x32_bf16 v[52:55], a[236:239], a[24:27], v[52:55]\n" \
"ds_read_b64_tr_b16 a[206:207], v215 offset:10240\n" \
"v_mfma_f32_16x16x32_bf16 v[52:55], a[208:211], a[28:31], v[52:55]\n" \
"ds_read_b64_tr_b16 a[224:225], v215 offset:12288\n" \
"s_cbranch_vccz Lg33_%=\n" \
"s_nop 0\n" \
"buffer_store_dwordx4 v[96:99], v210, %[srdz], s60 offen offset:0 sc1\n" \
"Lg33_%=:\n" \
"v_mfma_f32_16x16x32_bf16 v[84:87], a[236:239], a[40:43], v[84:87]\n" \
"ds_read_b64_tr_b16 a[226:227], v215 offset:14336\n" \
"v_mfma_f32_16x16x32_bf16 v[84:87], a[208:211], a[44:47], v[84:87]\n" \
"buffer_load_dwordx4 a[92:95], v208, %[srdz], s50 offen offset:3072 sc1\n" \
"v_mfma_f32_16x16x32_bf16 v[116:119], a[236:239], a[56:59], v[116:119]\n" \
"v_mfma_f32_16x16x32_bf16 v[116:119], a[208:211], a[60:63], v[116:119]\n" \
"s_waitcnt vmcnt(25) lgkmcnt(4)\n" \
"s_nop 0\n" \
"s_bitcmp1_b32 0xc0, s48\n" \
"s_cbranch_scc1 Lcw13_%=\n" \
"s_waitcnt vmcnt(16)\n" \
"Lcw13_%=:\n" \
"v_mfma_f32_16x16x32_bf16 v[28:31], a[212:215], a[0:3], v[28:31]\n" \
"buffer_load_dwordx4 a[96:99], v209, %[srdz], s50 offen offset:0 sc1\n" \
"v_mfma_f32_16x16x32_bf16 v[28:31], a[228:231], a[4:7], v[28:31]\n" \
"v_mfma_f32_16x16x32_bf16 v[60:63], a[212:215], a[16:19], v[60:63]\n" \
"buffer_load_dwordx4 v213, %[srdt], s49 offen offset:2048 lds\n" \
"v_mfma_f32_16x16x32_bf16 v[60:63], a[228:231], a[20:23], v[60:63]\n" \
"v_mfma_f32_16x16x32_bf16 v[92:95], a[212:215], a[32:35], v[92:95]\n" \
"s_cbranch_vccz Lg34_%=\n" \
"s_nop 0\n" \
"buffer_store_dwordx4 v[104:107], v210, %[srdz], s60 offen offset:1024 sc1\n" \
"Lg34_%=:\n" \
"v_mfma_f32_16x16x32_bf16 v[92:95], a[228:231], a[36:39], v[92:95]\n" \
"v_mfma_f32_16x16x32_bf16 v[124:127], a[212:215], a[48:51], v[124:127]\n" \
"ds_read_b64_tr_b16 a[232:233], v227 offset:0\n" \
"v_mfma_f32_16x16x32_bf16 v[124:127], a[228:231], a[52:55], v[124:127]\n" \
"ds_read_b64_tr_b16 a[234:235], v227 offset:2048\n" \
"v_mfma_f32_16x16x32_bf16 v[128:131], a[196:199], a[128:131], v[192:195]\n" \
"ds_read_b64_tr_b16 a[236:237], v227 offset:4096\n" \
"v_mfma_f32_16x16x32_bf16 v[128:131], a[200:203], a[132:135], v[128:131]\n" \
"ds_read_b64_tr_b16 a[238:239], v227 offset:6144\n" \
"buffer_load_dwordx4 a[100:103], v209, %[srdz], s50 offen offset:1024 sc1\n" \
"v_mfma_f32_16x16x32_bf16 v[132:135], a[196:199], a[144:147], v[196:199]\n" \
"v_mfma_f32_16x16x32_bf16 v[132:135], a[200:203], a[148:151], v[132:135]\n" \
"v_mfma_f32_16x16x32_bf16 v[136:139], a[196:199], a[160:163], v[200:203]\n" \
"v_mfma_f32_16x16x32_bf16 v[136:139], a[200:203], a[164:167], v[136:139]\n" \
"s_cbranch_vccz Lg35_%=\n" \
"s_nop 0\n" \
"buffer_store_dwordx4 v[112:115], v210, %[srdz], s60 offen offset:2048 sc1\n" \
"Lg35_%=:\n" \
"v_mfma_f32_16x16x32_bf16 v[140:143], a[196:199], a[176:179], v[204:207]\n" \
"ds_read_b64_tr_b16 a[208:209], v227 offset:8192\n" \
"v_mfma_f32_16x16x32_bf16 v[140:143], a[200:203], a[180:183], v[140:143]\n" \
"s_waitcnt vmcnt(26) lgkmcnt(5)\n" \
"s_nop 0\n" \
"s_bitcmp1_b32 0xc0, s48\n" \
"s_cbranch_scc1 Lcw14_%=\n" \
"s_waitcnt vmcnt(16)\n" \
"Lcw14_%=:\n" \
"v_mfma_f32_16x16x32_bf16 v[128:131], a[204:207], a[136:139], v[128:131]\n" \
"ds_read_b64_tr_b16 a[210:211], v227 offset:10240\n" \
"buffer_load_dwordx4 a[104:107], v209, %[srdz], s50 offen offset:2048 sc1\n" \
"v_mfma_f32_16x16x32_bf16 v[128:131], a[224:227], a[140:143], v[128:131]\n" \
"ds_read_b64_tr_b16 a[212:213], v227 offset:12288\n" \
"v_mfma_f32_16x16x32_bf16 v[132:135], a[204:207], a[152:155], v[132:135]\n" \
"ds_read_b64_tr_b16 a[214:215], v227 offset:14336\n" \
"v_mfma_f32_16x16x32_bf16 v[132:135], a[224:227], a[156:159], v[132:135]\n" \
"buffer_load_dwordx4 v214, %[srdt], s49 offen offset:3072 lds\n" \
"v_mfma_f32_16x16x32_bf16 v[136:139], a[204:207], a[168:171], v[136:139]\n" \
"v_mfma_f32_16x16x32_bf16 v[136:139], a[224:227], a[172:175], v[136:139]\n" \
"v_mfma_f32_16x16x32_bf16 v[140:143], a[204:207], a[184:187], v[140:143]\n" \
"ds_read_b64_tr_b16 a[228:229], v215 offset:512\n" \
"s_cbranch_vccz Lg36_%=\n" \
"s_nop 0\n" \
"buffer_store_dwordx4 v[120:123], v210, %[srdz], s60 offen offset:3072 sc1\n" \
"Lg36_%=:\n" \
"v_mfma_f32_16x16x32_bf16 v[140:143], a[224:227], a[188:191], v[140:143]\n" \
"ds_read_b64_tr_b16 a[230:231], v215 offset:2560\n" \
"v_mfma_f32_16x16x32_bf16 v[28:31], a[216:219], a[8:11], v[28:31]\n" \
"buffer_load_dwordx4 a[108:111], v209, %[srdz], s50 offen offset:3072 sc1\n" \
"v_mfma_f32_16x16x32_bf16 v[28:31], a[220:223], a[12:15], v[28:31]\n" \
"v_mfma_f32_16x16x32_bf16 v[60:63], a[216:219], a[24:27], v[60:63]\n" \
"v_mfma_f32_16x16x32_bf16 v[60:63], a[220:223], a[28:31], v[60:63]\n" \
"v_mfma_f32_16x16x32_bf16 v[92:95], a[216:219], a[40:43], v[92:95]\n" \
"buffer_load_dwordx4 a[112:115], v210, %[srdz], s50 offen offset:0 sc1\n" \
"v_mfma_f32_16x16x32_bf16 v[92:95], a[220:223], a[44:47], v[92:95]\n" \
"v_cmp_u_f32_e64 s[44:45], v128, v132\n" \
"v_mfma_f32_16x16x32_bf16 v[124:127], a[216:219], a[56:59], v[124:127]\n" \
"v_mfma_f32_16x16x32_bf16 v[124:127], a[220:223], a[60:63], v[124:127]\n" \
"v_cmp_u_f32_e64 s[56:57], v136, v140\n" \
"s_or_b64 s[56:57], s[56:57], s[44:45]\n" \
"s_cbranch_scc1 Lsp3_%=\n" \
"Lbk3_%=:\n" \
"s_waitcnt vmcnt(63) lgkmcnt(2)\n" \
"s_nop 0\n" \
"v_mfma_f32_16x16x32_bf16 v[144:147], a[232:235], a[128:131], v[144:147]\n" \
"ds_read_b64_tr_b16 a[216:217], v215 offset:4608\n" \
"v_mfma_f32_16x16x32_bf16 v[144:147], a[236:239], a[132:135], v[144:147]\n" \
"ds_read_b64_tr_b16 a[218:219], v215 offset:6656\n" \
"v_mfma_f32_16x16x32_bf16 v[148:151], a[232:235], a[144:147], v[148:151]\n" \
"ds_read_b64_tr_b16 a[196:197], v215 offset:8704\n" \
"v_mfma_f32_16x16x32_bf16 v[148:151], a[236:239], a[148:151], v[148:151]\n" \
"ds_read_b64_tr_b16 a[198:199], v215 offset:10752\n" \
"v_mfma_f32_16x16x32_bf16 v[152:155], a[232:235], a[160:163], v[152:155]\n" \
"ds_read_b64_tr_b16 a[200:201], v215 offset:12800\n" \
"v_mfma_f32_16x16x32_bf16 v[152:155], a[236:239], a[164:167], v[152:155]\n" \
"ds_read_b64_tr_b16 a[202:203], v215 offset:14848\n" \
"v_mfma_f32_16x16x32_bf16 v[156:159], a[232:235], a[176:179], v[156:159]\n" \
"ds_read_b64_tr_b16 a[204:205], v227 offset:512\n" \
"buffer_load_dwordx4 a[116:119], v210, %[srdz], s50 offen offset:1024 sc1\n" \
"v_mfma_f32_16x16x32_bf16 v[156:159], a[236:239], a[180:183], v[156:159]\n" \
"ds_read_b64_tr_b16 a[206:207], v227 offset:2560\n" \
"v_mfma_f32_16x16x32_bf16 v[144:147], a[208:211], a[136:139], v[144:147]\n" \
"ds_read_b64_tr_b16 a[220:221], v227 offset:4608\n" \
"v_mfma_f32_16x16x32_bf16 v[144:147], a[212:215], a[140:143], v[144:147]\n" \
"ds_read_b64_tr_b16 a[222:223], v227 offset:6656\n" \
"v_mfma_f32_16x16x32_bf16 v[148:151], a[208:211], a[152:155], v[148:151]\n" \
"s_add_u32 m0, %[m0b], 98304\n" \
"s_cmp_gt_u32 s48, 4\n" \
"s_cselect_b32 s49, 4, 5\n" \
"s_lshl_b32 s49, s49, 18\n" \
"s_or_b32 s49, s49, s58\n" \
"buffer_load_dwordx4 %[wo0], %[srdt], s49 offen offset:0 lds\n" \
"v_mfma_f32_16x16x32_bf16 v[148:151], a[212:215], a[156:159], v[148:151]\n" \
"v_mfma_f32_16x16x32_bf16 v[152:155], a[208:211], a[168:171], v[152:155]\n" \
"buffer_load_dwordx4 a[120:123], v210, %[srdz], s50 offen offset:2048 sc1\n" \
"v_mfma_f32_16x16x32_bf16 v[152:155], a[212:215], a[172:175], v[152:155]\n" \
"v_mfma_f32_16x16x32_bf16 v[156:159], a[208:211], a[184:187], v[156:159]\n" \
"ds_read_b64_tr_b16 a[224:225], v227 offset:8704\n" \
"v_mfma_f32_16x16x32_bf16 v[156:159], a[212:215], a[188:191], v[156:159]\n" \
"s_waitcnt vmcnt(63) lgkmcnt(5)\n" \
"s_nop 0\n" \
"v_mfma_f32_16x16x32_bf16 v[160:163], a[228:231], a[128:131], v[160:163]\n" \
"ds_read_b64_tr_b16 a[226:227], v227 offset:10752\n" \
"v_mfma_f32_16x16x32_bf16 v[160:163], a[216:219], a[132:135], v[160:163]\n" \
"ds_read_b64_tr_b16 a[232:233], v227 offset:12800\n" \
"v_mfma_f32_16x16x32_bf16 v[164:167], a[228:231], a[144:147], v[164:167]\n" \
"ds_read_b64_tr_b16 a[234:235], v227 offset:14848\n" \
"v_mfma_f32_16x16x32_bf16 v[164:167], a[216:219], a[148:151], v[164:167]\n" \
"buffer_load_dwordx4 a[124:127], v210, %[srdz], s50 offen offset:3072 sc1\n" \
"v_mfma_f32_16x16x32_bf16 v[168:171], a[228:231], a[160:163], v[168:171]\n" \
"v_mfma_f32_16x16x32_bf16 v[168:171], a[216:219], a[164:167], v[168:171]\n" \
"v_mfma_f32_16x16x32_bf16 v[172:175], a[228:231], a[176:179], v[172:175]\n" \
"v_mfma_f32_16x16x32_bf16 v[172:175], a[216:219], a[180:183], v[172:175]\n" \
"v_mfma_f32_16x16x32_bf16 v[160:163], a[196:199], a[136:139], v[160:163]\n" \
"v_mfma_f32_16x16x32_bf16 v[160:163], a[200:203], a[140:143], v[160:163]\n" \
"buffer_load_dwordx4 v212, %[srdt], s49 offen offset:1024 lds\n" \
"v_mfma_f32_16x16x32_bf16 v[164:167], a[196:199], a[152:155], v[164:167]\n" \
"v_mfma_f32_16x16x32_bf16 v[164:167], a[200:203], a[156:159], v[164:167]\n" \
"v_mfma_f32_16x16x32_bf16 v[168:171], a[196:199], a[168:171], v[168:171]\n" \
"s_waitcnt vmcnt(30) lgkmcnt(0)\n" \
"s_nop 0\n" \
"s_bitcmp1_b32 0xc0, s48\n" \
"s_cbranch_scc1 Lcw15_%=\n" \
"s_waitcnt vmcnt(21)\n" \
"Lcw15_%=:\n" \
"s_barrier\n" \
"s_nop 0\n" \
"v_mfma_f32_16x16x32_bf16 v[168:171], a[200:203], a[172:175], v[168:171]\n" \
"ds_read_b64_tr_b16 a[236:237], v215 offset:16384\n" \
"v_mfma_f32_16x16x32_bf16 v[172:175], a[196:199], a[184:187], v[172:175]\n" \
"ds_read_b64_tr_b16 a[238:239], v215 offset:18432\n" \
"v_mfma_f32_16x16x32_bf16 v[172:175], a[200:203], a[188:191], v[172:175]\n" \
"s_waitcnt vmcnt(63) lgkmcnt(2)\n" \
"s_nop 0\n" \
"v_mfma_f32_16x16x32_bf16 v[176:179], a[204:207], a[128:131], v[176:179]\n" \
"ds_read_b64_tr_b16 a[208:209], v215 offset:20480\n" \
"s_cmp_gt_u32 s48, 4\n" \
"s_cselect_b32 s50, 4, 5\n" \
"s_mul_i32 s50, s50, 152064\n" \
"buffer_load_dwordx4 v[0:3], %[oq0], %[srdz], s50 offen offset:0 sc1\n" \
"v_mfma_f32_16x16x32_bf16 v[176:179], a[220:223], a[132:135], v[176:179]\n" \
"ds_read_b64_tr_b16 a[210:211], v215 offset:22528\n" \
"v_mfma_f32_16x16x32_bf16 v[180:183], a[204:207], a[144:147], v[180:183]\n" \
"ds_read_b64_tr_b16 a[212:213], v215 offset:24576\n" \
"v_mfma_f32_16x16x32_bf16 v[180:183], a[220:223], a[148:151], v[180:183]\n" \
"ds_read_b64_tr_b16 a[214:215], v215 offset:26624\n" \
"v_mfma_f32_16x16x32_bf16 v[184:187], a[204:207], a[160:163], v[184:187]\n" \
"ds_read_b64_tr_b16 a[228:229], v215 offset:28672\n" \
"buffer_load_dwordx4 v[8:11], %[oq0], %[srdz], s50 offen offset:1024 sc1\n" \
"v_mfma_f32_16x16x32_bf16 v[184:187], a[220:223], a[164:167], v[184:187]\n" \
"ds_read_b64_tr_b16 a[230:231], v215 offset:30720\n" \
"v_mfma_f32_16x16x32_bf16 v[188:191], a[204:207], a[176:179], v[188:191]\n" \
"ds_read_b64_tr_b16 a[216:217], v227 offset:16384\n" \
"v_mfma_f32_16x16x32_bf16 v[188:191], a[220:223], a[180:183], v[188:191]\n" \
"ds_read_b64_tr_b16 a[218:219], v227 offset:18432\n" \
"v_mfma_f32_16x16x32_bf16 v[176:179], a[224:227], a[136:139], v[176:179]\n" \
"ds_read_b64_tr_b16 a[196:197], v227 offset:20480\n" \
"buffer_load_dwordx4 v[16:19], %[oq0], %[srdz], s50 offen offset:2048 sc1\n" \
"v_mfma_f32_16x16x32_bf16 v[176:179], a[232:235], a[140:143], v[176:179]\n" \
"ds_read_b64_tr_b16 a[198:199], v227 offset:22528\n" \
"v_mfma_f32_16x16x32_bf16 v[180:183], a[224:227], a[152:155], v[180:183]\n" \
"buffer_load_dwordx4 v213, %[srdt], s49 offen offset:2048 lds\n" \
"v_mfma_f32_16x16x32_bf16 v[180:183], a[232:235], a[156:159], v[180:183]\n" \
"v_mfma_f32_16x16x32_bf16 v[184:187], a[224:227], a[168:171], v[184:187]\n" \
"buffer_load_dwordx4 v[24:27], %[oq0], %[srdz], s50 offen offset:3072 sc1\n" \
"v_mfma_f32_16x16x32_bf16 v[184:187], a[232:235], a[172:175], v[184:187]\n" \
"v_mfma_f32_16x16x32_bf16 v[188:191], a[224:227], a[184:187], v[188:191]\n" \
"ds_read_b64_tr_b16 a[200:201], v227 offset:24576\n" \
"v_mfma_f32_16x16x32_bf16 v[188:191], a[232:235], a[188:191], v[188:191]\n" \
"s_waitcnt vmcnt(63) lgkmcnt(5)\n" \
"s_nop 0\n" \
"v_mfma_f32_16x16x32_bf16 v[4:7], a[236:239], a[128:131], v[4:7]\n" \
"ds_read_b64_tr_b16 a[202:203], v227 offset:26624\n" \
"buffer_load_dwordx4 v[32:35], v208, %[srdz], s50 offen offset:0 sc1\n" \
"v_mfma_f32_16x16x32_bf16 v[4:7], a[208:211], a[132:135], v[4:7]\n" \
"ds_read_b64_tr_b16 a[204:205], v227 offset:28672\n" \
"v_mfma_f32_16x16x32_bf16 v[36:39], a[236:239], a[144:147], v[36:39]\n" \
"ds_read_b64_tr_b16 a[206:207], v227 offset:30720\n" \
"v_mfma_f32_16x16x32_bf16 v[36:39], a[208:211], a[148:151], v[36:39]\n" \
"buffer_load_dwordx4 v[40:43], v208, %[srdz], s50 offen offset:1024 sc1\n" \
"v_mfma_f32_16x16x32_bf16 v[68:71], a[236:239], a[160:163], v[68:71]\n" \
"v_mfma_f32_16x16x32_bf16 v[68:71], a[208:211], a[164:167], v[68:71]\n" \
"buffer_load_dwordx4 v[48:51], v208, %[srdz], s50 offen offset:2048 sc1\n" \
"v_mfma_f32_16x16x32_bf16 v[100:103], a[236:239], a[176:179], v[100:103]\n" \
"ds_read_b64_tr_b16 a[220:221], v215 offset:16896\n" \
"v_mfma_f32_16x16x32_bf16 v[100:103], a[208:211], a[180:183], v[100:103]\n" \
"ds_read_b64_tr_b16 a[222:223], v215 offset:18944\n" \
"v_mfma_f32_16x16x32_bf16 v[4:7], a[212:215], a[136:139], v[4:7]\n" \
"ds_read_b64_tr_b16 a[224:225], v215 offset:20992\n" \
"v_mfma_f32_16x16x32_bf16 v[4:7], a[228:231], a[140:143], v[4:7]\n" \
"ds_read_b64_tr_b16 a[226:227], v215 offset:23040\n" \
"v_mfma_f32_16x16x32_bf16 v[36:39], a[212:215], a[152:155], v[36:39]\n" \
"buffer_load_dwordx4 v214, %[srdt], s49 offen offset:3072 lds\n" \
"v_mfma_f32_16x16x32_bf16 v[36:39], a[228:231], a[156:159], v[36:39]\n" \
"v_mfma_f32_16x16x32_bf16 v[68:71], a[212:215], a[168:171], v[68:71]\n" \
"buffer_load_dwordx4 v[56:59], v208, %[srdz], s50 offen offset:3072 sc1\n" \
"v_mfma_f32_16x16x32_bf16 v[68:71], a[228:231], a[172:175], v[68:71]\n" \
"v_mfma_f32_16x16x32_bf16 v[100:103], a[212:215], a[184:187], v[100:103]\n" \
"ds_read_b64_tr_b16 a[232:233], v215 offset:25088\n" \
"v_mfma_f32_16x16x32_bf16 v[100:103], a[228:231], a[188:191], v[100:103]\n" \
"s_waitcnt vmcnt(63) lgkmcnt(5)\n" \
"s_nop 0\n" \
"v_mfma_f32_16x16x32_bf16 v[12:15], a[216:219], a[128:131], v[12:15]\n" \
"ds_read_b64_tr_b16 a[234:235], v215 offset:27136\n" \
"buffer_load_dwordx4 v[64:67], v209, %[srdz], s50 offen offset:0 sc1\n" \
"v_mfma_f32_16x16x32_bf16 v[12:15], a[196:199], a[132:135], v[12:15]\n" \
"ds_read_b64_tr_b16 a[236:237], v215 offset:29184\n" \
"v_mfma_f32_16x16x32_bf16 v[44:47], a[216:219], a[144:147], v[44:47]\n" \
"ds_read_b64_tr_b16 a[238:239], v215 offset:31232\n" \
"v_mfma_f32_16x16x32_bf16 v[44:47], a[196:199], a[148:151], v[44:47]\n" \
"buffer_load_dwordx4 v[72:75], v209, %[srdz], s50 offen offset:1024 sc1\n" \
"v_mfma_f32_16x16x32_bf16 v[76:79], a[216:219], a[160:163], v[76:79]\n" \
"v_mfma_f32_16x16x32_bf16 v[76:79], a[196:199], a[164:167], v[76:79]\n" \
"buffer_load_dwordx4 v[80:83], v209, %[srdz], s50 offen offset:2048 sc1\n" \
"v_mfma_f32_16x16x32_bf16 v[108:111], a[216:219], a[176:179], v[108:111]\n" \
"ds_read_b64_tr_b16 a[208:209], v227 offset:16896\n" \
"v_mfma_f32_16x16x32_bf16 v[108:111], a[196:199], a[180:183], v[108:111]\n" \
"ds_read_b64_tr_b16 a[210:211], v227 offset:18944\n" \
"v_mfma_f32_16x16x32_bf16 v[12:15], a[200:203], a[136:139], v[12:15]\n" \
"ds_read_b64_tr_b16 a[212:213], v227 offset:25088\n" \
"v_mfma_f32_16x16x32_bf16 v[12:15], a[204:207], a[140:143], v[12:15]\n" \
"ds_read_b64_tr_b16 a[214:215], v227 offset:27136\n" \
"v_mfma_f32_16x16x32_bf16 v[44:47], a[200:203], a[152:155], v[44:47]\n" \
"ds_read_b64_tr_b16 a[228:229], v227 offset:20992\n" \
"v_mfma_f32_16x16x32_bf16 v[44:47], a[204:207], a[156:159], v[44:47]\n" \
"ds_read_b64_tr_b16 a[230:231], v227 offset:23040\n" \
"buffer_load_dwordx4 v[88:91], v209, %[srdz], s50 offen offset:3072 sc1\n" \
"v_mfma_f32_16x16x32_bf16 v[76:79], a[200:203], a[168:171], v[76:79]\n" \
"ds_read_b64_tr_b16 a[216:217], v227 offset:29184\n" \
"v_mfma_f32_16x16x32_bf16 v[76:79], a[204:207], a[172:175], v[76:79]\n" \
"ds_read_b64_tr_b16 a[218:219], v227 offset:31232\n" \
"v_mfma_f32_16x16x32_bf16 v[108:111], a[200:203], a[184:187], v[108:111]\n" \
"buffer_load_dwordx4 v[96:99], v210, %[srdz], s50 offen offset:0 sc1\n" \
"v_mfma_f32_16x16x32_bf16 v[108:111], a[204:207], a[188:191], v[108:111]\n" \
"s_waitcnt vmcnt(63) lgkmcnt(12)\n" \
"s_nop 0\n" \
"v_mfma_f32_16x16x32_bf16 v[20:23], a[220:223], a[128:131], v[20:23]\n" \
"s_add_u32 m0, %[m0b], 114688\n" \
"s_cmp_gt_u32 s48, 5\n" \
"s_cselect_b32 s49, 5, 6\n" \
"s_lshl_b32 s49, s49, 18\n" \
"s_or_b32 s49, s49, s58\n" \
"buffer_load_dwordx4 %[wo0], %[srdt], s49 offen offset:0 lds\n" \
"v_mfma_f32_16x16x32_bf16 v[20:23], a[224:227], a[132:135], v[20:23]\n" \
"v_mfma_f32_16x16x32_bf16 v[52:55], a[220:223], a[144:147], v[52:55]\n" \
"buffer_load_dwordx4 v[104:107], v210, %[srdz], s50 offen offset:1024 sc1\n" \
"v_mfma_f32_16x16x32_bf16 v[52:55], a[224:227], a[148:151], v[52:55]\n" \
"v_mfma_f32_16x16x32_bf16 v[84:87], a[220:223], a[160:163], v[84:87]\n" \
"s_waitcnt vmcnt(24) lgkmcnt(0)\n" \
"s_barrier\n" \
"v_mfma_f32_16x16x32_bf16 v[84:87], a[224:227], a[164:167], v[84:87]\n" \
"ds_read_b64_tr_b16 a[196:197], v215 offset:32768\n" \
"v_mfma_f32_16x16x32_bf16 v[116:119], a[220:223], a[176:179], v[116:119]\n" \
"ds_read_b64_tr_b16 a[198:199], v215 offset:34816\n" \
"v_mfma_f32_16x16x32_bf16 v[116:119], a[224:227], a[180:183], v[116:119]\n" \
"ds_read_b64_tr_b16 a[200:201], v215 offset:36864\n" \
"buffer_load_dwordx4 v[112:115], v210, %[srdz], s50 offen offset:2048 sc1\n" \
"v_mfma_f32_16x16x32_bf16 v[20:23], a[232:235], a[136:139], v[20:23]\n" \
"ds_read_b64_tr_b16 a[202:203], v215 offset:38912\n" \
"v_mfma_f32_16x16x32_bf16 v[20:23], a[236:239], a[140:143], v[20:23]\n" \
"ds_read_b64_tr_b16 a[204:205], v215 offset:40960\n" \
"v_mfma_f32_16x16x32_bf16 v[52:55], a[232:235], a[152:155], v[52:55]\n" \
"ds_read_b64_tr_b16 a[206:207], v215 offset:43008\n" \
"v_mfma_f32_16x16x32_bf16 v[52:55], a[236:239], a[156:159], v[52:55]\n" \
"ds_read_b64_tr_b16 a[220:221], v215 offset:45056\n" \
"buffer_load_dwordx4 v[120:123], v210, %[srdz], s50 offen offset:3072 sc1\n" \
"v_mfma_f32_16x16x32_bf16 v[84:87], a[232:235], a[168:171], v[84:87]\n" \
"ds_read_b64_tr_b16 a[222:223], v215 offset:47104\n" \
"v_readlane_b32 s54, %[thi], 16\n" \
"v_mfma_f32_16x16x32_bf16 v[84:87], a[236:239], a[172:175], v[84:87]\n" \
"v_mfma_f32_16x16x32_bf16 v[116:119], a[232:235], a[184:187], v[116:119]\n" \
"v_mfma_f32_16x16x32_bf16 v[116:119], a[236:239], a[188:191], v[116:119]\n" \
"s_waitcnt vmcnt(23) lgkmcnt(4)\n" \
"s_nop 0\n" \
"v_mfma_f32_16x16x32_bf16 v[28:31], a[208:211], a[128:131], v[28:31]\n" \
"buffer_load_dwordx4 v212, %[srdt], s49 offen offset:1024 lds\n" \
"v_mfma_f32_16x16x32_bf16 v[28:31], a[228:231], a[132:135], v[28:31]\n" \
"v_mfma_f32_16x16x32_bf16 v[60:63], a[208:211], a[144:147], v[60:63]\n" \
"s_cmp_gt_u32 s48, 5\n" \
"s_cselect_b32 s50, 5, 6\n" \
"s_mul_i32 s50, s50, 152064\n" \
"buffer_load_dwordx4 a[0:3], %[oq0], %[srdz], s50 offen offset:0 sc1\n" \
"v_mfma_f32_16x16x32_bf16 v[60:63], a[228:231], a[148:151], v[60:63]\n" \
"v_mfma_f32_16x16x32_bf16 v[92:95], a[208:211], a[160:163], v[92:95]\n" \
"buffer_load_dword v232, v226, %[srdz], s54 offen sc1\n" \
"v_mfma_f32_16x16x32_bf16 v[92:95], a[228:231], a[164:167], v[92:95]\n" \
"v_mfma_f32_16x16x32_bf16 v[124:127], a[208:211], a[176:179], v[124:127]\n" \
"ds_read_b64_tr_b16 a[224:225], v227 offset:32768\n" \
"buffer_load_dwordx4 a[4:7], %[oq0], %[srdz], s50 offen offset:1024 sc1\n" \
"v_mfma_f32_16x16x32_bf16 v[124:127], a[228:231], a[180:183], v[124:127]\n" \
"ds_read_b64_tr_b16 a[226:227], v227 offset:34816\n" \
"v_mfma_f32_16x16x32_bf16 v[192:195], a[196:199], a[64:67], v[128:131]\n" \
"ds_read_b64_tr_b16 a[232:233], v227 offset:36864\n" \
"v_mfma_f32_16x16x32_bf16 v[192:195], a[200:203], a[68:71], v[192:195]\n" \
"ds_read_b64_tr_b16 a[234:235], v227 offset:38912\n" \
"v_mfma_f32_16x16x32_bf16 v[196:199], a[196:199], a[80:83], v[132:135]\n" \
"buffer_load_dwordx4 a[8:11], %[oq0], %[srdz], s50 offen offset:2048 sc1\n" \
"v_mfma_f32_16x16x32_bf16 v[196:199], a[200:203], a[84:87], v[196:199]\n" \
"v_mfma_f32_16x16x32_bf16 v[200:203], a[196:199], a[96:99], v[136:139]\n" \
"buffer_load_dwordx4 a[12:15], %[oq0], %[srdz], s50 offen offset:3072 sc1\n" \
"v_mfma_f32_16x16x32_bf16 v[200:203], a[200:203], a[100:103], v[200:203]\n" \
"v_mfma_f32_16x16x32_bf16 v[204:207], a[196:199], a[112:115], v[140:143]\n" \
"ds_read_b64_tr_b16 a[236:237], v227 offset:40960\n" \
"v_mfma_f32_16x16x32_bf16 v[204:207], a[200:203], a[116:119], v[204:207]\n" \
"s_waitcnt vmcnt(26) lgkmcnt(5)\n" \
"s_nop 0\n" \
"v_mfma_f32_16x16x32_bf16 v[192:195], a[204:207], a[72:75], v[192:195]\n" \
"ds_read_b64_tr_b16 a[238:239], v227 offset:43008\n" \
"v_mfma_f32_16x16x32_bf16 v[192:195], a[220:223], a[76:79], v[192:195]\n" \
"ds_read_b64_tr_b16 a[208:209], v227 offset:45056\n" \
"buffer_load_dwordx4 v213, %[srdt], s49 offen offset:2048 lds\n" \
"v_mfma_f32_16x16x32_bf16 v[196:199], a[204:207], a[88:91], v[196:199]\n" \
"ds_read_b64_tr_b16 a[210:211], v227 offset:47104\n" \
"v_mfma_f32_16x16x32_bf16 v[196:199], a[220:223], a[92:95], v[196:199]\n" \
"buffer_load_dwordx4 a[16:19], v208, %[srdz], s50 offen offset:0 sc1\n" \
"v_mfma_f32_16x16x32_bf16 v[200:203], a[204:207], a[104:107], v[200:203]\n" \
"v_mfma_f32_16x16x32_bf16 v[200:203], a[220:223], a[108:111], v[200:203]\n" \
"buffer_load_dwordx4 a[20:23], v208, %[srdz], s50 offen offset:1024 sc1\n" \
"v_mfma_f32_16x16x32_bf16 v[204:207], a[204:207], a[120:123], v[204:207]\n" \
"ds_read_b64_tr_b16 a[228:229], v215 offset:33280\n" \
"v_mfma_f32_16x16x32_bf16 v[204:207], a[220:223], a[124:127], v[204:207]\n" \
"ds_read_b64_tr_b16 a[230:231], v215 offset:35328\n" \
"v_mfma_f32_16x16x32_bf16 v[28:31], a[212:215], a[136:139], v[28:31]\n" \
"buffer_load_dwordx4 a[24:27], v208, %[srdz], s50 offen offset:2048 sc1\n" \
"v_mfma_f32_16x16x32_bf16 v[28:31], a[216:219], a[140:143], v[28:31]\n" \
"v_mfma_f32_16x16x32_bf16 v[60:63], a[212:215], a[152:155], v[60:63]\n" \
"buffer_load_dwordx4 a[28:31], v208, %[srdz], s50 offen offset:3072 sc1\n" \
"v_mfma_f32_16x16x32_bf16 v[60:63], a[216:219], a[156:159], v[60:63]\n" \
"v_mfma_f32_16x16x32_bf16 v[92:95], a[212:215], a[168:171], v[92:95]\n" \
"v_mfma_f32_16x16x32_bf16 v[92:95], a[216:219], a[172:175], v[92:95]\n" \
"v_cmp_u_f32_e64 s[44:45], v192, v196\n" \
"buffer_load_dwordx4 v214, %[srdt], s49 offen offset:3072 lds\n" \
"v_mfma_f32_16x16x32_bf16 v[124:127], a[212:215], a[184:187], v[124:127]\n" \
"v_mfma_f32_16x16x32_bf16 v[124:127], a[216:219], a[188:191], v[124:127]\n" \
"v_cmp_u_f32_e64 s[56:57], v200, v204\n" \
"s_or_b64 s[56:57], s[56:57], s[44:45]\n" \
"s_cbranch_scc1 Lsp4_%=\n" \
"Lbk4_%=:\n" \
"s_waitcnt vmcnt(63) lgkmcnt(2)\n" \
"s_nop 0\n" \
"v_mfma_f32_16x16x32_bf16 v[144:147], a[224:227], a[64:67], v[144:147]\n" \
"ds_read_b64_tr_b16 a[212:213], v215 offset:37376\n" \
"v_mfma_f32_16x16x32_bf16 v[144:147], a[232:235], a[68:71], v[144:147]\n" \
"ds_read_b64_tr_b16 a[214:215], v215 offset:39424\n" \
"buffer_load_dwordx4 a[32:35], v209, %[srdz], s50 offen offset:0 sc1\n" \
"v_mfma_f32_16x16x32_bf16 v[148:151], a[224:227], a[80:83], v[148:151]\n" \
"ds_read_b64_tr_b16 a[196:197], v215 offset:41472\n" \
"v_mfma_f32_16x16x32_bf16 v[148:151], a[232:235], a[84:87], v[148:151]\n" \
"ds_read_b64_tr_b16 a[198:199], v215 offset:43520\n" \
"v_mfma_f32_16x16x32_bf16 v[152:155], a[224:227], a[96:99], v[152:155]\n" \
"ds_read_b64_tr_b16 a[200:201], v215 offset:45568\n" \
"v_mfma_f32_16x16x32_bf16 v[152:155], a[232:235], a[100:103], v[152:155]\n" \
"ds_read_b64_tr_b16 a[202:203], v215 offset:47616\n" \
"buffer_load_dwordx4 a[36:39], v209, %[srdz], s50 offen offset:1024 sc1\n" \
"v_mfma_f32_16x16x32_bf16 v[156:159], a[224:227], a[112:115], v[156:159]\n" \
"ds_read_b64_tr_b16 a[204:205], v227 offset:33280\n" \
"v_mfma_f32_16x16x32_bf16 v[156:159], a[232:235], a[116:119], v[156:159]\n" \
"ds_read_b64_tr_b16 a[206:207], v227 offset:35328\n" \
"v_mfma_f32_16x16x32_bf16 v[144:147], a[236:239], a[72:75], v[144:147]\n" \
"ds_read_b64_tr_b16 a[216:217], v227 offset:41472\n" \
"v_mfma_f32_16x16x32_bf16 v[144:147], a[208:211], a[76:79], v[144:147]\n" \
"ds_read_b64_tr_b16 a[218:219], v227 offset:43520\n" \
"buffer_load_dwordx4 a[40:43], v209, %[srdz], s50 offen offset:2048 sc1\n" \
"v_mfma_f32_16x16x32_bf16 v[148:151], a[236:239], a[88:91], v[148:151]\n" \
"ds_read_b64_tr_b16 a[220:221], v227 offset:37376\n" \
"v_mfma_f32_16x16x32_bf16 v[148:151], a[208:211], a[92:95], v[148:151]\n" \
"ds_read_b64_tr_b16 a[222:223], v227 offset:39424\n" \
"v_mfma_f32_16x16x32_bf16 v[152:155], a[236:239], a[104:107], v[152:155]\n" \
"ds_read_b64_tr_b16 a[224:225], v227 offset:45568\n" \
"v_mfma_f32_16x16x32_bf16 v[152:155], a[208:211], a[108:111], v[152:155]\n" \
"ds_read_b64_tr_b16 a[226:227], v227 offset:47616\n" \
"buffer_load_dwordx4 a[44:47], v209, %[srdz], s50 offen offset:3072 sc1\n" \
"v_mfma_f32_16x16x32_bf16 v[156:159], a[236:239], a[120:123], v[156:159]\n" \
"v_mfma_f32_16x16x32_bf16 v[156:159], a[208:211], a[124:127], v[156:159]\n" \
"s_waitcnt vmcnt(63) lgkmcnt(12)\n" \
"s_nop 0\n" \
"v_mfma_f32_16x16x32_bf16 v[160:163], a[228:231], a[64:67], v[160:163]\n" \
"buffer_load_dwordx4 a[48:51], v210, %[srdz], s50 offen offset:0 sc1\n" \
"v_mfma_f32_16x16x32_bf16 v[160:163], a[212:215], a[68:71], v[160:163]\n" \
"v_mfma_f32_16x16x32_bf16 v[164:167], a[228:231], a[80:83], v[164:167]\n" \
"buffer_load_dwordx4 a[52:55], v210, %[srdz], s50 offen offset:1024 sc1\n" \
"v_mfma_f32_16x16x32_bf16 v[164:167], a[212:215], a[84:87], v[164:167]\n" \
"v_mfma_f32_16x16x32_bf16 v[168:171], a[228:231], a[96:99], v[168:171]\n" \
"s_waitcnt vmcnt(28) lgkmcnt(0)\n" \
"s_barrier\n" \
"v_mfma_f32_16x16x32_bf16 v[168:171], a[212:215], a[100:103], v[168:171]\n" \
"ds_read_b64_tr_b16 a[232:233], v220 offset:32768\n" \
"v_mfma_f32_16x16x32_bf16 v[172:175], a[228:231], a[112:115], v[172:175]\n" \
"ds_read_b64_tr_b16 a[234:235], v220 offset:34816\n" \
"v_mfma_f32_16x16x32_bf16 v[172:175], a[212:215], a[116:119], v[172:175]\n" \
"ds_read_b64_tr_b16 a[236:237], v220 offset:36864\n" \
"v_mfma_f32_16x16x32_bf16 v[160:163], a[196:199], a[72:75], v[160:163]\n" \
"ds_read_b64_tr_b16 a[238:239], v220 offset:38912\n" \
"v_mfma_f32_16x16x32_bf16 v[160:163], a[200:203], a[76:79], v[160:163]\n" \
"ds_read_b64_tr_b16 a[208:209], v220 offset:40960\n" \
"s_add_u32 m0, %[m0b], 49152\n" \
"s_cmp_gt_u32 s48, 6\n" \
"s_cselect_b32 s49, 6, 7\n" \
"s_lshl_b32 s49, s49, 18\n" \
"s_or_b32 s49, s49, s58\n" \
"buffer_load_dwordx4 %[wo0], %[srdt], s49 offen offset:0 lds\n" \
"v_mfma_f32_16x16x32_bf16 v[164:167], a[196:199], a[88:91], v[164:167]\n" \
"ds_read_b64_tr_b16 a[210:211], v220 offset:43008\n" \
"v_mfma_f32_16x16x32_bf16 v[164:167], a[200:203], a[92:95], v[164:167]\n" \
"ds_read_b64_tr_b16 a[228:229], v220 offset:45056\n" \
"buffer_load_dwordx4 a[56:59], v210, %[srdz], s50 offen offset:2048 sc1\n" \
"v_mfma_f32_16x16x32_bf16 v[168:171], a[196:199], a[104:107], v[168:171]\n" \
"ds_read_b64_tr_b16 a[230:231], v220 offset:47104\n" \
"v_mfma_f32_16x16x32_bf16 v[168:171], a[200:203], a[108:111], v[168:171]\n" \
"buffer_load_dwordx4 a[60:63], v210, %[srdz], s50 offen offset:3072 sc1\n" \
"v_mfma_f32_16x16x32_bf16 v[172:175], a[196:199], a[120:123], v[172:175]\n" \
"v_mfma_f32_16x16x32_bf16 v[172:175], a[200:203], a[124:127], v[172:175]\n" \
"s_waitcnt vmcnt(23) lgkmcnt(4)\n" \
"s_nop 0\n" \
"v_mfma_f32_16x16x32_bf16 v[176:179], a[204:207], a[64:67], v[176:179]\n" \
"v_mfma_f32_16x16x32_bf16 v[176:179], a[220:223], a[68:71], v[176:179]\n" \
"s_cmp_gt_u32 s48, 6\n" \
"s_cselect_b32 s50, 6, 7\n" \
"s_mul_i32 s50, s50, 152064\n" \
"buffer_load_dwordx4 a[128:131], %[oq0], %[srdz], s50 offen offset:0 sc1\n" \
"v_mfma_f32_16x16x32_bf16 v[180:183], a[204:207], a[80:83], v[180:183]\n" \
"v_mfma_f32_16x16x32_bf16 v[180:183], a[220:223], a[84:87], v[180:183]\n" \
"v_mfma_f32_16x16x32_bf16 v[184:187], a[204:207], a[96:99], v[184:187]\n" \
"v_mfma_f32_16x16x32_bf16 v[184:187], a[220:223], a[100:103], v[184:187]\n" \
"buffer_load_dwordx4 v212, %[srdt], s49 offen offset:1024 lds\n" \
"v_mfma_f32_16x16x32_bf16 v[188:191], a[204:207], a[112:115], v[188:191]\n" \
"ds_read_b64_tr_b16 a[212:213], v221 offset:32768\n" \
"v_mfma_f32_16x16x32_bf16 v[188:191], a[220:223], a[116:119], v[188:191]\n" \
"ds_read_b64_tr_b16 a[214:215], v221 offset:34816\n" \
"v_mfma_f32_16x16x32_bf16 v[128:131], a[232:235], v[0:3], v[192:195]\n" \
"ds_read_b64_tr_b16 a[196:197], v221 offset:36864\n" \
"buffer_load_dwordx4 a[132:135], %[oq0], %[srdz], s50 offen offset:1024 sc1\n" \
"v_mfma_f32_16x16x32_bf16 v[128:131], a[236:239], v[8:11], v[128:131]\n" \
"ds_read_b64_tr_b16 a[198:199], v221 offset:38912\n" \
"v_mfma_f32_16x16x32_bf16 v[132:135], a[232:235], v[32:35], v[196:199]\n" \
"buffer_load_dwordx4 a[136:139], %[oq0], %[srdz], s50 offen offset:2048 sc1\n" \
"v_mfma_f32_16x16x32_bf16 v[132:135], a[236:239], v[40:43], v[132:135]\n" \
"v_mfma_f32_16x16x32_bf16 v[136:139], a[232:235], v[64:67], v[200:203]\n" \
"v_mfma_f32_16x16x32_bf16 v[136:139], a[236:239], v[72:75], v[136:139]\n" \
"buffer_load_dwordx4 a[140:143], %[oq0], %[srdz], s50 offen offset:3072 sc1\n" \
"v_mfma_f32_16x16x32_bf16 v[140:143], a[232:235], v[96:99], v[204:207]\n" \
"ds_read_b64_tr_b16 a[200:201], v221 offset:40960\n" \
"v_mfma_f32_16x16x32_bf16 v[140:143], a[236:239], v[104:107], v[140:143]\n" \
"s_waitcnt vmcnt(26) lgkmcnt(5)\n" \
"s_nop 0\n" \
"v_mfma_f32_16x16x32_bf16 v[128:131], a[208:211], v[16:19], v[128:131]\n" \
"ds_read_b64_tr_b16 a[202:203], v221 offset:43008\n" \
"v_mfma_f32_16x16x32_bf16 v[128:131], a[228:231], v[24:27], v[128:131]\n" \
"ds_read_b64_tr_b16 a[204:205], v221 offset:45056\n" \
"v_mfma_f32_16x16x32_bf16 v[132:135], a[208:211], v[48:51], v[132:135]\n" \
"ds_read_b64_tr_b16 a[206:207], v221 offset:47104\n" \
"v_mfma_f32_16x16x32_bf16 v[132:135], a[228:231], v[56:59], v[132:135]\n" \
"buffer_load_dwordx4 a[144:147], v208, %[srdz], s50 offen offset:0 sc1\n" \
"v_mfma_f32_16x16x32_bf16 v[136:139], a[208:211], v[80:83], v[136:139]\n" \
"v_mfma_f32_16x16x32_bf16 v[136:139], a[228:231], v[88:91], v[136:139]\n" \
"buffer_load_dwordx4 v213, %[srdt], s49 offen offset:2048 lds\n" \
"v_mfma_f32_16x16x32_bf16 v[140:143], a[208:211], v[112:115], v[140:143]\n" \
"ds_read_b64_tr_b16 a[220:221], v220 offset:33280\n" \
"v_mfma_f32_16x16x32_bf16 v[140:143], a[228:231], v[120:123], v[140:143]\n" \
"ds_read_b64_tr_b16 a[222:223], v220 offset:35328\n" \
"v_mfma_f32_16x16x32_bf16 v[176:179], a[216:219], a[72:75], v[176:179]\n" \
"buffer_load_dwordx4 a[148:151], v208, %[srdz], s50 offen offset:1024 sc1\n" \
"v_mfma_f32_16x16x32_bf16 v[176:179], a[224:227], a[76:79], v[176:179]\n" \
"v_mfma_f32_16x16x32_bf16 v[180:183], a[216:219], a[88:91], v[180:183]\n" \
"buffer_load_dwordx4 a[152:155], v208, %[srdz], s50 offen offset:2048 sc1\n" \
"v_mfma_f32_16x16x32_bf16 v[180:183], a[224:227], a[92:95], v[180:183]\n" \
"v_mfma_f32_16x16x32_bf16 v[184:187], a[216:219], a[104:107], v[184:187]\n" \
"v_mfma_f32_16x16x32_bf16 v[184:187], a[224:227], a[108:111], v[184:187]\n" \
"v_cmp_u_f32_e64 s[44:45], v128, v132\n" \
"buffer_load_dwordx4 a[156:159], v208, %[srdz], s50 offen offset:3072 sc1\n" \
"v_mfma_f32_16x16x32_bf16 v[188:191], a[216:219], a[120:123], v[188:191]\n" \
"v_mfma_f32_16x16x32_bf16 v[188:191], a[224:227], a[124:127], v[188:191]\n" \
"v_cmp_u_f32_e64 s[56:57], v136, v140\n" \
"s_or_b64 s[56:57], s[56:57], s[44:45]\n" \
"s_cbranch_scc1 Lsp5_%=\n" \
"Lbk5_%=:\n" \
"s_waitcnt vmcnt(63) lgkmcnt(2)\n" \
"s_nop 0\n" \
"v_mfma_f32_16x16x32_bf16 v[144:147], a[212:215], v[0:3], v[144:147]\n" \
"ds_read_b64_tr_b16 a[216:217], v220 offset:37376\n" \
"v_mfma_f32_16x16x32_bf16 v[144:147], a[196:199], v[8:11], v[144:147]\n" \
"ds_read_b64_tr_b16 a[218:219], v220 offset:39424\n" \
"v_mfma_f32_16x16x32_bf16 v[148:151], a[212:215], v[32:35], v[148:151]\n" \
"ds_read_b64_tr_b16 a[208:209], v220 offset:41472\n" \
"v_mfma_f32_16x16x32_bf16 v[148:151], a[196:199], v[40:43], v[148:151]\n" \
"ds_read_b64_tr_b16 a[210:211], v220 offset:43520\n" \
"v_mfma_f32_16x16x32_bf16 v[152:155], a[212:215], v[64:67], v[152:155]\n" \
"ds_read_b64_tr_b16 a[224:225], v220 offset:45568\n" \
"buffer_load_dwordx4 a[160:163], v209, %[srdz], s50 offen offset:0 sc1\n" \
"v_mfma_f32_16x16x32_bf16 v[152:155], a[196:199], v[72:75], v[152:155]\n" \
"ds_read_b64_tr_b16 a[226:227], v220 offset:47616\n" \
"v_mfma_f32_16x16x32_bf16 v[156:159], a[212:215], v[96:99], v[156:159]\n" \
"ds_read_b64_tr_b16 a[228:229], v221 offset:33280\n" \
"buffer_load_dwordx4 v214, %[srdt], s49 offen offset:3072 lds\n" \
"v_mfma_f32_16x16x32_bf16 v[156:159], a[196:199], v[104:107], v[156:159]\n" \
"ds_read_b64_tr_b16 a[230:231], v221 offset:35328\n" \
"v_mfma_f32_16x16x32_bf16 v[144:147], a[200:203], v[16:19], v[144:147]\n" \
"ds_read_b64_tr_b16 a[232:233], v221 offset:41472\n" \
"v_readlane_b32 s64, %[tlo], 10\n" \
"v_readlane_b32 s65, %[thi], 10\n" \
"buffer_load_dwordx4 a[164:167], v209, %[srdz], s50 offen offset:1024 sc1\n" \
"v_mfma_f32_16x16x32_bf16 v[144:147], a[204:207], v[24:27], v[144:147]\n" \
"ds_read_b64_tr_b16 a[234:235], v221 offset:43520\n" \
"v_mfma_f32_16x16x32_bf16 v[148:151], a[200:203], v[48:51], v[148:151]\n" \
"ds_read_b64_tr_b16 a[236:237], v221 offset:37376\n" \
"v_mfma_f32_16x16x32_bf16 v[148:151], a[204:207], v[56:59], v[148:151]\n" \
"ds_read_b64_tr_b16 a[238:239], v221 offset:39424\n" \
"v_mfma_f32_16x16x32_bf16 v[152:155], a[200:203], v[80:83], v[152:155]\n" \
"ds_read_b64_tr_b16 a[212:213], v221 offset:45568\n" \
"buffer_load_dwordx4 a[168:171], v209, %[srdz], s50 offen offset:2048 sc1\n" \
"v_mfma_f32_16x16x32_bf16 v[152:155], a[204:207], v[88:91], v[152:155]\n" \
"ds_read_b64_tr_b16 a[214:215], v221 offset:47616\n" \
"v_mfma_f32_16x16x32_bf16 v[156:159], a[200:203], v[112:115], v[156:159]\n" \
"buffer_load_dwordx4 a[172:175], v209, %[srdz], s50 offen offset:3072 sc1\n" \
"v_mfma_f32_16x16x32_bf16 v[156:159], a[204:207], v[120:123], v[156:159]\n" \
"s_waitcnt vmcnt(63) lgkmcnt(12)\n" \
"s_nop 0\n" \
"v_mfma_f32_16x16x32_bf16 v[160:163], a[220:223], v[0:3], v[160:163]\n" \
"v_mfma_f32_16x16x32_bf16 v[160:163], a[216:219], v[8:11], v[160:163]\n" \
"buffer_load_dwordx4 a[176:179], v210, %[srdz], s50 offen offset:0 sc1\n" \
"v_mfma_f32_16x16x32_bf16 v[164:167], a[220:223], v[32:35], v[164:167]\n" \
"v_readlane_b32 s63, %[thi], 33\n" \
"v_lshlrev_b32_e64 v234, 3, v224\n" \
"v_mfma_f32_16x16x32_bf16 v[164:167], a[216:219], v[40:43], v[164:167]\n" \
"v_mfma_f32_16x16x32_bf16 v[168:171], a[220:223], v[64:67], v[168:171]\n" \
"v_and_b32_e32 v234, 0xc00, v234\n" \
"v_lshlrev_b32_e64 v235, 2, v224\n" \
"s_waitcnt vmcnt(25) lgkmcnt(0)\n" \
"s_barrier\n" \
"v_mfma_f32_16x16x32_bf16 v[168:171], a[216:219], v[72:75], v[168:171]\n" \
"ds_read_b64_tr_b16 a[196:197], v220 offset:49152\n" \
".if . - adaln_body != 53248\n" \
".error \"size model\"\n" \
".endif\n" \
"Lk12_%=:\n" \
"s_cbranch_execz Lk13_%=\n" \
"s_nop 0\n" \
"v_mfma_f32_16x16x32_bf16 v[172:175], a[220:223], v[96:99], v[172:175]\n" \
"ds_read_b64_tr_b16 a[198:199], v220 offset:51200\n" \
"v_and_b32_e32 v235, 0x7000, v235\n" \
"v_bfe_u32 v230, v224, 9, 1\n" \
"v_mfma_f32_16x16x32_bf16 v[172:175], a[216:219], v[104:107], v[172:175]\n" \
"ds_read_b64_tr_b16 a[200:201], v220 offset:53248\n" \
"v_mfma_f32_16x16x32_bf16 v[160:163], a[208:211], v[16:19], v[160:163]\n" \
"ds_read_b64_tr_b16 a[202:203], v220 offset:55296\n" \
"v_lshl_or_b32 v235, v230, 2, v235\n" \
"v_readlane_b32 s52, %[thi], 35\n" \
"buffer_load_dwordx4 a[180:183], v210, %[srdz], s50 offen offset:1024 sc1\n" \
"v_mfma_f32_16x16x32_bf16 v[160:163], a[224:227], v[24:27], v[160:163]\n" \
"ds_read_b64_tr_b16 a[204:205], v220 offset:57344\n" \
"v_or_b32_e64 v234, v234, v235\n" \
"v_mfma_f32_16x16x32_bf16 v[164:167], a[208:211], v[48:51], v[164:167]\n" \
"ds_read_b64_tr_b16 a[206:207], v220 offset:59392\n" \
"v_mfma_f32_16x16x32_bf16 v[164:167], a[224:227], v[56:59], v[164:167]\n" \
"ds_read_b64_tr_b16 a[220:221], v220 offset:61440\n" \
"v_mfma_f32_16x16x32_bf16 v[168:171], a[208:211], v[80:83], v[168:171]\n" \
"ds_read_b64_tr_b16 a[222:223], v220 offset:63488\n" \
"v_mfma_f32_16x16x32_bf16 v[168:171], a[224:227], v[88:91], v[168:171]\n" \
"buffer_load_dword v235, v234, %[srdz], s63 offen sc1\n" \
"v_mfma_f32_16x16x32_bf16 v[172:175], a[208:211], v[112:115], v[172:175]\n" \
"v_mfma_f32_16x16x32_bf16 v[172:175], a[224:227], v[120:123], v[172:175]\n" \
"s_waitcnt vmcnt(63) lgkmcnt(4)\n" \
"s_nop 0\n" \
"v_mfma_f32_16x16x32_bf16 v[176:179], a[228:231], v[0:3], v[176:179]\n" \
"buffer_load_dwordx4 a[184:187], v210, %[srdz], s50 offen offset:2048 sc1\n" \
"v_mfma_f32_16x16x32_bf16 v[176:179], a[236:239], v[8:11], v[176:179]\n" \
"v_mfma_f32_16x16x32_bf16 v[180:183], a[228:231], v[32:35], v[180:183]\n" \
"buffer_load_dwordx4 a[188:191], v210, %[srdz], s50 offen offset:3072 sc1\n" \
"v_mfma_f32_16x16x32_bf16 v[180:183], a[236:239], v[40:43], v[180:183]\n" \
"v_mfma_f32_16x16x32_bf16 v[184:187], a[228:231], v[64:67], v[184:187]\n" \
"v_mfma_f32_16x16x32_bf16 v[184:187], a[236:239], v[72:75], v[184:187]\n" \
"v_mfma_f32_16x16x32_bf16 v[188:191], a[228:231], v[96:99], v[188:191]\n" \
"ds_read_b64_tr_b16 a[216:217], v221 offset:49152\n" \
"s_waitcnt vmcnt(23)\n" \
"v_cmp_eq_u32_e32 vcc, 0, v232\n" \
"s_cbranch_vccnz Lspp_%=\n" \
"Lbkp_%=:\n" \
"s_nop 0\n" \
"v_mfma_f32_16x16x32_bf16 v[188:191], a[236:239], v[104:107], v[188:191]\n" \
"ds_read_b64_tr_b16 a[218:219], v221 offset:51200\n" \
"v_mfma_f32_16x16x32_bf16 v[192:195], a[196:199], a[0:3], v[128:131]\n" \
"ds_read_b64_tr_b16 a[208:209], v221 offset:53248\n" \
"v_mfma_f32_16x16x32_bf16 v[192:195], a[200:203], a[4:7], v[192:195]\n" \
"ds_read_b64_tr_b16 a[210:211], v221 offset:55296\n" \
"s_add_u32 m0, %[m0b], 0\n" \
"s_cmp_gt_u32 s48, 3\n" \
"s_cselect_b32 s49, 3, 4\n" \
"s_lshl_b32 s49, s49, 18\n" \
"s_or_b32 s49, s49, s59\n" \
"s_nop 0\n" \
"buffer_load_dwordx4 %[wo0], %[srdt], s49 offen offset:0 lds\n" \
"v_mfma_f32_16x16x32_bf16 v[196:199], a[196:199], a[16:19], v[132:135]\n" \
"v_mfma_f32_16x16x32_bf16 v[196:199], a[200:203], a[20:23], v[196:199]\n" \
"v_mfma_f32_16x16x32_bf16 v[200:203], a[196:199], a[32:35], v[136:139]\n" \
"v_mfma_f32_16x16x32_bf16 v[200:203], a[200:203], a[36:39], v[200:203]\n" \
"v_mfma_f32_16x16x32_bf16 v[204:207], a[196:199], a[48:51], v[140:143]\n" \
"ds_read_b64_tr_b16 a[224:225], v221 offset:57344\n" \
"v_mfma_f32_16x16x32_bf16 v[204:207], a[200:203], a[52:55], v[204:207]\n" \
"s_waitcnt vmcnt(21) lgkmcnt(5)\n" \
"s_nop 0\n" \
"v_mfma_f32_16x16x32_bf16 v[192:195], a[204:207], a[8:11], v[192:195]\n" \
"ds_read_b64_tr_b16 a[226:227], v221 offset:59392\n" \
"v_mfma_f32_16x16x32_bf16 v[192:195], a[220:223], a[12:15], v[192:195]\n" \
"ds_read_b64_tr_b16 a[228:229], v221 offset:61440\n" \
"v_mfma_f32_16x16x32_bf16 v[196:199], a[204:207], a[24:27], v[196:199]\n" \
"ds_read_b64_tr_b16 a[230:231], v221 offset:63488\n" \
"v_mfma_f32_16x16x32_bf16 v[196:199], a[220:223], a[28:31], v[196:199]\n" \
"buffer_load_dwordx4 v212, %[srdt], s49 offen offset:1024 lds\n" \
"v_mfma_f32_16x16x32_bf16 v[200:203], a[204:207], a[40:43], v[200:203]\n" \
"v_mfma_f32_16x16x32_bf16 v[200:203], a[220:223], a[44:47], v[200:203]\n" \
"v_mfma_f32_16x16x32_bf16 v[204:207], a[204:207], a[56:59], v[204:207]\n" \
"ds_read_b64_tr_b16 a[236:237], v220 offset:49664\n" \
"v_mfma_f32_16x16x32_bf16 v[204:207], a[220:223], a[60:63], v[204:207]\n" \
"ds_read_b64_tr_b16 a[238:239], v220 offset:51712\n" \
"v_mfma_f32_16x16x32_bf16 v[176:179], a[232:235], v[16:19], v[176:179]\n" \
"v_mfma_f32_16x16x32_bf16 v[176:179], a[212:215], v[24:27], v[176:179]\n" \
"v_mfma_f32_16x16x32_bf16 v[180:183], a[232:235], v[48:51], v[180:183]\n" \
"buffer_load_dwordx4 v213, %[srdt], s49 offen offset:2048 lds\n" \
"v_mfma_f32_16x16x32_bf16 v[180:183], a[212:215], v[56:59], v[180:183]\n" \
"v_mfma_f32_16x16x32_bf16 v[184:187], a[232:235], v[80:83], v[184:187]\n" \
"s_add_u32 m0, %[m0b], 98304\n" \
"s_mov_b32 s61, 0\n" \
"s_nop 0\n" \
"buffer_load_dwordx4 %[vdn], s[64:67], s61 offen offset:0 lds\n" \
"v_mfma_f32_16x16x32_bf16 v[184:187], a[212:215], v[88:91], v[184:187]\n" \
"v_cmp_u_f32_e64 s[44:45], v192, v196\n" \
"v_mfma_f32_16x16x32_bf16 v[188:191], a[232:235], v[112:115], v[188:191]\n" \
"v_mfma_f32_16x16x32_bf16 v[188:191], a[212:215], v[120:123], v[188:191]\n" \
"v_cmp_u_f32_e64 s[56:57], v200, v204\n" \
"s_or_b64 s[56:57], s[56:57], s[44:45]\n" \
"s_cbranch_scc1 Lsp6_%=\n" \
"Lbk6_%=:\n" \
"s_waitcnt vmcnt(63) lgkmcnt(2)\n" \
"s_nop 0\n" \
"v_mfma_f32_16x16x32_bf16 v[144:147], a[216:219], a[0:3], v[144:147]\n" \
"ds_read_b64_tr_b16 a[232:233], v220 offset:53760\n" \
"v_mfma_f32_16x16x32_bf16 v[144:147], a[208:211], a[4:7], v[144:147]\n" \
"ds_read_b64_tr_b16 a[234:235], v220 offset:55808\n" \
"v_mfma_f32_16x16x32_bf16 v[148:151], a[216:219], a[16:19], v[148:151]\n" \
"ds_read_b64_tr_b16 a[196:197], v220 offset:57856\n" \
"v_mfma_f32_16x16x32_bf16 v[148:151], a[208:211], a[20:23], v[148:151]\n" \
"ds_read_b64_tr_b16 a[198:199], v220 offset:59904\n" \
"v_mfma_f32_16x16x32_bf16 v[152:155], a[216:219], a[32:35], v[152:155]\n" \
"ds_read_b64_tr_b16 a[200:201], v220 offset:61952\n" \
"v_mfma_f32_16x16x32_bf16 v[152:155], a[208:211], a[36:39], v[152:155]\n" \
"ds_read_b64_tr_b16 a[202:203], v220 offset:64000\n" \
"v_mfma_f32_16x16x32_bf16 v[156:159], a[216:219], a[48:51], v[156:159]\n" \
"ds_read_b64_tr_b16 a[204:205], v221 offset:49664\n" \
"v_mfma_f32_16x16x32_bf16 v[156:159], a[208:211], a[52:55], v[156:159]\n" \
"ds_read_b64_tr_b16 a[206:207], v221 offset:51712\n" \
"s_add_u32 m0, %[m0b], 0\n" \
"s_nop 0\n" \
"buffer_load_dwordx4 v214, %[srdt], s49 offen offset:3072 lds\n" \
"v_mfma_f32_16x16x32_bf16 v[144:147], a[224:227], a[8:11], v[144:147]\n" \
"ds_read_b64_tr_b16 a[212:213], v221 offset:57856\n" \
"v_mfma_f32_16x16x32_bf16 v[144:147], a[228:231], a[12:15], v[144:147]\n" \
"ds_read_b64_tr_b16 a[214:215], v221 offset:59904\n" \
"v_mfma_f32_16x16x32_bf16 v[148:151], a[224:227], a[24:27], v[148:151]\n" \
"ds_read_b64_tr_b16 a[220:221], v221 offset:53760\n" \
"v_mfma_f32_16x16x32_bf16 v[148:151], a[228:231], a[28:31], v[148:151]\n" \
"ds_read_b64_tr_b16 a[222:223], v221 offset:55808\n" \
"v_mfma_f32_16x16x32_bf16 v[152:155], a[224:227], a[40:43], v[152:155]\n" \
"ds_read_b64_tr_b16 a[216:217], v221 offset:61952\n" \
"v_mfma_f32_16x16x32_bf16 v[152:155], a[228:231], a[44:47], v[152:155]\n" \
"ds_read_b64_tr_b16 a[218:219], v221 offset:64000\n" \
"v_mfma_f32_16x16x32_bf16 v[156:159], a[224:227], a[56:59], v[156:159]\n" \
"v_xor_b32_e64 v218, 64, %[vdn]\n" \
"s_add_u32 m0, %[m0b], 99328\n" \
"s_mov_b32 s61, 4096\n" \
"buffer_load_dwordx4 v218, s[64:67], s61 offen offset:0 lds\n" \
"v_mfma_f32_16x16x32_bf16 v[156:159], a[228:231], a[60:63], v[156:159]\n" \
"s_waitcnt vmcnt(63) lgkmcnt(12)\n" \
"s_nop 0\n" \
"v_mfma_f32_16x16x32_bf16 v[160:163], a[236:239], a[0:3], v[160:163]\n" \
"v_mfma_f32_16x16x32_bf16 v[160:163], a[232:235], a[4:7], v[160:163]\n" \
"v_mfma_f32_16x16x32_bf16 v[164:167], a[236:239], a[16:19], v[164:167]\n" \
"v_mfma_f32_16x16x32_bf16 v[164:167], a[232:235], a[20:23], v[164:167]\n" \
"s_add_u32 m0, %[m0b], 16384\n" \
"s_cmp_gt_u32 s48, 4\n" \
"s_cselect_b32 s49, 4, 5\n" \
"s_lshl_b32 s49, s49, 18\n" \
"s_or_b32 s49, s49, s59\n" \
"buffer_load_dwordx4 %[wo0], %[srdt], s49 offen offset:0 lds\n" \
"v_mfma_f32_16x16x32_bf16 v[168:171], a[236:239], a[32:35], v[168:171]\n" \
"s_waitcnt vmcnt(15) lgkmcnt(0)\n" \
"s_barrier\n" \
"v_mfma_f32_16x16x32_bf16 v[168:171], a[232:235], a[36:39], v[168:171]\n" \
"ds_read_b64_tr_b16 a[208:209], v215 offset:49152\n" \
"v_mfma_f32_16x16x32_bf16 v[172:175], a[236:239], a[48:51], v[172:175]\n" \
"ds_read_b64_tr_b16 a[210:211], v215 offset:51200\n" \
"v_mfma_f32_16x16x32_bf16 v[172:175], a[232:235], a[52:55], v[172:175]\n" \
"ds_read_b64_tr_b16 a[224:225], v215 offset:53248\n" \
"v_mfma_f32_16x16x32_bf16 v[160:163], a[196:199], a[8:11], v[160:163]\n" \
"ds_read_b64_tr_b16 a[226:227], v215 offset:55296\n" \
"v_mfma_f32_16x16x32_bf16 v[160:163], a[200:203], a[12:15], v[160:163]\n" \
"ds_read_b64_tr_b16 a[228:229], v215 offset:57344\n" \
"v_mfma_f32_16x16x32_bf16 v[164:167], a[196:199], a[24:27], v[164:167]\n" \
"ds_read_b64_tr_b16 a[230:231], v215 offset:59392\n" \
"v_mfma_f32_16x16x32_bf16 v[164:167], a[200:203], a[28:31], v[164:167]\n" \
"ds_read_b64_tr_b16 a[236:237], v215 offset:61440\n" \
"v_mfma_f32_16x16x32_bf16 v[168:171], a[196:199], a[40:43], v[168:171]\n" \
"ds_read_b64_tr_b16 a[238:239], v215 offset:63488\n" \
"v_mfma_f32_16x16x32_bf16 v[168:171], a[200:203], a[44:47], v[168:171]\n" \
"buffer_load_dwordx4 v212, %[srdt], s49 offen offset:1024 lds\n" \
"v_mfma_f32_16x16x32_bf16 v[172:175], a[196:199], a[56:59], v[172:175]\n" \
"v_mfma_f32_16x16x32_bf16 v[172:175], a[200:203], a[60:63], v[172:175]\n" \
"s_waitcnt vmcnt(63) lgkmcnt(4)\n" \
"s_nop 0\n" \
"v_mfma_f32_16x16x32_bf16 v[176:179], a[204:207], a[0:3], v[176:179]\n" \
"v_xor_b32_e32 v218, 0x80, %[vdn]\n" \
"s_add_u32 m0, %[m0b], 100352\n" \
"s_mov_b32 s61, 8192\n" \
"buffer_load_dwordx4 v218, s[64:67], s61 offen offset:0 lds\n" \
"v_mfma_f32_16x16x32_bf16 v[176:179], a[220:223], a[4:7], v[176:179]\n" \
"v_mfma_f32_16x16x32_bf16 v[180:183], a[204:207], a[16:19], v[180:183]\n" \
"s_waitcnt vmcnt(11)\n" \
"v_and_b32_e32 v230, 1, v235\n" \
"v_mfma_f32_16x16x32_bf16 v[180:183], a[220:223], a[20:23], v[180:183]\n" \
"v_cmp_eq_u32_e64 s[68:69], 0, v230\n" \
"v_mfma_f32_16x16x32_bf16 v[184:187], a[204:207], a[32:35], v[184:187]\n" \
"s_cmp_lg_u64 s[68:69], 0\n" \
"s_cbranch_scc1 Ls1_%=\n" \
"Ls1b_%=:\n" \
"v_lshlrev_b32_e64 v216, 16, v235\n" \
"v_mfma_f32_16x16x32_bf16 v[184:187], a[220:223], a[36:39], v[184:187]\n" \
"v_and_b32_e32 v235, 0xffff0000, v235\n" \
"v_mfma_f32_16x16x32_bf16 v[188:191], a[204:207], a[48:51], v[188:191]\n" \
"ds_read_b64_tr_b16 a[232:233], v227 offset:49152\n" \
"v_add_f32_e64 v235, %[rd0], v235\n" \
"v_mfma_f32_16x16x32_bf16 v[188:191], a[220:223], a[52:55], v[188:191]\n" \
"ds_read_b64_tr_b16 a[234:235], v227 offset:51200\n" \
"v_bfe_u32 v234, v224, 9, 1\n" \
"v_mfma_f32_16x16x32_bf16 v[128:131], a[208:211], a[128:131], v[192:195]\n" \
"ds_read_b64_tr_b16 a[196:197], v227 offset:53248\n" \
"v_add_u32_e64 v230, s52, v234\n" \
"v_readlane_b32 s52, %[tlo], 36\n" \
"v_mfma_f32_16x16x32_bf16 v[128:131], a[224:227], a[132:135], v[128:131]\n" \
"ds_read_b64_tr_b16 a[198:199], v227 offset:55296\n" \
"v_permlane32_swap_b32_e32 v235, v216\n" \
"s_nop 0\n" \
"v_mfma_f32_16x16x32_bf16 v[132:135], a[208:211], a[144:147], v[196:199]\n" \
"v_add_f32_e64 v235, v235, v216\n" \
"s_add_u32 m0, %[m0b], 16384\n" \
"s_nop 0\n" \
"s_nop 0\n" \
"buffer_load_dwordx4 v213, %[srdt], s49 offen offset:2048 lds\n" \
"v_mfma_f32_16x16x32_bf16 v[132:135], a[224:227], a[148:151], v[132:135]\n" \
"v_mfma_f32_16x16x32_bf16 v[136:139], a[208:211], a[160:163], v[200:203]\n" \
"v_cmp_eq_u32_e64 s[68:69], 0, v234\n" \
"s_mul_i32 s63, %[wid], 4088\n" \
"v_mfma_f32_16x16x32_bf16 v[136:139], a[224:227], a[164:167], v[136:139]\n" \
"v_add_f32_dpp v235, v235, v235 quad_perm:[1,0,3,2] row_mask:0xf bank_mask:0xf\n" \
"s_sub_u32 s63, %[m0b], s63\n" \
"s_nop 0\n" \
"v_mfma_f32_16x16x32_bf16 v[140:143], a[208:211], a[176:179], v[204:207]\n" \
"ds_read_b64_tr_b16 a[200:201], v227 offset:57344\n" \
"v_lshl_add_u32 v234, v234, 2, s63\n" \
"v_readlane_b32 s63, %[thi], 36\n" \
"v_mfma_f32_16x16x32_bf16 v[140:143], a[224:227], a[180:183], v[140:143]\n" \
"s_waitcnt vmcnt(10) lgkmcnt(5)\n" \
"s_nop 0\n" \
"v_mfma_f32_16x16x32_bf16 v[128:131], a[228:231], a[136:139], v[128:131]\n" \
"ds_read_b64_tr_b16 a[202:203], v227 offset:59392\n" \
"v_add_f32_dpp v235, v235, v235 quad_perm:[2,3,0,1] row_mask:0xf bank_mask:0xf\n" \
"v_mfma_f32_16x16x32_bf16 v[128:131], a[236:239], a[140:143], v[128:131]\n" \
"ds_read_b64_tr_b16 a[204:205], v227 offset:61440\n" \
"v_sub_u32_e32 v230, 0x7ff, v230\n" \
"v_xor_b32_e32 v218, 0xc0, %[vdn]\n" \
"s_add_u32 m0, %[m0b], 101376\n" \
"s_mov_b32 s61, 12288\n" \
"buffer_load_dwordx4 v218, s[64:67], s61 offen offset:0 lds\n" \
"v_mfma_f32_16x16x32_bf16 v[132:135], a[228:231], a[152:155], v[132:135]\n" \
"ds_read_b64_tr_b16 a[206:207], v227 offset:63488\n" \
"v_add_f32_dpp v235, v235, v235 row_ror:8 row_mask:0xf bank_mask:0xf\n" \
"v_mfma_f32_16x16x32_bf16 v[132:135], a[236:239], a[156:159], v[132:135]\n" \
"v_mov_b32_e64 v216, v235\n" \
"v_add_u32_e32 v234, 0xcc04, v234\n" \
"s_add_u32 m0, %[m0b], 16384\n" \
"s_nop 0\n" \
"s_nop 0\n" \
"buffer_load_dwordx4 v214, %[srdt], s49 offen offset:3072 lds\n" \
"v_mfma_f32_16x16x32_bf16 v[136:139], a[228:231], a[168:171], v[136:139]\n" \
"v_mfma_f32_16x16x32_bf16 v[136:139], a[236:239], a[172:175], v[136:139]\n" \
"v_permlane16_swap_b32_e32 v235, v216\n" \
"s_nop 0\n" \
"v_mfma_f32_16x16x32_bf16 v[140:143], a[228:231], a[184:187], v[140:143]\n" \
"ds_read_b64_tr_b16 a[220:221], v215 offset:49664\n" \
"v_add_f32_e64 v235, v235, v216\n" \
"v_mfma_f32_16x16x32_bf16 v[140:143], a[236:239], a[188:191], v[140:143]\n" \
"ds_read_b64_tr_b16 a[222:223], v215 offset:51712\n" \
"v_mov_b32_e64 v216, v235\n" \
"v_mfma_f32_16x16x32_bf16 v[176:179], a[212:215], a[8:11], v[176:179]\n" \
"v_mov_b32_e64 %[rd0], s63\n" \
"v_mfma_f32_16x16x32_bf16 v[176:179], a[216:219], a[12:15], v[176:179]\n" \
"v_permlane32_swap_b32_e32 v235, v216\n" \
"s_nop 0\n" \
"v_mfma_f32_16x16x32_bf16 v[180:183], a[212:215], a[24:27], v[180:183]\n" \
"v_add_f32_e64 %[rd0], %[rd0], v216\n" \
"v_mfma_f32_16x16x32_bf16 v[180:183], a[216:219], a[28:31], v[180:183]\n" \
"v_add_f32_e64 v216, s52, v216\n" \
"v_mfma_f32_16x16x32_bf16 v[184:187], a[212:215], a[40:43], v[184:187]\n" \
"v_cndmask_b32_e64 v216, %[rd0], v216, s[68:69]\n" \
"v_mfma_f32_16x16x32_bf16 v[184:187], a[216:219], a[44:47], v[184:187]\n" \
"v_cmp_u_f32_e64 s[44:45], v128, v132\n" \
"v_rsq_f32_e64 v216, v216\n" \
"v_mfma_f32_16x16x32_bf16 v[188:191], a[212:215], a[56:59], v[188:191]\n" \
"v_mfma_f32_16x16x32_bf16 v[188:191], a[216:219], a[60:63], v[188:191]\n" \
"v_cmp_u_f32_e64 s[56:57], v136, v140\n" \
"s_or_b64 s[56:57], s[56:57], s[44:45]\n" \
"s_cbranch_scc1 Lsp7_%=\n" \
"Lbk7_%=:\n" \
"s_waitcnt vmcnt(63) lgkmcnt(2)\n" \
"s_nop 0\n" \
"v_mfma_f32_16x16x32_bf16 v[144:147], a[232:235], a[128:131], v[144:147]\n" \
"ds_read_b64_tr_b16 a[212:213], v215 offset:53760\n" \
"s_nop 0\n" \
"s_nop 0\n" \
"v_mfma_f32_16x16x32_bf16 v[144:147], a[196:199], a[132:135], v[144:147]\n" \
"ds_read_b64_tr_b16 a[214:215], v215 offset:55808\n" \
"v_mul_f32_e64 v235, v235, v216\n" \
"v_mfma_f32_16x16x32_bf16 v[148:151], a[232:235], a[144:147], v[148:151]\n" \
"ds_read_b64_tr_b16 a[208:209], v215 offset:57856\n" \
"v_max_f32_e32 v235, 0xff800000, v235\n" \
"v_mfma_f32_16x16x32_bf16 v[148:151], a[196:199], a[148:151], v[148:151]\n" \
"ds_read_b64_tr_b16 a[210:211], v215 offset:59904\n" \
"s_lshl_b32 s61, %[wid], 10\n" \
"v_lshrrev_b32_e32 v231, 3, v224\n" \
"v_add_u32_e32 v231, s61, v231\n" \
"v_ashrrev_i32_e32 v216, 31, v235\n" \
"s_add_u32 m0, %[m0b], 32768\n" \
"s_cmp_gt_u32 s48, 5\n" \
"s_cselect_b32 s49, 5, 6\n" \
"s_lshl_b32 s49, s49, 18\n" \
"s_or_b32 s49, s49, s59\n" \
"buffer_load_dwordx4 %[wo0], %[srdt], s49 offen offset:0 lds\n" \
"v_mfma_f32_16x16x32_bf16 v[152:155], a[232:235], a[160:163], v[152:155]\n" \
"ds_read_b64_tr_b16 a[216:217], v215 offset:61952\n" \
"v_readlane_b32 s44, %[tlo], 20\n" \
"v_readlane_b32 s45, %[thi], 20\n" \
"v_lshrrev_b32_e64 v236, 1, v231\n" \
"v_or_b32_e32 v216, 0x80000000, v216\n" \
"v_mfma_f32_16x16x32_bf16 v[152:155], a[196:199], a[164:167], v[152:155]\n" \
"ds_read_b64_tr_b16 a[218:219], v215 offset:64000\n" \
"v_xor_b32_e64 v235, v235, v216\n" \
"v_mfma_f32_16x16x32_bf16 v[156:159], a[232:235], a[176:179], v[156:159]\n" \
"ds_read_b64_tr_b16 a[224:225], v227 offset:49664\n" \
"v_and_b32_e32 v235, 0xfffff800, v235\n" \
"v_mfma_f32_16x16x32_bf16 v[156:159], a[196:199], a[180:183], v[156:159]\n" \
"ds_read_b64_tr_b16 a[226:227], v227 offset:51712\n" \
"v_or_b32_e64 v235, v235, v230\n" \
"v_mfma_f32_16x16x32_bf16 v[144:147], a[200:203], a[136:139], v[144:147]\n" \
"ds_read_b64_tr_b16 a[228:229], v227 offset:53760\n" \
"ds_write_b32 v234, v235 offset:65532\n" \
"v_mfma_f32_16x16x32_bf16 v[144:147], a[204:207], a[140:143], v[144:147]\n" \
"ds_read_b64_tr_b16 a[230:231], v227 offset:55808\n" \
"v_mfma_f32_16x16x32_bf16 v[148:151], a[200:203], a[152:155], v[148:151]\n" \
"s_mov_b32 s61, 262144\n" \
"buffer_load_dwordx4 v[192:195], v231, %[srdz], s61 offen offset:0 sc1\n" \
"v_mfma_f32_16x16x32_bf16 v[148:151], a[204:207], a[156:159], v[148:151]\n" \
"v_mfma_f32_16x16x32_bf16 v[152:155], a[200:203], a[168:171], v[152:155]\n" \
"buffer_load_dwordx4 v212, %[srdt], s49 offen offset:1024 lds\n" \
"v_mfma_f32_16x16x32_bf16 v[152:155], a[204:207], a[172:175], v[152:155]\n" \
"v_mfma_f32_16x16x32_bf16 v[156:159], a[200:203], a[184:187], v[156:159]\n" \
"ds_read_b64_tr_b16 a[236:237], v227 offset:57856\n" \
"v_mfma_f32_16x16x32_bf16 v[156:159], a[204:207], a[188:191], v[156:159]\n" \
"s_waitcnt vmcnt(63) lgkmcnt(6)\n" \
"s_nop 0\n" \
"v_mfma_f32_16x16x32_bf16 v[160:163], a[220:223], a[128:131], v[160:163]\n" \
"ds_read_b64_tr_b16 a[238:239], v227 offset:59904\n" \
"v_mfma_f32_16x16x32_bf16 v[160:163], a[212:215], a[132:135], v[160:163]\n" \
"ds_read_b64_tr_b16 a[232:233], v227 offset:61952\n" \
"v_mfma_f32_16x16x32_bf16 v[164:167], a[220:223], a[144:147], v[164:167]\n" \
"ds_read_b64_tr_b16 a[234:235], v227 offset:64000\n" \
"s_mov_b32 s61, 266240\n" \
"buffer_load_dwordx4 v[196:199], v231, %[srdz], s61 offen offset:0 sc1\n" \
"v_mfma_f32_16x16x32_bf16 v[164:167], a[212:215], a[148:151], v[164:167]\n" \
"v_mfma_f32_16x16x32_bf16 v[168:171], a[220:223], a[160:163], v[168:171]\n" \
"v_cvt_pk_bf16_f32 v128, v128, v129\n" \
"buffer_load_dwordx4 v213, %[srdt], s49 offen offset:2048 lds\n" \
"v_mfma_f32_16x16x32_bf16 v[168:171], a[212:215], a[164:167], v[168:171]\n" \
"v_cvt_pk_bf16_f32 v129, v130, v131\n" \
"v_mfma_f32_16x16x32_bf16 v[172:175], a[220:223], a[176:179], v[172:175]\n" \
"v_cvt_pk_bf16_f32 v130, v144, v145\n" \
"v_mfma_f32_16x16x32_bf16 v[172:175], a[212:215], a[180:183], v[172:175]\n" \
"v_cvt_pk_bf16_f32 v131, v146, v147\n" \
"v_mfma_f32_16x16x32_bf16 v[160:163], a[208:211], a[136:139], v[160:163]\n" \
"s_add_u32 s55, s46, 0\n" \
"s_nop 0\n" \
"buffer_store_dwordx4 v[128:131], %[vxs], %[srdz], s55 offen offset:0 sc1\n" \
"v_lshrrev_b32_e32 v230, 5, v224\n" \
"s_lshl_b32 s63, %[wid], 12\n" \
"v_mfma_f32_16x16x32_bf16 v[160:163], a[216:219], a[140:143], v[160:163]\n" \
"v_and_b32_e32 v230, 28, v230\n" \
"s_sub_u32 s63, %[m0b], s63\n" \
"v_mfma_f32_16x16x32_bf16 v[164:167], a[208:211], a[152:155], v[164:167]\n" \
"v_cvt_pk_bf16_f32 v132, v132, v133\n" \
"s_mov_b32 s61, 270336\n" \
"buffer_load_dwordx4 v[200:203], v231, %[srdz], s61 offen offset:0 sc1\n" \
"s_add_u32 s63, s63, 117760\n" \
"v_add_u32_e64 v230, s63, v230\n" \
"v_mfma_f32_16x16x32_bf16 v[164:167], a[216:219], a[156:159], v[164:167]\n" \
"v_cvt_pk_bf16_f32 v133, v134, v135\n" \
"v_mfma_f32_16x16x32_bf16 v[168:171], a[208:211], a[168:171], v[168:171]\n" \
"s_waitcnt vmcnt(14) lgkmcnt(0)\n" \
"s_barrier\n" \
"v_mfma_f32_16x16x32_bf16 v[168:171], a[216:219], a[172:175], v[168:171]\n" \
"ds_read_b64_tr_b16 a[196:197], v215 offset:0\n" \
"v_cvt_pk_bf16_f32 v134, v148, v149\n" \
"ds_read_b32 v235, v230 offset:0\n" \
"v_readlane_b32 s63, %[thi], 34\n" \
"v_mfma_f32_16x16x32_bf16 v[172:175], a[208:211], a[184:187], v[172:175]\n" \
"ds_read_b64_tr_b16 a[198:199], v215 offset:2048\n" \
"v_cvt_pk_bf16_f32 v135, v150, v151\n" \
"v_mfma_f32_16x16x32_bf16 v[172:175], a[216:219], a[188:191], v[172:175]\n" \
"s_waitcnt vmcnt(63) lgkmcnt(7)\n" \
"s_nop 0\n" \
"v_mfma_f32_16x16x32_bf16 v[176:179], a[224:227], a[128:131], v[176:179]\n" \
"ds_read_b64_tr_b16 a[200:201], v215 offset:4096\n" \
"v_cvt_pk_bf16_f32 v136, v136, v137\n" \
"v_mfma_f32_16x16x32_bf16 v[176:179], a[228:231], a[132:135], v[176:179]\n" \
"ds_read_b64_tr_b16 a[202:203], v215 offset:6144\n" \
"v_cvt_pk_bf16_f32 v137, v138, v139\n" \
"v_mfma_f32_16x16x32_bf16 v[180:183], a[224:227], a[144:147], v[180:183]\n" \
"ds_read_b64_tr_b16 a[204:205], v215 offset:8192\n" \
"s_add_u32 s55, s46, 304128\n" \
"buffer_store_dwordx4 v[132:135], %[vxs], %[srdz], s55 offen offset:0 sc1\n" \
"v_mfma_f32_16x16x32_bf16 v[180:183], a[228:231], a[148:151], v[180:183]\n" \
"ds_read_b64_tr_b16 a[206:207], v215 offset:10240\n" \
"v_cvt_pk_bf16_f32 v138, v152, v153\n" \
"v_mfma_f32_16x16x32_bf16 v[184:187], a[224:227], a[160:163], v[184:187]\n" \
"ds_read_b64_tr_b16 a[220:221], v215 offset:12288\n" \
"v_cvt_pk_bf16_f32 v139, v154, v155\n" \
"s_mov_b32 s61, 274432\n" \
"buffer_load_dwordx4 v[204:207], v231, %[srdz], s61 offen offset:0 sc1\n" \
"s_waitcnt lgkmcnt(6)\n" \
"s_nop 0\n" \
"v_min_u32_dpp v234, v235, v235 quad_perm:[1,0,3,2] row_mask:0xf bank_mask:0xf\n" \
"v_max_u32_dpp v235, v235, v235 quad_perm:[1,0,3,2] row_mask:0xf bank_mask:0xf\n" \
"v_mfma_f32_16x16x32_bf16 v[184:187], a[228:231], a[164:167], v[184:187]\n" \
"ds_read_b64_tr_b16 a[222:223], v215 offset:14336\n" \
"v_mfma_f32_16x16x32_bf16 v[188:191], a[224:227], a[176:179], v[188:191]\n" \
"ds_read_b64_tr_b16 a[212:213], v227 offset:0\n" \
"v_cvt_pk_bf16_f32 v140, v140, v141\n" \
"v_min_u32_dpp v216, v235, v235 quad_perm:[2,3,0,1] row_mask:0xf bank_mask:0xf\n" \
"v_max_u32_dpp v230, v234, v234 quad_perm:[2,3,0,1] row_mask:0xf bank_mask:0xf\n" \
"buffer_load_dwordx4 v214, %[srdt], s49 offen offset:3072 lds\n" \
"v_mfma_f32_16x16x32_bf16 v[188:191], a[228:231], a[180:183], v[188:191]\n" \
"ds_read_b64_tr_b16 a[214:215], v227 offset:2048\n" \
"v_cvt_pk_bf16_f32 v141, v142, v143\n" \
"v_max_u32_dpp v235, v235, v235 quad_perm:[2,3,0,1] row_mask:0xf bank_mask:0xf\n" \
"v_max_u32_e64 v234, v216, v230\n" \
"v_mfma_f32_16x16x32_bf16 v[176:179], a[236:239], a[136:139], v[176:179]\n" \
"ds_read_b64_tr_b16 a[208:209], v227 offset:4096\n" \
"s_add_u32 s55, s46, 608256\n" \
"buffer_store_dwordx4 v[136:139], %[vxs], %[srdz], s55 offen offset:0 sc1\n" \
"v_mfma_f32_16x16x32_bf16 v[176:179], a[232:235], a[140:143], v[176:179]\n" \
"ds_read_b64_tr_b16 a[210:211], v227 offset:6144\n" \
"v_cvt_pk_bf16_f32 v142, v156, v157\n" \
"v_min_u32_dpp v216, v235, v235 row_half_mirror row_mask:0xf bank_mask:0xf\n" \
"v_max_u32_dpp v230, v234, v234 row_half_mirror row_mask:0xf bank_mask:0xf\n" \
"v_mfma_f32_16x16x32_bf16 v[180:183], a[236:239], a[152:155], v[180:183]\n" \
"v_cvt_pk_bf16_f32 v143, v158, v159\n" \
"v_max_u32_dpp v235, v235, v235 row_half_mirror row_mask:0xf bank_mask:0xf\n" \
"v_max_u32_e64 v234, v216, v230\n" \
"v_mfma_f32_16x16x32_bf16 v[180:183], a[232:235], a[156:159], v[180:183]\n" \
"s_add_u32 s55, s46, 912384\n" \
"buffer_store_dwordx4 v[140:143], %[vxs], %[srdz], s55 offen offset:0 sc1\n" \
"v_lshlrev_b32_e64 v230, 4, v224\n" \
"v_mfma_f32_16x16x32_bf16 v[184:187], a[236:239], a[168:171], v[184:187]\n" \
"v_mfma_f32_16x16x32_bf16 v[184:187], a[232:235], a[172:175], v[184:187]\n" \
"global_load_dwordx2 v[232:233], v236, s[44:45]\n" \
"v_mfma_f32_16x16x32_bf16 v[188:191], a[236:239], a[184:187], v[188:191]\n" \
"ds_read_b64_tr_b16 a[216:217], v227 offset:8192\n" \
"v_mfma_f32_16x16x32_bf16 v[188:191], a[232:235], a[188:191], v[188:191]\n" \
"s_waitcnt vmcnt(63) lgkmcnt(5)\n" \
"s_nop 0\n" \
"v_mfma_f32_16x16x32_bf16 v[4:7], a[196:199], a[64:67], v[4:7]\n" \
"ds_read_b64_tr_b16 a[218:219], v227 offset:10240\n" \
"s_mov_b64 exec, 3\n" \
"s_nop 0\n" \
"buffer_atomic_or_x2 v[234:235], v230, %[srdz], s63 offen sc1\n" \
"s_mov_b64 exec, -1\n" \
"s_nop 0\n" \
"v_mfma_f32_16x16x32_bf16 v[4:7], a[200:203], a[68:71], v[4:7]\n" \
"ds_read_b64_tr_b16 a[224:225], v227 offset:12288\n" \
"v_mfma_f32_16x16x32_bf16 v[36:39], a[196:199], a[80:83], v[36:39]\n" \
"ds_read_b64_tr_b16 a[226:227], v227 offset:14336\n" \
"v_mfma_f32_16x16x32_bf16 v[36:39], a[200:203], a[84:87], v[36:39]\n" \
"v_mfma_f32_16x16x32_bf16 v[68:71], a[196:199], a[96:99], v[68:71]\n" \
"v_cvt_pk_bf16_f32 v160, v160, v161\n" \
"v_mfma_f32_16x16x32_bf16 v[68:71], a[200:203], a[100:103], v[68:71]\n" \
"v_cvt_pk_bf16_f32 v161, v162, v163\n" \
"v_mfma_f32_16x16x32_bf16 v[100:103], a[196:199], a[112:115], v[100:103]\n" \
"ds_read_b64_tr_b16 a[228:229], v215 offset:512\n" \
"v_cvt_pk_bf16_f32 v162, v176, v177\n" \
"v_mfma_f32_16x16x32_bf16 v[100:103], a[200:203], a[116:119], v[100:103]\n" \
"ds_read_b64_tr_b16 a[230:231], v215 offset:2560\n" \
"v_cvt_pk_bf16_f32 v163, v178, v179\n" \
"v_mfma_f32_16x16x32_bf16 v[4:7], a[204:207], a[72:75], v[4:7]\n" \
"ds_read_b64_tr_b16 a[236:237], v215 offset:4608\n" \
"v_mfma_f32_16x16x32_bf16 v[4:7], a[220:223], a[76:79], v[4:7]\n" \
"ds_read_b64_tr_b16 a[238:239], v215 offset:6656\n" \
"v_mfma_f32_16x16x32_bf16 v[36:39], a[204:207], a[88:91], v[36:39]\n" \
"s_add_u32 s55, s46, 4096\n" \
"buffer_store_dwordx4 v[160:163], %[vxs], %[srdz], s55 offen offset:0 sc1\n" \
"v_mfma_f32_16x16x32_bf16 v[36:39], a[220:223], a[92:95], v[36:39]\n" \
"v_cvt_pk_bf16_f32 v164, v164, v165\n" \
"v_cvt_pk_bf16_f32 v165, v166, v167\n" \
"v_mfma_f32_16x16x32_bf16 v[68:71], a[204:207], a[104:107], v[68:71]\n" \
"v_cvt_pk_bf16_f32 v166, v180, v181\n" \
"s_add_u32 m0, %[m0b], 49152\n" \
"s_cmp_gt_u32 s48, 6\n" \
"s_cselect_b32 s49, 6, 7\n" \
"s_lshl_b32 s49, s49, 18\n" \
"s_or_b32 s49, s49, s59\n" \
"buffer_load_dwordx4 %[wo0], %[srdt], s49 offen offset:0 lds\n" \
"v_mfma_f32_16x16x32_bf16 v[68:71], a[220:223], a[108:111], v[68:71]\n" \
"v_cvt_pk_bf16_f32 v167, v182, v183\n" \
"v_mfma_f32_16x16x32_bf16 v[100:103], a[204:207], a[120:123], v[100:103]\n" \
"ds_read_b64_tr_b16 a[232:233], v215 offset:8704\n" \
"v_mfma_f32_16x16x32_bf16 v[100:103], a[220:223], a[124:127], v[100:103]\n" \
"s_waitcnt vmcnt(63) lgkmcnt(5)\n" \
"s_nop 0\n" \
"v_mfma_f32_16x16x32_bf16 v[12:15], a[212:215], a[64:67], v[12:15]\n" \
"ds_read_b64_tr_b16 a[234:235], v215 offset:10752\n" \
"v_cvt_pk_bf16_f32 v168, v168, v169\n" \
"v_mfma_f32_16x16x32_bf16 v[12:15], a[208:211], a[68:71], v[12:15]\n" \
"ds_read_b64_tr_b16 a[196:197], v215 offset:12800\n" \
"s_add_u32 s55, s46, 308224\n" \
"buffer_store_dwordx4 v[164:167], %[vxs], %[srdz], s55 offen offset:0 sc1\n" \
"v_mfma_f32_16x16x32_bf16 v[44:47], a[212:215], a[80:83], v[44:47]\n" \
"ds_read_b64_tr_b16 a[198:199], v215 offset:14848\n" \
"v_cvt_pk_bf16_f32 v169, v170, v171\n" \
"v_mfma_f32_16x16x32_bf16 v[44:47], a[208:211], a[84:87], v[44:47]\n" \
"v_cvt_pk_bf16_f32 v170, v184, v185\n" \
"v_cvt_pk_bf16_f32 v171, v186, v187\n" \
"v_mfma_f32_16x16x32_bf16 v[76:79], a[212:215], a[96:99], v[76:79]\n" \
"s_add_u32 s55, s46, 612352\n" \
"buffer_store_dwordx4 v[168:171], %[vxs], %[srdz], s55 offen offset:0 sc1\n" \
"v_mfma_f32_16x16x32_bf16 v[76:79], a[208:211], a[100:103], v[76:79]\n" \
"v_mfma_f32_16x16x32_bf16 v[108:111], a[212:215], a[112:115], v[108:111]\n" \
"ds_read_b64_tr_b16 a[200:201], v227 offset:512\n" \
"v_cvt_pk_bf16_f32 v172, v172, v173\n" \
"buffer_load_dwordx4 v212, %[srdt], s49 offen offset:1024 lds\n" \
"v_mfma_f32_16x16x32_bf16 v[108:111], a[208:211], a[116:119], v[108:111]\n" \
"ds_read_b64_tr_b16 a[202:203], v227 offset:2560\n" \
"v_cvt_pk_bf16_f32 v173, v174, v175\n" \
"v_mfma_f32_16x16x32_bf16 v[12:15], a[216:219], a[72:75], v[12:15]\n" \
"ds_read_b64_tr_b16 a[204:205], v227 offset:4608\n" \
"v_cvt_pk_bf16_f32 v174, v188, v189\n" \
"v_mfma_f32_16x16x32_bf16 v[12:15], a[224:227], a[76:79], v[12:15]\n" \
"ds_read_b64_tr_b16 a[206:207], v227 offset:6656\n" \
"v_cvt_pk_bf16_f32 v175, v190, v191\n" \
"v_mfma_f32_16x16x32_bf16 v[44:47], a[216:219], a[88:91], v[44:47]\n" \
"s_add_u32 s55, s46, 916480\n" \
"buffer_store_dwordx4 v[172:175], %[vxs], %[srdz], s55 offen offset:0 sc1\n" \
"v_mfma_f32_16x16x32_bf16 v[44:47], a[224:227], a[92:95], v[44:47]\n" \
"v_mfma_f32_16x16x32_bf16 v[76:79], a[216:219], a[104:107], v[76:79]\n" \
"buffer_load_dwordx4 v213, %[srdt], s49 offen offset:2048 lds\n" \
"v_mfma_f32_16x16x32_bf16 v[76:79], a[224:227], a[108:111], v[76:79]\n" \
"v_mfma_f32_16x16x32_bf16 v[108:111], a[216:219], a[120:123], v[108:111]\n" \
"ds_read_b64_tr_b16 a[220:221], v227 offset:8704\n" \
"v_mfma_f32_16x16x32_bf16 v[108:111], a[224:227], a[124:127], v[108:111]\n" \
"s_waitcnt vmcnt(63) lgkmcnt(5)\n" \
"s_nop 0\n" \
"v_mfma_f32_16x16x32_bf16 v[20:23], a[228:231], a[64:67], v[20:23]\n" \
"ds_read_b64_tr_b16 a[222:223], v227 offset:10752\n" \
"v_mfma_f32_16x16x32_bf16 v[20:23], a[236:239], a[68:71], v[20:23]\n" \
"ds_read_b64_tr_b16 a[212:213], v227 offset:12800\n" \
"v_mfma_f32_16x16x32_bf16 v[52:55], a[228:231], a[80:83], v[52:55]\n" \
"ds_read_b64_tr_b16 a[214:215], v227 offset:14848\n" \
"v_mfma_f32_16x16x32_bf16 v[52:55], a[236:239], a[84:87], v[52:55]\n" \
"global_load_dword v237, %[vct], %[pct]\n" \
"v_mfma_f32_16x16x32_bf16 v[84:87], a[228:231], a[96:99], v[84:87]\n" \
"v_mfma_f32_16x16x32_bf16 v[84:87], a[236:239], a[100:103], v[84:87]\n" \
"v_xor_b32_e32 v218, 0x80, %[vdn]\n" \
"s_add_u32 m0, %[m0b], 114688\n" \
"s_mov_b32 s61, 16384\n" \
"buffer_load_dwordx4 v218, s[64:67], s61 offen offset:0 lds\n" \
"v_mfma_f32_16x16x32_bf16 v[116:119], a[228:231], a[112:115], v[116:119]\n" \
"v_mfma_f32_16x16x32_bf16 v[116:119], a[236:239], a[116:119], v[116:119]\n" \
"s_add_u32 m0, %[m0b], 49152\n" \
"s_nop 0\n" \
"s_nop 0\n" \
"buffer_load_dwordx4 v214, %[srdt], s49 offen offset:3072 lds\n" \
"v_mfma_f32_16x16x32_bf16 v[20:23], a[232:235], a[72:75], v[20:23]\n" \
"v_mfma_f32_16x16x32_bf16 v[20:23], a[196:199], a[76:79], v[20:23]\n" \
"v_mfma_f32_16x16x32_bf16 v[52:55], a[232:235], a[88:91], v[52:55]\n" \
"v_mfma_f32_16x16x32_bf16 v[52:55], a[196:199], a[92:95], v[52:55]\n" \
"v_mfma_f32_16x16x32_bf16 v[84:87], a[232:235], a[104:107], v[84:87]\n" \
"s_waitcnt vmcnt(24) lgkmcnt(0)\n" \
"s_barrier\n" \
"v_mfma_f32_16x16x32_bf16 v[84:87], a[196:199], a[108:111], v[84:87]\n" \
"ds_read_b64_tr_b16 a[208:209], v215 offset:16384\n" \
"v_mfma_f32_16x16x32_bf16 v[116:119], a[232:235], a[120:123], v[116:119]\n" \
"ds_read_b64_tr_b16 a[210:211], v215 offset:18432\n" \
"v_mfma_f32_16x16x32_bf16 v[116:119], a[196:199], a[124:127], v[116:119]\n" \
"s_waitcnt vmcnt(63) lgkmcnt(2)\n" \
"s_nop 0\n" \
"v_mfma_f32_16x16x32_bf16 v[28:31], a[200:203], a[64:67], v[28:31]\n" \
"ds_read_b64_tr_b16 a[216:217], v215 offset:20480\n" \
".if . - adaln_body != 57344\n" \
".error \"size model\"\n" \
".endif\n" \
"Lk13_%=:\n" \
"s_cbranch_execz Lk14_%=\n" \
"s_nop 0\n" \
"v_mfma_f32_16x16x32_bf16 v[28:31], a[204:207], a[68:71], v[28:31]\n" \
"ds_read_b64_tr_b16 a[218:219], v215 offset:22528\n" \
"v_mfma_f32_16x16x32_bf16 v[60:63], a[200:203], a[80:83], v[60:63]\n" \
"ds_read_b64_tr_b16 a[224:225], v215 offset:24576\n" \
"v_mfma_f32_16x16x32_bf16 v[60:63], a[204:207], a[84:87], v[60:63]\n" \
"ds_read_b64_tr_b16 a[226:227], v215 offset:26624\n" \
"v_mfma_f32_16x16x32_bf16 v[92:95], a[200:203], a[96:99], v[92:95]\n" \
"ds_read_b64_tr_b16 a[228:229], v215 offset:28672\n" \
"v_xor_b32_e32 v218, 0xc0, %[vdn]\n" \
"s_add_u32 m0, %[m0b], 115712\n" \
"s_mov_b32 s61, 20480\n" \
"buffer_load_dwordx4 v218, s[64:67], s61 offen offset:0 lds\n" \
"v_mfma_f32_16x16x32_bf16 v[92:95], a[204:207], a[100:103], v[92:95]\n" \
"ds_read_b64_tr_b16 a[230:231], v215 offset:30720\n" \
"v_mfma_f32_16x16x32_bf16 v[124:127], a[200:203], a[112:115], v[124:127]\n" \
"ds_read_b64_tr_b16 a[236:237], v227 offset:16384\n" \
"v_mfma_f32_16x16x32_bf16 v[124:127], a[204:207], a[116:119], v[124:127]\n" \
"ds_read_b64_tr_b16 a[238:239], v227 offset:18432\n" \
"v_mfma_f32_16x16x32_bf16 v[28:31], a[220:223], a[72:75], v[28:31]\n" \
"ds_read_b64_tr_b16 a[232:233], v227 offset:20480\n" \
"v_mfma_f32_16x16x32_bf16 v[28:31], a[212:215], a[76:79], v[28:31]\n" \
"ds_read_b64_tr_b16 a[234:235], v227 offset:22528\n" \
"v_mfma_f32_16x16x32_bf16 v[60:63], a[220:223], a[88:91], v[60:63]\n" \
"v_mfma_f32_16x16x32_bf16 v[60:63], a[212:215], a[92:95], v[60:63]\n" \
"v_mfma_f32_16x16x32_bf16 v[92:95], a[220:223], a[104:107], v[92:95]\n" \
"v_mfma_f32_16x16x32_bf16 v[92:95], a[212:215], a[108:111], v[92:95]\n" \
"v_mfma_f32_16x16x32_bf16 v[124:127], a[220:223], a[120:123], v[124:127]\n" \
"ds_read_b64_tr_b16 a[196:197], v227 offset:24576\n" \
"v_mfma_f32_16x16x32_bf16 v[124:127], a[212:215], a[124:127], v[124:127]\n" \
"s_waitcnt vmcnt(63) lgkmcnt(5)\n" \
"s_nop 0\n" \
"v_mfma_f32_16x16x32_bf16 v[4:7], a[208:211], v[0:3], v[4:7]\n" \
"ds_read_b64_tr_b16 a[198:199], v227 offset:26624\n" \
"v_mfma_f32_16x16x32_bf16 v[4:7], a[216:219], v[8:11], v[4:7]\n" \
"ds_read_b64_tr_b16 a[200:201], v227 offset:28672\n" \
"v_mfma_f32_16x16x32_bf16 v[36:39], a[208:211], v[32:35], v[36:39]\n" \
"ds_read_b64_tr_b16 a[202:203], v227 offset:30720\n" \
"v_mfma_f32_16x16x32_bf16 v[36:39], a[216:219], v[40:43], v[36:39]\n" \
"s_waitcnt vmcnt(12)\n" \
"s_nop 0\n" \
"v_and_or_b32 v236, v192, v193, 0\n" \
"v_bitop3_b32 v236, v236, v194, v195 bitop3:0x80\n" \
"s_add_u32 m0, %[m0b], 116736\n" \
"s_mov_b32 s61, 24576\n" \
"buffer_load_dwordx4 %[vdn], s[64:67], s61 offen offset:0 lds\n" \
"v_mfma_f32_16x16x32_bf16 v[68:71], a[208:211], v[64:67], v[68:71]\n" \
"v_bitop3_b32 v236, v236, v196, v197 bitop3:0x80\n" \
"v_mfma_f32_16x16x32_bf16 v[68:71], a[216:219], v[72:75], v[68:71]\n" \
"v_bitop3_b32 v236, v236, v198, v199 bitop3:0x80\n" \
"v_mfma_f32_16x16x32_bf16 v[100:103], a[208:211], v[96:99], v[100:103]\n" \
"v_bitop3_b32 v236, v236, v200, v201 bitop3:0x80\n" \
"v_mfma_f32_16x16x32_bf16 v[100:103], a[216:219], v[104:107], v[100:103]\n" \
"v_bitop3_b32 v236, v236, v202, v203 bitop3:0x80\n" \
"v_mfma_f32_16x16x32_bf16 v[4:7], a[224:227], v[16:19], v[4:7]\n" \
"v_bitop3_b32 v236, v236, v204, v205 bitop3:0x80\n" \
"v_mfma_f32_16x16x32_bf16 v[4:7], a[228:231], v[24:27], v[4:7]\n" \
"v_bitop3_b32 v236, v236, v206, v207 bitop3:0x80\n" \
"v_mfma_f32_16x16x32_bf16 v[36:39], a[224:227], v[48:51], v[36:39]\n" \
"s_mov_b32 s54, 0x07060302\n" \
"v_and_b32_e64 v236, 1, v236\n" \
"v_mfma_f32_16x16x32_bf16 v[36:39], a[228:231], v[56:59], v[36:39]\n" \
"v_and_b32_e32 v231, 0x170, v231\n" \
"v_mfma_f32_16x16x32_bf16 v[68:71], a[224:227], v[80:83], v[68:71]\n" \
"s_waitcnt vmcnt(16) lgkmcnt(15)\n" \
"s_barrier\n" \
"v_mfma_f32_16x16x32_bf16 v[68:71], a[228:231], v[88:91], v[68:71]\n" \
"ds_read_b64_tr_b16 a[204:205], v215 offset:32768\n" \
"v_cmp_eq_u32_e64 s[56:57], 0, v236\n" \
"v_mfma_f32_16x16x32_bf16 v[100:103], a[224:227], v[112:115], v[100:103]\n" \
"ds_read_b64_tr_b16 a[206:207], v215 offset:34816\n" \
"v_lshrrev_b32_e64 v236, 7, v224\n" \
"v_mfma_f32_16x16x32_bf16 v[100:103], a[228:231], v[120:123], v[100:103]\n" \
"s_waitcnt vmcnt(13) lgkmcnt(2)\n" \
"s_nop 0\n" \
"v_mfma_f32_16x16x32_bf16 v[12:15], a[236:239], v[0:3], v[12:15]\n" \
"ds_read_b64_tr_b16 a[220:221], v215 offset:36864\n" \
"v_and_or_b32 v231, v236, 8, v231\n" \
"v_mfma_f32_16x16x32_bf16 v[12:15], a[232:235], v[8:11], v[12:15]\n" \
"ds_read_b64_tr_b16 a[222:223], v215 offset:38912\n" \
"v_lshrrev_b32_e64 v236, 12, v224\n" \
"v_mfma_f32_16x16x32_bf16 v[44:47], a[236:239], v[32:35], v[44:47]\n" \
"ds_read_b64_tr_b16 a[212:213], v215 offset:40960\n" \
"v_lshl_or_b32 v231, v236, 7, v231\n" \
"v_mfma_f32_16x16x32_bf16 v[44:47], a[232:235], v[40:43], v[44:47]\n" \
"ds_read_b64_tr_b16 a[214:215], v215 offset:43008\n" \
"s_mul_i32 s61, %[wid], 3584\n" \
"s_sub_u32 s61, %[m0b], s61\n" \
"v_add_u32_e32 v231, s61, v231\n" \
"v_mfma_f32_16x16x32_bf16 v[76:79], a[236:239], v[64:67], v[76:79]\n" \
"ds_read_b64_tr_b16 a[208:209], v215 offset:45056\n" \
"s_cmp_lg_u64 s[56:57], 0\n" \
"s_cbranch_scc1 Lpars_%=\n" \
"Lparb_%=:\n" \
"v_mov_b32_e64 v234, 0\n" \
"v_mfma_f32_16x16x32_bf16 v[76:79], a[232:235], v[72:75], v[76:79]\n" \
"ds_read_b64_tr_b16 a[210:211], v215 offset:47104\n" \
"v_lshlrev_b32_e64 v184, 16, v232\n" \
"v_mfma_f32_16x16x32_bf16 v[108:111], a[236:239], v[96:99], v[108:111]\n" \
"ds_read_b64_tr_b16 a[216:217], v227 offset:32768\n" \
"v_and_b32_e32 v185, 0xffff0000, v232\n" \
"v_mfma_f32_16x16x32_bf16 v[108:111], a[232:235], v[104:107], v[108:111]\n" \
"ds_read_b64_tr_b16 a[218:219], v227 offset:34816\n" \
"v_lshlrev_b32_e32 v186, 16, v233\n" \
"v_xor_b32_e32 v218, 64, %[vdn]\n" \
"s_add_u32 m0, %[m0b], 117760\n" \
"s_mov_b32 s61, 28672\n" \
"buffer_load_dwordx4 v218, s[64:67], s61 offen offset:0 lds\n" \
"v_mfma_f32_16x16x32_bf16 v[12:15], a[196:199], v[16:19], v[12:15]\n" \
"ds_read_b64_tr_b16 a[224:225], v227 offset:36864\n" \
"v_and_b32_e32 v187, 0xffff0000, v233\n" \
"v_mfma_f32_16x16x32_bf16 v[12:15], a[200:203], v[24:27], v[12:15]\n" \
"ds_read_b64_tr_b16 a[226:227], v227 offset:38912\n" \
"v_mul_f32_e64 v188, v200, v184\n" \
"v_mfma_f32_16x16x32_bf16 v[44:47], a[196:199], v[48:51], v[44:47]\n" \
"v_mul_f32_e32 v189, v201, v185\n" \
"v_mul_f32_e32 v190, v202, v186\n" \
"v_mfma_f32_16x16x32_bf16 v[44:47], a[200:203], v[56:59], v[44:47]\n" \
"v_mul_f32_e32 v191, v203, v187\n" \
"v_mul_f32_e32 v235, v196, v184\n" \
"v_mfma_f32_16x16x32_bf16 v[76:79], a[196:199], v[80:83], v[76:79]\n" \
"v_fmac_f32_e32 v235, v197, v185\n" \
"s_nop 0\n" \
"v_mfma_f32_16x16x32_bf16 v[76:79], a[200:203], v[88:91], v[76:79]\n" \
"v_fmac_f32_e32 v235, v198, v186\n" \
"s_nop 0\n" \
"v_mfma_f32_16x16x32_bf16 v[108:111], a[196:199], v[112:115], v[108:111]\n" \
"ds_read_b64_tr_b16 a[228:229], v227 offset:40960\n" \
"v_fmac_f32_e32 v235, v199, v187\n" \
"s_nop 0\n" \
"v_mfma_f32_16x16x32_bf16 v[108:111], a[200:203], v[120:123], v[108:111]\n" \
"s_waitcnt vmcnt(63) lgkmcnt(5)\n" \
"s_nop 0\n" \
"v_mfma_f32_16x16x32_bf16 v[4:7], a[204:207], a[0:3], v[4:7]\n" \
"ds_read_b64_tr_b16 a[230:231], v227 offset:43008\n" \
"v_add_f32_e64 v184, v188, v189\n" \
"v_mfma_f32_16x16x32_bf16 v[4:7], a[220:223], a[4:7], v[4:7]\n" \
"ds_read_b64_tr_b16 a[236:237], v227 offset:45056\n" \
"v_add_f32_e64 v185, v190, v191\n" \
"v_mfma_f32_16x16x32_bf16 v[36:39], a[204:207], a[16:19], v[36:39]\n" \
"ds_read_b64_tr_b16 a[238:239], v227 offset:47104\n" \
"v_add_f32_e64 v184, v184, v185\n" \
"v_mfma_f32_16x16x32_bf16 v[36:39], a[220:223], a[20:23], v[36:39]\n" \
"s_waitcnt vmcnt(7)\n" \
"v_mov_b32_e32 v230, 1\n" \
"buffer_store_dword v230, v225, %[srdz], s62 offen offset:64 sc1\n" \
"v_mfma_f32_16x16x32_bf16 v[68:71], a[204:207], a[32:35], v[68:71]\n" \
"v_lshrrev_b32_e32 v236, 12, v224\n" \
"v_permlane32_swap_b32_e32 v184, v235\n" \
"v_mfma_f32_16x16x32_bf16 v[68:71], a[220:223], a[36:39], v[68:71]\n" \
"v_mov_b32_e64 v230, 1\n" \
"buffer_atomic_add v230, v225, %[srdz], s47 offen sc1\n" \
"v_mfma_f32_16x16x32_bf16 v[100:103], a[204:207], a[48:51], v[100:103]\n" \
"v_add_f32_e64 v184, v184, v235\n" \
"s_mul_i32 s61, %[wid], 4092\n" \
"v_mfma_f32_16x16x32_bf16 v[100:103], a[220:223], a[52:55], v[100:103]\n" \
"v_add_f32_dpp v184, v184, v184 quad_perm:[1,0,3,2] row_mask:0xf bank_mask:0xf\n" \
"s_sub_u32 s61, %[m0b], s61\n" \
"s_nop 0\n" \
"v_mfma_f32_16x16x32_bf16 v[4:7], a[212:215], a[8:11], v[4:7]\n" \
"v_add_f32_dpp v184, v184, v184 quad_perm:[2,3,0,1] row_mask:0xf bank_mask:0xf\n" \
"v_mfma_f32_16x16x32_bf16 v[4:7], a[208:211], a[12:15], v[4:7]\n" \
"v_lshl_add_u32 v236, v236, 4, s61\n" \
"v_mfma_f32_16x16x32_bf16 v[36:39], a[212:215], a[24:27], v[36:39]\n" \
"v_add_f32_dpp v184, v184, v184 row_ror:4 row_mask:0xf bank_mask:0xf\n" \
"v_mfma_f32_16x16x32_bf16 v[36:39], a[208:211], a[28:31], v[36:39]\n" \
"s_nop 0\n" \
"s_nop 0\n" \
"v_mfma_f32_16x16x32_bf16 v[68:71], a[212:215], a[40:43], v[68:71]\n" \
"s_waitcnt vmcnt(5) lgkmcnt(15)\n" \
"s_barrier\n" \
"v_mfma_f32_16x16x32_bf16 v[68:71], a[208:211], a[44:47], v[68:71]\n" \
"ds_read_b64_tr_b16 a[232:233], v215 offset:49152\n" \
"v_add_f32_dpp v184, v184, v184 row_ror:8 row_mask:0xf bank_mask:0xf\n" \
"v_mfma_f32_16x16x32_bf16 v[100:103], a[212:215], a[56:59], v[100:103]\n" \
"ds_read_b64_tr_b16 a[234:235], v215 offset:51200\n" \
"v_mov_b32_e64 v185, v184\n" \
"v_mfma_f32_16x16x32_bf16 v[100:103], a[208:211], a[60:63], v[100:103]\n" \
"s_waitcnt vmcnt(63) lgkmcnt(2)\n" \
"s_nop 0\n" \
"v_mfma_f32_16x16x32_bf16 v[12:15], a[216:219], a[0:3], v[12:15]\n" \
"ds_read_b64_tr_b16 a[196:197], v215 offset:53248\n" \
"s_nop 0\n" \
"s_nop 0\n" \
"v_mfma_f32_16x16x32_bf16 v[12:15], a[224:227], a[4:7], v[12:15]\n" \
"ds_read_b64_tr_b16 a[198:199], v215 offset:55296\n" \
"v_permlane16_swap_b32_e32 v184, v185\n" \
"s_nop 0\n" \
"v_mfma_f32_16x16x32_bf16 v[44:47], a[216:219], a[16:19], v[44:47]\n" \
"ds_read_b64_tr_b16 a[200:201], v215 offset:57344\n" \
"v_add_f32_e64 v184, v184, v185\n" \
"v_mfma_f32_16x16x32_bf16 v[44:47], a[224:227], a[20:23], v[44:47]\n" \
"ds_read_b64_tr_b16 a[202:203], v215 offset:59392\n" \
"v_bfe_u32 v186, v231, 3, 1\n" \
"v_mfma_f32_16x16x32_bf16 v[76:79], a[216:219], a[32:35], v[76:79]\n" \
"ds_read_b64_tr_b16 a[204:205], v215 offset:61440\n" \
"s_movk_i32 s61, 2040\n" \
"s_nop 0\n" \
"v_mfma_f32_16x16x32_bf16 v[76:79], a[224:227], a[36:39], v[76:79]\n" \
"ds_read_b64_tr_b16 a[206:207], v215 offset:63488\n" \
"v_mad_u32_u24 v186, v186, s61, v231\n" \
"v_mfma_f32_16x16x32_bf16 v[108:111], a[216:219], a[48:51], v[108:111]\n" \
"ds_read_b64_tr_b16 a[220:221], v227 offset:49152\n" \
"v_perm_b32 v196, v197, v196, s54\n" \
"v_mfma_f32_16x16x32_bf16 v[108:111], a[224:227], a[52:55], v[108:111]\n" \
"ds_read_b64_tr_b16 a[222:223], v227 offset:51200\n" \
"v_perm_b32 v197, v199, v198, s54\n" \
"v_mfma_f32_16x16x32_bf16 v[12:15], a[228:231], a[8:11], v[12:15]\n" \
"ds_read_b64_tr_b16 a[212:213], v227 offset:53248\n" \
"v_perm_b32 v200, v201, v200, s54\n" \
"v_mfma_f32_16x16x32_bf16 v[12:15], a[236:239], a[12:15], v[12:15]\n" \
"ds_read_b64_tr_b16 a[214:215], v227 offset:55296\n" \
"v_perm_b32 v201, v203, v202, s54\n" \
"v_mfma_f32_16x16x32_bf16 v[44:47], a[228:231], a[24:27], v[44:47]\n" \
"v_perm_b32 v204, v205, v204, s54\n" \
"v_perm_b32 v205, v207, v206, s54\n" \
"v_mfma_f32_16x16x32_bf16 v[44:47], a[236:239], a[28:31], v[44:47]\n" \
"ds_write_b32 v236, v184 offset:14368\n" \
"v_mfma_f32_16x16x32_bf16 v[76:79], a[228:231], a[40:43], v[76:79]\n" \
"ds_write_b32 v236, v234 offset:14336\n" \
"v_mfma_f32_16x16x32_bf16 v[76:79], a[236:239], a[44:47], v[76:79]\n" \
"ds_write_b128 v186, v[192:195] offset:6144\n" \
"v_mfma_f32_16x16x32_bf16 v[108:111], a[228:231], a[56:59], v[108:111]\n" \
"ds_read_b64_tr_b16 a[208:209], v227 offset:57344\n" \
"ds_write_b128 v186, v[188:191] offset:10240\n" \
"v_mfma_f32_16x16x32_bf16 v[108:111], a[236:239], a[60:63], v[108:111]\n" \
"s_waitcnt vmcnt(63) lgkmcnt(9)\n" \
"s_nop 0\n" \
"v_mfma_f32_16x16x32_bf16 v[4:7], a[232:235], a[128:131], v[4:7]\n" \
"ds_read_b64_tr_b16 a[210:211], v227 offset:59392\n" \
"ds_write_b64 v231, v[196:197] offset:0\n" \
"v_mfma_f32_16x16x32_bf16 v[4:7], a[196:199], a[132:135], v[4:7]\n" \
"ds_read_b64_tr_b16 a[216:217], v227 offset:61440\n" \
"ds_write_b64 v231, v[200:201] offset:2048\n" \
"v_mfma_f32_16x16x32_bf16 v[36:39], a[232:235], a[144:147], v[36:39]\n" \
"ds_read_b64_tr_b16 a[218:219], v227 offset:63488\n" \
"ds_write_b64 v231, v[204:205] offset:4096\n" \
"v_mfma_f32_16x16x32_bf16 v[36:39], a[196:199], a[148:151], v[36:39]\n" \
"v_mfma_f32_16x16x32_bf16 v[68:71], a[232:235], a[160:163], v[68:71]\n" \
"v_mfma_f32_16x16x32_bf16 v[68:71], a[196:199], a[164:167], v[68:71]\n" \
"v_mfma_f32_16x16x32_bf16 v[100:103], a[232:235], a[176:179], v[100:103]\n" \
"ds_read_b64_tr_b16 a[224:225], v215 offset:16896\n" \
"v_mfma_f32_16x16x32_bf16 v[100:103], a[196:199], a[180:183], v[100:103]\n" \
"ds_read_b64_tr_b16 a[226:227], v215 offset:18944\n" \
"v_mfma_f32_16x16x32_bf16 v[4:7], a[200:203], a[136:139], v[4:7]\n" \
"ds_read_b64_tr_b16 a[228:229], v215 offset:20992\n" \
"v_mfma_f32_16x16x32_bf16 v[4:7], a[204:207], a[140:143], v[4:7]\n" \
"ds_read_b64_tr_b16 a[230:231], v215 offset:23040\n" \
"v_mfma_f32_16x16x32_bf16 v[36:39], a[200:203], a[152:155], v[36:39]\n" \
"s_waitcnt vmcnt(63) lgkmcnt(4)\n" \
"s_barrier\n" \
"v_mfma_f32_16x16x32_bf16 v[36:39], a[204:207], a[156:159], v[36:39]\n" \
"v_mov_b32_e64 v192, s40\n" \
"v_mfma_f32_16x16x32_bf16 v[68:71], a[200:203], a[168:171], v[68:71]\n" \
"ds_read_b128 v[196:199], v192 offset:14368\n" \
"v_mfma_f32_16x16x32_bf16 v[68:71], a[204:207], a[172:175], v[68:71]\n" \
"ds_read_b128 v[128:131], %[kb0] offset:6144\n" \
"v_mfma_f32_16x16x32_bf16 v[100:103], a[200:203], a[184:187], v[100:103]\n" \
"ds_read_b64_tr_b16 a[236:237], v215 offset:25088\n" \
"ds_read_b128 v[132:135], %[kb0] offset:8192\n" \
"v_mfma_f32_16x16x32_bf16 v[100:103], a[204:207], a[188:191], v[100:103]\n" \
"s_waitcnt vmcnt(63) lgkmcnt(9)\n" \
"s_nop 0\n" \
"v_mfma_f32_16x16x32_bf16 v[12:15], a[220:223], a[128:131], v[12:15]\n" \
"ds_read_b64_tr_b16 a[238:239], v215 offset:27136\n" \
"ds_read_b128 v[136:139], %[kb0] offset:7168\n" \
"v_mfma_f32_16x16x32_bf16 v[12:15], a[212:215], a[132:135], v[12:15]\n" \
"ds_read_b64_tr_b16 a[232:233], v215 offset:29184\n" \
"ds_read_b128 v[140:143], %[kb0] offset:9216\n" \
"v_mfma_f32_16x16x32_bf16 v[44:47], a[220:223], a[144:147], v[44:47]\n" \
"ds_read_b64_tr_b16 a[234:235], v215 offset:31232\n" \
"ds_read_b128 v[144:147], %[vvb] offset:6144\n" \
"v_mfma_f32_16x16x32_bf16 v[44:47], a[212:215], a[148:151], v[44:47]\n" \
"ds_read_b128 v[148:151], %[vvb] offset:8192\n" \
"v_mfma_f32_16x16x32_bf16 v[76:79], a[220:223], a[160:163], v[76:79]\n" \
"ds_read_b128 v[152:155], %[vvb] offset:7168\n" \
"v_mfma_f32_16x16x32_bf16 v[76:79], a[212:215], a[164:167], v[76:79]\n" \
"ds_read_b128 v[156:159], %[vvb] offset:9216\n" \
"v_mfma_f32_16x16x32_bf16 v[108:111], a[220:223], a[176:179], v[108:111]\n" \
"ds_read_b64_tr_b16 a[196:197], v227 offset:16896\n" \
"ds_read_b128 v[160:163], %[kb0] offset:10240\n" \
"v_mfma_f32_16x16x32_bf16 v[108:111], a[212:215], a[180:183], v[108:111]\n" \
"ds_read_b64_tr_b16 a[198:199], v227 offset:18944\n" \
"ds_read_b128 v[164:167], %[kb0] offset:12288\n" \
"v_mfma_f32_16x16x32_bf16 v[12:15], a[208:211], a[136:139], v[12:15]\n" \
"ds_read_b64_tr_b16 a[200:201], v227 offset:20992\n" \
"ds_read_b128 v[168:171], %[kb0] offset:11264\n" \
"v_mfma_f32_16x16x32_bf16 v[12:15], a[216:219], a[140:143], v[12:15]\n" \
"ds_read_b64_tr_b16 a[202:203], v227 offset:23040\n" \
"ds_read_b128 v[172:175], %[kb0] offset:13312\n" \
"v_mfma_f32_16x16x32_bf16 v[44:47], a[208:211], a[152:155], v[44:47]\n" \
"ds_read_b128 v[176:179], %[vvb] offset:10240\n" \
"v_mfma_f32_16x16x32_bf16 v[44:47], a[216:219], a[156:159], v[44:47]\n" \
"ds_read_b128 v[180:183], %[vvb] offset:12288\n" \
"v_mfma_f32_16x16x32_bf16 v[76:79], a[208:211], a[168:171], v[76:79]\n" \
"v_mov_b32_e64 v230, 64\n" \
"buffer_atomic_add v230, v225, %[srdz], s47 offen sc1\n" \
"v_mfma_f32_16x16x32_bf16 v[76:79], a[216:219], a[172:175], v[76:79]\n" \
"ds_read_b128 v[184:187], %[vvb] offset:11264\n" \
"v_mfma_f32_16x16x32_bf16 v[108:111], a[208:211], a[184:187], v[108:111]\n" \
"ds_read_b64_tr_b16 a[204:205], v227 offset:25088\n" \
"ds_read_b128 v[188:191], %[vvb] offset:13312\n" \
"v_mfma_f32_16x16x32_bf16 v[108:111], a[216:219], a[188:191], v[108:111]\n" \
"s_waitcnt vmcnt(63) lgkmcnt(14)\n" \
"s_nop 0\n" \
"v_mfma_f32_16x16x32_bf16 v[20:23], a[224:227], v[0:3], v[20:23]\n" \
"ds_read_b64_tr_b16 a[206:207], v227 offset:27136\n" \
"s_waitcnt lgkmcnt(14)\n" \
"v_add_f32_e32 v223, v196, v197\n" \
"v_mfma_f32_16x16x32_bf16 v[20:23], a[228:231], v[8:11], v[20:23]\n" \
"ds_read_b64_tr_b16 a[220:221], v227 offset:29184\n" \
"v_add_f32_e64 v198, v198, v199\n" \
"v_mfma_f32_16x16x32_bf16 v[52:55], a[224:227], v[32:35], v[52:55]\n" \
"ds_read_b64_tr_b16 a[222:223], v227 offset:31232\n" \
"v_add_f32_e64 v223, v223, v198\n" \
"v_mfma_f32_16x16x32_bf16 v[52:55], a[228:231], v[40:43], v[52:55]\n" \
"ds_read_b128 v[192:195], %[kb0] offset:2048\n" \
"v_mfma_f32_16x16x32_bf16 v[84:87], a[224:227], v[64:67], v[84:87]\n" \
"v_cvt_pk_bf16_f32 v4, v4, v5\n" \
"ds_read_b128 v[196:199], %[kb0] offset:3072\n" \
"v_mfma_f32_16x16x32_bf16 v[84:87], a[228:231], v[72:75], v[84:87]\n" \
"v_cvt_pk_bf16_f32 v5, v6, v7\n" \
"ds_read_b128 v[200:203], %[vvb] offset:2048\n" \
"v_mfma_f32_16x16x32_bf16 v[116:119], a[224:227], v[96:99], v[116:119]\n" \
"ds_read_b64_tr_b16 a[212:213], v215 offset:33280\n" \
"v_cvt_pk_bf16_f32 v6, v12, v13\n" \
"v_mfma_f32_16x16x32_bf16 v[116:119], a[228:231], v[104:107], v[116:119]\n" \
"ds_read_b64_tr_b16 a[214:215], v215 offset:35328\n" \
"v_cvt_pk_bf16_f32 v7, v14, v15\n" \
"v_mfma_f32_16x16x32_bf16 v[20:23], a[236:239], v[16:19], v[20:23]\n" \
"ds_read_b64_tr_b16 a[208:209], v215 offset:37376\n" \
"ds_read_b128 v[204:207], %[vvb] offset:3072\n" \
"v_mfma_f32_16x16x32_bf16 v[20:23], a[232:235], v[24:27], v[20:23]\n" \
"ds_read_b64_tr_b16 a[210:211], v215 offset:39424\n" \
"v_mfma_f32_16x16x32_bf16 v[52:55], a[236:239], v[48:51], v[52:55]\n" \
"s_add_u32 s55, s46, 8192\n" \
"buffer_store_dwordx4 v[4:7], %[vxs], %[srdz], s55 offen offset:0 sc1\n" \
"v_mfma_f32_16x16x32_bf16 v[52:55], a[232:235], v[56:59], v[52:55]\n" \
"v_cvt_pk_bf16_f32 v36, v36, v37\n" \
"v_cvt_pk_bf16_f32 v37, v38, v39\n" \
"v_mfma_f32_16x16x32_bf16 v[84:87], a[236:239], v[80:83], v[84:87]\n" \
"v_cvt_pk_bf16_f32 v38, v44, v45\n" \
"v_mfma_f32_16x16x32_bf16 v[84:87], a[232:235], v[88:91], v[84:87]\n" \
"v_cvt_pk_bf16_f32 v39, v46, v47\n" \
"v_mfma_f32_16x16x32_bf16 v[116:119], a[236:239], v[112:115], v[116:119]\n" \
"ds_read_b64_tr_b16 a[216:217], v215 offset:41472\n" \
"v_mfma_f32_16x16x32_bf16 v[116:119], a[232:235], v[120:123], v[116:119]\n" \
"s_waitcnt vmcnt(63) lgkmcnt(9)\n" \
"s_nop 0\n" \
"v_mfma_f32_16x16x32_bf16 v[28:31], a[196:199], v[0:3], v[28:31]\n" \
"ds_read_b64_tr_b16 a[218:219], v215 offset:43520\n" \
"v_cvt_pk_bf16_f32 v68, v68, v69\n" \
"v_mfma_f32_16x16x32_bf16 v[28:31], a[200:203], v[8:11], v[28:31]\n" \
"ds_read_b64_tr_b16 a[224:225], v215 offset:45568\n" \
"s_add_u32 s55, s46, 312320\n" \
"buffer_store_dwordx4 v[36:39], %[vxs], %[srdz], s55 offen offset:0 sc1\n" \
"v_mfma_f32_16x16x32_bf16 v[60:63], a[196:199], v[32:35], v[60:63]\n" \
"ds_read_b64_tr_b16 a[226:227], v215 offset:47616\n" \
"v_cvt_pk_bf16_f32 v69, v70, v71\n" \
"v_mfma_f32_16x16x32_bf16 v[60:63], a[200:203], v[40:43], v[60:63]\n" \
"v_cvt_pk_bf16_f32 v70, v76, v77\n" \
"v_cvt_pk_bf16_f32 v71, v78, v79\n" \
"v_mfma_f32_16x16x32_bf16 v[92:95], a[196:199], v[64:67], v[92:95]\n" \
"s_add_u32 s55, s46, 616448\n" \
"buffer_store_dwordx4 v[68:71], %[vxs], %[srdz], s55 offen offset:0 sc1\n" \
"v_mfma_f32_16x16x32_bf16 v[92:95], a[200:203], v[72:75], v[92:95]\n" \
"v_accvgpr_read_b32 v1, a240\n" \
"v_mfma_f32_16x16x32_bf16 v[124:127], a[196:199], v[96:99], v[124:127]\n" \
"ds_read_b64_tr_b16 a[228:229], v227 offset:33280\n" \
"v_cvt_pk_bf16_f32 v100, v100, v101\n" \
"v_mfma_f32_16x16x32_bf16 v[124:127], a[200:203], v[104:107], v[124:127]\n" \
"ds_read_b64_tr_b16 a[230:231], v227 offset:35328\n" \
"v_cvt_pk_bf16_f32 v101, v102, v103\n" \
"v_mfma_f32_16x16x32_bf16 v[28:31], a[204:207], v[16:19], v[28:31]\n" \
"ds_read_b64_tr_b16 a[236:237], v227 offset:37376\n" \
"v_cvt_pk_bf16_f32 v102, v108, v109\n" \
"v_readlane_b32 s68, %[thi], 18\n" \
"v_lshlrev_b32_e64 v226, 7, v226\n" \
"v_mfma_f32_16x16x32_bf16 v[28:31], a[220:223], v[24:27], v[28:31]\n" \
"ds_read_b64_tr_b16 a[238:239], v227 offset:39424\n" \
"v_cvt_pk_bf16_f32 v103, v110, v111\n" \
"v_mfma_f32_16x16x32_bf16 v[60:63], a[204:207], v[48:51], v[60:63]\n" \
"s_add_u32 s55, s46, 920576\n" \
"buffer_store_dwordx4 v[100:103], %[vxs], %[srdz], s55 offen offset:0 sc1\n" \
"v_mfma_f32_16x16x32_bf16 v[60:63], a[220:223], v[56:59], v[60:63]\n" \
"v_lshlrev_b32_e64 v0, 16, v1\n" \
"v_mfma_f32_16x16x32_bf16 v[92:95], a[204:207], v[80:83], v[92:95]\n" \
"v_and_b32_e64 v1, s43, v1\n" \
"v_mfma_f32_16x16x32_bf16 v[92:95], a[220:223], v[88:91], v[92:95]\n" \
"v_accvgpr_read_b32 v3, a241\n" \
"v_mfma_f32_16x16x32_bf16 v[124:127], a[204:207], v[112:115], v[124:127]\n" \
"ds_read_b64_tr_b16 a[232:233], v227 offset:41472\n" \
"v_lshlrev_b32_e64 v2, 16, v3\n" \
"v_mfma_f32_16x16x32_bf16 v[124:127], a[220:223], v[120:123], v[124:127]\n" \
"s_waitcnt vmcnt(63) lgkmcnt(5)\n" \
"s_nop 0\n" \
"v_mfma_f32_16x16x32_bf16 v[20:23], a[212:215], a[0:3], v[20:23]\n" \
"ds_read_b64_tr_b16 a[234:235], v227 offset:43520\n" \
"v_and_b32_e64 v3, s43, v3\n" \
"v_mfma_f32_16x16x32_bf16 v[20:23], a[208:211], a[4:7], v[20:23]\n" \
"ds_read_b64_tr_b16 a[196:197], v227 offset:45568\n" \
"v_accvgpr_read_b32 v9, a244\n" \
"v_mfma_f32_16x16x32_bf16 v[52:55], a[212:215], a[16:19], v[52:55]\n" \
"ds_read_b64_tr_b16 a[198:199], v227 offset:47616\n" \
"v_lshlrev_b32_e64 v8, 16, v9\n" \
"v_mfma_f32_16x16x32_bf16 v[52:55], a[208:211], a[20:23], v[52:55]\n" \
"buffer_load_dword v216, v226, %[srdz], s68 offen offset:64 sc1\n" \
"v_mfma_f32_16x16x32_bf16 v[84:87], a[212:215], a[32:35], v[84:87]\n" \
"v_mov_b32_e32 v226, 0\n" \
"v_lshrrev_b32_e32 v217, 3, v224\n" \
"s_add_u32 s68, %[sin], 4096\n" \
"v_and_b32_e64 v9, s43, v9\n" \
"v_mfma_f32_16x16x32_bf16 v[84:87], a[208:211], a[36:39], v[84:87]\n" \
"v_accvgpr_read_b32 v11, a245\n" \
"v_mfma_f32_16x16x32_bf16 v[116:119], a[212:215], a[48:51], v[116:119]\n" \
"ds_read_b64_tr_b16 a[200:201], v215 offset:49664\n" \
"v_lshlrev_b32_e64 v10, 16, v11\n" \
"v_mfma_f32_16x16x32_bf16 v[116:119], a[208:211], a[52:55], v[116:119]\n" \
"ds_read_b64_tr_b16 a[202:203], v215 offset:51712\n" \
"v_and_b32_e64 v11, s43, v11\n" \
"v_mfma_f32_16x16x32_bf16 v[20:23], a[216:219], a[8:11], v[20:23]\n" \
"ds_read_b64_tr_b16 a[204:205], v215 offset:53760\n" \
"v_accvgpr_read_b32 v33, a248\n" \
"v_mfma_f32_16x16x32_bf16 v[20:23], a[224:227], a[12:15], v[20:23]\n" \
"ds_read_b64_tr_b16 a[206:207], v215 offset:55808\n" \
"v_lshlrev_b32_e64 v32, 16, v33\n" \
"v_mfma_f32_16x16x32_bf16 v[52:55], a[216:219], a[24:27], v[52:55]\n" \
"ds_read_b64_tr_b16 a[220:221], v215 offset:57856\n" \
"v_and_b32_e64 v33, s43, v33\n" \
"v_mfma_f32_16x16x32_bf16 v[52:55], a[224:227], a[28:31], v[52:55]\n" \
"ds_read_b64_tr_b16 a[222:223], v215 offset:59904\n" \
"v_accvgpr_read_b32 v35, a249\n" \
"v_mfma_f32_16x16x32_bf16 v[84:87], a[216:219], a[40:43], v[84:87]\n" \
"ds_read_b64_tr_b16 a[212:213], v215 offset:61952\n" \
"v_lshlrev_b32_e64 v34, 16, v35\n" \
"v_mfma_f32_16x16x32_bf16 v[84:87], a[224:227], a[44:47], v[84:87]\n" \
"ds_read_b64_tr_b16 a[214:215], v215 offset:64000\n" \
"v_and_b32_e64 v35, s43, v35\n" \
"v_mfma_f32_16x16x32_bf16 v[116:119], a[216:219], a[56:59], v[116:119]\n" \
"ds_read_b64_tr_b16 a[208:209], v227 offset:49664\n" \
"v_accvgpr_read_b32 v41, a252\n" \
"v_mfma_f32_16x16x32_bf16 v[116:119], a[224:227], a[60:63], v[116:119]\n" \
"s_waitcnt vmcnt(63) lgkmcnt(9)\n" \
"s_nop 0\n" \
"v_mfma_f32_16x16x32_bf16 v[28:31], a[228:231], a[0:3], v[28:31]\n" \
"ds_read_b64_tr_b16 a[210:211], v227 offset:51712\n" \
"v_lshlrev_b32_e64 v40, 16, v41\n" \
"v_mfma_f32_16x16x32_bf16 v[28:31], a[236:239], a[4:7], v[28:31]\n" \
"ds_read_b64_tr_b16 a[216:217], v227 offset:53760\n" \
"v_and_b32_e64 v41, s43, v41\n" \
"v_mfma_f32_16x16x32_bf16 v[60:63], a[228:231], a[16:19], v[60:63]\n" \
"ds_read_b64_tr_b16 a[218:219], v227 offset:55808\n" \
"v_accvgpr_read_b32 v43, a253\n" \
"v_mfma_f32_16x16x32_bf16 v[60:63], a[236:239], a[20:23], v[60:63]\n" \
"ds_read_b64_tr_b16 a[224:225], v227 offset:57856\n" \
"v_lshlrev_b32_e64 v42, 16, v43\n" \
"v_mfma_f32_16x16x32_bf16 v[92:95], a[228:231], a[32:35], v[92:95]\n" \
"ds_read_b64_tr_b16 a[226:227], v227 offset:59904\n" \
"v_and_b32_e64 v43, s43, v43\n" \
"v_mfma_f32_16x16x32_bf16 v[92:95], a[236:239], a[36:39], v[92:95]\n" \
"v_accvgpr_read_b32 v17, a242\n" \
"v_mfma_f32_16x16x32_bf16 v[124:127], a[228:231], a[48:51], v[124:127]\n" \
"v_lshlrev_b32_e64 v16, 16, v17\n" \
"v_mfma_f32_16x16x32_bf16 v[124:127], a[236:239], a[52:55], v[124:127]\n" \
"ds_read_b64_tr_b16 a[228:229], v227 offset:61952\n" \
"v_and_b32_e64 v17, s43, v17\n" \
"v_mfma_f32_16x16x32_bf16 v[28:31], a[232:235], a[8:11], v[28:31]\n" \
"ds_read_b64_tr_b16 a[230:231], v227 offset:64000\n" \
"v_accvgpr_read_b32 v19, a243\n" \
"v_mfma_f32_16x16x32_bf16 v[28:31], a[196:199], a[12:15], v[28:31]\n" \
"v_lshlrev_b32_e64 v18, 16, v19\n" \
"v_mfma_f32_16x16x32_bf16 v[60:63], a[232:235], a[24:27], v[60:63]\n" \
"v_and_b32_e64 v19, s43, v19\n" \
"v_mfma_f32_16x16x32_bf16 v[60:63], a[196:199], a[28:31], v[60:63]\n" \
"v_accvgpr_read_b32 v25, a246\n" \
"v_mfma_f32_16x16x32_bf16 v[92:95], a[232:235], a[40:43], v[92:95]\n" \
"v_lshlrev_b32_e64 v24, 16, v25\n" \
"v_mfma_f32_16x16x32_bf16 v[92:95], a[196:199], a[44:47], v[92:95]\n" \
"v_and_b32_e64 v25, s43, v25\n" \
"v_mfma_f32_16x16x32_bf16 v[124:127], a[232:235], a[56:59], v[124:127]\n" \
"v_accvgpr_read_b32 v27, a247\n" \
"v_mfma_f32_16x16x32_bf16 v[124:127], a[196:199], a[60:63], v[124:127]\n" \
"s_waitcnt vmcnt(63) lgkmcnt(0)\n" \
"s_nop 0\n" \
"v_mfma_f32_16x16x32_bf16 v[20:23], a[200:203], a[128:131], v[20:23]\n" \
"v_lshlrev_b32_e64 v26, 16, v27\n" \
"v_mfma_f32_16x16x32_bf16 v[20:23], a[204:207], a[132:135], v[20:23]\n" \
"v_and_b32_e64 v27, s43, v27\n" \
"v_mfma_f32_16x16x32_bf16 v[20:23], a[220:223], a[136:139], v[20:23]\n" \
"v_accvgpr_read_b32 v49, a250\n" \
"v_mfma_f32_16x16x32_bf16 v[20:23], a[212:215], a[140:143], v[20:23]\n" \
"v_lshlrev_b32_e64 v48, 16, v49\n" \
"v_mfma_f32_16x16x32_bf16 v[28:31], a[208:211], a[128:131], v[28:31]\n" \
"v_and_b32_e64 v49, s43, v49\n" \
"v_mfma_f32_16x16x32_bf16 v[28:31], a[216:219], a[132:135], v[28:31]\n" \
"v_accvgpr_read_b32 v51, a251\n" \
"v_mfma_f32_16x16x32_bf16 v[28:31], a[224:227], a[136:139], v[28:31]\n" \
"v_lshlrev_b32_e64 v50, 16, v51\n" \
"v_mfma_f32_16x16x32_bf16 v[28:31], a[228:231], a[140:143], v[28:31]\n" \
"v_and_b32_e64 v51, s43, v51\n" \
"v_mfma_f32_16x16x32_bf16 v[52:55], a[200:203], a[144:147], v[52:55]\n" \
"v_accvgpr_read_b32 v57, a254\n" \
"v_mfma_f32_16x16x32_bf16 v[52:55], a[204:207], a[148:151], v[52:55]\n" \
"v_lshlrev_b32_e64 v56, 16, v57\n" \
"v_mfma_f32_16x16x32_bf16 v[52:55], a[220:223], a[152:155], v[52:55]\n" \
"v_and_b32_e64 v57, s43, v57\n" \
"v_mfma_f32_16x16x32_bf16 v[52:55], a[212:215], a[156:159], v[52:55]\n" \
"v_cvt_pk_bf16_f32 v20, v20, v21\n" \
"v_cvt_pk_bf16_f32 v21, v22, v23\n" \
"v_accvgpr_read_b32 v59, a255\n" \
"v_mfma_f32_16x16x32_bf16 v[60:63], a[208:211], a[144:147], v[60:63]\n" \
"v_lshlrev_b32_e64 v58, 16, v59\n" \
"v_mfma_f32_16x16x32_bf16 v[60:63], a[216:219], a[148:151], v[60:63]\n" \
"v_cvt_pk_bf16_f32 v22, v28, v29\n" \
"v_cvt_pk_bf16_f32 v23, v30, v31\n" \
"v_and_b32_e64 v59, s43, v59\n" \
"v_mfma_f32_16x16x32_bf16 v[60:63], a[224:227], a[152:155], v[60:63]\n" \
"s_add_u32 s55, s46, 12288\n" \
"buffer_store_dwordx4 v[20:23], %[vxs], %[srdz], s55 offen offset:0 sc1\n" \
"v_mfma_f32_16x16x32_bf16 v[60:63], a[228:231], a[156:159], v[60:63]\n" \
"v_mfma_f32_16x16x32_bf16 v[84:87], a[200:203], a[160:163], v[84:87]\n" \
"v_mfma_f32_16x16x32_bf16 v[84:87], a[204:207], a[164:167], v[84:87]\n" \
"v_mfma_f32_16x16x32_bf16 v[84:87], a[220:223], a[168:171], v[84:87]\n" \
"v_mfma_f32_16x16x32_bf16 v[84:87], a[212:215], a[172:175], v[84:87]\n" \
"v_cvt_pk_bf16_f32 v52, v52, v53\n" \
"v_cvt_pk_bf16_f32 v53, v54, v55\n" \
"v_mfma_f32_16x16x32_bf16 v[92:95], a[208:211], a[160:163], v[92:95]\n" \
"v_mfma_f32_16x16x32_bf16 v[92:95], a[216:219], a[164:167], v[92:95]\n" \
"v_cvt_pk_bf16_f32 v54, v60, v61\n" \
"v_cvt_pk_bf16_f32 v55, v62, v63\n" \
"v_mfma_f32_16x16x32_bf16 v[92:95], a[224:227], a[168:171], v[92:95]\n" \
"s_add_u32 s55, s46, 316416\n" \
"buffer_store_dwordx4 v[52:55], %[vxs], %[srdz], s55 offen offset:0 sc1\n" \
"v_mfma_f32_16x16x32_bf16 v[92:95], a[228:231], a[172:175], v[92:95]\n" \
"v_mfma_f32_16x16x32_bf16 v[116:119], a[200:203], a[176:179], v[116:119]\n" \
"v_mfma_f32_16x16x32_bf16 v[116:119], a[204:207], a[180:183], v[116:119]\n" \
"v_mfma_f32_16x16x32_bf16 v[116:119], a[220:223], a[184:187], v[116:119]\n" \
"v_mfma_f32_16x16x32_bf16 v[116:119], a[212:215], a[188:191], v[116:119]\n" \
"v_cvt_pk_bf16_f32 v84, v84, v85\n" \
"v_cvt_pk_bf16_f32 v85, v86, v87\n" \
"v_mfma_f32_16x16x32_bf16 v[124:127], a[208:211], a[176:179], v[124:127]\n" \
"v_mfma_f32_16x16x32_bf16 v[124:127], a[216:219], a[180:183], v[124:127]\n" \
"v_cvt_pk_bf16_f32 v86, v92, v93\n" \
"v_cvt_pk_bf16_f32 v87, v94, v95\n" \
"v_mfma_f32_16x16x32_bf16 v[124:127], a[224:227], a[184:187], v[124:127]\n" \
".if . - adaln_body != 61440\n" \
".error \"size model\"\n" \
".endif\n" \
"Lk14_%=:\n" \
"s_cbranch_execz Lk15_%=\n" \
"s_nop 0\n" \
"s_add_u32 s55, s46, 620544\n" \
"buffer_store_dwordx4 v[84:87], %[vxs], %[srdz], s55 offen offset:0 sc1\n" \
"v_mfma_f32_16x16x32_bf16 v[124:127], a[228:231], a[188:191], v[124:127]\n" \
"v_cvt_pk_bf16_f32 v116, v116, v117\n" \
"v_cvt_pk_bf16_f32 v117, v118, v119\n" \
"s_waitcnt lgkmcnt(0)\n" \
"s_nop 4\n" \
"v_cvt_pk_bf16_f32 v118, v124, v125\n" \
"v_cvt_pk_bf16_f32 v119, v126, v127\n" \
"s_add_u32 s55, s46, 924672\n" \
"buffer_store_dwordx4 v[116:119], %[vxs], %[srdz], s55 offen offset:0 sc1\n" \
"buffer_load_dword v229, v226, %[srdz], s53 offen sc1\n" \
"v_readlane_b32 s62, %[tlo], 35\n" \
"v_lshrrev_b32_e32 v72, 2, v224\n" \
"s_add_u32 s54, %[sin], 8192\n" \
"s_add_u32 s55, %[sin], 12288\n" \
"s_movk_i32 s56, 0x1000\n" \
"s_movk_i32 s57, 0x2000\n" \
"s_movk_i32 s58, 0x3000\n" \
"buffer_load_dwordx4 v[64:67], v72, %[srdz], s62 offen offset:0 sc1\n" \
"buffer_load_dwordx4 v[68:71], v72, %[srdz], s62 offen offset:16 sc1\n" \
"v_mov_b32_e32 v226, v211\n" \
"v_bfe_u32 v221, v222, 3, 1\n" \
"v_and_b32_e32 v208, 1, v222\n" \
"v_lshl_or_b32 v221, v221, 1, v208\n" \
"s_movk_i32 s63, 272\n" \
"v_mad_u32_u24 v221, v221, s63, %[vx0]\n" \
"v_readlane_b32 s64, %[tlo], 25\n" \
"v_readlane_b32 s65, %[thi], 25\n" \
"s_mov_b32 s66, 0x400000\n" \
"s_mov_b32 s67, 0x20000\n" \
"v_readlane_b32 s48, %[tlo], 27\n" \
"v_readlane_b32 s49, %[thi], 27\n" \
"v_readlane_b32 s69, %[tlo], 21\n" \
"v_accvgpr_write_b32 a128, -1\n" \
"v_lshrrev_b32_e32 v214, 4, v224\n" \
"s_add_u32 s62, %[m0b], 65536\n" \
"v_add_u32_e32 v215, %[m0t], v214\n" \
"v_add_u32_e32 v214, s62, v214\n" \
"ds_read_b64_tr_b16 a[64:65], v214 offset:0\n" \
"v_and_b32_e32 v4, 1, v222\n" \
"ds_read_b64_tr_b16 a[66:67], v214 offset:1024\n" \
"v_bfe_u32 v5, v222, 1, 3\n" \
"ds_read_b64_tr_b16 a[96:97], v215 offset:0\n" \
"v_lshlrev_b32_e32 v4, 4, v4\n" \
"ds_read_b64_tr_b16 a[98:99], v215 offset:1024\n" \
"v_mov_b32_e32 v6, 0x3f80\n" \
"ds_read_b64_tr_b16 a[68:69], v214 offset:512\n" \
"v_lshlrev_b32_e64 v5, v5, 1\n" \
"ds_read_b64_tr_b16 a[70:71], v214 offset:1536\n" \
"v_lshlrev_b32_e32 v4, v4, v6\n" \
"ds_read_b64_tr_b16 a[100:101], v215 offset:512\n" \
"v_bfe_i32 v230, v5, 0, 1\n" \
"ds_read_b64_tr_b16 a[102:103], v215 offset:1536\n" \
"v_bfe_i32 v231, v5, 1, 1\n" \
"ds_read_b64_tr_b16 a[72:73], v214 offset:16384\n" \
"v_bfe_i32 v232, v5, 2, 1\n" \
"ds_read_b64_tr_b16 a[74:75], v214 offset:17408\n" \
"v_bfe_i32 v233, v5, 3, 1\n" \
"ds_read_b64_tr_b16 a[104:105], v215 offset:4096\n" \
"v_bfe_i32 v234, v5, 4, 1\n" \
"ds_read_b64_tr_b16 a[106:107], v215 offset:5120\n" \
"v_bfe_i32 v235, v5, 5, 1\n" \
"ds_read_b64_tr_b16 a[76:77], v214 offset:16896\n" \
"v_bfe_i32 v236, v5, 6, 1\n" \
"ds_read_b64_tr_b16 a[78:79], v214 offset:17920\n" \
"v_bfe_i32 v237, v5, 7, 1\n" \
"ds_read_b64_tr_b16 a[108:109], v215 offset:4608\n" \
"v_and_b32_e32 v230, v4, v230\n" \
"ds_read_b64_tr_b16 a[110:111], v215 offset:5632\n" \
"v_and_b32_e32 v231, v4, v231\n" \
"ds_read_b64_tr_b16 a[80:81], v214 offset:2048\n" \
"v_and_b32_e32 v232, v4, v232\n" \
"ds_read_b64_tr_b16 a[82:83], v214 offset:3072\n" \
"v_and_b32_e32 v233, v4, v233\n" \
"ds_read_b64_tr_b16 a[112:113], v215 offset:2048\n" \
"v_and_b32_e32 v234, v4, v234\n" \
"ds_read_b64_tr_b16 a[114:115], v215 offset:3072\n" \
"v_and_b32_e32 v235, v4, v235\n" \
"ds_read_b64_tr_b16 a[84:85], v214 offset:2560\n" \
"v_and_b32_e32 v236, v4, v236\n" \
"ds_read_b64_tr_b16 a[86:87], v214 offset:3584\n" \
"v_and_b32_e32 v237, v4, v237\n" \
"ds_read_b64_tr_b16 a[116:117], v215 offset:2560\n" \
"ds_read_b64_tr_b16 a[118:119], v215 offset:3584\n" \
"ds_read_b64_tr_b16 a[88:89], v214 offset:18432\n" \
"ds_read_b64_tr_b16 a[90:91], v214 offset:19456\n" \
"ds_read_b64_tr_b16 a[120:121], v215 offset:6144\n" \
"ds_read_b64_tr_b16 a[122:123], v215 offset:7168\n" \
"ds_read_b64_tr_b16 a[92:93], v214 offset:18944\n" \
"ds_read_b64_tr_b16 a[94:95], v214 offset:19968\n" \
"ds_read_b64_tr_b16 a[124:125], v215 offset:6656\n" \
"ds_read_b64_tr_b16 a[126:127], v215 offset:7680\n" \
"v_lshrrev_b32_e32 v208, 1, v217\n" \
"v_and_b32_e32 v210, 24, v208\n" \
"v_bfe_u32 v215, v222, 4, 1\n" \
"v_lshl_or_b32 v210, v215, 14, v210\n" \
"v_lshl_or_b32 v210, v215, 7, v210\n" \
"v_bfe_u32 v215, v222, 2, 2\n" \
"v_lshl_or_b32 v210, v215, 9, v210\n" \
"v_lshrrev_b32_e32 v211, 5, v222\n" \
"v_xor_b32_e32 v215, v215, v211\n" \
"v_lshl_or_b32 v210, v215, 5, v210\n" \
"s_add_u32 s62, %[m0b], 98304\n" \
"v_add_u32_e32 v210, s62, v210\n" \
"v_xor_b32_e32 v211, 64, v210\n" \
"v_xor_b32_e32 v213, 0x80, v210\n" \
"v_xor_b32_e32 v214, 0xc0, v210\n" \
"v_readlane_b32 s62, %[thi], 28\n" \
"v_and_b32_e32 v100, 7, v222\n" \
"v_lshrrev_b32_e32 v103, 1, v222\n" \
"v_and_b32_e32 v101, 1, v222\n" \
"v_lshlrev_b32_e32 v103, 13, v103\n" \
"v_lshl_or_b32 v103, v101, 4, v103\n" \
"v_add_u32_e32 v100, s62, v100\n" \
"v_accvgpr_write_b32 a34, v103\n" \
"v_lshrrev_b32_e32 v101, 8, v100\n" \
"v_lshlrev_b32_e32 v101, 7, v101\n" \
"v_bfe_u32 v103, v100, 6, 2\n" \
"v_lshl_or_b32 v101, v103, 5, v101\n" \
"v_bfe_u32 v103, v100, 1, 1\n" \
"v_lshl_or_b32 v101, v103, 4, v101\n" \
"v_bfe_u32 v103, v100, 2, 4\n" \
"v_or_b32_e32 v101, v103, v101\n" \
"v_lshrrev_b32_e32 v102, 8, v101\n" \
"v_lshlrev_b32_e32 v102, 9, v102\n" \
"v_accvgpr_write_b32 a32, v101\n" \
"v_bfe_u32 v103, v101, 6, 1\n" \
"v_lshl_or_b32 v102, v103, 8, v102\n" \
"v_bfe_u32 v103, v101, 7, 1\n" \
"v_lshl_or_b32 v102, v103, 7, v102\n" \
"v_bfe_u32 v103, v101, 4, 1\n" \
"v_lshl_or_b32 v102, v103, 6, v102\n" \
"v_bfe_u32 v103, v101, 2, 2\n" \
"v_lshl_or_b32 v102, v103, 4, v102\n" \
"v_bfe_u32 v103, v101, 5, 1\n" \
"v_lshl_or_b32 v102, v103, 3, v102\n" \
"v_bfe_u32 v103, v101, 0, 2\n" \
"v_lshl_or_b32 v102, v103, 1, v102\n" \
"v_accvgpr_write_b32 a33, v102\n" \
"s_mov_b32 s51, 0x10000\n" \
"s_mov_b32 s50, 0\n" \
"s_waitcnt vmcnt(7)\n" \
"v_cmp_eq_u32_e32 vcc, 0, v216\n" \
"s_mov_b32 %[got], 0\n" \
"s_cbranch_vccnz TLf1_%=\n" \
"s_mov_b32 %[got], 1\n" \
"buffer_load_dwordx4 a[196:199], v217, %[srdz], %[sin] offen offset:0 sc1\n" \
"buffer_load_dwordx4 a[200:203], v217, %[srdz], %[sin] offen offset:1024 sc1\n" \
"buffer_load_dwordx4 a[204:207], v217, %[srdz], %[sin] offen offset:2048 sc1\n" \
"buffer_load_dwordx4 a[208:211], v217, %[srdz], %[sin] offen offset:3072 sc1\n" \
"buffer_load_dwordx4 a[212:215], v217, %[srdz], s68 offen offset:0 sc1\n" \
"buffer_load_dwordx4 a[216:219], v217, %[srdz], s68 offen offset:1024 sc1\n" \
"buffer_load_dwordx4 a[220:223], v217, %[srdz], s68 offen offset:2048 sc1\n" \
"buffer_load_dwordx4 a[224:227], v217, %[srdz], s68 offen offset:3072 sc1\n" \
"buffer_load_dwordx4 a[164:167], v217, %[srdz], s54 offen offset:0 sc1\n" \
"buffer_load_dwordx4 a[168:171], v217, %[srdz], s54 offen offset:1024 sc1\n" \
"buffer_load_dwordx4 a[172:175], v217, %[srdz], s54 offen offset:2048 sc1\n" \
"buffer_load_dwordx4 a[176:179], v217, %[srdz], s54 offen offset:3072 sc1\n" \
"TLf1_%=:\n" \
"ds_read_b64_tr_b16 a[0:1], v210 offset:0\n" \
"ds_read_b64_tr_b16 a[2:3], v213 offset:2048\n" \
"ds_read_b64_tr_b16 a[4:5], v211 offset:0\n" \
"ds_read_b64_tr_b16 a[6:7], v214 offset:2048\n" \
"ds_read_b64_tr_b16 a[8:9], v213 offset:0\n" \
"ds_read_b64_tr_b16 a[10:11], v210 offset:2048\n" \
"ds_read_b64_tr_b16 a[12:13], v214 offset:0\n" \
"ds_read_b64_tr_b16 a[14:15], v211 offset:2048\n" \
"ds_read_b64_tr_b16 a[16:17], v210 offset:256\n" \
"ds_read_b64_tr_b16 a[18:19], v213 offset:2304\n" \
"ds_read_b64_tr_b16 a[20:21], v211 offset:256\n" \
"ds_read_b64_tr_b16 a[22:23], v214 offset:2304\n" \
"ds_read_b64_tr_b16 a[24:25], v213 offset:256\n" \
"ds_read_b64_tr_b16 a[26:27], v210 offset:2304\n" \
"ds_read_b64_tr_b16 a[28:29], v214 offset:256\n" \
"ds_read_b64_tr_b16 a[30:31], v211 offset:2304\n" \
"s_waitcnt vmcnt(12)\n" \
"s_cmp_lg_u32 %[got], 0\n" \
"s_cbranch_scc1 TLrp4_%=\n" \
"s_waitcnt vmcnt(0)\n" \
"v_and_b32_e32 v208, 32, v229\n" \
"v_cmp_eq_u32_e32 vcc, 0, v208\n" \
"s_cbranch_vccz TLrl2_%=\n" \
"v_mov_b32_e32 v216, s40\n" \
"TLrq2_%=:\n" \
"buffer_load_dword v225, off, %[srdz], s53 offset:0 sc1\n" \
"s_mov_b32 s52, 10\n" \
"TLrs2_%=:\n" \
"ds_read_b32 v229, v216 offset:14336\n" \
"s_waitcnt lgkmcnt(0)\n" \
"v_and_b32_e32 v208, 32, v229\n" \
"v_cmp_eq_u32_e32 vcc, 0, v208\n" \
"s_cbranch_vccz TLrd2_%=\n" \
"s_sub_u32 s52, s52, 1\n" \
"s_cbranch_scc0 TLrs2_%=\n" \
"s_sub_u32 s51, s51, 1\n" \
"s_cselect_b32 s51, 0, s51\n" \
"s_waitcnt vmcnt(0)\n" \
"v_and_b32_e32 v208, 32, v225\n" \
"v_cmp_eq_u32_e32 vcc, 0, v208\n" \
"s_cbranch_scc1 TLrl2_%=\n" \
"s_cbranch_vccnz TLrq2_%=\n" \
"v_mov_b32_e32 v229, v225\n" \
"s_mov_b64 exec, 1\n" \
"ds_max_u32 v216, v225 offset:14336\n" \
"s_mov_b64 exec, -1\n" \
"TLrd2_%=:\n" \
"s_sleep 0\n" \
"TLrl2_%=:\n" \
"buffer_load_dwordx4 a[196:199], v217, %[srdz], %[sin] offen offset:0 sc1\n" \
"buffer_load_dwordx4 a[200:203], v217, %[srdz], %[sin] offen offset:1024 sc1\n" \
"buffer_load_dwordx4 a[204:207], v217, %[srdz], %[sin] offen offset:2048 sc1\n" \
"buffer_load_dwordx4 a[208:211], v217, %[srdz], %[sin] offen offset:3072 sc1\n" \
"buffer_load_dwordx4 a[212:215], v217, %[srdz], s68 offen offset:0 sc1\n" \
"buffer_load_dwordx4 a[216:219], v217, %[srdz], s68 offen offset:1024 sc1\n" \
"buffer_load_dwordx4 a[220:223], v217, %[srdz], s68 offen offset:2048 sc1\n" \
"buffer_load_dwordx4 a[224:227], v217, %[srdz], s68 offen offset:3072 sc1\n" \
"v_and_b32_e32 v44, 0x800, v229\n" \
"v_cmp_eq_u32_e32 vcc, 0, v44\n" \
"s_cbranch_vccnz TLrn_%=\n" \
"buffer_load_dwordx4 a[164:167], v217, %[srdz], s54 offen offset:0 sc1\n" \
"buffer_load_dwordx4 a[168:171], v217, %[srdz], s54 offen offset:1024 sc1\n" \
"buffer_load_dwordx4 a[172:175], v217, %[srdz], s54 offen offset:2048 sc1\n" \
"buffer_load_dwordx4 a[176:179], v217, %[srdz], s54 offen offset:3072 sc1\n" \
"s_branch TLrp5_%=\n" \
"TLrp4_%=:\n" \
"v_and_b32_e32 v44, 0x800, v229\n" \
"v_cmp_eq_u32_e32 vcc, 0, v44\n" \
"s_cbranch_vccnz TLrn_%=\n" \
"TLrp5_%=:\n" \
"s_mov_b32 s50, 1\n" \
"buffer_load_dwordx4 a[180:183], v217, %[srdz], s55 offen offset:0 sc1\n" \
"buffer_load_dwordx4 a[184:187], v217, %[srdz], s55 offen offset:1024 sc1\n" \
"buffer_load_dwordx4 a[188:191], v217, %[srdz], s55 offen offset:2048 sc1\n" \
"buffer_load_dwordx4 a[192:195], v217, %[srdz], s55 offen offset:3072 sc1\n" \
"TLrn_%=:\n" \
"v_min3_u32 v208, v64, v65, v66\n" \
"v_min3_u32 v209, v67, v68, v69\n" \
"v_min3_u32 v208, v208, v70, v71\n" \
"v_min_u32_e32 v208, v208, v209\n" \
"v_cmp_eq_u32_e32 vcc, 0, v208\n" \
"s_cbranch_vccnz TLpk_%=\n" \
"TLpkb_%=:\n" \
"v_min_u32_e32 v208, v65, v67\n" \
"v_min_u32_e32 v209, v69, v71\n" \
"v_max_u32_e32 v65, v65, v67\n" \
"v_max_u32_e32 v69, v69, v71\n" \
"v_max3_u32 v64, v208, v64, v66\n" \
"v_max3_u32 v68, v209, v68, v70\n" \
"v_min_u32_e32 v208, v65, v69\n" \
"v_max_u32_e32 v65, v65, v69\n" \
"v_max3_u32 v64, v208, v64, v68\n" \
"v_mov_b64_e32 v[68:69], v[64:65]\n" \
"v_min_u32_dpp v208, v65, v65 quad_perm:[1,0,3,2] row_mask:0xf bank_mask:0xf\n" \
"v_max_u32_dpp v209, v64, v64 quad_perm:[1,0,3,2] row_mask:0xf bank_mask:0xf\n" \
"v_max_u32_dpp v65, v65, v65 quad_perm:[1,0,3,2] row_mask:0xf bank_mask:0xf\n" \
"v_max_u32_e32 v64, v208, v209\n" \
"s_nop 0\n" \
"v_min_u32_dpp v208, v65, v65 quad_perm:[2,3,0,1] row_mask:0xf bank_mask:0xf\n" \
"v_max_u32_dpp v209, v64, v64 quad_perm:[2,3,0,1] row_mask:0xf bank_mask:0xf\n" \
"v_max_u32_dpp v65, v65, v65 quad_perm:[2,3,0,1] row_mask:0xf bank_mask:0xf\n" \
"v_max_u32_e32 v64, v208, v209\n" \
"s_nop 0\n" \
"v_min_u32_dpp v208, v65, v65 row_half_mirror row_mask:0xf bank_mask:0xf\n" \
"v_max_u32_dpp v209, v64, v64 row_half_mirror row_mask:0xf bank_mask:0xf\n" \
"v_max_u32_dpp v65, v65, v65 row_half_mirror row_mask:0xf bank_mask:0xf\n" \
"v_max_u32_e32 v64, v208, v209\n" \
"s_nop 0\n" \
"v_min_u32_dpp v208, v65, v65 row_mirror row_mask:0xf bank_mask:0xf\n" \
"v_max_u32_dpp v209, v64, v64 row_mirror row_mask:0xf bank_mask:0xf\n" \
"v_max_u32_dpp v65, v65, v65 row_mirror row_mask:0xf bank_mask:0xf\n" \
"v_max_u32_e32 v64, v208, v209\n" \
"v_mov_b32_e32 v67, v65\n" \
"v_mov_b32_e32 v66, v64\n" \
"s_nop 0\n" \
"v_permlane16_swap_b32_e32 v65, v67\n" \
"v_permlane16_swap_b32_e32 v64, v66\n" \
"v_min_u32_e32 v208, v65, v67\n" \
"v_max_u32_e32 v65, v65, v67\n" \
"v_max3_u32 v64, v208, v64, v66\n" \
"v_mov_b32_e32 v67, v65\n" \
"v_mov_b32_e32 v66, v64\n" \
"s_nop 0\n" \
"v_permlane32_swap_b32_e32 v65, v67\n" \
"v_permlane32_swap_b32_e32 v64, v66\n" \
"v_min_u32_e32 v208, v65, v67\n" \
"v_max_u32_e32 v65, v65, v67\n" \
"v_max3_u32 v64, v208, v64, v66\n" \
"v_readfirstlane_b32 s44, v65\n" \
"v_readfirstlane_b32 s45, v64\n" \
"v_cmp_ge_u32_e32 vcc, v68, v64\n" \
"s_nop 1\n" \
"v_cndmask_b32_e64 v68, v68, 0, vcc\n" \
"v_cmp_ge_u32_e32 vcc, v69, v64\n" \
"s_nop 1\n" \
"v_cndmask_b32_e32 v69, v69, v68, vcc\n" \
"v_cndmask_b32_e64 v68, v68, 0, vcc\n" \
"s_nop 0\n" \
"v_min_u32_dpp v208, v69, v69 quad_perm:[1,0,3,2] row_mask:0xf bank_mask:0xf\n" \
"v_max_u32_dpp v209, v68, v68 quad_perm:[1,0,3,2] row_mask:0xf bank_mask:0xf\n" \
"v_max_u32_dpp v69, v69, v69 quad_perm:[1,0,3,2] row_mask:0xf bank_mask:0xf\n" \
"v_max_u32_e32 v68, v208, v209\n" \
"s_nop 0\n" \
"v_min_u32_dpp v208, v69, v69 quad_perm:[2,3,0,1] row_mask:0xf bank_mask:0xf\n" \
"v_max_u32_dpp v209, v68, v68 quad_perm:[2,3,0,1] row_mask:0xf bank_mask:0xf\n" \
"v_max_u32_dpp v69, v69, v69 quad_perm:[2,3,0,1] row_mask:0xf bank_mask:0xf\n" \
"v_max_u32_e32 v68, v208, v209\n" \
"s_nop 0\n" \
"v_min_u32_dpp v208, v69, v69 row_half_mirror row_mask:0xf bank_mask:0xf\n" \
"v_max_u32_dpp v209, v68, v68 row_half_mirror row_mask:0xf bank_mask:0xf\n" \
"v_max_u32_dpp v69, v69, v69 row_half_mirror row_mask:0xf bank_mask:0xf\n" \
"v_max_u32_e32 v68, v208, v209\n" \
"s_nop 0\n" \
"v_min_u32_dpp v208, v69, v69 row_mirror row_mask:0xf bank_mask:0xf\n" \
"v_max_u32_dpp v209, v68, v68 row_mirror row_mask:0xf bank_mask:0xf\n" \
"v_max_u32_dpp v69, v69, v69 row_mirror row_mask:0xf bank_mask:0xf\n" \
"v_max_u32_e32 v68, v208, v209\n" \
"v_mov_b32_e32 v67, v69\n" \
"v_mov_b32_e32 v66, v68\n" \
"s_nop 0\n" \
"v_permlane16_swap_b32_e32 v69, v67\n" \
"v_permlane16_swap_b32_e32 v68, v66\n" \
"v_min_u32_e32 v208, v69, v67\n" \
"v_max_u32_e32 v69, v69, v67\n" \
"v_max3_u32 v68, v208, v68, v66\n" \
"v_mov_b32_e32 v67, v69\n" \
"v_mov_b32_e32 v66, v68\n" \
"s_nop 0\n" \
"v_permlane32_swap_b32_e32 v69, v67\n" \
"v_permlane32_swap_b32_e32 v68, v66\n" \
"v_min_u32_e32 v208, v69, v67\n" \
"v_max_u32_e32 v69, v69, v67\n" \
"v_max3_u32 v68, v208, v68, v66\n" \
"v_readfirstlane_b32 s46, v69\n" \
"v_readfirstlane_b32 s47, v68\n" \
"s_andn2_b32 s44, 0x7ff, s44\n" \
"s_andn2_b32 s45, 0x7ff, s45\n" \
"s_andn2_b32 s46, 0x7ff, s46\n" \
"s_andn2_b32 s47, 0x7ff, s47\n" \
"v_and_b32_e32 v213, 4, v222\n" \
"v_mov_b32_e32 v208, s44\n" \
"v_cmp_ne_u32_e32 vcc, 0, v213\n" \
"v_mov_b32_e32 v214, s45\n" \
"v_mov_b32_e32 v209, s46\n" \
"v_mov_b32_e32 v215, s47\n" \
"v_cndmask_b32_e32 v208, v208, v214, vcc\n" \
"v_cndmask_b32_e32 v209, v209, v215, vcc\n" \
"v_cmp_lt_u32_e32 vcc, 31, v222\n" \
"v_and_b32_e32 v211, 0xf0, %[kb0]\n" \
"s_lshl_b32 s62, %[wid], 9\n" \
"v_cndmask_b32_e32 v210, v208, v209, vcc\n" \
"v_lshrrev_b32_e32 %[vdn], 6, v210\n" \
"v_lshlrev_b32_e32 %[vdn], 9, %[vdn]\n" \
"v_lshlrev_b32_e32 v213, 11, v210\n" \
"v_lshrrev_b32_e32 v214, 1, v210\n" \
"v_and_b32_e32 v214, 0x80, v214\n" \
"v_lshrrev_b32_e32 v215, 1, v211\n" \
"v_and_b32_e32 v215, 56, v215\n" \
"v_add3_u32 v213, v213, v214, v215\n" \
"v_and_b32_e32 v215, 0x80, v211\n" \
"v_lshlrev_b32_e32 v215, 1, v215\n" \
"v_add3_u32 v80, v213, v215, s62\n" \
"v_xor_b32_e32 v81, 0x80, v80\n" \
"s_mov_b32 s63, 278528\n" \
"buffer_load_dword %[vxs], %[vdn], %[srdz], s63 offen offset:0 sc1\n" \
"global_load_dwordx2 v[88:89], v80, %[px] offset:0\n" \
"global_load_dwordx2 v[90:91], v80, %[px] offset:64\n" \
"global_load_dwordx2 v[92:93], v81, %[px] offset:0\n" \
"global_load_dwordx2 v[94:95], v81, %[px] offset:64\n" \
"v_and_b32_e32 v213, 0x100, v210\n" \
"s_add_u32 s63, s62, s40\n" \
"v_add3_u32 v213, v213, v211, s63\n" \
"v_xor_b32_e32 v214, 0x100, v213\n" \
"ds_read_b128 v[104:107], v213 offset:2048\n" \
"ds_read_b128 v[108:111], v214 offset:2048\n" \
"ds_read_b128 v[64:67], v213 offset:6144\n" \
"ds_read_b128 v[68:71], v213 offset:8192\n" \
"ds_read_b128 v[72:75], v214 offset:6144\n" \
"ds_read_b128 v[76:79], v214 offset:8192\n" \
"ds_read_b128 v[112:115], v213 offset:10240\n" \
"ds_read_b128 v[116:119], v213 offset:12288\n" \
"ds_read_b128 v[120:123], v214 offset:10240\n" \
"ds_read_b128 v[124:127], v214 offset:12288\n" \
"s_movk_i32 s63, 2112\n" \
"v_mad_u32_u24 %[oq0], v219, s63, v213\n" \
"v_mad_u32_u24 %[wo0], v219, s63, v214\n" \
"s_lshl_b32 s62, %[wid], 10\n" \
"s_add_u32 s62, s62, 7340032\n" \
"v_and_b32_e32 v215, 0x3b0, v217\n" \
"v_add_u32_e32 v215, s62, v215\n" \
"s_mov_b32 s63, 38016\n" \
"v_lshrrev_b32_e32 %[vct], 1, v208\n" \
"v_and_b32_e32 v208, 1, v208\n" \
"v_mad_u32_u24 %[vct], %[vct], s63, v215\n" \
"v_lshl_add_u32 %[vct], v208, 6, %[vct]\n" \
"v_lshrrev_b32_e32 %[rd0], 1, v209\n" \
"v_and_b32_e32 v209, 1, v209\n" \
"v_mad_u32_u24 %[rd0], %[rd0], s63, v215\n" \
"v_lshl_add_u32 %[rd0], v209, 6, %[rd0]\n" \
"s_waitcnt vmcnt(13) lgkmcnt(0)\n" \
"s_cmp_lg_u32 s50, 0\n" \
"s_cbranch_scc1 TLrw_%=\n" \
"s_waitcnt vmcnt(5)\n" \
"TLrw_%=:\n" \
"v_mfma_f32_16x16x32_bf16 v[208:211], v[230:233], a[196:199], 0\n" \
"v_mfma_f32_16x16x32_bf16 v[212:215], v[230:233], a[204:207], 0\n" \
"v_mfma_f32_16x16x32_bf16 v[208:211], v[234:237], a[200:203], v[208:211]\n" \
"v_mfma_f32_16x16x32_bf16 v[212:215], v[234:237], a[208:211], v[212:215]\n" \
"v_lshlrev_b32_e32 v96, 16, v104\n" \
"v_and_b32_e32 v97, s43, v104\n" \
"v_lshlrev_b32_e32 v98, 16, v105\n" \
"v_and_b32_e32 v99, s43, v105\n" \
"v_lshlrev_b32_e32 v100, 16, v106\n" \
"v_and_b32_e32 v101, s43, v106\n" \
"v_lshlrev_b32_e32 v102, 16, v107\n" \
"v_and_b32_e32 v103, s43, v107\n" \
"v_pk_fma_f32 v[0:1], v[208:209], v[128:129], v[0:1]\n" \
"v_pk_fma_f32 v[2:3], v[210:211], v[130:131], v[2:3]\n" \
"v_pk_fma_f32 v[8:9], v[212:213], v[136:137], v[8:9]\n" \
"v_pk_fma_f32 v[10:11], v[214:215], v[138:139], v[10:11]\n" \
"v_pk_mul_f32 v[6:7], v[0:1], v[160:161]\n" \
"v_pk_mul_f32 v[12:13], v[0:1], v[0:1]\n" \
"v_pk_add_f32 v[4:5], v[0:1], v[2:3]\n" \
"v_pk_fma_f32 v[6:7], v[2:3], v[162:163], v[6:7]\n" \
"v_pk_fma_f32 v[12:13], v[2:3], v[2:3], v[12:13]\n" \
"v_pk_add_f32 v[4:5], v[4:5], v[8:9]\n" \
"v_pk_fma_f32 v[6:7], v[8:9], v[168:169], v[6:7]\n" \
"v_pk_fma_f32 v[12:13], v[8:9], v[8:9], v[12:13]\n" \
"v_pk_add_f32 v[4:5], v[4:5], v[10:11]\n" \
"v_pk_fma_f32 v[6:7], v[10:11], v[170:171], v[6:7]\n" \
"v_pk_fma_f32 v[12:13], v[10:11], v[10:11], v[12:13]\n" \
"v_mfma_f32_16x16x32_bf16 v[208:211], v[230:233], a[212:215], 0\n" \
"v_mfma_f32_16x16x32_bf16 v[212:215], v[230:233], a[220:223], 0\n" \
"v_mfma_f32_16x16x32_bf16 v[208:211], v[234:237], a[216:219], v[208:211]\n" \
"v_mfma_f32_16x16x32_bf16 v[212:215], v[234:237], a[224:227], v[212:215]\n" \
"v_lshlrev_b32_e32 v104, 16, v108\n" \
"v_and_b32_e32 v105, s43, v108\n" \
"v_lshlrev_b32_e32 v106, 16, v109\n" \
"v_and_b32_e32 v107, s43, v109\n" \
"v_lshlrev_b32_e32 v108, 16, v110\n" \
"v_and_b32_e32 v109, s43, v110\n" \
"v_lshlrev_b32_e32 v110, 16, v111\n" \
"v_and_b32_e32 v111, s43, v111\n" \
"v_pk_fma_f32 v[16:17], v[208:209], v[132:133], v[16:17]\n" \
"v_pk_fma_f32 v[18:19], v[210:211], v[134:135], v[18:19]\n" \
"v_pk_fma_f32 v[24:25], v[212:213], v[140:141], v[24:25]\n" \
"v_pk_fma_f32 v[26:27], v[214:215], v[142:143], v[26:27]\n" \
"v_pk_add_f32 v[4:5], v[4:5], v[16:17]\n" \
"v_pk_fma_f32 v[6:7], v[16:17], v[164:165], v[6:7]\n" \
"v_pk_fma_f32 v[12:13], v[16:17], v[16:17], v[12:13]\n" \
"v_pk_add_f32 v[4:5], v[4:5], v[18:19]\n" \
"v_pk_fma_f32 v[6:7], v[18:19], v[166:167], v[6:7]\n" \
"v_pk_fma_f32 v[12:13], v[18:19], v[18:19], v[12:13]\n" \
"v_pk_add_f32 v[4:5], v[4:5], v[24:25]\n" \
"v_pk_fma_f32 v[6:7], v[24:25], v[172:173], v[6:7]\n" \
"v_pk_fma_f32 v[12:13], v[24:25], v[24:25], v[12:13]\n" \
"v_pk_add_f32 v[4:5], v[4:5], v[26:27]\n" \
"v_pk_fma_f32 v[6:7], v[26:27], v[174:175], v[6:7]\n" \
"v_pk_fma_f32 v[12:13], v[26:27], v[26:27], v[12:13]\n" \
"s_cmp_lg_u32 s50, 0\n" \
"s_cbranch_scc1 TLre_%=\n" \
"v_and_b32_e32 v208, 0x800, v229\n" \
"v_cmp_eq_u32_e32 vcc, 0, v208\n" \
"s_cbranch_vccz TLrl4_%=\n" \
"v_mov_b32_e32 v216, s40\n" \
"TLrq4_%=:\n" \
"buffer_load_dword v225, off, %[srdz], s53 offset:0 sc1\n" \
"s_mov_b32 s52, 10\n" \
"TLrs4_%=:\n" \
"ds_read_b32 v229, v216 offset:14336\n" \
"s_waitcnt lgkmcnt(0)\n" \
"v_and_b32_e32 v208, 0x800, v229\n" \
"v_cmp_eq_u32_e32 vcc, 0, v208\n" \
"s_cbranch_vccz TLrd4_%=\n" \
"s_sub_u32 s52, s52, 1\n" \
"s_cbranch_scc0 TLrs4_%=\n" \
"s_sub_u32 s51, s51, 1\n" \
"s_cselect_b32 s51, 0, s51\n" \
"s_waitcnt vmcnt(0)\n" \
"v_and_b32_e32 v208, 0x800, v225\n" \
"v_cmp_eq_u32_e32 vcc, 0, v208\n" \
"s_cbranch_scc1 TLrl4_%=\n" \
"s_cbranch_vccnz TLrq4_%=\n" \
"v_mov_b32_e32 v229, v225\n" \
"s_mov_b64 exec, 1\n" \
"ds_max_u32 v216, v225 offset:14336\n" \
"s_mov_b64 exec, -1\n" \
"TLrd4_%=:\n" \
"s_sleep 4\n" \
"TLrl4_%=:\n" \
"s_mov_b32 s63, 278528\n" \
"s_nop 0\n" \
"buffer_load_dword %[vxs], %[vdn], %[srdz], s63 offen offset:0 sc1\n" \
"buffer_load_dwordx4 a[164:167], v217, %[srdz], s54 offen offset:0 sc1\n" \
"buffer_load_dwordx4 a[168:171], v217, %[srdz], s54 offen offset:1024 sc1\n" \
"buffer_load_dwordx4 a[172:175], v217, %[srdz], s54 offen offset:2048 sc1\n" \
"buffer_load_dwordx4 a[176:179], v217, %[srdz], s54 offen offset:3072 sc1\n" \
"buffer_load_dwordx4 a[180:183], v217, %[srdz], s55 offen offset:0 sc1\n" \
"buffer_load_dwordx4 a[184:187], v217, %[srdz], s55 offen offset:1024 sc1\n" \
"buffer_load_dwordx4 a[188:191], v217, %[srdz], s55 offen offset:2048 sc1\n" \
"buffer_load_dwordx4 a[192:195], v217, %[srdz], s55 offen offset:3072 sc1\n" \
"s_waitcnt vmcnt(4)\n" \
"s_branch TLrf_%=\n" \
"TLre_%=:\n" \
"s_waitcnt vmcnt(9)\n" \
"TLrf_%=:\n" \
"v_mfma_f32_16x16x32_bf16 v[208:211], v[230:233], a[164:167], 0\n" \
"v_mfma_f32_16x16x32_bf16 v[212:215], v[230:233], a[172:175], 0\n" \
"v_mfma_f32_16x16x32_bf16 v[208:211], v[234:237], a[168:171], v[208:211]\n" \
"v_mfma_f32_16x16x32_bf16 v[212:215], v[234:237], a[176:179], v[212:215]\n" \
"s_lshl_b32 s62, %[wid], 3\n" \
"v_and_b32_e32 v216, 4, v222\n" \
"s_add_u32 s62, s62, s40\n" \
"v_lshlrev_b32_e32 v229, 4, v224\n" \
"v_add_u32_e32 v216, s62, v216\n" \
"s_movk_i32 s52, 0x1000\n" \
"s_nop 0\n" \
"v_pk_fma_f32 v[32:33], v[208:209], v[144:145], v[32:33]\n" \
"v_pk_fma_f32 v[34:35], v[210:211], v[146:147], v[34:35]\n" \
"v_pk_fma_f32 v[40:41], v[212:213], v[152:153], v[40:41]\n" \
"v_pk_fma_f32 v[42:43], v[214:215], v[154:155], v[42:43]\n" \
"v_pk_mul_f32 v[36:37], v[32:33], v[176:177]\n" \
"v_pk_mul_f32 v[38:39], v[32:33], v[32:33]\n" \
"v_pk_add_f32 v[14:15], v[32:33], v[34:35]\n" \
"v_pk_fma_f32 v[36:37], v[34:35], v[178:179], v[36:37]\n" \
"v_pk_fma_f32 v[38:39], v[34:35], v[34:35], v[38:39]\n" \
"v_pk_add_f32 v[14:15], v[14:15], v[40:41]\n" \
"v_pk_fma_f32 v[36:37], v[40:41], v[184:185], v[36:37]\n" \
"v_pk_fma_f32 v[38:39], v[40:41], v[40:41], v[38:39]\n" \
"v_pk_add_f32 v[14:15], v[14:15], v[42:43]\n" \
"v_pk_fma_f32 v[36:37], v[42:43], v[186:187], v[36:37]\n" \
"v_pk_fma_f32 v[38:39], v[42:43], v[42:43], v[38:39]\n" \
"s_waitcnt vmcnt(0)\n" \
"v_and_b32_e32 v225, 7, v222\n" \
"v_lshl_add_u32 v225, v225, 2, s40\n" \
"v_and_b32_e32 v44, 0x820, %[vxs]\n" \
"v_cmp_ne_u32_e32 vcc, 0x820, v44\n" \
"s_mov_b32 s59, 0\n" \
"s_cbranch_vccnz TLcn_%=\n" \
"s_mov_b32 s59, 1\n" \
"buffer_load_dwordx4 a[128:131], %[vct], %[srdz], 0 offen offset:0 sc1\n" \
"buffer_load_dwordx4 a[132:135], %[rd0], %[srdz], 0 offen offset:0 sc1\n" \
"buffer_load_dwordx4 a[136:139], %[vct], %[srdz], s56 offen offset:0 sc1\n" \
"buffer_load_dwordx4 a[140:143], %[rd0], %[srdz], s56 offen offset:0 sc1\n" \
"buffer_load_dwordx4 a[144:147], %[vct], %[srdz], s57 offen offset:0 sc1\n" \
"buffer_load_dwordx4 a[148:151], %[rd0], %[srdz], s57 offen offset:0 sc1\n" \
"buffer_load_dwordx4 a[152:155], %[vct], %[srdz], s58 offen offset:0 sc1\n" \
"buffer_load_dwordx4 a[156:159], %[rd0], %[srdz], s58 offen offset:0 sc1\n" \
"TLcn_%=:\n" \
"v_mfma_f32_16x16x32_bf16 v[208:211], v[230:233], a[180:183], 0\n" \
"v_mfma_f32_16x16x32_bf16 v[212:215], v[230:233], a[188:191], 0\n" \
"v_mfma_f32_16x16x32_bf16 v[208:211], v[234:237], a[184:187], v[208:211]\n" \
"v_mfma_f32_16x16x32_bf16 v[212:215], v[234:237], a[192:195], v[212:215]\n" \
"v_lshlrev_b32_e32 v80, 16, v88\n" \
"v_and_b32_e32 v81, s43, v88\n" \
"v_lshlrev_b32_e32 v82, 16, v89\n" \
"v_and_b32_e32 v83, s43, v89\n" \
"v_lshlrev_b32_e32 v84, 16, v90\n" \
"v_and_b32_e32 v85, s43, v90\n" \
"v_lshlrev_b32_e32 v86, 16, v91\n" \
"v_pk_fma_f32 v[48:49], v[208:209], v[148:149], v[48:49]\n" \
"v_pk_fma_f32 v[50:51], v[210:211], v[150:151], v[50:51]\n" \
"v_pk_fma_f32 v[56:57], v[212:213], v[156:157], v[56:57]\n" \
"v_pk_fma_f32 v[58:59], v[214:215], v[158:159], v[58:59]\n" \
"v_pk_add_f32 v[14:15], v[14:15], v[48:49]\n" \
"v_pk_fma_f32 v[36:37], v[48:49], v[180:181], v[36:37]\n" \
"v_pk_fma_f32 v[38:39], v[48:49], v[48:49], v[38:39]\n" \
"v_pk_add_f32 v[14:15], v[14:15], v[50:51]\n" \
"v_pk_fma_f32 v[36:37], v[50:51], v[182:183], v[36:37]\n" \
"v_pk_fma_f32 v[38:39], v[50:51], v[50:51], v[38:39]\n" \
"v_pk_add_f32 v[14:15], v[14:15], v[56:57]\n" \
"v_pk_fma_f32 v[36:37], v[56:57], v[188:189], v[36:37]\n" \
"v_pk_fma_f32 v[38:39], v[56:57], v[56:57], v[38:39]\n" \
"v_pk_add_f32 v[14:15], v[14:15], v[58:59]\n" \
"v_pk_fma_f32 v[36:37], v[58:59], v[190:191], v[36:37]\n" \
"v_pk_fma_f32 v[38:39], v[58:59], v[58:59], v[38:39]\n" \
"v_pk_add_f32 v[4:5], v[4:5], v[14:15]\n" \
"v_pk_add_f32 v[6:7], v[6:7], v[36:37]\n" \
"v_pk_add_f32 v[12:13], v[12:13], v[38:39]\n" \
"v_add_f32_e32 v4, v4, v5\n" \
"v_add_f32_e32 v6, v6, v7\n" \
"v_add_f32_e32 v12, v12, v13\n" \
"v_and_b32_e32 v87, s43, v91\n" \
"v_mov_b32_e32 v208, v12\n" \
"v_permlane32_swap_b32_e32 v4, v6\n" \
"v_lshlrev_b32_e32 v88, 16, v92\n" \
"v_permlane32_swap_b32_e32 v12, v208\n" \
"v_add_f32_e32 v4, v4, v6\n" \
"v_add_f32_e32 v12, v12, v208\n" \
"v_and_b32_e32 v89, s43, v92\n" \
"v_lshlrev_b32_e32 v90, 16, v93\n" \
"v_permlane16_swap_b32_e32 v4, v12\n" \
"v_add_f32_e32 v4, v4, v12\n" \
"v_and_b32_e32 v91, s43, v93\n" \
"v_lshlrev_b32_e32 v92, 16, v94\n" \
"v_add_f32_dpp v4, v4, v4 quad_perm:[1,0,3,2] row_mask:0xf bank_mask:0xf\n" \
"v_and_b32_e32 v93, s43, v94\n" \
"v_lshlrev_b32_e32 v94, 16, v95\n" \
"v_add_f32_dpp v4, v4, v4 quad_perm:[2,3,0,1] row_mask:0xf bank_mask:0xf\n" \
"v_and_b32_e32 v95, s43, v95\n" \
"s_nop 0\n" \
"v_add_f32_dpp v4, v4, v4 row_ror:8 row_mask:0xf bank_mask:0xf\n" \
"v_mov_b32_e32 v12, v4\n" \
"s_nop 0\n" \
"s_nop 0\n" \
"v_permlane16_swap_b32_e32 v4, v12\n" \
"v_mov_b32_e32 v6, v4\n" \
"v_mul_f32_e32 v12, 0x3a800000, v12\n" \
"s_nop 0\n" \
"v_permlane32_swap_b32_e32 v4, v6\n" \
"v_mul_f32_e32 v218, 0x3a800000, v4\n" \
"v_mul_f32_e32 v208, v218, v218\n" \
"v_sub_f32_e32 v12, v12, v208\n" \
"v_cmp_nle_f32_e32 vcc, v208, v12\n" \
"v_add_f32_e32 v12, 0x3727c5ac, v12\n" \
"v_rsq_f32_e32 v220, v12\n" \
"v_fma_f32 v210, -v218, v223, v6\n" \
"s_mov_b64 exec, 17\n" \
"v_mul_f32_e32 v210, v210, v220\n" \
"v_max_f32_e32 v210, 0xff800000, v210\n" \
"v_ashrrev_i32_e32 v208, 31, v210\n" \
"v_or_b32_e32 v208, 0x80000000, v208\n" \
"v_xor_b32_e32 v210, v210, v208\n" \
"v_and_b32_e32 v210, 0xfffff800, v210\n" \
"v_or_b32_e32 v210, v210, %[ta0]\n" \
"ds_write_b32 v216, v210 offset:14400\n" \
"s_mov_b64 exec, -1\n" \
"s_cbranch_vccnz TLrare_%=\n" \
"TLrok_%=:\n" \
"s_movk_i32 s52, 0x1000\n" \
"s_waitcnt lgkmcnt(0)\n" \
"s_barrier\n" \
"ds_read_b32 v209, v225 offset:14400\n" \
"v_mov_b32_e32 v227, 1\n" \
"v_mov_b32_e32 v191, 0\n" \
"v_mov_b32_e32 v190, 0\n" \
"v_mov_b32_e32 v225, -1\n" \
"s_mov_b32 s41, 1\n" \
"s_cmp_lg_u32 %[wbytes], 0\n" \
"s_cselect_b32 s42, -8, 0\n" \
"s_waitcnt lgkmcnt(0)\n" \
"v_min_u32_dpp v208, v209, v209 quad_perm:[1,0,3,2] row_mask:0xf bank_mask:0xf\n" \
"v_max_u32_dpp v209, v209, v209 quad_perm:[1,0,3,2] row_mask:0xf bank_mask:0xf\n" \
"s_nop 1\n" \
"v_min_u32_dpp v210, v209, v209 quad_perm:[2,3,0,1] row_mask:0xf bank_mask:0xf\n" \
"v_max_u32_dpp v211, v208, v208 quad_perm:[2,3,0,1] row_mask:0xf bank_mask:0xf\n" \
"v_max_u32_dpp v209, v209, v209 quad_perm:[2,3,0,1] row_mask:0xf bank_mask:0xf\n" \
"v_max_u32_e32 v208, v210, v211\n" \
"s_nop 0\n" \
"v_min_u32_dpp v210, v209, v209 row_half_mirror row_mask:0xf bank_mask:0xf\n" \
"v_max_u32_dpp v211, v208, v208 row_half_mirror row_mask:0xf bank_mask:0xf\n" \
"v_max_u32_dpp v209, v209, v209 row_half_mirror row_mask:0xf bank_mask:0xf\n" \
"v_max_u32_e32 v208, v210, v211\n" \
"s_mov_b64 exec, 3\n" \
"buffer_atomic_or_x2 v[208:209], v229, %[srdz], s48 offen sc1\n" \
"s_mov_b64 exec, -1\n" \
"s_waitcnt vmcnt(1)\n" \
"TLcs_%=:\n" \
"v_mfma_f32_16x16x32_bf16 v[128:131], v[230:233], a[128:131], 0\n" \
"v_mfma_f32_16x16x32_bf16 v[132:135], v[230:233], a[136:139], 0\n" \
"v_mfma_f32_16x16x32_bf16 v[128:131], v[234:237], a[132:135], v[128:131]\n" \
"v_mfma_f32_16x16x32_bf16 v[132:135], v[234:237], a[140:143], v[132:135]\n" \
"v_mfma_f32_16x16x32_bf16 v[136:139], v[230:233], a[144:147], 0\n" \
"v_mfma_f32_16x16x32_bf16 v[140:143], v[230:233], a[152:155], 0\n" \
"v_mfma_f32_16x16x32_bf16 v[136:139], v[234:237], a[148:151], v[136:139]\n" \
"v_mfma_f32_16x16x32_bf16 v[140:143], v[234:237], a[156:159], v[140:143]\n" \
"s_nop 2\n" \
"v_pk_fma_f32 v[144:145], v[128:129], v[64:65], v[80:81]\n" \
"v_pk_fma_f32 v[146:147], v[130:131], v[66:67], v[82:83]\n" \
"v_pk_fma_f32 v[148:149], v[132:133], v[68:69], v[84:85]\n" \
"v_pk_fma_f32 v[150:151], v[134:135], v[70:71], v[86:87]\n" \
"v_pk_mul_f32 v[160:161], v[144:145], v[96:97]\n" \
"v_pk_mul_f32 v[162:163], v[146:147], v[98:99]\n" \
".if . - adaln_body != 65536\n" \
".error \"size model\"\n" \
".endif\n" \
"Lk15_%=:\n" \
"s_cbranch_execz Lk16_%=\n" \
"s_nop 0\n" \
"v_pk_mul_f32 v[164:165], v[148:149], v[100:101]\n" \
"v_pk_mul_f32 v[166:167], v[150:151], v[102:103]\n" \
"v_cvt_pk_bf16_f32 v176, v160, v161\n" \
"v_cvt_pk_bf16_f32 v177, v162, v163\n" \
"v_cvt_pk_bf16_f32 v178, v164, v165\n" \
"v_cvt_pk_bf16_f32 v179, v166, v167\n" \
"ds_write_b128 %[oq0], v[176:179] offset:16384\n" \
"v_pk_fma_f32 v[152:153], v[136:137], v[72:73], v[88:89]\n" \
"v_pk_fma_f32 v[154:155], v[138:139], v[74:75], v[90:91]\n" \
"v_pk_fma_f32 v[156:157], v[140:141], v[76:77], v[92:93]\n" \
"v_pk_fma_f32 v[158:159], v[142:143], v[78:79], v[94:95]\n" \
"v_pk_mul_f32 v[168:169], v[152:153], v[104:105]\n" \
"v_pk_mul_f32 v[170:171], v[154:155], v[106:107]\n" \
"v_pk_mul_f32 v[172:173], v[156:157], v[108:109]\n" \
"v_pk_mul_f32 v[174:175], v[158:159], v[110:111]\n" \
"v_cvt_pk_bf16_f32 v180, v168, v169\n" \
"v_cvt_pk_bf16_f32 v181, v170, v171\n" \
"v_cvt_pk_bf16_f32 v182, v172, v173\n" \
"v_cvt_pk_bf16_f32 v183, v174, v175\n" \
"ds_write_b128 %[wo0], v[180:183] offset:16384\n" \
"ds_read_b128 v[4:7], v226 offset:0\n" \
"ds_read_b128 v[12:15], v226 offset:64\n" \
"ds_read_b128 v[20:23], v226 offset:128\n" \
"ds_read_b128 v[28:31], v226 offset:192\n" \
"ds_read_b128 v[36:39], v226 offset:256\n" \
"ds_read_b128 v[44:47], v226 offset:320\n" \
"ds_read_b128 v[52:55], v226 offset:384\n" \
"ds_read_b128 v[60:63], v226 offset:448\n" \
"v_cmp_u_f32_e32 vcc, v128, v132\n" \
"v_cmp_u_f32_e64 s[62:63], v136, v140\n" \
"s_or_b64 vcc, vcc, s[62:63]\n" \
"s_cbranch_scc1 TLcx_%=\n" \
"TLco_%=:\n" \
"v_pk_add_f32 v[184:185], v[144:145], v[146:147]\n" \
"v_pk_mul_f32 v[186:187], v[144:145], v[144:145]\n" \
"v_pk_mul_f32 v[188:189], v[144:145], v[112:113]\n" \
"v_pk_fma_f32 v[186:187], v[146:147], v[146:147], v[186:187]\n" \
"v_pk_fma_f32 v[188:189], v[146:147], v[114:115], v[188:189]\n" \
"v_pk_add_f32 v[184:185], v[184:185], v[148:149]\n" \
"v_pk_fma_f32 v[186:187], v[148:149], v[148:149], v[186:187]\n" \
"v_pk_fma_f32 v[188:189], v[148:149], v[116:117], v[188:189]\n" \
"v_pk_add_f32 v[184:185], v[184:185], v[150:151]\n" \
"v_pk_fma_f32 v[186:187], v[150:151], v[150:151], v[186:187]\n" \
"v_pk_fma_f32 v[188:189], v[150:151], v[118:119], v[188:189]\n" \
"v_pk_add_f32 v[184:185], v[184:185], v[152:153]\n" \
"v_pk_fma_f32 v[186:187], v[152:153], v[152:153], v[186:187]\n" \
"v_pk_fma_f32 v[188:189], v[152:153], v[120:121], v[188:189]\n" \
"v_pk_add_f32 v[184:185], v[184:185], v[154:155]\n" \
"v_pk_fma_f32 v[186:187], v[154:155], v[154:155], v[186:187]\n" \
"v_pk_fma_f32 v[188:189], v[154:155], v[122:123], v[188:189]\n" \
"v_pk_add_f32 v[184:185], v[184:185], v[156:157]\n" \
"v_pk_fma_f32 v[186:187], v[156:157], v[156:157], v[186:187]\n" \
"v_pk_fma_f32 v[188:189], v[156:157], v[124:125], v[188:189]\n" \
"v_pk_add_f32 v[184:185], v[184:185], v[158:159]\n" \
"v_pk_fma_f32 v[186:187], v[158:159], v[158:159], v[186:187]\n" \
"v_pk_fma_f32 v[188:189], v[158:159], v[126:127], v[188:189]\n" \
"v_add_f32_e32 v184, v184, v185\n" \
"v_add_f32_e32 v186, v186, v187\n" \
"v_add_f32_e32 v188, v188, v189\n" \
"s_waitcnt lgkmcnt(7)\n" \
"v_mfma_f32_16x16x32_bf16 v[208:211], a[64:67], v[4:7], 0\n" \
"v_mfma_f32_16x16x32_bf16 v[212:215], a[96:99], v[4:7], 0\n" \
"v_permlane16_swap_b32_e32 v184, v186\n" \
"v_mov_b32_e32 v189, v188\n" \
"s_waitcnt lgkmcnt(6)\n" \
"v_mfma_f32_16x16x32_bf16 v[208:211], a[68:71], v[12:15], v[208:211]\n" \
"v_mfma_f32_16x16x32_bf16 v[212:215], a[100:103], v[12:15], v[212:215]\n" \
"v_add_f32_e32 v184, v184, v186\n" \
"v_permlane16_swap_b32_e32 v188, v189\n" \
"s_waitcnt lgkmcnt(5)\n" \
"v_mfma_f32_16x16x32_bf16 v[208:211], a[72:75], v[20:23], v[208:211]\n" \
"v_mfma_f32_16x16x32_bf16 v[212:215], a[104:107], v[20:23], v[212:215]\n" \
"v_add_f32_e32 v188, v188, v189\n" \
"v_add_u32_e32 v176, 1632, %[kd0]\n" \
"s_waitcnt lgkmcnt(4)\n" \
"v_mfma_f32_16x16x32_bf16 v[208:211], a[76:79], v[28:31], v[208:211]\n" \
"v_mfma_f32_16x16x32_bf16 v[212:215], a[108:111], v[28:31], v[212:215]\n" \
"v_add_f32_dpp v184, v184, v184 quad_perm:[1,0,3,2] row_mask:0xf bank_mask:0xf\n" \
"v_add_f32_dpp v188, v188, v188 quad_perm:[1,0,3,2] row_mask:0xf bank_mask:0xf\n" \
"s_waitcnt lgkmcnt(3)\n" \
"v_mfma_f32_16x16x32_bf16 v[208:211], a[80:83], v[36:39], v[208:211]\n" \
"v_mfma_f32_16x16x32_bf16 v[212:215], a[112:115], v[36:39], v[212:215]\n" \
"v_add_f32_dpp v184, v184, v184 quad_perm:[2,3,0,1] row_mask:0xf bank_mask:0xf\n" \
"v_add_f32_dpp v188, v188, v188 quad_perm:[2,3,0,1] row_mask:0xf bank_mask:0xf\n" \
"s_waitcnt lgkmcnt(2)\n" \
"v_mfma_f32_16x16x32_bf16 v[208:211], a[84:87], v[44:47], v[208:211]\n" \
"v_mfma_f32_16x16x32_bf16 v[212:215], a[116:119], v[44:47], v[212:215]\n" \
"v_add_f32_dpp v184, v184, v184 row_ror:8 row_mask:0xf bank_mask:0xf\n" \
"v_add_f32_dpp v188, v188, v188 row_ror:8 row_mask:0xf bank_mask:0xf\n" \
"s_waitcnt lgkmcnt(1)\n" \
"v_mfma_f32_16x16x32_bf16 v[208:211], a[88:91], v[52:55], v[208:211]\n" \
"v_mfma_f32_16x16x32_bf16 v[212:215], a[120:123], v[52:55], v[212:215]\n" \
"s_mov_b32 vcc_lo, 0xaaaaaaaa\n" \
"s_mov_b32 vcc_hi, 0xaaaaaaaa\n" \
"v_cndmask_b32_e32 v184, v184, v188, vcc\n" \
"s_waitcnt lgkmcnt(0)\n" \
"v_mfma_f32_16x16x32_bf16 v[208:211], a[92:95], v[60:63], v[208:211]\n" \
"v_mfma_f32_16x16x32_bf16 v[212:215], a[124:127], v[60:63], v[212:215]\n" \
"s_mov_b32 exec_lo, 0x330033\n" \
"s_mov_b32 exec_hi, 0x330033\n" \
"ds_write_b32 v228, v184 offset:0\n" \
"s_mov_b32 exec_lo, 0x3f003f\n" \
"s_mov_b32 exec_hi, 0x3f003f\n" \
"s_nop 1\n" \
"ds_write2_b32 %[kd0], v208, v209 offset0:0 offset1:4\n" \
"ds_write2_b32 %[kd0], v210, v211 offset0:8 offset1:12\n" \
"ds_write2_b32 v176, v212, v213 offset0:0 offset1:4\n" \
"ds_write2_b32 v176, v214, v215 offset0:8 offset1:12\n" \
"s_mov_b64 exec, -1\n" \
"s_waitcnt lgkmcnt(0)\n" \
"s_barrier\n" \
"ds_read_b128 v[4:7], %[vg0] offset:0\n" \
"ds_read_b128 v[12:15], %[vg0] offset:16\n" \
"ds_read_b128 v[20:23], %[vg0] offset:32\n" \
"ds_read_b128 v[28:31], v221 offset:0\n" \
"ds_read_b128 v[36:39], %[vx0] offset:1088\n" \
"ds_read_b128 v[44:47], %[vx0] offset:1360\n" \
"ds_read_b128 v[52:55], v221 offset:1632\n" \
"ds_read_b128 v[60:63], %[vx0] offset:2720\n" \
"ds_read_b128 v[128:131], %[vx0] offset:2992\n" \
"v_bfe_u32 v208, v222, 3, 1\n" \
"v_and_b32_e32 v209, 1, v222\n" \
"v_lshl_or_b32 v208, v208, 1, v209\n" \
"s_waitcnt lgkmcnt(6)\n" \
"v_pk_add_f32 v[4:5], v[4:5], v[6:7]\n" \
"v_pk_add_f32 v[12:13], v[12:13], v[14:15]\n" \
"v_pk_add_f32 v[20:21], v[20:21], v[22:23]\n" \
"v_add_f32_e32 v4, v4, v5\n" \
"v_add_f32_e32 v12, v12, v13\n" \
"v_add_f32_e32 v20, v20, v21\n" \
"v_mul_f32_e32 v4, 0x3a800000, v4\n" \
"v_mul_f32_e32 v12, 0x3a800000, v12\n" \
"v_mul_f32_e32 v209, v4, v4\n" \
"v_sub_f32_e32 v12, v12, v209\n" \
"s_waitcnt lgkmcnt(3)\n" \
"v_cmp_nle_f32_e64 s[50:51], v209, v12\n" \
"v_add_f32_e32 v12, 0x3727c5ac, v12\n" \
"v_pk_add_f32 v[28:29], v[28:29], v[30:31]\n" \
"v_rsq_f32_e32 v132, v12\n" \
"v_pk_add_f32 v[36:37], v[36:37], v[38:39]\n" \
"v_fma_f32 v20, -v4, v223, v20\n" \
"v_pk_add_f32 v[44:45], v[44:45], v[46:47]\n" \
"v_add_f32_e32 v28, v28, v29\n" \
"v_add_f32_e32 v36, v36, v37\n" \
"v_add_f32_e32 v44, v44, v45\n" \
"v_mul_f32_e32 v20, v20, v132\n" \
"s_waitcnt lgkmcnt(0)\n" \
"v_pk_add_f32 v[52:53], v[52:53], v[54:55]\n" \
"v_max_f32_e32 v210, 0xff800000, v20\n" \
"v_pk_add_f32 v[60:61], v[60:61], v[62:63]\n" \
"v_ashrrev_i32_e32 v209, 31, v210\n" \
"v_pk_add_f32 v[128:129], v[128:129], v[130:131]\n" \
"v_or_b32_e32 v209, 0x80000000, v209\n" \
"v_add_f32_e32 v52, v52, v53\n" \
"v_xor_b32_e32 v210, v210, v209\n" \
"v_add_f32_e32 v60, v60, v61\n" \
"v_and_b32_e32 v210, 0xfffff800, v210\n" \
"v_add_f32_e32 v128, v128, v129\n" \
"v_and_or_b32 v210, v210, v225, v208\n" \
"v_fma_f32 v28, -v4, v36, v28\n" \
"v_fma_f32 v52, -v4, v60, v52\n" \
"v_fma_f32 v28, v28, v132, v44\n" \
"v_min_u32_dpp v211, v210, v210 quad_perm:[1,0,3,2] row_mask:0xf bank_mask:0xf\n" \
"v_fma_f32 v52, v52, v132, v128\n" \
"v_max_u32_dpp v210, v210, v210 quad_perm:[1,0,3,2] row_mask:0xf bank_mask:0xf\n" \
"v_cvt_pk_bf16_f32 v28, 0, v28\n" \
"v_cvt_pk_bf16_f32 v52, 0, v52\n" \
"v_min_u32_dpp v212, v210, v210 row_ror:8 row_mask:0xf bank_mask:0xf\n" \
"v_mul_f32_e32 v214, 0xbfb8aa3b, v28\n" \
"v_max_u32_dpp v213, v211, v211 row_ror:8 row_mask:0xf bank_mask:0xf\n" \
"v_exp_f32_e32 v214, v214\n" \
"v_max_u32_dpp v210, v210, v210 row_ror:8 row_mask:0xf bank_mask:0xf\n" \
"v_add_f32_e32 v214, 1.0, v214\n" \
"v_max_u32_e32 v211, v212, v213\n" \
"v_rcp_f32_e32 v214, v214\n" \
"s_nop 0\n" \
"v_mul_f32_e32 v28, v28, v214\n" \
"v_cvt_pk_bf16_f32 v28, 0, v28\n" \
"v_mul_f32_e32 v28, v28, v52\n" \
"v_cvt_pk_bf16_f32 v28, 0, v28\n" \
"v_cndmask_b32_e32 v133, v210, v211, vcc\n" \
"v_readfirstlane_b32 s60, v210\n" \
"v_readfirstlane_b32 s61, v211\n" \
"v_xor_b32_e32 v212, v133, v208\n" \
"v_and_b32_e32 v213, 1, v212\n" \
"v_and_b32_e32 v212, 2, v212\n" \
"v_cmp_ne_u32_e64 s[54:55], 0, v213\n" \
"v_cmp_ne_u32_e64 s[56:57], 0, v212\n" \
"s_mov_b32 s62, 0x07060302\n" \
"v_mov_b32_dpp v213, v28 quad_perm:[1,0,3,2] row_mask:0xf bank_mask:0xf\n" \
"v_cndmask_b32_e64 v28, v28, v213, s[54:55]\n" \
"s_nop 1\n" \
"v_mov_b32_dpp v214, v28 row_ror:8 row_mask:0xf bank_mask:0xf\n" \
"v_cndmask_b32_e64 v28, v28, v214, s[56:57]\n" \
"s_mov_b32 vcc_lo, 0xcccccccc\n" \
"s_mov_b32 vcc_hi, 0xcccccccc\n" \
"v_mov_b32_dpp v213, v28 quad_perm:[2,3,0,1] row_mask:0xf bank_mask:0xf\n" \
"s_mov_b32 s58, 0x30003\n" \
"s_mov_b32 s59, 0xc000c\n" \
"v_perm_b32 v214, v213, v28, s62\n" \
"v_perm_b32 v215, v28, v213, s62\n" \
"v_cndmask_b32_e32 v214, v214, v215, vcc\n" \
"s_mov_b32 vcc_lo, 0xf0f0f0f0\n" \
"s_mov_b32 vcc_hi, 0xf0f0f0f0\n" \
"v_mov_b32_dpp v213, v214 row_ror:4 row_mask:0xf bank_mask:0xf\n" \
"v_cndmask_b32_e32 v164, v214, v213, vcc\n" \
"v_cndmask_b32_e32 v165, v213, v214, vcc\n" \
"v_mov_b32_e32 v166, v164\n" \
"v_mov_b32_e32 v167, v165\n" \
"s_nop 0\n" \
"v_permlane32_swap_b32_e32 v164, v166\n" \
"v_permlane32_swap_b32_e32 v165, v167\n" \
"s_nop 0\n" \
"v_permlane16_swap_b32_e32 v164, v166\n" \
"v_permlane16_swap_b32_e32 v165, v167\n" \
"v_cndmask_b32_e64 v176, 0, v164, s[58:59]\n" \
"v_cndmask_b32_e64 v177, 0, v165, s[58:59]\n" \
"v_cndmask_b32_e64 v178, 0, v166, s[58:59]\n" \
"v_cndmask_b32_e64 v179, 0, v167, s[58:59]\n" \
"s_lshl_b64 s[58:59], s[58:59], 4\n" \
"v_cndmask_b32_e64 v180, 0, v164, s[58:59]\n" \
"v_cndmask_b32_e64 v181, 0, v165, s[58:59]\n" \
"v_cndmask_b32_e64 v182, 0, v166, s[58:59]\n" \
"v_cndmask_b32_e64 v183, 0, v167, s[58:59]\n" \
"s_lshl_b64 s[58:59], s[58:59], 4\n" \
"v_mfma_f32_16x16x32_bf16 v[168:171], v[176:179], a[0:3], 0\n" \
"v_mfma_f32_16x16x32_bf16 v[172:175], v[176:179], a[16:19], 0\n" \
"v_cndmask_b32_e64 v184, 0, v164, s[58:59]\n" \
"v_cndmask_b32_e64 v185, 0, v165, s[58:59]\n" \
"v_cndmask_b32_e64 v186, 0, v166, s[58:59]\n" \
"v_cndmask_b32_e64 v187, 0, v167, s[58:59]\n" \
"s_lshl_b64 s[58:59], s[58:59], 4\n" \
"v_mfma_f32_16x16x32_bf16 v[168:171], v[180:183], a[4:7], v[168:171]\n" \
"v_mfma_f32_16x16x32_bf16 v[172:175], v[180:183], a[20:23], v[172:175]\n" \
"v_cndmask_b32_e64 v136, 0, v164, s[58:59]\n" \
"v_cndmask_b32_e64 v137, 0, v165, s[58:59]\n" \
"v_cndmask_b32_e64 v138, 0, v166, s[58:59]\n" \
"v_cndmask_b32_e64 v139, 0, v167, s[58:59]\n" \
"v_mfma_f32_16x16x32_bf16 v[168:171], v[184:187], a[8:11], v[168:171]\n" \
"v_mfma_f32_16x16x32_bf16 v[172:175], v[184:187], a[24:27], v[172:175]\n" \
"v_mfma_f32_16x16x32_bf16 v[168:171], v[136:139], a[12:15], v[168:171]\n" \
"v_mfma_f32_16x16x32_bf16 v[172:175], v[136:139], a[28:31], v[172:175]\n" \
"ds_read_b128 v[64:67], %[kb0] offset:0\n" \
"ds_read_b128 v[68:71], %[kb0] offset:1024\n" \
"ds_read_b128 v[72:75], %[vvb] offset:0\n" \
"ds_read_b128 v[76:79], %[vvb] offset:1024\n" \
"ds_read_b128 v[80:83], %[kb0] offset:4096\n" \
"ds_read_b128 v[84:87], %[kb0] offset:5120\n" \
"ds_read_b128 v[88:91], %[vvb] offset:4096\n" \
"ds_read_b128 v[92:95], %[vvb] offset:5120\n" \
"v_and_or_b32 v168, v168, s42, v227\n" \
"v_and_or_b32 v169, v169, s42, v227\n" \
"v_and_or_b32 v170, v170, s42, v227\n" \
"v_and_or_b32 v171, v171, s42, v227\n" \
"buffer_store_dwordx4 v[168:171], v217, %[srdz], s69 offen offset:0 sc1\n" \
"v_and_or_b32 v172, v172, s42, v227\n" \
"v_and_or_b32 v173, v173, s42, v227\n" \
"v_and_or_b32 v174, v174, s42, v227\n" \
"v_and_or_b32 v175, v175, s42, v227\n" \
"buffer_store_dwordx4 v[172:175], v217, %[srdz], s69 offen offset:1024 sc1\n" \
"s_movk_i32 s62, -128\n" \
"s_sub_u32 s63, 65536, s40\n" \
"v_mul_i32_i24_e64 v208, v219, s62\n" \
"v_add_u32_e32 v208, s63, v208\n" \
"v_lshl_add_u32 v209, %[oq0], 1, v208\n" \
"v_lshl_add_u32 v210, %[wo0], 1, v208\n" \
"ds_write_b128 v209, v[144:147] offset:0\n" \
"ds_write_b128 v209, v[148:151] offset:16\n" \
"ds_write_b128 v210, v[152:155] offset:0\n" \
"ds_write_b128 v210, v[156:159] offset:16\n" \
"v_mov_b32_dpp v211, v4 quad_perm:[1,0,3,2] row_mask:0xf bank_mask:0xf\n" \
"v_mov_b32_dpp v213, v132 quad_perm:[1,0,3,2] row_mask:0xf bank_mask:0xf\n" \
"v_mov_b32_dpp v215, v20 quad_perm:[1,0,3,2] row_mask:0xf bank_mask:0xf\n" \
"v_cndmask_b32_e64 v4, v4, v211, s[54:55]\n" \
"v_cndmask_b32_e64 v132, v132, v213, s[54:55]\n" \
"v_cndmask_b32_e64 v20, v20, v215, s[54:55]\n" \
"v_mov_b32_dpp v212, v4 row_ror:8 row_mask:0xf bank_mask:0xf\n" \
"v_mov_b32_dpp v214, v132 row_ror:8 row_mask:0xf bank_mask:0xf\n" \
"v_mov_b32_dpp v208, v20 row_ror:8 row_mask:0xf bank_mask:0xf\n" \
"v_cndmask_b32_e64 v4, v4, v212, s[56:57]\n" \
"v_cndmask_b32_e64 v132, v132, v214, s[56:57]\n" \
"v_cndmask_b32_e64 v20, v20, v208, s[56:57]\n" \
"v_mov_b32_e32 v126, v132\n" \
"v_and_b32_e32 v125, 3, v133\n" \
"s_bitcmp1_b32 s60, 0\n" \
"s_cselect_b32 s56, s45, s44\n" \
"s_cselect_b32 s57, s47, s46\n" \
"s_bitcmp1_b32 s60, 1\n" \
"s_cselect_b32 s60, s57, s56\n" \
"s_bitcmp1_b32 s61, 0\n" \
"s_cselect_b32 s56, s45, s44\n" \
"s_cselect_b32 s57, s47, s46\n" \
"s_bitcmp1_b32 s61, 1\n" \
"s_cselect_b32 s61, s57, s56\n" \
"s_cmp_lg_u64 s[50:51], 0\n" \
"s_cselect_b32 s53, 1, 0\n" \
"s_mov_b32 s51, 0x10000\n" \
"s_cmp_lg_u32 s41, 1\n" \
"s_cbranch_scc1 TLls_%=\n" \
"v_mul_f32_e64 v208, -v218, v220\n" \
"v_pk_fma_f32 v[128:129], v[0:1], v[220:221], v[208:209] op_sel_hi:[1,0,0]\n" \
"v_pk_fma_f32 v[130:131], v[2:3], v[220:221], v[208:209] op_sel_hi:[1,0,0]\n" \
"v_pk_fma_f32 v[132:133], v[8:9], v[220:221], v[208:209] op_sel_hi:[1,0,0]\n" \
"v_pk_fma_f32 v[134:135], v[10:11], v[220:221], v[208:209] op_sel_hi:[1,0,0]\n" \
"v_pk_fma_f32 v[136:137], v[16:17], v[220:221], v[208:209] op_sel_hi:[1,0,0]\n" \
"v_pk_fma_f32 v[138:139], v[18:19], v[220:221], v[208:209] op_sel_hi:[1,0,0]\n" \
"v_pk_fma_f32 v[140:141], v[24:25], v[220:221], v[208:209] op_sel_hi:[1,0,0]\n" \
"v_pk_fma_f32 v[142:143], v[26:27], v[220:221], v[208:209] op_sel_hi:[1,0,0]\n" \
"v_pk_fma_f32 v[144:145], v[32:33], v[220:221], v[208:209] op_sel_hi:[1,0,0]\n" \
"v_pk_fma_f32 v[146:147], v[34:35], v[220:221], v[208:209] op_sel_hi:[1,0,0]\n" \
"v_pk_fma_f32 v[148:149], v[40:41], v[220:221], v[208:209] op_sel_hi:[1,0,0]\n" \
"v_pk_fma_f32 v[150:151], v[42:43], v[220:221], v[208:209] op_sel_hi:[1,0,0]\n" \
"v_pk_fma_f32 v[152:153], v[48:49], v[220:221], v[208:209] op_sel_hi:[1,0,0]\n" \
"v_pk_fma_f32 v[154:155], v[50:51], v[220:221], v[208:209] op_sel_hi:[1,0,0]\n" \
"v_pk_fma_f32 v[156:157], v[56:57], v[220:221], v[208:209] op_sel_hi:[1,0,0]\n" \
"v_pk_fma_f32 v[158:159], v[58:59], v[220:221], v[208:209] op_sel_hi:[1,0,0]\n" \
"v_sub_u32_e32 v216, 0x7ff, %[ta0]\n" \
"v_cmp_eq_u32_e64 s[56:57], s60, v216\n" \
"v_cmp_eq_u32_e64 s[58:59], s61, v216\n" \
"s_cmp_eq_u32 %[wid], 3\n" \
"s_cselect_b32 s49, s49, 0x80000000\n" \
"v_lshrrev_b32_e32 v216, 2, v224\n" \
"s_waitcnt lgkmcnt(0)\n" \
"v_lshlrev_b32_e32 v160, 16, v192\n" \
"v_and_b32_e32 v161, s43, v192\n" \
"v_lshlrev_b32_e32 v176, 16, v64\n" \
"v_and_b32_e32 v177, s43, v64\n" \
"v_lshlrev_b32_e32 v162, 16, v193\n" \
"v_and_b32_e32 v163, s43, v193\n" \
"v_lshlrev_b32_e32 v178, 16, v65\n" \
"v_and_b32_e32 v179, s43, v65\n" \
"v_lshlrev_b32_e32 v164, 16, v196\n" \
"v_and_b32_e32 v165, s43, v196\n" \
"v_lshlrev_b32_e32 v180, 16, v68\n" \
"v_and_b32_e32 v181, s43, v68\n" \
"v_lshlrev_b32_e32 v166, 16, v197\n" \
"v_and_b32_e32 v167, s43, v197\n" \
"v_lshlrev_b32_e32 v182, 16, v69\n" \
"v_and_b32_e32 v183, s43, v69\n" \
"v_pk_fma_f32 v[128:129], v[128:129], v[160:161], v[176:177]\n" \
"v_pk_fma_f32 v[130:131], v[130:131], v[162:163], v[178:179]\n" \
"v_pk_fma_f32 v[132:133], v[132:133], v[164:165], v[180:181]\n" \
"v_pk_fma_f32 v[134:135], v[134:135], v[166:167], v[182:183]\n" \
"v_lshlrev_b32_e32 v168, 16, v80\n" \
"v_and_b32_e32 v169, s43, v80\n" \
"v_lshlrev_b32_e32 v170, 16, v81\n" \
"v_and_b32_e32 v171, s43, v81\n" \
"v_lshlrev_b32_e32 v172, 16, v84\n" \
"v_and_b32_e32 v173, s43, v84\n" \
"v_lshlrev_b32_e32 v174, 16, v85\n" \
"v_and_b32_e32 v175, s43, v85\n" \
"v_pk_fma_f32 v[128:129], v[128:129], v[168:169], v[0:1]\n" \
"v_pk_fma_f32 v[130:131], v[130:131], v[170:171], v[2:3]\n" \
"v_pk_fma_f32 v[132:133], v[132:133], v[172:173], v[8:9]\n" \
"v_pk_fma_f32 v[134:135], v[134:135], v[174:175], v[10:11]\n" \
"v_cvt_pk_bf16_f32 v128, v128, v129\n" \
"v_cvt_pk_bf16_f32 v130, v130, v131\n" \
"v_cvt_pk_bf16_f32 v132, v132, v133\n" \
"v_cvt_pk_bf16_f32 v134, v134, v135\n" \
"v_lshlrev_b32_e32 v160, 16, v194\n" \
"v_and_b32_e32 v161, s43, v194\n" \
"v_lshlrev_b32_e32 v176, 16, v66\n" \
"v_and_b32_e32 v177, s43, v66\n" \
"v_lshlrev_b32_e32 v162, 16, v195\n" \
"v_and_b32_e32 v163, s43, v195\n" \
"v_lshlrev_b32_e32 v178, 16, v67\n" \
"v_and_b32_e32 v179, s43, v67\n" \
"v_lshlrev_b32_e32 v164, 16, v198\n" \
"v_and_b32_e32 v165, s43, v198\n" \
"v_lshlrev_b32_e32 v180, 16, v70\n" \
"v_and_b32_e32 v181, s43, v70\n" \
"v_lshlrev_b32_e32 v166, 16, v199\n" \
"v_and_b32_e32 v167, s43, v199\n" \
"v_lshlrev_b32_e32 v182, 16, v71\n" \
"v_and_b32_e32 v183, s43, v71\n" \
"v_pk_fma_f32 v[136:137], v[136:137], v[160:161], v[176:177]\n" \
"v_pk_fma_f32 v[138:139], v[138:139], v[162:163], v[178:179]\n" \
"v_pk_fma_f32 v[140:141], v[140:141], v[164:165], v[180:181]\n" \
"v_pk_fma_f32 v[142:143], v[142:143], v[166:167], v[182:183]\n" \
"v_lshlrev_b32_e32 v168, 16, v82\n" \
"v_and_b32_e32 v169, s43, v82\n" \
"v_lshlrev_b32_e32 v170, 16, v83\n" \
"v_and_b32_e32 v171, s43, v83\n" \
"v_lshlrev_b32_e32 v172, 16, v86\n" \
"v_and_b32_e32 v173, s43, v86\n" \
"v_lshlrev_b32_e32 v174, 16, v87\n" \
"v_and_b32_e32 v175, s43, v87\n" \
"v_pk_fma_f32 v[136:137], v[136:137], v[168:169], v[16:17]\n" \
"v_pk_fma_f32 v[138:139], v[138:139], v[170:171], v[18:19]\n" \
"v_pk_fma_f32 v[140:141], v[140:141], v[172:173], v[24:25]\n" \
"v_pk_fma_f32 v[142:143], v[142:143], v[174:175], v[26:27]\n" \
"v_cvt_pk_bf16_f32 v136, v136, v137\n" \
"v_cvt_pk_bf16_f32 v138, v138, v139\n" \
"v_cvt_pk_bf16_f32 v140, v140, v141\n" \
"v_cvt_pk_bf16_f32 v142, v142, v143\n" \
"buffer_load_dwordx4 v[112:115], v216, %[srdz], s49 offen offset:0 sc1\n" \
"buffer_load_dwordx4 v[116:119], v216, %[srdz], s49 offen offset:16 sc1\n" \
"v_lshlrev_b32_e32 v160, 16, v200\n" \
"v_and_b32_e32 v161, s43, v200\n" \
"v_lshlrev_b32_e32 v176, 16, v72\n" \
"v_and_b32_e32 v177, s43, v72\n" \
"v_lshlrev_b32_e32 v162, 16, v201\n" \
"v_and_b32_e32 v163, s43, v201\n" \
"v_lshlrev_b32_e32 v178, 16, v73\n" \
"v_and_b32_e32 v179, s43, v73\n" \
"v_lshlrev_b32_e32 v164, 16, v204\n" \
"v_and_b32_e32 v165, s43, v204\n" \
"v_lshlrev_b32_e32 v180, 16, v76\n" \
"v_and_b32_e32 v181, s43, v76\n" \
"v_lshlrev_b32_e32 v166, 16, v205\n" \
"v_and_b32_e32 v167, s43, v205\n" \
"v_lshlrev_b32_e32 v182, 16, v77\n" \
"v_and_b32_e32 v183, s43, v77\n" \
"v_pk_fma_f32 v[144:145], v[144:145], v[160:161], v[176:177]\n" \
"v_pk_fma_f32 v[146:147], v[146:147], v[162:163], v[178:179]\n" \
"v_pk_fma_f32 v[148:149], v[148:149], v[164:165], v[180:181]\n" \
"v_pk_fma_f32 v[150:151], v[150:151], v[166:167], v[182:183]\n" \
"v_lshlrev_b32_e32 v168, 16, v88\n" \
"v_and_b32_e32 v169, s43, v88\n" \
"v_lshlrev_b32_e32 v170, 16, v89\n" \
"v_and_b32_e32 v171, s43, v89\n" \
"v_lshlrev_b32_e32 v172, 16, v92\n" \
"v_and_b32_e32 v173, s43, v92\n" \
"v_lshlrev_b32_e32 v174, 16, v93\n" \
"v_and_b32_e32 v175, s43, v93\n" \
"v_pk_fma_f32 v[144:145], v[144:145], v[168:169], v[32:33]\n" \
"v_pk_fma_f32 v[146:147], v[146:147], v[170:171], v[34:35]\n" \
"v_pk_fma_f32 v[148:149], v[148:149], v[172:173], v[40:41]\n" \
"v_pk_fma_f32 v[150:151], v[150:151], v[174:175], v[42:43]\n" \
"v_cvt_pk_bf16_f32 v144, v144, v145\n" \
"v_cvt_pk_bf16_f32 v146, v146, v147\n" \
"v_cvt_pk_bf16_f32 v148, v148, v149\n" \
"v_cvt_pk_bf16_f32 v150, v150, v151\n" \
"v_lshlrev_b32_e32 v160, 16, v202\n" \
"v_and_b32_e32 v161, s43, v202\n" \
"v_lshlrev_b32_e32 v176, 16, v74\n" \
"v_and_b32_e32 v177, s43, v74\n" \
"v_lshlrev_b32_e32 v162, 16, v203\n" \
"v_and_b32_e32 v163, s43, v203\n" \
"v_lshlrev_b32_e32 v178, 16, v75\n" \
"v_and_b32_e32 v179, s43, v75\n" \
"v_lshlrev_b32_e32 v164, 16, v206\n" \
"v_and_b32_e32 v165, s43, v206\n" \
"v_lshlrev_b32_e32 v180, 16, v78\n" \
"v_and_b32_e32 v181, s43, v78\n" \
"v_lshlrev_b32_e32 v166, 16, v207\n" \
"v_and_b32_e32 v167, s43, v207\n" \
"v_lshlrev_b32_e32 v182, 16, v79\n" \
"v_and_b32_e32 v183, s43, v79\n" \
"v_pk_fma_f32 v[152:153], v[152:153], v[160:161], v[176:177]\n" \
"v_pk_fma_f32 v[154:155], v[154:155], v[162:163], v[178:179]\n" \
"v_pk_fma_f32 v[156:157], v[156:157], v[164:165], v[180:181]\n" \
"v_pk_fma_f32 v[158:159], v[158:159], v[166:167], v[182:183]\n" \
"v_lshlrev_b32_e32 v168, 16, v90\n" \
"v_and_b32_e32 v169, s43, v90\n" \
"v_lshlrev_b32_e32 v170, 16, v91\n" \
"v_and_b32_e32 v171, s43, v91\n" \
"v_lshlrev_b32_e32 v172, 16, v94\n" \
"v_and_b32_e32 v173, s43, v94\n" \
"v_lshlrev_b32_e32 v174, 16, v95\n" \
"v_and_b32_e32 v175, s43, v95\n" \
"v_pk_fma_f32 v[152:153], v[152:153], v[168:169], v[48:49]\n" \
"v_pk_fma_f32 v[154:155], v[154:155], v[170:171], v[50:51]\n" \
"v_pk_fma_f32 v[156:157], v[156:157], v[172:173], v[56:57]\n" \
"v_pk_fma_f32 v[158:159], v[158:159], v[174:175], v[58:59]\n" \
"v_cvt_pk_bf16_f32 v152, v152, v153\n" \
"v_cvt_pk_bf16_f32 v154, v154, v155\n" \
"v_cvt_pk_bf16_f32 v156, v156, v157\n" \
"v_cvt_pk_bf16_f32 v158, v158, v159\n" \
"s_or_b64 %[lat], s[56:57], s[58:59]\n" \
"s_mov_b32 vcc_lo, 0x55555555\n" \
"s_mov_b32 vcc_hi, 0x55555555\n" \
"v_bfrev_b32_e32 v208, 1\n" \
"v_cndmask_b32_e64 v208, %[vd0], v208, %[lat]\n" \
"v_cndmask_b32_dpp v96, v136, v128, vcc quad_perm:[1,0,3,2] row_mask:0xf bank_mask:0xf\n" \
"v_cndmask_b32_dpp v97, v138, v130, vcc quad_perm:[1,0,3,2] row_mask:0xf bank_mask:0xf\n" \
"v_cndmask_b32_dpp v100, v140, v132, vcc quad_perm:[1,0,3,2] row_mask:0xf bank_mask:0xf\n" \
"v_cndmask_b32_dpp v101, v142, v134, vcc quad_perm:[1,0,3,2] row_mask:0xf bank_mask:0xf\n" \
"v_cndmask_b32_dpp v104, v152, v144, vcc quad_perm:[1,0,3,2] row_mask:0xf bank_mask:0xf\n" \
"v_cndmask_b32_dpp v105, v154, v146, vcc quad_perm:[1,0,3,2] row_mask:0xf bank_mask:0xf\n" \
"v_cndmask_b32_dpp v108, v156, v148, vcc quad_perm:[1,0,3,2] row_mask:0xf bank_mask:0xf\n" \
"v_cndmask_b32_dpp v109, v158, v150, vcc quad_perm:[1,0,3,2] row_mask:0xf bank_mask:0xf\n" \
"s_not_b64 vcc, vcc\n" \
"v_xor_b32_e32 v209, 0x80, v208\n" \
"v_cndmask_b32_dpp v98, v128, v136, vcc quad_perm:[1,0,3,2] row_mask:0xf bank_mask:0xf\n" \
"v_cndmask_b32_dpp v99, v130, v138, vcc quad_perm:[1,0,3,2] row_mask:0xf bank_mask:0xf\n" \
"v_cndmask_b32_dpp v102, v132, v140, vcc quad_perm:[1,0,3,2] row_mask:0xf bank_mask:0xf\n" \
"v_cndmask_b32_dpp v103, v134, v142, vcc quad_perm:[1,0,3,2] row_mask:0xf bank_mask:0xf\n" \
"v_cndmask_b32_dpp v106, v144, v152, vcc quad_perm:[1,0,3,2] row_mask:0xf bank_mask:0xf\n" \
"v_cndmask_b32_dpp v107, v146, v154, vcc quad_perm:[1,0,3,2] row_mask:0xf bank_mask:0xf\n" \
"v_cndmask_b32_dpp v110, v148, v156, vcc quad_perm:[1,0,3,2] row_mask:0xf bank_mask:0xf\n" \
"v_cndmask_b32_dpp v111, v150, v158, vcc quad_perm:[1,0,3,2] row_mask:0xf bank_mask:0xf\n" \
"buffer_store_dwordx4 v[96:99], v208, s[64:67], 0 offen offset:0 sc1\n" \
"buffer_store_dwordx4 v[100:103], v208, s[64:67], 0 offen offset:1024 sc1\n" \
"buffer_store_dwordx4 v[104:107], v209, s[64:67], 0 offen offset:0 sc1\n" \
"buffer_store_dwordx4 v[108:111], v209, s[64:67], 0 offen offset:1024 sc1\n" \
"TLls_%=:\n" \
"v_readlane_b32 s63, %[tlo], 28\n" \
"v_accvgpr_read_b32 v124, a32\n" \
"v_accvgpr_read_b32 v214, a33\n" \
"v_accvgpr_read_b32 v73, a34\n" \
"s_add_u32 s62, s40, 65536\n" \
"v_lshl_add_u32 v125, v125, 11, v214\n" \
"v_mov_b32_e32 v72, s40\n" \
"v_lshl_add_u32 v125, v125, 1, s62\n" \
"v_add_u32_e32 v214, s40, v214\n" \
"v_and_b32_e32 v74, 1, v222\n" \
"s_add_u32 s58, s63, 0x100000\n" \
"s_lshl_b32 s59, s41, 2\n" \
"s_sub_u32 s59, s59, %[wid]\n" \
"s_cmp_eq_u32 s59, 1\n" \
"s_cselect_b32 s59, 1, 0\n" \
"s_cbranch_scc0 TLkw_%=\n" \
"s_waitcnt vmcnt(4)\n" \
"TLk2_%=:\n" \
"v_min3_u32 v64, v112, v113, v114\n" \
"v_min3_u32 v65, v115, v116, v117\n" \
"v_min3_u32 v64, v64, v118, v119\n" \
"v_min_u32_e32 v64, v64, v65\n" \
"v_cmp_eq_u32_e32 vcc, 0, v64\n" \
"s_cbranch_vccnz TLk1_%=\n" \
"TLkw_%=:\n" \
"s_waitcnt lgkmcnt(0)\n" \
"s_barrier\n" \
"buffer_load_dwordx4 v[128:131], v73, %[srdz], s63 offen offset:0 sc1\n" \
"buffer_load_dwordx4 v[132:135], v73, %[srdz], s58 offen offset:0 sc1\n" \
"ds_read_b32 v120, v125 offset:0\n" \
"ds_read_u16 v121, v214 offset:2048\n" \
"ds_read_u16 v212, v214 offset:0\n" \
"ds_read_u16 v123, v214 offset:4096\n" \
"ds_read_b128 v[68:71], v72 offset:14384\n" \
"s_cmp_eq_u32 s59, 0\n" \
"s_cbranch_scc0 TLg1_%=\n" \
"s_waitcnt lgkmcnt(0)\n" \
"v_lshlrev_b32_e32 v121, 16, v121\n" \
"v_lshlrev_b32_e32 v212, 16, v212\n" \
"v_lshlrev_b32_e32 v123, 16, v123\n" \
"v_sub_f32_e32 v213, v120, v4\n" \
"v_mul_f32_e32 v213, v213, v126\n" \
"v_fma_f32 v127, v213, v121, v212\n" \
"v_add_f32_e32 v120, v120, v191\n" \
"v_add_f32_e32 v64, v68, v69\n" \
"v_add_f32_e32 v65, v70, v71\n" \
"v_add_f32_e32 v122, v64, v65\n" \
"v_add_f32_e32 v122, v122, v20\n" \
"v_cvt_pk_bf16_f32 v122, 0, v122\n" \
"v_mul_f32_e32 v122, 0xbfb8aa3b, v122\n" \
"v_exp_f32_e32 v122, v122\n" \
"s_nop 0\n" \
"v_add_f32_e32 v122, 1.0, v122\n" \
"v_rcp_f32_e32 v122, v122\n" \
"s_nop 0\n" \
"v_cvt_pk_bf16_f32 v122, 0, v122\n" \
"TLg2_%=:\n" \
"s_lshl_b32 s62, %[wid], 5\n" \
"v_lshrrev_b32_e32 v66, 2, v222\n" \
"s_add_u32 s62, s62, s40\n" \
"v_and_b32_e32 v66, 12, v66\n" \
"v_lshl_or_b32 v66, v74, 4, v66\n" \
"v_add_u32_e32 v66, s62, v66\n" \
"TLg3_%=:\n" \
"s_waitcnt vmcnt(0)\n" \
"v_bitop3_b32 v65, v132, v133, v134 bitop3:0x80\n" \
"v_bitop3_b32 v67, v128, v129, v130 bitop3:0x80\n" \
"v_bitop3_b32 v65, v65, v135, v131 bitop3:0x80\n" \
"v_pk_add_f32 v[128:129], v[128:129], v[132:133]\n" \
"v_bitop3_b32 v65, v65, v67, s41 bitop3:0x80\n" \
"v_pk_add_f32 v[130:131], v[130:131], v[134:135]\n" \
"v_cmp_eq_u32_e32 vcc, 0, v65\n" \
"s_nop 0\n" \
"v_permlane32_swap_b32_e32 v128, v130\n" \
"v_permlane32_swap_b32_e32 v129, v131\n" \
"v_add_f32_e32 v128, v128, v130\n" \
"v_add_f32_e32 v129, v129, v131\n" \
"s_cbranch_vccnz TLga_%=\n" \
"TLgb_%=:\n" \
"s_nop 0\n" \
"v_permlane16_swap_b32_e32 v128, v129\n" \
"v_add_f32_e32 v128, v128, v129\n" \
"s_nop 1\n" \
"v_add_f32_dpp v128, v128, v128 row_ror:8 row_mask:0xf bank_mask:0xf\n" \
"s_nop 1\n" \
"v_add_f32_dpp v128, v128, v128 row_ror:4 row_mask:0xf bank_mask:0xf\n" \
"s_nop 1\n" \
"v_add_f32_dpp v128, v128, v128 quad_perm:[2,3,0,1] row_mask:0xf bank_mask:0xf\n" \
"ds_write_b32 v66, v128 offset:29696\n" \
"v_and_b32_e32 v65, 7, v222\n" \
"v_mov_b32_e32 v74, s60\n" \
"v_lshl_add_u32 v65, v65, 2, s40\n" \
"v_mov_b32_e32 v75, s61\n" \
"s_mov_b32 vcc_lo, 0xaaaaaaaa\n" \
"s_min_u32 s56, s60, s61\n" \
"v_cndmask_b32_e32 v74, v74, v75, vcc\n" \
"s_max_u32 s57, s60, s61\n" \
"v_lshl_or_b32 v74, v74, 10, v124\n" \
"v_lshlrev_b32_e32 v74, 1, v74\n" \
"s_waitcnt lgkmcnt(0)\n" \
"s_barrier\n" \
"ds_read_b64 v[66:67], v72 offset:14432\n" \
"ds_read_b32 v68, v65 offset:29696\n" \
"ds_read_b32 v69, v65 offset:29728\n" \
"ds_read_b32 v70, v65 offset:29760\n" \
"ds_read_b32 v71, v65 offset:29792\n" \
"s_waitcnt lgkmcnt(4)\n" \
"v_readfirstlane_b32 %[k2], v66\n" \
"v_readfirstlane_b32 %[k1], v67\n" \
"s_waitcnt lgkmcnt(0)\n" \
"v_add_f32_e32 v68, v68, v69\n" \
"v_add_f32_e32 v70, v70, v71\n" \
"s_andn2_b32 %[k1], 0x7ff, %[k1]\n" \
"s_andn2_b32 %[k2], 0x7ff, %[k2]\n" \
"v_add_f32_e32 v68, v68, v70\n" \
"s_min_u32 s62, %[k1], %[k2]\n" \
"s_max_u32 s63, %[k1], %[k2]\n" \
"v_fma_f32 v68, v68, v122, v127\n" \
"s_cmp_lg_u64 s[62:63], s[56:57]\n" \
"s_cselect_b32 %[got], 2, 0\n" \
"v_fma_f32 v68, v68, v123, v120\n" \
"s_or_b32 %[got], %[got], s53\n" \
"s_cbranch_scc1 TLagain_%=\n" \
"TLfin_%=:\n" \
"s_lshr_b32 %[got], s41, 1\n" \
"s_cmp_lg_u32 %[wid], 0\n" \
"s_cselect_b32 s58, 0x80000000, 0\n" \
"v_cvt_pk_bf16_f32 v68, v68, v68\n" \
"s_mov_b64 exec, 0xff\n" \
"buffer_store_short v68, v74, s[64:67], s58 offen offset:0 sc1\n" \
"s_mov_b64 exec, -1\n" \
"s_branch Lend_%=\n" \
"TLpk_%=:\n" \
"v_readlane_b32 s62, %[tlo], 35\n" \
"v_lshrrev_b32_e32 v72, 2, v224\n" \
"s_mov_b32 s51, 0x10000\n" \
"s_nop 4\n" \
"TLpk1_%=:\n" \
"buffer_load_dwordx4 v[64:67], v72, %[srdz], s62 offen offset:0 sc1\n" \
"buffer_load_dwordx4 v[68:71], v72, %[srdz], s62 offen offset:16 sc1\n" \
"s_waitcnt vmcnt(0)\n" \
"v_min3_u32 v208, v64, v65, v66\n" \
"v_min3_u32 v209, v67, v68, v69\n" \
"v_min3_u32 v208, v208, v70, v71\n" \
"v_min_u32_e32 v208, v208, v209\n" \
"v_cmp_eq_u32_e32 vcc, 0, v208\n" \
"s_sub_u32 s51, s51, 1\n" \
"s_cbranch_scc1 TLpkb_%=\n" \
"s_cbranch_vccz TLpkb_%=\n" \
"s_sleep 8\n" \
"s_branch TLpk1_%=\n" \
"TLrare_%=:\n" \
"v_cmp_u_f32_e32 vcc, v14, v15\n" \
"s_cbranch_vccz TLvar_%=\n" \
"s_sub_u32 s52, s52, 1\n" \
"s_cbranch_scc1 TLvar_%=\n" \
"buffer_load_dwordx4 a[164:167], v217, %[srdz], s54 offen offset:0 sc1\n" \
"buffer_load_dwordx4 a[168:171], v217, %[srdz], s54 offen offset:1024 sc1\n" \
"buffer_load_dwordx4 a[172:175], v217, %[srdz], s54 offen offset:2048 sc1\n" \
"buffer_load_dwordx4 a[176:179], v217, %[srdz], s54 offen offset:3072 sc1\n" \
"buffer_load_dwordx4 a[180:183], v217, %[srdz], s55 offen offset:0 sc1\n" \
"buffer_load_dwordx4 a[184:187], v217, %[srdz], s55 offen offset:1024 sc1\n" \
"buffer_load_dwordx4 a[188:191], v217, %[srdz], s55 offen offset:2048 sc1\n" \
"buffer_load_dwordx4 a[192:195], v217, %[srdz], s55 offen offset:3072 sc1\n" \
"v_accvgpr_read_b32 v33, a248\n" \
"v_lshlrev_b32_e32 v32, 16, v33\n" \
"v_and_b32_e32 v33, s43, v33\n" \
".if . - adaln_body != 69632\n" \
".error \"size model\"\n" \
".endif\n" \
"Lk16_%=:\n" \
"s_cbranch_execz adaln_kr\n" \
"s_nop 0\n" \
"v_accvgpr_read_b32 v35, a249\n" \
"v_lshlrev_b32_e32 v34, 16, v35\n" \
"v_and_b32_e32 v35, s43, v35\n" \
"v_accvgpr_read_b32 v41, a252\n" \
"v_lshlrev_b32_e32 v40, 16, v41\n" \
"v_and_b32_e32 v41, s43, v41\n" \
"v_accvgpr_read_b32 v43, a253\n" \
"v_lshlrev_b32_e32 v42, 16, v43\n" \
"v_and_b32_e32 v43, s43, v43\n" \
"v_accvgpr_read_b32 v49, a250\n" \
"v_lshlrev_b32_e32 v48, 16, v49\n" \
"v_and_b32_e32 v49, s43, v49\n" \
"v_accvgpr_read_b32 v51, a251\n" \
"v_lshlrev_b32_e32 v50, 16, v51\n" \
"v_and_b32_e32 v51, s43, v51\n" \
"v_accvgpr_read_b32 v57, a254\n" \
"v_lshlrev_b32_e32 v56, 16, v57\n" \
"v_and_b32_e32 v57, s43, v57\n" \
"v_accvgpr_read_b32 v59, a255\n" \
"v_lshlrev_b32_e32 v58, 16, v59\n" \
"v_and_b32_e32 v59, s43, v59\n" \
"v_pk_mul_f32 v[6:7], v[0:1], v[160:161]\n" \
"v_pk_mul_f32 v[12:13], v[0:1], v[0:1]\n" \
"v_pk_add_f32 v[4:5], v[0:1], v[2:3]\n" \
"v_pk_fma_f32 v[6:7], v[2:3], v[162:163], v[6:7]\n" \
"v_pk_fma_f32 v[12:13], v[2:3], v[2:3], v[12:13]\n" \
"v_pk_add_f32 v[4:5], v[4:5], v[8:9]\n" \
"v_pk_fma_f32 v[6:7], v[8:9], v[168:169], v[6:7]\n" \
"v_pk_fma_f32 v[12:13], v[8:9], v[8:9], v[12:13]\n" \
"v_pk_add_f32 v[4:5], v[4:5], v[10:11]\n" \
"v_pk_fma_f32 v[6:7], v[10:11], v[170:171], v[6:7]\n" \
"v_pk_fma_f32 v[12:13], v[10:11], v[10:11], v[12:13]\n" \
"v_pk_add_f32 v[4:5], v[4:5], v[16:17]\n" \
"v_pk_fma_f32 v[6:7], v[16:17], v[164:165], v[6:7]\n" \
"v_pk_fma_f32 v[12:13], v[16:17], v[16:17], v[12:13]\n" \
"v_pk_add_f32 v[4:5], v[4:5], v[18:19]\n" \
"v_pk_fma_f32 v[6:7], v[18:19], v[166:167], v[6:7]\n" \
"v_pk_fma_f32 v[12:13], v[18:19], v[18:19], v[12:13]\n" \
"v_pk_add_f32 v[4:5], v[4:5], v[24:25]\n" \
"v_pk_fma_f32 v[6:7], v[24:25], v[172:173], v[6:7]\n" \
"v_pk_fma_f32 v[12:13], v[24:25], v[24:25], v[12:13]\n" \
"v_pk_add_f32 v[4:5], v[4:5], v[26:27]\n" \
"v_pk_fma_f32 v[6:7], v[26:27], v[174:175], v[6:7]\n" \
"v_pk_fma_f32 v[12:13], v[26:27], v[26:27], v[12:13]\n" \
"s_waitcnt vmcnt(0)\n" \
"s_nop 1\n" \
"v_mfma_f32_16x16x32_bf16 v[208:211], v[230:233], a[164:167], 0\n" \
"v_mfma_f32_16x16x32_bf16 v[212:215], v[230:233], a[172:175], 0\n" \
"v_mfma_f32_16x16x32_bf16 v[208:211], v[234:237], a[168:171], v[208:211]\n" \
"v_mfma_f32_16x16x32_bf16 v[212:215], v[234:237], a[176:179], v[212:215]\n" \
"s_nop 6\n" \
"v_pk_fma_f32 v[32:33], v[208:209], v[144:145], v[32:33]\n" \
"v_pk_fma_f32 v[34:35], v[210:211], v[146:147], v[34:35]\n" \
"v_pk_fma_f32 v[40:41], v[212:213], v[152:153], v[40:41]\n" \
"v_pk_fma_f32 v[42:43], v[214:215], v[154:155], v[42:43]\n" \
"v_pk_mul_f32 v[36:37], v[32:33], v[176:177]\n" \
"v_pk_mul_f32 v[38:39], v[32:33], v[32:33]\n" \
"v_pk_add_f32 v[14:15], v[32:33], v[34:35]\n" \
"v_pk_fma_f32 v[36:37], v[34:35], v[178:179], v[36:37]\n" \
"v_pk_fma_f32 v[38:39], v[34:35], v[34:35], v[38:39]\n" \
"v_pk_add_f32 v[14:15], v[14:15], v[40:41]\n" \
"v_pk_fma_f32 v[36:37], v[40:41], v[184:185], v[36:37]\n" \
"v_pk_fma_f32 v[38:39], v[40:41], v[40:41], v[38:39]\n" \
"v_pk_add_f32 v[14:15], v[14:15], v[42:43]\n" \
"v_pk_fma_f32 v[36:37], v[42:43], v[186:187], v[36:37]\n" \
"v_pk_fma_f32 v[38:39], v[42:43], v[42:43], v[38:39]\n" \
"s_nop 7\n" \
"v_mfma_f32_16x16x32_bf16 v[208:211], v[230:233], a[180:183], 0\n" \
"v_mfma_f32_16x16x32_bf16 v[212:215], v[230:233], a[188:191], 0\n" \
"v_mfma_f32_16x16x32_bf16 v[208:211], v[234:237], a[184:187], v[208:211]\n" \
"v_mfma_f32_16x16x32_bf16 v[212:215], v[234:237], a[192:195], v[212:215]\n" \
"s_nop 6\n" \
"v_pk_fma_f32 v[48:49], v[208:209], v[148:149], v[48:49]\n" \
"v_pk_fma_f32 v[50:51], v[210:211], v[150:151], v[50:51]\n" \
"v_pk_fma_f32 v[56:57], v[212:213], v[156:157], v[56:57]\n" \
"v_pk_fma_f32 v[58:59], v[214:215], v[158:159], v[58:59]\n" \
"v_pk_add_f32 v[14:15], v[14:15], v[48:49]\n" \
"v_pk_fma_f32 v[36:37], v[48:49], v[180:181], v[36:37]\n" \
"v_pk_fma_f32 v[38:39], v[48:49], v[48:49], v[38:39]\n" \
"v_pk_add_f32 v[14:15], v[14:15], v[50:51]\n" \
"v_pk_fma_f32 v[36:37], v[50:51], v[182:183], v[36:37]\n" \
"v_pk_fma_f32 v[38:39], v[50:51], v[50:51], v[38:39]\n" \
"v_pk_add_f32 v[14:15], v[14:15], v[56:57]\n" \
"v_pk_fma_f32 v[36:37], v[56:57], v[188:189], v[36:37]\n" \
"v_pk_fma_f32 v[38:39], v[56:57], v[56:57], v[38:39]\n" \
"v_pk_add_f32 v[14:15], v[14:15], v[58:59]\n" \
"v_pk_fma_f32 v[36:37], v[58:59], v[190:191], v[36:37]\n" \
"v_pk_fma_f32 v[38:39], v[58:59], v[58:59], v[38:39]\n" \
"v_pk_add_f32 v[4:5], v[4:5], v[14:15]\n" \
"v_pk_add_f32 v[6:7], v[6:7], v[36:37]\n" \
"v_pk_add_f32 v[12:13], v[12:13], v[38:39]\n" \
"v_add_f32_e32 v4, v4, v5\n" \
"v_add_f32_e32 v6, v6, v7\n" \
"v_add_f32_e32 v12, v12, v13\n" \
"v_mov_b32_e32 v208, v12\n" \
"v_permlane32_swap_b32_e32 v4, v6\n" \
"s_nop 0\n" \
"v_permlane32_swap_b32_e32 v12, v208\n" \
"v_add_f32_e32 v4, v4, v6\n" \
"v_add_f32_e32 v12, v12, v208\n" \
"s_nop 0\n" \
"s_nop 0\n" \
"v_permlane16_swap_b32_e32 v4, v12\n" \
"v_add_f32_e32 v4, v4, v12\n" \
"s_nop 1\n" \
"v_add_f32_dpp v4, v4, v4 quad_perm:[1,0,3,2] row_mask:0xf bank_mask:0xf\n" \
"s_nop 1\n" \
"v_add_f32_dpp v4, v4, v4 quad_perm:[2,3,0,1] row_mask:0xf bank_mask:0xf\n" \
"s_nop 1\n" \
"v_add_f32_dpp v4, v4, v4 row_ror:8 row_mask:0xf bank_mask:0xf\n" \
"v_mov_b32_e32 v12, v4\n" \
"s_nop 0\n" \
"s_nop 0\n" \
"v_permlane16_swap_b32_e32 v4, v12\n" \
"v_mov_b32_e32 v6, v4\n" \
"v_mul_f32_e32 v12, 0x3a800000, v12\n" \
"s_nop 0\n" \
"v_permlane32_swap_b32_e32 v4, v6\n" \
"v_mul_f32_e32 v218, 0x3a800000, v4\n" \
"v_mul_f32_e32 v208, v218, v218\n" \
"v_sub_f32_e32 v12, v12, v208\n" \
"v_cmp_nle_f32_e32 vcc, v208, v12\n" \
"v_add_f32_e32 v12, 0x3727c5ac, v12\n" \
"v_rsq_f32_e32 v220, v12\n" \
"v_fma_f32 v210, -v218, v223, v6\n" \
"s_mov_b64 exec, 17\n" \
"v_mul_f32_e32 v210, v210, v220\n" \
"v_max_f32_e32 v210, 0xff800000, v210\n" \
"v_ashrrev_i32_e32 v208, 31, v210\n" \
"v_or_b32_e32 v208, 0x80000000, v208\n" \
"v_xor_b32_e32 v210, v210, v208\n" \
"v_and_b32_e32 v210, 0xfffff800, v210\n" \
"v_or_b32_e32 v210, v210, %[ta0]\n" \
"ds_write_b32 v216, v210 offset:14400\n" \
"s_mov_b64 exec, -1\n" \
"s_cbranch_vccnz TLrare_%=\n" \
"s_branch TLrok_%=\n" \
"TLvar_%=:\n" \
"v_sub_f32_e32 v210, 0, v218\n" \
"v_pk_add_f32 v[212:213], v[0:1], v[210:211] op_sel_hi:[1,0]\n" \
"v_pk_mul_f32 v[214:215], v[212:213], v[212:213]\n" \
"v_pk_add_f32 v[212:213], v[2:3], v[210:211] op_sel_hi:[1,0]\n" \
"v_pk_fma_f32 v[214:215], v[212:213], v[212:213], v[214:215]\n" \
"v_pk_add_f32 v[212:213], v[8:9], v[210:211] op_sel_hi:[1,0]\n" \
"v_pk_fma_f32 v[214:215], v[212:213], v[212:213], v[214:215]\n" \
"v_pk_add_f32 v[212:213], v[10:11], v[210:211] op_sel_hi:[1,0]\n" \
"v_pk_fma_f32 v[214:215], v[212:213], v[212:213], v[214:215]\n" \
"v_pk_add_f32 v[212:213], v[16:17], v[210:211] op_sel_hi:[1,0]\n" \
"v_pk_fma_f32 v[214:215], v[212:213], v[212:213], v[214:215]\n" \
"v_pk_add_f32 v[212:213], v[18:19], v[210:211] op_sel_hi:[1,0]\n" \
"v_pk_fma_f32 v[214:215], v[212:213], v[212:213], v[214:215]\n" \
"v_pk_add_f32 v[212:213], v[24:25], v[210:211] op_sel_hi:[1,0]\n" \
"v_pk_fma_f32 v[214:215], v[212:213], v[212:213], v[214:215]\n" \
"v_pk_add_f32 v[212:213], v[26:27], v[210:211] op_sel_hi:[1,0]\n" \
"v_pk_fma_f32 v[214:215], v[212:213], v[212:213], v[214:215]\n" \
"v_pk_add_f32 v[212:213], v[32:33], v[210:211] op_sel_hi:[1,0]\n" \
"v_pk_fma_f32 v[214:215], v[212:213], v[212:213], v[214:215]\n" \
"v_pk_add_f32 v[212:213], v[34:35], v[210:211] op_sel_hi:[1,0]\n" \
"v_pk_fma_f32 v[214:215], v[212:213], v[212:213], v[214:215]\n" \
"v_pk_add_f32 v[212:213], v[40:41], v[210:211] op_sel_hi:[1,0]\n" \
"v_pk_fma_f32 v[214:215], v[212:213], v[212:213], v[214:215]\n" \
"v_pk_add_f32 v[212:213], v[42:43], v[210:211] op_sel_hi:[1,0]\n" \
"v_pk_fma_f32 v[214:215], v[212:213], v[212:213], v[214:215]\n" \
"v_pk_add_f32 v[212:213], v[48:49], v[210:211] op_sel_hi:[1,0]\n" \
"v_pk_fma_f32 v[214:215], v[212:213], v[212:213], v[214:215]\n" \
"v_pk_add_f32 v[212:213], v[50:51], v[210:211] op_sel_hi:[1,0]\n" \
"v_pk_fma_f32 v[214:215], v[212:213], v[212:213], v[214:215]\n" \
"v_pk_add_f32 v[212:213], v[56:57], v[210:211] op_sel_hi:[1,0]\n" \
"v_pk_fma_f32 v[214:215], v[212:213], v[212:213], v[214:215]\n" \
"v_pk_add_f32 v[212:213], v[58:59], v[210:211] op_sel_hi:[1,0]\n" \
"v_pk_fma_f32 v[214:215], v[212:213], v[212:213], v[214:215]\n" \
"v_add_f32_e32 v12, v214, v215\n" \
"s_nop 1\n" \
"v_add_f32_dpp v12, v12, v12 quad_perm:[1,0,3,2] row_mask:0xf bank_mask:0xf\n" \
"s_nop 1\n" \
"v_add_f32_dpp v12, v12, v12 quad_perm:[2,3,0,1] row_mask:0xf bank_mask:0xf\n" \
"s_nop 1\n" \
"v_add_f32_dpp v12, v12, v12 row_ror:8 row_mask:0xf bank_mask:0xf\n" \
"v_mov_b32_e32 v210, v12\n" \
"s_nop 0\n" \
"s_nop 0\n" \
"v_permlane16_swap_b32_e32 v12, v210\n" \
"v_add_f32_e32 v12, v12, v210\n" \
"v_mov_b32_e32 v210, v12\n" \
"s_nop 0\n" \
"s_nop 0\n" \
"v_permlane32_swap_b32_e32 v12, v210\n" \
"v_add_f32_e32 v12, v12, v210\n" \
"v_mul_f32_e32 v12, 0x3a800000, v12\n" \
"v_add_f32_e32 v12, 0x3727c5ac, v12\n" \
"v_rsq_f32_e32 v220, v12\n" \
"v_fma_f32 v210, -v218, v223, v6\n" \
"s_mov_b64 exec, 17\n" \
"v_mul_f32_e32 v210, v210, v220\n" \
"v_max_f32_e32 v210, 0xff800000, v210\n" \
"v_ashrrev_i32_e32 v208, 31, v210\n" \
"v_or_b32_e32 v208, 0x80000000, v208\n" \
"v_xor_b32_e32 v210, v210, v208\n" \
"v_and_b32_e32 v210, 0xfffff800, v210\n" \
"v_or_b32_e32 v210, v210, %[ta0]\n" \
"ds_write_b32 v216, v210 offset:14400\n" \
"s_mov_b64 exec, -1\n" \
"s_branch TLrok_%=\n" \
"TLcw_%=:\n" \
"s_waitcnt lgkmcnt(0)\n" \
"s_mov_b32 s63, 278528\n" \
"s_mov_b32 s51, 0x10000\n" \
"TLcw1_%=:\n" \
"buffer_load_dword %[vxs], %[vdn], %[srdz], s63 offen offset:0 sc1\n" \
"s_waitcnt vmcnt(0)\n" \
"v_and_b32_e32 v44, 0x820, %[vxs]\n" \
"v_cmp_ne_u32_e32 vcc, 0x820, v44\n" \
"s_sub_u32 s51, s51, 1\n" \
"s_cbranch_scc1 TLcw2_%=\n" \
"s_nop 3\n" \
"s_cbranch_vccz TLcw2_%=\n" \
"s_sleep 8\n" \
"s_branch TLcw1_%=\n" \
"TLcw2_%=:\n" \
"s_sleep 4\n" \
"s_mov_b32 s59, 1\n" \
"buffer_load_dwordx4 a[128:131], %[vct], %[srdz], 0 offen offset:0 sc1\n" \
"buffer_load_dwordx4 a[132:135], %[rd0], %[srdz], 0 offen offset:0 sc1\n" \
"buffer_load_dwordx4 a[136:139], %[vct], %[srdz], s56 offen offset:0 sc1\n" \
"buffer_load_dwordx4 a[140:143], %[rd0], %[srdz], s56 offen offset:0 sc1\n" \
"buffer_load_dwordx4 a[144:147], %[vct], %[srdz], s57 offen offset:0 sc1\n" \
"buffer_load_dwordx4 a[148:151], %[rd0], %[srdz], s57 offen offset:0 sc1\n" \
"buffer_load_dwordx4 a[152:155], %[vct], %[srdz], s58 offen offset:0 sc1\n" \
"buffer_load_dwordx4 a[156:159], %[rd0], %[srdz], s58 offen offset:0 sc1\n" \
"s_waitcnt vmcnt(0)\n" \
"s_branch TLcs_%=\n" \
"TLcx_%=:\n" \
"s_cmp_eq_u32 s59, 0\n" \
"s_cbranch_scc1 TLcw_%=\n" \
"s_sub_u32 s52, s52, 1\n" \
"s_cbranch_scc1 TLco_%=\n" \
"buffer_load_dwordx4 a[128:131], %[vct], %[srdz], 0 offen offset:0 sc1\n" \
"buffer_load_dwordx4 a[132:135], %[rd0], %[srdz], 0 offen offset:0 sc1\n" \
"buffer_load_dwordx4 a[136:139], %[vct], %[srdz], s56 offen offset:0 sc1\n" \
"buffer_load_dwordx4 a[140:143], %[rd0], %[srdz], s56 offen offset:0 sc1\n" \
"buffer_load_dwordx4 a[144:147], %[vct], %[srdz], s57 offen offset:0 sc1\n" \
"buffer_load_dwordx4 a[148:151], %[rd0], %[srdz], s57 offen offset:0 sc1\n" \
"buffer_load_dwordx4 a[152:155], %[vct], %[srdz], s58 offen offset:0 sc1\n" \
"buffer_load_dwordx4 a[156:159], %[rd0], %[srdz], s58 offen offset:0 sc1\n" \
"s_waitcnt vmcnt(0) lgkmcnt(0)\n" \
"s_branch TLcs_%=\n" \
"TLk1_%=:\n" \
"s_sub_u32 s51, s51, 1\n" \
"s_cbranch_scc1 TLkw_%=\n" \
"s_sleep 3\n" \
"buffer_load_dwordx4 v[112:115], v216, %[srdz], s49 offen offset:0 sc1\n" \
"buffer_load_dwordx4 v[116:119], v216, %[srdz], s49 offen offset:16 sc1\n" \
"s_waitcnt vmcnt(0)\n" \
"s_branch TLk2_%=\n" \
"TLg1_%=:\n" \
"s_waitcnt lgkmcnt(0)\n" \
"v_min_u32_e32 v64, v113, v115\n" \
"v_min_u32_e32 v65, v117, v119\n" \
"v_max_u32_e32 v113, v113, v115\n" \
"v_max_u32_e32 v117, v117, v119\n" \
"v_max3_u32 v112, v64, v112, v114\n" \
"v_max3_u32 v116, v65, v116, v118\n" \
"v_min_u32_e32 v64, v113, v117\n" \
"v_max_u32_e32 v113, v113, v117\n" \
"v_max3_u32 v112, v64, v112, v116\n" \
"s_nop 0\n" \
"v_min_u32_dpp v64, v113, v113 quad_perm:[1,0,3,2] row_mask:0xf bank_mask:0xf\n" \
"v_max_u32_dpp v65, v112, v112 quad_perm:[1,0,3,2] row_mask:0xf bank_mask:0xf\n" \
"v_max_u32_dpp v113, v113, v113 quad_perm:[1,0,3,2] row_mask:0xf bank_mask:0xf\n" \
"v_max_u32_e32 v112, v64, v65\n" \
"s_nop 0\n" \
"v_min_u32_dpp v64, v113, v113 quad_perm:[2,3,0,1] row_mask:0xf bank_mask:0xf\n" \
"v_max_u32_dpp v65, v112, v112 quad_perm:[2,3,0,1] row_mask:0xf bank_mask:0xf\n" \
"v_max_u32_dpp v113, v113, v113 quad_perm:[2,3,0,1] row_mask:0xf bank_mask:0xf\n" \
"v_max_u32_e32 v112, v64, v65\n" \
"s_nop 0\n" \
"v_min_u32_dpp v64, v113, v113 row_half_mirror row_mask:0xf bank_mask:0xf\n" \
"v_max_u32_dpp v65, v112, v112 row_half_mirror row_mask:0xf bank_mask:0xf\n" \
"v_max_u32_dpp v113, v113, v113 row_half_mirror row_mask:0xf bank_mask:0xf\n" \
"v_max_u32_e32 v112, v64, v65\n" \
"s_nop 0\n" \
"v_min_u32_dpp v64, v113, v113 row_mirror row_mask:0xf bank_mask:0xf\n" \
"v_max_u32_dpp v65, v112, v112 row_mirror row_mask:0xf bank_mask:0xf\n" \
"v_max_u32_dpp v113, v113, v113 row_mirror row_mask:0xf bank_mask:0xf\n" \
"v_max_u32_e32 v112, v64, v65\n" \
"v_mov_b32_e32 v115, v113\n" \
"v_mov_b32_e32 v114, v112\n" \
"s_nop 0\n" \
"v_permlane16_swap_b32_e32 v113, v115\n" \
"v_permlane16_swap_b32_e32 v112, v114\n" \
"v_min_u32_e32 v64, v113, v115\n" \
"v_max_u32_e32 v113, v113, v115\n" \
"v_max3_u32 v112, v64, v112, v114\n" \
"v_mov_b32_e32 v115, v113\n" \
"v_mov_b32_e32 v114, v112\n" \
"s_nop 0\n" \
"v_permlane32_swap_b32_e32 v113, v115\n" \
"v_permlane32_swap_b32_e32 v112, v114\n" \
"v_min_u32_e32 v64, v113, v115\n" \
"v_max_u32_e32 v113, v113, v115\n" \
"v_max3_u32 v112, v64, v112, v114\n" \
"s_mov_b64 exec, 1\n" \
"ds_write_b64 v72, v[112:113] offset:14432\n" \
"s_mov_b64 exec, -1\n" \
"s_branch TLg2_%=\n" \
"TLga_%=:\n" \
"s_sub_u32 s51, s51, 1\n" \
"s_cbranch_scc1 TLgb_%=\n" \
"buffer_load_dwordx4 v[128:131], v73, %[srdz], s63 offen offset:0 sc1\n" \
"buffer_load_dwordx4 v[132:135], v73, %[srdz], s58 offen offset:0 sc1\n" \
"s_branch TLg3_%=\n" \
"TLagain_%=:\n" \
"s_cmp_eq_u32 s41, 4\n" \
"s_cbranch_scc1 TLfin_%=\n" \
"s_lshl_b32 s41, s41, 1\n" \
"v_add_f32_e32 v191, v191, v4\n" \
"v_lshl_or_b32 v227, v227, 1, 1\n" \
"v_readlane_b32 s62, v191, 0\n" \
"v_readlane_b32 s63, v191, 1\n" \
"s_bitcmp1_b32 %[got], 1\n" \
"s_cselect_b32 s44, %[k1], s60\n" \
"s_cselect_b32 s45, %[k2], s61\n" \
"s_cselect_b32 s62, 0, s62\n" \
"s_cselect_b32 s63, 0, s63\n" \
"s_mov_b32 s46, s44\n" \
"s_mov_b32 s47, s45\n" \
"v_mov_b32_e32 v208, s62\n" \
"v_mov_b32_e32 v209, s63\n" \
"s_mov_b32 vcc_lo, 0xaaaaaaaa\n" \
"s_mov_b32 vcc_hi, 0xaaaaaaaa\n" \
"v_cndmask_b32_e32 v191, v208, v209, vcc\n" \
"s_mov_b32 vcc_lo, 0xf0f0f0f0\n" \
"s_mov_b32 vcc_hi, 0xf0f0f0f0\n" \
"v_cndmask_b32_e32 v190, v208, v209, vcc\n" \
"s_mov_b32 vcc_lo, 0xff00ff\n" \
"s_mov_b32 vcc_hi, 0xff00ff\n" \
"v_cndmask_b32_e64 v225, 0, -1, vcc\n" \
"v_sub_u32_e32 v216, 0x7ff, %[ta0]\n" \
"v_cmp_ne_u32_e64 s[56:57], s44, v216\n" \
"v_cmp_ne_u32_e64 s[58:59], s45, v216\n" \
"s_and_b64 s[56:57], s[56:57], s[58:59]\n" \
"s_and_b64 s[56:57], s[56:57], %[lat]\n" \
"s_mov_b64 %[lat], 0\n" \
"v_bfrev_b32_e32 v208, 1\n" \
"v_cndmask_b32_e64 v208, v208, %[vd0], s[56:57]\n" \
"v_xor_b32_e32 v209, 0x80, v208\n" \
"buffer_store_dwordx4 v[96:99], v208, s[64:67], 0 offen offset:0 sc1\n" \
"buffer_store_dwordx4 v[100:103], v208, s[64:67], 0 offen offset:1024 sc1\n" \
"buffer_store_dwordx4 v[104:107], v209, s[64:67], 0 offen offset:0 sc1\n" \
"buffer_store_dwordx4 v[108:111], v209, s[64:67], 0 offen offset:1024 sc1\n" \
"s_movk_i32 s56, 0x1000\n" \
"s_movk_i32 s57, 0x2000\n" \
"s_movk_i32 s58, 0x3000\n" \
"s_movk_i32 s52, 0x1000\n" \
"s_mov_b32 s59, 1\n" \
"s_nop 1\n" \
"v_and_b32_e32 v213, 4, v222\n" \
"v_mov_b32_e32 v208, s44\n" \
"v_cmp_ne_u32_e32 vcc, 0, v213\n" \
"v_mov_b32_e32 v214, s45\n" \
"v_mov_b32_e32 v209, s46\n" \
"v_mov_b32_e32 v215, s47\n" \
"v_cndmask_b32_e32 v208, v208, v214, vcc\n" \
"v_cndmask_b32_e32 v209, v209, v215, vcc\n" \
"v_cmp_lt_u32_e32 vcc, 31, v222\n" \
"v_and_b32_e32 v211, 0xf0, %[kb0]\n" \
"s_lshl_b32 s62, %[wid], 9\n" \
"v_cndmask_b32_e32 v210, v208, v209, vcc\n" \
"v_lshrrev_b32_e32 %[vdn], 6, v210\n" \
"v_lshlrev_b32_e32 %[vdn], 9, %[vdn]\n" \
"v_lshlrev_b32_e32 v213, 11, v210\n" \
"v_lshrrev_b32_e32 v214, 1, v210\n" \
"v_and_b32_e32 v214, 0x80, v214\n" \
"v_lshrrev_b32_e32 v215, 1, v211\n" \
"v_and_b32_e32 v215, 56, v215\n" \
"v_add3_u32 v213, v213, v214, v215\n" \
"v_and_b32_e32 v215, 0x80, v211\n" \
"v_lshlrev_b32_e32 v215, 1, v215\n" \
"v_add3_u32 v80, v213, v215, s62\n" \
"v_xor_b32_e32 v81, 0x80, v80\n" \
"s_mov_b32 s63, 278528\n" \
"buffer_load_dword %[vxs], %[vdn], %[srdz], s63 offen offset:0 sc1\n" \
"global_load_dwordx2 v[88:89], v80, %[px] offset:0\n" \
"global_load_dwordx2 v[90:91], v80, %[px] offset:64\n" \
"global_load_dwordx2 v[92:93], v81, %[px] offset:0\n" \
"global_load_dwordx2 v[94:95], v81, %[px] offset:64\n" \
"v_and_b32_e32 v213, 0x100, v210\n" \
"s_add_u32 s63, s62, s40\n" \
"v_add3_u32 v213, v213, v211, s63\n" \
"v_xor_b32_e32 v214, 0x100, v213\n" \
"ds_read_b128 v[104:107], v213 offset:2048\n" \
"ds_read_b128 v[108:111], v214 offset:2048\n" \
"ds_read_b128 v[64:67], v213 offset:6144\n" \
"ds_read_b128 v[68:71], v213 offset:8192\n" \
"ds_read_b128 v[72:75], v214 offset:6144\n" \
"ds_read_b128 v[76:79], v214 offset:8192\n" \
"ds_read_b128 v[112:115], v213 offset:10240\n" \
"ds_read_b128 v[116:119], v213 offset:12288\n" \
"ds_read_b128 v[120:123], v214 offset:10240\n" \
"ds_read_b128 v[124:127], v214 offset:12288\n" \
"s_movk_i32 s63, 2112\n" \
"v_mad_u32_u24 %[oq0], v219, s63, v213\n" \
"v_mad_u32_u24 %[wo0], v219, s63, v214\n" \
"s_lshl_b32 s62, %[wid], 10\n" \
"s_add_u32 s62, s62, 7340032\n" \
"v_and_b32_e32 v215, 0x3b0, v217\n" \
"v_add_u32_e32 v215, s62, v215\n" \
"s_mov_b32 s63, 38016\n" \
"v_lshrrev_b32_e32 %[vct], 1, v208\n" \
"v_and_b32_e32 v208, 1, v208\n" \
"v_mad_u32_u24 %[vct], %[vct], s63, v215\n" \
"v_lshl_add_u32 %[vct], v208, 6, %[vct]\n" \
"v_lshrrev_b32_e32 %[rd0], 1, v209\n" \
"v_and_b32_e32 v209, 1, v209\n" \
"v_mad_u32_u24 %[rd0], %[rd0], s63, v215\n" \
"v_lshl_add_u32 %[rd0], v209, 6, %[rd0]\n" \
"buffer_load_dwordx4 a[128:131], %[vct], %[srdz], 0 offen offset:0 sc1\n" \
"buffer_load_dwordx4 a[132:135], %[rd0], %[srdz], 0 offen offset:0 sc1\n" \
"buffer_load_dwordx4 a[136:139], %[vct], %[srdz], s56 offen offset:0 sc1\n" \
"buffer_load_dwordx4 a[140:143], %[rd0], %[srdz], s56 offen offset:0 sc1\n" \
"buffer_load_dwordx4 a[144:147], %[vct], %[srdz], s57 offen offset:0 sc1\n" \
"buffer_load_dwordx4 a[148:151], %[rd0], %[srdz], s57 offen offset:0 sc1\n" \
"buffer_load_dwordx4 a[152:155], %[vct], %[srdz], s58 offen offset:0 sc1\n" \
"buffer_load_dwordx4 a[156:159], %[rd0], %[srdz], s58 offen offset:0 sc1\n" \
"s_waitcnt vmcnt(0) lgkmcnt(0)\n" \
"v_lshlrev_b32_e32 v96, 16, v104\n" \
"v_and_b32_e32 v97, s43, v104\n" \
"v_lshlrev_b32_e32 v98, 16, v105\n" \
"v_and_b32_e32 v99, s43, v105\n" \
"v_lshlrev_b32_e32 v100, 16, v106\n" \
"v_and_b32_e32 v101, s43, v106\n" \
"v_lshlrev_b32_e32 v102, 16, v107\n" \
"v_and_b32_e32 v103, s43, v107\n" \
"v_lshlrev_b32_e32 v104, 16, v108\n" \
"v_and_b32_e32 v105, s43, v108\n" \
"v_lshlrev_b32_e32 v106, 16, v109\n" \
"v_and_b32_e32 v107, s43, v109\n" \
"v_lshlrev_b32_e32 v108, 16, v110\n" \
"v_and_b32_e32 v109, s43, v110\n" \
"v_lshlrev_b32_e32 v110, 16, v111\n" \
"v_and_b32_e32 v111, s43, v111\n" \
"v_lshlrev_b32_e32 v80, 16, v88\n" \
"v_and_b32_e32 v81, s43, v88\n" \
"v_lshlrev_b32_e32 v82, 16, v89\n" \
"v_and_b32_e32 v83, s43, v89\n" \
"v_lshlrev_b32_e32 v84, 16, v90\n" \
"v_and_b32_e32 v85, s43, v90\n" \
"v_lshlrev_b32_e32 v86, 16, v91\n" \
"v_and_b32_e32 v87, s43, v91\n" \
"v_lshlrev_b32_e32 v88, 16, v92\n" \
"v_and_b32_e32 v89, s43, v92\n" \
"v_lshlrev_b32_e32 v90, 16, v93\n" \
"v_and_b32_e32 v91, s43, v93\n" \
"v_lshlrev_b32_e32 v92, 16, v94\n" \
"v_and_b32_e32 v93, s43, v94\n" \
"v_lshlrev_b32_e32 v94, 16, v95\n" \
"v_and_b32_e32 v95, s43, v95\n" \
"v_sub_f32_e32 v80, v80, v190\n" \
"v_sub_f32_e32 v81, v81, v190\n" \
"v_sub_f32_e32 v82, v82, v190\n" \
"v_sub_f32_e32 v83, v83, v190\n" \
"v_sub_f32_e32 v84, v84, v190\n" \
"v_sub_f32_e32 v85, v85, v190\n" \
"v_sub_f32_e32 v86, v86, v190\n" \
"v_sub_f32_e32 v87, v87, v190\n" \
"v_sub_f32_e32 v88, v88, v190\n" \
"v_sub_f32_e32 v89, v89, v190\n" \
"v_sub_f32_e32 v90, v90, v190\n" \
"v_sub_f32_e32 v91, v91, v190\n" \
"v_sub_f32_e32 v92, v92, v190\n" \
"v_sub_f32_e32 v93, v93, v190\n" \
"v_sub_f32_e32 v94, v94, v190\n" \
"v_sub_f32_e32 v95, v95, v190\n" \
"s_branch TLcs_%=\n" \
"Lsp1_%=:\n" \
"s_movk_i32 s51, 0x1000\n" \
"s_cmp_gt_u32 s48, 0\n" \
"s_cselect_b32 s61, 0, 1\n" \
"s_nop 0\n" \
"s_mul_i32 s61, s61, 152064\n" \
"Lsq1_%=:\n" \
"buffer_load_dwordx4 a[64:67], %[oq0], %[srdz], s61 offen offset:0 sc1\n" \
"buffer_load_dwordx4 a[68:71], %[oq0], %[srdz], s61 offen offset:1024 sc1\n" \
"buffer_load_dwordx4 a[72:75], %[oq0], %[srdz], s61 offen offset:2048 sc1\n" \
"buffer_load_dwordx4 a[76:79], %[oq0], %[srdz], s61 offen offset:3072 sc1\n" \
"buffer_load_dwordx4 a[80:83], v208, %[srdz], s61 offen offset:0 sc1\n" \
"buffer_load_dwordx4 a[84:87], v208, %[srdz], s61 offen offset:1024 sc1\n" \
"buffer_load_dwordx4 a[88:91], v208, %[srdz], s61 offen offset:2048 sc1\n" \
"buffer_load_dwordx4 a[92:95], v208, %[srdz], s61 offen offset:3072 sc1\n" \
"buffer_load_dwordx4 a[96:99], v209, %[srdz], s61 offen offset:0 sc1\n" \
"buffer_load_dwordx4 a[100:103], v209, %[srdz], s61 offen offset:1024 sc1\n" \
"buffer_load_dwordx4 a[104:107], v209, %[srdz], s61 offen offset:2048 sc1\n" \
"buffer_load_dwordx4 a[108:111], v209, %[srdz], s61 offen offset:3072 sc1\n" \
"buffer_load_dwordx4 a[112:115], v210, %[srdz], s61 offen offset:0 sc1\n" \
"buffer_load_dwordx4 a[116:119], v210, %[srdz], s61 offen offset:1024 sc1\n" \
"buffer_load_dwordx4 a[120:123], v210, %[srdz], s61 offen offset:2048 sc1\n" \
"buffer_load_dwordx4 a[124:127], v210, %[srdz], s61 offen offset:3072 sc1\n" \
"s_waitcnt vmcnt(0)\n" \
"s_nop 0\n" \
"v_mfma_f32_16x16x32_bf16 v[128:131], a[236:239], a[64:67], v[192:195]\n" \
"v_mfma_f32_16x16x32_bf16 v[128:131], a[196:199], a[68:71], v[128:131]\n" \
"v_mfma_f32_16x16x32_bf16 v[128:131], a[200:203], a[72:75], v[128:131]\n" \
"v_mfma_f32_16x16x32_bf16 v[128:131], a[204:207], a[76:79], v[128:131]\n" \
"v_mfma_f32_16x16x32_bf16 v[132:135], a[236:239], a[80:83], v[196:199]\n" \
"v_mfma_f32_16x16x32_bf16 v[132:135], a[196:199], a[84:87], v[132:135]\n" \
"v_mfma_f32_16x16x32_bf16 v[132:135], a[200:203], a[88:91], v[132:135]\n" \
"v_mfma_f32_16x16x32_bf16 v[132:135], a[204:207], a[92:95], v[132:135]\n" \
"v_mfma_f32_16x16x32_bf16 v[136:139], a[236:239], a[96:99], v[200:203]\n" \
"v_mfma_f32_16x16x32_bf16 v[136:139], a[196:199], a[100:103], v[136:139]\n" \
"v_mfma_f32_16x16x32_bf16 v[136:139], a[200:203], a[104:107], v[136:139]\n" \
"v_mfma_f32_16x16x32_bf16 v[136:139], a[204:207], a[108:111], v[136:139]\n" \
"v_mfma_f32_16x16x32_bf16 v[140:143], a[236:239], a[112:115], v[204:207]\n" \
"v_mfma_f32_16x16x32_bf16 v[140:143], a[196:199], a[116:119], v[140:143]\n" \
"v_mfma_f32_16x16x32_bf16 v[140:143], a[200:203], a[120:123], v[140:143]\n" \
"v_mfma_f32_16x16x32_bf16 v[140:143], a[204:207], a[124:127], v[140:143]\n" \
"s_nop 7\n" \
"s_nop 0\n" \
"v_cmp_u_f32_e64 s[44:45], v128, v132\n" \
"v_cmp_u_f32_e64 s[56:57], v136, v140\n" \
"s_or_b64 s[56:57], s[56:57], s[44:45]\n" \
"s_cbranch_scc0 Lbk1_%=\n" \
"s_sub_u32 s51, s51, 1\n" \
"s_cbranch_scc0 Lsq1_%=\n" \
"s_branch Lbk1_%=\n" \
"Lsp2_%=:\n" \
"s_movk_i32 s51, 0x1000\n" \
"s_cmp_gt_u32 s48, 1\n" \
"s_cselect_b32 s61, 1, 2\n" \
"s_mul_i32 s61, s61, 152064\n" \
"Lsq2_%=:\n" \
"buffer_load_dwordx4 a[0:3], %[oq0], %[srdz], s61 offen offset:0 sc1\n" \
"buffer_load_dwordx4 a[4:7], %[oq0], %[srdz], s61 offen offset:1024 sc1\n" \
"buffer_load_dwordx4 a[8:11], %[oq0], %[srdz], s61 offen offset:2048 sc1\n" \
"buffer_load_dwordx4 a[12:15], %[oq0], %[srdz], s61 offen offset:3072 sc1\n" \
"buffer_load_dwordx4 a[16:19], v208, %[srdz], s61 offen offset:0 sc1\n" \
"buffer_load_dwordx4 a[20:23], v208, %[srdz], s61 offen offset:1024 sc1\n" \
"buffer_load_dwordx4 a[24:27], v208, %[srdz], s61 offen offset:2048 sc1\n" \
"buffer_load_dwordx4 a[28:31], v208, %[srdz], s61 offen offset:3072 sc1\n" \
"buffer_load_dwordx4 a[32:35], v209, %[srdz], s61 offen offset:0 sc1\n" \
"buffer_load_dwordx4 a[36:39], v209, %[srdz], s61 offen offset:1024 sc1\n" \
"buffer_load_dwordx4 a[40:43], v209, %[srdz], s61 offen offset:2048 sc1\n" \
"buffer_load_dwordx4 a[44:47], v209, %[srdz], s61 offen offset:3072 sc1\n" \
"buffer_load_dwordx4 a[48:51], v210, %[srdz], s61 offen offset:0 sc1\n" \
"buffer_load_dwordx4 a[52:55], v210, %[srdz], s61 offen offset:1024 sc1\n" \
"buffer_load_dwordx4 a[56:59], v210, %[srdz], s61 offen offset:2048 sc1\n" \
"buffer_load_dwordx4 a[60:63], v210, %[srdz], s61 offen offset:3072 sc1\n" \
"s_waitcnt vmcnt(0)\n" \
"s_nop 0\n" \
"v_mfma_f32_16x16x32_bf16 v[192:195], a[196:199], a[0:3], v[128:131]\n" \
"v_mfma_f32_16x16x32_bf16 v[192:195], a[200:203], a[4:7], v[192:195]\n" \
"v_mfma_f32_16x16x32_bf16 v[192:195], a[204:207], a[8:11], v[192:195]\n" \
"v_mfma_f32_16x16x32_bf16 v[192:195], a[232:235], a[12:15], v[192:195]\n" \
"v_mfma_f32_16x16x32_bf16 v[196:199], a[196:199], a[16:19], v[132:135]\n" \
"v_mfma_f32_16x16x32_bf16 v[196:199], a[200:203], a[20:23], v[196:199]\n" \
"v_mfma_f32_16x16x32_bf16 v[196:199], a[204:207], a[24:27], v[196:199]\n" \
"v_mfma_f32_16x16x32_bf16 v[196:199], a[232:235], a[28:31], v[196:199]\n" \
"v_mfma_f32_16x16x32_bf16 v[200:203], a[196:199], a[32:35], v[136:139]\n" \
"v_mfma_f32_16x16x32_bf16 v[200:203], a[200:203], a[36:39], v[200:203]\n" \
"v_mfma_f32_16x16x32_bf16 v[200:203], a[204:207], a[40:43], v[200:203]\n" \
"v_mfma_f32_16x16x32_bf16 v[200:203], a[232:235], a[44:47], v[200:203]\n" \
"v_mfma_f32_16x16x32_bf16 v[204:207], a[196:199], a[48:51], v[140:143]\n" \
"v_mfma_f32_16x16x32_bf16 v[204:207], a[200:203], a[52:55], v[204:207]\n" \
"v_mfma_f32_16x16x32_bf16 v[204:207], a[204:207], a[56:59], v[204:207]\n" \
"v_mfma_f32_16x16x32_bf16 v[204:207], a[232:235], a[60:63], v[204:207]\n" \
"s_nop 7\n" \
"s_nop 0\n" \
"v_cmp_u_f32_e64 s[44:45], v192, v196\n" \
"v_cmp_u_f32_e64 s[56:57], v200, v204\n" \
"s_or_b64 s[56:57], s[56:57], s[44:45]\n" \
"s_cbranch_scc0 Lbk2_%=\n" \
"s_sub_u32 s51, s51, 1\n" \
"s_cbranch_scc0 Lsq2_%=\n" \
"s_branch Lbk2_%=\n" \
"Lsp3_%=:\n" \
"s_movk_i32 s51, 0x1000\n" \
"s_cmp_gt_u32 s48, 2\n" \
"s_cselect_b32 s61, 2, 3\n" \
"s_mul_i32 s61, s61, 152064\n" \
"Lsq3_%=:\n" \
"buffer_load_dwordx4 a[128:131], %[oq0], %[srdz], s61 offen offset:0 sc1\n" \
"buffer_load_dwordx4 a[132:135], %[oq0], %[srdz], s61 offen offset:1024 sc1\n" \
"buffer_load_dwordx4 a[136:139], %[oq0], %[srdz], s61 offen offset:2048 sc1\n" \
"buffer_load_dwordx4 a[140:143], %[oq0], %[srdz], s61 offen offset:3072 sc1\n" \
"buffer_load_dwordx4 a[144:147], v208, %[srdz], s61 offen offset:0 sc1\n" \
"buffer_load_dwordx4 a[148:151], v208, %[srdz], s61 offen offset:1024 sc1\n" \
"buffer_load_dwordx4 a[152:155], v208, %[srdz], s61 offen offset:2048 sc1\n" \
"buffer_load_dwordx4 a[156:159], v208, %[srdz], s61 offen offset:3072 sc1\n" \
"buffer_load_dwordx4 a[160:163], v209, %[srdz], s61 offen offset:0 sc1\n" \
"buffer_load_dwordx4 a[164:167], v209, %[srdz], s61 offen offset:1024 sc1\n" \
"buffer_load_dwordx4 a[168:171], v209, %[srdz], s61 offen offset:2048 sc1\n" \
"buffer_load_dwordx4 a[172:175], v209, %[srdz], s61 offen offset:3072 sc1\n" \
"buffer_load_dwordx4 a[176:179], v210, %[srdz], s61 offen offset:0 sc1\n" \
"buffer_load_dwordx4 a[180:183], v210, %[srdz], s61 offen offset:1024 sc1\n" \
"buffer_load_dwordx4 a[184:187], v210, %[srdz], s61 offen offset:2048 sc1\n" \
"buffer_load_dwordx4 a[188:191], v210, %[srdz], s61 offen offset:3072 sc1\n" \
"s_waitcnt vmcnt(0)\n" \
"s_nop 0\n" \
"v_mfma_f32_16x16x32_bf16 v[128:131], a[196:199], a[128:131], v[192:195]\n" \
"v_mfma_f32_16x16x32_bf16 v[128:131], a[200:203], a[132:135], v[128:131]\n" \
"v_mfma_f32_16x16x32_bf16 v[128:131], a[204:207], a[136:139], v[128:131]\n" \
"v_mfma_f32_16x16x32_bf16 v[128:131], a[224:227], a[140:143], v[128:131]\n" \
"v_mfma_f32_16x16x32_bf16 v[132:135], a[196:199], a[144:147], v[196:199]\n" \
"v_mfma_f32_16x16x32_bf16 v[132:135], a[200:203], a[148:151], v[132:135]\n" \
"v_mfma_f32_16x16x32_bf16 v[132:135], a[204:207], a[152:155], v[132:135]\n" \
"v_mfma_f32_16x16x32_bf16 v[132:135], a[224:227], a[156:159], v[132:135]\n" \
"v_mfma_f32_16x16x32_bf16 v[136:139], a[196:199], a[160:163], v[200:203]\n" \
"v_mfma_f32_16x16x32_bf16 v[136:139], a[200:203], a[164:167], v[136:139]\n" \
"v_mfma_f32_16x16x32_bf16 v[136:139], a[204:207], a[168:171], v[136:139]\n" \
"v_mfma_f32_16x16x32_bf16 v[136:139], a[224:227], a[172:175], v[136:139]\n" \
"v_mfma_f32_16x16x32_bf16 v[140:143], a[196:199], a[176:179], v[204:207]\n" \
"v_mfma_f32_16x16x32_bf16 v[140:143], a[200:203], a[180:183], v[140:143]\n" \
"v_mfma_f32_16x16x32_bf16 v[140:143], a[204:207], a[184:187], v[140:143]\n" \
"v_mfma_f32_16x16x32_bf16 v[140:143], a[224:227], a[188:191], v[140:143]\n" \
"s_nop 7\n" \
"s_nop 0\n" \
"v_cmp_u_f32_e64 s[44:45], v128, v132\n" \
"v_cmp_u_f32_e64 s[56:57], v136, v140\n" \
"s_or_b64 s[56:57], s[56:57], s[44:45]\n" \
"s_cbranch_scc0 Lbk3_%=\n" \
"s_sub_u32 s51, s51, 1\n" \
"s_cbranch_scc0 Lsq3_%=\n" \
"s_branch Lbk3_%=\n" \
"Lsp4_%=:\n" \
"s_movk_i32 s51, 0x1000\n" \
"s_cmp_gt_u32 s48, 3\n" \
"s_cselect_b32 s61, 3, 4\n" \
"s_mul_i32 s61, s61, 152064\n" \
"Lsq4_%=:\n" \
"buffer_load_dwordx4 a[64:67], %[oq0], %[srdz], s61 offen offset:0 sc1\n" \
"buffer_load_dwordx4 a[68:71], %[oq0], %[srdz], s61 offen offset:1024 sc1\n" \
"buffer_load_dwordx4 a[72:75], %[oq0], %[srdz], s61 offen offset:2048 sc1\n" \
"buffer_load_dwordx4 a[76:79], %[oq0], %[srdz], s61 offen offset:3072 sc1\n" \
"buffer_load_dwordx4 a[80:83], v208, %[srdz], s61 offen offset:0 sc1\n" \
"buffer_load_dwordx4 a[84:87], v208, %[srdz], s61 offen offset:1024 sc1\n" \
"buffer_load_dwordx4 a[88:91], v208, %[srdz], s61 offen offset:2048 sc1\n" \
"buffer_load_dwordx4 a[92:95], v208, %[srdz], s61 offen offset:3072 sc1\n" \
"buffer_load_dwordx4 a[96:99], v209, %[srdz], s61 offen offset:0 sc1\n" \
"buffer_load_dwordx4 a[100:103], v209, %[srdz], s61 offen offset:1024 sc1\n" \
"buffer_load_dwordx4 a[104:107], v209, %[srdz], s61 offen offset:2048 sc1\n" \
"buffer_load_dwordx4 a[108:111], v209, %[srdz], s61 offen offset:3072 sc1\n" \
"buffer_load_dwordx4 a[112:115], v210, %[srdz], s61 offen offset:0 sc1\n" \
"buffer_load_dwordx4 a[116:119], v210, %[srdz], s61 offen offset:1024 sc1\n" \
"buffer_load_dwordx4 a[120:123], v210, %[srdz], s61 offen offset:2048 sc1\n" \
"buffer_load_dwordx4 a[124:127], v210, %[srdz], s61 offen offset:3072 sc1\n" \
"s_waitcnt vmcnt(0)\n" \
"s_nop 0\n" \
"v_mfma_f32_16x16x32_bf16 v[192:195], a[196:199], a[64:67], v[128:131]\n" \
"v_mfma_f32_16x16x32_bf16 v[192:195], a[200:203], a[68:71], v[192:195]\n" \
"v_mfma_f32_16x16x32_bf16 v[192:195], a[204:207], a[72:75], v[192:195]\n" \
"v_mfma_f32_16x16x32_bf16 v[192:195], a[220:223], a[76:79], v[192:195]\n" \
"v_mfma_f32_16x16x32_bf16 v[196:199], a[196:199], a[80:83], v[132:135]\n" \
"v_mfma_f32_16x16x32_bf16 v[196:199], a[200:203], a[84:87], v[196:199]\n" \
"v_mfma_f32_16x16x32_bf16 v[196:199], a[204:207], a[88:91], v[196:199]\n" \
"v_mfma_f32_16x16x32_bf16 v[196:199], a[220:223], a[92:95], v[196:199]\n" \
"v_mfma_f32_16x16x32_bf16 v[200:203], a[196:199], a[96:99], v[136:139]\n" \
"v_mfma_f32_16x16x32_bf16 v[200:203], a[200:203], a[100:103], v[200:203]\n" \
"v_mfma_f32_16x16x32_bf16 v[200:203], a[204:207], a[104:107], v[200:203]\n" \
"v_mfma_f32_16x16x32_bf16 v[200:203], a[220:223], a[108:111], v[200:203]\n" \
"v_mfma_f32_16x16x32_bf16 v[204:207], a[196:199], a[112:115], v[140:143]\n" \
"v_mfma_f32_16x16x32_bf16 v[204:207], a[200:203], a[116:119], v[204:207]\n" \
"v_mfma_f32_16x16x32_bf16 v[204:207], a[204:207], a[120:123], v[204:207]\n" \
"v_mfma_f32_16x16x32_bf16 v[204:207], a[220:223], a[124:127], v[204:207]\n" \
"s_nop 7\n" \
"s_nop 0\n" \
"v_cmp_u_f32_e64 s[44:45], v192, v196\n" \
"v_cmp_u_f32_e64 s[56:57], v200, v204\n" \
"s_or_b64 s[56:57], s[56:57], s[44:45]\n" \
"s_cbranch_scc0 Lbk4_%=\n" \
"s_sub_u32 s51, s51, 1\n" \
"s_cbranch_scc0 Lsq4_%=\n" \
"s_branch Lbk4_%=\n" \
"Lsp5_%=:\n" \
"s_movk_i32 s51, 0x1000\n" \
"s_cmp_gt_u32 s48, 4\n" \
"s_cselect_b32 s61, 4, 5\n" \
"s_mul_i32 s61, s61, 152064\n" \
"Lsq5_%=:\n" \
"buffer_load_dwordx4 v[0:3], %[oq0], %[srdz], s61 offen offset:0 sc1\n" \
"buffer_load_dwordx4 v[8:11], %[oq0], %[srdz], s61 offen offset:1024 sc1\n" \
"buffer_load_dwordx4 v[16:19], %[oq0], %[srdz], s61 offen offset:2048 sc1\n" \
"buffer_load_dwordx4 v[24:27], %[oq0], %[srdz], s61 offen offset:3072 sc1\n" \
"buffer_load_dwordx4 v[32:35], v208, %[srdz], s61 offen offset:0 sc1\n" \
"buffer_load_dwordx4 v[40:43], v208, %[srdz], s61 offen offset:1024 sc1\n" \
"buffer_load_dwordx4 v[48:51], v208, %[srdz], s61 offen offset:2048 sc1\n" \
"buffer_load_dwordx4 v[56:59], v208, %[srdz], s61 offen offset:3072 sc1\n" \
"buffer_load_dwordx4 v[64:67], v209, %[srdz], s61 offen offset:0 sc1\n" \
"buffer_load_dwordx4 v[72:75], v209, %[srdz], s61 offen offset:1024 sc1\n" \
"buffer_load_dwordx4 v[80:83], v209, %[srdz], s61 offen offset:2048 sc1\n" \
"buffer_load_dwordx4 v[88:91], v209, %[srdz], s61 offen offset:3072 sc1\n" \
"buffer_load_dwordx4 v[96:99], v210, %[srdz], s61 offen offset:0 sc1\n" \
"buffer_load_dwordx4 v[104:107], v210, %[srdz], s61 offen offset:1024 sc1\n" \
"buffer_load_dwordx4 v[112:115], v210, %[srdz], s61 offen offset:2048 sc1\n" \
"buffer_load_dwordx4 v[120:123], v210, %[srdz], s61 offen offset:3072 sc1\n" \
"s_waitcnt vmcnt(0)\n" \
"s_nop 0\n" \
"v_mfma_f32_16x16x32_bf16 v[128:131], a[232:235], v[0:3], v[192:195]\n" \
"v_mfma_f32_16x16x32_bf16 v[128:131], a[236:239], v[8:11], v[128:131]\n" \
"v_mfma_f32_16x16x32_bf16 v[128:131], a[208:211], v[16:19], v[128:131]\n" \
"v_mfma_f32_16x16x32_bf16 v[128:131], a[228:231], v[24:27], v[128:131]\n" \
"v_mfma_f32_16x16x32_bf16 v[132:135], a[232:235], v[32:35], v[196:199]\n" \
"v_mfma_f32_16x16x32_bf16 v[132:135], a[236:239], v[40:43], v[132:135]\n" \
"v_mfma_f32_16x16x32_bf16 v[132:135], a[208:211], v[48:51], v[132:135]\n" \
"v_mfma_f32_16x16x32_bf16 v[132:135], a[228:231], v[56:59], v[132:135]\n" \
"v_mfma_f32_16x16x32_bf16 v[136:139], a[232:235], v[64:67], v[200:203]\n" \
"v_mfma_f32_16x16x32_bf16 v[136:139], a[236:239], v[72:75], v[136:139]\n" \
"v_mfma_f32_16x16x32_bf16 v[136:139], a[208:211], v[80:83], v[136:139]\n" \
"v_mfma_f32_16x16x32_bf16 v[136:139], a[228:231], v[88:91], v[136:139]\n" \
"v_mfma_f32_16x16x32_bf16 v[140:143], a[232:235], v[96:99], v[204:207]\n" \
"v_mfma_f32_16x16x32_bf16 v[140:143], a[236:239], v[104:107], v[140:143]\n" \
"v_mfma_f32_16x16x32_bf16 v[140:143], a[208:211], v[112:115], v[140:143]\n" \
"v_mfma_f32_16x16x32_bf16 v[140:143], a[228:231], v[120:123], v[140:143]\n" \
"s_nop 7\n" \
"s_nop 0\n" \
"v_cmp_u_f32_e64 s[44:45], v128, v132\n" \
"v_cmp_u_f32_e64 s[56:57], v136, v140\n" \
"s_or_b64 s[56:57], s[56:57], s[44:45]\n" \
"s_cbranch_scc0 Lbk5_%=\n" \
"s_sub_u32 s51, s51, 1\n" \
"s_cbranch_scc0 Lsq5_%=\n" \
"s_branch Lbk5_%=\n" \
"Lsp6_%=:\n" \
"s_movk_i32 s51, 0x1000\n" \
"s_cmp_gt_u32 s48, 5\n" \
"s_cselect_b32 s61, 5, 6\n" \
"s_mul_i32 s61, s61, 152064\n" \
"Lsq6_%=:\n" \
"buffer_load_dwordx4 a[0:3], %[oq0], %[srdz], s61 offen offset:0 sc1\n" \
"buffer_load_dwordx4 a[4:7], %[oq0], %[srdz], s61 offen offset:1024 sc1\n" \
"buffer_load_dwordx4 a[8:11], %[oq0], %[srdz], s61 offen offset:2048 sc1\n" \
"buffer_load_dwordx4 a[12:15], %[oq0], %[srdz], s61 offen offset:3072 sc1\n" \
"buffer_load_dwordx4 a[16:19], v208, %[srdz], s61 offen offset:0 sc1\n" \
"buffer_load_dwordx4 a[20:23], v208, %[srdz], s61 offen offset:1024 sc1\n" \
"buffer_load_dwordx4 a[24:27], v208, %[srdz], s61 offen offset:2048 sc1\n" \
"buffer_load_dwordx4 a[28:31], v208, %[srdz], s61 offen offset:3072 sc1\n" \
"buffer_load_dwordx4 a[32:35], v209, %[srdz], s61 offen offset:0 sc1\n" \
"buffer_load_dwordx4 a[36:39], v209, %[srdz], s61 offen offset:1024 sc1\n" \
"buffer_load_dwordx4 a[40:43], v209, %[srdz], s61 offen offset:2048 sc1\n" \
"buffer_load_dwordx4 a[44:47], v209, %[srdz], s61 offen offset:3072 sc1\n" \
"buffer_load_dwordx4 a[48:51], v210, %[srdz], s61 offen offset:0 sc1\n" \
"buffer_load_dwordx4 a[52:55], v210, %[srdz], s61 offen offset:1024 sc1\n" \
"buffer_load_dwordx4 a[56:59], v210, %[srdz], s61 offen offset:2048 sc1\n" \
"buffer_load_dwordx4 a[60:63], v210, %[srdz], s61 offen offset:3072 sc1\n" \
"s_waitcnt vmcnt(0)\n" \
"s_nop 0\n" \
"v_mfma_f32_16x16x32_bf16 v[192:195], a[196:199], a[0:3], v[128:131]\n" \
"v_mfma_f32_16x16x32_bf16 v[192:195], a[200:203], a[4:7], v[192:195]\n" \
"v_mfma_f32_16x16x32_bf16 v[192:195], a[204:207], a[8:11], v[192:195]\n" \
"v_mfma_f32_16x16x32_bf16 v[192:195], a[220:223], a[12:15], v[192:195]\n" \
"v_mfma_f32_16x16x32_bf16 v[196:199], a[196:199], a[16:19], v[132:135]\n" \
"v_mfma_f32_16x16x32_bf16 v[196:199], a[200:203], a[20:23], v[196:199]\n" \
"v_mfma_f32_16x16x32_bf16 v[196:199], a[204:207], a[24:27], v[196:199]\n" \
"v_mfma_f32_16x16x32_bf16 v[196:199], a[220:223], a[28:31], v[196:199]\n" \
"v_mfma_f32_16x16x32_bf16 v[200:203], a[196:199], a[32:35], v[136:139]\n" \
"v_mfma_f32_16x16x32_bf16 v[200:203], a[200:203], a[36:39], v[200:203]\n" \
"v_mfma_f32_16x16x32_bf16 v[200:203], a[204:207], a[40:43], v[200:203]\n" \
"v_mfma_f32_16x16x32_bf16 v[200:203], a[220:223], a[44:47], v[200:203]\n" \
"v_mfma_f32_16x16x32_bf16 v[204:207], a[196:199], a[48:51], v[140:143]\n" \
"v_mfma_f32_16x16x32_bf16 v[204:207], a[200:203], a[52:55], v[204:207]\n" \
"v_mfma_f32_16x16x32_bf16 v[204:207], a[204:207], a[56:59], v[204:207]\n" \
"v_mfma_f32_16x16x32_bf16 v[204:207], a[220:223], a[60:63], v[204:207]\n" \
"s_nop 7\n" \
"s_nop 0\n" \
"v_cmp_u_f32_e64 s[44:45], v192, v196\n" \
"v_cmp_u_f32_e64 s[56:57], v200, v204\n" \
"s_or_b64 s[56:57], s[56:57], s[44:45]\n" \
"s_cbranch_scc0 Lbk6_%=\n" \
"s_sub_u32 s51, s51, 1\n" \
"s_cbranch_scc0 Lsq6_%=\n" \
"s_branch Lbk6_%=\n" \
"Lsp7_%=:\n" \
"s_movk_i32 s51, 0x1000\n" \
"s_cmp_gt_u32 s48, 6\n" \
"s_cselect_b32 s61, 6, 7\n" \
"s_mul_i32 s61, s61, 152064\n" \
"Lsq7_%=:\n" \
"buffer_load_dwordx4 a[128:131], %[oq0], %[srdz], s61 offen offset:0 sc1\n" \
"buffer_load_dwordx4 a[132:135], %[oq0], %[srdz], s61 offen offset:1024 sc1\n" \
"buffer_load_dwordx4 a[136:139], %[oq0], %[srdz], s61 offen offset:2048 sc1\n" \
"buffer_load_dwordx4 a[140:143], %[oq0], %[srdz], s61 offen offset:3072 sc1\n" \
"buffer_load_dwordx4 a[144:147], v208, %[srdz], s61 offen offset:0 sc1\n" \
"buffer_load_dwordx4 a[148:151], v208, %[srdz], s61 offen offset:1024 sc1\n" \
"buffer_load_dwordx4 a[152:155], v208, %[srdz], s61 offen offset:2048 sc1\n" \
"buffer_load_dwordx4 a[156:159], v208, %[srdz], s61 offen offset:3072 sc1\n" \
"buffer_load_dwordx4 a[160:163], v209, %[srdz], s61 offen offset:0 sc1\n" \
"buffer_load_dwordx4 a[164:167], v209, %[srdz], s61 offen offset:1024 sc1\n" \
"buffer_load_dwordx4 a[168:171], v209, %[srdz], s61 offen offset:2048 sc1\n" \
"buffer_load_dwordx4 a[172:175], v209, %[srdz], s61 offen offset:3072 sc1\n" \
"buffer_load_dwordx4 a[176:179], v210, %[srdz], s61 offen offset:0 sc1\n" \
"buffer_load_dwordx4 a[180:183], v210, %[srdz], s61 offen offset:1024 sc1\n" \
"buffer_load_dwordx4 a[184:187], v210, %[srdz], s61 offen offset:2048 sc1\n" \
"buffer_load_dwordx4 a[188:191], v210, %[srdz], s61 offen offset:3072 sc1\n" \
"s_waitcnt vmcnt(0)\n" \
"s_nop 0\n" \
"v_mfma_f32_16x16x32_bf16 v[128:131], a[208:211], a[128:131], v[192:195]\n" \
"v_mfma_f32_16x16x32_bf16 v[128:131], a[224:227], a[132:135], v[128:131]\n" \
"v_mfma_f32_16x16x32_bf16 v[128:131], a[228:231], a[136:139], v[128:131]\n" \
"v_mfma_f32_16x16x32_bf16 v[128:131], a[236:239], a[140:143], v[128:131]\n" \
"v_mfma_f32_16x16x32_bf16 v[132:135], a[208:211], a[144:147], v[196:199]\n" \
"v_mfma_f32_16x16x32_bf16 v[132:135], a[224:227], a[148:151], v[132:135]\n" \
"v_mfma_f32_16x16x32_bf16 v[132:135], a[228:231], a[152:155], v[132:135]\n" \
"v_mfma_f32_16x16x32_bf16 v[132:135], a[236:239], a[156:159], v[132:135]\n" \
"v_mfma_f32_16x16x32_bf16 v[136:139], a[208:211], a[160:163], v[200:203]\n" \
"v_mfma_f32_16x16x32_bf16 v[136:139], a[224:227], a[164:167], v[136:139]\n" \
"v_mfma_f32_16x16x32_bf16 v[136:139], a[228:231], a[168:171], v[136:139]\n" \
"v_mfma_f32_16x16x32_bf16 v[136:139], a[236:239], a[172:175], v[136:139]\n" \
"v_mfma_f32_16x16x32_bf16 v[140:143], a[208:211], a[176:179], v[204:207]\n" \
"v_mfma_f32_16x16x32_bf16 v[140:143], a[224:227], a[180:183], v[140:143]\n" \
"v_mfma_f32_16x16x32_bf16 v[140:143], a[228:231], a[184:187], v[140:143]\n" \
"v_mfma_f32_16x16x32_bf16 v[140:143], a[236:239], a[188:191], v[140:143]\n" \
"s_nop 7\n" \
"s_nop 0\n" \
"v_cmp_u_f32_e64 s[44:45], v128, v132\n" \
"v_cmp_u_f32_e64 s[56:57], v136, v140\n" \
"s_or_b64 s[56:57], s[56:57], s[44:45]\n" \
"s_cbranch_scc0 Lbk7_%=\n" \
"s_sub_u32 s51, s51, 1\n" \
"s_cbranch_scc0 Lsq7_%=\n" \
"s_branch Lbk7_%=\n" \
"Lspk_%=:\n" \
"s_nop 0\n" \
"s_mov_b32 s51, 0x10000\n" \
"Lsqk_%=:\n" \
"buffer_load_dword v229, v226, %[srdz], s50 offen sc1\n" \
"s_waitcnt vmcnt(0)\n" \
"s_nop 0\n" \
"v_cmp_eq_u32_e64 s[56:57], 0, v229\n" \
"s_cmp_lg_u64 s[56:57], 0\n" \
"s_cbranch_scc0 Lbkk_%=\n" \
"s_sub_u32 s51, s51, 1\n" \
"s_cbranch_scc0 Lsqk_%=\n" \
"s_branch Lbkk_%=\n" \
"Lspp_%=:\n" \
"s_nop 0\n" \
"s_mov_b32 s51, 0x10000\n" \
"Lsqp_%=:\n" \
"buffer_load_dword v232, v226, %[srdz], s54 offen sc1\n" \
"s_waitcnt vmcnt(0)\n" \
"v_cmp_eq_u32_e32 vcc, 0, v232\n" \
"s_cbranch_vccz Lbkp_%=\n" \
"s_sub_u32 s51, s51, 1\n" \
"s_cbranch_scc0 Lsqp_%=\n" \
"s_branch Lbkp_%=\n" \
"Lpars_%=:\n" \
"s_mov_b32 s51, 0x10000\n" \
"s_lshl_b32 s61, %[wid], 10\n" \
"v_lshrrev_b32_e32 v235, 3, v224\n" \
"v_add_u32_e64 v235, s61, v235\n" \
"Lpar1_%=:\n" \
"s_mov_b32 s61, 262144\n" \
"s_nop 0\n" \
"s_nop 0\n" \
"buffer_load_dwordx4 v[192:195], v235, %[srdz], s61 offen offset:0 sc1\n" \
"s_mov_b32 s61, 266240\n" \
"s_nop 0\n" \
"s_nop 0\n" \
"buffer_load_dwordx4 v[196:199], v235, %[srdz], s61 offen offset:0 sc1\n" \
"s_mov_b32 s61, 270336\n" \
"s_nop 0\n" \
"s_nop 0\n" \
"buffer_load_dwordx4 v[200:203], v235, %[srdz], s61 offen offset:0 sc1\n" \
"s_mov_b32 s61, 274432\n" \
"s_nop 0\n" \
"s_nop 0\n" \
"buffer_load_dwordx4 v[204:207], v235, %[srdz], s61 offen offset:0 sc1\n" \
"s_waitcnt vmcnt(0)\n" \
"s_nop 0\n" \
"v_and_or_b32 v236, v192, v193, 0\n" \
"v_bitop3_b32 v236, v236, v194, v195 bitop3:0x80\n" \
"v_bitop3_b32 v236, v236, v196, v197 bitop3:0x80\n" \
"v_bitop3_b32 v236, v236, v198, v199 bitop3:0x80\n" \
"v_bitop3_b32 v236, v236, v200, v201 bitop3:0x80\n" \
"v_bitop3_b32 v236, v236, v202, v203 bitop3:0x80\n" \
"v_bitop3_b32 v236, v236, v204, v205 bitop3:0x80\n" \
"v_bitop3_b32 v236, v236, v206, v207 bitop3:0x80\n" \
"v_and_b32_e64 v236, 1, v236\n" \
"v_cmp_eq_u32_e64 s[56:57], 0, v236\n" \
"s_cmp_lg_u64 s[56:57], 0\n" \
"s_cbranch_scc0 Lparb_%=\n" \
"s_sub_u32 s51, s51, 1\n" \
"s_cbranch_scc0 Lpar1_%=\n" \
"s_branch Lparb_%=\n" \
"Ls0_%=:\n" \
"s_movk_i32 s51, 0x1000\n" \
"s_waitcnt vmcnt(0)\n" \
"Ls01_%=:\n" \
"s_nop 0\n" \
"s_mov_b32 s40, 606208\n" \
"buffer_load_dwordx4 a[176:179], v237, %[srdz], s40 offen sc1\n" \
"s_mov_b32 s40, 610304\n" \
"buffer_load_dwordx4 a[180:183], v237, %[srdz], s40 offen sc1\n" \
"s_mov_b32 s40, 614400\n" \
"buffer_load_dwordx4 a[184:187], v237, %[srdz], s40 offen sc1\n" \
"s_mov_b32 s40, 618496\n" \
"buffer_load_dwordx4 a[188:191], v237, %[srdz], s40 offen sc1\n" \
"s_waitcnt vmcnt(0)\n" \
"s_nop 0\n" \
"v_mfma_f32_16x16x32_bf16 v[232:235], a[176:179], a[176:179], 0\n" \
"v_mfma_f32_16x16x32_bf16 v[232:235], a[180:183], a[180:183], v[232:235]\n" \
"v_mfma_f32_16x16x32_bf16 v[232:235], a[184:187], a[184:187], v[232:235]\n" \
"v_mfma_f32_16x16x32_bf16 v[232:235], a[188:191], a[188:191], v[232:235]\n" \
"s_nop 7\n" \
"s_nop 0\n" \
"v_readlane_b32 s40, v232, 0\n" \
"v_mfma_f32_16x16x32_bf16 v[216:219], a[176:179], v[0:3], 0\n" \
"v_mfma_f32_16x16x32_bf16 v[216:219], a[180:183], v[8:11], v[216:219]\n" \
"v_mfma_f32_16x16x32_bf16 v[216:219], a[184:187], v[16:19], v[216:219]\n" \
"v_mfma_f32_16x16x32_bf16 v[216:219], a[188:191], v[24:27], v[216:219]\n" \
"s_nop 7\n" \
"s_nop 0\n" \
"v_mov_b32_dpp v231, v216 quad_perm:[0,1,2,3] row_mask:0x1 bank_mask:0xf\n" \
"v_mfma_f32_16x16x32_bf16 v[232:235], a[176:179], v[32:35], 0\n" \
"v_mfma_f32_16x16x32_bf16 v[232:235], a[180:183], v[40:43], v[232:235]\n" \
"v_mfma_f32_16x16x32_bf16 v[232:235], a[184:187], v[48:51], v[232:235]\n" \
"v_mfma_f32_16x16x32_bf16 v[232:235], a[188:191], v[56:59], v[232:235]\n" \
"s_nop 7\n" \
"s_nop 0\n" \
"v_mov_b32_dpp v231, v232 quad_perm:[0,1,2,3] row_mask:0x2 bank_mask:0xf\n" \
"v_mfma_f32_16x16x32_bf16 v[216:219], a[176:179], v[64:67], 0\n" \
"v_mfma_f32_16x16x32_bf16 v[216:219], a[180:183], v[72:75], v[216:219]\n" \
"v_mfma_f32_16x16x32_bf16 v[216:219], a[184:187], v[80:83], v[216:219]\n" \
"v_mfma_f32_16x16x32_bf16 v[216:219], a[188:191], v[88:91], v[216:219]\n" \
"s_nop 7\n" \
"s_nop 0\n" \
"v_mov_b32_dpp v231, v216 quad_perm:[0,1,2,3] row_mask:0x4 bank_mask:0xf\n" \
"v_mfma_f32_16x16x32_bf16 v[232:235], a[176:179], v[96:99], 0\n" \
"v_mfma_f32_16x16x32_bf16 v[232:235], a[180:183], v[104:107], v[232:235]\n" \
"v_mfma_f32_16x16x32_bf16 v[232:235], a[184:187], v[112:115], v[232:235]\n" \
"v_mfma_f32_16x16x32_bf16 v[232:235], a[188:191], v[120:123], v[232:235]\n" \
"s_nop 7\n" \
"s_nop 0\n" \
"v_mov_b32_dpp v231, v232 quad_perm:[0,1,2,3] row_mask:0x8 bank_mask:0xf\n" \
"v_mov_b32_e32 v230, 0\n" \
"v_mov_b32_e32 v236, 0\n" \
"v_mov_b32_e32 v222, 0\n" \
"v_mov_b32_e32 v223, 0\n" \
"v_dot2c_f32_bf16_e32 v230, v0, v0\n" \
"v_dot2c_f32_bf16_e32 v236, v32, v32\n" \
"v_dot2c_f32_bf16_e32 v222, v64, v64\n" \
"v_dot2c_f32_bf16_e32 v223, v96, v96\n" \
"v_dot2c_f32_bf16_e32 v230, v1, v1\n" \
"v_dot2c_f32_bf16_e32 v236, v33, v33\n" \
"v_dot2c_f32_bf16_e32 v222, v65, v65\n" \
"v_dot2c_f32_bf16_e32 v223, v97, v97\n" \
"v_dot2c_f32_bf16_e32 v230, v2, v2\n" \
"v_dot2c_f32_bf16_e32 v236, v34, v34\n" \
"v_dot2c_f32_bf16_e32 v222, v66, v66\n" \
"v_dot2c_f32_bf16_e32 v223, v98, v98\n" \
"v_dot2c_f32_bf16_e32 v230, v3, v3\n" \
"v_dot2c_f32_bf16_e32 v236, v35, v35\n" \
"v_dot2c_f32_bf16_e32 v222, v67, v67\n" \
"v_dot2c_f32_bf16_e32 v223, v99, v99\n" \
"v_dot2c_f32_bf16_e32 v230, v8, v8\n" \
"v_dot2c_f32_bf16_e32 v236, v40, v40\n" \
"v_dot2c_f32_bf16_e32 v222, v72, v72\n" \
"v_dot2c_f32_bf16_e32 v223, v104, v104\n" \
"v_dot2c_f32_bf16_e32 v230, v9, v9\n" \
"v_dot2c_f32_bf16_e32 v236, v41, v41\n" \
"v_dot2c_f32_bf16_e32 v222, v73, v73\n" \
"v_dot2c_f32_bf16_e32 v223, v105, v105\n" \
"v_dot2c_f32_bf16_e32 v230, v10, v10\n" \
"v_dot2c_f32_bf16_e32 v236, v42, v42\n" \
"v_dot2c_f32_bf16_e32 v222, v74, v74\n" \
"v_dot2c_f32_bf16_e32 v223, v106, v106\n" \
"v_dot2c_f32_bf16_e32 v230, v11, v11\n" \
"v_dot2c_f32_bf16_e32 v236, v43, v43\n" \
"v_dot2c_f32_bf16_e32 v222, v75, v75\n" \
"v_dot2c_f32_bf16_e32 v223, v107, v107\n" \
"v_dot2c_f32_bf16_e32 v230, v16, v16\n" \
"v_dot2c_f32_bf16_e32 v236, v48, v48\n" \
"v_dot2c_f32_bf16_e32 v222, v80, v80\n" \
"v_dot2c_f32_bf16_e32 v223, v112, v112\n" \
"v_dot2c_f32_bf16_e32 v230, v17, v17\n" \
"v_dot2c_f32_bf16_e32 v236, v49, v49\n" \
"v_dot2c_f32_bf16_e32 v222, v81, v81\n" \
"v_dot2c_f32_bf16_e32 v223, v113, v113\n" \
"v_dot2c_f32_bf16_e32 v230, v18, v18\n" \
"v_dot2c_f32_bf16_e32 v236, v50, v50\n" \
"v_dot2c_f32_bf16_e32 v222, v82, v82\n" \
"v_dot2c_f32_bf16_e32 v223, v114, v114\n" \
"v_dot2c_f32_bf16_e32 v230, v19, v19\n" \
"v_dot2c_f32_bf16_e32 v236, v51, v51\n" \
"v_dot2c_f32_bf16_e32 v222, v83, v83\n" \
"v_dot2c_f32_bf16_e32 v223, v115, v115\n" \
"v_dot2c_f32_bf16_e32 v230, v24, v24\n" \
"v_dot2c_f32_bf16_e32 v236, v56, v56\n" \
"v_dot2c_f32_bf16_e32 v222, v88, v88\n" \
"v_dot2c_f32_bf16_e32 v223, v120, v120\n" \
"v_dot2c_f32_bf16_e32 v230, v25, v25\n" \
"v_dot2c_f32_bf16_e32 v236, v57, v57\n" \
"v_dot2c_f32_bf16_e32 v222, v89, v89\n" \
"v_dot2c_f32_bf16_e32 v223, v121, v121\n" \
"v_dot2c_f32_bf16_e32 v230, v26, v26\n" \
"v_dot2c_f32_bf16_e32 v236, v58, v58\n" \
"v_dot2c_f32_bf16_e32 v222, v90, v90\n" \
"v_dot2c_f32_bf16_e32 v223, v122, v122\n" \
"v_dot2c_f32_bf16_e32 v230, v27, v27\n" \
"v_dot2c_f32_bf16_e32 v236, v59, v59\n" \
"v_dot2c_f32_bf16_e32 v222, v91, v91\n" \
"v_dot2c_f32_bf16_e32 v223, v123, v123\n" \
"s_nop 2\n" \
"v_permlane32_swap_b32_e32 v230, v222\n" \
"v_permlane32_swap_b32_e32 v236, v223\n" \
"v_add_f32_e32 v230, v230, v222\n" \
"v_add_f32_e32 v236, v236, v223\n" \
"s_nop 0\n" \
"s_nop 0\n" \
"v_permlane16_swap_b32_e32 v230, v236\n" \
"v_add_f32_e32 v230, v230, v236\n" \
"v_mul_f32_e32 v230, s40, v230\n" \
"v_mul_f32_e32 v230, s63, v230\n" \
"v_lshrrev_b32_e32 v219, 5, v224\n" \
"v_cmp_u_f32_e64 s[42:43], v231, v231\n" \
"s_cmp_lg_u64 s[42:43], 0\n" \
"s_cbranch_scc0 Ls0b_%=\n" \
"s_sub_u32 s51, s51, 1\n" \
"s_cbranch_scc0 Ls01_%=\n" \
"s_branch Ls0b_%=\n" \
"Ls1_%=:\n" \
"s_nop 0\n" \
"s_mov_b32 s51, 0x10000\n" \
"Ls11_%=:\n" \
"buffer_load_dword v235, v234, %[srdz], s63 offen sc1\n" \
"s_waitcnt vmcnt(0)\n" \
"v_and_b32_e32 v230, 1, v235\n" \
"v_cmp_eq_u32_e64 s[68:69], 0, v230\n" \
"s_cmp_lg_u64 s[68:69], 0\n" \
"s_cbranch_scc0 Ls1b_%=\n" \
"s_sub_u32 s51, s51, 1\n" \
"s_cbranch_scc0 Ls11_%=\n" \
"s_branch Ls1b_%=\n" \
"Lus_%=:\n" \
"s_nop 0\n" \
"s_mov_b32 s51, 0x10000\n" \
"v_lshrrev_b32_e64 v235, 3, v224\n" \
"s_waitcnt vmcnt(0)\n" \
"Lus1_%=:\n" \
"s_lshl_b32 s62, %[wid], 10\n" \
"s_add_u32 s62, s62, 262144\n" \
"s_add_u32 s55, s62, 8192\n" \
"v_readlane_b32 s68, %[tlo], 20\n" \
"v_readlane_b32 s69, %[thi], 20\n" \
"s_lshl_b32 s63, %[wid], 9\n" \
"v_lshrrev_b32_e32 v225, 4, v224\n" \
"v_add_u32_e64 v225, s63, v225\n" \
"buffer_load_dwordx4 v[230:233], v235, %[srdz], s62 offen sc1\n" \
"s_nop 0\n" \
"s_nop 0\n" \
"global_load_dwordx2 v[226:227], v225, s[68:69]\n" \
"buffer_load_dwordx4 v[234:237], v235, %[srdz], s55 offen sc1\n" \
"s_waitcnt vmcnt(0)\n" \
"s_nop 0\n" \
"v_bitop3_b32 v225, v230, v231, v232 bitop3:0x80\n" \
"v_bitop3_b32 v225, v225, v233, v234 bitop3:0x80\n" \
"v_bitop3_b32 v225, v225, v235, v236 bitop3:0x80\n" \
"v_bitop3_b32 v225, v225, v237, 1 bitop3:0x80\n" \
"v_cmp_eq_u32_e32 vcc, 0, v225\n" \
"s_cbranch_vccz Lus2_%=\n" \
"v_lshrrev_b32_e32 v235, 3, v224\n" \
"s_sub_u32 s51, s51, 1\n" \
"s_cbranch_scc0 Lus1_%=\n" \
"Lus2_%=:\n" \
"s_branch Lub_%=\n" \
"Lwalk_%=:\n" \
"s_mov_b64 exec, 0\n" \
"s_cmp_eq_u32 %[wid], 1\n" \
"s_cbranch_scc1 Lk0_%=\n" \
"s_cmp_eq_u32 %[wid], 2\n" \
"s_cbranch_scc1 Lk6_%=\n" \
"s_branch Lk12_%=\n" \
"adaln_kr:\n" \
"s_mov_b64 exec, -1\n" \
"s_branch Lgo_%=\n" \
"Lslow_%=:\n" \
"s_mov_b32 s52, 1\n" \
"s_mov_b32 s48, 0\n" \
"s_mov_b32 s51, 32\n" \
"s_nop 0\n" \
"s_mov_b32 s62, 0x80000000\n" \
"s_mov_b32 s63, 0x80000000\n" \
"s_waitcnt vmcnt(0)\n" \
"s_nop 0\n" \
"v_readlane_b32 s64, %[tlo], 9\n" \
"v_readlane_b32 s65, %[thi], 9\n" \
"s_mov_b32 s66, %[wbytes]\n" \
"v_mov_b32_e32 v229, %[vg0]\n" \
"s_mov_b32 s44, %[m0t]\n" \
"s_mov_b32 s45, 0\n" \
"v_xor_b32_e64 v208, 64, %[kb0]\n" \
"v_xor_b32_e32 v209, 0x80, %[kb0]\n" \
"v_xor_b32_e32 v210, 0xc0, %[kb0]\n" \
"v_xor_b32_e64 v212, 64, %[kd0]\n" \
"v_xor_b32_e32 v213, 0x80, %[kd0]\n" \
"v_xor_b32_e32 v214, 0xc0, %[kd0]\n" \
"v_xor_b32_e64 v211, 32, %[vvb]\n" \
"v_add_u32_e32 v215, 0xfffffc80, %[vd0]\n" \
"s_mov_b32 s66, 0\n" \
"s_nop 0\n" \
"v_mov_b32_e32 v225, 0xff800000\n" \
"v_mov_b32_e32 v226, 0xff800000\n" \
"v_mov_b32_e32 v227, 0xff800000\n" \
"v_mov_b32_e32 v228, 0xff800000\n" \
"s_waitcnt vmcnt(0) lgkmcnt(0)\n" \
"s_barrier\n" \
"Lsl_%=:\n" \
"s_mov_b32 m0, s40\n" \
"s_nop 0\n" \
"buffer_load_dwordx4 %[kd0], %[srdk], s48 offen offset:0 lds\n" \
"buffer_load_dwordx4 v212, %[srdk], s48 offen offset:1024 lds\n" \
"buffer_load_dwordx4 v213, %[srdk], s48 offen offset:2048 lds\n" \
"buffer_load_dwordx4 v214, %[srdk], s48 offen offset:3072 lds\n" \
"s_waitcnt vmcnt(0)\n" \
"s_barrier\n" \
"ds_read_b128 a[64:67], %[kb0] offset:0\n" \
"ds_read_b128 a[68:71], v208 offset:0\n" \
"ds_read_b128 a[72:75], v209 offset:0\n" \
"ds_read_b128 a[76:79], v210 offset:0\n" \
"ds_read_b128 a[80:83], %[kb0] offset:4096\n" \
"ds_read_b128 a[84:87], v208 offset:4096\n" \
"ds_read_b128 a[88:91], v209 offset:4096\n" \
"ds_read_b128 a[92:95], v210 offset:4096\n" \
"ds_read_b128 a[96:99], %[kb0] offset:8192\n" \
"ds_read_b128 a[100:103], v208 offset:8192\n" \
"ds_read_b128 a[104:107], v209 offset:8192\n" \
"ds_read_b128 a[108:111], v210 offset:8192\n" \
"ds_read_b128 a[112:115], %[kb0] offset:12288\n" \
"ds_read_b128 a[116:119], v208 offset:12288\n" \
"ds_read_b128 a[120:123], v209 offset:12288\n" \
"ds_read_b128 a[124:127], v210 offset:12288\n" \
"s_waitcnt lgkmcnt(0)\n" \
"s_barrier\n" \
"v_mfma_f32_16x16x32_bf16 v[128:131], a[64:67], a[0:3], 0\n" \
"v_mfma_f32_16x16x32_bf16 v[128:131], a[68:71], a[4:7], v[128:131]\n" \
"v_mfma_f32_16x16x32_bf16 v[128:131], a[72:75], a[8:11], v[128:131]\n" \
"v_mfma_f32_16x16x32_bf16 v[128:131], a[76:79], a[12:15], v[128:131]\n" \
"v_mfma_f32_16x16x32_bf16 v[132:135], a[80:83], a[0:3], 0\n" \
"v_mfma_f32_16x16x32_bf16 v[132:135], a[84:87], a[4:7], v[132:135]\n" \
"v_mfma_f32_16x16x32_bf16 v[132:135], a[88:91], a[8:11], v[132:135]\n" \
"v_mfma_f32_16x16x32_bf16 v[132:135], a[92:95], a[12:15], v[132:135]\n" \
"v_mfma_f32_16x16x32_bf16 v[136:139], a[64:67], a[16:19], 0\n" \
"v_mfma_f32_16x16x32_bf16 v[136:139], a[68:71], a[20:23], v[136:139]\n" \
"v_mfma_f32_16x16x32_bf16 v[136:139], a[72:75], a[24:27], v[136:139]\n" \
"v_mfma_f32_16x16x32_bf16 v[136:139], a[76:79], a[28:31], v[136:139]\n" \
"v_mfma_f32_16x16x32_bf16 v[140:143], a[80:83], a[16:19], 0\n" \
"v_mfma_f32_16x16x32_bf16 v[140:143], a[84:87], a[20:23], v[140:143]\n" \
"v_mfma_f32_16x16x32_bf16 v[140:143], a[88:91], a[24:27], v[140:143]\n" \
"v_mfma_f32_16x16x32_bf16 v[140:143], a[92:95], a[28:31], v[140:143]\n" \
"v_mfma_f32_16x16x32_bf16 v[144:147], a[64:67], a[32:35], 0\n" \
"v_mfma_f32_16x16x32_bf16 v[144:147], a[68:71], a[36:39], v[144:147]\n" \
"v_mfma_f32_16x16x32_bf16 v[144:147], a[72:75], a[40:43], v[144:147]\n" \
"v_mfma_f32_16x16x32_bf16 v[144:147], a[76:79], a[44:47], v[144:147]\n" \
"v_mfma_f32_16x16x32_bf16 v[148:151], a[80:83], a[32:35], 0\n" \
"v_mfma_f32_16x16x32_bf16 v[148:151], a[84:87], a[36:39], v[148:151]\n" \
"v_mfma_f32_16x16x32_bf16 v[148:151], a[88:91], a[40:43], v[148:151]\n" \
"v_mfma_f32_16x16x32_bf16 v[148:151], a[92:95], a[44:47], v[148:151]\n" \
"v_mfma_f32_16x16x32_bf16 v[152:155], a[64:67], a[48:51], 0\n" \
"v_mfma_f32_16x16x32_bf16 v[152:155], a[68:71], a[52:55], v[152:155]\n" \
"v_mfma_f32_16x16x32_bf16 v[152:155], a[72:75], a[56:59], v[152:155]\n" \
"v_mfma_f32_16x16x32_bf16 v[152:155], a[76:79], a[60:63], v[152:155]\n" \
"v_mfma_f32_16x16x32_bf16 v[156:159], a[80:83], a[48:51], 0\n" \
"v_mfma_f32_16x16x32_bf16 v[156:159], a[84:87], a[52:55], v[156:159]\n" \
"v_mfma_f32_16x16x32_bf16 v[156:159], a[88:91], a[56:59], v[156:159]\n" \
"v_mfma_f32_16x16x32_bf16 v[156:159], a[92:95], a[60:63], v[156:159]\n" \
"s_nop 7\n" \
"s_nop 0\n" \
"v_max3_f32 v230, v128, v129, v130\n" \
"v_max3_f32 v231, v131, v132, v133\n" \
"v_max3_f32 v230, v230, v134, v135\n" \
"v_max3_f32 v225, v230, v231, v225\n" \
"v_max3_f32 v230, v136, v137, v138\n" \
"v_max3_f32 v231, v139, v140, v141\n" \
"v_max3_f32 v230, v230, v142, v143\n" \
"v_max3_f32 v226, v230, v231, v226\n" \
"v_max3_f32 v230, v144, v145, v146\n" \
"v_max3_f32 v231, v147, v148, v149\n" \
"v_max3_f32 v230, v230, v150, v151\n" \
"v_max3_f32 v227, v230, v231, v227\n" \
"v_max3_f32 v230, v152, v153, v154\n" \
"v_max3_f32 v231, v155, v156, v157\n" \
"v_max3_f32 v230, v230, v158, v159\n" \
"v_max3_f32 v228, v230, v231, v228\n" \
"s_nop 3\n" \
"s_nop 0\n" \
"v_mfma_f32_16x16x32_bf16 v[128:131], a[96:99], a[0:3], 0\n" \
"v_mfma_f32_16x16x32_bf16 v[128:131], a[100:103], a[4:7], v[128:131]\n" \
"v_mfma_f32_16x16x32_bf16 v[128:131], a[104:107], a[8:11], v[128:131]\n" \
"v_mfma_f32_16x16x32_bf16 v[128:131], a[108:111], a[12:15], v[128:131]\n" \
"v_mfma_f32_16x16x32_bf16 v[132:135], a[112:115], a[0:3], 0\n" \
"v_mfma_f32_16x16x32_bf16 v[132:135], a[116:119], a[4:7], v[132:135]\n" \
"v_mfma_f32_16x16x32_bf16 v[132:135], a[120:123], a[8:11], v[132:135]\n" \
"v_mfma_f32_16x16x32_bf16 v[132:135], a[124:127], a[12:15], v[132:135]\n" \
"v_mfma_f32_16x16x32_bf16 v[136:139], a[96:99], a[16:19], 0\n" \
"v_mfma_f32_16x16x32_bf16 v[136:139], a[100:103], a[20:23], v[136:139]\n" \
"v_mfma_f32_16x16x32_bf16 v[136:139], a[104:107], a[24:27], v[136:139]\n" \
"v_mfma_f32_16x16x32_bf16 v[136:139], a[108:111], a[28:31], v[136:139]\n" \
"v_mfma_f32_16x16x32_bf16 v[140:143], a[112:115], a[16:19], 0\n" \
"v_mfma_f32_16x16x32_bf16 v[140:143], a[116:119], a[20:23], v[140:143]\n" \
"v_mfma_f32_16x16x32_bf16 v[140:143], a[120:123], a[24:27], v[140:143]\n" \
"v_mfma_f32_16x16x32_bf16 v[140:143], a[124:127], a[28:31], v[140:143]\n" \
"v_mfma_f32_16x16x32_bf16 v[144:147], a[96:99], a[32:35], 0\n" \
"v_mfma_f32_16x16x32_bf16 v[144:147], a[100:103], a[36:39], v[144:147]\n" \
"v_mfma_f32_16x16x32_bf16 v[144:147], a[104:107], a[40:43], v[144:147]\n" \
"v_mfma_f32_16x16x32_bf16 v[144:147], a[108:111], a[44:47], v[144:147]\n" \
"v_mfma_f32_16x16x32_bf16 v[148:151], a[112:115], a[32:35], 0\n" \
"v_mfma_f32_16x16x32_bf16 v[148:151], a[116:119], a[36:39], v[148:151]\n" \
"v_mfma_f32_16x16x32_bf16 v[148:151], a[120:123], a[40:43], v[148:151]\n" \
"v_mfma_f32_16x16x32_bf16 v[148:151], a[124:127], a[44:47], v[148:151]\n" \
"v_mfma_f32_16x16x32_bf16 v[152:155], a[96:99], a[48:51], 0\n" \
"v_mfma_f32_16x16x32_bf16 v[152:155], a[100:103], a[52:55], v[152:155]\n" \
"v_mfma_f32_16x16x32_bf16 v[152:155], a[104:107], a[56:59], v[152:155]\n" \
"v_mfma_f32_16x16x32_bf16 v[152:155], a[108:111], a[60:63], v[152:155]\n" \
"v_mfma_f32_16x16x32_bf16 v[156:159], a[112:115], a[48:51], 0\n" \
"v_mfma_f32_16x16x32_bf16 v[156:159], a[116:119], a[52:55], v[156:159]\n" \
"v_mfma_f32_16x16x32_bf16 v[156:159], a[120:123], a[56:59], v[156:159]\n" \
"v_mfma_f32_16x16x32_bf16 v[156:159], a[124:127], a[60:63], v[156:159]\n" \
"s_nop 7\n" \
"s_nop 0\n" \
"v_max3_f32 v230, v128, v129, v130\n" \
"v_max3_f32 v231, v131, v132, v133\n" \
"v_max3_f32 v230, v230, v134, v135\n" \
"v_max3_f32 v225, v230, v231, v225\n" \
"v_max3_f32 v230, v136, v137, v138\n" \
"v_max3_f32 v231, v139, v140, v141\n" \
"v_max3_f32 v230, v230, v142, v143\n" \
"v_max3_f32 v226, v230, v231, v226\n" \
"v_max3_f32 v230, v144, v145, v146\n" \
"v_max3_f32 v231, v147, v148, v149\n" \
"v_max3_f32 v230, v230, v150, v151\n" \
"v_max3_f32 v227, v230, v231, v227\n" \
"v_max3_f32 v230, v152, v153, v154\n" \
"v_max3_f32 v231, v155, v156, v157\n" \
"v_max3_f32 v230, v230, v158, v159\n" \
"v_max3_f32 v228, v230, v231, v228\n" \
"s_nop 3\n" \
"s_add_u32 s48, s48, s54\n" \
"s_sub_u32 s51, s51, 1\n" \
"s_cmp_lg_u32 s51, 0\n" \
"s_cbranch_scc1 Lsl_%=\n" \
"s_nop 0\n" \
"v_mov_b32_e32 v232, v225\n" \
"v_mov_b32_e32 v233, v226\n" \
"v_mov_b32_e32 v234, v227\n" \
"v_mov_b32_e32 v235, v228\n" \
"s_nop 0\n" \
"v_permlane16_swap_b32_e32 v225, v232\n" \
"v_permlane16_swap_b32_e32 v226, v233\n" \
"v_permlane16_swap_b32_e32 v227, v234\n" \
"v_permlane16_swap_b32_e32 v228, v235\n" \
"v_max_f32_e32 v225, v225, v232\n" \
"v_max_f32_e32 v226, v226, v233\n" \
"v_max_f32_e32 v227, v227, v234\n" \
"v_max_f32_e32 v228, v228, v235\n" \
"v_mov_b32_e32 v232, v225\n" \
"v_mov_b32_e32 v233, v226\n" \
"v_mov_b32_e32 v234, v227\n" \
"v_mov_b32_e32 v235, v228\n" \
"s_nop 0\n" \
"v_permlane32_swap_b32_e32 v225, v232\n" \
"v_permlane32_swap_b32_e32 v226, v233\n" \
"v_permlane32_swap_b32_e32 v227, v234\n" \
"v_permlane32_swap_b32_e32 v228, v235\n" \
"v_max_f32_e32 v225, v225, v232\n" \
"v_max_f32_e32 v226, v226, v233\n" \
"v_max_f32_e32 v227, v227, v234\n" \
"v_max_f32_e32 v228, v228, v235\n" \
"s_cmp_lg_u32 s69, 0\n" \
"s_cbranch_scc1 Lnewm_%=\n" \
"v_sub_f32_e32 v225, 0, v160\n" \
"v_sub_f32_e32 v226, 0, v164\n" \
"v_sub_f32_e64 v227, 0, v168\n" \
"v_sub_f32_e32 v228, 0, v172\n" \
"Lnewm_%=:\n" \
"s_mov_b32 s48, 0\n" \
"s_mov_b32 m0, s40\n" \
"s_nop 0\n" \
"buffer_load_dwordx4 %[kd0], %[srdk], s48 offen offset:0 lds\n" \
"buffer_load_dwordx4 v212, %[srdk], s48 offen offset:1024 lds\n" \
"buffer_load_dwordx4 v213, %[srdk], s48 offen offset:2048 lds\n" \
"buffer_load_dwordx4 v214, %[srdk], s48 offen offset:3072 lds\n" \
"s_mov_b32 s48, 16384\n" \
"s_mov_b32 m0, s41\n" \
"s_nop 0\n" \
"buffer_load_dwordx4 %[kd0], %[srdk], s48 offen offset:0 lds\n" \
"buffer_load_dwordx4 v212, %[srdk], s48 offen offset:1024 lds\n" \
"buffer_load_dwordx4 v213, %[srdk], s48 offen offset:2048 lds\n" \
"buffer_load_dwordx4 v214, %[srdk], s48 offen offset:3072 lds\n" \
"s_add_u32 m0, %[m0b], 65536\n" \
"s_nop 0\n" \
"s_nop 0\n" \
"buffer_load_dwordx4 %[vd0], %[srdv], s48 offen offset:0 lds\n" \
"buffer_load_dwordx4 v215, %[srdv], s48 offen offset:1024 lds\n" \
"buffer_load_dwordx4 %[vd0], %[srdv], s48 offen offset:2048 lds\n" \
"buffer_load_dwordx4 v215, %[srdv], s48 offen offset:3072 lds\n" \
"s_mov_b32 s48, 32768\n" \
"s_mov_b32 m0, s42\n" \
"s_nop 0\n" \
"buffer_load_dwordx4 %[kd0], %[srdk], s48 offen offset:0 lds\n" \
"buffer_load_dwordx4 v212, %[srdk], s48 offen offset:1024 lds\n" \
"buffer_load_dwordx4 v213, %[srdk], s48 offen offset:2048 lds\n" \
"buffer_load_dwordx4 v214, %[srdk], s48 offen offset:3072 lds\n" \
"s_add_u32 m0, %[m0b], 81920\n" \
"s_nop 0\n" \
"s_nop 0\n" \
"buffer_load_dwordx4 %[vd0], %[srdv], s48 offen offset:0 lds\n" \
"buffer_load_dwordx4 v215, %[srdv], s48 offen offset:1024 lds\n" \
"buffer_load_dwordx4 %[vd0], %[srdv], s48 offen offset:2048 lds\n" \
"buffer_load_dwordx4 v215, %[srdv], s48 offen offset:3072 lds\n" \
"s_mov_b32 s48, 49152\n" \
"s_mov_b32 m0, s43\n" \
"s_nop 0\n" \
"buffer_load_dwordx4 %[kd0], %[srdk], s48 offen offset:0 lds\n" \
"buffer_load_dwordx4 v212, %[srdk], s48 offen offset:1024 lds\n" \
"buffer_load_dwordx4 v213, %[srdk], s48 offen offset:2048 lds\n" \
"buffer_load_dwordx4 v214, %[srdk], s48 offen offset:3072 lds\n" \
"s_add_u32 m0, %[m0b], 98304\n" \
"s_nop 0\n" \
"s_nop 0\n" \
"buffer_load_dwordx4 %[vd0], %[srdv], s48 offen offset:0 lds\n" \
"buffer_load_dwordx4 v215, %[srdv], s48 offen offset:1024 lds\n" \
"buffer_load_dwordx4 %[vd0], %[srdv], s48 offen offset:2048 lds\n" \
"buffer_load_dwordx4 v215, %[srdv], s48 offen offset:3072 lds\n" \
"s_waitcnt vmcnt(0)\n" \
"s_barrier\n" \
"ds_read_b128 a[64:67], %[kb0] offset:0\n" \
"ds_read_b128 a[68:71], v208 offset:0\n" \
"ds_read_b128 a[72:75], v209 offset:0\n" \
"ds_read_b128 a[76:79], v210 offset:0\n" \
"ds_read_b128 a[80:83], %[kb0] offset:4096\n" \
"ds_read_b128 a[84:87], v208 offset:4096\n" \
"ds_read_b128 a[88:91], v209 offset:4096\n" \
"ds_read_b128 a[92:95], v210 offset:4096\n" \
"s_mov_b32 s48, 65536\n" \
"s_mov_b32 s60, 0\n" \
"s_waitcnt lgkmcnt(0)\n" \
"v_mfma_f32_16x16x32_bf16 v[128:131], a[64:67], a[0:3], 0\n" \
"ds_read_b128 a[96:99], %[kb0] offset:8192\n" \
"v_mfma_f32_16x16x32_bf16 v[128:131], a[68:71], a[4:7], v[128:131]\n" \
"ds_read_b128 a[100:103], v208 offset:8192\n" \
"v_mfma_f32_16x16x32_bf16 v[128:131], a[72:75], a[8:11], v[128:131]\n" \
"ds_read_b128 a[104:107], v209 offset:8192\n" \
"v_mfma_f32_16x16x32_bf16 v[128:131], a[76:79], a[12:15], v[128:131]\n" \
"ds_read_b128 a[108:111], v210 offset:8192\n" \
"v_mfma_f32_16x16x32_bf16 v[132:135], a[80:83], a[0:3], 0\n" \
"ds_read_b128 a[112:115], %[kb0] offset:12288\n" \
"v_mfma_f32_16x16x32_bf16 v[132:135], a[84:87], a[4:7], v[132:135]\n" \
"ds_read_b128 a[116:119], v208 offset:12288\n" \
"v_mfma_f32_16x16x32_bf16 v[132:135], a[88:91], a[8:11], v[132:135]\n" \
"ds_read_b128 a[120:123], v209 offset:12288\n" \
"v_mfma_f32_16x16x32_bf16 v[132:135], a[92:95], a[12:15], v[132:135]\n" \
"ds_read_b128 a[124:127], v210 offset:12288\n" \
"v_mfma_f32_16x16x32_bf16 v[136:139], a[64:67], a[16:19], 0\n" \
"ds_read_b64_tr_b16 a[128:129], %[vvb] offset:0\n" \
"v_mfma_f32_16x16x32_bf16 v[136:139], a[68:71], a[20:23], v[136:139]\n" \
"ds_read_b64_tr_b16 a[130:131], %[vvb] offset:4096\n" \
"v_mfma_f32_16x16x32_bf16 v[136:139], a[72:75], a[24:27], v[136:139]\n" \
"ds_read_b64_tr_b16 a[132:133], v211 offset:0\n" \
"v_mfma_f32_16x16x32_bf16 v[136:139], a[76:79], a[28:31], v[136:139]\n" \
"ds_read_b64_tr_b16 a[134:135], v211 offset:4096\n" \
"v_mfma_f32_16x16x32_bf16 v[140:143], a[80:83], a[16:19], 0\n" \
"ds_read_b64_tr_b16 a[136:137], %[vvb] offset:512\n" \
"v_mfma_f32_16x16x32_bf16 v[140:143], a[84:87], a[20:23], v[140:143]\n" \
"ds_read_b64_tr_b16 a[138:139], %[vvb] offset:4608\n" \
"v_mfma_f32_16x16x32_bf16 v[140:143], a[88:91], a[24:27], v[140:143]\n" \
"ds_read_b64_tr_b16 a[140:141], v211 offset:512\n" \
"v_mfma_f32_16x16x32_bf16 v[140:143], a[92:95], a[28:31], v[140:143]\n" \
"ds_read_b64_tr_b16 a[142:143], v211 offset:4608\n" \
"v_mfma_f32_16x16x32_bf16 v[144:147], a[64:67], a[32:35], 0\n" \
"ds_read_b64_tr_b16 a[144:145], %[vvb] offset:1024\n" \
"v_mfma_f32_16x16x32_bf16 v[144:147], a[68:71], a[36:39], v[144:147]\n" \
"ds_read_b64_tr_b16 a[146:147], %[vvb] offset:5120\n" \
"v_mfma_f32_16x16x32_bf16 v[144:147], a[72:75], a[40:43], v[144:147]\n" \
"ds_read_b64_tr_b16 a[148:149], v211 offset:1024\n" \
"v_mfma_f32_16x16x32_bf16 v[144:147], a[76:79], a[44:47], v[144:147]\n" \
"ds_read_b64_tr_b16 a[150:151], v211 offset:5120\n" \
"v_mfma_f32_16x16x32_bf16 v[148:151], a[80:83], a[32:35], 0\n" \
"ds_read_b64_tr_b16 a[152:153], %[vvb] offset:1536\n" \
"v_mfma_f32_16x16x32_bf16 v[148:151], a[84:87], a[36:39], v[148:151]\n" \
"ds_read_b64_tr_b16 a[154:155], %[vvb] offset:5632\n" \
"v_mfma_f32_16x16x32_bf16 v[148:151], a[88:91], a[40:43], v[148:151]\n" \
"ds_read_b64_tr_b16 a[156:157], v211 offset:1536\n" \
"v_mfma_f32_16x16x32_bf16 v[148:151], a[92:95], a[44:47], v[148:151]\n" \
"ds_read_b64_tr_b16 a[158:159], v211 offset:5632\n" \
"v_mfma_f32_16x16x32_bf16 v[152:155], a[64:67], a[48:51], 0\n" \
"v_mfma_f32_16x16x32_bf16 v[152:155], a[68:71], a[52:55], v[152:155]\n" \
"v_mfma_f32_16x16x32_bf16 v[152:155], a[72:75], a[56:59], v[152:155]\n" \
"v_mfma_f32_16x16x32_bf16 v[152:155], a[76:79], a[60:63], v[152:155]\n" \
"v_mfma_f32_16x16x32_bf16 v[156:159], a[80:83], a[48:51], 0\n" \
"v_mfma_f32_16x16x32_bf16 v[156:159], a[84:87], a[52:55], v[156:159]\n" \
"v_mfma_f32_16x16x32_bf16 v[156:159], a[88:91], a[56:59], v[156:159]\n" \
"v_mfma_f32_16x16x32_bf16 v[156:159], a[92:95], a[60:63], v[156:159]\n" \
"s_branch Lhavem_%=\n" \
"s_nop 0\n" \
"Lend_%=:\n" \
"s_waitcnt lgkmcnt(0)\n" \
"s_nop 0\n" \
""

#define SRD_WORD3 0x00020000
DI u32x4 ld16_sc1(const void* p) { u32x4 v; asm volatile("global_load_dwordx4 %0, %1, off sc1" : "=v"(v) : "v"(p) : "memory"); return v; }
DI unsigned ord_key(float r, unsigned row) {
  const unsigned b = __float_as_uint(r), o = (b & 0x80000000u) ? ~b : (b | 0x80000000u);
  return max(o & 0xfffff800u, 0x800u) | (2047u - row);
}
DI void top2_merge(unsigned& a1, unsigned& a2, unsigned b1, unsigned b2) {
  const unsigned n1 = max(a1, b1), n2 = max(min(a1, b1), max(a2, b2)); a1 = n1; a2 = n2;
}
template <int CTRL> DI void top2_dpp(unsigned& k1, unsigned& k2) {
  const unsigned p1 = __builtin_amdgcn_update_dpp(0u, k1, CTRL, 0xf, 0xf, true), p2 = __builtin_amdgcn_update_dpp(0u, k2, CTRL, 0xf, 0xf, true); top2_merge(k1, k2, p1, p2);
}
DI float row_sum(float v) {
  v = dpp_add<DPP_XOR1>(v); v = dpp_add<DPP_XOR2>(v); v = dpp_add<DPP_ROR8>(v);
  v = swap16_add(v, v); return swap32_add(v, v);
}
DI unsigned h2_pair(float x0, float x1, float m, float rden, unsigned c1w, unsigned sbw) { return ln_pair(x0, x1, m, rden, lo16(c1w), hi16(c1w), lo16(sbw), hi16(sbw)); }
#define PAGE(n, nx) asm volatile("s_branch adaln_pe" #n "\n.p2align 12\nadaln_pg" #n ":\n\ts_cbranch_execz " nx "\nadaln_pe" #n ":")
constexpr int MOE_WGS = DFF / 16;
constexpr unsigned RETRY_CAP = 4096;
#define ASLEEP 2
#define KLOOK 4
#define YSPLIT 8
#define FSLEEP 14
#define KSLEEP 3
#define YEARLY 8
#define FREST 0
#define KL0 1u
#define KLS 2u
#define KST 2u
#define KCHK 8
#define KPAUSE 2
#define KEY_SEL b

#define WBYTES (moe ? DM * DFF * 2 : 0)
namespace fa {
constexpr unsigned LDS_BYTES = 163840;
constexpr float QSCALE = 0.12751743082f;
struct Args { const u16 *x, *wmod, *wq, *kc, *vc, *wo, *wg, *wu, *wd, *c, *wr; char* ws; u16* out;
};
__global__ __launch_bounds__(256, 1) void fattn_k(const Args p) {
  extern __shared__ __attribute__((aligned(16))) char lds[];
  const unsigned wid = __builtin_amdgcn_readfirstlane(threadIdx.x >> 6);
  const unsigned b = blockIdx.x, idx = b >> 3, kg = b & 3u, m = idx & 7u, hd = 8u * kg + m, y = (b & 4u) + (idx >> 3);
  const unsigned ldsb = (unsigned)(size_t)lds;
  const unsigned m0b = ldsb + 4096u * wid, m0t = ldsb + 131072u + 8192u * wid;
  const __amdgpu_buffer_rsrc_t srdz = __builtin_amdgcn_make_buffer_rsrc((void*)p.ws, (short)0, (int)WS_END, SRD_WORD3);
  const bool moe = b < MOE_WGS;
  const unsigned sl = 4u * ((b & 7u) * 7u + (idx >> 2)) + (idx & 3u), yp2 = (y & 1u) * 2u;
  const unsigned sin = (unsigned)WS_XIN + ((y * 32u + hd) * 4u + wid) * BOX;
  u32x2 xq[8]; unsigned fl, fl2, got, got2; unsigned long long tag;
  unsigned k1, k2; unsigned long long lat;
  const unsigned sxf = (unsigned)WS_XFL + (y * 4u + wid) * 512u, sxl = (unsigned)WS_XFL + (y * 4u + kg) * 512u;
#define FQ(i)
#define TQ(i)
  {
    const unsigned tid = threadIdx.x, lane = tid & 63u, l16 = lane & 15u, g = lane >> 4, hi = lane >> 5, r8 = (lane >> 2) & 7u;
    unsigned kb0 = ldsb + 256u * l16 + 16u * (g ^ l16);
    unsigned vvb = ldsb + 65536u + 2048u * hi + 288u * (g & 1u) + 64u * ((lane >> 2) & 3u) + 8u * (lane & 3u);
    unsigned vwb = ldsb + 65536u + 2048u * g + 64u * ((lane >> 2) & 3u) + 16u * (lane & 3u);
    unsigned kd0 = 4096u * wid + 256u * g + 16u * (l16 ^ g);
    unsigned vd0 = 4096u * wid + 256u * r8 + 64u * hi + 16u * ((lane & 3u) ^ (2u * (r8 >> 2))) + 1024u;
    unsigned wd0 = 8192u * (16u * wid + r8) + 64u * hi + 16u * (lane & 3u) + 4096u;
    unsigned hq0 = (unsigned)WS_H + (16u * y + 4u * wid) * 32768u + 16u * lane;
    unsigned oq0 = (unsigned)WS_XIN + ((y * 32u + kg * 8u) * 4u + wid) * BOX + 20480u + 16u * lane;
    const unsigned flag = ldsb + 49152u + 4u * wid;
    const __amdgpu_buffer_rsrc_t srdw = __builtin_amdgcn_make_buffer_rsrc((void*)((const char*)p.wq + 256u * hd - 4096), (short)0, (int)(DM * DQ * 2 - 256u * hd + 4096u), SRD_WORD3);
    const __amdgpu_buffer_rsrc_t srdk = __builtin_amdgcn_make_buffer_rsrc((void*)((const char*)p.kc + (size_t)hd * (SEQ * 256)), (short)0, SEQ * 256, SRD_WORD3);
    const __amdgpu_buffer_rsrc_t srdv = __builtin_amdgcn_make_buffer_rsrc((void*)((const char*)p.vc + (size_t)hd * (SEQ * 256) - 17408), (short)0, SEQ * 256 + 17408, SRD_WORD3);
    unsigned long long pct; unsigned clen;
    asm volatile("s_getpc_b64 %0\n\ts_mov_b32 %1, adaln_text_end@rel32@lo+4" : "=s"(pct), "=s"(clen));
    unsigned vct = min(((idx * 4u + wid) * 7u + min(lane, 6u)) * 128u, (clen - 4u) & ~3u);
#define XNT " nt"
#define WNT " nt"
      const unsigned gr = 128u * (b & 7u) + 4u * (tid >> 3), gc = 64u * (b >> 3) + 8u * (tid & 7u), gran0 = b;
      u32x4 w[4], xs[4]; u32x2 ep, cv;
#define XV(i) ({ u32x4 v_; asm volatile("v_accvgpr_read_b32 %0, a[%4]\n\tv_accvgpr_read_b32 %1, a[%4 + 1]\n\tv_accvgpr_read_b32 %2, a[%4 + 2]\n\tv_accvgpr_read_b32 %3, a[%4 + 3]" : "=v"(v_[0]), "=v"(v_[1]), "=v"(v_[2]), "=v"(v_[3]) : "n"(200 + 4 * (i))); v_; })
#define CPOS 1
      #pragma unroll
      for (int j = 0; j < 5; ++j) {
        if (j == CPOS) asm volatile("global_load_dwordx2 %0, %1, %2" : "=v"(cv) : "v"(gr * 2u), "s"(p.c) : "memory");
        if (j < 4) asm volatile("global_load_dwordx4 %0, %1, %2" WNT : "=v"(w[j]) : "v"(((gr + j) * NMOD + gc) * 2u), "s"(p.wmod) : "memory");
      }
      #pragma unroll
      for (int q = 0; q < 4; ++q) asm volatile("global_load_dwordx4 %0, %1, %2 offset:%3" XNT : "=v"(xs[q]) : "v"((8u * (y * 32u + hd) + 2u * wid) * 2048u + 16u * lane), "s"(p.x), "n"(1024 * q) : "memory");
      asm volatile("global_load_dwordx2 %0, %1, %2 sc1" : "=v"(ep) : "v"(0u), "s"(p.ws + WS_EP) : "memory");
      {
        const unsigned zo = b * Z_SHARE + tid * 16u - (unsigned)(WS_U - WS_Z);
        u32x4 zv;
        #pragma unroll
        for (int e = 0; e < 4; ++e) { const unsigned h = (zo + 4u * e + 1u) * 2654435761u; zv[e] = zo >= 16384u ? 0u : NORM == 1 && (zo & 2048u) ? 0x3f803f80u | ((h ^ (h >> 15)) * 0x2c1b3c6du & 0x80008000u) : ~0u; }
        asm volatile("buffer_store_dwordx4 %0, %1, %2, 0 offen sc1\n\ts_nop 1" :: "v"(zv), "v"(tid < Z_SHARE / 16u ? (unsigned)WS_Z + b * Z_SHARE + tid * 16u : 0x80000000u), "s"(srdz) : "memory");
      }
      FQ(6);
      asm volatile("global_load_dword a255, %0, %1" :: "v"(vct), "s"(pct) : "memory");
#define PRET 0
      asm volatile("s_mov_b64 exec, 0\n\ts_cmp_eq_u32 %0, 1\n\ts_cbranch_scc1 adaln_pg5\n\ts_cmp_eq_u32 %0, 2\n\ts_cbranch_scc1 adaln_pg6\n\ts_cmp_eq_u32 %0, 3\n\ts_cbranch_scc1 adaln_pg7\nadaln_fr:\n\ts_mov_b64 exec, -1" :: "s"(wid) : "scc");
      FQ(7);
      float* red = (float*)(lds + 131072 + 4096);
      auto silu = [](float v) { return rbf(v * __builtin_amdgcn_rcpf(1.f + __builtin_amdgcn_exp2f(v * -1.44269504089f))); };
      asm volatile("s_waitcnt vmcnt(%1)" : "+v"(cv) : "n"(11 - CPOS) : "memory");
      unsigned sp[2] = {cvtpk(silu(lo16(cv[0])), silu(hi16(cv[0]))), cvtpk(silu(lo16(cv[1])), silu(hi16(cv[1])))};
      unsigned gsa = lane < 4u ? (unsigned)WS_RST + ((8u * (y * 32u + hd) + 2u * wid) * 2u + lane) * 8u : 0x80000000u;
      unsigned gma = wid ? 0x80000000u : (unsigned)WS_MOD + (gran0 * 64u + lane) * 8u, gra = ldsb + 131072u + 4096u + lane * 4u;
      asm volatile("s_waitcnt vmcnt(7)" : "+v"(sp[0]), "+v"(sp[1]), "+v"(gsa), "+v"(gma), "+v"(gra), "+v"(w[0]), "+v"(w[1]), "+v"(w[2]), "+v"(w[3]) :: "memory");
      FQ(4);
      const unsigned rho = 0;
      unsigned tq0 = (idx * 8u + wid * 2u + ((lane >> 3) & 1u)) * 8192u + (b & 4u) * 256u + (lane & 7u) * 128u + 4096u - 256u * m;
#define WQT 0
#define KVT2 0
#define WQT2 4
#define TNT ""
      asm volatile("buffer_load_dword a255, %0, %1, 0 offen" TNT :: "v"(tq0), "s"(srdw) : "memory");
      {
        float acc[8];
        #pragma unroll
        for (int e = 0; e < 8; ++e) acc[e] = 0.f;
        #pragma unroll
        for (int j = 0; j < 2; ++j) {
          #pragma unroll
          for (int e = 0; e < 8; ++e) acc[e] = __builtin_amdgcn_fdot2_f32_bf16(__builtin_bit_cast(bf16x2, __builtin_amdgcn_perm(w[2 * j + 1][e >> 1], w[2 * j][e >> 1], (e & 1) ? 0x07060302u : 0x05040100u)), __builtin_bit_cast(bf16x2, sp[j]), acc[e], false);
        }
        float t[4], u[2];
        #pragma unroll
        for (int e = 0; e < 4; ++e) t[e] = swap32_add(acc[e], acc[e + 4]);
        #pragma unroll
        for (int e = 0; e < 2; ++e) u[e] = dpp_add<DPP_ROR8>(swap16_add(t[e], t[e + 2]));
        if ((lane & 8u) == 0) *(f32x2*)&red[wid * 64u + 8u * (tid & 7u) + 2u * g] = f32x2{u[0], u[1]};
      }
      unsigned gst, vx0r, vx1r;
      auto rowstats = [&]() __attribute__((always_inline)) {
      float stv4;
      {
        float t4[4] = {0.f, 0.f, 0.f, 0.f};
        #pragma unroll
        for (int e = 0; e < 16; ++e) {
          const unsigned xw = xs[e >> 2][e & 3]; const bf16x2 v = __builtin_bit_cast(bf16x2, xw);
          t4[2 * (e >> 3)] = __builtin_amdgcn_fdot2_f32_bf16(v, __builtin_bit_cast(bf16x2, 0x3f803f80u), t4[2 * (e >> 3)], false);
          t4[2 * (e >> 3) + 1] = __builtin_amdgcn_fdot2_f32_bf16(v, v, t4[2 * (e >> 3) + 1], false);
        }
        float u = swap16_add(swap32_add(t4[0], t4[2]), swap32_add(t4[1], t4[3]));
        u = dpp_add<DPP_XOR1>(u); u = dpp_add<DPP_XOR2>(u); u = dpp_add<DPP_ROR4>(u); u = dpp_add<DPP_ROR8>(u);
        const f32x2 mean = f32x2{__uint_as_float(__builtin_amdgcn_readlane(__float_as_uint(u), 0)), __uint_as_float(__builtin_amdgcn_readlane(__float_as_uint(u), 32))} * (1.f / DM);
        f32x2 v2 = f32x2{__uint_as_float(__builtin_amdgcn_readlane(__float_as_uint(u), 16)), __uint_as_float(__builtin_amdgcn_readlane(__float_as_uint(u), 48))} * (1.f / DM) - mean * mean;
        if (__builtin_expect(!(mean[0] * mean[0] <= v2[0] && mean[1] * mean[1] <= v2[1]), 0)) {
          f32x2 sq = {0.f, 0.f};
          #pragma unroll
          for (int e = 0; e < 8; ++e) {
            const f32x2 d0 = f32x2{lo16(xs[e >> 2][e & 3]), lo16(xs[2 + (e >> 2)][e & 3])} - mean, d1 = f32x2{hi16(xs[e >> 2][e & 3]), hi16(xs[2 + (e >> 2)][e & 3])} - mean;
            sq = __builtin_elementwise_fma(d1, d1, __builtin_elementwise_fma(d0, d0, sq));
          }
          v2 = f32x2{wave_sum(sq[0]), wave_sum(sq[1])} * (1.f / DM);
        }
        asm("s_nop 1\n\tv_readfirstlane_b32 %0, %2\n\tv_readfirstlane_b32 %1, %3" : "=&s"(vx0r), "=&s"(vx1r) : "v"(v2[0] + 1e-5f), "v"(v2[1] + 1e-5f));
        const float rs_ = __builtin_amdgcn_rcpf(rbf(__builtin_amdgcn_sqrtf(rbf(rbf((lane & 2u) ? v2[1] : v2[0]) + 1e-5f))));
        stv4 = rs_ * ((lane & 1u) ? 1.f : -rbf((lane & 2u) ? mean[1] : mean[0]));
      }
      gst = (__float_as_uint(stv4) + 0x80u) & 0xffffff00u; };
#define STP 0
      if (STP == 0) { asm volatile("s_waitcnt vmcnt(4)" : "+v"(xs[0]), "+v"(xs[1]), "+v"(xs[2]), "+v"(xs[3]) :: "memory"); rowstats(); asm volatile("" : "+v"(gst)); }
      asm volatile("s_waitcnt vmcnt(%5) lgkmcnt(0)\n\ts_barrier" : "+v"(xs[0]), "+v"(xs[1]), "+v"(xs[2]), "+v"(xs[3]), "+v"(ep) : "n"(PRET ? 3 : 2) : "memory");

      tag = (((unsigned long long)__builtin_amdgcn_readfirstlane(ep[1]) << 32) | __builtin_amdgcn_readfirstlane(ep[0])) + 1ull;
      const unsigned tl = (unsigned)tag & 0xffu, th = (unsigned)(tag >> 8);
      auto gran = [&](float v) { return ((unsigned long long)th << 32) | (((__float_as_uint(v) + 0x80u) & 0xffffff00u) | tl); };
      {
        float r0, r1, r2, r3;
        if (STP == 0) asm volatile("buffer_store_dwordx2 %0, %1, %2, 0 offen sc1" :: "v"(u32x2{gst | tl, th}), "v"(gsa), "s"(srdz) : "memory");
        asm volatile("ds_read_b32 %0, %4\n\tds_read_b32 %1, %4 offset:256\n\tds_read_b32 %2, %4 offset:512\n\tds_read_b32 %3, %4 offset:768\n\ts_waitcnt lgkmcnt(0)"
                     : "=&v"(r0), "=&v"(r1), "=&v"(r2), "=&v"(r3) : "v"(gra) : "memory");
        asm volatile("buffer_store_dwordx2 %0, %1, %2, 0 offen sc1\n\ts_nop 0" :: "v"(u32x2{((__float_as_uint((r0 + r1) + (r2 + r3)) + 0x80u) & 0xffffff00u) | tl, th}), "v"(gma), "s"(srdz) : "memory");
        if (STP == 1) { rowstats(); asm volatile("buffer_store_dwordx2 %0, %1, %2, 0 offen sc1\n\ts_nop 0" :: "v"(u32x2{gst | tl, th}), "v"(gsa), "s"(srdz) : "memory"); }
      }
      FQ(5);
#define SWQ 0
#define SWQ0 0x400000
      const unsigned rb0 = 16u * y + 4u * (hd >> 3), ch = 16u * (hd & 7u) + (tid >> 4);
      asm volatile("global_load_dwordx2 a[216:217], %0, %1" :: "v"(tid * 8u), "s"(p.c) : "memory");
      #pragma unroll
      for (int i = 0; i < 4; ++i) asm volatile("global_load_dwordx4 a[%2:%2 + 3], %0, %1" :: "v"(((rb0 + i) * 16u + l16) * 2048u + ch * 16u), "s"(p.x), "n"(200 + 4 * i) : "memory");
      PAGE(5, "adaln_fr");
      unsigned lane2 = lane; asm volatile("" : "+v"(lane2));
#define lane lane2
    unsigned wo0 = (32u * wid + r8) * 2048u + m * 256u + 64u * hi + 16u * ((lane & 3u) ^ (2u * (r8 >> 2)));
    const __amdgpu_buffer_rsrc_t srdt = __builtin_amdgcn_make_buffer_rsrc((void*)((const char*)p.wo + (size_t)kg * (DM * DM * 2)), (short)0, DM * DM * 2, SRD_WORD3);
    const __amdgpu_buffer_rsrc_t srdp = __builtin_amdgcn_make_buffer_rsrc((void*)((const char*)(y < 4u ? p.kc : p.vc) + (size_t)hd * (SEQ * 256) + (y & 3u) * 4096u), (short)0, (int)(KVPAR && (b & 1u) ? 0u : (KVPAR ? 0x400000u : 0u) + SEQ * 256 - (y & 3u) * 4096u), SRD_WORD3);
    unsigned vg0 = (256u * wid + (lane >> 1)) * (DFF * 2u) + sl * 32u + 16u * (lane & 1u);
    unsigned vdn = (16u * sl + hi) * 2048u + 512u * wid + 16u * ((lane & 31u) ^ (2u * hi));
    const unsigned mc0 = 2u * DM + 64u * ((idx >> 2) * 8u + (b & 7u)) + 16u * (idx & 3u);
    const unsigned md0 = (256u * wid + (lane >> 1)) * (NMOD * 2u) + mc0 * 2u + 16u * (lane & 1u);
    unsigned sd0 = ldsb + 131072u + 8192u + 512u * wid + 16u * g;
    fl = md0;
    unsigned rd0 = ldsb + 131072u + 7168u + 8192u * ((lane >> 2) & 3u) + 256u * wid + 4u * (lane & 3u);
    unsigned ta0 = m0t + 256u * g + 32u * ((lane >> 2) & 3u) + 8u * (lane & 3u);
    const unsigned c8 = (b * 4u + wid) * 8192u, cd = moe ? sl * 32768u + wid * 8192u : 0u, cw = (idx * 4u + wid) * 16384u;
    const unsigned cm = moe ? ((sl & 3u) * 4u + wid) * 64u * (DFF * 2u) + (sl >> 2) * 128u : 0u;
    unsigned vxs = BOX * (l16 >> 1) + 64u * (l16 & 1u) + 16u * g;
    const unsigned sxs = (unsigned)WS_XIN + (y * 128u + wid * 32u) * BOX + (m >> 1) * 1024u + kg * 256u + (m & 1u) * 128u;
    unsigned vx0 = ((256u * y + 8u * hd + 2u * wid + ((lane >> 2) & 1u)) * DM + 256u * hi + 128u * (l16 >> 3) + 16u * (g & 1u) + 4u * (lane & 3u) + 32u * yp2) * 2u;
    unsigned tlo = 0, thi = 0;
#define tab(ln, lo, hi2) asm("v_writelane_b32 %0, %2, " #ln "\n\tv_writelane_b32 %1, %3, " #ln : "+v"(tlo), "+v"(thi) : "s"(lo), "s"(hi2))
#define tabp(ln, q, off) do { const unsigned long long a_ = (size_t)(q) + (off); tab(ln, (unsigned)a_, (unsigned)(a_ >> 32)); } while (0)
#define TORD 0
#define TAB_X(n)  tabp(n, p.x, ((y * 32u + hd) * 2u + (wid & 1u)) * 8192u)
#define TAB_U(n)  tabp(n, p.wu, moe ? c8 : 0u)
#define TAB_O(n)  tabp(n, p.wo, c8)
#define TAB_D(n)  tabp(n, p.wd, cd)
    TAB_X(1); TAB_U(2); TAB_O(3); TAB_D(4);
    tabp(5, p.wo, kg * 2097152u + cw); tabp(6, p.wo, kg * 2097152u + cw + 8192u); tabp(7, p.wg, cm);
    tabp(17, p.wmod, 0u); tab(18, NP_FILL == 8 ? sin + 20480u : (unsigned)WS_XIN + ((y * 32u + kg * 8u + (m >> 1)) * 4u + wid) * BOX + 20480u + (m & 1u) * 8192u, (unsigned)WS_OFL + y * 16384u + kg * 128u);
    tab(19, (unsigned)WS_OFL + (y * 32u + hd) * 512u + wid * 128u, (unsigned)WS_OFL + (y * 32u + kg * 8u) * 512u + wid * 128u);
    tabp(8, p.wg, 0u); tabp(9, p.wu, 0u); tabp(10, p.wd, 0u);
    tab(11, m, m * 4u * BOX); tab(12, (y & 1u) * 128u, 128u - (y & 1u) * 128u);
    tab(13, sxl, (unsigned)WS_PFL + (y * 32u + hd) * 512u + kg * 128u); tab(14, sxs, sxf);
    tab(15, (unsigned)WS_MODT + (mc0 - 2u * DM + 4u * wid) * 4u, (mc0 >> 10) == 4u ? 0x3f800000u : 0u);
    tab(16, (unsigned)WS_PFL + y * 16384u + (hd * 4u + wid) * 4u, (unsigned)WS_PFL + (y * 32u + hd) * 512u + wid * 128u);
    const char* psig = p.ws + WS_CNT + y * 8192u + hd * 8u;
    const char* pcnt = p.ws + WS_CNT + y * 8192u + hd * 256u;
    tabp(20, p.wr, 0u); tab(21, (unsigned)WS_PART + b * 8192u + wid * 2048u, 0u);
    tabp(25, p.out, 0u); tab(26, 256u * y + 8u * hd + 2u * wid, (y & 1u) * 256u); tab(27, (unsigned)WS_KEYS + b * 8u + wid * 4096u, (unsigned)WS_KEYS + (b & 7u) * 2048u);
    tab(28, (unsigned)WS_PART + ((b & 7u) * 32u + idx) * 32u + wid * 262144u, ((b & 7u) * 32u + idx) * 8u);
    tab(31, (128u * m + 32u * (y >> 1) + 8u * (y & 1u)) * 2048u + 512u * wid, (unsigned)WS_U + (y >> 1) * 4096u + hd * 64u + (y & 1u) * 32u); tab(32, hd * 64u, 0u);
    tab(36, vx0r, vx1r);
    tab(33, (unsigned)WS_PX + (y * 32u + hd) * 1024u + wid * 256u, (unsigned)WS_PX + y * 32768u + (8u * hd + 2u * wid) * 4u); tab(34, (256u * y + 64u * wid) * 2048u + 64u * hd, (unsigned)WS_PK + b * 8u + wid * 4096u);
    tab(35, (unsigned)WS_PK + (b & 7u) * 2048u, 256u * y + 8u * hd + 2u * wid);
      asm volatile("" : "+v"(tlo), "+v"(thi), "+v"(wo0), "+v"(vg0), "+v"(vdn), "+v"(ta0), "+v"(vxs), "+v"(vx0), "+v"(rd0), "+v"(fl), "+v"(sd0), "+v"(kb0), "+v"(vvb), "+v"(vwb), "+v"(kd0), "+v"(vd0), "+v"(wd0), "+v"(hq0), "+v"(oq0));
#undef lane
      float* par = (float*)(lds + 131072);
      u32x2 mp[8]; u32x4 rs;
      auto look = [&]() __attribute__((always_inline)) {
        #pragma unroll
        for (int k = 0; k < 8; ++k) asm volatile("global_load_dwordx2 %0, %1, %2 offset:%3 sc1" : "=v"(mp[k]) : "v"(((wid >> 1) * 16u + 2u * (hd & 7u) + (wid & 1u)) * 4096u + lane * 8u), "s"(p.ws + WS_MOD), "n"(512 * k) : "memory");
        asm volatile("global_load_dwordx4 %0, %1, %2 sc1" : "=v"(rs) : "v"(((rb0 + wid) * 16u + l16) * 16u), "s"(p.ws + WS_RST) : "memory");
      };
#define LOOKED "+v"(mp[0]), "+v"(mp[1]), "+v"(mp[2]), "+v"(mp[3]), "+v"(mp[4]), "+v"(mp[5]), "+v"(mp[6]), "+v"(mp[7]), "+v"(rs)
      look();
      #pragma unroll
      for (int q = 0; q < 14; ++q)
        asm volatile("s_mov_b32 m0, %0\n\ts_nop 0\n\tbuffer_load_dwordx4 %1, %2, %3 offen lds" :: "s"(m0b + 65536u + 16384u * (q >> 2) + 1024u * (q & 3)), "v"(wd0), "s"(srdw),
                     "s"(0x80000u * ((2u * rho + (q >> 2)) & 15u) + 65536u * ((q >> 1) & 1) + 128u * (q & 1)) : "m0", "memory");
#define KVT 0
#define KV0 0
      #pragma unroll
      for (int j = 0; j < KVT; ++j) asm volatile("buffer_load_dword a255, %0, %1, %2 offen" :: "v"(lane < 32u ? lane * 128u : 0x80000000u), "s"(srdp), "s"((KV0 + wid + 4u * j) * 16384u) : "memory");
      asm volatile("s_waitcnt vmcnt(%0)" :: "n"(27 + KVT) : "memory");
      { const u32x4 cs = XV(4); *(u32x2*)(lds + 131072 + 8192 + tid * 8u) = u32x2{cvtpk(silu(lo16(cs[0])), silu(hi16(cs[0]))), cvtpk(silu(lo16(cs[1])), silu(hi16(cs[1])))}; }
      asm volatile("s_waitcnt vmcnt(%9)" : LOOKED : "n"(14 + KVT) : "memory");
      for (unsigned spins = 0;; ++spins) {
        unsigned bl = (rs[0] ^ tl) | (rs[2] ^ tl), bh = (rs[1] ^ th) | (rs[3] ^ th);
        #pragma unroll
        for (int k = 0; k < 8; ++k) { bl |= mp[k][0] ^ tl; bh |= mp[k][1] ^ th; }
        if (__all(((bl & 0xffu) | bh) == 0u) || spins > SPIN_CAP) break;
        __builtin_amdgcn_s_sleep(ASLEEP);
        look();
        asm volatile("s_waitcnt vmcnt(0)" : LOOKED :: "memory");
      }
      {
        float v = 0.f;
        #pragma unroll
        for (int k = 0; k < 8; ++k) v += __uint_as_float(mp[k][0] & 0xffffff00u);
        v = rbf(v); par[tid] = (tid < 128u ? v : rbf(1.f + v)) * QSCALE;
        if (lane < 16u) *(u32x2*)(par + 256u + 8u * lane + 2u * wid) = u32x2{rs[0] & 0xffffff00u, rs[2] & 0xffffff00u};
      }
      PAGE(6, "adaln_fr");
      asm volatile("s_waitcnt lgkmcnt(0)\n\ts_barrier" ::: "memory");
      asm volatile("buffer_load_dword a255, %0, %1, %2 offen" TNT :: "v"(tq0), "s"(srdw), "s"(0x200000u) : "memory");
      FQ(0);
      {
        const float* pc = par + 8u * (tid >> 4);
        const f32x4 a0 = *(const f32x4*)pc, a1 = *(const f32x4*)(pc + 4), c0 = *(const f32x4*)(pc + 128), c1 = *(const f32x4*)(pc + 132);
        const f32x4 t0 = *(const f32x4*)(par + 256u + 8u * l16), t1 = *(const f32x4*)(par + 260u + 8u * l16);
        #pragma unroll
        for (int i = 0; i < 4; ++i) {
          const float mn = i < 2 ? t0[2 * i] : t1[2 * i - 4], rd = i < 2 ? t0[2 * i + 1] : t1[2 * i - 3];
          u32x4 o = XV(i);
          #pragma unroll
          for (int e = 0; e < 4; ++e) {
            const f32x2 sc = e < 2 ? f32x2{c0[2 * e], c0[2 * e + 1]} : f32x2{c1[2 * e - 4], c1[2 * e - 3]}, sh = e < 2 ? f32x2{a0[2 * e], a0[2 * e + 1]} : f32x2{a1[2 * e - 4], a1[2 * e - 3]};
            o[e] = cvtpk2(__builtin_elementwise_fma(__builtin_elementwise_fma(unpk(o[e]), f32x2{rd, rd}, f32x2{mn, mn}), sc, sh));
          }
          st16_wt(p.ws + WS_H + (rb0 + i) * 32768u + ch * 256u + 16u * l16, o);
        }
      }
      FQ(1);
    asm volatile(FA_BODY
      : FA_OUTS
      : [m0b] "s"(m0b), [flag] "s"(flag), [srdz] "s"(srdz), [srdw] "s"(srdw), [srdk] "s"(srdk), [srdv] "s"(srdv), [m0t] "s"(m0t), [srdt] "s"(srdt), [srdp] "s"(srdp),
        [tag] "s"(tag), [pcnt] "s"(pcnt), [psig] "s"(psig), [wid] "s"(wid), [px] "s"(p.x), [pct] "s"(pct), [sin] "s"(sin), [tlo] "v"(tlo), [thi] "v"(thi), [wbytes] "s"(WBYTES)
      : FA_CLOBBERS);
  }
  if (b == 255u && wid == 0u) __hip_atomic_store((gu64*)(p.ws + WS_EP), tag, RLX_AGENT);
  asm volatile("adaln_text_end:");
}
}

extern "C" void kernel_launch(void* const* d_in, const int* in_sizes, int n_in, void* d_out, int out_size, void* d_ws, size_t ws_size, hipStream_t stream) {
  (void)in_sizes; (void)n_in; (void)out_size;
  if (ws_size < WS_END) return;
  const u16* x    = (const u16*)d_in[0];
  const u16* kc   = (const u16*)d_in[1];
  const u16* vc   = (const u16*)d_in[2];
  const u16* wq   = (const u16*)d_in[3];
  const u16* wo   = (const u16*)d_in[4];
  const u16* wr   = (const u16*)d_in[6];
  const u16* wg   = (const u16*)d_in[7];
  const u16* wu   = (const u16*)d_in[8];
  const u16* wd   = (const u16*)d_in[9];
  const u16* c    = (const u16*)d_in[10];
  const u16* wmod = (const u16*)d_in[11];
  u16* out = (u16*)d_out;
  char* ws = (char*)d_ws;
  fa::Args fr;
  fr.x = x; fr.wmod = wmod; fr.wq = wq; fr.kc = kc; fr.vc = vc; fr.wo = wo; fr.wg = wg; fr.wu = wu; fr.wd = wd; fr.ws = ws; fr.out = out; fr.c = c; fr.wr = wr;
  hipFuncSetAttribute((const void*)fa::fattn_k, hipFuncAttributeMaxDynamicSharedMemorySize, (int)fa::LDS_BYTES);
  hipLaunchKernelGGL(fa::fattn_k, dim3(256), dim3(256), fa::LDS_BYTES, stream, fr);
}
```
